```python
import torch

def _softmax(x, axis=-1):
    return torch.softmax(x, dim=axis)

def _gelu(x):
    return torch.nn.functional.gelu(x, approximate='tanh')

def _silu(x):
    return torch.nn.functional.silu(x)

def _sigmoid(x):
    return torch.sigmoid(x)

def _layernorm(x, axis=-1, eps=1e-05):
    m = x.mean(dim=axis, keepdim=True)
    v = x.var(dim=axis, keepdim=True, unbiased=False)
    return (x - m) / torch.sqrt(v + eps)

def _scatter_add(tgt, idx, src, axis=0):
    return tgt.clone().index_add_(axis, idx, src)

def _segment_sum(src, seg, n):
    out = torch.zeros((n,) + src.shape[1:], dtype=src.dtype)
    return out.index_add_(0, seg, src)

def _pad(x, pw):
    flat = tuple((v for pair in reversed(pw) for v in pair))
    return torch.nn.functional.pad(x, flat)
kernel_name = 'concat_heads__memory_aug_recurrent_mem_tr__ssm_conv_s4__prompt'

def setup_inputs():

    _w = lambda a: (lambda t: t.to(torch.bfloat16) if t.is_floating_point() else t)(torch.as_tensor(a))

    def _A_setup():
        B, S, Dm = (1, 2048, 1024)
        H, Dh = (32, 128)
        M, seg = (8, 32)
        seg = min(seg, S)
        S = S // seg * seg
        return {'x': _w((lambda *s: torch.randn(*s, dtype=torch.bfloat16) * 0.05)(B, S, Dm)), 'mem0': _w((lambda *s: torch.randn(*s, dtype=torch.bfloat16) * 0.4)(B, M, Dm)), 'Wq': _w((lambda *s: torch.randn(*s, dtype=torch.bfloat16) * 6.4)(Dm, H, Dh) * Dm ** (-0.5)), 'Wk': _w((lambda *s: torch.randn(*s, dtype=torch.bfloat16) * 6.4)(Dm, H, Dh) * Dm ** (-0.5)), 'Wv': _w((lambda *s: torch.randn(*s, dtype=torch.bfloat16) * 0.4)(Dm, H, Dh) * Dm ** (-0.5)), 'Wo': _w((lambda *s: torch.randn(*s, dtype=torch.bfloat16) * 0.4)(H, Dh, Dm) * (H * Dh) ** (-0.5)), 'seg_len': seg}
    _A_in = _A_setup()

    def _B_setup():
        B, L, D, N = (1, 2048, 1024, 128)
        A_re = -(lambda _x: torch.exp(torch.as_tensor(_x)))((lambda *s: torch.randn(*s, dtype=torch.bfloat16) * 6.4)(D, N))
        A_im = torch.pi * (lambda *s: torch.randn(*s, dtype=torch.bfloat16) * 0.05)(D, N)
        B_re = (lambda *s: torch.randn(*s, dtype=torch.bfloat16) * 0.1)(D, N)
        B_im = (lambda *s: torch.randn(*s, dtype=torch.bfloat16) * 0.1)(D, N)
        C_re = (lambda *s: torch.randn(*s, dtype=torch.bfloat16) * 0.1)(D, N)
        C_im = (lambda *s: torch.randn(*s, dtype=torch.bfloat16) * 0.1)(D, N)
        log_dt = (lambda *s: torch.randn(*s, dtype=torch.bfloat16) * 0.05)(D) * 0.5 - 4.0
        Dskip = (lambda *s: torch.randn(*s, dtype=torch.bfloat16) * 0.05)(D)
        return {'u': _w((lambda *s: torch.randn(*s, dtype=torch.bfloat16) * 0.05)(B, L, D)), 'A_re': _w(A_re), 'A_im': _w(A_im), 'B_re': _w(B_re), 'B_im': _w(B_im), 'C_re': _w(C_re), 'C_im': _w(C_im), 'log_dt': _w(log_dt), 'Dskip': _w(Dskip)}
    _B_in = _B_setup()
    return {'A_x': _A_in['x'], 'A_mem0': _A_in['mem0'], 'A_Wq': _A_in['Wq'], 'A_Wk': _A_in['Wk'], 'A_Wv': _A_in['Wv'], 'A_Wo': _A_in['Wo'], 'A_seg_len': _A_in['seg_len'], 'B_u': _B_in['u'], 'B_A_re': _B_in['A_re'], 'B_A_im': _B_in['A_im'], 'B_B_re': _B_in['B_re'], 'B_B_im': _B_in['B_im'], 'B_C_re': _B_in['C_re'], 'B_C_im': _B_in['C_im'], 'B_log_dt': _B_in['log_dt'], 'B_Dskip': _B_in['Dskip']}

def reference(A_x, A_mem0, A_Wq, A_Wk, A_Wv, A_Wo, A_seg_len, B_u, B_A_re, B_A_im, B_B_re, B_B_im, B_C_re, B_C_im, B_log_dt, B_Dskip):

    def _A_ref(x, mem0, Wq, Wk, Wv, Wo, seg_len):
        B, S, Dm = x.shape
        H, Dh = (Wq.shape[1], Wq.shape[2])
        M = mem0.shape[1]
        scale = Dh ** (-0.5)
        n_seg = S // seg_len
        L = M + seg_len
        idx = torch.arange(L)
        mask = (idx[:, None] < M) | (idx[:, None] >= idx[None, :])
        mem = mem0
        outs = []
        for s in range(n_seg):
            xs = x[:, s * seg_len:(s + 1) * seg_len, :].clone()
            h = torch.concatenate([mem, xs], axis=1)
            q = torch.einsum('btd,dhe->bhte', h, Wq)
            k = torch.einsum('btd,dhe->bhte', h, Wk)
            v = torch.einsum('btd,dhe->bhte', h, Wv)
            scores = torch.einsum('bhqe,bhke->bhqk', q, k) * scale
            scores = torch.where(mask[None, None], scores, -1000000000.0)
            p = _softmax(scores, axis=-1)
            o = torch.einsum('bhqk,bhke->bhqe', p, v)
            o = torch.einsum('bhte,hed->btd', o, Wo)
            mem = o[:, :M, :].clone()
            outs.append(o[:, M:, :])
        return torch.concatenate(outs, axis=1)

    def _B_ref(u, A_re, A_im, B_re, B_im, C_re, C_im, log_dt, Dskip):
        Bsz, L, D = u.shape
        dt = torch.exp(log_dt)[:, None]
        A = A_re + 1j * A_im
        Bc = B_re + 1j * B_im
        Cc = C_re + 1j * C_im
        logdA = dt * A
        dB = dt * Bc
        l = (lambda x, dt: x.to(dt))(torch.arange(L), A_re.dtype)
        P = torch.exp(logdA[:, :, None] * l[None, None, :])
        K = torch.einsum('dn,dnl,dn->dl', Cc, P, dB).real
        L2 = 2 * L
        Kp = _pad(K, ((0, 0), (0, L)))
        up = (lambda x, *d: x.permute(*d))(u, 0, 2, 1)
        up = _pad(up, ((0, 0), (0, 0), (0, L)))
        Kf = (lambda x, axes=(-2, -1): torch.fft.rfft2(x.float(), dim=axes))(Kp[:, None, :], axes=(-2, -1))
        Uf = (lambda x, axes=(-2, -1): torch.fft.rfft2(x.float(), dim=axes))(up[:, :, None, :], axes=(-2, -1))
        Yf = Uf * Kf[None, :, :, :]
        y = (lambda x, s, axes=(-2, -1): torch.fft.irfft2(x, s=s, dim=axes).to(torch.bfloat16))(Yf, (1, L2), axes=(-2, -1))[:, :, 0, :L]
        y = (lambda x, *d: x.permute(*d))(y, 0, 2, 1) + u * Dskip[None, None, :]
        return y
    a = _A_ref(A_x, A_mem0, A_Wq, A_Wk, A_Wv, A_Wo, A_seg_len)
    b = _B_ref(B_u, B_A_re, B_A_im, B_B_re, B_B_im, B_C_re, B_C_im, B_log_dt, B_Dskip)
    return torch.concatenate([a, b], axis=-1)
```

```cpp
#include <hip/hip_runtime.h>
#include <hip/hip_bf16.h>
#include <stdint.h>

#define S_LEN 2048
#define DM 1024
#define NH 32
#define DH 128
#define HD 4096
#define NSEG 64
#define SSM_L 2048
#define SSM_D 1024
#define SSM_N 128
#define OUTLD 2048

typedef unsigned short u16;
typedef unsigned int u32;
typedef unsigned long long u64;
typedef __attribute__((ext_vector_type(8))) short bf16x8;
typedef __attribute__((ext_vector_type(4))) short bf16x4;
typedef __attribute__((ext_vector_type(4))) float f32x4;
typedef __attribute__((ext_vector_type(16))) float f32x16;
typedef __attribute__((ext_vector_type(4))) unsigned int u32x4;
typedef __attribute__((ext_vector_type(2))) unsigned int u32x2;
typedef __attribute__((ext_vector_type(2))) float f32x2;
typedef __attribute__((ext_vector_type(2))) __bf16 bf2_t;
typedef short v4i16_t __attribute__((ext_vector_type(4)));
#define LAS __attribute__((address_space(3)))

#define SENT 0xFFFFFFFFu
#define RLX_AGENT __ATOMIC_RELAXED, __HIP_MEMORY_SCOPE_AGENT
#define SC1 16

__device__ __forceinline__ float bf2f(u16 v) { return __uint_as_float(((u32)v) << 16); }
__device__ __forceinline__ u32 pk2(float a, float b) {
  f32x2 f = {a, b};
  bf2_t h = __builtin_convertvector(f, bf2_t);
  return __builtin_bit_cast(u32, h);
}
__device__ __forceinline__ float rbf(float f) { return __uint_as_float(pk2(f, 0.f) << 16); }
__device__ __forceinline__ u32 nosent(u32 v) { return v < 0xFFFFFFFEu ? v : 0xFFFFFFFEu; }
__device__ __forceinline__ bool okv(u32x4 v) { return v[0] != SENT && v[1] != SENT && v[2] != SENT && v[3] != SENT; }
__device__ __forceinline__ bf16x8 asb(u32x4 v) { return __builtin_bit_cast(bf16x8, v); }
__device__ __forceinline__ void sleepn(int n) { for (int k = 0; k < n; ++k) __builtin_amdgcn_s_sleep(1); }
#define WAIT_VM0() asm volatile("s_waitcnt vmcnt(0)" ::: "memory")
#define BAR() do { asm volatile("s_waitcnt lgkmcnt(0)" ::: "memory"); __builtin_amdgcn_s_barrier(); asm volatile("" ::: "memory"); } while (0)

__device__ __forceinline__ float xmax16(float x) {
  auto r = __builtin_amdgcn_permlane16_swap(__float_as_uint(x), __float_as_uint(x), false, false);
  return fmaxf(__uint_as_float(r[0]), __uint_as_float(r[1]));
}
__device__ __forceinline__ float xmax32(float x) {
  auto r = __builtin_amdgcn_permlane32_swap(__float_as_uint(x), __float_as_uint(x), false, false);
  return fmaxf(__uint_as_float(r[0]), __uint_as_float(r[1]));
}
__device__ __forceinline__ float xadd16(float x) {
  auto r = __builtin_amdgcn_permlane16_swap(__float_as_uint(x), __float_as_uint(x), false, false);
  return __uint_as_float(r[0]) + __uint_as_float(r[1]);
}
__device__ __forceinline__ float xadd32(float x) {
  auto r = __builtin_amdgcn_permlane32_swap(__float_as_uint(x), __float_as_uint(x), false, false);
  return __uint_as_float(r[0]) + __uint_as_float(r[1]);
}
__device__ __forceinline__ u32 ror8(u32 x) { return (u32)__builtin_amdgcn_update_dpp(0, (int)x, 0x128, 0xf, 0xf, true); }
template <int N> __device__ __forceinline__ float rorf(float x) {
  return __uint_as_float((u32)__builtin_amdgcn_update_dpp(0, (int)__float_as_uint(x), 0x120 + N, 0xf, 0xf, true));
}
__device__ __forceinline__ float ror8f(float x) { return __uint_as_float(ror8(__float_as_uint(x))); }

#define MFMA32(a, b, c) __builtin_amdgcn_mfma_f32_16x16x32_bf16(a, b, c, 0, 0, 0)
#define MFMA16(a, b, c) __builtin_amdgcn_mfma_f32_16x16x16bf16_1k(a, b, c, 0, 0, 0)
#define MFMA_AV0(acc, wa, xb) asm volatile("v_mfma_f32_16x16x32_bf16 %0, %1, %2, 0" : "=&v"(acc) : "a"(wa), "v"(xb))
#define MFMA_AV(acc, wa, xb) asm volatile("v_mfma_f32_16x16x32_bf16 %0, %1, %2, %0" : "+v"(acc) : "a"(wa), "v"(xb))
#define MFMA_VA0(acc, xa, wb) asm volatile("v_mfma_f32_16x16x32_bf16 %0, %1, %2, 0" : "=&v"(acc) : "v"(xa), "a"(wb))
#define MFMA_VA(acc, xa, wb) asm volatile("v_mfma_f32_16x16x32_bf16 %0, %1, %2, %0" : "+v"(acc) : "v"(xa), "a"(wb))
#define MFMA_VV0(acc, xa, xb) asm volatile("v_mfma_f32_16x16x32_bf16 %0, %1, %2, 0" : "=&v"(acc) : "v"(xa), "v"(xb))
#define MFMA16_VV(acc, xa, xb) asm volatile("v_mfma_f32_16x16x16_bf16 %0, %1, %2, %0" : "+v"(acc) : "v"(xa), "v"(xb))
#define MFMA_ACC(acc, xa, xb) asm volatile("v_mfma_f32_16x16x32_bf16 %0, %1, %2, %0" : "+a"(acc) : "v"(xa), "v"(xb))
#define NOP_IN() asm volatile("s_nop 1" ::: "memory")
#define PIN(x) asm volatile("" : "+v"(x))
#define PINA(x) asm volatile("" : "+a"(x))

__device__ __forceinline__ bf16x4 trread(const char LAS* p) {
  return __builtin_amdgcn_ds_read_tr16_b64_v4i16((LAS v4i16_t*)p);
}
__device__ __forceinline__ bf16x8 cat8(bf16x4 a, bf16x4 b) {
  return __builtin_shufflevector(a, b, 0, 1, 2, 3, 4, 5, 6, 7);
}

#define CW_TICKET 0
#define CW_POL 256
#define CW_INIT 288
#define CW_SSMQ 352
#define CW_DEAD 320
#define CW_PROG 384
#define CW_OXCNT 1408
#define CW_KVFLAG 3456
#define CTRL_BYTES 32768
#define EX_PAGES 20
#define EX_BYTES ((u64)EX_PAGES << 21)
#define EXP_C 0
#define EXP_A 2
#define EXP_B 4
#define QKV_SZ ((u64)S_LEN * HD * 2)
#define WS_Q_OFF (EX_BYTES)
#define WS_K_OFF (WS_Q_OFF + QKV_SZ)
#define WS_VT_OFF (WS_K_OFF + QKV_SZ)
#define WS_OX_OFF (WS_VT_OFF + QKV_SZ)
#define WS_KT_OFF (WS_OX_OFF + QKV_SZ)

#define SPIN_CAP 300000
#define NPROBE 5
#define GAP_A 4
#define GAP_C 5
#define DLY_A 8
#define DLY_B 2
#define DLY_C 9

struct Args {
  const u16 *x, *mem0, *Wq, *Wk, *Wv, *Wo;
  const u16 *u, *A_re, *A_im, *B_re, *B_im, *C_re, *C_im, *log_dt, *Dskip;
  u16* out;
  u32* ctrl;
  char* ex;
};

__device__ __forceinline__ bool is_dead(u32* ctrl) { return __hip_atomic_load(ctrl + CW_DEAD, RLX_AGENT) != 0; }
__device__ __forceinline__ void set_dead(u32* ctrl) { __hip_atomic_store(ctrl + CW_DEAD, 1u, RLX_AGENT); }
__device__ __forceinline__ void wait_ge(u32* ctrl, int word, u32 want, int slp) {
  for (int spins = 0;; ++spins) {
    asm volatile("" ::: "memory");
    u32 v = __builtin_amdgcn_readfirstlane(__hip_atomic_load(ctrl + word, RLX_AGENT));
    if (v >= want) break;
    if (spins > SPIN_CAP) { set_dead(ctrl); break; }
    if ((spins & 63) == 63 && __builtin_amdgcn_readfirstlane((int)is_dead(ctrl))) break;
    sleepn(slp);
  }
}
__device__ __forceinline__ u32 wait_pol(u32* ctrl) {
  const int lane = threadIdx.x & 63;
  u32 v = lane < EX_PAGES ? (ctrl[CW_POL + lane] & 1u) : 0u;
  u32 m = (u32)__builtin_amdgcn_readfirstlane((u32)__ballot(v != 0));
  const u32 full = (1u << EX_PAGES) - 1u, alt = 0xAAAAAAAAu & full;
  const int agree = __builtin_popcount((m ^ alt) & full);
  if (agree <= 5) m = alt; else if (agree >= EX_PAGES - 5) m = alt ^ full;
  return m;
}
__device__ __forceinline__ u32 exoff(u32 pol, u32 page, u32 pair) { return (page << 21) + (pair << 14) + (((pol >> page) & 1u) << 13); }
__device__ __forceinline__ u32 offA(u32 pol, int s, int stripe) { u32 pr = 2 * s + stripe; return exoff(pol, EXP_A + (pr >> 7), pr & 127); }
__device__ __forceinline__ u32 offB(u32 pol, int s, int h) { u32 pr = 32 * s + h; return exoff(pol, EXP_B + (pr >> 7), pr & 127); }
__device__ __forceinline__ u32 offC(u32 pol, int slot, int red) { return exoff(pol, EXP_C + slot, red); }
#define STAMP(i)
#define SUB(i, x)
#define SPINS(i, n)
__device__ __forceinline__ u32 max4(u32x4 v) { return max(max(v[0], v[1]), max(v[2], v[3])); }
#define POLL_FAIL(spins) ((++(spins) > SPIN_CAP) ? (set_dead(ctrl), true) : (((spins) & 255) == 255 && __builtin_amdgcn_readfirstlane((int)is_dead(ctrl))))

__device__ __forceinline__ void role_rec(const Args& a, char LAS* smem, const int cu) {
  const int tid = threadIdx.x, lane = tid & 63, w = __builtin_amdgcn_readfirstlane(tid >> 6);
  const int c = lane & 15, g = lane >> 4;
  const int head = cu >> 2, j = cu & 3;
  u32* ctrl = a.ctrl;
  const __amdgpu_buffer_rsrc_t ex = __builtin_amdgcn_make_buffer_rsrc(a.ex, 0, (int)EX_BYTES, 0x00020000);
  const __amdgpu_buffer_rsrc_t rk = __builtin_amdgcn_make_buffer_rsrc(a.ex + WS_K_OFF, 0, (int)(2 * QKV_SZ), 0x00020000);
  const u64 rtx = __builtin_amdgcn_s_memrealtime();
  const u32 pol = wait_pol(ctrl);
  {
    const u32x4 sv = (u32x4){SENT, SENT, SENT, SENT};
    for (int i = tid; i < 4 * 96; i += 256) {
      int s = i / 96, q = i - s * 96;
      __builtin_amdgcn_raw_buffer_store_b128(sv, ex, offB(pol, s, head) + j * 1536 + q * 16, 0, SC1);
    }
    for (int i = tid; i < NSEG * 8; i += 256) {
      int s = 1 + (i >> 3), q = i & 7;
      __builtin_amdgcn_raw_buffer_store_b128(sv, ex, offA(pol, s, cu >> 6) + (cu & 63) * 128 + q * 16, 0, SC1);
    }
    for (int i = tid; i < 2 * 32 * 8; i += 256) {
      int slot = i >> 8, red = 32 * j + ((i >> 3) & 31), q = i & 7;
      __builtin_amdgcn_raw_buffer_store_b128(sv, ex, offC(pol, slot, red) + head * 128 + q * 16, 0, SC1);
    }
    if (tid < 8) {
      u32x4 v = *(const u32x4*)(a.mem0 + (size_t)tid * DM + 8 * cu);
#pragma unroll
      for (int e = 0; e < 4; ++e) v[e] = nosent(v[e]);
      __builtin_amdgcn_raw_buffer_store_b128(v, ex, offA(pol, 0, cu >> 6) + (cu & 63) * 128 + tid * 16, 0, SC1);
    }
  }
  bf16x8 wf[3][2][8];
  bf16x8 wo[4][4];
  {
    const int q = c >> 2, p = lane & 3;
    const size_t go = (size_t)(tid >> 2) * HD + (tid & 3) * 8 + head * DH + 32 * j;
    const u32 lo = (tid >> 2) * 64 + (tid & 3) * 16;
    char LAS* bufA = smem; char LAS* bufB = smem + 65536;
    u32x4 s0[16], s1[16], s2[16];
#pragma unroll
    for (int i = 0; i < 16; ++i) s0[i] = *(const u32x4*)(a.Wq + go + (size_t)i * 64 * HD);
#pragma unroll
    for (int i = 0; i < 16; ++i) s1[i] = *(const u32x4*)(a.Wk + go + (size_t)i * 64 * HD);
#pragma unroll
    for (int i = 0; i < 16; ++i) s2[i] = *(const u32x4*)(a.Wv + go + (size_t)i * 64 * HD);
#pragma unroll
    for (int i = 0; i < 16; ++i) *(LAS u32x4*)(bufA + lo + i * 4096) = s0[i];
    {
      const u16* W = a.Wo + (size_t)head * DH * DM + 256 * j + (size_t)(tid >> 5) * DM + (tid & 31) * 8;
#pragma unroll
      for (int i = 0; i < 16; ++i) s0[i] = *(const u32x4*)(W + (size_t)i * 8 * DM);
    }
#pragma unroll
    for (int i = 0; i < 16; ++i) *(LAS u32x4*)(bufB + lo + i * 4096) = s1[i];
    __syncthreads();
#pragma unroll
    for (int mt = 0; mt < 2; ++mt)
#pragma unroll
      for (int t = 0; t < 2; ++t)
#pragma unroll
        for (int ks = 0; ks < 8; ++ks) {
          const char LAS* ad = (mt ? bufB : bufA) + (256 * w + 32 * ks + 8 * g + q) * 64 + (8 * p + 4 * t) * 2;
          wf[mt][t][ks] = cat8(trread(ad), trread(ad + 4 * 64));
        }
    __syncthreads();
#pragma unroll
    for (int i = 0; i < 16; ++i) *(LAS u32x4*)(bufA + lo + i * 4096) = s2[i];
#pragma unroll
    for (int i = 0; i < 16; ++i) *(LAS u32x4*)(bufB + (i * 8 + (tid >> 5)) * 512 + (tid & 31) * 16) = s0[i];
    __syncthreads();
#pragma unroll
    for (int t = 0; t < 2; ++t)
#pragma unroll
      for (int ks = 0; ks < 8; ++ks) {
        const char LAS* ad = bufA + (256 * w + 32 * ks + 8 * g + q) * 64 + (16 * t + 4 * p) * 2;
        wf[2][t][ks] = cat8(trread(ad), trread(ad + 4 * 64));
      }
#pragma unroll
    for (int ct = 0; ct < 4; ++ct)
#pragma unroll
      for (int ks = 0; ks < 4; ++ks) {
        const char LAS* ad = bufB + (32 * ks + 4 * g + q) * 512 + (64 * w + 32 * (ct >> 1) + 8 * p + 4 * (ct & 1)) * 2;
        wo[ct][ks] = cat8(trread(ad), trread(ad + 16 * 512));
      }
    WAIT_VM0();
    __syncthreads();
    if (tid == 0) __hip_atomic_fetch_add(ctrl + CW_INIT, 1u, RLX_AGENT);
    if (w == 0) wait_ge(ctrl, CW_INIT, 128, 6);
    __syncthreads();
  }
  while ((long long)(__builtin_amdgcn_s_memrealtime() - rtx) < 4000) __builtin_amdgcn_s_sleep(1);
  char LAS* l_part = smem;
  char LAS* l_sp = smem + 24576;
  char LAS* l_og = smem + 36864;
  char LAS* l_red = smem + 40960;
  const u32 vA = (4 * (c >> 3) + g) * 128 + (c & 7) * 16;
  const u32 vQK = c * 64 + g * 16;
  const u32 vV = 1024 + (c * 2 + (g & 1)) * 16;
  const u32 vKx = (u32)((c * HD + head * DH + 32 * w + 8 * g) * 2);
  const u32 vVx = (u32)(QKV_SZ + ((size_t)(head * DH + 32 * w + c) * 32 + 8 * g) * 2);
  const u32 vCs = (8 * w + 4 * (c >> 3) + g) * 16384 + (c & 7) * 16;
  const u32 vCr = c * 128 + (2 * w + (g >> 1)) * 16 + (g & 1) * 8;
  const u32 lRed = (((tid >> 5) * 2) * 64 + (((tid >> 5) < 2) ? (((tid & 31) >> 3) * 16 + (tid & 7)) : (tid & 31))) * 16;
  const u32 vBs = ((tid >> 5) < 2) ? (u32)(((tid >> 5) * 8 + (tid & 7)) * 64 + ((tid & 31) >> 3) * 16) : (u32)(1024 + ((tid & 15) * 2 + ((tid & 31) >> 4)) * 16);
  const u32 lSa = ((c >> 3) * 64 + g * 16 + c) * 16, lSb = (128 + g * 16 + (c & 7)) * 16 + (c >> 3) * 8;
  const int fK = CW_KVFLAG + 16 + (head >> 1), fV = CW_KVFLAG + 32 + (head >> 1);

  u32x4 kx[2], vx[2];
  u32 nfK = 0, nfV = 0;
  auto fetch_k = [&](int s) {
    if ((s & 7) == 0 && __builtin_amdgcn_readfirstlane(min(nfK, nfV)) < 2) {
      wait_ge(ctrl, fK + (s >> 3) * 48, 2, 4); wait_ge(ctrl, fV + (s >> 3) * 48, 2, 4);
    }
    asm volatile("" ::: "memory");
    kx[0] = __builtin_amdgcn_raw_buffer_load_b128(rk, vKx, s * 32 * HD * 2, SC1);
    kx[1] = __builtin_amdgcn_raw_buffer_load_b128(rk, vKx + 16 * HD * 2, s * 32 * HD * 2, SC1);
    if ((s & 7) == 7 && s + 1 < NSEG) {
      nfK = __hip_atomic_load(ctrl + fK + ((s + 1) >> 3) * 48, RLX_AGENT);
      nfV = __hip_atomic_load(ctrl + fV + ((s + 1) >> 3) * 48, RLX_AGENT);
    } else { nfK = 0; nfV = 0; }
  };
  auto fetch_v = [&](int s) {
    asm volatile("" ::: "memory");
    vx[0] = __builtin_amdgcn_raw_buffer_load_b128(rk, vVx, s * (NH * DH * 64), SC1);
    vx[1] = __builtin_amdgcn_raw_buffer_load_b128(rk, vVx + 16 * 64, s * (NH * DH * 64), SC1);
  };
  u32 soA = offA(pol, 0, w >> 1) + (w & 1) * 4096;
  int spins = 0;
#define SLOW() (++spins, ((spins > SPIN_CAP) ? (set_dead(ctrl), true) : ((spins & 255) == 255 && __builtin_amdgcn_readfirstlane((int)is_dead(ctrl)))))
#define PINS(x) asm volatile("" : "+s"(x))
  for (int s = 0; s < NSEG; ++s) {
    u32 soBw = offB(pol, s, head) + j * 1536, soBr = offB(pol, s, head) + w * 1536;
    u32 soCw = offC(pol, s & 1, 32 * j) + head * 128, soCp = offC(pol, (s & 1) ^ 1, 32 * j) + head * 128, soCr = offC(pol, s & 1, cu);
    u32 soAw = offA(pol, s + 1, cu >> 6), soAn = offA(pol, s + 1, w >> 1) + (w & 1) * 4096;
    PINS(soBw); PINS(soBr); PINS(soCw); PINS(soCp); PINS(soCr); PINS(soAw); PINS(soAn);
    if (s > 0) sleepn(DLY_A);
    STAMP(0);
    u32x4 ma[4];
    {
#pragma unroll
      for (int i = 0; i < 4; ++i) ma[i] = __builtin_amdgcn_raw_buffer_load_b128(ex, vA + i * 1024, soA, SC1);
      sleepn(GAP_A);
      asm volatile("" ::: "memory");
      u32x4 mb[4];
#pragma unroll
      for (int i = 0; i < 4; ++i) mb[i] = __builtin_amdgcn_raw_buffer_load_b128(ex, vA + i * 1024, soA, SC1);
      u32 mx = max(max(max4(ma[0]), max4(ma[1])), max(max4(ma[2]), max4(ma[3])));
      if (__builtin_expect(__builtin_amdgcn_ballot_w64(mx == SENT) != 0, 0)) {
#pragma unroll
        for (int i = 0; i < 4; ++i) ma[i] = mb[i];
        while (true) {
          mx = max(max(max4(ma[0]), max4(ma[1])), max(max4(ma[2]), max4(ma[3])));
          if (__builtin_amdgcn_ballot_w64(mx == SENT) == 0) break;
          if (SLOW()) break;
          asm volatile("" ::: "memory");
#pragma unroll
          for (int i = 0; i < 4; ++i) ma[i] = __builtin_amdgcn_raw_buffer_load_b128(ex, vA + i * 1024, soA, SC1);
        }
        ++spins;
      }
    }
    SPINS(0, spins); spins = 0;
    STAMP(1);
    bf16x8 mf[8];
#pragma unroll
    for (int ks = 0; ks < 8; ++ks) {
      u32x4 t = ma[ks >> 1];
      if (ks & 1) {
#pragma unroll
        for (int e = 0; e < 4; ++e) t[e] = ror8(ma[ks >> 1][e]);
      }
      mf[ks] = asb(t);
    }
    {
      f32x4 acc[4];
      NOP_IN();
#pragma unroll
      for (int ks = 0; ks < 8; ++ks) {
        if (ks == 1) { PIN(mf[1]); PIN(mf[3]); PIN(mf[5]); PIN(mf[7]); NOP_IN(); }
        if (ks == 0) {
          MFMA_AV0(acc[0], wf[0][0][0], mf[0]); MFMA_AV0(acc[1], wf[0][1][0], mf[0]);
          MFMA_AV0(acc[2], wf[1][0][0], mf[0]); MFMA_AV0(acc[3], wf[1][1][0], mf[0]);
        } else {
          MFMA_AV(acc[0], wf[0][0][ks], mf[ks]); MFMA_AV(acc[1], wf[0][1][ks], mf[ks]);
          MFMA_AV(acc[2], wf[1][0][ks], mf[ks]); MFMA_AV(acc[3], wf[1][1][ks], mf[ks]);
          if (ks == 1) fetch_k(s);
          if (ks == 4) fetch_v(s);
        }
      }
      asm volatile("s_nop 7" ::: "memory");
#pragma unroll
      for (int t = 0; t < 4; ++t) { PIN(acc[t]); *(LAS f32x4*)(l_part + ((w * 6 + t) * 64 + lane) * 16) = acc[t]; }
    }
    STAMP(2);
    BAR();
    STAMP(3);
    auto publish = [&]() {
      f32x4 p[8];
#pragma unroll
      for (int ww = 0; ww < 4; ++ww) {
        p[2 * ww] = *(const LAS f32x4*)(l_part + lRed + ww * 6144);
        p[2 * ww + 1] = *(const LAS f32x4*)(l_part + lRed + ww * 6144 + 1024);
      }
#pragma unroll
      for (int i = 0; i < 8; ++i) PIN(p[i]);
      f32x4 s0 = (p[0] + p[2]) + (p[4] + p[6]), s1 = (p[1] + p[3]) + (p[5] + p[7]);
      u32x4 o = (u32x4){nosent(pk2(s0[0], s0[1])), nosent(pk2(s0[2], s0[3])), nosent(pk2(s1[0], s1[1])), nosent(pk2(s1[2], s1[3]))};
      __builtin_amdgcn_raw_buffer_store_b128(o, ex, vBs, soBw, SC1);
    };
    if (w == 0) publish();
    if (tid < 96 && s + 4 < NSEG) __builtin_amdgcn_raw_buffer_store_b128((u32x4){SENT, SENT, SENT, SENT}, ex, vBs, offB(pol, s + 4, head) + j * 1536, SC1);
    if (s > 0) {
      __builtin_amdgcn_raw_buffer_store_b128((u32x4){SENT, SENT, SENT, SENT}, ex, vCs, soCp, SC1);
    }
    {
      f32x4 acc[2];
      NOP_IN();
#pragma unroll
      for (int ks = 0; ks < 8; ++ks) {
        if (ks == 0) { MFMA_VA0(acc[0], mf[0], wf[2][0][0]); MFMA_VA0(acc[1], mf[0], wf[2][1][0]); }
        else { MFMA_VA(acc[0], mf[ks], wf[2][0][ks]); MFMA_VA(acc[1], mf[ks], wf[2][1][ks]); }
      }
      asm volatile("s_nop 7" ::: "memory");
#pragma unroll
      for (int t = 0; t < 2; ++t) { PIN(acc[t]); *(LAS f32x4*)(l_part + ((w * 6 + 4 + t) * 64 + lane) * 16) = acc[t]; }
    }
    BAR();
    if (w == 1 && lane < 32) publish();
    STAMP(4);
    if (s == NSEG - 1) break;
    sleepn(DLY_B);
    u32x4 rqk, rv;
    while (true) {
      asm volatile("" ::: "memory");
      rqk = __builtin_amdgcn_raw_buffer_load_b128(ex, vQK, soBr, SC1);
      u32 mx = max4(rqk);
      if (__builtin_expect(__builtin_amdgcn_ballot_w64(mx == SENT) == 0, 1)) break;
      if (SLOW()) break;
    }
    SPINS(1, spins); spins = 0;
    rv = __builtin_amdgcn_raw_buffer_load_b128(ex, vV, soBr, SC1);
    asm volatile("" ::: "memory");
    STAMP(5);
    {
      f32x4 t0, t1, t2;
      MFMA_VV0(t0, kx[0], rqk); MFMA_VV0(t1, kx[1], rqk); MFMA_VV0(t2, rqk, rqk);
      asm volatile("s_nop 5" ::: "memory");
      PIN(t0); PIN(t1);
      *(LAS f32x4*)(l_sp + ((w * 3 + 0) * 64 + lane) * 16) = t0;
      *(LAS f32x4*)(l_sp + ((w * 3 + 1) * 64 + (lane ^ 8)) * 16) = t1;
      asm volatile("s_nop 1" ::: "memory");
      PIN(t2);
      *(LAS f32x4*)(l_sp + ((w * 3 + 2) * 64 + lane) * 16) = t2;
    }
    BAR();
    STAMP(6);
    {
      const int hi = c >> 3;
      f32x4 pa[4]; f32x2 pb[4];
#pragma unroll
      for (int ww = 0; ww < 4; ++ww) {
        pa[ww] = *(const LAS f32x4*)(l_sp + lSa + ww * 3072);
        pb[ww] = *(const LAS f32x2*)(l_sp + lSb + ww * 3072);
      }
#pragma unroll
      for (int ww = 0; ww < 4; ++ww) { PIN(pa[ww]); PIN(pb[ww]); }
      if (j == 0 && tid == 0) __hip_atomic_store(ctrl + CW_PROG + head * 32, (u32)(s + 1), RLX_AGENT);
      f32x4 ta = (pa[0] + pa[1]) + (pa[2] + pa[3]); f32x2 tb = (pb[0] + pb[1]) + (pb[2] + pb[3]);
      float sc[6] = {ta[0], ta[1], ta[2], ta[3], tb[0], tb[1]};
      SUB(11, sc[0]);
      const float scale = 0.08838834764831845f;
#pragma unroll
      for (int e = 0; e < 6; e += 2) {
        u32 r = pk2(sc[e], sc[e + 1]);
        r = pk2(__uint_as_float(r << 16) * scale, __uint_as_float(r & 0xffff0000u) * scale);
        sc[e] = __uint_as_float(r << 16); sc[e + 1] = __uint_as_float(r & 0xffff0000u);
      }
      if (g < 2) { sc[4] = -INFINITY; sc[5] = -INFINITY; }
      float m = fmaxf(fmaxf(fmaxf(sc[0], sc[1]), fmaxf(sc[2], sc[3])), fmaxf(sc[4], sc[5]));
      m = fmaxf(m, ror8f(m));
      m = xmax32(xmax16(m));
      SUB(12, m);
      const float l2e = 1.4426950408889634f;
      const float ml = m * l2e;
#pragma unroll
      for (int i = 0; i < 6; ++i) { sc[i] = __builtin_amdgcn_exp2f(__builtin_fmaf(sc[i], l2e, -ml)); }
      float sum = ((sc[0] + sc[1]) + (sc[2] + sc[3])) + (sc[4] + sc[5]);
      sum += ror8f(sum);
      sum = xadd32(xadd16(sum));
      SUB(13, sum);
      const float inv = __builtin_amdgcn_rcpf(sum);
      const u32 a0 = pk2(sc[0] * inv, sc[1] * inv), a1 = pk2(sc[2] * inv, sc[3] * inv), a2 = pk2(sc[4] * inv, sc[5] * inv);
      const u32 b0 = ror8(a0), b1 = ror8(a1), b2 = ror8(a2);
      u32x4 pp = (u32x4){hi ? b0 : a0, hi ? b1 : a1, hi ? a0 : b0, hi ? a1 : b1};
      u32x2 p2 = (u32x2){hi ? b2 : a2, hi ? a2 : b2};
      while (__builtin_expect(__builtin_amdgcn_ballot_w64(max4(rv) == SENT) != 0, 0)) {
        if (SLOW()) break;
        asm volatile("" ::: "memory");
        rv = __builtin_amdgcn_raw_buffer_load_b128(ex, vV, soBr, SC1);
      }
      spins = 0;
      u32x2 vm0 = (u32x2){g >= 2 ? rv[0] : 0u, g >= 2 ? rv[1] : 0u}, vm1 = (u32x2){g >= 2 ? rv[2] : 0u, g >= 2 ? rv[3] : 0u};
      PIN(pp); PIN(p2); PIN(vm0); PIN(vm1);
      STAMP(14);
      f32x4 o0, o1;
      NOP_IN();
      MFMA_VV0(o0, vx[0], pp); MFMA_VV0(o1, vx[1], pp);
      asm volatile("s_nop 3" ::: "memory");
      MFMA16_VV(o0, vm0, p2); MFMA16_VV(o1, vm1, p2);
      asm volatile("s_nop 7" ::: "memory");
      PIN(o0); PIN(o1);
      STAMP(15);
      u32x4 of = (u32x4){pk2(o0[0], o0[1]), pk2(o0[2], o0[3]), pk2(o1[0], o1[1]), pk2(o1[2], o1[3])};
      *(LAS u32x4*)(l_og + (w * 64 + lane) * 16) = of;
      STAMP(16);
    }
    BAR();
    STAMP(7);
    {
      u32x4 of[4];
#pragma unroll
      for (int ks = 0; ks < 4; ++ks) of[ks] = *(const LAS u32x4*)(l_og + (ks * 64 + lane) * 16);
      f32x4 acc[4];
#pragma unroll
      for (int ks = 0; ks < 4; ++ks) {
        bf16x8 o8 = asb(of[ks]);
#pragma unroll
        for (int ct = 0; ct < 4; ++ct) {
          if (ks == 0) MFMA_VV0(acc[ct], wo[ct][0], o8); else MFMA_AV(acc[ct], wo[ct][ks], o8);
        }
      }
      asm volatile("s_nop 7" ::: "memory");
      PIN(acc[0]); PIN(acc[1]); PIN(acc[2]); PIN(acc[3]);
      {
        const f32x4 v0 = c < 8 ? acc[0] : acc[2], v1 = c < 8 ? acc[1] : acc[3];
        __builtin_amdgcn_raw_buffer_store_b128((u32x4){nosent(pk2(v0[0], v0[1])), nosent(pk2(v0[2], v0[3])), nosent(pk2(v1[0], v1[1])), nosent(pk2(v1[2], v1[3]))},
                                               ex, vCs, soCw, SC1);
      }
    }
    STAMP(8);
    sleepn(DLY_C);
    {
      u32x2 r0, r1;
      {
        r0 = __builtin_amdgcn_raw_buffer_load_b64(ex, vCr, soCr, SC1);
        r1 = __builtin_amdgcn_raw_buffer_load_b64(ex, vCr + 2048, soCr, SC1);
        sleepn(GAP_C);
        asm volatile("" ::: "memory");
        u32x2 q0 = __builtin_amdgcn_raw_buffer_load_b64(ex, vCr, soCr, SC1);
        u32x2 q1 = __builtin_amdgcn_raw_buffer_load_b64(ex, vCr + 2048, soCr, SC1);
        u32 mx = max(max(r0[0], r0[1]), max(r1[0], r1[1]));
        if (__builtin_expect(__builtin_amdgcn_ballot_w64(mx == SENT) != 0, 0)) {
          r0 = q0; r1 = q1;
          while (true) {
            mx = max(max(r0[0], r0[1]), max(r1[0], r1[1]));
            if (__builtin_amdgcn_ballot_w64(mx == SENT) == 0) break;
            if (SLOW()) break;
            asm volatile("" ::: "memory");
            r0 = __builtin_amdgcn_raw_buffer_load_b64(ex, vCr, soCr, SC1);
            r1 = __builtin_amdgcn_raw_buffer_load_b64(ex, vCr + 2048, soCr, SC1);
          }
          ++spins;
        }
      }
      SPINS(2, spins); spins = 0;
      STAMP(9);
      float x0 = __uint_as_float(r0[0] << 16) + __uint_as_float(r1[0] << 16), x1 = __uint_as_float(r0[0] & 0xffff0000u) + __uint_as_float(r1[0] & 0xffff0000u);
      float x2 = __uint_as_float(r0[1] << 16) + __uint_as_float(r1[1] << 16), x3 = __uint_as_float(r0[1] & 0xffff0000u) + __uint_as_float(r1[1] & 0xffff0000u);
      asm volatile("s_nop 1\n\t"
                   "v_add_f32_dpp %0, %0, %0 row_ror:8 row_mask:0xf bank_mask:0xf\n\tv_add_f32_dpp %1, %1, %1 row_ror:8 row_mask:0xf bank_mask:0xf\n\t"
                   "v_add_f32_dpp %2, %2, %2 row_ror:8 row_mask:0xf bank_mask:0xf\n\tv_add_f32_dpp %3, %3, %3 row_ror:8 row_mask:0xf bank_mask:0xf\n\t"
                   "v_add_f32_dpp %0, %0, %0 row_ror:4 row_mask:0xf bank_mask:0xf\n\tv_add_f32_dpp %1, %1, %1 row_ror:4 row_mask:0xf bank_mask:0xf\n\t"
                   "v_add_f32_dpp %2, %2, %2 row_ror:4 row_mask:0xf bank_mask:0xf\n\tv_add_f32_dpp %3, %3, %3 row_ror:4 row_mask:0xf bank_mask:0xf\n\t"
                   "v_add_f32_dpp %0, %0, %0 row_ror:2 row_mask:0xf bank_mask:0xf\n\tv_add_f32_dpp %1, %1, %1 row_ror:2 row_mask:0xf bank_mask:0xf\n\t"
                   "v_add_f32_dpp %2, %2, %2 row_ror:2 row_mask:0xf bank_mask:0xf\n\tv_add_f32_dpp %3, %3, %3 row_ror:2 row_mask:0xf bank_mask:0xf\n\t"
                   "v_add_f32_dpp %0, %0, %0 row_ror:1 row_mask:0xf bank_mask:0xf\n\tv_add_f32_dpp %1, %1, %1 row_ror:1 row_mask:0xf bank_mask:0xf\n\t"
                   "v_add_f32_dpp %2, %2, %2 row_ror:1 row_mask:0xf bank_mask:0xf\n\tv_add_f32_dpp %3, %3, %3 row_ror:1 row_mask:0xf bank_mask:0xf\n\t"
                   "s_nop 0"
                   : "+v"(x0), "+v"(x1), "+v"(x2), "+v"(x3));
      SUB(17, x0);
      if (c == 0) *(LAS u32x2*)(l_red + (4 * w + g) * 8) = (u32x2){nosent(pk2(x0, x1)), nosent(pk2(x2, x3))};
    }
    BAR();
    STAMP(18);
    if (tid < 8) {
      u32x4 o = *(const LAS u32x4*)(l_red + tid * 16);
      __builtin_amdgcn_raw_buffer_store_b128(o, ex, (cu & 63) * 128 + tid * 16, soAw, SC1);
    }
    STAMP(10);
    soA = soAn;
  }
}
__device__ __forceinline__ void role_xa(const Args& a, const int idx) {
  const int tid = threadIdx.x, lane = tid & 63, w = __builtin_amdgcn_readfirstlane(tid >> 6);
  const int c = lane & 15, g = lane >> 4;
  const int head = idx * 4 + w;
  u32* ctrl = a.ctrl;
  const __amdgpu_buffer_rsrc_t ex = __builtin_amdgcn_make_buffer_rsrc(a.ex, 0, (int)EX_BYTES, 0x00020000);
  const __amdgpu_buffer_rsrc_t rq = __builtin_amdgcn_make_buffer_rsrc(a.ex + WS_Q_OFF, 0, (int)(4 * QKV_SZ), 0x00020000);
  const u32 pol = wait_pol(ctrl);
  sleepn(400);
  wait_ge(ctrl, CW_INIT, 128, 32);
  const u32 vRow = (u32)((c * HD + head * DH + 8 * g) * 2);
  const u32 vVx = (u32)(2 * QKV_SZ + ((size_t)(head * DH + c) * 32 + 8 * g) * 2);
  const u32 vQK = c * 64 + g * 16;
  const u32 vV = 1024 + (c * 2 + (g & 1)) * 16;
  u32x4 qf[2][4], kf[2][4], vf[8], km[4], vm[4];
  u32 known = 0;
  bool have = false;
  auto fetchm = [&](int s) {
    const u32 so = offB(pol, s, head);
#pragma unroll
    for (int jj = 0; jj < 4; ++jj) {
      km[jj] = __builtin_amdgcn_raw_buffer_load_b128(ex, vQK + jj * 1536, so, SC1);
      vm[jj] = __builtin_amdgcn_raw_buffer_load_b128(ex, vV + jj * 1536, so, SC1);
    }
  };
  auto flags = [&](int s) {
    if ((s & 7) == 0) {
      const int f = CW_KVFLAG + (s >> 3) * 48 + (head >> 1);
      wait_ge(ctrl, f, 2, 8); wait_ge(ctrl, f + 16, 2, 8); wait_ge(ctrl, f + 32, 2, 8);
    }
    asm volatile("" ::: "memory");
  };
  auto fetchq = [&](int s) {
#pragma unroll
    for (int t = 0; t < 2; ++t)
#pragma unroll
      for (int ks = 0; ks < 4; ++ks) qf[t][ks] = __builtin_amdgcn_raw_buffer_load_b128(rq, vRow + t * 16 * HD * 2 + ks * 64, s * 32 * HD * 2, SC1);
  };
  auto fetchk = [&](int s) {
#pragma unroll
    for (int t = 0; t < 2; ++t)
#pragma unroll
      for (int ks = 0; ks < 4; ++ks) kf[t][ks] = __builtin_amdgcn_raw_buffer_load_b128(rq, vRow + t * 16 * HD * 2 + ks * 64, (u32)QKV_SZ + s * 32 * HD * 2, SC1);
  };
  auto fetchv = [&](int s) {
#pragma unroll
    for (int t = 0; t < 8; ++t) vf[t] = __builtin_amdgcn_raw_buffer_load_b128(rq, vVx + t * 16 * 64, s * (NH * DH * 64), SC1);
  };
  flags(0); fetchq(0); fetchk(0); fetchv(0);
  for (int s = 0; s < NSEG; ++s) {
    f32x4 st[2][3];
#pragma unroll
    for (int qt = 0; qt < 2; ++qt)
#pragma unroll
      for (int t = 0; t < 2; ++t) {
        f32x4 acc = (f32x4){0, 0, 0, 0};
#pragma unroll
        for (int ks = 0; ks < 4; ++ks) acc = MFMA32(asb(kf[t][ks]), asb(qf[qt][ks]), acc);
        st[qt][t] = acc;
      }
    if (s + 1 < NSEG) { flags(s + 1); fetchk(s + 1); }
    {
      const u32 need = (u32)(s + 1 < NSEG ? s + 1 : NSEG - 1);
      if (!have && known < need) {
        for (int spins = 0;; ++spins) {
          asm volatile("" ::: "memory");
          known = __builtin_amdgcn_readfirstlane(__hip_atomic_load(ctrl + CW_PROG + head * 32, RLX_AGENT));
          if (known >= need) break;
          if (spins > SPIN_CAP) { set_dead(ctrl); break; }
          if ((spins & 63) == 63 && __builtin_amdgcn_readfirstlane((int)is_dead(ctrl))) break;
          sleepn(4);
        }
      }
      int spins = 0;
      while (true) {
        asm volatile("" ::: "memory");
        if (!have) fetchm(s);
        have = false;
        u32 mx = 0;
#pragma unroll
        for (int jj = 0; jj < 4; ++jj) mx = max(mx, max(max4(km[jj]), max4(vm[jj])));
        if (__builtin_amdgcn_ballot_w64(mx == SENT) == 0) break;
        if (POLL_FAIL(spins)) break;
        sleepn(2);
      }
    }
    bf16x8 pb32[2]; bf16x4 pb16[2];
#pragma unroll
    for (int qt = 0; qt < 2; ++qt) {
      f32x4 acc = (f32x4){0, 0, 0, 0};
#pragma unroll
      for (int ks = 0; ks < 4; ++ks) acc = MFMA32(asb(km[ks]), asb(qf[qt][ks]), acc);
      st[qt][2] = acc;
    }
    asm volatile("" ::: "memory");
    if (s + 1 < NSEG) fetchq(s + 1);
#pragma unroll
    for (int qt = 0; qt < 2; ++qt) {
      float sc[12];
      const float scale = 0.08838834764831845f;
      const int qi = qt * 16 + c;
#pragma unroll
      for (int i = 0; i < 3; ++i)
#pragma unroll
        for (int e = 0; e < 4; e += 2) {
          u32 r = pk2(st[qt][i][e], st[qt][i][e + 1]);
          float x0 = __uint_as_float(r << 16) * scale, x1 = __uint_as_float(r & 0xffff0000u) * scale;
          r = pk2(x0, x1);
          sc[i * 4 + e] = __uint_as_float(r << 16); sc[i * 4 + e + 1] = __uint_as_float(r & 0xffff0000u);
        }
#pragma unroll
      for (int i = 0; i < 2; ++i)
#pragma unroll
        for (int e = 0; e < 4; ++e) if (i * 16 + 4 * g + e > qi) sc[i * 4 + e] = -INFINITY;
      if (g < 2) { sc[8] = -INFINITY; sc[9] = -INFINITY; sc[10] = -INFINITY; sc[11] = -INFINITY; }
      float m = sc[0];
#pragma unroll
      for (int i = 1; i < 12; ++i) m = fmaxf(m, sc[i]);
      m = xmax32(xmax16(m));
      const float l2e = 1.4426950408889634f;
      float sum = 0.f;
#pragma unroll
      for (int i = 0; i < 12; ++i) { sc[i] = __builtin_amdgcn_exp2f((sc[i] - m) * l2e); sum += sc[i]; }
      sum = xadd32(xadd16(sum));
      const float inv = __builtin_amdgcn_rcpf(sum);
      u32x4 pp = (u32x4){pk2(sc[0] * inv, sc[1] * inv), pk2(sc[2] * inv, sc[3] * inv), pk2(sc[4] * inv, sc[5] * inv), pk2(sc[6] * inv, sc[7] * inv)};
      u32x2 p2 = (u32x2){pk2(sc[8] * inv, sc[9] * inv), pk2(sc[10] * inv, sc[11] * inv)};
      pb32[qt] = asb(pp); pb16[qt] = __builtin_bit_cast(bf16x4, p2);
    }
#pragma unroll
    for (int m = 0; m < 4; ++m) {
      u32x2 vm0 = (u32x2){g >= 2 ? vm[m][0] : 0u, g >= 2 ? vm[m][1] : 0u}, vm1 = (u32x2){g >= 2 ? vm[m][2] : 0u, g >= 2 ? vm[m][3] : 0u};
#pragma unroll
      for (int qt = 0; qt < 2; ++qt) {
        f32x4 z = (f32x4){0, 0, 0, 0};
        f32x4 o0 = MFMA32(asb(vf[2 * m]), pb32[qt], z), o1 = MFMA32(asb(vf[2 * m + 1]), pb32[qt], z);
        o0 = MFMA16(__builtin_bit_cast(bf16x4, vm0), pb16[qt], o0);
        o1 = MFMA16(__builtin_bit_cast(bf16x4, vm1), pb16[qt], o1);
        u32x4 of = (u32x4){pk2(o0[0], o0[1]), pk2(o0[2], o0[3]), pk2(o1[0], o1[1]), pk2(o1[2], o1[3])};
        __builtin_amdgcn_raw_buffer_store_b128(of, rq, lane * 16 + (m * 2 + qt) * 1024, (u32)(3 * QKV_SZ) + (s * 128 + head * 4) * 2048, SC1);
      }
    }
    asm volatile("" ::: "memory");
    if (s + 1 < NSEG) {
      fetchv(s + 1);
      if (known >= (u32)(s + 2)) { fetchm(s + 1); have = true; asm volatile("s_waitcnt vmcnt(16)" ::: "memory"); }
      else asm volatile("s_waitcnt vmcnt(8)" ::: "memory");
    } else WAIT_VM0();
    if (lane == 0) __hip_atomic_fetch_add(ctrl + CW_OXCNT + s * 32, 1u, RLX_AGENT);
  }
}

__device__ __forceinline__ void role_xo(const Args& a, char LAS* smem, const int cs) {
  const int tid = threadIdx.x, lane = tid & 63, w = __builtin_amdgcn_readfirstlane(tid >> 6);
  const int c = lane & 15, g = lane >> 4;
  u32* ctrl = a.ctrl;
  const __amdgpu_buffer_rsrc_t ro = __builtin_amdgcn_make_buffer_rsrc(a.ex + WS_OX_OFF, 0, (int)QKV_SZ, 0x00020000);
  bf16x8 wo[32][2];
  sleepn(500);
  {
    const int q = c >> 2, p = lane & 3;
#pragma unroll
    for (int half = 0; half < 2; ++half) {
      const u16* W = a.Wo + (size_t)(2048 * half) * DM + 32 * cs;
#pragma unroll
      for (int b = 0; b < 2; ++b) {
        u32x4 st[16];
#pragma unroll
        for (int i = 0; i < 16; ++i) st[i] = *(const u32x4*)(W + (size_t)(b * 1024 + i * 64 + (tid >> 2)) * DM + (tid & 3) * 8);
#pragma unroll
        for (int i = 0; i < 16; ++i) *(LAS u32x4*)(smem + (b * 1024 + i * 64 + (tid >> 2)) * 64 + (tid & 3) * 16) = st[i];
      }
      __syncthreads();
      if ((w >> 1) == half) {
#pragma unroll
        for (int ks = 0; ks < 32; ++ks)
#pragma unroll
          for (int ct = 0; ct < 2; ++ct) {
            const char LAS* ad = smem + ((w & 1) * 1024 + 32 * ks + 4 * g + q) * 64 + (16 * ct + 4 * p) * 2;
            wo[ks][ct] = cat8(trread(ad), trread(ad + 16 * 64));
          }
      }
      __syncthreads();
    }
  }
  char LAS* l_p = smem;
  for (int s = 0; s < NSEG; ++s) {
    if (w == 0) wait_ge(ctrl, CW_OXCNT + s * 32, NH, 4);
    __syncthreads();
    f32x4 acc[2][2];
    const u32 so = (u32)(s * 128 + w * 32) * 2048;
    u32x4 of[2][8][2];
#pragma unroll
    for (int kb = 0; kb < 2; ++kb)
#pragma unroll
      for (int k = 0; k < 8; ++k)
#pragma unroll
        for (int qt = 0; qt < 2; ++qt) of[kb][k][qt] = __builtin_amdgcn_raw_buffer_load_b128(ro, lane * 16 + ((kb * 8 + k) * 2 + qt) * 1024, so, SC1);
#pragma unroll
    for (int kb = 0; kb < 4; ++kb) {
#pragma unroll
      for (int k = 0; k < 8; ++k) {
#pragma unroll
        for (int qt = 0; qt < 2; ++qt)
#pragma unroll
          for (int ct = 0; ct < 2; ++ct) {
            bf16x8 o8 = asb(of[kb & 1][k][qt]);
            if (kb == 0 && k == 0) MFMA_AV0(acc[qt][ct], wo[0][ct], o8); else MFMA_AV(acc[qt][ct], wo[kb * 8 + k][ct], o8);
          }
        if (kb < 2) {
#pragma unroll
          for (int qt = 0; qt < 2; ++qt) of[kb & 1][k][qt] = __builtin_amdgcn_raw_buffer_load_b128(ro, lane * 16 + (((kb + 2) * 8 + k) * 2 + qt) * 1024, so, SC1);
        }
      }
    }
    asm volatile("s_nop 7" ::: "memory");
#pragma unroll
    for (int i = 0; i < 4; ++i) { PIN(acc[i >> 1][i & 1]); *(LAS f32x4*)(l_p + ((w * 4 + i) * 64 + lane) * 16) = acc[i >> 1][i & 1]; }
    __syncthreads();
    {
      f32x4 v = *(const LAS f32x4*)(l_p + ((0 * 4 + w) * 64 + lane) * 16) + *(const LAS f32x4*)(l_p + ((1 * 4 + w) * 64 + lane) * 16) +
                (*(const LAS f32x4*)(l_p + ((2 * 4 + w) * 64 + lane) * 16) + *(const LAS f32x4*)(l_p + ((3 * 4 + w) * 64 + lane) * 16));
      u32x2 o = (u32x2){pk2(v[0], v[1]), pk2(v[2], v[3])};
      *(u32x2*)(a.out + (size_t)(s * 32 + (w >> 1) * 16 + c) * OUTLD + 32 * cs + 16 * (w & 1) + 4 * g) = o;
    }
  }
}

#define G_STAGE 32768
template <bool VMAT, int NI>
__device__ __forceinline__ void gemm_tile(const Args& a, char LAS* smem, const u16* __restrict__ W, const int m0, const int n0, const int mat) {
  const int tid = threadIdx.x, lane = tid & 63, w = __builtin_amdgcn_readfirstlane(tid >> 6);
  const int c = lane & 15, g = lane >> 4, q = c >> 2, p = lane & 3;
  const int wm = w & 1, wn = w >> 1;
  const __amdgpu_buffer_rsrc_t rx = __builtin_amdgcn_make_buffer_rsrc((void*)(a.x + (size_t)m0 * DM), 0, 256 * DM * 2, 0x00020000);
  const __amdgpu_buffer_rsrc_t rw = __builtin_amdgcn_make_buffer_rsrc((void*)(W + n0), 0, DM * HD * 2, 0x00020000);
  const u32 gA = (u32)(((64 * w + (lane >> 2)) * DM + (lane & 3) * 8) * 2);
  const u32 gB = (u32)(((8 * w + (lane >> 5)) * HD + (lane & 31) * 8) * 2);
  const u32 wA = (64 * w + (lane >> 2)) * 64 + (((lane & 3) ^ ((lane >> 3) & 3)) * 16);
  u32 wB[2];
#pragma unroll
  for (int h = 0; h < 2; ++h)
    wB[h] = 16384 + (8 * w + (lane >> 5)) * 512 + (((lane & 31) ^ (((2 * h + (lane >> 5)) << 2) | ((w & 1) << 1))) * 16);
  u32x4 rs[2][8];
  auto gload = [&](u32x4 (&r)[8], int st, int i) {
    if (NI == 8 || w < 2) r[i] = __builtin_amdgcn_raw_buffer_load_b128(rx, gA + i * 16 * DM * 2, st * 64, 0);
    r[4 + i] = __builtin_amdgcn_raw_buffer_load_b128(rw, gB + i * 2 * HD * 2, st * 32 * HD * 2, 0);
  };
  auto lwrite = [&](u32x4 (&r)[8], int st, int i) {
    char LAS* base = smem + (st & 3) * G_STAGE;
    if (NI == 8 || w < 2) *(LAS u32x4*)(base + wA + i * 1024) = r[i];
    *(LAS u32x4*)(base + wB[i & 1] + i * 1024) = r[4 + i];
  };
  f32x4 acc[NI][8];
#pragma unroll
  for (int i = 0; i < NI; ++i)
#pragma unroll
    for (int jn = 0; jn < 8; ++jn) acc[i][jn] = (f32x4){0, 0, 0, 0};
  const u32 aoff = (16 * NI * wm + c) * 64 + ((g ^ ((c >> 1) & 3)) * 16);
  const int fB = (q << 2) | ((g & 1) << 1);
  u32 boff[4];
#pragma unroll
  for (int u = 0; u < 4; ++u) {
    const int chunk = VMAT ? (16 * wn + 4 * u + (p >> 1)) : (16 * wn + 4 * u + p);
    boff[u] = 16384 + (8 * g + q) * 512 + ((chunk ^ fB) * 16) + (VMAT ? (p & 1) * 8 : 0);
  }
  bf16x8 af[2][8], bfr[2][8];
  auto rdA = [&](bf16x8 (&f)[8], int st, int i) { f[i] = *(const LAS bf16x8*)(smem + (st & 3) * G_STAGE + aoff + i * 1024); };
  auto rdB = [&](bf16x8 (&f)[8], int st, int jn) {
    const char LAS* ad = smem + (st & 3) * G_STAGE + (VMAT ? (boff[jn >> 1] ^ ((jn & 1) * 32)) : (boff[jn >> 1] + (jn & 1) * 8));
    f[jn] = cat8(trread(ad), trread(ad + 4 * 512));
  };
#pragma unroll
  for (int i = 0; i < 4; ++i) gload(rs[0], 0, i);
#pragma unroll
  for (int i = 0; i < 4; ++i) gload(rs[1], 1, i);
#pragma unroll
  for (int i = 0; i < 4; ++i) lwrite(rs[0], 0, i);
#pragma unroll
  for (int i = 0; i < 4; ++i) gload(rs[0], 2, i);
#pragma unroll
  for (int i = 0; i < 4; ++i) lwrite(rs[1], 1, i);
#pragma unroll
  for (int i = 0; i < 4; ++i) gload(rs[1], 3, i);
  BAR();
#pragma unroll
  for (int i = 0; i < 8; ++i) { if (i < NI) rdA(af[0], 0, i); rdB(bfr[0], 0, i); }
  auto stage = [&](int st, bf16x8 (&fa)[8], bf16x8 (&fb)[8], bf16x8 (&na)[8], bf16x8 (&nb)[8], u32x4 (&rn)[8], const bool RD, const bool WR, const bool LD) __attribute__((always_inline)) {
    BAR();
#pragma unroll
    for (int i = 0; i < 8; ++i) { if (i < NI) PIN(fa[i]); PIN(fb[i]); }
    NOP_IN();
#pragma unroll
    for (int i = 0; i < 8; ++i) {
#pragma unroll
      for (int jn = 0; jn < 8; ++jn) {
        if (NI == 8 || (jn & 1) == 0) { const int ii = NI == 8 ? i : (i >> 1), jj = NI == 8 ? jn : ((i & 1) * 4 + (jn >> 1));
          if (VMAT) MFMA_ACC(acc[ii][jj], fa[ii], fb[jj]); else MFMA_ACC(acc[ii][jj], fb[jj], fa[ii]); }
        if (jn == 1) { asm volatile("" ::: "memory"); if (RD) rdB(nb, st + 1, i); asm volatile("" ::: "memory"); }
        if (jn == 3) { asm volatile("" ::: "memory"); if (RD && i < NI) rdA(na, st + 1, i); asm volatile("" ::: "memory"); }
        if (jn == 5 && (i & 1) == 0) { asm volatile("" ::: "memory"); if (WR) lwrite(rn, st + 2, i >> 1); asm volatile("" ::: "memory"); }
        if (jn == 5 && (i & 1) == 1) { asm volatile("" ::: "memory"); if (LD) gload(rn, st + 4, i >> 1); asm volatile("" ::: "memory"); }
      }
    }
  };
  for (int st = 0; st < 28; st += 2) {
    stage(st, af[0], bfr[0], af[1], bfr[1], rs[0], true, true, true);
    stage(st + 1, af[1], bfr[1], af[0], bfr[0], rs[1], true, true, true);
  }
  stage(28, af[0], bfr[0], af[1], bfr[1], rs[0], true, true, false);
  stage(29, af[1], bfr[1], af[0], bfr[0], rs[1], true, true, false);
  stage(30, af[0], bfr[0], af[1], bfr[1], rs[0], true, false, false);
  stage(31, af[1], bfr[1], af[0], bfr[0], rs[1], false, false, false);
  asm volatile("s_nop 7" ::: "memory");
#pragma unroll
  for (int i = 0; i < NI; ++i)
#pragma unroll
    for (int jn = 0; jn < 8; ++jn) PINA(acc[i][jn]);
  const __amdgpu_buffer_rsrc_t rq = __builtin_amdgcn_make_buffer_rsrc(a.ex + WS_Q_OFF, 0, (int)(3 * QKV_SZ), 0x00020000);
  if (!VMAT) {
#pragma unroll
    for (int i = 0; i < NI; ++i)
#pragma unroll
      for (int u = 0; u < 4; u += 2) {
        u32x4 x = (u32x4){pk2(acc[i][2 * u][0], acc[i][2 * u][1]), pk2(acc[i][2 * u][2], acc[i][2 * u][3]),
                          pk2(acc[i][2 * u + 1][0], acc[i][2 * u + 1][1]), pk2(acc[i][2 * u + 1][2], acc[i][2 * u + 1][3])};
        u32x4 y0 = (u32x4){pk2(acc[i][2 * u + 2][0], acc[i][2 * u + 2][1]), pk2(acc[i][2 * u + 2][2], acc[i][2 * u + 2][3]),
                           pk2(acc[i][2 * u + 3][0], acc[i][2 * u + 3][1]), pk2(acc[i][2 * u + 3][2], acc[i][2 * u + 3][3])};
        u32x4 y, va, vb;
#pragma unroll
        for (int e = 0; e < 4; ++e) { y[e] = ror8(y0[e]); va[e] = c < 8 ? x[e] : y[e]; vb[e] = c < 8 ? y[e] : x[e]; }
        const u32 col = n0 + 128 * wn + 32 * u;
        const u32 offa = (u32)(((m0 + 16 * NI * wm + 16 * i + (c & 7)) * HD + col + 32 * (c >> 3) + 8 * g) * 2);
        const u32 offb = (u32)(((m0 + 16 * NI * wm + 16 * i + 8 + (c & 7)) * HD + col + 32 * (1 - (c >> 3)) + 8 * g) * 2);
        __builtin_amdgcn_raw_buffer_store_b128(va, rq, offa, (u32)(mat * QKV_SZ), SC1);
        __builtin_amdgcn_raw_buffer_store_b128(vb, rq, offb, (u32)(mat * QKV_SZ), SC1);
      }
  } else {
#pragma unroll
    for (int u = 0; u < NI / 2; ++u)
#pragma unroll
      for (int jn = 0; jn < 8; ++jn) {
        u32x4 o = (u32x4){pk2(acc[2 * u][jn][0], acc[2 * u][jn][1]), pk2(acc[2 * u][jn][2], acc[2 * u][jn][3]),
                          pk2(acc[2 * u + 1][jn][0], acc[2 * u + 1][jn][1]), pk2(acc[2 * u + 1][jn][2], acc[2 * u + 1][jn][3])};
        const int seg = (m0 + 16 * NI * wm + 32 * u) >> 5, col = n0 + 128 * wn + 16 * jn + c;
        u32 off = (u32)((((size_t)seg * HD + col) * 32 + 8 * g) * 2);
        __builtin_amdgcn_raw_buffer_store_b128(o, rq, off, (u32)(2 * QKV_SZ), SC1);
      }
  }
  WAIT_VM0();
  __syncthreads();
}

__device__ __forceinline__ void role_gemm(const Args& a, char LAS* smem, const int cx, const int rank) {
  {
    const int np = rank >> 1, mat = 1 + (np >> 2), n = mat * 16 + 4 * cx + (np & 3), n0 = (n & 15) * 256, m0 = (rank & 1) * 128;
    if (mat == 2) gemm_tile<true, 4>(a, smem, a.Wv, m0, n0, 2);
    else gemm_tile<false, 4>(a, smem, a.Wk, m0, n0, 1);
    if (threadIdx.x == 0) __hip_atomic_fetch_add(a.ctrl + CW_KVFLAG + n, 1u, RLX_AGENT);
  }
  for (int t = rank; t < 88; t += 16) {
    int np, m;
    if (t < 8) { np = t; m = 1; }
    else if (t < 16) { np = 8 + ((t - 8) >> 1); m = t & 1; }
    else { const int tt = t + 8, pr = tt / 24, r = tt - pr * 24; np = r >> 1; m = 2 * pr + (r & 1); }
    const int mat = ((np >> 2) + 1) % 3;
    const int n = mat * 16 + 4 * cx + (np & 3), n0 = (n & 15) * 256;
    if (mat == 2) gemm_tile<true, 8>(a, smem, a.Wv, m * 256, n0, 2);
    else gemm_tile<false, 8>(a, smem, mat == 0 ? a.Wq : a.Wk, m * 256, n0, mat);
    if (threadIdx.x == 0) __hip_atomic_store(a.ctrl + CW_KVFLAG + m * 48 + n, 2u, RLX_AGENT);
  }
}
#define S_UROW 4160
#define S_RROW 4192
#define S_LU 0
#define S_LR (8 * S_UROW)
#define S_LP (S_LR + 16 * S_RROW)
__device__ __forceinline__ void cexp_split(float are, float aim, float l, float cr, float ci, bool neg, float& xr, float& xi) {
  const float e = __builtin_amdgcn_exp2f(are * l * 1.4426950408889634f);
  const float rv = __builtin_amdgcn_fractf(aim * l * 0.15915494309189535f);
  const float pr = e * __builtin_amdgcn_cosf(rv), pi = e * __builtin_amdgcn_sinf(rv);
  xr = cr * pr - ci * pi; xi = cr * pi + ci * pr;
  if (neg) xi = -xi;
}
__device__ __forceinline__ void split2(float a, float b, u32& hi, u32& lo) {
  hi = pk2(a, b);
  lo = pk2(a - __uint_as_float(hi << 16), b - __uint_as_float(hi & 0xffff0000u));
}
__device__ __forceinline__ void role_ssm(const Args& a, char LAS* smem) {
  const int tid = threadIdx.x, lane = tid & 63, w = __builtin_amdgcn_readfirstlane(tid >> 6);
  const int c = lane & 15, g = lane >> 4;
  __syncthreads();
  for (int i = tid; i < (S_LP - S_LR) / 16; i += 256) *(LAS u32x4*)(smem + S_LR + i * 16) = (u32x4){0, 0, 0, 0};
  for (int i = tid; i < 8 * 4; i += 256) *(LAS u32x4*)(smem + S_LU + (i >> 2) * S_UROW + (i & 3) * 16) = (u32x4){0, 0, 0, 0};
  float la[3];
  la[0] = (float)c;
  la[1] = c < 8 ? (float)(256 + 2 * c) : (float)(512 + 256 * ((c - 8) >> 2) + 4 * ((c - 8) & 3));
  la[2] = c < 8 ? (float)(1024 + 256 * (c >> 1) + 8 * (c & 1)) : (c == 8 ? 2048.f : 0.f);
  while (true) {
    __syncthreads();
    if (tid == 0) *(LAS int*)(smem + S_LP) = (int)__hip_atomic_fetch_add(a.ctrl + CW_SSMQ, 1u, RLX_AGENT);
    __syncthreads();
    const int grp = __builtin_amdgcn_readfirstlane(*(LAS int*)(smem + S_LP));
    if (grp >= SSM_D / 8) break;
    const int d0 = grp * 8;
    for (int r = tid; r < SSM_L / 2; r += 256) {
      const u32x4 v0 = *(const u32x4*)(a.u + (size_t)(2 * r) * SSM_D + d0), v1 = *(const u32x4*)(a.u + (size_t)(2 * r + 1) * SSM_D + d0);
#pragma unroll
      for (int e = 0; e < 4; ++e) {
        *(LAS u32*)(smem + S_LU + (2 * e) * S_UROW + 64 + r * 4) = (v0[e] & 0xffffu) | (v1[e] << 16);
        *(LAS u32*)(smem + S_LU + (2 * e + 1) * S_UROW + 64 + r * 4) = (v0[e] >> 16) | (v1[e] & 0xffff0000u);
      }
    }
    for (int ch = 0; ch < 8; ++ch) {
      const int d = d0 + ch;
      const float dt = rbf(expf(bf2f(a.log_dt[d])));
      f32x4 ka[3];
#pragma unroll
      for (int t = 0; t < 3; ++t) ka[t] = (f32x4){0, 0, 0, 0};
#pragma unroll
      for (int ks = 0; ks < 2; ++ks) {
        const size_t o = (size_t)d * SSM_N + 32 * w + 16 * ks + 4 * g;
        const u32x2 Ar = *(const u32x2*)(a.A_re + o), Ai = *(const u32x2*)(a.A_im + o), Br = *(const u32x2*)(a.B_re + o),
                    Bi = *(const u32x2*)(a.B_im + o), Cr = *(const u32x2*)(a.C_re + o), Ci = *(const u32x2*)(a.C_im + o);
        float are[4], aim[4], cbr[4], cbi[4];
#pragma unroll
        for (int i = 0; i < 4; ++i) {
          auto el = [&](const u32x2& v) { u32 x = v[i >> 1]; return __uint_as_float((i & 1) ? (x & 0xffff0000u) : (x << 16)); };
          are[i] = dt * el(Ar); aim[i] = dt * el(Ai);
          const float br = dt * el(Br), bi = dt * el(Bi), cr = el(Cr), ci = el(Ci);
          cbr[i] = cr * br - ci * bi; cbi[i] = cr * bi + ci * br;
        }
        u32x4 yh, yl;
#pragma unroll
        for (int i = 0; i < 4; ++i) { float xr, xi; cexp_split(are[i], aim[i], (float)(16 * c), 1.f, 0.f, true, xr, xi); u32 h, l; split2(xr, xi, h, l); yh[i] = h; yl[i] = l; }
#pragma unroll
        for (int t = 0; t < 3; ++t) {
          u32x4 xh, xl;
#pragma unroll
          for (int i = 0; i < 4; ++i) { float xr, xi; cexp_split(are[i], aim[i], la[t], cbr[i], cbi[i], false, xr, xi); u32 h, l; split2(xr, xi, h, l); xh[i] = h; xl[i] = l; }
          ka[t] = MFMA32(asb(xh), asb(yh), ka[t]);
          ka[t] = MFMA32(asb(xh), asb(yl), ka[t]);
          ka[t] = MFMA32(asb(xl), asb(yh), ka[t]);
        }
      }
      __syncthreads();
#pragma unroll
      for (int t = 0; t < 3; ++t) *(LAS f32x4*)(smem + S_LP + ((w * 3 + t) * 64 + lane) * 16) = ka[t];
      __syncthreads();
      for (int k = 0; k < 8; ++k) {
        const int l = tid + 256 * k;
        const int lv = (int)__uint_as_float(pk2((float)l, 0.f) << 16);
        int t, r, ap;
        if (lv < 256) { t = 0; r = lv & 15; ap = lv >> 4; }
        else if (lv < 512) { const int x = lv - 256; t = 1; r = (x & 15) >> 1; ap = x >> 4; }
        else if (lv < 1024) { const int x = lv - 512; t = 1; r = 8 + 4 * (x >> 8) + ((x & 15) >> 2); ap = (x & 255) >> 4; }
        else if (lv < 2048) { const int x = lv - 1024; t = 2; r = 2 * (x >> 8) + ((x & 15) >> 3); ap = (x & 255) >> 4; }
        else { t = 2; r = 8; ap = 0; }
        const int po = (t * 64 + (r >> 2) * 16 + ap) * 16 + (r & 3) * 4;
        const float kv = (*(const LAS float*)(smem + S_LP + po) + *(const LAS float*)(smem + S_LP + 3072 + po)) +
                         (*(const LAS float*)(smem + S_LP + 6144 + po) + *(const LAS float*)(smem + S_LP + 9216 + po));
        const u32 h = pk2(kv, 0.f) & 0xffffu;
        const u32 lo = pk2(kv - __uint_as_float(h << 16), 0.f) & 0xffffu;
        const int m = 2047 - l + 8;
#pragma unroll
        for (int cp = 0; cp < 8; ++cp) {
          *(LAS u16*)(smem + S_LR + cp * S_RROW + (m - cp) * 2) = (u16)h;
          *(LAS u16*)(smem + S_LR + (8 + cp) * S_RROW + (m - cp) * 2) = (u16)lo;
        }
      }
      __syncthreads();
      const int b = lane & 31, h = lane >> 5;
      const int cp = (2047 - b) & 7;
      const char LAS* rk = smem + S_LR + cp * S_RROW + (2047 - b - cp + 8 + 8 * h) * 2;
      const char LAS* ru = smem + S_LU + ch * S_UROW + 64 + 16 * h;
      f32x16 y0, y1;
#pragma unroll
      for (int i = 0; i < 16; ++i) { y0[i] = 0.f; y1[i] = 0.f; }
      for (int t = w; t < 64; t += 4) {
#pragma unroll
        for (int ks = 0; ks < 2; ++ks) {
          const bf16x8 kh = *(const LAS bf16x8*)(rk - 64 * t + 32 * ks), kl = *(const LAS bf16x8*)(rk + 8 * S_RROW - 64 * t + 32 * ks);
          const bf16x8 u1 = *(const LAS bf16x8*)(ru + 64 * max(32 + b - t, -1) + 32 * ks);
          y1 = __builtin_amdgcn_mfma_f32_32x32x16_bf16(kh, u1, y1, 0, 0, 0);
          y1 = __builtin_amdgcn_mfma_f32_32x32x16_bf16(kl, u1, y1, 0, 0, 0);
          if (t < 32) {
            const bf16x8 u0 = *(const LAS bf16x8*)(ru + 64 * max(b - t, -1) + 32 * ks);
            y0 = __builtin_amdgcn_mfma_f32_32x32x16_bf16(kh, u0, y0, 0, 0, 0);
            y0 = __builtin_amdgcn_mfma_f32_32x32x16_bf16(kl, u0, y0, 0, 0, 0);
          }
        }
      }
      const float ds = bf2f(a.Dskip[d]);
#pragma unroll
      for (int cg = 0; cg < 2; ++cg) {
        __syncthreads();
#pragma unroll
        for (int i4 = 0; i4 < 4; ++i4) {
          const f32x16& y = cg ? y1 : y0;
          *(LAS f32x4*)(smem + S_LP + ((w * 4 + i4) * 64 + lane) * 16) = (f32x4){y[4 * i4], y[4 * i4 + 1], y[4 * i4 + 2], y[4 * i4 + 3]};
        }
        __syncthreads();
        const f32x4 v = *(const LAS f32x4*)(smem + S_LP + (w * 64 + lane) * 16) + *(const LAS f32x4*)(smem + S_LP + ((4 + w) * 64 + lane) * 16) +
                        (*(const LAS f32x4*)(smem + S_LP + ((8 + w) * 64 + lane) * 16) + *(const LAS f32x4*)(smem + S_LP + ((12 + w) * 64 + lane) * 16));
        const int l = 32 * (32 * cg + b) + 8 * w + 4 * h;
        LAS u32x2* up = (LAS u32x2*)(smem + S_LU + ch * S_UROW + 64 + l * 2);
        const u32x2 uv = *up;
        const float r0 = rbf(v[0]) + rbf(__uint_as_float(uv[0] << 16) * ds), r1 = rbf(v[1]) + rbf(__uint_as_float(uv[0] & 0xffff0000u) * ds);
        const float r2 = rbf(v[2]) + rbf(__uint_as_float(uv[1] << 16) * ds), r3 = rbf(v[3]) + rbf(__uint_as_float(uv[1] & 0xffff0000u) * ds);
        *up = (u32x2){pk2(r0, r1), pk2(r2, r3)};
      }
    }
    __syncthreads();
    for (int r = tid; r < SSM_L / 2; r += 256) {
      u32x4 v0, v1;
#pragma unroll
      for (int e = 0; e < 4; ++e) {
        const u32 x0 = *(const LAS u32*)(smem + S_LU + (2 * e) * S_UROW + 64 + r * 4), x1 = *(const LAS u32*)(smem + S_LU + (2 * e + 1) * S_UROW + 64 + r * 4);
        v0[e] = (x0 & 0xffffu) | (x1 << 16); v1[e] = (x0 >> 16) | (x1 & 0xffff0000u);
      }
      *(u32x4*)(a.out + (size_t)(2 * r) * OUTLD + DM + d0) = v0;
      *(u32x4*)(a.out + (size_t)(2 * r + 1) * OUTLD + DM + d0) = v1;
    }
  }
}
__global__ void __launch_bounds__(128) prep_kernel(u32* ctrl, char* ex) {
  __shared__ u32 best[2];
  const int b = blockIdx.x, tid = threadIdx.x, w = tid >> 6;
  for (int i = b * 128 + tid; i < CTRL_BYTES / 4; i += EX_PAGES * 128)
    if ((i < CW_POL || i >= CW_POL + 32) && (i < 6000 || i >= 6064)) ctrl[i] = 0;
  if (w == 0) {
    u32* p0 = (u32*)(ex + ((size_t)b << 21));
    u32 b0 = 0xffffffffu, b1 = 0xffffffffu;
    for (int rep = 0; rep < NPROBE; ++rep) {
      u32* pp = p0 + ((rep * 5) & 31) * 64;
      u64 t0 = __builtin_amdgcn_s_memtime();
      u32 r0 = __hip_atomic_fetch_or(pp, 0u, RLX_AGENT);
      asm volatile("s_waitcnt vmcnt(0)" :: "v"(r0) : "memory");
      u64 t1 = __builtin_amdgcn_s_memtime();
      u32 r1 = __hip_atomic_fetch_or(pp + 2048, 0u, RLX_AGENT);
      asm volatile("s_waitcnt vmcnt(0)" :: "v"(r1) : "memory");
      u64 t2 = __builtin_amdgcn_s_memtime();
      b0 = min(b0, (u32)(t1 - t0)); b1 = min(b1, (u32)(t2 - t1));
    }
    if (tid == 0) { best[0] = b0; best[1] = b1; }
  }
  if (tid == 0) {
    const u32 xcc = (u32)__builtin_amdgcn_s_getreg(20 | (3 << 11)) & 7u;
    const u32 fast1 = best[1] < best[0] ? 1u : 0u;
    ctrl[CW_POL + b] = xcc < 4 ? fast1 : (fast1 ^ 1u);
  }
}

__global__ void __launch_bounds__(256, 1) mega_kernel(Args a) {
  __shared__ __attribute__((aligned(16))) char smem_raw[163840];
  char LAS* smem = (char LAS*)smem_raw;
  const int tid = threadIdx.x;
  if (tid == 0) {
    const u32 xcc = (u32)__builtin_amdgcn_s_getreg(20 | (3 << 11)) & 7u;
    int role = -1;
    for (int k = 0; k < 8 && role < 0; ++k) {
      const u32 cc = (xcc + k) & 7u;
      const u32 t = __hip_atomic_fetch_add(a.ctrl + CW_TICKET + cc * 32, 1u, RLX_AGENT);
      if (t < 32) role = (int)(cc * 32 + t);
    }
    *(LAS int*)smem = role;
  }
  __syncthreads();
  const int role = __builtin_amdgcn_readfirstlane(*(LAS int*)smem);
  __syncthreads();
  if (role < 0) return;
  if (role < 128) role_rec(a, smem, role);
  else {
    const int cx = (role - 128) >> 5, t = role & 31;
    if (t < 8) role_xo(a, smem, cx * 8 + t);
    else if (t < 10) role_xa(a, cx * 2 + t - 8);
    else {
      if (t >= 16) role_gemm(a, smem, cx, t - 16);
      role_ssm(a, smem);
    }
  }
}

extern "C" void kernel_launch(void* const* d_in, const int* in_sizes, int n_in, void* d_out, int out_size,
                              void* d_ws, size_t ws_size, hipStream_t stream) {
  Args a;
  a.x = (const u16*)d_in[0]; a.mem0 = (const u16*)d_in[1];
  a.Wq = (const u16*)d_in[2]; a.Wk = (const u16*)d_in[3]; a.Wv = (const u16*)d_in[4]; a.Wo = (const u16*)d_in[5];
  a.u = (const u16*)d_in[7]; a.A_re = (const u16*)d_in[8]; a.A_im = (const u16*)d_in[9];
  a.B_re = (const u16*)d_in[10]; a.B_im = (const u16*)d_in[11]; a.C_re = (const u16*)d_in[12]; a.C_im = (const u16*)d_in[13];
  a.log_dt = (const u16*)d_in[14]; a.Dskip = (const u16*)d_in[15];
  a.out = (u16*)d_out;
  char* ws = (char*)d_ws;
  a.ctrl = (u32*)ws;
  a.ex = (char*)(((uintptr_t)ws + CTRL_BYTES + (2u << 20) - 1) & ~(uintptr_t)((2u << 20) - 1));
  prep_kernel<<<EX_PAGES, 128, 0, stream>>>(a.ctrl, a.ex);
  mega_kernel<<<256, 256, 0, stream>>>(a);
}
```

```cpp
#include <hip/hip_runtime.h>
#include <hip/hip_bf16.h>
#include <stdint.h>

#define S_LEN 2048
#define DM 1024
#define NH 32
#define DH 128
#define HD 4096
#define NSEG 64
#define SSM_L 2048
#define SSM_D 1024
#define SSM_N 128
#define OUTLD 2048

typedef unsigned short u16;
typedef unsigned int u32;
typedef unsigned long long u64;
typedef __attribute__((ext_vector_type(8))) short bf16x8;
typedef __attribute__((ext_vector_type(4))) short bf16x4;
typedef __attribute__((ext_vector_type(4))) float f32x4;
typedef __attribute__((ext_vector_type(16))) float f32x16;
typedef __attribute__((ext_vector_type(4))) unsigned int u32x4;
typedef __attribute__((ext_vector_type(2))) unsigned int u32x2;
typedef __attribute__((ext_vector_type(2))) float f32x2;
typedef __attribute__((ext_vector_type(2))) __bf16 bf2_t;
typedef short v4i16_t __attribute__((ext_vector_type(4)));
#define LAS __attribute__((address_space(3)))

#define SENT 0xFFFFFFFFu
#define RLX_AGENT __ATOMIC_RELAXED, __HIP_MEMORY_SCOPE_AGENT
#define SC1 16

__device__ __forceinline__ float bf2f(u16 v) { return __uint_as_float(((u32)v) << 16); }
__device__ __forceinline__ u32 pk2(float a, float b) {
  f32x2 f = {a, b};
  bf2_t h = __builtin_convertvector(f, bf2_t);
  return __builtin_bit_cast(u32, h);
}
__device__ __forceinline__ float rbf(float f) { return __uint_as_float(pk2(f, 0.f) << 16); }
__device__ __forceinline__ u32 nosent(u32 v) { return v < 0xFFFFFFFEu ? v : 0xFFFFFFFEu; }
__device__ __forceinline__ bool okv(u32x4 v) { return v[0] != SENT && v[1] != SENT && v[2] != SENT && v[3] != SENT; }
__device__ __forceinline__ bf16x8 asb(u32x4 v) { return __builtin_bit_cast(bf16x8, v); }
__device__ __forceinline__ void sleepn(int n) { for (int k = 0; k < n; ++k) __builtin_amdgcn_s_sleep(1); }
#define WAIT_VM0() asm volatile("s_waitcnt vmcnt(0)" ::: "memory")
#define BAR() do { asm volatile("s_waitcnt lgkmcnt(0)" ::: "memory"); __builtin_amdgcn_s_barrier(); asm volatile("" ::: "memory"); } while (0)

__device__ __forceinline__ float xmax16(float x) {
  auto r = __builtin_amdgcn_permlane16_swap(__float_as_uint(x), __float_as_uint(x), false, false);
  return fmaxf(__uint_as_float(r[0]), __uint_as_float(r[1]));
}
__device__ __forceinline__ float xmax32(float x) {
  auto r = __builtin_amdgcn_permlane32_swap(__float_as_uint(x), __float_as_uint(x), false, false);
  return fmaxf(__uint_as_float(r[0]), __uint_as_float(r[1]));
}
__device__ __forceinline__ float xadd16(float x) {
  auto r = __builtin_amdgcn_permlane16_swap(__float_as_uint(x), __float_as_uint(x), false, false);
  return __uint_as_float(r[0]) + __uint_as_float(r[1]);
}
__device__ __forceinline__ float xadd32(float x) {
  auto r = __builtin_amdgcn_permlane32_swap(__float_as_uint(x), __float_as_uint(x), false, false);
  return __uint_as_float(r[0]) + __uint_as_float(r[1]);
}
__device__ __forceinline__ u32 ror8(u32 x) { return (u32)__builtin_amdgcn_update_dpp(0, (int)x, 0x128, 0xf, 0xf, true); }
template <int N> __device__ __forceinline__ float rorf(float x) {
  return __uint_as_float((u32)__builtin_amdgcn_update_dpp(0, (int)__float_as_uint(x), 0x120 + N, 0xf, 0xf, true));
}
__device__ __forceinline__ float ror8f(float x) { return __uint_as_float(ror8(__float_as_uint(x))); }

#define MFMA32(a, b, c) __builtin_amdgcn_mfma_f32_16x16x32_bf16(a, b, c, 0, 0, 0)
#define MFMA16(a, b, c) __builtin_amdgcn_mfma_f32_16x16x16bf16_1k(a, b, c, 0, 0, 0)
#define MFMA_AV0(acc, wa, xb) asm volatile("v_mfma_f32_16x16x32_bf16 %0, %1, %2, 0" : "=&v"(acc) : "a"(wa), "v"(xb))
#define MFMA_AV(acc, wa, xb) asm volatile("v_mfma_f32_16x16x32_bf16 %0, %1, %2, %0" : "+v"(acc) : "a"(wa), "v"(xb))
#define MFMA_VA0(acc, xa, wb) asm volatile("v_mfma_f32_16x16x32_bf16 %0, %1, %2, 0" : "=&v"(acc) : "v"(xa), "a"(wb))
#define MFMA_VA(acc, xa, wb) asm volatile("v_mfma_f32_16x16x32_bf16 %0, %1, %2, %0" : "+v"(acc) : "v"(xa), "a"(wb))
#define MFMA_VV0(acc, xa, xb) asm volatile("v_mfma_f32_16x16x32_bf16 %0, %1, %2, 0" : "=&v"(acc) : "v"(xa), "v"(xb))
#define MFMA16_VV(acc, xa, xb) asm volatile("v_mfma_f32_16x16x16_bf16 %0, %1, %2, %0" : "+v"(acc) : "v"(xa), "v"(xb))
#define MFMA_ACC(acc, xa, xb) asm volatile("v_mfma_f32_16x16x32_bf16 %0, %1, %2, %0" : "+a"(acc) : "v"(xa), "v"(xb))
#define NOP_IN() asm volatile("s_nop 1" ::: "memory")
#define PIN(x) asm volatile("" : "+v"(x))
#define PINA(x) asm volatile("" : "+a"(x))

__device__ __forceinline__ bf16x4 trread(const char LAS* p) {
  return __builtin_amdgcn_ds_read_tr16_b64_v4i16((LAS v4i16_t*)p);
}
__device__ __forceinline__ bf16x8 cat8(bf16x4 a, bf16x4 b) {
  return __builtin_shufflevector(a, b, 0, 1, 2, 3, 4, 5, 6, 7);
}

#define CW_TICKET 0
#define CW_POL 256
#define CW_INIT 288
#define CW_SSMQ 352
#define CW_DEAD 320
#define CW_PROG 384
#define CW_XPROG 1408
#define CW_GPROG 3456
#define CTRL_BYTES 32768
#define EX_PAGES 24
#define EX_BYTES ((u64)EX_PAGES << 21)
#define EXP_C 0
#define EXP_A 20
#define EXP_B 2
#define EXP_DBG 19
#define QKV_SZ ((u64)S_LEN * HD * 4)
#define WS_Q_OFF (EX_BYTES)
#define WS_K_OFF (WS_Q_OFF + QKV_SZ)
#define WS_VT_OFF (WS_K_OFF + QKV_SZ)
#define WS_OX_OFF (WS_VT_OFF + QKV_SZ)
#define EX_BYTES32 50331648
#define QKV_SZ32 33554432
#define WS_K_OFF32 83886080
static_assert(EX_BYTES32 == EX_BYTES && QKV_SZ32 == QKV_SZ && WS_K_OFF32 == WS_K_OFF, "layout");

#define SPIN_CAP 300000
#define NPROBE 5
#define F_B 1350
#define F_C 3330
#define F_A 3100
#ifndef M_C
#define M_C 150
#endif
#ifndef M_A
#define M_A 150
#endif
#define X_FAIL 250
#define GAP_B 4
#define GAP_V 4
#define X_FAIL2 650
#define XO_SLEEP 200
#define T_LATE 220
#define D_PF (-200)
#define GAP_0 2
#define STR_(x) #x
#define STR(x) STR_(x)

struct Args {
  const u16 *x, *mem0, *Wq, *Wk, *Wv, *Wo;
  const u16 *u, *A_re, *A_im, *B_re, *B_im, *C_re, *C_im, *log_dt, *Dskip;
  u16* out;
  u32* ctrl;
  char* ex;
};

__device__ __forceinline__ bool is_dead(u32* ctrl) { return __hip_atomic_load(ctrl + CW_DEAD, RLX_AGENT) != 0; }
__device__ __forceinline__ void set_dead(u32* ctrl) { __hip_atomic_store(ctrl + CW_DEAD, 1u, RLX_AGENT); }
__device__ __forceinline__ void wait_ge(u32* ctrl, int word, u32 want, int slp) {
  for (int spins = 0;; ++spins) {
    asm volatile("" ::: "memory");
    u32 v = __builtin_amdgcn_readfirstlane(__hip_atomic_load(ctrl + word, RLX_AGENT));
    if (v >= want) break;
    if (spins > SPIN_CAP) { set_dead(ctrl); break; }
    if ((spins & 63) == 63 && __builtin_amdgcn_readfirstlane((int)is_dead(ctrl))) break;
    sleepn(slp);
  }
}
__device__ __forceinline__ u32 wait_pol(u32* ctrl) {
  const int lane = threadIdx.x & 63;
  u32 v = lane < EX_PAGES ? (ctrl[CW_POL + lane] & 1u) : 0u;
  u32 m = (u32)__builtin_amdgcn_readfirstlane((u32)__ballot(v != 0));
  const u32 full = (1u << EX_PAGES) - 1u;
  m = __builtin_popcount((m ^ 0xAAAAAAAAu) & full) <= EX_PAGES / 2 ? 0xAAAAAAAAu : 0x55555555u;
  return m;
}
__device__ __forceinline__ u32 exoff(u32 pol, u32 page, u32 pair) { return (page << 21) + (pair << 14) + (((pol >> page) & 1u) << 13); }
__device__ __forceinline__ u32 offB(u32 pol, int s, int h) { u32 pr = 32 * s + h; return exoff(pol, EXP_B + (pr >> 7), pr & 127); }
__device__ __forceinline__ u32 max4(u32x4 v) { return max(max(v[0], v[1]), max(v[2], v[3])); }
#define POLL_FAIL(spins) ((++(spins) > SPIN_CAP) ? (set_dead(ctrl), true) : (((spins) & 255) == 255 && __builtin_amdgcn_readfirstlane((int)is_dead(ctrl))))
#define REC_ASM \
  "s_mov_b64 exec, -1\n" \
  "v_mbcnt_lo_u32_b32 v0, -1, 0\n" \
  "v_mbcnt_hi_u32_b32 v0, -1, v0\n" \
  "s_lshr_b32 s70, %6, 2\n" \
  "s_and_b32 s71, %6, 3\n" \
  "v_lshlrev_b32 v1, 4, v0\n" \
  "v_and_b32 v27, 7, v0\n" \
  "v_lshrrev_b32 v33, 2, v0\n" \
  "v_lshlrev_b32 v33, 13, v33\n" \
  "v_and_b32 v28, 3, v0\n" \
  "v_lshl_add_u32 v33, v28, 4, v33\n" \
  "v_lshrrev_b32 v34, 3, v0\n" \
  "v_lshlrev_b32 v34, 11, v34\n" \
  "v_lshl_add_u32 v34, v27, 4, v34\n" \
  "s_lshl_b32 s60, %7, 21\n" \
  "s_mov_b64 s[36:37], %0\n" \
  "s_and_b32 s37, s37, 0xffff\n" \
  "s_mov_b32 s38, 8388608\n" \
  "s_mov_b32 s39, 0x00020000\n" \
  "s_mov_b64 s[40:41], %1\n" \
  "s_and_b32 s41, s41, 0xffff\n" \
  "s_mov_b32 s42, 8388608\n" \
  "s_mov_b32 s43, 0x00020000\n" \
  "s_mov_b64 s[44:45], %2\n" \
  "s_and_b32 s45, s45, 0xffff\n" \
  "s_mov_b32 s46, 8388608\n" \
  "s_mov_b32 s47, 0x00020000\n" \
  "s_add_u32 s59, %7, %6\n" \
  "s_add_u32 s59, s59, 0\n" \
  "s_and_b32 s59, s59, 3\n" \
  "s_lshl_b32 s59, s59, 19\n" \
  "s_add_u32 s59, s59, s60\n" \
  "buffer_load_dwordx4 a[0:3], v33, s[36:39], s59 offen\n" \
  "s_add_u32 s59, s59, 131072\n" \
  "buffer_load_dwordx4 a[4:7], v33, s[36:39], s59 offen\n" \
  "s_add_u32 s59, s59, 131072\n" \
  "buffer_load_dwordx4 a[8:11], v33, s[36:39], s59 offen\n" \
  "s_add_u32 s59, s59, 131072\n" \
  "buffer_load_dwordx4 a[12:15], v33, s[36:39], s59 offen\n" \
  "s_add_u32 s59, %7, %6\n" \
  "s_add_u32 s59, s59, 1\n" \
  "s_and_b32 s59, s59, 3\n" \
  "s_lshl_b32 s59, s59, 19\n" \
  "s_add_u32 s59, s59, s60\n" \
  "buffer_load_dwordx4 a[16:19], v33, s[36:39], s59 offen\n" \
  "s_add_u32 s59, s59, 131072\n" \
  "buffer_load_dwordx4 a[20:23], v33, s[36:39], s59 offen\n" \
  "s_add_u32 s59, s59, 131072\n" \
  "buffer_load_dwordx4 a[24:27], v33, s[36:39], s59 offen\n" \
  "s_add_u32 s59, s59, 131072\n" \
  "buffer_load_dwordx4 a[28:31], v33, s[36:39], s59 offen\n" \
  "s_add_u32 s59, %7, %6\n" \
  "s_add_u32 s59, s59, 2\n" \
  "s_and_b32 s59, s59, 3\n" \
  "s_lshl_b32 s59, s59, 19\n" \
  "s_add_u32 s59, s59, s60\n" \
  "buffer_load_dwordx4 a[32:35], v33, s[36:39], s59 offen\n" \
  "s_add_u32 s59, s59, 131072\n" \
  "buffer_load_dwordx4 a[36:39], v33, s[36:39], s59 offen\n" \
  "s_add_u32 s59, s59, 131072\n" \
  "buffer_load_dwordx4 a[40:43], v33, s[36:39], s59 offen\n" \
  "s_add_u32 s59, s59, 131072\n" \
  "buffer_load_dwordx4 a[44:47], v33, s[36:39], s59 offen\n" \
  "s_add_u32 s59, %7, %6\n" \
  "s_add_u32 s59, s59, 3\n" \
  "s_and_b32 s59, s59, 3\n" \
  "s_lshl_b32 s59, s59, 19\n" \
  "s_add_u32 s59, s59, s60\n" \
  "buffer_load_dwordx4 a[48:51], v33, s[36:39], s59 offen\n" \
  "s_add_u32 s59, s59, 131072\n" \
  "buffer_load_dwordx4 a[52:55], v33, s[36:39], s59 offen\n" \
  "s_add_u32 s59, s59, 131072\n" \
  "buffer_load_dwordx4 a[56:59], v33, s[36:39], s59 offen\n" \
  "s_add_u32 s59, s59, 131072\n" \
  "buffer_load_dwordx4 a[60:63], v33, s[36:39], s59 offen\n" \
  "s_add_u32 s59, %7, %6\n" \
  "s_add_u32 s59, s59, 0\n" \
  "s_and_b32 s59, s59, 3\n" \
  "s_lshl_b32 s59, s59, 19\n" \
  "s_add_u32 s59, s59, s60\n" \
  "buffer_load_dwordx4 a[64:67], v33, s[40:43], s59 offen\n" \
  "s_add_u32 s59, s59, 131072\n" \
  "buffer_load_dwordx4 a[68:71], v33, s[40:43], s59 offen\n" \
  "s_add_u32 s59, s59, 131072\n" \
  "buffer_load_dwordx4 a[72:75], v33, s[40:43], s59 offen\n" \
  "s_add_u32 s59, s59, 131072\n" \
  "buffer_load_dwordx4 a[76:79], v33, s[40:43], s59 offen\n" \
  "s_add_u32 s59, %7, %6\n" \
  "s_add_u32 s59, s59, 1\n" \
  "s_and_b32 s59, s59, 3\n" \
  "s_lshl_b32 s59, s59, 19\n" \
  "s_add_u32 s59, s59, s60\n" \
  "buffer_load_dwordx4 a[80:83], v33, s[40:43], s59 offen\n" \
  "s_add_u32 s59, s59, 131072\n" \
  "buffer_load_dwordx4 a[84:87], v33, s[40:43], s59 offen\n" \
  "s_add_u32 s59, s59, 131072\n" \
  "buffer_load_dwordx4 a[88:91], v33, s[40:43], s59 offen\n" \
  "s_add_u32 s59, s59, 131072\n" \
  "buffer_load_dwordx4 a[92:95], v33, s[40:43], s59 offen\n" \
  "s_add_u32 s59, %7, %6\n" \
  "s_add_u32 s59, s59, 2\n" \
  "s_and_b32 s59, s59, 3\n" \
  "s_lshl_b32 s59, s59, 19\n" \
  "s_add_u32 s59, s59, s60\n" \
  "buffer_load_dwordx4 a[96:99], v33, s[40:43], s59 offen\n" \
  "s_add_u32 s59, s59, 131072\n" \
  "buffer_load_dwordx4 a[100:103], v33, s[40:43], s59 offen\n" \
  "s_add_u32 s59, s59, 131072\n" \
  "buffer_load_dwordx4 a[104:107], v33, s[40:43], s59 offen\n" \
  "s_add_u32 s59, s59, 131072\n" \
  "buffer_load_dwordx4 a[108:111], v33, s[40:43], s59 offen\n" \
  "s_add_u32 s59, %7, %6\n" \
  "s_add_u32 s59, s59, 3\n" \
  "s_and_b32 s59, s59, 3\n" \
  "s_lshl_b32 s59, s59, 19\n" \
  "s_add_u32 s59, s59, s60\n" \
  "buffer_load_dwordx4 a[112:115], v33, s[40:43], s59 offen\n" \
  "s_add_u32 s59, s59, 131072\n" \
  "buffer_load_dwordx4 a[116:119], v33, s[40:43], s59 offen\n" \
  "s_add_u32 s59, s59, 131072\n" \
  "buffer_load_dwordx4 a[120:123], v33, s[40:43], s59 offen\n" \
  "s_add_u32 s59, s59, 131072\n" \
  "buffer_load_dwordx4 a[124:127], v33, s[40:43], s59 offen\n" \
  "s_add_u32 s59, %7, %6\n" \
  "s_add_u32 s59, s59, 0\n" \
  "s_and_b32 s59, s59, 3\n" \
  "s_lshl_b32 s59, s59, 19\n" \
  "s_add_u32 s59, s59, s60\n" \
  "buffer_load_dwordx4 a[128:131], v33, s[44:47], s59 offen\n" \
  "s_add_u32 s59, s59, 131072\n" \
  "buffer_load_dwordx4 a[132:135], v33, s[44:47], s59 offen\n" \
  "s_add_u32 s59, s59, 131072\n" \
  "buffer_load_dwordx4 a[136:139], v33, s[44:47], s59 offen\n" \
  "s_add_u32 s59, s59, 131072\n" \
  "buffer_load_dwordx4 a[140:143], v33, s[44:47], s59 offen\n" \
  "s_add_u32 s59, %7, %6\n" \
  "s_add_u32 s59, s59, 1\n" \
  "s_and_b32 s59, s59, 3\n" \
  "s_lshl_b32 s59, s59, 19\n" \
  "s_add_u32 s59, s59, s60\n" \
  "buffer_load_dwordx4 a[144:147], v33, s[44:47], s59 offen\n" \
  "s_add_u32 s59, s59, 131072\n" \
  "buffer_load_dwordx4 a[148:151], v33, s[44:47], s59 offen\n" \
  "s_add_u32 s59, s59, 131072\n" \
  "buffer_load_dwordx4 a[152:155], v33, s[44:47], s59 offen\n" \
  "s_add_u32 s59, s59, 131072\n" \
  "buffer_load_dwordx4 a[156:159], v33, s[44:47], s59 offen\n" \
  "s_add_u32 s59, %7, %6\n" \
  "s_add_u32 s59, s59, 2\n" \
  "s_and_b32 s59, s59, 3\n" \
  "s_lshl_b32 s59, s59, 19\n" \
  "s_add_u32 s59, s59, s60\n" \
  "buffer_load_dwordx4 a[160:163], v33, s[44:47], s59 offen\n" \
  "s_add_u32 s59, s59, 131072\n" \
  "buffer_load_dwordx4 a[164:167], v33, s[44:47], s59 offen\n" \
  "s_add_u32 s59, s59, 131072\n" \
  "buffer_load_dwordx4 a[168:171], v33, s[44:47], s59 offen\n" \
  "s_add_u32 s59, s59, 131072\n" \
  "buffer_load_dwordx4 a[172:175], v33, s[44:47], s59 offen\n" \
  "s_add_u32 s59, %7, %6\n" \
  "s_add_u32 s59, s59, 3\n" \
  "s_and_b32 s59, s59, 3\n" \
  "s_lshl_b32 s59, s59, 19\n" \
  "s_add_u32 s59, s59, s60\n" \
  "buffer_load_dwordx4 a[176:179], v33, s[44:47], s59 offen\n" \
  "s_add_u32 s59, s59, 131072\n" \
  "buffer_load_dwordx4 a[180:183], v33, s[44:47], s59 offen\n" \
  "s_add_u32 s59, s59, 131072\n" \
  "buffer_load_dwordx4 a[184:187], v33, s[44:47], s59 offen\n" \
  "s_add_u32 s59, s59, 131072\n" \
  "buffer_load_dwordx4 a[188:191], v33, s[44:47], s59 offen\n" \
  "s_mov_b64 s[36:37], %3\n" \
  "s_and_b32 s37, s37, 0xffff\n" \
  "s_mov_b32 s38, 262144\n" \
  "s_mov_b32 s39, 0x00020000\n" \
  "s_waitcnt vmcnt(48)\n" \
  "s_barrier\n" \
  "s_lshl_b32 s59, %7, 7\n" \
  "s_waitcnt vmcnt(46)\n" \
  "buffer_load_dwordx4 a[192:195], v34, s[36:39], s59 offen\n" \
  "s_add_u32 s59, s59, 16384\n" \
  "buffer_load_dwordx4 a[196:199], v34, s[36:39], s59 offen\n" \
  "s_add_u32 s59, s59, 16384\n" \
  "buffer_load_dwordx4 a[200:203], v34, s[36:39], s59 offen\n" \
  "s_add_u32 s59, s59, 16384\n" \
  "buffer_load_dwordx4 a[204:207], v34, s[36:39], s59 offen\n" \
  "s_add_u32 s59, s59, 16384\n" \
  "buffer_load_dwordx4 a[208:211], v34, s[36:39], s59 offen\n" \
  "s_add_u32 s59, s59, 16384\n" \
  "buffer_load_dwordx4 a[212:215], v34, s[36:39], s59 offen\n" \
  "s_add_u32 s59, s59, 16384\n" \
  "buffer_load_dwordx4 a[216:219], v34, s[36:39], s59 offen\n" \
  "s_add_u32 s59, s59, 16384\n" \
  "buffer_load_dwordx4 a[220:223], v34, s[36:39], s59 offen\n" \
  "s_add_u32 s59, s59, 16384\n" \
  "buffer_load_dwordx4 a[224:227], v34, s[36:39], s59 offen\n" \
  "s_add_u32 s59, s59, 16384\n" \
  "buffer_load_dwordx4 a[228:231], v34, s[36:39], s59 offen\n" \
  "s_add_u32 s59, s59, 16384\n" \
  "buffer_load_dwordx4 a[232:235], v34, s[36:39], s59 offen\n" \
  "s_add_u32 s59, s59, 16384\n" \
  "buffer_load_dwordx4 a[236:239], v34, s[36:39], s59 offen\n" \
  "s_add_u32 s59, s59, 16384\n" \
  "buffer_load_dwordx4 a[240:243], v34, s[36:39], s59 offen\n" \
  "s_add_u32 s59, s59, 16384\n" \
  "buffer_load_dwordx4 a[244:247], v34, s[36:39], s59 offen\n" \
  "s_add_u32 s59, s59, 16384\n" \
  "buffer_load_dwordx4 a[248:251], v34, s[36:39], s59 offen\n" \
  "s_add_u32 s59, s59, 16384\n" \
  "buffer_load_dwordx4 a[252:255], v34, s[36:39], s59 offen\n" \
  "s_mov_b64 s[44:45], %5\n" \
  "s_and_b32 s45, s45, 0xffff\n" \
  "s_mov_b32 s46, " STR(CTRL_BYTES) "\n" \
  "s_mov_b32 s47, 0x00020000\n" \
  "v_mov_b32 v27, " STR(CW_INIT) "*4\n" \
  "v_mov_b32 v28, 1\n" \
  "v_or_b32 v25, %7, v0\n" \
  "v_lshl_add_u32 v27, v25, 24, v27\n" \
  "s_waitcnt vmcnt(60)\n" \
  "buffer_atomic_add v28, v27, s[44:47], 0 offen\n" \
  "s_nop 1\n" \
  "s_memtime s[72:73]\n" \
  "s_memrealtime s[74:75]\n" \
  "s_nop 1\n" \
  "s_memrealtime s[76:77]\n" \
  "s_nop 1\n" \
  "s_memrealtime s[78:79]\n" \
  "s_nop 1\n" \
  "s_memrealtime s[80:81]\n" \
  "s_nop 1\n" \
  "s_memrealtime s[82:83]\n" \
  "s_nop 1\n" \
  "s_memrealtime s[84:85]\n" \
  "s_nop 1\n" \
  "s_memrealtime s[86:87]\n" \
  "s_nop 1\n" \
  "s_memrealtime s[88:89]\n" \
  "s_nop 1\n" \
  "s_memrealtime s[90:91]\n" \
  "s_mov_b32 s59, 0\n" \
  "s_waitcnt lgkmcnt(0)\n" \
  "s_cmp_eq_u32 s76, s74\n" \
  "s_addc_u32 s59, s59, 0\n" \
  "s_cmp_eq_u32 s78, s74\n" \
  "s_addc_u32 s59, s59, 0\n" \
  "s_cmp_eq_u32 s80, s74\n" \
  "s_addc_u32 s59, s59, 0\n" \
  "s_cmp_eq_u32 s82, s74\n" \
  "s_addc_u32 s59, s59, 0\n" \
  "s_cmp_eq_u32 s84, s74\n" \
  "s_addc_u32 s59, s59, 0\n" \
  "s_cmp_eq_u32 s86, s74\n" \
  "s_addc_u32 s59, s59, 0\n" \
  "s_cmp_eq_u32 s88, s74\n" \
  "s_addc_u32 s59, s59, 0\n" \
  "s_cmp_eq_u32 s90, s74\n" \
  "s_addc_u32 s59, s59, 0\n" \
  "s_mul_i32 s59, s59, 12\n" \
  "s_add_u32 s68, s72, s59\n" \
  "s_add_u32 s59, s74, 4\n" \
  "s_mul_i32 s69, s59, 24\n" \
  "s_sub_u32 s62, s69, s68\n" \
  "s_mov_b32 s56, s68\n" \
  "s_mul_i32 s59, %7, 40960\n" \
  "s_add_u32 s94, s59, %9\n" \
  "v_and_b32 v36, 15, v0\n" \
  "v_lshrrev_b32 v37, 4, v0\n" \
  "v_lshrrev_b32 v38, 2, v36\n" \
  "v_and_b32 v39, 3, v0\n" \
  "v_lshrrev_b32 v27, 5, v0\n" \
  "v_lshlrev_b32 v27, 7, v27\n" \
  "v_xor_b32 v40, v1, v27\n" \
  "v_and_b32 v27, 1, v37\n" \
  "v_lshlrev_b32 v27, 5, v27\n" \
  "v_xor_b32 v40, v40, v27\n" \
  "v_add_u32 v40, s94, v40\n" \
  "v_add_u32 v35, s94, v1\n" \
  "v_lshrrev_b32 v46, 1, v37\n" \
  "v_lshlrev_b32 v46, 4, v46\n" \
  "v_and_b32 v27, 1, v37\n" \
  "v_lshl_add_u32 v46, v27, 2, v46\n" \
  "v_and_b32 v28, 1, v38\n" \
  "v_add_u32 v46, v46, v28\n" \
  "v_lshrrev_b32 v28, 1, v38\n" \
  "v_lshl_add_u32 v46, v28, 3, v46\n" \
  "v_lshlrev_b32 v27, 5, v27\n" \
  "v_lshl_add_u32 v27, v28, 7, v27\n" \
  "v_lshlrev_b32 v28, 6, v46\n" \
  "v_lshl_add_u32 v28, v39, 3, v28\n" \
  "v_xor_b32 v27, v27, v28\n" \
  "v_xor_b32 v28, 0, v27\n" \
  "v_add_u32 v42, s94, v28\n" \
  "v_xor_b32 v28, 128, v27\n" \
  "v_add_u32 v43, s94, v28\n" \
  "v_xor_b32 v28, 32, v27\n" \
  "v_add_u32 v44, s94, v28\n" \
  "v_xor_b32 v28, 160, v27\n" \
  "v_add_u32 v45, s94, v28\n" \
  "v_and_b32 v27, 0xffffffef, v27\n" \
  "v_lshrrev_b32 v28, 1, v39\n" \
  "v_lshl_add_u32 v27, v28, 14, v27\n" \
  "s_add_u32 s59, %7, 0\n" \
  "s_and_b32 s59, s59, 3\n" \
  "s_lshl_b32 s59, s59, 4\n" \
  "v_xor_b32 v28, s59, v27\n" \
  "v_add_u32 v52, s94, v28\n" \
  "v_xor_b32 v28, 128, v28\n" \
  "v_add_u32 v53, s94, v28\n" \
  "s_add_u32 s59, %7, 1\n" \
  "s_and_b32 s59, s59, 3\n" \
  "s_lshl_b32 s59, s59, 4\n" \
  "v_xor_b32 v28, s59, v27\n" \
  "v_add_u32 v54, s94, v28\n" \
  "v_xor_b32 v28, 128, v28\n" \
  "v_add_u32 v55, s94, v28\n" \
  "s_add_u32 s59, %7, 2\n" \
  "s_and_b32 s59, s59, 3\n" \
  "s_lshl_b32 s59, s59, 4\n" \
  "v_xor_b32 v28, s59, v27\n" \
  "v_add_u32 v56, s94, v28\n" \
  "v_xor_b32 v28, 128, v28\n" \
  "v_add_u32 v57, s94, v28\n" \
  "s_add_u32 s59, %7, 3\n" \
  "s_and_b32 s59, s59, 3\n" \
  "s_lshl_b32 s59, s59, 4\n" \
  "v_xor_b32 v28, s59, v27\n" \
  "v_add_u32 v58, s94, v28\n" \
  "v_xor_b32 v28, 128, v28\n" \
  "v_add_u32 v59, s94, v28\n" \
  "v_lshlrev_b32 v49, 7, v46\n" \
  "v_lshl_add_u32 v49, v39, 5, v49\n" \
  "v_add_u32 v49, s94, v49\n" \
  "s_waitcnt vmcnt(61)\n" \
  "ds_write_b128 v40, a[0:3] offset:0\n" \
  "ds_write_b128 v40, a[4:7] offset:1024\n" \
  "ds_write_b128 v40, a[8:11] offset:2048\n" \
  "ds_write_b128 v40, a[12:15] offset:3072\n" \
  "s_waitcnt vmcnt(57)\n" \
  "ds_write_b128 v40, a[16:19] offset:4096\n" \
  "ds_write_b128 v40, a[20:23] offset:5120\n" \
  "ds_write_b128 v40, a[24:27] offset:6144\n" \
  "ds_write_b128 v40, a[28:31] offset:7168\n" \
  "s_waitcnt vmcnt(53)\n" \
  "ds_write_b128 v40, a[32:35] offset:8192\n" \
  "ds_write_b128 v40, a[36:39] offset:9216\n" \
  "ds_write_b128 v40, a[40:43] offset:10240\n" \
  "ds_write_b128 v40, a[44:47] offset:11264\n" \
  "s_waitcnt vmcnt(49)\n" \
  "ds_write_b128 v40, a[48:51] offset:12288\n" \
  "ds_write_b128 v40, a[52:55] offset:13312\n" \
  "ds_write_b128 v40, a[56:59] offset:14336\n" \
  "ds_write_b128 v40, a[60:63] offset:15360\n" \
  "s_waitcnt vmcnt(45)\n" \
  "ds_write_b128 v40, a[64:67] offset:16384\n" \
  "ds_write_b128 v40, a[68:71] offset:17408\n" \
  "ds_write_b128 v40, a[72:75] offset:18432\n" \
  "ds_write_b128 v40, a[76:79] offset:19456\n" \
  "s_waitcnt vmcnt(41)\n" \
  "ds_write_b128 v40, a[80:83] offset:20480\n" \
  "ds_write_b128 v40, a[84:87] offset:21504\n" \
  "ds_write_b128 v40, a[88:91] offset:22528\n" \
  "ds_write_b128 v40, a[92:95] offset:23552\n" \
  "s_waitcnt vmcnt(37)\n" \
  "ds_write_b128 v40, a[96:99] offset:24576\n" \
  "ds_write_b128 v40, a[100:103] offset:25600\n" \
  "ds_write_b128 v40, a[104:107] offset:26624\n" \
  "ds_write_b128 v40, a[108:111] offset:27648\n" \
  "s_waitcnt vmcnt(33)\n" \
  "ds_write_b128 v40, a[112:115] offset:28672\n" \
  "ds_write_b128 v40, a[116:119] offset:29696\n" \
  "ds_write_b128 v40, a[120:123] offset:30720\n" \
  "ds_write_b128 v40, a[124:127] offset:31744\n" \
  "s_waitcnt lgkmcnt(0)\n" \
  "ds_read_b64_tr_b16 a[0:1], v52 offset:0\n" \
  "ds_read_b64_tr_b16 a[2:3], v53 offset:0\n" \
  "ds_read_b64_tr_b16 a[4:5], v52 offset:2048\n" \
  "ds_read_b64_tr_b16 a[6:7], v53 offset:2048\n" \
  "ds_read_b64_tr_b16 a[8:9], v54 offset:0\n" \
  "ds_read_b64_tr_b16 a[10:11], v55 offset:0\n" \
  "ds_read_b64_tr_b16 a[12:13], v54 offset:2048\n" \
  "ds_read_b64_tr_b16 a[14:15], v55 offset:2048\n" \
  "ds_read_b64_tr_b16 a[16:17], v56 offset:0\n" \
  "ds_read_b64_tr_b16 a[18:19], v57 offset:0\n" \
  "ds_read_b64_tr_b16 a[20:21], v56 offset:2048\n" \
  "ds_read_b64_tr_b16 a[22:23], v57 offset:2048\n" \
  "ds_read_b64_tr_b16 a[24:25], v58 offset:0\n" \
  "ds_read_b64_tr_b16 a[26:27], v59 offset:0\n" \
  "ds_read_b64_tr_b16 a[28:29], v58 offset:2048\n" \
  "ds_read_b64_tr_b16 a[30:31], v59 offset:2048\n" \
  "ds_read_b64_tr_b16 a[32:33], v52 offset:4096\n" \
  "ds_read_b64_tr_b16 a[34:35], v53 offset:4096\n" \
  "ds_read_b64_tr_b16 a[36:37], v52 offset:6144\n" \
  "ds_read_b64_tr_b16 a[38:39], v53 offset:6144\n" \
  "ds_read_b64_tr_b16 a[40:41], v54 offset:4096\n" \
  "ds_read_b64_tr_b16 a[42:43], v55 offset:4096\n" \
  "ds_read_b64_tr_b16 a[44:45], v54 offset:6144\n" \
  "ds_read_b64_tr_b16 a[46:47], v55 offset:6144\n" \
  "ds_read_b64_tr_b16 a[48:49], v56 offset:4096\n" \
  "ds_read_b64_tr_b16 a[50:51], v57 offset:4096\n" \
  "ds_read_b64_tr_b16 a[52:53], v56 offset:6144\n" \
  "ds_read_b64_tr_b16 a[54:55], v57 offset:6144\n" \
  "ds_read_b64_tr_b16 a[56:57], v58 offset:4096\n" \
  "ds_read_b64_tr_b16 a[58:59], v59 offset:4096\n" \
  "ds_read_b64_tr_b16 a[60:61], v58 offset:6144\n" \
  "ds_read_b64_tr_b16 a[62:63], v59 offset:6144\n" \
  "ds_read_b64_tr_b16 a[64:65], v52 offset:8192\n" \
  "ds_read_b64_tr_b16 a[66:67], v53 offset:8192\n" \
  "ds_read_b64_tr_b16 a[68:69], v52 offset:10240\n" \
  "ds_read_b64_tr_b16 a[70:71], v53 offset:10240\n" \
  "ds_read_b64_tr_b16 a[72:73], v54 offset:8192\n" \
  "ds_read_b64_tr_b16 a[74:75], v55 offset:8192\n" \
  "ds_read_b64_tr_b16 a[76:77], v54 offset:10240\n" \
  "ds_read_b64_tr_b16 a[78:79], v55 offset:10240\n" \
  "ds_read_b64_tr_b16 a[80:81], v56 offset:8192\n" \
  "ds_read_b64_tr_b16 a[82:83], v57 offset:8192\n" \
  "ds_read_b64_tr_b16 a[84:85], v56 offset:10240\n" \
  "ds_read_b64_tr_b16 a[86:87], v57 offset:10240\n" \
  "ds_read_b64_tr_b16 a[88:89], v58 offset:8192\n" \
  "ds_read_b64_tr_b16 a[90:91], v59 offset:8192\n" \
  "ds_read_b64_tr_b16 a[92:93], v58 offset:10240\n" \
  "ds_read_b64_tr_b16 a[94:95], v59 offset:10240\n" \
  "ds_read_b64_tr_b16 a[96:97], v52 offset:12288\n" \
  "ds_read_b64_tr_b16 a[98:99], v53 offset:12288\n" \
  "ds_read_b64_tr_b16 a[100:101], v52 offset:14336\n" \
  "ds_read_b64_tr_b16 a[102:103], v53 offset:14336\n" \
  "ds_read_b64_tr_b16 a[104:105], v54 offset:12288\n" \
  "ds_read_b64_tr_b16 a[106:107], v55 offset:12288\n" \
  "ds_read_b64_tr_b16 a[108:109], v54 offset:14336\n" \
  "ds_read_b64_tr_b16 a[110:111], v55 offset:14336\n" \
  "ds_read_b64_tr_b16 a[112:113], v56 offset:12288\n" \
  "ds_read_b64_tr_b16 a[114:115], v57 offset:12288\n" \
  "ds_read_b64_tr_b16 a[116:117], v56 offset:14336\n" \
  "ds_read_b64_tr_b16 a[118:119], v57 offset:14336\n" \
  "ds_read_b64_tr_b16 a[120:121], v58 offset:12288\n" \
  "ds_read_b64_tr_b16 a[122:123], v59 offset:12288\n" \
  "ds_read_b64_tr_b16 a[124:125], v58 offset:14336\n" \
  "ds_read_b64_tr_b16 a[126:127], v59 offset:14336\n" \
  "s_waitcnt lgkmcnt(0)\n" \
  "s_waitcnt vmcnt(17)\n" \
  "ds_write_b128 v40, a[128:131] offset:0\n" \
  "ds_write_b128 v40, a[132:135] offset:1024\n" \
  "ds_write_b128 v40, a[136:139] offset:2048\n" \
  "ds_write_b128 v40, a[140:143] offset:3072\n" \
  "ds_write_b128 v40, a[144:147] offset:4096\n" \
  "ds_write_b128 v40, a[148:151] offset:5120\n" \
  "ds_write_b128 v40, a[152:155] offset:6144\n" \
  "ds_write_b128 v40, a[156:159] offset:7168\n" \
  "ds_write_b128 v40, a[160:163] offset:8192\n" \
  "ds_write_b128 v40, a[164:167] offset:9216\n" \
  "ds_write_b128 v40, a[168:171] offset:10240\n" \
  "ds_write_b128 v40, a[172:175] offset:11264\n" \
  "ds_write_b128 v40, a[176:179] offset:12288\n" \
  "ds_write_b128 v40, a[180:183] offset:13312\n" \
  "ds_write_b128 v40, a[184:187] offset:14336\n" \
  "ds_write_b128 v40, a[188:191] offset:15360\n" \
  "s_waitcnt lgkmcnt(0)\n" \
  "ds_read_b64_tr_b16 a[128:129], v42 offset:0\n" \
  "ds_read_b64_tr_b16 a[130:131], v43 offset:0\n" \
  "ds_read_b64_tr_b16 a[132:133], v42 offset:2048\n" \
  "ds_read_b64_tr_b16 a[134:135], v43 offset:2048\n" \
  "ds_read_b64_tr_b16 a[136:137], v44 offset:0\n" \
  "ds_read_b64_tr_b16 a[138:139], v45 offset:0\n" \
  "ds_read_b64_tr_b16 a[140:141], v44 offset:2048\n" \
  "ds_read_b64_tr_b16 a[142:143], v45 offset:2048\n" \
  "ds_read_b64_tr_b16 a[144:145], v42 offset:4096\n" \
  "ds_read_b64_tr_b16 a[146:147], v43 offset:4096\n" \
  "ds_read_b64_tr_b16 a[148:149], v42 offset:6144\n" \
  "ds_read_b64_tr_b16 a[150:151], v43 offset:6144\n" \
  "ds_read_b64_tr_b16 a[152:153], v44 offset:4096\n" \
  "ds_read_b64_tr_b16 a[154:155], v45 offset:4096\n" \
  "ds_read_b64_tr_b16 a[156:157], v44 offset:6144\n" \
  "ds_read_b64_tr_b16 a[158:159], v45 offset:6144\n" \
  "ds_read_b64_tr_b16 a[160:161], v42 offset:8192\n" \
  "ds_read_b64_tr_b16 a[162:163], v43 offset:8192\n" \
  "ds_read_b64_tr_b16 a[164:165], v42 offset:10240\n" \
  "ds_read_b64_tr_b16 a[166:167], v43 offset:10240\n" \
  "ds_read_b64_tr_b16 a[168:169], v44 offset:8192\n" \
  "ds_read_b64_tr_b16 a[170:171], v45 offset:8192\n" \
  "ds_read_b64_tr_b16 a[172:173], v44 offset:10240\n" \
  "ds_read_b64_tr_b16 a[174:175], v45 offset:10240\n" \
  "ds_read_b64_tr_b16 a[176:177], v42 offset:12288\n" \
  "ds_read_b64_tr_b16 a[178:179], v43 offset:12288\n" \
  "ds_read_b64_tr_b16 a[180:181], v42 offset:14336\n" \
  "ds_read_b64_tr_b16 a[182:183], v43 offset:14336\n" \
  "ds_read_b64_tr_b16 a[184:185], v44 offset:12288\n" \
  "ds_read_b64_tr_b16 a[186:187], v45 offset:12288\n" \
  "ds_read_b64_tr_b16 a[188:189], v44 offset:14336\n" \
  "ds_read_b64_tr_b16 a[190:191], v45 offset:14336\n" \
  "s_waitcnt vmcnt(0)\n" \
  "ds_write_b128 v35, a[192:195] offset:16384\n" \
  "ds_write_b128 v35, a[196:199] offset:17408\n" \
  "ds_write_b128 v35, a[200:203] offset:18432\n" \
  "ds_write_b128 v35, a[204:207] offset:19456\n" \
  "ds_write_b128 v35, a[208:211] offset:20480\n" \
  "ds_write_b128 v35, a[212:215] offset:21504\n" \
  "ds_write_b128 v35, a[216:219] offset:22528\n" \
  "ds_write_b128 v35, a[220:223] offset:23552\n" \
  "ds_write_b128 v35, a[224:227] offset:24576\n" \
  "ds_write_b128 v35, a[228:231] offset:25600\n" \
  "ds_write_b128 v35, a[232:235] offset:26624\n" \
  "ds_write_b128 v35, a[236:239] offset:27648\n" \
  "ds_write_b128 v35, a[240:243] offset:28672\n" \
  "ds_write_b128 v35, a[244:247] offset:29696\n" \
  "ds_write_b128 v35, a[248:251] offset:30720\n" \
  "ds_write_b128 v35, a[252:255] offset:31744\n" \
  "s_waitcnt lgkmcnt(0)\n" \
  "ds_read_b64_tr_b16 a[192:193], v49 offset:16384\n" \
  "ds_read_b64_tr_b16 a[194:195], v49 offset:16640\n" \
  "ds_read_b64_tr_b16 a[196:197], v49 offset:20480\n" \
  "ds_read_b64_tr_b16 a[198:199], v49 offset:20736\n" \
  "ds_read_b64_tr_b16 a[200:201], v49 offset:24576\n" \
  "ds_read_b64_tr_b16 a[202:203], v49 offset:24832\n" \
  "ds_read_b64_tr_b16 a[204:205], v49 offset:28672\n" \
  "ds_read_b64_tr_b16 a[206:207], v49 offset:28928\n" \
  "ds_read_b64_tr_b16 a[208:209], v49 offset:16400\n" \
  "ds_read_b64_tr_b16 a[210:211], v49 offset:16656\n" \
  "ds_read_b64_tr_b16 a[212:213], v49 offset:20496\n" \
  "ds_read_b64_tr_b16 a[214:215], v49 offset:20752\n" \
  "ds_read_b64_tr_b16 a[216:217], v49 offset:24592\n" \
  "ds_read_b64_tr_b16 a[218:219], v49 offset:24848\n" \
  "ds_read_b64_tr_b16 a[220:221], v49 offset:28688\n" \
  "ds_read_b64_tr_b16 a[222:223], v49 offset:28944\n" \
  "ds_read_b64_tr_b16 a[224:225], v49 offset:16392\n" \
  "ds_read_b64_tr_b16 a[226:227], v49 offset:16648\n" \
  "ds_read_b64_tr_b16 a[228:229], v49 offset:20488\n" \
  "ds_read_b64_tr_b16 a[230:231], v49 offset:20744\n" \
  "ds_read_b64_tr_b16 a[232:233], v49 offset:24584\n" \
  "ds_read_b64_tr_b16 a[234:235], v49 offset:24840\n" \
  "ds_read_b64_tr_b16 a[236:237], v49 offset:28680\n" \
  "ds_read_b64_tr_b16 a[238:239], v49 offset:28936\n" \
  "ds_read_b64_tr_b16 a[240:241], v49 offset:16408\n" \
  "ds_read_b64_tr_b16 a[242:243], v49 offset:16664\n" \
  "ds_read_b64_tr_b16 a[244:245], v49 offset:20504\n" \
  "ds_read_b64_tr_b16 a[246:247], v49 offset:20760\n" \
  "ds_read_b64_tr_b16 a[248:249], v49 offset:24600\n" \
  "ds_read_b64_tr_b16 a[250:251], v49 offset:24856\n" \
  "ds_read_b64_tr_b16 a[252:253], v49 offset:28696\n" \
  "ds_read_b64_tr_b16 a[254:255], v49 offset:28952\n" \
  "s_mov_b64 s[36:37], %4\n" \
  "s_and_b32 s37, s37, 0xffff\n" \
  "s_mov_b32 s38, " STR(EX_BYTES32) "\n" \
  "s_mov_b32 s39, 0x00020000\n" \
  "s_mov_b64 s[40:41], %4\n" \
  "s_add_u32 s40, s40, " STR(WS_K_OFF32) "\n" \
  "s_addc_u32 s41, s41, 0\n" \
  "s_and_b32 s41, s41, 0xffff\n" \
  "s_mov_b32 s42, 3*" STR(QKV_SZ32) "\n" \
  "s_mov_b32 s43, 0x00020000\n" \
  "v_lshrrev_b32 v50, 5, v0\n" \
  "v_and_b32 v51, 31, v0\n" \
  "v_mov_b32 v2, 0x4444\n" \
  "v_mov_b32 v27, 0xeeee\n" \
  "v_cmp_lt_u32 vcc, 1, v39\n" \
  "s_mul_i32 s59, %7, 1280\n" \
  "s_add_u32 s59, s59, %9\n" \
  "v_cndmask_b32 v2, v2, v27, vcc\n" \
  "v_cndmask_b32 v29, 0, -1, vcc\n" \
  "v_cndmask_b32 v4, -1, 0, vcc\n" \
  "v_mul_u32_u24 v27, 80, v37\n" \
  "v_add_lshl_u32 v27, v27, v36, 2\n" \
  "v_add_u32 v5, s59, v27\n" \
  "v_lshlrev_b32 v27, 3, v0\n" \
  "s_add_u32 s60, %9, 8192\n" \
  "v_add_u32 v27, s60, v27\n" \
  "s_mul_i32 s59, %7, 1536\n" \
  "v_add_u32 v6, s59, v27\n" \
  "s_add_u32 s59, %7, 1\n" \
  "s_and_b32 s59, s59, 3\n" \
  "s_mul_i32 s59, s59, 1536\n" \
  "v_add_u32 v3, s59, v27\n" \
  "s_add_u32 s59, %7, 2\n" \
  "s_and_b32 s59, s59, 3\n" \
  "s_mul_i32 s59, s59, 1536\n" \
  "v_add_u32 v244, s59, v27\n" \
  "s_add_u32 s59, %7, 3\n" \
  "s_and_b32 s59, s59, 3\n" \
  "s_mul_i32 s59, s59, 1536\n" \
  "v_add_u32 v245, s59, v27\n" \
  "v_and_b32 v27, 1, v37\n" \
  "v_lshlrev_b32 v27, 1, v27\n" \
  "v_and_b32 v28, 1, v38\n" \
  "v_add_u32 v27, v27, v28\n" \
  "v_mul_u32_u24 v27, 160, v27\n" \
  "v_lshrrev_b32 v28, 1, v38\n" \
  "v_lshl_add_u32 v27, v28, 5, v27\n" \
  "v_lshl_add_u32 v27, v39, 3, v27\n" \
  "s_and_b32 s59, %7, 1\n" \
  "s_mul_i32 s59, s59, 640\n" \
  "s_lshr_b32 s60, %7, 1\n" \
  "s_lshl_b32 s60, s60, 6\n" \
  "s_add_u32 s59, s59, s60\n" \
  "s_add_u32 s59, s59, %9\n" \
  "v_add_u32 v7, s59, v27\n" \
  "v_mov_b32 v24, %9\n" \
  "s_lshl_b32 s94, s70, 14\n" \
  "v_add_u32 v17, s94, v1\n" \
  "v_add_u32 v17, 4096, v17\n" \
  "s_lshl_b32 s59, s71, 2\n" \
  "s_add_u32 s59, s59, %7\n" \
  "s_lshl_b32 s59, s59, 8\n" \
  "s_add_u32 s59, s59, s94\n" \
  "v_and_b32 v8, 0x38, v0\n" \
  "v_lshlrev_b32 v8, 2, v8\n" \
  "v_and_b32 v27, 1, v0\n" \
  "v_lshl_add_u32 v8, v27, 4, v8\n" \
  "v_and_b32 v27, 6, v0\n" \
  "v_lshl_add_u32 v8, v27, 1, v8\n" \
  "v_add_u32 v8, s59, v8\n" \
  "v_and_b32 v27, 7, v0\n" \
  "v_lshlrev_b32 v27, 4, v27\n" \
  "v_lshrrev_b32 v28, 3, v0\n" \
  "v_add_u32 v15, s59, v1\n" \
  "s_lshl_b32 s59, s71, 2\n" \
  "s_add_u32 s59, s59, %7\n" \
  "s_lshl_b32 s59, s59, 7\n" \
  "s_add_u32 s59, s59, 4096\n" \
  "s_add_u32 s59, s59, s94\n" \
  "v_lshlrev_b32 v9, 2, v51\n" \
  "v_add_u32 v9, s59, v9\n" \
  "v_lshl_add_u32 v9, v50, 31, v9\n" \
  "v_add_u32 v25, s59, v27\n" \
  "v_cmp_eq_u32 vcc, 2, v28\n" \
  "s_add_u32 s59, s94, 6144\n" \
  "s_and_b32 s60, %6, 31\n" \
  "s_or_b32 s60, s60, %7\n" \
  "v_cndmask_b32 v15, v15, v25, vcc\n" \
  "s_cmp_eq_u32 s60, 1\n" \
  "s_cselect_b32 s60, 0, 0x80000000\n" \
  "s_and_b32 s61, %6, 1\n" \
  "s_lshl_b32 s61, s61, 31\n" \
  "s_or_b32 s60, s60, s61\n" \
  "s_or_b32 s59, s59, s60\n" \
  "v_add_u32 v25, s59, v27\n" \
  "v_cmp_eq_u32 vcc, 3, v28\n" \
  "v_lshlrev_b32 v14, 2, v51\n" \
  "v_add_u32 v14, s59, v14\n" \
  "v_cndmask_b32 v15, v15, v25, vcc\n" \
  "v_lshlrev_b32 v25, 31, v50\n" \
  "v_or_b32 v14, v14, v25\n" \
  "v_or_b32 v15, v15, v25\n" \
  "v_lshlrev_b32 v13, 17, v39\n" \
  "v_add_u32 v13, 6144, v13\n" \
  "s_lshl_b32 s59, s71, 2\n" \
  "s_add_u32 s59, s59, %7\n" \
  "s_lshl_b32 s59, s59, 16\n" \
  "v_lshlrev_b32 v10, 14, v37\n" \
  "v_and_b32 v25, 1, v28\n" \
  "v_lshl_add_u32 v10, v25, 12, v10\n" \
  "v_add3_u32 v10, v10, v27, s59\n" \
  "s_lshl_b32 s60, %7, 4\n" \
  "s_add_u32 s60, s60, s70\n" \
  "v_lshl_add_u32 v25, v28, 1, s60\n" \
  "v_and_b32 v25, 31, v25\n" \
  "v_lshl_add_u32 v10, v25, 7, v10\n" \
  "s_lshr_b32 s59, %6, 1\n" \
  "s_lshl_b32 s59, s59, 14\n" \
  "s_and_b32 s60, %6, 1\n" \
  "s_lshl_b32 s60, s60, 12\n" \
  "s_add_u32 s59, s59, s60\n" \
  "s_lshl_b32 s60, %7, 10\n" \
  "s_add_u32 s59, s59, s60\n" \
  "v_add_u32 v11, s59, v1\n" \
  "s_lshr_b32 s59, %6, 6\n" \
  "s_lshl_b32 s59, s59, 14\n" \
  "s_and_b32 s60, %6, 63\n" \
  "s_lshl_b32 s60, s60, 7\n" \
  "s_add_u32 s59, s59, s60\n" \
  "v_add_u32 v12, s59, v27\n" \
  "v_lshrrev_b32 v25, 3, v36\n" \
  "v_lshl_add_u32 v25, v50, 1, v25\n" \
  "v_add_u32 v25, " STR(EXP_A) ", v25\n" \
  "v_lshl_add_u32 v12, v25, 21, v12\n" \
  "v_lshrrev_b32 v25, v25, %8\n" \
  "v_and_b32 v25, 1, v25\n" \
  "v_lshl_add_u32 v12, v25, 13, v12\n" \
  "v_lshl_add_u32 v12, v37, 31, v12\n" \
  "v_and_b32 v27, 1, v37\n" \
  "v_lshlrev_b32 v27, 5, v27\n" \
  "v_xor_b32 v27, v1, v27\n" \
  "s_add_u32 s59, %9, 32768\n" \
  "v_add_u32 v19, s59, v27\n" \
  "s_and_b32 s60, %7, 1\n" \
  "s_lshl_b32 s60, s60, 13\n" \
  "v_add_u32 v18, s60, v19\n" \
  "v_and_b32 v20, 15, v46\n" \
  "v_lshlrev_b32 v20, 4, v20\n" \
  "v_lshl_add_u32 v20, v50, 10, v20\n" \
  "v_lshl_add_u32 v20, v39, 8, v20\n" \
  "v_and_b32 v27, 1, v39\n" \
  "v_lshlrev_b32 v27, 5, v27\n" \
  "v_xor_b32 v20, v20, v27\n" \
  "v_xor_b32 v21, 32, v20\n" \
  "s_add_u32 s59, s59, 8192\n" \
  "v_add_u32 v20, s59, v20\n" \
  "v_add_u32 v21, s59, v21\n" \
  "v_and_b32 v22, 1, v0\n" \
  "v_lshlrev_b32 v22, 7, v22\n" \
  "s_lshl_b32 s59, s70, 8\n" \
  "s_add_u32 s59, s59, " STR(CW_GPROG) "*4\n" \
  "v_add_u32 v22, s59, v22\n" \
  "s_add_u32 s59, %7, %6\n" \
  "s_add_u32 s59, s59, 0\n" \
  "s_and_b32 s59, s59, 3\n" \
  "s_lshl_b32 s59, s59, 10\n" \
  "v_add_u32 v80, s59, v1\n" \
  "v_add_u32 v84, s59, v19\n" \
  "v_mov_b32 v88, -1\n" \
  "s_add_u32 s59, %7, %6\n" \
  "s_add_u32 s59, s59, 1\n" \
  "s_and_b32 s59, s59, 3\n" \
  "s_lshl_b32 s59, s59, 10\n" \
  "v_add_u32 v81, s59, v1\n" \
  "v_add_u32 v85, s59, v19\n" \
  "v_mov_b32 v89, -1\n" \
  "s_add_u32 s59, %7, %6\n" \
  "s_add_u32 s59, s59, 2\n" \
  "s_and_b32 s59, s59, 3\n" \
  "s_lshl_b32 s59, s59, 10\n" \
  "v_add_u32 v82, s59, v1\n" \
  "v_add_u32 v86, s59, v19\n" \
  "v_mov_b32 v90, -1\n" \
  "s_add_u32 s59, %7, %6\n" \
  "s_add_u32 s59, s59, 3\n" \
  "s_and_b32 s59, s59, 3\n" \
  "s_lshl_b32 s59, s59, 10\n" \
  "v_add_u32 v83, s59, v1\n" \
  "v_add_u32 v87, s59, v19\n" \
  "v_mov_b32 v91, -1\n" \
  "v_mov_b32 v248, 0\n" \
  "v_mov_b32 v27, 0x3f80\n" \
  "v_and_b32 v25, 7, v36\n" \
  "v_cmp_eq_u32 vcc, 0, v25\n" \
  "v_mov_b32 v28, 1.0\n" \
  "s_nop 0\n" \
  "v_cndmask_b32 v248, v248, v27, vcc\n" \
  "v_cmp_eq_u32 vcc, 1, v25\n" \
  "s_nop 1\n" \
  "v_cndmask_b32 v248, v248, v28, vcc\n" \
  "v_mov_b32 v249, 0\n" \
  "v_mov_b32 v27, 0x3f80\n" \
  "v_and_b32 v25, 7, v36\n" \
  "v_cmp_eq_u32 vcc, 2, v25\n" \
  "v_mov_b32 v28, 1.0\n" \
  "s_nop 0\n" \
  "v_cndmask_b32 v249, v249, v27, vcc\n" \
  "v_cmp_eq_u32 vcc, 3, v25\n" \
  "s_nop 1\n" \
  "v_cndmask_b32 v249, v249, v28, vcc\n" \
  "v_mov_b32 v250, 0\n" \
  "v_mov_b32 v27, 0x3f80\n" \
  "v_and_b32 v25, 7, v36\n" \
  "v_cmp_eq_u32 vcc, 4, v25\n" \
  "v_mov_b32 v28, 1.0\n" \
  "s_nop 0\n" \
  "v_cndmask_b32 v250, v250, v27, vcc\n" \
  "v_cmp_eq_u32 vcc, 5, v25\n" \
  "s_nop 1\n" \
  "v_cndmask_b32 v250, v250, v28, vcc\n" \
  "v_mov_b32 v251, 0\n" \
  "v_mov_b32 v27, 0x3f80\n" \
  "v_and_b32 v25, 7, v36\n" \
  "v_cmp_eq_u32 vcc, 6, v25\n" \
  "v_mov_b32 v28, 1.0\n" \
  "s_nop 0\n" \
  "v_cndmask_b32 v251, v251, v27, vcc\n" \
  "v_cmp_eq_u32 vcc, 7, v25\n" \
  "s_nop 1\n" \
  "v_cndmask_b32 v251, v251, v28, vcc\n" \
  "v_and_b32 v27, 1, v0\n" \
  "v_cmp_eq_u32 vcc, 1, v27\n" \
  "v_mov_b32 v246, 0x05040100\n" \
  "v_mov_b32 v28, 0x03020706\n" \
  "v_cndmask_b32 v246, v246, v28, vcc\n" \
  "v_mov_b32 v252, 0x3f803f80\n" \
  "v_mov_b32 v253, 0x3f803f80\n" \
  "v_mov_b32 v254, 0x3f803f80\n" \
  "v_mov_b32 v255, 0\n" \
  "v_mov_b32 v30, 0\n" \
  "v_mov_b32 v31, 0\n" \
  "s_mov_b32 s92, 0x3e0293ee\n" \
  "s_mov_b32 s58, 0\n" \
  "s_mov_b32 s57, 1\n" \
  "s_mov_b32 s48, 0\n" \
  "s_lshr_b32 s59, %7, 1\n" \
  "s_lshl_b32 s59, s59, 14\n" \
  "s_and_b32 s60, %7, 1\n" \
  "s_lshl_b32 s60, s60, 12\n" \
  "s_add_u32 s93, s59, s60\n" \
  "s_lshr_b32 s60, %6, 5\n" \
  "s_add_u32 s60, s60, " STR(EXP_A) "\n" \
  "s_lshl_b32 s60, s60, 21\n" \
  "s_add_u32 s60, s60, s93\n" \
  "s_mov_b32 s53, 0\n" \
  "s_lshr_b32 s59, s60, 21\n" \
  "s_lshr_b32 s59, %8, s59\n" \
  "s_and_b32 s59, s59, 1\n" \
  "s_lshl_b32 s59, s59, 13\n" \
  "s_or_b32 s93, s60, s59\n" \
  "s_mov_b32 s60, " STR(EXP_C) "<<21\n" \
  "s_lshr_b32 s59, s60, 21\n" \
  "s_lshr_b32 s59, %8, s59\n" \
  "s_and_b32 s59, s59, 1\n" \
  "s_lshl_b32 s59, s59, 13\n" \
  "s_or_b32 s52, s60, s59\n" \
  "s_add_u32 s51, s52, 1<<20\n" \
  "s_and_b32 s59, %7, 1\n" \
  "s_mul_i32 s59, s59, " STR(QKV_SZ32) "\n" \
  "s_lshl_b32 s60, s70, 14\n" \
  "s_add_u32 s59, s59, s60\n" \
  "v_add_u32 v247, s59, v1\n" \
  "v_add_u32 v16, 4096, v247\n" \
  "s_lshr_b32 s60, %7, 1\n" \
  "s_lshr_b32 s59, s60, 2\n" \
  "s_xnor_b32 s59, s59, %8\n" \
  "s_and_b32 s59, s59, 1\n" \
  "s_lshl_b32 s59, s59, 13\n" \
  "s_lshl_b32 s55, s60, 19\n" \
  "s_or_b32 s55, s55, s59\n" \
  "s_waitcnt lgkmcnt(0)\n" \
  "v_mov_b32 v25, " STR(CW_INIT) "*4\n" \
  "LI_%=:\n" \
  "buffer_load_dword v23, v25, s[44:47], 0 offen sc1\n" \
  "s_waitcnt vmcnt(0)\n" \
  "v_cmp_gt_u32 vcc, 128, v23\n" \
  "s_cbranch_vccz LI2_%=\n" \
  "s_sleep 6\n" \
  "s_add_u32 s58, s58, 1\n" \
  "s_cmp_lt_u32 s58, " STR(SPIN_CAP) "\n" \
  "s_cbranch_scc1 LI_%=\n" \
  "v_mov_b32 v27, " STR(CW_DEAD) "*4\n" \
  "v_mov_b32 v28, 1\n" \
  "buffer_store_dword v28, v27, s[44:47], 0 offen sc1\n" \
  "s_branch LI2_%=\n" \
  "LI2_%=:\n" \
  "buffer_load_dword v23, v22, s[44:47], 0 offen sc1\n" \
  "s_waitcnt vmcnt(0)\n" \
  "v_cmp_gt_u32 vcc, 2, v23\n" \
  "s_cbranch_vccz LI3_%=\n" \
  "s_sleep 4\n" \
  "s_add_u32 s58, s58, 1\n" \
  "s_cmp_lt_u32 s58, " STR(SPIN_CAP) "\n" \
  "s_cbranch_scc1 LI2_%=\n" \
  "v_mov_b32 v27, " STR(CW_DEAD) "*4\n" \
  "v_mov_b32 v28, 1\n" \
  "buffer_store_dword v28, v27, s[44:47], 0 offen sc1\n" \
  "s_branch LI3_%=\n" \
  "LI3_%=:\n" \
  "buffer_load_dwordx4 v[200:203], v247, s[40:43], s55 offen offset:0 sc1\n" \
  "buffer_load_dwordx4 v[204:207], v247, s[40:43], s55 offen offset:1024 sc1\n" \
  "buffer_load_dwordx4 v[208:211], v247, s[40:43], s55 offen offset:2048 sc1\n" \
  "buffer_load_dwordx4 v[212:215], v247, s[40:43], s55 offen offset:3072 sc1\n" \
  "buffer_load_dwordx4 v[216:219], v16, s[40:43], s55 offen offset:0 sc1\n" \
  "buffer_load_dwordx4 v[220:223], v16, s[40:43], s55 offen offset:1024 sc1\n" \
  "buffer_load_dwordx4 v[224:227], v16, s[40:43], s55 offen offset:2048 sc1\n" \
  "buffer_load_dwordx4 v[228:231], v16, s[40:43], s55 offen offset:3072 sc1\n" \
  "s_barrier\n" \
  "v_mov_b32 v27, 1\n" \
  "ds_write_b32 v24, v27 offset:14336\n" \
  "s_cmp_gt_u32 %7, 1\n" \
  "s_cbranch_scc1 LI4_%=\n" \
  "s_waitcnt vmcnt(0)\n" \
  "ds_write_b128 v18, v[200:203] offset:0\n" \
  "ds_write_b128 v18, v[204:207] offset:1024\n" \
  "ds_write_b128 v18, v[208:211] offset:2048\n" \
  "ds_write_b128 v18, v[212:215] offset:3072\n" \
  "ds_write_b128 v18, v[216:219] offset:4096\n" \
  "ds_write_b128 v18, v[220:223] offset:5120\n" \
  "ds_write_b128 v18, v[224:227] offset:6144\n" \
  "ds_write_b128 v18, v[228:231] offset:7168\n" \
  "LI4_%=:\n" \
  "s_waitcnt lgkmcnt(0)\n" \
  "s_barrier\n" \
  "ds_read_b64_tr_b16 v[136:137], v20 offset:0\n" \
  "ds_read_b64_tr_b16 v[138:139], v21 offset:0\n" \
  "ds_read_b64_tr_b16 v[152:153], v20 offset:8\n" \
  "ds_read_b64_tr_b16 v[154:155], v21 offset:8\n" \
  "ds_read_b64_tr_b16 v[140:141], v20 offset:2048\n" \
  "ds_read_b64_tr_b16 v[142:143], v21 offset:2048\n" \
  "ds_read_b64_tr_b16 v[156:157], v20 offset:2056\n" \
  "ds_read_b64_tr_b16 v[158:159], v21 offset:2056\n" \
  "ds_read_b64_tr_b16 v[144:145], v20 offset:4096\n" \
  "ds_read_b64_tr_b16 v[146:147], v21 offset:4096\n" \
  "ds_read_b64_tr_b16 v[160:161], v20 offset:4104\n" \
  "ds_read_b64_tr_b16 v[162:163], v21 offset:4104\n" \
  "ds_read_b64_tr_b16 v[148:149], v20 offset:6144\n" \
  "ds_read_b64_tr_b16 v[150:151], v21 offset:6144\n" \
  "ds_read_b64_tr_b16 v[164:165], v20 offset:6152\n" \
  "ds_read_b64_tr_b16 v[166:167], v21 offset:6152\n" \
  "ds_read_b128 v[104:107], v84 offset:0\n" \
  "ds_read_b128 v[108:111], v85 offset:0\n" \
  "ds_read_b128 v[112:115], v86 offset:0\n" \
  "ds_read_b128 v[116:119], v87 offset:0\n" \
  "ds_read_b128 v[120:123], v84 offset:4096\n" \
  "ds_read_b128 v[124:127], v85 offset:4096\n" \
  "ds_read_b128 v[128:131], v86 offset:4096\n" \
  "ds_read_b128 v[132:135], v87 offset:4096\n" \
  "s_waitcnt lgkmcnt(0)\n" \
  "v_mfma_f32_16x16x32_bf16 v[184:187], v[136:139], a[224:227], 0\n" \
  "v_mfma_f32_16x16x32_bf16 v[188:191], v[152:155], a[224:227], 0\n" \
  "v_mfma_f32_16x16x32_bf16 v[192:195], v[136:139], a[240:243], 0\n" \
  "v_mfma_f32_16x16x32_bf16 v[196:199], v[152:155], a[240:243], 0\n" \
  "v_mfma_f32_16x16x32_bf16 v[184:187], v[140:143], a[228:231], v[184:187]\n" \
  "v_mfma_f32_16x16x32_bf16 v[188:191], v[156:159], a[228:231], v[188:191]\n" \
  "v_mfma_f32_16x16x32_bf16 v[192:195], v[140:143], a[244:247], v[192:195]\n" \
  "v_mfma_f32_16x16x32_bf16 v[196:199], v[156:159], a[244:247], v[196:199]\n" \
  "v_mfma_f32_16x16x32_bf16 v[184:187], v[144:147], a[232:235], v[184:187]\n" \
  "v_mfma_f32_16x16x32_bf16 v[188:191], v[160:163], a[232:235], v[188:191]\n" \
  "v_mfma_f32_16x16x32_bf16 v[192:195], v[144:147], a[248:251], v[192:195]\n" \
  "v_mfma_f32_16x16x32_bf16 v[196:199], v[160:163], a[248:251], v[196:199]\n" \
  "v_mfma_f32_16x16x32_bf16 v[184:187], v[148:151], a[236:239], v[184:187]\n" \
  "v_mfma_f32_16x16x32_bf16 v[188:191], v[164:167], a[236:239], v[188:191]\n" \
  "v_mfma_f32_16x16x32_bf16 v[192:195], v[148:151], a[252:255], v[192:195]\n" \
  "v_mfma_f32_16x16x32_bf16 v[196:199], v[164:167], a[252:255], v[196:199]\n" \
  "s_nop 7\n" \
  "v_cvt_pk_bf16_f32 v176, v184, v185\n" \
  "v_cvt_pk_bf16_f32 v177, v186, v187\n" \
  "v_cvt_pk_bf16_f32 v178, v188, v189\n" \
  "v_cvt_pk_bf16_f32 v179, v190, v191\n" \
  "v_cvt_pk_bf16_f32 v180, v192, v193\n" \
  "v_cvt_pk_bf16_f32 v181, v194, v195\n" \
  "v_cvt_pk_bf16_f32 v182, v196, v197\n" \
  "v_cvt_pk_bf16_f32 v183, v198, v199\n" \
  "v_mfma_f32_16x16x32_bf16 v[184:187], v[136:139], a[192:195], 0\n" \
  "v_mfma_f32_16x16x32_bf16 v[188:191], v[152:155], a[192:195], 0\n" \
  "v_mfma_f32_16x16x32_bf16 v[192:195], v[136:139], a[208:211], 0\n" \
  "v_mfma_f32_16x16x32_bf16 v[196:199], v[152:155], a[208:211], 0\n" \
  "v_mfma_f32_16x16x32_bf16 v[184:187], v[140:143], a[196:199], v[184:187]\n" \
  "v_mfma_f32_16x16x32_bf16 v[188:191], v[156:159], a[196:199], v[188:191]\n" \
  "v_mfma_f32_16x16x32_bf16 v[192:195], v[140:143], a[212:215], v[192:195]\n" \
  "v_mfma_f32_16x16x32_bf16 v[196:199], v[156:159], a[212:215], v[196:199]\n" \
  "v_mfma_f32_16x16x32_bf16 v[184:187], v[144:147], a[200:203], v[184:187]\n" \
  "v_mfma_f32_16x16x32_bf16 v[188:191], v[160:163], a[200:203], v[188:191]\n" \
  "v_mfma_f32_16x16x32_bf16 v[192:195], v[144:147], a[216:219], v[192:195]\n" \
  "v_mfma_f32_16x16x32_bf16 v[196:199], v[160:163], a[216:219], v[196:199]\n" \
  "v_mfma_f32_16x16x32_bf16 v[184:187], v[148:151], a[204:207], v[184:187]\n" \
  "v_mfma_f32_16x16x32_bf16 v[188:191], v[164:167], a[204:207], v[188:191]\n" \
  "v_mfma_f32_16x16x32_bf16 v[192:195], v[148:151], a[220:223], v[192:195]\n" \
  "v_mfma_f32_16x16x32_bf16 v[196:199], v[164:167], a[220:223], v[196:199]\n" \
  "s_memtime s[72:73]\n" \
  "s_memrealtime s[74:75]\n" \
  "s_nop 1\n" \
  "s_memrealtime s[76:77]\n" \
  "s_nop 1\n" \
  "s_memrealtime s[78:79]\n" \
  "s_nop 1\n" \
  "s_memrealtime s[80:81]\n" \
  "s_nop 1\n" \
  "s_memrealtime s[82:83]\n" \
  "s_nop 1\n" \
  "s_memrealtime s[84:85]\n" \
  "s_nop 1\n" \
  "s_memrealtime s[86:87]\n" \
  "s_nop 1\n" \
  "s_memrealtime s[88:89]\n" \
  "s_nop 1\n" \
  "s_memrealtime s[90:91]\n" \
  "s_mov_b32 s59, 0\n" \
  "s_waitcnt lgkmcnt(0)\n" \
  "s_cmp_eq_u32 s76, s74\n" \
  "s_addc_u32 s59, s59, 0\n" \
  "s_cmp_eq_u32 s78, s74\n" \
  "s_addc_u32 s59, s59, 0\n" \
  "s_cmp_eq_u32 s80, s74\n" \
  "s_addc_u32 s59, s59, 0\n" \
  "s_cmp_eq_u32 s82, s74\n" \
  "s_addc_u32 s59, s59, 0\n" \
  "s_cmp_eq_u32 s84, s74\n" \
  "s_addc_u32 s59, s59, 0\n" \
  "s_cmp_eq_u32 s86, s74\n" \
  "s_addc_u32 s59, s59, 0\n" \
  "s_cmp_eq_u32 s88, s74\n" \
  "s_addc_u32 s59, s59, 0\n" \
  "s_cmp_eq_u32 s90, s74\n" \
  "s_addc_u32 s59, s59, 0\n" \
  "s_mul_i32 s59, s59, 12\n" \
  "s_add_u32 s68, s72, s59\n" \
  "s_add_u32 s59, s74, 4\n" \
  "s_mul_i32 s69, s59, 24\n" \
  "v_cvt_pk_bf16_f32 v168, v184, v185\n" \
  "v_cvt_pk_bf16_f32 v169, v186, v187\n" \
  "v_cvt_pk_bf16_f32 v170, v188, v189\n" \
  "v_cvt_pk_bf16_f32 v171, v190, v191\n" \
  "v_cvt_pk_bf16_f32 v172, v192, v193\n" \
  "v_cvt_pk_bf16_f32 v173, v194, v195\n" \
  "v_cvt_pk_bf16_f32 v174, v196, v197\n" \
  "v_cvt_pk_bf16_f32 v175, v198, v199\n" \
  "s_sub_u32 s59, s69, s68\n" \
  "s_sub_i32 s60, s59, s62\n" \
  "s_mov_b32 s62, s59\n" \
  "s_sub_u32 s59, s68, s56\n" \
  "s_mov_b32 s56, s68\n" \
  "v_cvt_f32_u32 v27, s59\n" \
  "v_cvt_f32_i32 v28, s60\n" \
  "v_rcp_f32 v27, v27\n" \
  "v_mul_f32 v28, 0x45900000, v28\n" \
  "v_mul_f32 v27, v27, v28\n" \
  "v_cvt_i32_f32 v27, v27\n" \
  "s_nop 0\n" \
  "v_readfirstlane_b32 s63, v27\n" \
  "s_mul_i32 s63, s63, (" STR(F_B) "+" STR(F_A) "+130)/18\n" \
  "s_ashr_i32 s63, s63, 8\n" \
  "s_min_i32 s63, s63, 1500\n" \
  "s_max_i32 s63, s63, -1500\n" \
  "s_add_u32 s49, s53, s93\n" \
  "v_mov_b64 v[48:49], 0\n" \
  "v_mov_b64 v[50:51], 0\n" \
  "v_mov_b64 v[52:53], 0\n" \
  "v_mov_b64 v[54:55], 0\n" \
  "v_mov_b64 v[56:57], 0\n" \
  "v_mov_b64 v[58:59], 0\n" \
  "v_mov_b64 v[60:61], 0\n" \
  "v_mov_b64 v[62:63], 0\n" \
  "v_mov_b64 v[64:65], 0\n" \
  "v_mov_b64 v[66:67], 0\n" \
  "v_mov_b64 v[68:69], 0\n" \
  "v_mov_b64 v[70:71], 0\n" \
  "s_cmp_gt_u32 %7, 1\n" \
  "s_cbranch_scc1 LM_%=\n" \
  ".p2align 6\n" \
  "LL_%=:\n" \
  "s_barrier\n" \
  "s_memtime s[64:65]\n" \
  "buffer_load_dwordx4 v[32:35], v80, s[36:39], s49 offen sc1\n" \
  "v_mfma_f32_16x16x32_bf16 v[184:187], v[136:139], a[224:227], 0\n" \
  "s_xor_b32 s51, s51, 1<<20\n" \
  "s_xor_b32 s52, s52, 1<<20\n" \
  "v_mfma_f32_16x16x32_bf16 v[188:191], v[152:155], a[224:227], 0\n" \
  "s_add_u32 s53, s53, 32768\n" \
  "s_lshl_b32 s60, s48, 19\n" \
  "v_mfma_f32_16x16x32_bf16 v[192:195], v[136:139], a[240:243], 0\n" \
  "s_add_u32 s60, s60, " STR(EXP_B) "<<21\n" \
  "s_lshr_b32 s59, s60, 21\n" \
  "buffer_load_dwordx4 v[36:39], v81, s[36:39], s49 offen sc1\n" \
  "v_mfma_f32_16x16x32_bf16 v[196:199], v[152:155], a[240:243], 0\n" \
  "s_lshr_b32 s59, %8, s59\n" \
  "s_and_b32 s94, s59, 1\n" \
  "v_mfma_f32_16x16x32_bf16 v[184:187], v[140:143], a[228:231], v[184:187]\n" \
  "s_lshl_b32 s94, s94, 13\n" \
  "s_or_b32 s50, s60, s94\n" \
  "v_mfma_f32_16x16x32_bf16 v[188:191], v[156:159], a[228:231], v[188:191]\n" \
  "s_lshl_b32 s94, s70, 14\n" \
  "s_add_u32 s91, s50, s94\n" \
  "buffer_load_dwordx4 v[40:43], v82, s[36:39], s49 offen sc1\n" \
  "v_mfma_f32_16x16x32_bf16 v[192:195], v[140:143], a[244:247], v[192:195]\n" \
  "s_and_b32 s94, s59, 2\n" \
  "s_lshl_b32 s94, s94, 12\n" \
  "v_mfma_f32_16x16x32_bf16 v[196:199], v[156:159], a[244:247], v[196:199]\n" \
  "s_add_u32 s60, s60, 1<<21\n" \
  "s_or_b32 s54, s60, s94\n" \
  "v_mfma_f32_16x16x32_bf16 v[184:187], v[144:147], a[232:235], v[184:187]\n" \
  "buffer_load_dwordx4 v[44:47], v83, s[36:39], s49 offen sc1\n" \
  "v_mfma_f32_16x16x32_bf16 v[188:191], v[160:163], a[232:235], v[188:191]\n" \
  "v_mfma_f32_16x16x32_bf16 v[192:195], v[144:147], a[248:251], v[192:195]\n" \
  "v_mfma_f32_16x16x32_bf16 v[196:199], v[160:163], a[248:251], v[196:199]\n" \
  "v_mfma_f32_16x16x32_bf16 v[184:187], v[148:151], a[236:239], v[184:187]\n" \
  "v_mfma_f32_16x16x32_bf16 v[188:191], v[164:167], a[236:239], v[188:191]\n" \
  "buffer_store_dwordx4 v[88:91], v11, s[36:39], s52 offen sc1\n" \
  "buffer_store_dwordx4 v[88:91], v15, s[36:39], s54 offen sc1\n" \
  "v_mfma_f32_16x16x32_bf16 v[192:195], v[148:151], a[252:255], v[192:195]\n" \
  "v_mfma_f32_16x16x32_bf16 v[196:199], v[164:167], a[252:255], v[196:199]\n" \
  "s_waitcnt lgkmcnt(0)\n" \
  "s_sub_i32 s59, s64, s68\n" \
  "s_mul_i32 s59, s59, %7\n" \
  "s_cmp_gt_i32 s59, " STR(T_LATE) "\n" \
  "s_cbranch_scc1 LT_%=\n" \
  "LT2_%=:\n" \
  "s_nop 1\n" \
  "v_cvt_pk_bf16_f32 v176, v184, v185\n" \
  "v_cvt_pk_bf16_f32 v177, v186, v187\n" \
  "v_cvt_pk_bf16_f32 v178, v188, v189\n" \
  "v_cvt_pk_bf16_f32 v179, v190, v191\n" \
  "v_cvt_pk_bf16_f32 v180, v192, v193\n" \
  "v_cvt_pk_bf16_f32 v181, v194, v195\n" \
  "v_cvt_pk_bf16_f32 v182, v196, v197\n" \
  "v_cvt_pk_bf16_f32 v183, v198, v199\n" \
  "LS1_%=:\n" \
  "s_waitcnt vmcnt(5)\n" \
  "v_smfmac_f32_16x16x64_bf16 v[52:55], v[32:35], a[8:15], v2\n" \
  "v_smfmac_f32_16x16x64_bf16 v[56:59], v[32:35], a[16:23], v2\n" \
  "v_smfmac_f32_16x16x64_bf16 v[60:63], v[32:35], a[24:31], v2\n" \
  "v_smfmac_f32_16x16x64_bf16 v[48:51], v[32:35], a[0:7], v2\n" \
  "s_waitcnt vmcnt(4)\n" \
  "v_smfmac_f32_16x16x64_bf16 v[52:55], v[36:39], a[40:47], v2\n" \
  "v_smfmac_f32_16x16x64_bf16 v[56:59], v[36:39], a[48:55], v2\n" \
  "v_smfmac_f32_16x16x64_bf16 v[60:63], v[36:39], a[56:63], v2\n" \
  "v_smfmac_f32_16x16x64_bf16 v[48:51], v[36:39], a[32:39], v2\n" \
  "s_waitcnt vmcnt(3)\n" \
  "v_smfmac_f32_16x16x64_bf16 v[52:55], v[40:43], a[72:79], v2\n" \
  "v_smfmac_f32_16x16x64_bf16 v[56:59], v[40:43], a[80:87], v2\n" \
  "v_smfmac_f32_16x16x64_bf16 v[60:63], v[40:43], a[88:95], v2\n" \
  "v_smfmac_f32_16x16x64_bf16 v[48:51], v[40:43], a[64:71], v2\n" \
  "s_waitcnt vmcnt(2)\n" \
  "v_smfmac_f32_16x16x64_bf16 v[52:55], v[44:47], a[104:111], v2\n" \
  "v_smfmac_f32_16x16x64_bf16 v[56:59], v[44:47], a[112:119], v2\n" \
  "v_smfmac_f32_16x16x64_bf16 v[60:63], v[44:47], a[120:127], v2\n" \
  "v_add_f32 v52, v52, v54\n" \
  "v_add_f32 v53, v53, v55\n" \
  "v_smfmac_f32_16x16x64_bf16 v[48:51], v[44:47], a[96:103], v2\n" \
  "v_add_f32 v56, v56, v58\n" \
  "v_add_f32 v57, v57, v59\n" \
  "ds_write_b64 v3, v[52:53] offset:0\n" \
  "v_add_f32 v60, v60, v62\n" \
  "v_add_f32 v61, v61, v63\n" \
  "v_smfmac_f32_16x16x64_bf16 v[64:67], v[32:35], a[128:135], v2\n" \
  "ds_write_b64 v244, v[56:57] offset:512\n" \
  "v_smfmac_f32_16x16x64_bf16 v[68:71], v[32:35], a[136:143], v2\n" \
  "ds_write_b64 v245, v[60:61] offset:1024\n" \
  "v_cmp_u_f32 vcc, v52, v53\n" \
  "v_smfmac_f32_16x16x64_bf16 v[64:67], v[36:39], a[144:151], v2\n" \
  "v_add_f32 v48, v48, v50\n" \
  "v_add_f32 v49, v49, v51\n" \
  "v_smfmac_f32_16x16x64_bf16 v[68:71], v[36:39], a[152:159], v2\n" \
  "v_smfmac_f32_16x16x64_bf16 v[64:67], v[40:43], a[160:167], v2\n" \
  "s_cbranch_vccnz LA_%=\n" \
  "LA4_%=:\n" \
  "s_waitcnt lgkmcnt(0)\n" \
  "s_barrier\n" \
  "ds_read_b64 v[72:73], v6 offset:0\n" \
  "ds_read_b64 v[74:75], v6 offset:512\n" \
  "ds_read_b64 v[76:77], v6 offset:1024\n" \
  "ds_read_b32 v26, v24 offset:14336\n" \
  "v_smfmac_f32_16x16x64_bf16 v[68:71], v[40:43], a[168:175], v2\n" \
  "v_smfmac_f32_16x16x64_bf16 v[64:67], v[44:47], a[176:183], v2\n" \
  "s_waitcnt lgkmcnt(3)\n" \
  "v_add_f32 v48, v48, v72\n" \
  "v_add_f32 v49, v49, v73\n" \
  "s_waitcnt lgkmcnt(1)\n" \
  "v_add_f32 v74, v74, v76\n" \
  "v_add_f32 v75, v75, v77\n" \
  "v_add_f32 v48, v48, v74\n" \
  "v_add_f32 v49, v49, v75\n" \
  "v_cvt_pk_bf16_f32 v27, v48, v49\n" \
  "v_smfmac_f32_16x16x64_bf16 v[68:71], v[44:47], a[184:191], v2\n" \
  "v_mov_b32_dpp v28, v27 quad_perm:[1,0,3,2] row_mask:0xf bank_mask:0xf\n" \
  "v_perm_b32 v27, v28, v27, v246\n" \
  "buffer_store_dword v27, v8, s[36:39], s50 offen sc1\n" \
  "v_add_f32 v64, v64, v66\n" \
  "v_add_f32 v65, v65, v67\n" \
  "s_nop 2\n" \
  "v_add_f32 v68, v68, v70\n" \
  "v_add_f32 v69, v69, v71\n" \
  "ds_write2_b32 v5, v64, v65 offset0:0 offset1:40\n" \
  "ds_write2_b32 v5, v68, v69 offset0:16 offset1:56\n" \
  "v_mov_b64 v[184:185], 0\n" \
  "s_add_u32 s49, s53, s93\n" \
  "s_add_u32 s60, s48, 1\n" \
  "s_add_i32 s61, s64, " STR(F_B) "\n" \
  "v_mov_b64 v[186:187], 0\n" \
  "s_movk_i32 s94, " STR(X_FAIL) "\n" \
  "s_nop 0\n" \
  "v_mov_b64 v[188:189], 0\n" \
  "s_waitcnt lgkmcnt(0)\n" \
  "s_barrier\n" \
  "s_memtime s[66:67]\n" \
  "ds_read_b64 v[72:73], v7 offset:0\n" \
  "ds_read_b64 v[74:75], v7 offset:1280\n" \
  "ds_read_b64 v[76:77], v7 offset:2560\n" \
  "ds_read_b64 v[78:79], v7 offset:3840\n" \
  "v_readfirstlane_b32 s59, v26\n" \
  "s_cmp_eq_u32 s57, 0\n" \
  "s_cselect_b32 s57, s94, " STR(X_FAIL2) "\n" \
  "s_cmp_eq_u32 s59, s60\n" \
  "v_mov_b64 v[190:191], 0\n" \
  "s_cselect_b32 s57, s57, 0\n" \
  "s_nop 1\n" \
  "v_mov_b64 v[192:193], 0\n" \
  "s_cmp_eq_u32 s48, 63\n" \
  "s_waitcnt lgkmcnt(0)\n" \
  "v_pk_add_f32 v[72:73], v[72:73], v[74:75]\n" \
  "v_pk_add_f32 v[76:77], v[76:77], v[78:79]\n" \
  "v_pk_add_f32 v[72:73], v[72:73], v[76:77]\n" \
  "v_cvt_pk_bf16_f32 v27, v72, v73\n" \
  "buffer_store_dword v27, v9, s[36:39], s50 offen sc1\n" \
  "s_cbranch_scc1 LE_%=\n" \
  "s_sub_i32 s61, s61, s66\n" \
  "s_max_i32 s61, s61, s57\n" \
  "s_add_u32 s94, s66, s61\n" \
  "s_cmp_gt_i32 s61, 24\n" \
  "s_cbranch_scc1 LW_b_%=\n" \
  "LB_b_%=:\n" \
  "buffer_load_dwordx4 v[32:35], v80, s[36:39], s91 offen sc1\n" \
  "s_add_u32 s59, s94, s62\n" \
  "s_add_u32 s59, s59, s63\n" \
  "s_add_u32 s59, s59, s57\n" \
  "s_add_u32 s59, s59, s57\n" \
  "s_add_u32 s54, s94, s57\n" \
  "s_add_u32 s54, s54, " STR(F_C) "-" STR(F_B) "\n" \
  "s_add_u32 s60, s64, " STR(F_C) "\n" \
  "s_cmp_eq_u32 s57, 0\n" \
  "s_cselect_b32 s54, s60, s54\n" \
  "s_min_u32 s59, s59, -2\n" \
  "v_mov_b32 v28, s59\n" \
  "buffer_store_dword v28, v14, s[36:39], s50 offen sc1\n" \
  "v_mov_b64 v[194:195], 0\n" \
  "v_mov_b64 v[196:197], 0\n" \
  "v_mov_b64 v[198:199], 0\n" \
  "buffer_load_dwordx4 v[36:39], v81, s[36:39], s91 offen sc1\n" \
  "ds_read_b64_tr_b16 v[136:137], v20 offset:0\n" \
  "ds_read_b64_tr_b16 v[138:139], v21 offset:0\n" \
  "ds_read_b64_tr_b16 v[152:153], v20 offset:8\n" \
  "ds_read_b64_tr_b16 v[154:155], v21 offset:8\n" \
  "s_nop " STR(GAP_B) "\n" \
  "buffer_load_dwordx4 v[40:43], v82, s[36:39], s91 offen sc1\n" \
  "ds_read_b64_tr_b16 v[140:141], v20 offset:2048\n" \
  "ds_read_b64_tr_b16 v[142:143], v21 offset:2048\n" \
  "ds_read_b64_tr_b16 v[156:157], v20 offset:2056\n" \
  "ds_read_b64_tr_b16 v[158:159], v21 offset:2056\n" \
  "s_nop " STR(GAP_B) "\n" \
  "buffer_load_dwordx4 v[44:47], v83, s[36:39], s91 offen sc1\n" \
  "ds_read_b64_tr_b16 v[144:145], v20 offset:4096\n" \
  "ds_read_b64_tr_b16 v[146:147], v21 offset:4096\n" \
  "ds_read_b64_tr_b16 v[160:161], v20 offset:4104\n" \
  "ds_read_b64_tr_b16 v[162:163], v21 offset:4104\n" \
  "s_nop " STR(GAP_V) "\n" \
  "buffer_load_dwordx4 v[96:99], v17, s[36:39], s50 offen offset:0 sc1\n" \
  "ds_read_b64_tr_b16 v[148:149], v20 offset:6144\n" \
  "ds_read_b64_tr_b16 v[150:151], v21 offset:6144\n" \
  "ds_read_b64_tr_b16 v[164:165], v20 offset:6152\n" \
  "ds_read_b64_tr_b16 v[166:167], v21 offset:6152\n" \
  "s_nop " STR(GAP_B) "\n" \
  "buffer_load_dwordx4 v[100:103], v17, s[36:39], s50 offen offset:1024 sc1\n" \
  "s_mov_b32 vcc_lo, 0x33333333\n" \
  "s_mov_b32 vcc_hi, 0x33333333\n" \
  "LS2_%=:\n" \
  "s_waitcnt vmcnt(6)\n" \
  "v_mfma_f32_16x16x32_bf16 v[228:231], v[120:123], v[32:35], 0\n" \
  "v_mfma_f32_16x16x32_bf16 v[224:227], v[104:107], v[32:35], 0\n" \
  "v_mfma_f32_16x16x32_bf16 v[232:235], v[32:35], v[32:35], 0\n" \
  "s_waitcnt vmcnt(4)\n" \
  "v_mfma_f32_16x16x32_bf16 v[228:231], v[124:127], v[36:39], v[228:231]\n" \
  "v_mfma_f32_16x16x32_bf16 v[224:227], v[108:111], v[36:39], v[224:227]\n" \
  "v_mfma_f32_16x16x32_bf16 v[232:235], v[36:39], v[36:39], v[232:235]\n" \
  "s_waitcnt vmcnt(3)\n" \
  "v_mfma_f32_16x16x32_bf16 v[228:231], v[128:131], v[40:43], v[228:231]\n" \
  "v_mfma_f32_16x16x32_bf16 v[224:227], v[112:115], v[40:43], v[224:227]\n" \
  "v_mfma_f32_16x16x32_bf16 v[232:235], v[40:43], v[40:43], v[232:235]\n" \
  "s_waitcnt vmcnt(2)\n" \
  "v_mfma_f32_16x16x32_bf16 v[228:231], v[132:135], v[44:47], v[228:231]\n" \
  "v_mfma_f32_16x16x32_bf16 v[224:227], v[116:119], v[44:47], v[224:227]\n" \
  "v_mfma_f32_16x16x32_bf16 v[232:235], v[44:47], v[44:47], v[232:235]\n" \
  "s_nop 3\n" \
  "v_cndmask_b32_dpp v224, v228, v224, vcc quad_perm:[2,3,0,1] row_mask:0xf bank_mask:0xf\n" \
  "v_cndmask_b32_dpp v225, v229, v225, vcc quad_perm:[2,3,0,1] row_mask:0xf bank_mask:0xf\n" \
  "v_cndmask_b32_dpp v226, v230, v226, vcc quad_perm:[2,3,0,1] row_mask:0xf bank_mask:0xf\n" \
  "v_cndmask_b32_dpp v227, v231, v227, vcc quad_perm:[2,3,0,1] row_mask:0xf bank_mask:0xf\n" \
  "v_cndmask_b32_dpp v234, v235, v234, vcc quad_perm:[2,3,0,1] row_mask:0xf bank_mask:0xf\n" \
  "v_max3_f32 v25, v224, v225, v226\n" \
  "v_max3_f32 v25, v25, v227, v234\n" \
  "s_waitcnt vmcnt(1)\n" \
  "v_smfmac_f32_16x16x64_bf16 v[184:187], v[96:99], a[192:199], v2\n" \
  "v_max_f32_dpp v26, v25, v25 quad_perm:[2,3,0,1] row_mask:0xf bank_mask:0xf\n" \
  "v_max_f32_dpp v25, v25, v25 quad_perm:[2,3,0,1] row_mask:0xf bank_mask:0xf\n" \
  "v_smfmac_f32_16x16x64_bf16 v[188:191], v[96:99], a[208:215], v2\n" \
  "v_permlane16_swap_b32 v25, v26\n" \
  "v_max_f32 v27, v25, v26\n" \
  "v_max_f32 v25, v25, v26\n" \
  "v_smfmac_f32_16x16x64_bf16 v[192:195], v[96:99], a[224:231], v2\n" \
  "v_permlane32_swap_b32 v25, v27\n" \
  "v_max_f32 v25, v25, v27\n" \
  "v_mul_f32 v25, s92, v25\n" \
  "v_fma_f32 v224, v224, s92, -v25\n" \
  "v_smfmac_f32_16x16x64_bf16 v[196:199], v[96:99], a[240:247], v2\n" \
  "v_fma_f32 v225, v225, s92, -v25\n" \
  "v_exp_f32 v224, v224\n" \
  "v_exp_f32 v225, v225\n" \
  "s_waitcnt vmcnt(0)\n" \
  "v_smfmac_f32_16x16x64_bf16 v[184:187], v[100:103], a[200:207], v2\n" \
  "v_fma_f32 v226, v226, s92, -v25\n" \
  "v_smfmac_f32_16x16x64_bf16 v[188:191], v[100:103], a[216:223], v2\n" \
  "v_fma_f32 v227, v227, s92, -v25\n" \
  "v_smfmac_f32_16x16x64_bf16 v[192:195], v[100:103], a[232:239], v2\n" \
  "v_exp_f32 v226, v226\n" \
  "v_smfmac_f32_16x16x64_bf16 v[196:199], v[100:103], a[248:255], v2\n" \
  "v_exp_f32 v227, v227\n" \
  "v_cvt_pk_bf16_f32 v228, v224, v225\n" \
  "v_cvt_pk_bf16_f32 v229, v226, v227\n" \
  "v_and_b32 v236, v228, v4\n" \
  "v_and_b32 v237, v229, v4\n" \
  "v_and_b32_dpp v238, v228, v4 quad_perm:[2,3,0,1] row_mask:0xf bank_mask:0xf\n" \
  "v_and_b32_dpp v239, v229, v4 quad_perm:[2,3,0,1] row_mask:0xf bank_mask:0xf\n" \
  "v_fma_f32 v234, v234, s92, -v25\n" \
  "v_and_b32_dpp v240, v228, v29 quad_perm:[2,3,0,1] row_mask:0xf bank_mask:0xf\n" \
  "v_mfma_f32_16x16x32_bf16 v[48:51], v[168:171], v[236:239], 0\n" \
  "v_and_b32_dpp v241, v229, v29 quad_perm:[2,3,0,1] row_mask:0xf bank_mask:0xf\n" \
  "v_and_b32 v242, v228, v29\n" \
  "v_mfma_f32_16x16x32_bf16 v[52:55], v[176:179], v[236:239], 0\n" \
  "v_and_b32 v243, v229, v29\n" \
  "v_exp_f32 v234, v234\n" \
  "v_cvt_pk_bf16_f32 v216, v184, v185\n" \
  "v_mfma_f32_16x16x32_bf16 v[48:51], v[172:175], v[240:243], v[48:51]\n" \
  "v_mov_b32_dpp v25, v234 quad_perm:[0,1,0,1] row_mask:0xf bank_mask:0xf\n" \
  "v_mov_b32_dpp v26, v234 quad_perm:[2,3,2,3] row_mask:0xf bank_mask:0xf\n" \
  "v_mfma_f32_16x16x32_bf16 v[52:55], v[180:183], v[240:243], v[52:55]\n" \
  "v_cvt_pk_bf16_f32 v25, v25, v26\n" \
  "v_and_b32 v230, v25, v4\n" \
  "v_and_b32 v231, v25, v4\n" \
  "v_and_b32 v232, v25, v29\n" \
  "v_and_b32 v233, v25, v29\n" \
  "v_mfma_f32_16x16x32_bf16 v[56:59], v[252:255], v[228:231], 0\n" \
  "v_cvt_pk_bf16_f32 v217, v186, v187\n" \
  "v_cvt_pk_bf16_f32 v218, v188, v189\n" \
  "v_cvt_pk_bf16_f32 v219, v190, v191\n" \
  "v_cvt_pk_bf16_f32 v220, v192, v193\n" \
  "v_cvt_pk_bf16_f32 v221, v194, v195\n" \
  "v_mfma_f32_16x16x32_bf16 v[48:51], v[216:219], v[230:233], v[48:51]\n" \
  "v_cvt_pk_bf16_f32 v222, v196, v197\n" \
  "v_cvt_pk_bf16_f32 v223, v198, v199\n" \
  "v_add_f32_dpp v56, v56, v56 quad_perm:[2,3,0,1] row_mask:0xf bank_mask:0xf\n" \
  "v_rcp_f32 v56, v56\n" \
  "v_mfma_f32_16x16x32_bf16 v[52:55], v[220:223], v[230:233], v[52:55]\n" \
  "s_nop 2\n" \
  "v_cmp_u_f32 vcc, v48, v48\n" \
  "v_mul_f32 v48, v48, v56\n" \
  "v_mul_f32 v49, v49, v56\n" \
  "v_mul_f32 v50, v50, v56\n" \
  "v_mul_f32 v51, v51, v56\n" \
  "v_pk_mul_f32 v[52:53], v[52:53], v[56:57] op_sel_hi:[1,0]\n" \
  "v_pk_mul_f32 v[54:55], v[54:55], v[56:57] op_sel_hi:[1,0]\n" \
  "v_cvt_pk_bf16_f32 v72, v48, v49\n" \
  "v_cvt_pk_bf16_f32 v73, v50, v51\n" \
  "v_cvt_pk_bf16_f32 v74, v52, v53\n" \
  "v_cvt_pk_bf16_f32 v75, v54, v55\n" \
  "s_cbranch_vccnz LBF_%=\n" \
  "LBG_%=:\n" \
  "buffer_store_dwordx4 v[72:75], v10, s[36:39], s51 offen sc1\n" \
  "s_memtime s[66:67]\n" \
  "s_mov_b32 s61, s54\n" \
  "s_mov_b32 s59, " STR(M_C) "\n" \
  "s_cmp_lg_u32 %7, 0\n" \
  "s_waitcnt lgkmcnt(0)\n" \
  "s_cbranch_scc1 LO_%=\n" \
  "s_sub_i32 s61, s61, s66\n" \
  "s_max_i32 s61, s61, s59\n" \
  "s_cmp_gt_i32 s61, 24\n" \
  "s_cbranch_scc1 LW_c_%=\n" \
  "LB_c_%=:\n" \
  "buffer_load_dwordx4 v[32:35], v11, s[36:39], s51 offen offset:0 sc1\n" \
  "buffer_load_dwordx4 v[36:39], v11, s[36:39], s51 offen offset:1024 sc1\n" \
  "buffer_load_dwordx4 v[40:43], v11, s[36:39], s51 offen offset:2048 sc1\n" \
  "buffer_load_dwordx4 v[44:47], v11, s[36:39], s51 offen offset:3072 sc1\n" \
  "s_mov_b32 s61, " STR(M_A) "\n" \
  "v_mfma_f32_16x16x32_bf16 v[184:187], v[136:139], a[192:195], 0\n" \
  "v_mov_b64 v[48:49], 0\n" \
  "v_mfma_f32_16x16x32_bf16 v[188:191], v[152:155], a[192:195], 0\n" \
  "v_mov_b64 v[50:51], 0\n" \
  "v_mfma_f32_16x16x32_bf16 v[192:195], v[136:139], a[208:211], 0\n" \
  "v_mov_b64 v[52:53], 0\n" \
  "v_mfma_f32_16x16x32_bf16 v[196:199], v[152:155], a[208:211], 0\n" \
  "v_mov_b64 v[54:55], 0\n" \
  "v_mfma_f32_16x16x32_bf16 v[184:187], v[140:143], a[196:199], v[184:187]\n" \
  "v_mov_b64 v[56:57], 0\n" \
  "v_mfma_f32_16x16x32_bf16 v[188:191], v[156:159], a[196:199], v[188:191]\n" \
  "v_mov_b64 v[58:59], 0\n" \
  "v_mfma_f32_16x16x32_bf16 v[192:195], v[140:143], a[212:215], v[192:195]\n" \
  "v_mov_b64 v[60:61], 0\n" \
  "v_mfma_f32_16x16x32_bf16 v[196:199], v[156:159], a[212:215], v[196:199]\n" \
  "v_mov_b64 v[62:63], 0\n" \
  "v_mfma_f32_16x16x32_bf16 v[184:187], v[144:147], a[200:203], v[184:187]\n" \
  "v_mov_b64 v[64:65], 0\n" \
  "ds_read_b128 v[104:107], v84 offset:0\n" \
  "v_mfma_f32_16x16x32_bf16 v[188:191], v[160:163], a[200:203], v[188:191]\n" \
  "v_mov_b64 v[66:67], 0\n" \
  "ds_read_b128 v[108:111], v85 offset:0\n" \
  "v_mfma_f32_16x16x32_bf16 v[192:195], v[144:147], a[216:219], v[192:195]\n" \
  "v_mov_b64 v[68:69], 0\n" \
  "ds_read_b128 v[112:115], v86 offset:0\n" \
  "v_mfma_f32_16x16x32_bf16 v[196:199], v[160:163], a[216:219], v[196:199]\n" \
  "v_mov_b64 v[70:71], 0\n" \
  "ds_read_b128 v[116:119], v87 offset:0\n" \
  "v_mfma_f32_16x16x32_bf16 v[184:187], v[148:151], a[204:207], v[184:187]\n" \
  "ds_read_b128 v[120:123], v84 offset:4096\n" \
  "v_mfma_f32_16x16x32_bf16 v[188:191], v[164:167], a[204:207], v[188:191]\n" \
  "ds_read_b128 v[124:127], v85 offset:4096\n" \
  "v_mfma_f32_16x16x32_bf16 v[192:195], v[148:151], a[220:223], v[192:195]\n" \
  "ds_read_b128 v[128:131], v86 offset:4096\n" \
  "v_mfma_f32_16x16x32_bf16 v[196:199], v[164:167], a[220:223], v[196:199]\n" \
  "ds_read_b128 v[132:135], v87 offset:4096\n" \
  "s_add_u32 s48, s48, 1\n" \
  "s_nop 5\n" \
  "v_cvt_pk_bf16_f32 v168, v184, v185\n" \
  "v_cvt_pk_bf16_f32 v169, v186, v187\n" \
  "v_cvt_pk_bf16_f32 v170, v188, v189\n" \
  "v_cvt_pk_bf16_f32 v171, v190, v191\n" \
  "v_cvt_pk_bf16_f32 v172, v192, v193\n" \
  "v_cvt_pk_bf16_f32 v173, v194, v195\n" \
  "v_cvt_pk_bf16_f32 v174, v196, v197\n" \
  "v_cvt_pk_bf16_f32 v175, v198, v199\n" \
  "LS3_%=:\n" \
  "s_waitcnt vmcnt(3)\n" \
  "v_mfma_f32_16x16x32_bf16 v[224:227], v[248:251], v[32:35], 0\n" \
  "s_waitcnt vmcnt(2)\n" \
  "v_mfma_f32_16x16x32_bf16 v[224:227], v[248:251], v[36:39], v[224:227]\n" \
  "s_waitcnt vmcnt(1)\n" \
  "v_mfma_f32_16x16x32_bf16 v[224:227], v[248:251], v[40:43], v[224:227]\n" \
  "s_waitcnt vmcnt(0)\n" \
  "v_mfma_f32_16x16x32_bf16 v[224:227], v[248:251], v[44:47], v[224:227]\n" \
  "s_nop 7\n" \
  "v_cmp_u_f32 vcc, v224, v224\n" \
  "v_add_f32_dpp v224, v224, v224 row_ror:8 row_mask:0xf bank_mask:0xf\n" \
  "v_add_f32_dpp v225, v225, v225 row_ror:8 row_mask:0xf bank_mask:0xf\n" \
  "v_add_f32_dpp v226, v226, v226 row_ror:8 row_mask:0xf bank_mask:0xf\n" \
  "v_add_f32_dpp v227, v227, v227 row_ror:8 row_mask:0xf bank_mask:0xf\n" \
  "v_cvt_pk_bf16_f32 v228, v224, v225\n" \
  "v_cvt_pk_bf16_f32 v229, v226, v227\n" \
  "s_cbranch_vccnz LC_%=\n" \
  "LC2_%=:\n" \
  "v_permlane16_swap_b32 v228, v230\n" \
  "v_permlane16_swap_b32 v229, v231\n" \
  "buffer_store_dwordx4 v[228:231], v12, s[36:39], s53 offen sc1\n" \
  "s_waitcnt lgkmcnt(0)\n" \
  "s_cmp_gt_i32 s61, 24\n" \
  "s_cbranch_scc1 LW_a_%=\n" \
  "LB_a_%=:\n" \
  "s_branch LL_%=\n" \
  "LO_%=:\n" \
  "s_cmp_eq_u32 %7, 1\n" \
  "s_cbranch_scc1 LK_%=\n" \
  "s_sub_i32 s61, s61, s66\n" \
  "s_max_i32 s61, s61, s59\n" \
  "s_add_i32 s61, s61, " STR(D_PF) "\n" \
  "s_add_u32 s56, s48, 3\n" \
  "s_min_u32 s56, s56, 64\n" \
  "v_cmp_gt_u32 vcc, s56, v23\n" \
  "s_lshl_b32 s59, s70, 7\n" \
  "s_add_u32 s59, s59, " STR(CW_PROG) "*4\n" \
  "s_lshl_b32 s60, s71, 2\n" \
  "s_or_b32 s60, s60, %7\n" \
  "s_cmp_eq_u32 s60, 3\n" \
  "s_cselect_b32 s59, s59, 0x80000000\n" \
  "v_lshl_add_u32 v27, v0, 24, s59\n" \
  "v_add_u32 v28, s48, v255\n" \
  "v_add_u32 v28, 1, v28\n" \
  "buffer_store_dword v28, v27, s[44:47], 0 offen sc1\n" \
  "v_mfma_f32_16x16x32_bf16 v[184:187], v[136:139], a[192:195], 0\n" \
  "v_mov_b64 v[48:49], 0\n" \
  "v_mfma_f32_16x16x32_bf16 v[188:191], v[152:155], a[192:195], 0\n" \
  "v_mov_b64 v[50:51], 0\n" \
  "v_mfma_f32_16x16x32_bf16 v[192:195], v[136:139], a[208:211], 0\n" \
  "v_mov_b64 v[52:53], 0\n" \
  "v_mfma_f32_16x16x32_bf16 v[196:199], v[152:155], a[208:211], 0\n" \
  "v_mov_b64 v[54:55], 0\n" \
  "v_mfma_f32_16x16x32_bf16 v[184:187], v[140:143], a[196:199], v[184:187]\n" \
  "v_mov_b64 v[56:57], 0\n" \
  "v_mfma_f32_16x16x32_bf16 v[188:191], v[156:159], a[196:199], v[188:191]\n" \
  "v_mov_b64 v[58:59], 0\n" \
  "v_mfma_f32_16x16x32_bf16 v[192:195], v[140:143], a[212:215], v[192:195]\n" \
  "v_mov_b64 v[60:61], 0\n" \
  "v_mfma_f32_16x16x32_bf16 v[196:199], v[156:159], a[212:215], v[196:199]\n" \
  "v_mov_b64 v[62:63], 0\n" \
  "v_mfma_f32_16x16x32_bf16 v[184:187], v[144:147], a[200:203], v[184:187]\n" \
  "v_mov_b64 v[64:65], 0\n" \
  "ds_read_b128 v[104:107], v84 offset:0\n" \
  "v_mfma_f32_16x16x32_bf16 v[188:191], v[160:163], a[200:203], v[188:191]\n" \
  "v_mov_b64 v[66:67], 0\n" \
  "ds_read_b128 v[108:111], v85 offset:0\n" \
  "v_mfma_f32_16x16x32_bf16 v[192:195], v[144:147], a[216:219], v[192:195]\n" \
  "v_mov_b64 v[68:69], 0\n" \
  "ds_read_b128 v[112:115], v86 offset:0\n" \
  "v_mfma_f32_16x16x32_bf16 v[196:199], v[160:163], a[216:219], v[196:199]\n" \
  "v_mov_b64 v[70:71], 0\n" \
  "ds_read_b128 v[116:119], v87 offset:0\n" \
  "v_mfma_f32_16x16x32_bf16 v[184:187], v[148:151], a[204:207], v[184:187]\n" \
  "ds_read_b128 v[120:123], v84 offset:4096\n" \
  "v_mfma_f32_16x16x32_bf16 v[188:191], v[164:167], a[204:207], v[188:191]\n" \
  "ds_read_b128 v[124:127], v85 offset:4096\n" \
  "v_mfma_f32_16x16x32_bf16 v[192:195], v[148:151], a[220:223], v[192:195]\n" \
  "ds_read_b128 v[128:131], v86 offset:4096\n" \
  "v_mfma_f32_16x16x32_bf16 v[196:199], v[164:167], a[220:223], v[196:199]\n" \
  "ds_read_b128 v[132:135], v87 offset:4096\n" \
  "s_add_u32 s48, s48, 1\n" \
  "s_sub_u32 s60, s56, 1\n" \
  "s_lshr_b32 s59, s60, 2\n" \
  "s_xnor_b32 s59, s59, %8\n" \
  "s_and_b32 s59, s59, 1\n" \
  "s_lshl_b32 s59, s59, 13\n" \
  "s_lshl_b32 s55, s60, 19\n" \
  "s_or_b32 s55, s55, s59\n" \
  "s_cbranch_vccnz LF_%=\n" \
  "LF2_%=:\n" \
  "s_cmp_gt_i32 s61, 24\n" \
  "s_cbranch_scc1 LW_p_%=\n" \
  "LB_p_%=:\n" \
  "buffer_load_dwordx4 v[200:203], v247, s[40:43], s55 offen offset:0 sc1\n" \
  "buffer_load_dwordx4 v[204:207], v247, s[40:43], s55 offen offset:1024 sc1\n" \
  "buffer_load_dwordx4 v[208:211], v247, s[40:43], s55 offen offset:2048 sc1\n" \
  "buffer_load_dwordx4 v[212:215], v247, s[40:43], s55 offen offset:3072 sc1\n" \
  "buffer_load_dwordx4 v[216:219], v16, s[40:43], s55 offen offset:0 sc1\n" \
  "buffer_load_dwordx4 v[220:223], v16, s[40:43], s55 offen offset:1024 sc1\n" \
  "buffer_load_dwordx4 v[224:227], v16, s[40:43], s55 offen offset:2048 sc1\n" \
  "buffer_load_dwordx4 v[228:231], v16, s[40:43], s55 offen offset:3072 sc1\n" \
  "buffer_load_dword v23, v22, s[44:47], 0 offen sc1\n" \
  "v_cvt_pk_bf16_f32 v168, v184, v185\n" \
  "v_cvt_pk_bf16_f32 v169, v186, v187\n" \
  "v_cvt_pk_bf16_f32 v170, v188, v189\n" \
  "v_cvt_pk_bf16_f32 v171, v190, v191\n" \
  "v_cvt_pk_bf16_f32 v172, v192, v193\n" \
  "v_cvt_pk_bf16_f32 v173, v194, v195\n" \
  "v_cvt_pk_bf16_f32 v174, v196, v197\n" \
  "v_cvt_pk_bf16_f32 v175, v198, v199\n" \
  "v_mfma_f32_16x16x32_bf16 v[184:187], v[136:139], a[224:227], 0\n" \
  "v_mfma_f32_16x16x32_bf16 v[188:191], v[152:155], a[224:227], 0\n" \
  "v_mfma_f32_16x16x32_bf16 v[192:195], v[136:139], a[240:243], 0\n" \
  "v_mfma_f32_16x16x32_bf16 v[196:199], v[152:155], a[240:243], 0\n" \
  "v_mfma_f32_16x16x32_bf16 v[184:187], v[140:143], a[228:231], v[184:187]\n" \
  "v_mfma_f32_16x16x32_bf16 v[188:191], v[156:159], a[228:231], v[188:191]\n" \
  "v_mfma_f32_16x16x32_bf16 v[192:195], v[140:143], a[244:247], v[192:195]\n" \
  "v_mfma_f32_16x16x32_bf16 v[196:199], v[156:159], a[244:247], v[196:199]\n" \
  "v_mfma_f32_16x16x32_bf16 v[184:187], v[144:147], a[232:235], v[184:187]\n" \
  "v_mfma_f32_16x16x32_bf16 v[188:191], v[160:163], a[232:235], v[188:191]\n" \
  "v_mfma_f32_16x16x32_bf16 v[192:195], v[144:147], a[248:251], v[192:195]\n" \
  "v_mfma_f32_16x16x32_bf16 v[196:199], v[160:163], a[248:251], v[196:199]\n" \
  "v_mfma_f32_16x16x32_bf16 v[184:187], v[148:151], a[236:239], v[184:187]\n" \
  "v_mfma_f32_16x16x32_bf16 v[188:191], v[164:167], a[236:239], v[188:191]\n" \
  "v_mfma_f32_16x16x32_bf16 v[192:195], v[148:151], a[252:255], v[192:195]\n" \
  "v_mfma_f32_16x16x32_bf16 v[196:199], v[164:167], a[252:255], v[196:199]\n" \
  "s_nop 7\n" \
  "v_cvt_pk_bf16_f32 v176, v184, v185\n" \
  "v_cvt_pk_bf16_f32 v177, v186, v187\n" \
  "v_cvt_pk_bf16_f32 v178, v188, v189\n" \
  "v_cvt_pk_bf16_f32 v179, v190, v191\n" \
  "v_cvt_pk_bf16_f32 v180, v192, v193\n" \
  "v_cvt_pk_bf16_f32 v181, v194, v195\n" \
  "v_cvt_pk_bf16_f32 v182, v196, v197\n" \
  "v_cvt_pk_bf16_f32 v183, v198, v199\n" \
  "s_waitcnt lgkmcnt(0)\n" \
  "s_branch LM_%=\n" \
  "LK_%=:\n" \
  "buffer_load_dword v25, v13, s[36:39], s50 offen sc1\n" \
  "v_mfma_f32_16x16x32_bf16 v[184:187], v[136:139], a[192:195], 0\n" \
  "v_mov_b64 v[48:49], 0\n" \
  "v_mfma_f32_16x16x32_bf16 v[188:191], v[152:155], a[192:195], 0\n" \
  "v_mov_b64 v[50:51], 0\n" \
  "v_mfma_f32_16x16x32_bf16 v[192:195], v[136:139], a[208:211], 0\n" \
  "v_mov_b64 v[52:53], 0\n" \
  "v_mfma_f32_16x16x32_bf16 v[196:199], v[152:155], a[208:211], 0\n" \
  "v_mov_b64 v[54:55], 0\n" \
  "v_mfma_f32_16x16x32_bf16 v[184:187], v[140:143], a[196:199], v[184:187]\n" \
  "v_mov_b64 v[56:57], 0\n" \
  "v_mfma_f32_16x16x32_bf16 v[188:191], v[156:159], a[196:199], v[188:191]\n" \
  "v_mov_b64 v[58:59], 0\n" \
  "v_mfma_f32_16x16x32_bf16 v[192:195], v[140:143], a[212:215], v[192:195]\n" \
  "v_mov_b64 v[60:61], 0\n" \
  "v_mfma_f32_16x16x32_bf16 v[196:199], v[156:159], a[212:215], v[196:199]\n" \
  "v_mov_b64 v[62:63], 0\n" \
  "v_mfma_f32_16x16x32_bf16 v[184:187], v[144:147], a[200:203], v[184:187]\n" \
  "v_mov_b64 v[64:65], 0\n" \
  "ds_read_b128 v[104:107], v84 offset:0\n" \
  "v_mfma_f32_16x16x32_bf16 v[188:191], v[160:163], a[200:203], v[188:191]\n" \
  "v_mov_b64 v[66:67], 0\n" \
  "ds_read_b128 v[108:111], v85 offset:0\n" \
  "v_mfma_f32_16x16x32_bf16 v[192:195], v[144:147], a[216:219], v[192:195]\n" \
  "v_mov_b64 v[68:69], 0\n" \
  "ds_read_b128 v[112:115], v86 offset:0\n" \
  "v_mfma_f32_16x16x32_bf16 v[196:199], v[160:163], a[216:219], v[196:199]\n" \
  "v_mov_b64 v[70:71], 0\n" \
  "ds_read_b128 v[116:119], v87 offset:0\n" \
  "v_mfma_f32_16x16x32_bf16 v[184:187], v[148:151], a[204:207], v[184:187]\n" \
  "ds_read_b128 v[120:123], v84 offset:4096\n" \
  "v_mfma_f32_16x16x32_bf16 v[188:191], v[164:167], a[204:207], v[188:191]\n" \
  "ds_read_b128 v[124:127], v85 offset:4096\n" \
  "v_mfma_f32_16x16x32_bf16 v[192:195], v[148:151], a[220:223], v[192:195]\n" \
  "ds_read_b128 v[128:131], v86 offset:4096\n" \
  "v_mfma_f32_16x16x32_bf16 v[196:199], v[164:167], a[220:223], v[196:199]\n" \
  "ds_read_b128 v[132:135], v87 offset:4096\n" \
  "s_add_u32 s48, s48, 1\n" \
  "s_nop 5\n" \
  "v_cvt_pk_bf16_f32 v168, v184, v185\n" \
  "v_cvt_pk_bf16_f32 v169, v186, v187\n" \
  "v_cvt_pk_bf16_f32 v170, v188, v189\n" \
  "v_cvt_pk_bf16_f32 v171, v190, v191\n" \
  "v_cvt_pk_bf16_f32 v172, v192, v193\n" \
  "v_cvt_pk_bf16_f32 v173, v194, v195\n" \
  "v_cvt_pk_bf16_f32 v174, v196, v197\n" \
  "v_cvt_pk_bf16_f32 v175, v198, v199\n" \
  "s_waitcnt lgkmcnt(0)\n" \
  "s_memtime s[72:73]\n" \
  "s_memrealtime s[74:75]\n" \
  "s_nop 1\n" \
  "s_memrealtime s[76:77]\n" \
  "s_nop 1\n" \
  "s_memrealtime s[78:79]\n" \
  "s_nop 1\n" \
  "s_memrealtime s[80:81]\n" \
  "s_nop 1\n" \
  "s_memrealtime s[82:83]\n" \
  "s_nop 1\n" \
  "s_memrealtime s[84:85]\n" \
  "s_nop 1\n" \
  "s_memrealtime s[86:87]\n" \
  "s_nop 1\n" \
  "s_memrealtime s[88:89]\n" \
  "s_nop 1\n" \
  "s_memrealtime s[90:91]\n" \
  "s_mov_b32 s59, 0\n" \
  "s_waitcnt lgkmcnt(0)\n" \
  "s_cmp_eq_u32 s76, s74\n" \
  "s_addc_u32 s59, s59, 0\n" \
  "s_cmp_eq_u32 s78, s74\n" \
  "s_addc_u32 s59, s59, 0\n" \
  "s_cmp_eq_u32 s80, s74\n" \
  "s_addc_u32 s59, s59, 0\n" \
  "s_cmp_eq_u32 s82, s74\n" \
  "s_addc_u32 s59, s59, 0\n" \
  "s_cmp_eq_u32 s84, s74\n" \
  "s_addc_u32 s59, s59, 0\n" \
  "s_cmp_eq_u32 s86, s74\n" \
  "s_addc_u32 s59, s59, 0\n" \
  "s_cmp_eq_u32 s88, s74\n" \
  "s_addc_u32 s59, s59, 0\n" \
  "s_cmp_eq_u32 s90, s74\n" \
  "s_addc_u32 s59, s59, 0\n" \
  "s_mul_i32 s59, s59, 12\n" \
  "s_add_u32 s68, s72, s59\n" \
  "s_add_u32 s59, s74, 4\n" \
  "s_mul_i32 s69, s59, 24\n" \
  "s_sub_u32 s59, s69, s68\n" \
  "s_sub_i32 s60, s59, s62\n" \
  "s_mov_b32 s62, s59\n" \
  "s_sub_u32 s59, s68, s56\n" \
  "s_mov_b32 s56, s68\n" \
  "v_cvt_f32_u32 v27, s59\n" \
  "v_cvt_f32_i32 v28, s60\n" \
  "v_rcp_f32 v27, v27\n" \
  "v_mul_f32 v28, 0x45900000, v28\n" \
  "v_mul_f32 v27, v27, v28\n" \
  "v_cvt_i32_f32 v27, v27\n" \
  "s_nop 0\n" \
  "v_readfirstlane_b32 s63, v27\n" \
  "s_mul_i32 s63, s63, (" STR(F_B) "+" STR(F_A) "+130)/18\n" \
  "s_ashr_i32 s63, s63, 8\n" \
  "s_min_i32 s63, s63, 1500\n" \
  "s_max_i32 s63, s63, -1500\n" \
  "LK2_%=:\n" \
  "s_waitcnt vmcnt(0)\n" \
  "v_cmp_eq_u32 vcc, -1, v25\n" \
  "s_cbranch_vccnz LK3_%=\n" \
  "LK4_%=:\n" \
  "v_subrev_u32 v25, s69, v25\n" \
  "s_memtime s[66:67]\n" \
  "v_readlane_b32 s59, v25, 0\n" \
  "v_readlane_b32 s60, v25, 1\n" \
  "v_readlane_b32 s94, v25, 2\n" \
  "v_readlane_b32 s61, v25, 3\n" \
  "s_max_i32 s59, s59, s60\n" \
  "s_max_i32 s61, s61, s94\n" \
  "s_max_i32 s61, s61, s59\n" \
  "s_add_u32 s61, s61, s68\n" \
  "s_add_u32 s61, s61, " STR(F_A) "\n" \
  "s_mov_b32 s68, s61\n" \
  "s_waitcnt lgkmcnt(0)\n" \
  "s_sub_i32 s61, s61, s66\n" \
  "s_cmp_gt_i32 s61, 24\n" \
  "s_cbranch_scc1 LW_k_%=\n" \
  "LB_k_%=:\n" \
  "s_branch LL_%=\n" \
  ".p2align 6\n" \
  "LM_%=:\n" \
  "s_barrier\n" \
  "s_memtime s[64:65]\n" \
  "buffer_load_dwordx4 v[32:35], v80, s[36:39], s49 offen sc1\n" \
  "s_xor_b32 s51, s51, 1<<20\n" \
  "s_xor_b32 s52, s52, 1<<20\n" \
  "s_add_u32 s53, s53, 32768\n" \
  "s_lshl_b32 s60, s48, 19\n" \
  "s_add_u32 s60, s60, " STR(EXP_B) "<<21\n" \
  "s_lshr_b32 s59, s60, 21\n" \
  "s_nop 5\n" \
  "buffer_load_dwordx4 v[36:39], v81, s[36:39], s49 offen sc1\n" \
  "s_lshr_b32 s59, %8, s59\n" \
  "s_and_b32 s94, s59, 1\n" \
  "s_lshl_b32 s94, s94, 13\n" \
  "s_or_b32 s50, s60, s94\n" \
  "s_lshl_b32 s94, s70, 14\n" \
  "s_add_u32 s91, s50, s94\n" \
  "s_nop 5\n" \
  "buffer_load_dwordx4 v[40:43], v82, s[36:39], s49 offen sc1\n" \
  "s_and_b32 s94, s59, 2\n" \
  "s_lshl_b32 s94, s94, 12\n" \
  "s_add_u32 s60, s60, 1<<21\n" \
  "s_or_b32 s54, s60, s94\n" \
  "s_nop 7\n" \
  "buffer_load_dwordx4 v[44:47], v83, s[36:39], s49 offen sc1\n" \
  "s_waitcnt vmcnt(4)\n" \
  "ds_write_b128 v18, v[200:203] offset:0\n" \
  "ds_write_b128 v18, v[204:207] offset:1024\n" \
  "ds_write_b128 v18, v[208:211] offset:2048\n" \
  "ds_write_b128 v18, v[212:215] offset:3072\n" \
  "buffer_store_dwordx4 v[88:91], v11, s[36:39], s52 offen sc1\n" \
  "buffer_store_dwordx4 v[88:91], v15, s[36:39], s54 offen sc1\n" \
  "ds_write_b128 v18, v[216:219] offset:4096\n" \
  "ds_write_b128 v18, v[220:223] offset:5120\n" \
  "ds_write_b128 v18, v[224:227] offset:6144\n" \
  "ds_write_b128 v18, v[228:231] offset:7168\n" \
  "s_branch LS1_%=\n" \
  "LT_%=:\n" \
  "s_add_u32 s59, s48, 1\n" \
  "v_mov_b32 v27, s59\n" \
  "ds_write_b32 v24, v27 offset:14336\n" \
  "s_branch LT2_%=\n" \
  "LA_%=:\n" \
  "v_max3_u32 v25, v32, v33, v34\n" \
  "v_max3_u32 v25, v25, v35, v36\n" \
  "v_max3_u32 v25, v25, v37, v38\n" \
  "v_max3_u32 v25, v25, v39, v40\n" \
  "v_max3_u32 v25, v25, v41, v42\n" \
  "v_max3_u32 v25, v25, v43, v44\n" \
  "v_max3_u32 v25, v25, v45, v46\n" \
  "v_max_u32 v25, v25, v47\n" \
  "v_cmp_eq_u32 vcc, -1, v25\n" \
  "s_cbranch_vccz LA4_%=\n" \
  "LA1_%=:\n" \
  "buffer_load_dwordx4 v[32:35], v80, s[36:39], s49 offen sc1\n" \
  "buffer_load_dwordx4 v[36:39], v81, s[36:39], s49 offen sc1\n" \
  "buffer_load_dwordx4 v[40:43], v82, s[36:39], s49 offen sc1\n" \
  "buffer_load_dwordx4 v[44:47], v83, s[36:39], s49 offen sc1\n" \
  "s_add_u32 s59, s48, 1\n" \
  "v_mov_b32 v27, s59\n" \
  "ds_write_b32 v24, v27 offset:14336\n" \
  "s_waitcnt vmcnt(0) lgkmcnt(0)\n" \
  "v_max3_u32 v25, v32, v33, v34\n" \
  "v_max3_u32 v25, v25, v35, v36\n" \
  "v_max3_u32 v25, v25, v37, v38\n" \
  "v_max3_u32 v25, v25, v39, v40\n" \
  "v_max3_u32 v25, v25, v41, v42\n" \
  "v_max3_u32 v25, v25, v43, v44\n" \
  "v_max3_u32 v25, v25, v45, v46\n" \
  "v_max_u32 v25, v25, v47\n" \
  "v_cmp_eq_u32 vcc, -1, v25\n" \
  "s_cbranch_vccz LA2_%=\n" \
  "s_add_u32 s58, s58, 1\n" \
  "s_cmp_lt_u32 s58, " STR(SPIN_CAP) "\n" \
  "s_cbranch_scc1 LA1_%=\n" \
  "v_mov_b32 v27, " STR(CW_DEAD) "*4\n" \
  "v_mov_b32 v28, 1\n" \
  "buffer_store_dword v28, v27, s[44:47], 0 offen sc1\n" \
  "s_branch LA4_%=\n" \
  "LA2_%=:\n" \
  "v_mov_b64 v[48:49], 0\n" \
  "v_mov_b64 v[50:51], 0\n" \
  "v_mov_b64 v[52:53], 0\n" \
  "v_mov_b64 v[54:55], 0\n" \
  "v_mov_b64 v[56:57], 0\n" \
  "v_mov_b64 v[58:59], 0\n" \
  "v_mov_b64 v[60:61], 0\n" \
  "v_mov_b64 v[62:63], 0\n" \
  "v_mov_b64 v[64:65], 0\n" \
  "v_mov_b64 v[66:67], 0\n" \
  "v_mov_b64 v[68:69], 0\n" \
  "v_mov_b64 v[70:71], 0\n" \
  "s_nop 1\n" \
  "s_branch LS1_%=\n" \
  "LW_b_%=:\n" \
  "s_min_i32 s61, s61, 4000\n" \
  "s_sub_i32 s61, s61, 72\n" \
  "s_max_i32 s61, s61, 0\n" \
  "s_bitcmp0_b32 s61, 4\n" \
  "s_cbranch_scc1 LX_b_%=\n" \
  "s_nop 8\n" \
  "LX_b_%=:\n" \
  "s_lshr_b32 s61, s61, 5\n" \
  "s_cbranch_scc0 LB_b_%=\n" \
  ".p2align 4\n" \
  "LY_b_%=:\n" \
  "s_sub_u32 s61, s61, 1\n" \
  "s_cmp_lg_u32 s61, 0\n" \
  "s_cbranch_scc1 LY_b_%=\n" \
  "s_branch LB_b_%=\n" \
  "LBF_%=:\n" \
  "v_max3_u32 v25, v32, v33, v34\n" \
  "v_max3_u32 v25, v25, v35, v36\n" \
  "v_max3_u32 v25, v25, v37, v38\n" \
  "v_max3_u32 v25, v25, v39, v40\n" \
  "v_max3_u32 v25, v25, v41, v42\n" \
  "v_max3_u32 v25, v25, v43, v44\n" \
  "v_max3_u32 v25, v25, v45, v46\n" \
  "v_max_u32 v25, v25, v47\n" \
  "v_max3_u32 v25, v25, v96, v97\n" \
  "v_max3_u32 v25, v25, v98, v99\n" \
  "v_max3_u32 v25, v25, v100, v101\n" \
  "v_max3_u32 v25, v25, v102, v103\n" \
  "v_cmp_eq_u32 vcc, -1, v25\n" \
  "s_cbranch_vccz LBG_%=\n" \
  "LB1_%=:\n" \
  "buffer_load_dwordx4 v[32:35], v80, s[36:39], s91 offen sc1\n" \
  "buffer_load_dwordx4 v[36:39], v81, s[36:39], s91 offen sc1\n" \
  "buffer_load_dwordx4 v[40:43], v82, s[36:39], s91 offen sc1\n" \
  "buffer_load_dwordx4 v[44:47], v83, s[36:39], s91 offen sc1\n" \
  "buffer_load_dwordx4 v[96:99], v17, s[36:39], s50 offen offset:0 sc1\n" \
  "buffer_load_dwordx4 v[100:103], v17, s[36:39], s50 offen offset:1024 sc1\n" \
  "v_mov_b64 v[184:185], 0\n" \
  "v_mov_b64 v[186:187], 0\n" \
  "v_mov_b64 v[188:189], 0\n" \
  "v_mov_b64 v[190:191], 0\n" \
  "v_mov_b64 v[192:193], 0\n" \
  "v_mov_b64 v[194:195], 0\n" \
  "v_mov_b64 v[196:197], 0\n" \
  "v_mov_b64 v[198:199], 0\n" \
  "s_waitcnt vmcnt(0)\n" \
  "v_max3_u32 v25, v32, v33, v34\n" \
  "v_max3_u32 v25, v25, v35, v36\n" \
  "v_max3_u32 v25, v25, v37, v38\n" \
  "v_max3_u32 v25, v25, v39, v40\n" \
  "v_max3_u32 v25, v25, v41, v42\n" \
  "v_max3_u32 v25, v25, v43, v44\n" \
  "v_max3_u32 v25, v25, v45, v46\n" \
  "v_max_u32 v25, v25, v47\n" \
  "v_max3_u32 v25, v25, v96, v97\n" \
  "v_max3_u32 v25, v25, v98, v99\n" \
  "v_max3_u32 v25, v25, v100, v101\n" \
  "v_max3_u32 v25, v25, v102, v103\n" \
  "v_cmp_eq_u32 vcc, -1, v25\n" \
  "s_cbranch_vccz LB2_%=\n" \
  "s_add_u32 s58, s58, 1\n" \
  "s_cmp_lt_u32 s58, " STR(SPIN_CAP) "\n" \
  "s_cbranch_scc1 LB1_%=\n" \
  "v_mov_b32 v27, " STR(CW_DEAD) "*4\n" \
  "v_mov_b32 v28, 1\n" \
  "buffer_store_dword v28, v27, s[44:47], 0 offen sc1\n" \
  "s_branch LBG_%=\n" \
  "LB2_%=:\n" \
  "s_mov_b32 vcc_lo, 0x33333333\n" \
  "s_mov_b32 vcc_hi, 0x33333333\n" \
  "s_branch LS2_%=\n" \
  "LW_c_%=:\n" \
  "s_min_i32 s61, s61, 4000\n" \
  "s_sub_i32 s61, s61, 72\n" \
  "s_max_i32 s61, s61, 0\n" \
  "s_bitcmp0_b32 s61, 4\n" \
  "s_cbranch_scc1 LX_c_%=\n" \
  "s_nop 8\n" \
  "LX_c_%=:\n" \
  "s_lshr_b32 s61, s61, 5\n" \
  "s_cbranch_scc0 LB_c_%=\n" \
  ".p2align 4\n" \
  "LY_c_%=:\n" \
  "s_sub_u32 s61, s61, 1\n" \
  "s_cmp_lg_u32 s61, 0\n" \
  "s_cbranch_scc1 LY_c_%=\n" \
  "s_branch LB_c_%=\n" \
  "LW_a_%=:\n" \
  "s_min_i32 s61, s61, 4000\n" \
  "s_sub_i32 s61, s61, 72\n" \
  "s_max_i32 s61, s61, 0\n" \
  "s_bitcmp0_b32 s61, 4\n" \
  "s_cbranch_scc1 LX_a_%=\n" \
  "s_nop 8\n" \
  "LX_a_%=:\n" \
  "s_lshr_b32 s61, s61, 5\n" \
  "s_cbranch_scc0 LB_a_%=\n" \
  ".p2align 4\n" \
  "LY_a_%=:\n" \
  "s_sub_u32 s61, s61, 1\n" \
  "s_cmp_lg_u32 s61, 0\n" \
  "s_cbranch_scc1 LY_a_%=\n" \
  "s_branch LB_a_%=\n" \
  "LC_%=:\n" \
  "v_max3_u32 v25, v32, v33, v34\n" \
  "v_max3_u32 v25, v25, v35, v36\n" \
  "v_max3_u32 v25, v25, v37, v38\n" \
  "v_max3_u32 v25, v25, v39, v40\n" \
  "v_max3_u32 v25, v25, v41, v42\n" \
  "v_max3_u32 v25, v25, v43, v44\n" \
  "v_max3_u32 v25, v25, v45, v46\n" \
  "v_max_u32 v25, v25, v47\n" \
  "v_cmp_eq_u32 vcc, -1, v25\n" \
  "s_cbranch_vccz LC2_%=\n" \
  "LC1_%=:\n" \
  "buffer_load_dwordx4 v[32:35], v11, s[36:39], s51 offen offset:0 sc1\n" \
  "buffer_load_dwordx4 v[36:39], v11, s[36:39], s51 offen offset:1024 sc1\n" \
  "buffer_load_dwordx4 v[40:43], v11, s[36:39], s51 offen offset:2048 sc1\n" \
  "buffer_load_dwordx4 v[44:47], v11, s[36:39], s51 offen offset:3072 sc1\n" \
  "s_waitcnt vmcnt(0)\n" \
  "v_max3_u32 v25, v32, v33, v34\n" \
  "v_max3_u32 v25, v25, v35, v36\n" \
  "v_max3_u32 v25, v25, v37, v38\n" \
  "v_max3_u32 v25, v25, v39, v40\n" \
  "v_max3_u32 v25, v25, v41, v42\n" \
  "v_max3_u32 v25, v25, v43, v44\n" \
  "v_max3_u32 v25, v25, v45, v46\n" \
  "v_max_u32 v25, v25, v47\n" \
  "v_cmp_eq_u32 vcc, -1, v25\n" \
  "s_cbranch_vccz LS3_%=\n" \
  "s_add_u32 s58, s58, 1\n" \
  "s_cmp_lt_u32 s58, " STR(SPIN_CAP) "\n" \
  "s_cbranch_scc1 LC1_%=\n" \
  "v_mov_b32 v27, " STR(CW_DEAD) "*4\n" \
  "v_mov_b32 v28, 1\n" \
  "buffer_store_dword v28, v27, s[44:47], 0 offen sc1\n" \
  "s_branch LC2_%=\n" \
  "LW_p_%=:\n" \
  "s_min_i32 s61, s61, 4000\n" \
  "s_sub_i32 s61, s61, 72\n" \
  "s_max_i32 s61, s61, 0\n" \
  "s_bitcmp0_b32 s61, 4\n" \
  "s_cbranch_scc1 LX_p_%=\n" \
  "s_nop 8\n" \
  "LX_p_%=:\n" \
  "s_lshr_b32 s61, s61, 5\n" \
  "s_cbranch_scc0 LB_p_%=\n" \
  ".p2align 4\n" \
  "LY_p_%=:\n" \
  "s_sub_u32 s61, s61, 1\n" \
  "s_cmp_lg_u32 s61, 0\n" \
  "s_cbranch_scc1 LY_p_%=\n" \
  "s_branch LB_p_%=\n" \
  "LF_%=:\n" \
  "buffer_load_dword v23, v22, s[44:47], 0 offen sc1\n" \
  "s_mov_b32 s61, 0\n" \
  "s_waitcnt vmcnt(0)\n" \
  "v_cmp_gt_u32 vcc, s56, v23\n" \
  "s_cbranch_vccz LF2_%=\n" \
  "s_sleep 2\n" \
  "s_add_u32 s58, s58, 1\n" \
  "s_cmp_lt_u32 s58, " STR(SPIN_CAP) "\n" \
  "s_cbranch_scc1 LF_%=\n" \
  "v_mov_b32 v27, " STR(CW_DEAD) "*4\n" \
  "v_mov_b32 v28, 1\n" \
  "buffer_store_dword v28, v27, s[44:47], 0 offen sc1\n" \
  "s_branch LF2_%=\n" \
  "LW_k_%=:\n" \
  "s_min_i32 s61, s61, 4000\n" \
  "s_sub_i32 s61, s61, 72\n" \
  "s_max_i32 s61, s61, 0\n" \
  "s_bitcmp0_b32 s61, 4\n" \
  "s_cbranch_scc1 LX_k_%=\n" \
  "s_nop 8\n" \
  "LX_k_%=:\n" \
  "s_lshr_b32 s61, s61, 5\n" \
  "s_cbranch_scc0 LB_k_%=\n" \
  ".p2align 4\n" \
  "LY_k_%=:\n" \
  "s_sub_u32 s61, s61, 1\n" \
  "s_cmp_lg_u32 s61, 0\n" \
  "s_cbranch_scc1 LY_k_%=\n" \
  "s_branch LB_k_%=\n" \
  "LK3_%=:\n" \
  "buffer_load_dword v25, v13, s[36:39], s50 offen sc1\n" \
  "s_add_u32 s58, s58, 1\n" \
  "s_cmp_lt_u32 s58, " STR(SPIN_CAP) "\n" \
  "s_cbranch_scc1 LK2_%=\n" \
  "v_mov_b32 v27, " STR(CW_DEAD) "*4\n" \
  "v_mov_b32 v28, 1\n" \
  "buffer_store_dword v28, v27, s[44:47], 0 offen sc1\n" \
  "s_branch LK4_%=\n" \
  "LE_%=:\n" \
  "s_endpgm\n" \
  ""

#define GEMM_ASM \
  "v_mbcnt_lo_u32_b32 v4, -1, 0\n" \
  "v_mbcnt_hi_u32_b32 v4, -1, v4\n" \
  "v_and_b32 v116, 15, v4\n" \
  "v_lshrrev_b32 v117, 4, v4\n" \
  "s_lshl_b32 s86, %7, 13\n" \
  "s_add_u32 s86, s86, %8\n" \
  "v_lshrrev_b32 v5, 3, v4\n" \
  "v_and_b32 v6, 7, v4\n" \
  "v_lshlrev_b32 v8, 13, v5\n" \
  "v_lshl_add_u32 v8, v6, 4, v8\n" \
  "v_and_b32 v9, 1, v117\n" \
  "v_lshlrev_b32 v9, 5, v9\n" \
  "v_xor_b32 v8, v8, v9\n" \
  "v_xor_b32 v9, 64, v8\n" \
  "s_mov_b64 s[68:69], %1\n" \
  "s_and_b32 s69, s69, 0xffff\n" \
  "s_mov_b32 s70, 8388608\n" \
  "s_mov_b32 s71, 0x00020000\n" \
  "s_lshl_b32 s92, %7, 19\n" \
  "s_add_u32 s92, s92, 0\n" \
  "s_add_u32 m0, s86, 98304\n" \
  "s_nop 0\n" \
  "buffer_load_dwordx4 v8, s[68:71], s92 offen lds\n" \
  "s_add_u32 m0, s86, 99328\n" \
  "s_add_u32 s92, s92, 65536\n" \
  "buffer_load_dwordx4 v9, s[68:71], s92 offen lds\n" \
  "s_add_u32 m0, s86, 100352\n" \
  "s_add_u32 s92, s92, 65536\n" \
  "buffer_load_dwordx4 v8, s[68:71], s92 offen lds\n" \
  "s_add_u32 m0, s86, 101376\n" \
  "s_add_u32 s92, s92, 65536\n" \
  "buffer_load_dwordx4 v9, s[68:71], s92 offen lds\n" \
  "s_add_u32 m0, s86, 102400\n" \
  "s_add_u32 s92, s92, 65536\n" \
  "buffer_load_dwordx4 v8, s[68:71], s92 offen lds\n" \
  "s_add_u32 m0, s86, 103424\n" \
  "s_add_u32 s92, s92, 65536\n" \
  "buffer_load_dwordx4 v9, s[68:71], s92 offen lds\n" \
  "s_add_u32 m0, s86, 104448\n" \
  "s_add_u32 s92, s92, 65536\n" \
  "buffer_load_dwordx4 v8, s[68:71], s92 offen lds\n" \
  "s_add_u32 m0, s86, 105472\n" \
  "s_add_u32 s92, s92, 65536\n" \
  "buffer_load_dwordx4 v9, s[68:71], s92 offen lds\n" \
  "s_lshl_b32 s92, %7, 19\n" \
  "s_add_u32 s92, s92, 2097152\n" \
  "s_add_u32 m0, s86, 131072\n" \
  "s_nop 0\n" \
  "buffer_load_dwordx4 v8, s[68:71], s92 offen lds\n" \
  "s_add_u32 m0, s86, 132096\n" \
  "s_add_u32 s92, s92, 65536\n" \
  "buffer_load_dwordx4 v9, s[68:71], s92 offen lds\n" \
  "s_add_u32 m0, s86, 133120\n" \
  "s_add_u32 s92, s92, 65536\n" \
  "buffer_load_dwordx4 v8, s[68:71], s92 offen lds\n" \
  "s_add_u32 m0, s86, 134144\n" \
  "s_add_u32 s92, s92, 65536\n" \
  "buffer_load_dwordx4 v9, s[68:71], s92 offen lds\n" \
  "s_add_u32 m0, s86, 135168\n" \
  "s_add_u32 s92, s92, 65536\n" \
  "buffer_load_dwordx4 v8, s[68:71], s92 offen lds\n" \
  "s_add_u32 m0, s86, 136192\n" \
  "s_add_u32 s92, s92, 65536\n" \
  "buffer_load_dwordx4 v9, s[68:71], s92 offen lds\n" \
  "s_add_u32 m0, s86, 137216\n" \
  "s_add_u32 s92, s92, 65536\n" \
  "buffer_load_dwordx4 v8, s[68:71], s92 offen lds\n" \
  "s_add_u32 m0, s86, 138240\n" \
  "s_add_u32 s92, s92, 65536\n" \
  "buffer_load_dwordx4 v9, s[68:71], s92 offen lds\n" \
  "s_lshl_b32 s92, %7, 19\n" \
  "s_add_u32 s92, s92, 4194304\n" \
  "s_add_u32 m0, s86, 0\n" \
  "s_nop 0\n" \
  "buffer_load_dwordx4 v8, s[68:71], s92 offen lds\n" \
  "s_add_u32 m0, s86, 1024\n" \
  "s_add_u32 s92, s92, 65536\n" \
  "buffer_load_dwordx4 v9, s[68:71], s92 offen lds\n" \
  "s_add_u32 m0, s86, 2048\n" \
  "s_add_u32 s92, s92, 65536\n" \
  "buffer_load_dwordx4 v8, s[68:71], s92 offen lds\n" \
  "s_add_u32 m0, s86, 3072\n" \
  "s_add_u32 s92, s92, 65536\n" \
  "buffer_load_dwordx4 v9, s[68:71], s92 offen lds\n" \
  "s_add_u32 m0, s86, 4096\n" \
  "s_add_u32 s92, s92, 65536\n" \
  "buffer_load_dwordx4 v8, s[68:71], s92 offen lds\n" \
  "s_add_u32 m0, s86, 5120\n" \
  "s_add_u32 s92, s92, 65536\n" \
  "buffer_load_dwordx4 v9, s[68:71], s92 offen lds\n" \
  "s_add_u32 m0, s86, 6144\n" \
  "s_add_u32 s92, s92, 65536\n" \
  "buffer_load_dwordx4 v8, s[68:71], s92 offen lds\n" \
  "s_add_u32 m0, s86, 7168\n" \
  "s_add_u32 s92, s92, 65536\n" \
  "buffer_load_dwordx4 v9, s[68:71], s92 offen lds\n" \
  "s_lshl_b32 s92, %7, 19\n" \
  "s_add_u32 s92, s92, 6291456\n" \
  "s_add_u32 m0, s86, 32768\n" \
  "s_nop 0\n" \
  "buffer_load_dwordx4 v8, s[68:71], s92 offen lds\n" \
  "s_add_u32 m0, s86, 33792\n" \
  "s_add_u32 s92, s92, 65536\n" \
  "buffer_load_dwordx4 v9, s[68:71], s92 offen lds\n" \
  "s_add_u32 m0, s86, 34816\n" \
  "s_add_u32 s92, s92, 65536\n" \
  "buffer_load_dwordx4 v8, s[68:71], s92 offen lds\n" \
  "s_add_u32 m0, s86, 35840\n" \
  "s_add_u32 s92, s92, 65536\n" \
  "buffer_load_dwordx4 v9, s[68:71], s92 offen lds\n" \
  "s_add_u32 m0, s86, 36864\n" \
  "s_add_u32 s92, s92, 65536\n" \
  "buffer_load_dwordx4 v8, s[68:71], s92 offen lds\n" \
  "s_add_u32 m0, s86, 37888\n" \
  "s_add_u32 s92, s92, 65536\n" \
  "buffer_load_dwordx4 v9, s[68:71], s92 offen lds\n" \
  "s_add_u32 m0, s86, 38912\n" \
  "s_add_u32 s92, s92, 65536\n" \
  "buffer_load_dwordx4 v8, s[68:71], s92 offen lds\n" \
  "s_add_u32 m0, s86, 39936\n" \
  "s_add_u32 s92, s92, 65536\n" \
  "buffer_load_dwordx4 v9, s[68:71], s92 offen lds\n" \
  "s_mov_b64 s[72:73], %3\n" \
  "s_and_b32 s73, s73, 0xffff\n" \
  "s_mov_b32 s74, 4194304\n" \
  "s_mov_b32 s75, 0x00020000\n" \
  "s_mov_b64 s[76:77], %4\n" \
  "s_and_b32 s77, s77, 0xffff\n" \
  "s_mov_b32 s78, 3*" STR(QKV_SZ32) "\n" \
  "s_mov_b32 s79, 0x00020000\n" \
  "s_mov_b64 s[80:81], %5\n" \
  "s_and_b32 s81, s81, 0xffff\n" \
  "s_mov_b32 s82, " STR(CTRL_BYTES) "\n" \
  "s_mov_b32 s83, 0x00020000\n" \
  "v_xor_b32 v6, v6, v117\n" \
  "s_and_b32 s84, %7, 1\n" \
  "s_lshl_b32 s84, s84, 2\n" \
  "v_xor_b32 v6, s84, v6\n" \
  "v_lshlrev_b32 v7, 11, v5\n" \
  "v_lshl_add_u32 v7, v6, 4, v7\n" \
  "s_lshl_b32 s87, %7, 14\n" \
  "s_add_u32 s96, s87, 63*65536\n" \
  "v_lshrrev_b32 v5, 1, v116\n" \
  "v_xor_b32 v5, v5, v117\n" \
  "v_lshlrev_b32 v5, 4, v5\n" \
  "v_lshl_add_u32 v5, v116, 7, v5\n" \
  "v_add_u32 v10, %8, v5\n" \
  "v_xor_b32 v5, 64, v5\n" \
  "v_add_u32 v11, %8, v5\n" \
  "v_add_u32 v12, 0x10000, v10\n" \
  "v_add_u32 v13, 0x10000, v11\n" \
  "s_lshr_b32 s84, %6, 1\n" \
  "s_lshl_b32 s84, s84, 14\n" \
  "s_and_b32 s85, %6, 1\n" \
  "s_lshl_b32 s85, s85, 2\n" \
  "s_add_u32 s85, s85, %7\n" \
  "s_lshl_b32 s91, s85, 9\n" \
  "s_add_u32 s91, s91, s84\n" \
  "v_and_b32 v5, 1, v117\n" \
  "v_lshlrev_b32 v14, 12, v5\n" \
  "v_lshrrev_b32 v5, 1, v117\n" \
  "v_lshl_add_u32 v14, v5, 8, v14\n" \
  "v_lshl_add_u32 v14, v116, 4, v14\n" \
  "v_add_u32 v14, s91, v14\n" \
  "s_lshl_b32 s91, s85, 10\n" \
  "s_add_u32 s91, s91, s84\n" \
  "v_lshlrev_b32 v15, 4, v4\n" \
  "v_add_u32 v15, s91, v15\n" \
  "s_lshl_b32 s84, %6, 7\n" \
  "s_add_u32 s84, s84, " STR(CW_GPROG) "*4\n" \
  "v_or_b32 v5, %7, v4\n" \
  "v_lshl_add_u32 v16, v5, 24, s84\n" \
  "v_mov_b32 v17, 0\n" \
  "v_or_b32 v14, 0x80000000, v14\n" \
  "v_or_b32 v15, 0x80000000, v15\n" \
  "v_lshrrev_b32 v5, 2, v116\n" \
  "v_lshl_add_u32 v6, v117, 3, v5\n" \
  "v_lshlrev_b32 v6, 7, v6\n" \
  "v_lshrrev_b32 v5, 1, v5\n" \
  "v_and_b32 v119, 1, v117\n" \
  "v_lshl_add_u32 v5, v119, 1, v5\n" \
  "v_xor_b32 v5, %7, v5\n" \
  "v_lshl_add_u32 v6, v5, 5, v6\n" \
  "v_and_b32 v5, 3, v4\n" \
  "v_lshl_add_u32 v6, v5, 3, v6\n" \
  "v_add_u32 v118, %8, v6\n" \
  "v_add_u32 v119, 0x10000, v118\n" \
  "v_add_u32 v120, 0x20000, v118\n" \
  "s_lshl_b32 s84, %7, 10\n" \
  "s_add_u32 s91, s84, %8\n" \
  "s_waitcnt vmcnt(24) lgkmcnt(0)\n" \
  "s_barrier\n" \
  "s_mov_b64 s[68:69], %2\n" \
  "s_and_b32 s69, s69, 0xffff\n" \
  "s_mov_b32 s70, 8388608\n" \
  "s_mov_b32 s71, 0x00020000\n" \
  "s_lshl_b32 s92, %7, 19\n" \
  "s_add_u32 s92, s92, 0\n" \
  "s_add_u32 m0, s86, 65536\n" \
  "s_nop 0\n" \
  "buffer_load_dwordx4 v8, s[68:71], s92 offen lds\n" \
  "s_add_u32 m0, s86, 66560\n" \
  "s_add_u32 s92, s92, 65536\n" \
  "buffer_load_dwordx4 v9, s[68:71], s92 offen lds\n" \
  "s_add_u32 m0, s86, 67584\n" \
  "s_add_u32 s92, s92, 65536\n" \
  "buffer_load_dwordx4 v8, s[68:71], s92 offen lds\n" \
  "s_add_u32 m0, s86, 68608\n" \
  "s_add_u32 s92, s92, 65536\n" \
  "buffer_load_dwordx4 v9, s[68:71], s92 offen lds\n" \
  "s_add_u32 m0, s86, 69632\n" \
  "s_add_u32 s92, s92, 65536\n" \
  "buffer_load_dwordx4 v8, s[68:71], s92 offen lds\n" \
  "s_add_u32 m0, s86, 70656\n" \
  "s_add_u32 s92, s92, 65536\n" \
  "buffer_load_dwordx4 v9, s[68:71], s92 offen lds\n" \
  "s_add_u32 m0, s86, 71680\n" \
  "s_add_u32 s92, s92, 65536\n" \
  "buffer_load_dwordx4 v8, s[68:71], s92 offen lds\n" \
  "s_add_u32 m0, s86, 72704\n" \
  "s_add_u32 s92, s92, 65536\n" \
  "buffer_load_dwordx4 v9, s[68:71], s92 offen lds\n" \
  "ds_read_b64_tr_b16 a[0:1], v119 offset:32768\n" \
  "ds_read_b64_tr_b16 a[2:3], v119 offset:33280\n" \
  "ds_read_b64_tr_b16 a[4:5], v119 offset:36864\n" \
  "ds_read_b64_tr_b16 a[6:7], v119 offset:37376\n" \
  "ds_read_b64_tr_b16 a[8:9], v119 offset:40960\n" \
  "ds_read_b64_tr_b16 a[10:11], v119 offset:41472\n" \
  "ds_read_b64_tr_b16 a[12:13], v119 offset:45056\n" \
  "ds_read_b64_tr_b16 a[14:15], v119 offset:45568\n" \
  "ds_read_b64_tr_b16 a[16:17], v119 offset:49152\n" \
  "ds_read_b64_tr_b16 a[18:19], v119 offset:49664\n" \
  "ds_read_b64_tr_b16 a[20:21], v119 offset:53248\n" \
  "ds_read_b64_tr_b16 a[22:23], v119 offset:53760\n" \
  "ds_read_b64_tr_b16 a[24:25], v119 offset:57344\n" \
  "ds_read_b64_tr_b16 a[26:27], v119 offset:57856\n" \
  "ds_read_b64_tr_b16 a[28:29], v119 offset:61440\n" \
  "ds_read_b64_tr_b16 a[30:31], v119 offset:61952\n" \
  "s_waitcnt vmcnt(24) lgkmcnt(0)\n" \
  "s_barrier\n" \
  "s_lshl_b32 s92, %7, 19\n" \
  "s_add_u32 s92, s92, 2097152\n" \
  "s_add_u32 m0, s86, 98304\n" \
  "s_nop 0\n" \
  "buffer_load_dwordx4 v8, s[68:71], s92 offen lds\n" \
  "s_add_u32 m0, s86, 99328\n" \
  "s_add_u32 s92, s92, 65536\n" \
  "buffer_load_dwordx4 v9, s[68:71], s92 offen lds\n" \
  "s_add_u32 m0, s86, 100352\n" \
  "s_add_u32 s92, s92, 65536\n" \
  "buffer_load_dwordx4 v8, s[68:71], s92 offen lds\n" \
  "s_add_u32 m0, s86, 101376\n" \
  "s_add_u32 s92, s92, 65536\n" \
  "buffer_load_dwordx4 v9, s[68:71], s92 offen lds\n" \
  "s_add_u32 m0, s86, 102400\n" \
  "s_add_u32 s92, s92, 65536\n" \
  "buffer_load_dwordx4 v8, s[68:71], s92 offen lds\n" \
  "s_add_u32 m0, s86, 103424\n" \
  "s_add_u32 s92, s92, 65536\n" \
  "buffer_load_dwordx4 v9, s[68:71], s92 offen lds\n" \
  "s_add_u32 m0, s86, 104448\n" \
  "s_add_u32 s92, s92, 65536\n" \
  "buffer_load_dwordx4 v8, s[68:71], s92 offen lds\n" \
  "s_add_u32 m0, s86, 105472\n" \
  "s_add_u32 s92, s92, 65536\n" \
  "buffer_load_dwordx4 v9, s[68:71], s92 offen lds\n" \
  "ds_read_b64_tr_b16 a[32:33], v120 offset:0\n" \
  "ds_read_b64_tr_b16 a[34:35], v120 offset:512\n" \
  "ds_read_b64_tr_b16 a[36:37], v120 offset:4096\n" \
  "ds_read_b64_tr_b16 a[38:39], v120 offset:4608\n" \
  "ds_read_b64_tr_b16 a[40:41], v120 offset:8192\n" \
  "ds_read_b64_tr_b16 a[42:43], v120 offset:8704\n" \
  "ds_read_b64_tr_b16 a[44:45], v120 offset:12288\n" \
  "ds_read_b64_tr_b16 a[46:47], v120 offset:12800\n" \
  "ds_read_b64_tr_b16 a[48:49], v120 offset:16384\n" \
  "ds_read_b64_tr_b16 a[50:51], v120 offset:16896\n" \
  "ds_read_b64_tr_b16 a[52:53], v120 offset:20480\n" \
  "ds_read_b64_tr_b16 a[54:55], v120 offset:20992\n" \
  "ds_read_b64_tr_b16 a[56:57], v120 offset:24576\n" \
  "ds_read_b64_tr_b16 a[58:59], v120 offset:25088\n" \
  "ds_read_b64_tr_b16 a[60:61], v120 offset:28672\n" \
  "ds_read_b64_tr_b16 a[62:63], v120 offset:29184\n" \
  "s_waitcnt vmcnt(24) lgkmcnt(0)\n" \
  "s_barrier\n" \
  "s_lshl_b32 s92, %7, 19\n" \
  "s_add_u32 s92, s92, 4194304\n" \
  "s_add_u32 m0, s86, 131072\n" \
  "s_nop 0\n" \
  "buffer_load_dwordx4 v8, s[68:71], s92 offen lds\n" \
  "s_add_u32 m0, s86, 132096\n" \
  "s_add_u32 s92, s92, 65536\n" \
  "buffer_load_dwordx4 v9, s[68:71], s92 offen lds\n" \
  "s_add_u32 m0, s86, 133120\n" \
  "s_add_u32 s92, s92, 65536\n" \
  "buffer_load_dwordx4 v8, s[68:71], s92 offen lds\n" \
  "s_add_u32 m0, s86, 134144\n" \
  "s_add_u32 s92, s92, 65536\n" \
  "buffer_load_dwordx4 v9, s[68:71], s92 offen lds\n" \
  "s_add_u32 m0, s86, 135168\n" \
  "s_add_u32 s92, s92, 65536\n" \
  "buffer_load_dwordx4 v8, s[68:71], s92 offen lds\n" \
  "s_add_u32 m0, s86, 136192\n" \
  "s_add_u32 s92, s92, 65536\n" \
  "buffer_load_dwordx4 v9, s[68:71], s92 offen lds\n" \
  "s_add_u32 m0, s86, 137216\n" \
  "s_add_u32 s92, s92, 65536\n" \
  "buffer_load_dwordx4 v8, s[68:71], s92 offen lds\n" \
  "s_add_u32 m0, s86, 138240\n" \
  "s_add_u32 s92, s92, 65536\n" \
  "buffer_load_dwordx4 v9, s[68:71], s92 offen lds\n" \
  "ds_read_b64_tr_b16 a[64:65], v118 offset:0\n" \
  "ds_read_b64_tr_b16 a[66:67], v118 offset:512\n" \
  "ds_read_b64_tr_b16 a[68:69], v118 offset:4096\n" \
  "ds_read_b64_tr_b16 a[70:71], v118 offset:4608\n" \
  "ds_read_b64_tr_b16 a[72:73], v118 offset:8192\n" \
  "ds_read_b64_tr_b16 a[74:75], v118 offset:8704\n" \
  "ds_read_b64_tr_b16 a[76:77], v118 offset:12288\n" \
  "ds_read_b64_tr_b16 a[78:79], v118 offset:12800\n" \
  "ds_read_b64_tr_b16 a[80:81], v118 offset:16384\n" \
  "ds_read_b64_tr_b16 a[82:83], v118 offset:16896\n" \
  "ds_read_b64_tr_b16 a[84:85], v118 offset:20480\n" \
  "ds_read_b64_tr_b16 a[86:87], v118 offset:20992\n" \
  "ds_read_b64_tr_b16 a[88:89], v118 offset:24576\n" \
  "ds_read_b64_tr_b16 a[90:91], v118 offset:25088\n" \
  "ds_read_b64_tr_b16 a[92:93], v118 offset:28672\n" \
  "ds_read_b64_tr_b16 a[94:95], v118 offset:29184\n" \
  "s_waitcnt vmcnt(24) lgkmcnt(0)\n" \
  "s_barrier\n" \
  "s_lshl_b32 s92, %7, 19\n" \
  "s_add_u32 s92, s92, 6291456\n" \
  "s_add_u32 m0, s86, 0\n" \
  "s_nop 0\n" \
  "buffer_load_dwordx4 v8, s[68:71], s92 offen lds\n" \
  "s_add_u32 m0, s86, 1024\n" \
  "s_add_u32 s92, s92, 65536\n" \
  "buffer_load_dwordx4 v9, s[68:71], s92 offen lds\n" \
  "s_add_u32 m0, s86, 2048\n" \
  "s_add_u32 s92, s92, 65536\n" \
  "buffer_load_dwordx4 v8, s[68:71], s92 offen lds\n" \
  "s_add_u32 m0, s86, 3072\n" \
  "s_add_u32 s92, s92, 65536\n" \
  "buffer_load_dwordx4 v9, s[68:71], s92 offen lds\n" \
  "s_add_u32 m0, s86, 4096\n" \
  "s_add_u32 s92, s92, 65536\n" \
  "buffer_load_dwordx4 v8, s[68:71], s92 offen lds\n" \
  "s_add_u32 m0, s86, 5120\n" \
  "s_add_u32 s92, s92, 65536\n" \
  "buffer_load_dwordx4 v9, s[68:71], s92 offen lds\n" \
  "s_add_u32 m0, s86, 6144\n" \
  "s_add_u32 s92, s92, 65536\n" \
  "buffer_load_dwordx4 v8, s[68:71], s92 offen lds\n" \
  "s_add_u32 m0, s86, 7168\n" \
  "s_add_u32 s92, s92, 65536\n" \
  "buffer_load_dwordx4 v9, s[68:71], s92 offen lds\n" \
  "ds_read_b64_tr_b16 a[96:97], v118 offset:32768\n" \
  "ds_read_b64_tr_b16 a[98:99], v118 offset:33280\n" \
  "ds_read_b64_tr_b16 a[100:101], v118 offset:36864\n" \
  "ds_read_b64_tr_b16 a[102:103], v118 offset:37376\n" \
  "ds_read_b64_tr_b16 a[104:105], v118 offset:40960\n" \
  "ds_read_b64_tr_b16 a[106:107], v118 offset:41472\n" \
  "ds_read_b64_tr_b16 a[108:109], v118 offset:45056\n" \
  "ds_read_b64_tr_b16 a[110:111], v118 offset:45568\n" \
  "ds_read_b64_tr_b16 a[112:113], v118 offset:49152\n" \
  "ds_read_b64_tr_b16 a[114:115], v118 offset:49664\n" \
  "ds_read_b64_tr_b16 a[116:117], v118 offset:53248\n" \
  "ds_read_b64_tr_b16 a[118:119], v118 offset:53760\n" \
  "ds_read_b64_tr_b16 a[120:121], v118 offset:57344\n" \
  "ds_read_b64_tr_b16 a[122:123], v118 offset:57856\n" \
  "ds_read_b64_tr_b16 a[124:125], v118 offset:61440\n" \
  "ds_read_b64_tr_b16 a[126:127], v118 offset:61952\n" \
  "s_waitcnt vmcnt(24) lgkmcnt(0)\n" \
  "s_barrier\n" \
  "s_mov_b64 s[68:69], %0\n" \
  "s_and_b32 s69, s69, 0xffff\n" \
  "s_mov_b32 s70, 8388608\n" \
  "s_mov_b32 s71, 0x00020000\n" \
  "s_lshl_b32 s92, %7, 19\n" \
  "s_add_u32 s92, s92, 0\n" \
  "s_add_u32 m0, s86, 32768\n" \
  "s_nop 0\n" \
  "buffer_load_dwordx4 v8, s[68:71], s92 offen lds\n" \
  "s_add_u32 m0, s86, 33792\n" \
  "s_add_u32 s92, s92, 65536\n" \
  "buffer_load_dwordx4 v9, s[68:71], s92 offen lds\n" \
  "s_add_u32 m0, s86, 34816\n" \
  "s_add_u32 s92, s92, 65536\n" \
  "buffer_load_dwordx4 v8, s[68:71], s92 offen lds\n" \
  "s_add_u32 m0, s86, 35840\n" \
  "s_add_u32 s92, s92, 65536\n" \
  "buffer_load_dwordx4 v9, s[68:71], s92 offen lds\n" \
  "s_add_u32 m0, s86, 36864\n" \
  "s_add_u32 s92, s92, 65536\n" \
  "buffer_load_dwordx4 v8, s[68:71], s92 offen lds\n" \
  "s_add_u32 m0, s86, 37888\n" \
  "s_add_u32 s92, s92, 65536\n" \
  "buffer_load_dwordx4 v9, s[68:71], s92 offen lds\n" \
  "s_add_u32 m0, s86, 38912\n" \
  "s_add_u32 s92, s92, 65536\n" \
  "buffer_load_dwordx4 v8, s[68:71], s92 offen lds\n" \
  "s_add_u32 m0, s86, 39936\n" \
  "s_add_u32 s92, s92, 65536\n" \
  "buffer_load_dwordx4 v9, s[68:71], s92 offen lds\n" \
  "ds_read_b64_tr_b16 a[128:129], v119 offset:0\n" \
  "ds_read_b64_tr_b16 a[130:131], v119 offset:512\n" \
  "ds_read_b64_tr_b16 a[132:133], v119 offset:4096\n" \
  "ds_read_b64_tr_b16 a[134:135], v119 offset:4608\n" \
  "ds_read_b64_tr_b16 a[136:137], v119 offset:8192\n" \
  "ds_read_b64_tr_b16 a[138:139], v119 offset:8704\n" \
  "ds_read_b64_tr_b16 a[140:141], v119 offset:12288\n" \
  "ds_read_b64_tr_b16 a[142:143], v119 offset:12800\n" \
  "ds_read_b64_tr_b16 a[144:145], v119 offset:16384\n" \
  "ds_read_b64_tr_b16 a[146:147], v119 offset:16896\n" \
  "ds_read_b64_tr_b16 a[148:149], v119 offset:20480\n" \
  "ds_read_b64_tr_b16 a[150:151], v119 offset:20992\n" \
  "ds_read_b64_tr_b16 a[152:153], v119 offset:24576\n" \
  "ds_read_b64_tr_b16 a[154:155], v119 offset:25088\n" \
  "ds_read_b64_tr_b16 a[156:157], v119 offset:28672\n" \
  "ds_read_b64_tr_b16 a[158:159], v119 offset:29184\n" \
  "s_waitcnt vmcnt(24) lgkmcnt(0)\n" \
  "s_barrier\n" \
  "s_lshl_b32 s92, %7, 19\n" \
  "s_add_u32 s92, s92, 2097152\n" \
  "s_add_u32 m0, s86, 65536\n" \
  "s_nop 0\n" \
  "buffer_load_dwordx4 v8, s[68:71], s92 offen lds\n" \
  "s_add_u32 m0, s86, 66560\n" \
  "s_add_u32 s92, s92, 65536\n" \
  "buffer_load_dwordx4 v9, s[68:71], s92 offen lds\n" \
  "s_add_u32 m0, s86, 67584\n" \
  "s_add_u32 s92, s92, 65536\n" \
  "buffer_load_dwordx4 v8, s[68:71], s92 offen lds\n" \
  "s_add_u32 m0, s86, 68608\n" \
  "s_add_u32 s92, s92, 65536\n" \
  "buffer_load_dwordx4 v9, s[68:71], s92 offen lds\n" \
  "s_add_u32 m0, s86, 69632\n" \
  "s_add_u32 s92, s92, 65536\n" \
  "buffer_load_dwordx4 v8, s[68:71], s92 offen lds\n" \
  "s_add_u32 m0, s86, 70656\n" \
  "s_add_u32 s92, s92, 65536\n" \
  "buffer_load_dwordx4 v9, s[68:71], s92 offen lds\n" \
  "s_add_u32 m0, s86, 71680\n" \
  "s_add_u32 s92, s92, 65536\n" \
  "buffer_load_dwordx4 v8, s[68:71], s92 offen lds\n" \
  "s_add_u32 m0, s86, 72704\n" \
  "s_add_u32 s92, s92, 65536\n" \
  "buffer_load_dwordx4 v9, s[68:71], s92 offen lds\n" \
  "ds_read_b64_tr_b16 a[160:161], v119 offset:32768\n" \
  "ds_read_b64_tr_b16 a[162:163], v119 offset:33280\n" \
  "ds_read_b64_tr_b16 a[164:165], v119 offset:36864\n" \
  "ds_read_b64_tr_b16 a[166:167], v119 offset:37376\n" \
  "ds_read_b64_tr_b16 a[168:169], v119 offset:40960\n" \
  "ds_read_b64_tr_b16 a[170:171], v119 offset:41472\n" \
  "ds_read_b64_tr_b16 a[172:173], v119 offset:45056\n" \
  "ds_read_b64_tr_b16 a[174:175], v119 offset:45568\n" \
  "ds_read_b64_tr_b16 a[176:177], v119 offset:49152\n" \
  "ds_read_b64_tr_b16 a[178:179], v119 offset:49664\n" \
  "ds_read_b64_tr_b16 a[180:181], v119 offset:53248\n" \
  "ds_read_b64_tr_b16 a[182:183], v119 offset:53760\n" \
  "ds_read_b64_tr_b16 a[184:185], v119 offset:57344\n" \
  "ds_read_b64_tr_b16 a[186:187], v119 offset:57856\n" \
  "ds_read_b64_tr_b16 a[188:189], v119 offset:61440\n" \
  "ds_read_b64_tr_b16 a[190:191], v119 offset:61952\n" \
  "s_waitcnt vmcnt(24) lgkmcnt(0)\n" \
  "s_barrier\n" \
  "s_lshl_b32 s92, %7, 19\n" \
  "s_add_u32 s92, s92, 4194304\n" \
  "s_add_u32 m0, s86, 98304\n" \
  "s_nop 0\n" \
  "buffer_load_dwordx4 v8, s[68:71], s92 offen lds\n" \
  "s_add_u32 m0, s86, 99328\n" \
  "s_add_u32 s92, s92, 65536\n" \
  "buffer_load_dwordx4 v9, s[68:71], s92 offen lds\n" \
  "s_add_u32 m0, s86, 100352\n" \
  "s_add_u32 s92, s92, 65536\n" \
  "buffer_load_dwordx4 v8, s[68:71], s92 offen lds\n" \
  "s_add_u32 m0, s86, 101376\n" \
  "s_add_u32 s92, s92, 65536\n" \
  "buffer_load_dwordx4 v9, s[68:71], s92 offen lds\n" \
  "s_add_u32 m0, s86, 102400\n" \
  "s_add_u32 s92, s92, 65536\n" \
  "buffer_load_dwordx4 v8, s[68:71], s92 offen lds\n" \
  "s_add_u32 m0, s86, 103424\n" \
  "s_add_u32 s92, s92, 65536\n" \
  "buffer_load_dwordx4 v9, s[68:71], s92 offen lds\n" \
  "s_add_u32 m0, s86, 104448\n" \
  "s_add_u32 s92, s92, 65536\n" \
  "buffer_load_dwordx4 v8, s[68:71], s92 offen lds\n" \
  "s_add_u32 m0, s86, 105472\n" \
  "s_add_u32 s92, s92, 65536\n" \
  "buffer_load_dwordx4 v9, s[68:71], s92 offen lds\n" \
  "ds_read_b64_tr_b16 a[192:193], v120 offset:0\n" \
  "ds_read_b64_tr_b16 a[194:195], v120 offset:512\n" \
  "ds_read_b64_tr_b16 a[196:197], v120 offset:4096\n" \
  "ds_read_b64_tr_b16 a[198:199], v120 offset:4608\n" \
  "ds_read_b64_tr_b16 a[200:201], v120 offset:8192\n" \
  "ds_read_b64_tr_b16 a[202:203], v120 offset:8704\n" \
  "ds_read_b64_tr_b16 a[204:205], v120 offset:12288\n" \
  "ds_read_b64_tr_b16 a[206:207], v120 offset:12800\n" \
  "ds_read_b64_tr_b16 a[208:209], v120 offset:16384\n" \
  "ds_read_b64_tr_b16 a[210:211], v120 offset:16896\n" \
  "ds_read_b64_tr_b16 a[212:213], v120 offset:20480\n" \
  "ds_read_b64_tr_b16 a[214:215], v120 offset:20992\n" \
  "ds_read_b64_tr_b16 a[216:217], v120 offset:24576\n" \
  "ds_read_b64_tr_b16 a[218:219], v120 offset:25088\n" \
  "ds_read_b64_tr_b16 a[220:221], v120 offset:28672\n" \
  "ds_read_b64_tr_b16 a[222:223], v120 offset:29184\n" \
  "s_waitcnt vmcnt(24) lgkmcnt(0)\n" \
  "s_barrier\n" \
  "s_lshl_b32 s92, %7, 19\n" \
  "s_add_u32 s92, s92, 6291456\n" \
  "s_add_u32 m0, s86, 131072\n" \
  "s_nop 0\n" \
  "buffer_load_dwordx4 v8, s[68:71], s92 offen lds\n" \
  "s_add_u32 m0, s86, 132096\n" \
  "s_add_u32 s92, s92, 65536\n" \
  "buffer_load_dwordx4 v9, s[68:71], s92 offen lds\n" \
  "s_add_u32 m0, s86, 133120\n" \
  "s_add_u32 s92, s92, 65536\n" \
  "buffer_load_dwordx4 v8, s[68:71], s92 offen lds\n" \
  "s_add_u32 m0, s86, 134144\n" \
  "s_add_u32 s92, s92, 65536\n" \
  "buffer_load_dwordx4 v9, s[68:71], s92 offen lds\n" \
  "s_add_u32 m0, s86, 135168\n" \
  "s_add_u32 s92, s92, 65536\n" \
  "buffer_load_dwordx4 v8, s[68:71], s92 offen lds\n" \
  "s_add_u32 m0, s86, 136192\n" \
  "s_add_u32 s92, s92, 65536\n" \
  "buffer_load_dwordx4 v9, s[68:71], s92 offen lds\n" \
  "s_add_u32 m0, s86, 137216\n" \
  "s_add_u32 s92, s92, 65536\n" \
  "buffer_load_dwordx4 v8, s[68:71], s92 offen lds\n" \
  "s_add_u32 m0, s86, 138240\n" \
  "s_add_u32 s92, s92, 65536\n" \
  "buffer_load_dwordx4 v9, s[68:71], s92 offen lds\n" \
  "ds_read_b64_tr_b16 a[224:225], v118 offset:0\n" \
  "ds_read_b64_tr_b16 a[226:227], v118 offset:512\n" \
  "ds_read_b64_tr_b16 a[228:229], v118 offset:4096\n" \
  "ds_read_b64_tr_b16 a[230:231], v118 offset:4608\n" \
  "ds_read_b64_tr_b16 a[232:233], v118 offset:8192\n" \
  "ds_read_b64_tr_b16 a[234:235], v118 offset:8704\n" \
  "ds_read_b64_tr_b16 a[236:237], v118 offset:12288\n" \
  "ds_read_b64_tr_b16 a[238:239], v118 offset:12800\n" \
  "ds_read_b64_tr_b16 a[240:241], v118 offset:16384\n" \
  "ds_read_b64_tr_b16 a[242:243], v118 offset:16896\n" \
  "ds_read_b64_tr_b16 a[244:245], v118 offset:20480\n" \
  "ds_read_b64_tr_b16 a[246:247], v118 offset:20992\n" \
  "ds_read_b64_tr_b16 a[248:249], v118 offset:24576\n" \
  "ds_read_b64_tr_b16 a[250:251], v118 offset:25088\n" \
  "ds_read_b64_tr_b16 a[252:253], v118 offset:28672\n" \
  "ds_read_b64_tr_b16 a[254:255], v118 offset:29184\n" \
  "s_waitcnt vmcnt(24) lgkmcnt(0)\n" \
  "s_barrier\n" \
  "s_add_u32 m0, s91, 0\n" \
  "s_nop 0\n" \
  "buffer_load_dwordx4 v7, s[72:75], s87 offen lds\n" \
  "s_add_u32 s87, s87, 128\n" \
  "s_add_u32 m0, s91, 4096\n" \
  "s_nop 0\n" \
  "buffer_load_dwordx4 v7, s[72:75], s87 offen lds\n" \
  "s_add_u32 s87, s87, 128\n" \
  "s_add_u32 m0, s91, 8192\n" \
  "s_nop 0\n" \
  "buffer_load_dwordx4 v7, s[72:75], s87 offen lds\n" \
  "s_add_u32 s87, s87, 128\n" \
  "s_add_u32 m0, s91, 12288\n" \
  "s_nop 0\n" \
  "buffer_load_dwordx4 v7, s[72:75], s87 offen lds\n" \
  "s_add_u32 s87, s87, 128\n" \
  "s_add_u32 m0, s91, 16384\n" \
  "s_nop 0\n" \
  "buffer_load_dwordx4 v7, s[72:75], s87 offen lds\n" \
  "s_add_u32 s87, s87, 128\n" \
  "s_add_u32 m0, s91, 20480\n" \
  "s_nop 0\n" \
  "buffer_load_dwordx4 v7, s[72:75], s87 offen lds\n" \
  "s_add_u32 s87, s87, 128\n" \
  "s_add_u32 m0, s91, 24576\n" \
  "s_nop 0\n" \
  "buffer_load_dwordx4 v7, s[72:75], s87 offen lds\n" \
  "s_add_u32 s87, s87, 128\n" \
  "s_add_u32 m0, s91, 28672\n" \
  "s_nop 0\n" \
  "buffer_load_dwordx4 v7, s[72:75], s87 offen lds\n" \
  "s_add_u32 s87, s87, 128\n" \
  "ds_read_b64_tr_b16 v[128:129], v118 offset:32768\n" \
  "ds_read_b64_tr_b16 v[130:131], v118 offset:33280\n" \
  "ds_read_b64_tr_b16 v[132:133], v118 offset:36864\n" \
  "ds_read_b64_tr_b16 v[134:135], v118 offset:37376\n" \
  "ds_read_b64_tr_b16 v[136:137], v118 offset:40960\n" \
  "ds_read_b64_tr_b16 v[138:139], v118 offset:41472\n" \
  "ds_read_b64_tr_b16 v[140:141], v118 offset:45056\n" \
  "ds_read_b64_tr_b16 v[142:143], v118 offset:45568\n" \
  "ds_read_b64_tr_b16 v[144:145], v118 offset:49152\n" \
  "ds_read_b64_tr_b16 v[146:147], v118 offset:49664\n" \
  "ds_read_b64_tr_b16 v[148:149], v118 offset:53248\n" \
  "ds_read_b64_tr_b16 v[150:151], v118 offset:53760\n" \
  "ds_read_b64_tr_b16 v[152:153], v118 offset:57344\n" \
  "ds_read_b64_tr_b16 v[154:155], v118 offset:57856\n" \
  "ds_read_b64_tr_b16 v[156:157], v118 offset:61440\n" \
  "ds_read_b64_tr_b16 v[158:159], v118 offset:61952\n" \
  "s_waitcnt vmcnt(24) lgkmcnt(0)\n" \
  "s_barrier\n" \
  "s_add_u32 m0, s91, 32768\n" \
  "s_nop 0\n" \
  "buffer_load_dwordx4 v7, s[72:75], s87 offen lds\n" \
  "s_add_u32 s87, s87, 128\n" \
  "s_add_u32 m0, s91, 36864\n" \
  "s_nop 0\n" \
  "buffer_load_dwordx4 v7, s[72:75], s87 offen lds\n" \
  "s_add_u32 s87, s87, 128\n" \
  "s_add_u32 m0, s91, 40960\n" \
  "s_nop 0\n" \
  "buffer_load_dwordx4 v7, s[72:75], s87 offen lds\n" \
  "s_add_u32 s87, s87, 128\n" \
  "s_add_u32 m0, s91, 45056\n" \
  "s_nop 0\n" \
  "buffer_load_dwordx4 v7, s[72:75], s87 offen lds\n" \
  "s_add_u32 s87, s87, 128\n" \
  "s_add_u32 m0, s91, 49152\n" \
  "s_nop 0\n" \
  "buffer_load_dwordx4 v7, s[72:75], s87 offen lds\n" \
  "s_add_u32 s87, s87, 128\n" \
  "s_add_u32 m0, s91, 53248\n" \
  "s_nop 0\n" \
  "buffer_load_dwordx4 v7, s[72:75], s87 offen lds\n" \
  "s_add_u32 s87, s87, 128\n" \
  "s_add_u32 m0, s91, 57344\n" \
  "s_nop 0\n" \
  "buffer_load_dwordx4 v7, s[72:75], s87 offen lds\n" \
  "s_add_u32 s87, s87, 128\n" \
  "s_add_u32 m0, s91, 61440\n" \
  "s_nop 0\n" \
  "buffer_load_dwordx4 v7, s[72:75], s87 offen lds\n" \
  "s_add_u32 s87, s87, 63616\n" \
  "s_min_u32 s87, s87, s96\n" \
  "ds_read_b64_tr_b16 v[160:161], v119 offset:0\n" \
  "ds_read_b64_tr_b16 v[162:163], v119 offset:512\n" \
  "ds_read_b64_tr_b16 v[164:165], v119 offset:4096\n" \
  "ds_read_b64_tr_b16 v[166:167], v119 offset:4608\n" \
  "ds_read_b64_tr_b16 v[168:169], v119 offset:8192\n" \
  "ds_read_b64_tr_b16 v[170:171], v119 offset:8704\n" \
  "ds_read_b64_tr_b16 v[172:173], v119 offset:12288\n" \
  "ds_read_b64_tr_b16 v[174:175], v119 offset:12800\n" \
  "ds_read_b64_tr_b16 v[176:177], v119 offset:16384\n" \
  "ds_read_b64_tr_b16 v[178:179], v119 offset:16896\n" \
  "ds_read_b64_tr_b16 v[180:181], v119 offset:20480\n" \
  "ds_read_b64_tr_b16 v[182:183], v119 offset:20992\n" \
  "ds_read_b64_tr_b16 v[184:185], v119 offset:24576\n" \
  "ds_read_b64_tr_b16 v[186:187], v119 offset:25088\n" \
  "ds_read_b64_tr_b16 v[188:189], v119 offset:28672\n" \
  "ds_read_b64_tr_b16 v[190:191], v119 offset:29184\n" \
  "s_waitcnt vmcnt(24) lgkmcnt(0)\n" \
  "s_barrier\n" \
  "s_add_u32 m0, s91, 65536\n" \
  "s_nop 0\n" \
  "buffer_load_dwordx4 v7, s[72:75], s87 offen lds\n" \
  "s_add_u32 s87, s87, 128\n" \
  "s_add_u32 m0, s91, 69632\n" \
  "s_nop 0\n" \
  "buffer_load_dwordx4 v7, s[72:75], s87 offen lds\n" \
  "s_add_u32 s87, s87, 128\n" \
  "s_add_u32 m0, s91, 73728\n" \
  "s_nop 0\n" \
  "buffer_load_dwordx4 v7, s[72:75], s87 offen lds\n" \
  "s_add_u32 s87, s87, 128\n" \
  "s_add_u32 m0, s91, 77824\n" \
  "s_nop 0\n" \
  "buffer_load_dwordx4 v7, s[72:75], s87 offen lds\n" \
  "s_add_u32 s87, s87, 128\n" \
  "s_add_u32 m0, s91, 81920\n" \
  "s_nop 0\n" \
  "buffer_load_dwordx4 v7, s[72:75], s87 offen lds\n" \
  "s_add_u32 s87, s87, 128\n" \
  "s_add_u32 m0, s91, 86016\n" \
  "s_nop 0\n" \
  "buffer_load_dwordx4 v7, s[72:75], s87 offen lds\n" \
  "s_add_u32 s87, s87, 128\n" \
  "s_add_u32 m0, s91, 90112\n" \
  "s_nop 0\n" \
  "buffer_load_dwordx4 v7, s[72:75], s87 offen lds\n" \
  "s_add_u32 s87, s87, 128\n" \
  "s_add_u32 m0, s91, 94208\n" \
  "s_nop 0\n" \
  "buffer_load_dwordx4 v7, s[72:75], s87 offen lds\n" \
  "s_add_u32 s87, s87, 128\n" \
  "ds_read_b64_tr_b16 v[192:193], v119 offset:32768\n" \
  "ds_read_b64_tr_b16 v[194:195], v119 offset:33280\n" \
  "ds_read_b64_tr_b16 v[196:197], v119 offset:36864\n" \
  "ds_read_b64_tr_b16 v[198:199], v119 offset:37376\n" \
  "ds_read_b64_tr_b16 v[200:201], v119 offset:40960\n" \
  "ds_read_b64_tr_b16 v[202:203], v119 offset:41472\n" \
  "ds_read_b64_tr_b16 v[204:205], v119 offset:45056\n" \
  "ds_read_b64_tr_b16 v[206:207], v119 offset:45568\n" \
  "ds_read_b64_tr_b16 v[208:209], v119 offset:49152\n" \
  "ds_read_b64_tr_b16 v[210:211], v119 offset:49664\n" \
  "ds_read_b64_tr_b16 v[212:213], v119 offset:53248\n" \
  "ds_read_b64_tr_b16 v[214:215], v119 offset:53760\n" \
  "ds_read_b64_tr_b16 v[216:217], v119 offset:57344\n" \
  "ds_read_b64_tr_b16 v[218:219], v119 offset:57856\n" \
  "ds_read_b64_tr_b16 v[220:221], v119 offset:61440\n" \
  "ds_read_b64_tr_b16 v[222:223], v119 offset:61952\n" \
  "s_waitcnt vmcnt(24) lgkmcnt(0)\n" \
  "s_barrier\n" \
  "ds_read_b64_tr_b16 v[224:225], v120 offset:0\n" \
  "ds_read_b64_tr_b16 v[226:227], v120 offset:512\n" \
  "ds_read_b64_tr_b16 v[228:229], v120 offset:4096\n" \
  "ds_read_b64_tr_b16 v[230:231], v120 offset:4608\n" \
  "ds_read_b64_tr_b16 v[232:233], v120 offset:8192\n" \
  "ds_read_b64_tr_b16 v[234:235], v120 offset:8704\n" \
  "ds_read_b64_tr_b16 v[236:237], v120 offset:12288\n" \
  "ds_read_b64_tr_b16 v[238:239], v120 offset:12800\n" \
  "ds_read_b64_tr_b16 v[240:241], v120 offset:16384\n" \
  "ds_read_b64_tr_b16 v[242:243], v120 offset:16896\n" \
  "ds_read_b64_tr_b16 v[244:245], v120 offset:20480\n" \
  "ds_read_b64_tr_b16 v[246:247], v120 offset:20992\n" \
  "ds_read_b64_tr_b16 v[248:249], v120 offset:24576\n" \
  "ds_read_b64_tr_b16 v[250:251], v120 offset:25088\n" \
  "ds_read_b64_tr_b16 v[252:253], v120 offset:28672\n" \
  "ds_read_b64_tr_b16 v[254:255], v120 offset:29184\n" \
  "s_waitcnt vmcnt(8) lgkmcnt(0)\n" \
  "s_barrier\n" \
  "ds_read_b128 v[68:71], v10 offset:0\n" \
  "ds_read_b128 v[72:75], v10 offset:2048\n" \
  "ds_read_b128 v[76:79], v11 offset:0\n" \
  "ds_read_b128 v[80:83], v11 offset:2048\n" \
  "ds_read_b128 v[84:87], v10 offset:4096\n" \
  "ds_read_b128 v[88:91], v10 offset:6144\n" \
  "s_mov_b32 s85, 0\n" \
  ".p2align 6\n" \
  "LG_%=:\n" \
  "s_cmp_eq_u32 s85, 64\n" \
  "s_cbranch_scc1 LX0_%=\n" \
  "s_waitcnt vmcnt(8)\n" \
  "s_barrier\n" \
  "s_waitcnt lgkmcnt(4)\n" \
  "v_mfma_f32_16x16x32_bf16 v[20:23], v[128:131], v[68:71], 0\n" \
  "ds_read_b128 v[92:95], v11 offset:4096\n" \
  "s_sub_u32 s89, s85, 1\n" \
  "s_and_b32 s89, s89, 63\n" \
  "v_mfma_f32_16x16x32_bf16 v[28:31], a[0:3], v[68:71], 0\n" \
  "ds_read_b128 v[96:99], v11 offset:6144\n" \
  "s_lshr_b32 s84, s89, 2\n" \
  "s_xnor_b32 s84, s84, %9\n" \
  "v_mfma_f32_16x16x32_bf16 v[36:39], v[68:71], a[128:131], 0\n" \
  "s_and_b32 s84, s84, 1\n" \
  "s_lshl_b32 s84, s84, 13\n" \
  "v_mfma_f32_16x16x32_bf16 v[24:27], v[128:131], v[72:75], 0\n" \
  "s_lshl_b32 s88, s89, 19\n" \
  "s_or_b32 s88, s88, s84\n" \
  "v_mfma_f32_16x16x32_bf16 v[32:35], a[0:3], v[72:75], 0\n" \
  "s_add_u32 s89, s88, " STR(QKV_SZ32) "\n" \
  "s_add_u32 s90, s89, " STR(QKV_SZ32) "\n" \
  "v_mfma_f32_16x16x32_bf16 v[40:43], v[72:75], a[128:131], 0\n" \
  "s_add_u32 s85, s85, 1\n" \
  "s_waitcnt lgkmcnt(4)\n" \
  "v_mfma_f32_16x16x32_bf16 v[20:23], v[132:135], v[76:79], v[20:23]\n" \
  "ds_read_b128 v[68:71], v10 offset:8192\n" \
  "v_cvt_pk_bf16_f32 v116, v44, v45\n" \
  "v_mfma_f32_16x16x32_bf16 v[28:31], a[4:7], v[76:79], v[28:31]\n" \
  "ds_read_b128 v[72:75], v10 offset:10240\n" \
  "v_cvt_pk_bf16_f32 v117, v46, v47\n" \
  "v_mfma_f32_16x16x32_bf16 v[36:39], v[76:79], a[132:135], v[36:39]\n" \
  "v_cvt_pk_bf16_f32 v118, v48, v49\n" \
  "v_mfma_f32_16x16x32_bf16 v[24:27], v[132:135], v[80:83], v[24:27]\n" \
  "v_cvt_pk_bf16_f32 v119, v50, v51\n" \
  "v_mfma_f32_16x16x32_bf16 v[32:35], a[4:7], v[80:83], v[32:35]\n" \
  "v_cvt_pk_bf16_f32 v120, v52, v53\n" \
  "v_mfma_f32_16x16x32_bf16 v[40:43], v[80:83], a[132:135], v[40:43]\n" \
  "v_cvt_pk_bf16_f32 v121, v54, v55\n" \
  "s_waitcnt lgkmcnt(4)\n" \
  "v_mfma_f32_16x16x32_bf16 v[20:23], v[136:139], v[84:87], v[20:23]\n" \
  "ds_read_b128 v[76:79], v11 offset:8192\n" \
  "v_cvt_pk_bf16_f32 v122, v56, v57\n" \
  "v_mfma_f32_16x16x32_bf16 v[28:31], a[8:11], v[84:87], v[28:31]\n" \
  "ds_read_b128 v[80:83], v11 offset:10240\n" \
  "v_cvt_pk_bf16_f32 v123, v58, v59\n" \
  "v_mfma_f32_16x16x32_bf16 v[36:39], v[84:87], a[136:139], v[36:39]\n" \
  "v_cvt_pk_bf16_f32 v124, v60, v61\n" \
  "v_mfma_f32_16x16x32_bf16 v[24:27], v[136:139], v[88:91], v[24:27]\n" \
  "v_cvt_pk_bf16_f32 v125, v62, v63\n" \
  "v_mfma_f32_16x16x32_bf16 v[32:35], a[8:11], v[88:91], v[32:35]\n" \
  "v_cvt_pk_bf16_f32 v126, v64, v65\n" \
  "v_mfma_f32_16x16x32_bf16 v[40:43], v[88:91], a[136:139], v[40:43]\n" \
  "v_cvt_pk_bf16_f32 v127, v66, v67\n" \
  "s_waitcnt lgkmcnt(4)\n" \
  "v_mfma_f32_16x16x32_bf16 v[20:23], v[140:143], v[92:95], v[20:23]\n" \
  "ds_read_b128 v[84:87], v10 offset:12288\n" \
  "v_permlane16_swap_b32 v116, v118\n" \
  "v_mfma_f32_16x16x32_bf16 v[28:31], a[12:15], v[92:95], v[28:31]\n" \
  "ds_read_b128 v[88:91], v10 offset:14336\n" \
  "v_permlane16_swap_b32 v117, v119\n" \
  "v_mfma_f32_16x16x32_bf16 v[36:39], v[92:95], a[140:143], v[36:39]\n" \
  "v_permlane16_swap_b32 v120, v122\n" \
  "v_mfma_f32_16x16x32_bf16 v[24:27], v[140:143], v[96:99], v[24:27]\n" \
  "v_permlane16_swap_b32 v121, v123\n" \
  "v_mfma_f32_16x16x32_bf16 v[32:35], a[12:15], v[96:99], v[32:35]\n" \
  "v_mfma_f32_16x16x32_bf16 v[40:43], v[96:99], a[140:143], v[40:43]\n" \
  "s_waitcnt lgkmcnt(4)\n" \
  "v_mfma_f32_16x16x32_bf16 v[20:23], v[144:147], v[68:71], v[20:23]\n" \
  "ds_read_b128 v[92:95], v11 offset:12288\n" \
  "buffer_store_dwordx4 v[116:119], v14, s[76:79], s88 offen sc1\n" \
  "v_mfma_f32_16x16x32_bf16 v[28:31], a[16:19], v[68:71], v[28:31]\n" \
  "ds_read_b128 v[96:99], v11 offset:14336\n" \
  "buffer_store_dwordx4 v[120:123], v14, s[76:79], s89 offen sc1\n" \
  "v_mfma_f32_16x16x32_bf16 v[36:39], v[68:71], a[144:147], v[36:39]\n" \
  "buffer_store_dwordx4 v[124:127], v15, s[76:79], s90 offen sc1\n" \
  "v_mfma_f32_16x16x32_bf16 v[24:27], v[144:147], v[72:75], v[24:27]\n" \
  "v_mfma_f32_16x16x32_bf16 v[32:35], a[16:19], v[72:75], v[32:35]\n" \
  "v_mfma_f32_16x16x32_bf16 v[40:43], v[72:75], a[144:147], v[40:43]\n" \
  "s_waitcnt lgkmcnt(4)\n" \
  "v_mfma_f32_16x16x32_bf16 v[20:23], v[148:151], v[76:79], v[20:23]\n" \
  "ds_read_b128 v[68:71], v10 offset:16384\n" \
  "v_mfma_f32_16x16x32_bf16 v[28:31], a[20:23], v[76:79], v[28:31]\n" \
  "ds_read_b128 v[72:75], v10 offset:18432\n" \
  "v_mfma_f32_16x16x32_bf16 v[36:39], v[76:79], a[148:151], v[36:39]\n" \
  "v_mfma_f32_16x16x32_bf16 v[24:27], v[148:151], v[80:83], v[24:27]\n" \
  "s_add_u32 m0, s91, 98304\n" \
  "s_nop 0\n" \
  "buffer_load_dwordx4 v7, s[72:75], s87 offen lds\n" \
  "s_add_u32 s87, s87, 128\n" \
  "v_mfma_f32_16x16x32_bf16 v[32:35], a[20:23], v[80:83], v[32:35]\n" \
  "v_mfma_f32_16x16x32_bf16 v[40:43], v[80:83], a[148:151], v[40:43]\n" \
  "s_waitcnt lgkmcnt(4)\n" \
  "v_mfma_f32_16x16x32_bf16 v[20:23], v[152:155], v[84:87], v[20:23]\n" \
  "ds_read_b128 v[76:79], v11 offset:16384\n" \
  "v_mfma_f32_16x16x32_bf16 v[28:31], a[24:27], v[84:87], v[28:31]\n" \
  "ds_read_b128 v[80:83], v11 offset:18432\n" \
  "v_mfma_f32_16x16x32_bf16 v[36:39], v[84:87], a[152:155], v[36:39]\n" \
  "v_mfma_f32_16x16x32_bf16 v[24:27], v[152:155], v[88:91], v[24:27]\n" \
  "s_add_u32 m0, s91, 102400\n" \
  "s_nop 0\n" \
  "buffer_load_dwordx4 v7, s[72:75], s87 offen lds\n" \
  "s_add_u32 s87, s87, 128\n" \
  "v_mfma_f32_16x16x32_bf16 v[32:35], a[24:27], v[88:91], v[32:35]\n" \
  "v_mfma_f32_16x16x32_bf16 v[40:43], v[88:91], a[152:155], v[40:43]\n" \
  "s_waitcnt lgkmcnt(4)\n" \
  "v_mfma_f32_16x16x32_bf16 v[20:23], v[156:159], v[92:95], v[20:23]\n" \
  "ds_read_b128 v[84:87], v10 offset:20480\n" \
  "v_mfma_f32_16x16x32_bf16 v[28:31], a[28:31], v[92:95], v[28:31]\n" \
  "ds_read_b128 v[88:91], v10 offset:22528\n" \
  "v_mfma_f32_16x16x32_bf16 v[36:39], v[92:95], a[156:159], v[36:39]\n" \
  "v_mfma_f32_16x16x32_bf16 v[24:27], v[156:159], v[96:99], v[24:27]\n" \
  "s_add_u32 m0, s91, 106496\n" \
  "s_nop 0\n" \
  "buffer_load_dwordx4 v7, s[72:75], s87 offen lds\n" \
  "s_add_u32 s87, s87, 128\n" \
  "v_mfma_f32_16x16x32_bf16 v[32:35], a[28:31], v[96:99], v[32:35]\n" \
  "v_mfma_f32_16x16x32_bf16 v[40:43], v[96:99], a[156:159], v[40:43]\n" \
  "s_waitcnt lgkmcnt(4)\n" \
  "v_mfma_f32_16x16x32_bf16 v[20:23], v[160:163], v[68:71], v[20:23]\n" \
  "ds_read_b128 v[92:95], v11 offset:20480\n" \
  "v_mfma_f32_16x16x32_bf16 v[28:31], a[32:35], v[68:71], v[28:31]\n" \
  "ds_read_b128 v[96:99], v11 offset:22528\n" \
  "v_mfma_f32_16x16x32_bf16 v[36:39], v[68:71], a[160:163], v[36:39]\n" \
  "v_mfma_f32_16x16x32_bf16 v[24:27], v[160:163], v[72:75], v[24:27]\n" \
  "s_add_u32 m0, s91, 110592\n" \
  "s_nop 0\n" \
  "buffer_load_dwordx4 v7, s[72:75], s87 offen lds\n" \
  "s_add_u32 s87, s87, 128\n" \
  "v_mfma_f32_16x16x32_bf16 v[32:35], a[32:35], v[72:75], v[32:35]\n" \
  "v_mfma_f32_16x16x32_bf16 v[40:43], v[72:75], a[160:163], v[40:43]\n" \
  "s_waitcnt lgkmcnt(4)\n" \
  "v_mfma_f32_16x16x32_bf16 v[20:23], v[164:167], v[76:79], v[20:23]\n" \
  "ds_read_b128 v[68:71], v10 offset:24576\n" \
  "v_mfma_f32_16x16x32_bf16 v[28:31], a[36:39], v[76:79], v[28:31]\n" \
  "ds_read_b128 v[72:75], v10 offset:26624\n" \
  "v_mfma_f32_16x16x32_bf16 v[36:39], v[76:79], a[164:167], v[36:39]\n" \
  "v_mfma_f32_16x16x32_bf16 v[24:27], v[164:167], v[80:83], v[24:27]\n" \
  "s_add_u32 m0, s91, 114688\n" \
  "s_nop 0\n" \
  "buffer_load_dwordx4 v7, s[72:75], s87 offen lds\n" \
  "s_add_u32 s87, s87, 128\n" \
  "v_mfma_f32_16x16x32_bf16 v[32:35], a[36:39], v[80:83], v[32:35]\n" \
  "v_mfma_f32_16x16x32_bf16 v[40:43], v[80:83], a[164:167], v[40:43]\n" \
  "s_waitcnt lgkmcnt(4)\n" \
  "v_mfma_f32_16x16x32_bf16 v[20:23], v[168:171], v[84:87], v[20:23]\n" \
  "ds_read_b128 v[76:79], v11 offset:24576\n" \
  "v_mfma_f32_16x16x32_bf16 v[28:31], a[40:43], v[84:87], v[28:31]\n" \
  "ds_read_b128 v[80:83], v11 offset:26624\n" \
  "v_mfma_f32_16x16x32_bf16 v[36:39], v[84:87], a[168:171], v[36:39]\n" \
  "v_mfma_f32_16x16x32_bf16 v[24:27], v[168:171], v[88:91], v[24:27]\n" \
  "s_add_u32 m0, s91, 118784\n" \
  "s_nop 0\n" \
  "buffer_load_dwordx4 v7, s[72:75], s87 offen lds\n" \
  "s_add_u32 s87, s87, 128\n" \
  "v_mfma_f32_16x16x32_bf16 v[32:35], a[40:43], v[88:91], v[32:35]\n" \
  "v_mfma_f32_16x16x32_bf16 v[40:43], v[88:91], a[168:171], v[40:43]\n" \
  "s_waitcnt lgkmcnt(4)\n" \
  "v_mfma_f32_16x16x32_bf16 v[20:23], v[172:175], v[92:95], v[20:23]\n" \
  "ds_read_b128 v[84:87], v10 offset:28672\n" \
  "v_mfma_f32_16x16x32_bf16 v[28:31], a[44:47], v[92:95], v[28:31]\n" \
  "ds_read_b128 v[88:91], v10 offset:30720\n" \
  "v_mfma_f32_16x16x32_bf16 v[36:39], v[92:95], a[172:175], v[36:39]\n" \
  "v_mfma_f32_16x16x32_bf16 v[24:27], v[172:175], v[96:99], v[24:27]\n" \
  "s_add_u32 m0, s91, 122880\n" \
  "s_nop 0\n" \
  "buffer_load_dwordx4 v7, s[72:75], s87 offen lds\n" \
  "s_add_u32 s87, s87, 128\n" \
  "v_mfma_f32_16x16x32_bf16 v[32:35], a[44:47], v[96:99], v[32:35]\n" \
  "v_mfma_f32_16x16x32_bf16 v[40:43], v[96:99], a[172:175], v[40:43]\n" \
  "s_waitcnt lgkmcnt(4)\n" \
  "v_mfma_f32_16x16x32_bf16 v[20:23], v[176:179], v[68:71], v[20:23]\n" \
  "ds_read_b128 v[92:95], v11 offset:28672\n" \
  "v_mfma_f32_16x16x32_bf16 v[28:31], a[48:51], v[68:71], v[28:31]\n" \
  "ds_read_b128 v[96:99], v11 offset:30720\n" \
  "v_mfma_f32_16x16x32_bf16 v[36:39], v[68:71], a[176:179], v[36:39]\n" \
  "v_mfma_f32_16x16x32_bf16 v[24:27], v[176:179], v[72:75], v[24:27]\n" \
  "s_add_u32 m0, s91, 126976\n" \
  "s_nop 0\n" \
  "buffer_load_dwordx4 v7, s[72:75], s87 offen lds\n" \
  "s_add_u32 s87, s87, 63616\n" \
  "s_min_u32 s87, s87, s96\n" \
  "v_mfma_f32_16x16x32_bf16 v[32:35], a[48:51], v[72:75], v[32:35]\n" \
  "v_mfma_f32_16x16x32_bf16 v[40:43], v[72:75], a[176:179], v[40:43]\n" \
  "s_waitcnt lgkmcnt(4)\n" \
  "v_mfma_f32_16x16x32_bf16 v[20:23], v[180:183], v[76:79], v[20:23]\n" \
  "ds_read_b128 v[68:71], v10 offset:32768\n" \
  "v_mfma_f32_16x16x32_bf16 v[28:31], a[52:55], v[76:79], v[28:31]\n" \
  "ds_read_b128 v[72:75], v10 offset:34816\n" \
  "v_mfma_f32_16x16x32_bf16 v[36:39], v[76:79], a[180:183], v[36:39]\n" \
  "v_mfma_f32_16x16x32_bf16 v[24:27], v[180:183], v[80:83], v[24:27]\n" \
  "v_mfma_f32_16x16x32_bf16 v[32:35], a[52:55], v[80:83], v[32:35]\n" \
  "v_mfma_f32_16x16x32_bf16 v[40:43], v[80:83], a[180:183], v[40:43]\n" \
  "s_waitcnt lgkmcnt(4)\n" \
  "v_mfma_f32_16x16x32_bf16 v[20:23], v[184:187], v[84:87], v[20:23]\n" \
  "ds_read_b128 v[76:79], v11 offset:32768\n" \
  "v_mfma_f32_16x16x32_bf16 v[28:31], a[56:59], v[84:87], v[28:31]\n" \
  "ds_read_b128 v[80:83], v11 offset:34816\n" \
  "v_mfma_f32_16x16x32_bf16 v[36:39], v[84:87], a[184:187], v[36:39]\n" \
  "v_mfma_f32_16x16x32_bf16 v[24:27], v[184:187], v[88:91], v[24:27]\n" \
  "v_mfma_f32_16x16x32_bf16 v[32:35], a[56:59], v[88:91], v[32:35]\n" \
  "v_mfma_f32_16x16x32_bf16 v[40:43], v[88:91], a[184:187], v[40:43]\n" \
  "s_waitcnt lgkmcnt(4)\n" \
  "v_mfma_f32_16x16x32_bf16 v[20:23], v[188:191], v[92:95], v[20:23]\n" \
  "ds_read_b128 v[84:87], v10 offset:36864\n" \
  "v_mfma_f32_16x16x32_bf16 v[28:31], a[60:63], v[92:95], v[28:31]\n" \
  "ds_read_b128 v[88:91], v10 offset:38912\n" \
  "v_mfma_f32_16x16x32_bf16 v[36:39], v[92:95], a[188:191], v[36:39]\n" \
  "v_mfma_f32_16x16x32_bf16 v[24:27], v[188:191], v[96:99], v[24:27]\n" \
  "v_mfma_f32_16x16x32_bf16 v[32:35], a[60:63], v[96:99], v[32:35]\n" \
  "v_mfma_f32_16x16x32_bf16 v[40:43], v[96:99], a[188:191], v[40:43]\n" \
  "s_waitcnt vmcnt(8)\n" \
  "s_barrier\n" \
  "s_waitcnt lgkmcnt(4)\n" \
  "v_mfma_f32_16x16x32_bf16 v[20:23], v[192:195], v[68:71], v[20:23]\n" \
  "ds_read_b128 v[92:95], v11 offset:36864\n" \
  "buffer_store_dword v17, v16, s[80:83], 0 offen sc1\n" \
  "v_mfma_f32_16x16x32_bf16 v[28:31], a[64:67], v[68:71], v[28:31]\n" \
  "ds_read_b128 v[96:99], v11 offset:38912\n" \
  "v_add_u32 v17, 1, v17\n" \
  "v_mfma_f32_16x16x32_bf16 v[36:39], v[68:71], a[192:195], v[36:39]\n" \
  "v_and_b32 v14, 0x7fffffff, v14\n" \
  "v_mfma_f32_16x16x32_bf16 v[24:27], v[192:195], v[72:75], v[24:27]\n" \
  "v_and_b32 v15, 0x7fffffff, v15\n" \
  "v_mfma_f32_16x16x32_bf16 v[32:35], a[64:67], v[72:75], v[32:35]\n" \
  "v_mfma_f32_16x16x32_bf16 v[40:43], v[72:75], a[192:195], v[40:43]\n" \
  "s_waitcnt lgkmcnt(4)\n" \
  "v_mfma_f32_16x16x32_bf16 v[20:23], v[196:199], v[76:79], v[20:23]\n" \
  "ds_read_b128 v[68:71], v10 offset:40960\n" \
  "v_mfma_f32_16x16x32_bf16 v[28:31], a[68:71], v[76:79], v[28:31]\n" \
  "ds_read_b128 v[72:75], v10 offset:43008\n" \
  "v_mfma_f32_16x16x32_bf16 v[36:39], v[76:79], a[196:199], v[36:39]\n" \
  "v_mfma_f32_16x16x32_bf16 v[24:27], v[196:199], v[80:83], v[24:27]\n" \
  "v_mfma_f32_16x16x32_bf16 v[32:35], a[68:71], v[80:83], v[32:35]\n" \
  "v_mfma_f32_16x16x32_bf16 v[40:43], v[80:83], a[196:199], v[40:43]\n" \
  "s_waitcnt lgkmcnt(4)\n" \
  "v_mfma_f32_16x16x32_bf16 v[20:23], v[200:203], v[84:87], v[20:23]\n" \
  "ds_read_b128 v[76:79], v11 offset:40960\n" \
  "v_mfma_f32_16x16x32_bf16 v[28:31], a[72:75], v[84:87], v[28:31]\n" \
  "ds_read_b128 v[80:83], v11 offset:43008\n" \
  "v_mfma_f32_16x16x32_bf16 v[36:39], v[84:87], a[200:203], v[36:39]\n" \
  "v_mfma_f32_16x16x32_bf16 v[24:27], v[200:203], v[88:91], v[24:27]\n" \
  "v_mfma_f32_16x16x32_bf16 v[32:35], a[72:75], v[88:91], v[32:35]\n" \
  "v_mfma_f32_16x16x32_bf16 v[40:43], v[88:91], a[200:203], v[40:43]\n" \
  "s_waitcnt lgkmcnt(4)\n" \
  "v_mfma_f32_16x16x32_bf16 v[20:23], v[204:207], v[92:95], v[20:23]\n" \
  "ds_read_b128 v[84:87], v10 offset:45056\n" \
  "v_mfma_f32_16x16x32_bf16 v[28:31], a[76:79], v[92:95], v[28:31]\n" \
  "ds_read_b128 v[88:91], v10 offset:47104\n" \
  "v_mfma_f32_16x16x32_bf16 v[36:39], v[92:95], a[204:207], v[36:39]\n" \
  "v_mfma_f32_16x16x32_bf16 v[24:27], v[204:207], v[96:99], v[24:27]\n" \
  "v_mfma_f32_16x16x32_bf16 v[32:35], a[76:79], v[96:99], v[32:35]\n" \
  "v_mfma_f32_16x16x32_bf16 v[40:43], v[96:99], a[204:207], v[40:43]\n" \
  "s_waitcnt lgkmcnt(4)\n" \
  "v_mfma_f32_16x16x32_bf16 v[20:23], v[208:211], v[68:71], v[20:23]\n" \
  "ds_read_b128 v[92:95], v11 offset:45056\n" \
  "v_mfma_f32_16x16x32_bf16 v[28:31], a[80:83], v[68:71], v[28:31]\n" \
  "ds_read_b128 v[96:99], v11 offset:47104\n" \
  "v_mfma_f32_16x16x32_bf16 v[36:39], v[68:71], a[208:211], v[36:39]\n" \
  "v_mfma_f32_16x16x32_bf16 v[24:27], v[208:211], v[72:75], v[24:27]\n" \
  "v_mfma_f32_16x16x32_bf16 v[32:35], a[80:83], v[72:75], v[32:35]\n" \
  "v_mfma_f32_16x16x32_bf16 v[40:43], v[72:75], a[208:211], v[40:43]\n" \
  "s_waitcnt lgkmcnt(4)\n" \
  "v_mfma_f32_16x16x32_bf16 v[20:23], v[212:215], v[76:79], v[20:23]\n" \
  "ds_read_b128 v[68:71], v10 offset:49152\n" \
  "v_mfma_f32_16x16x32_bf16 v[28:31], a[84:87], v[76:79], v[28:31]\n" \
  "ds_read_b128 v[72:75], v10 offset:51200\n" \
  "v_mfma_f32_16x16x32_bf16 v[36:39], v[76:79], a[212:215], v[36:39]\n" \
  "v_mfma_f32_16x16x32_bf16 v[24:27], v[212:215], v[80:83], v[24:27]\n" \
  "s_add_u32 m0, s91, 0\n" \
  "s_nop 0\n" \
  "buffer_load_dwordx4 v7, s[72:75], s87 offen lds\n" \
  "s_add_u32 s87, s87, 128\n" \
  "v_mfma_f32_16x16x32_bf16 v[32:35], a[84:87], v[80:83], v[32:35]\n" \
  "v_mfma_f32_16x16x32_bf16 v[40:43], v[80:83], a[212:215], v[40:43]\n" \
  "s_waitcnt lgkmcnt(4)\n" \
  "v_mfma_f32_16x16x32_bf16 v[20:23], v[216:219], v[84:87], v[20:23]\n" \
  "ds_read_b128 v[76:79], v11 offset:49152\n" \
  "v_mfma_f32_16x16x32_bf16 v[28:31], a[88:91], v[84:87], v[28:31]\n" \
  "ds_read_b128 v[80:83], v11 offset:51200\n" \
  "v_mfma_f32_16x16x32_bf16 v[36:39], v[84:87], a[216:219], v[36:39]\n" \
  "v_mfma_f32_16x16x32_bf16 v[24:27], v[216:219], v[88:91], v[24:27]\n" \
  "s_add_u32 m0, s91, 4096\n" \
  "s_nop 0\n" \
  "buffer_load_dwordx4 v7, s[72:75], s87 offen lds\n" \
  "s_add_u32 s87, s87, 128\n" \
  "v_mfma_f32_16x16x32_bf16 v[32:35], a[88:91], v[88:91], v[32:35]\n" \
  "v_mfma_f32_16x16x32_bf16 v[40:43], v[88:91], a[216:219], v[40:43]\n" \
  "s_waitcnt lgkmcnt(4)\n" \
  "v_mfma_f32_16x16x32_bf16 v[20:23], v[220:223], v[92:95], v[20:23]\n" \
  "ds_read_b128 v[84:87], v10 offset:53248\n" \
  "v_mfma_f32_16x16x32_bf16 v[28:31], a[92:95], v[92:95], v[28:31]\n" \
  "ds_read_b128 v[88:91], v10 offset:55296\n" \
  "v_mfma_f32_16x16x32_bf16 v[36:39], v[92:95], a[220:223], v[36:39]\n" \
  "v_mfma_f32_16x16x32_bf16 v[24:27], v[220:223], v[96:99], v[24:27]\n" \
  "s_add_u32 m0, s91, 8192\n" \
  "s_nop 0\n" \
  "buffer_load_dwordx4 v7, s[72:75], s87 offen lds\n" \
  "s_add_u32 s87, s87, 128\n" \
  "v_mfma_f32_16x16x32_bf16 v[32:35], a[92:95], v[96:99], v[32:35]\n" \
  "v_mfma_f32_16x16x32_bf16 v[40:43], v[96:99], a[220:223], v[40:43]\n" \
  "s_waitcnt lgkmcnt(4)\n" \
  "v_mfma_f32_16x16x32_bf16 v[20:23], v[224:227], v[68:71], v[20:23]\n" \
  "ds_read_b128 v[92:95], v11 offset:53248\n" \
  "v_mfma_f32_16x16x32_bf16 v[28:31], a[96:99], v[68:71], v[28:31]\n" \
  "ds_read_b128 v[96:99], v11 offset:55296\n" \
  "v_mfma_f32_16x16x32_bf16 v[36:39], v[68:71], a[224:227], v[36:39]\n" \
  "v_mfma_f32_16x16x32_bf16 v[24:27], v[224:227], v[72:75], v[24:27]\n" \
  "s_add_u32 m0, s91, 12288\n" \
  "s_nop 0\n" \
  "buffer_load_dwordx4 v7, s[72:75], s87 offen lds\n" \
  "s_add_u32 s87, s87, 128\n" \
  "v_mfma_f32_16x16x32_bf16 v[32:35], a[96:99], v[72:75], v[32:35]\n" \
  "v_mfma_f32_16x16x32_bf16 v[40:43], v[72:75], a[224:227], v[40:43]\n" \
  "s_waitcnt lgkmcnt(4)\n" \
  "v_mfma_f32_16x16x32_bf16 v[20:23], v[228:231], v[76:79], v[20:23]\n" \
  "ds_read_b128 v[68:71], v10 offset:57344\n" \
  "v_mfma_f32_16x16x32_bf16 v[28:31], a[100:103], v[76:79], v[28:31]\n" \
  "ds_read_b128 v[72:75], v10 offset:59392\n" \
  "v_mfma_f32_16x16x32_bf16 v[36:39], v[76:79], a[228:231], v[36:39]\n" \
  "v_mfma_f32_16x16x32_bf16 v[24:27], v[228:231], v[80:83], v[24:27]\n" \
  "s_add_u32 m0, s91, 16384\n" \
  "s_nop 0\n" \
  "buffer_load_dwordx4 v7, s[72:75], s87 offen lds\n" \
  "s_add_u32 s87, s87, 128\n" \
  "v_mfma_f32_16x16x32_bf16 v[32:35], a[100:103], v[80:83], v[32:35]\n" \
  "v_mfma_f32_16x16x32_bf16 v[40:43], v[80:83], a[228:231], v[40:43]\n" \
  "s_waitcnt lgkmcnt(4)\n" \
  "v_mfma_f32_16x16x32_bf16 v[20:23], v[232:235], v[84:87], v[20:23]\n" \
  "ds_read_b128 v[76:79], v11 offset:57344\n" \
  "v_mfma_f32_16x16x32_bf16 v[28:31], a[104:107], v[84:87], v[28:31]\n" \
  "ds_read_b128 v[80:83], v11 offset:59392\n" \
  "v_mfma_f32_16x16x32_bf16 v[36:39], v[84:87], a[232:235], v[36:39]\n" \
  "v_mfma_f32_16x16x32_bf16 v[24:27], v[232:235], v[88:91], v[24:27]\n" \
  "s_add_u32 m0, s91, 20480\n" \
  "s_nop 0\n" \
  "buffer_load_dwordx4 v7, s[72:75], s87 offen lds\n" \
  "s_add_u32 s87, s87, 128\n" \
  "v_mfma_f32_16x16x32_bf16 v[32:35], a[104:107], v[88:91], v[32:35]\n" \
  "v_mfma_f32_16x16x32_bf16 v[40:43], v[88:91], a[232:235], v[40:43]\n" \
  "s_waitcnt lgkmcnt(4)\n" \
  "v_mfma_f32_16x16x32_bf16 v[20:23], v[236:239], v[92:95], v[20:23]\n" \
  "ds_read_b128 v[84:87], v10 offset:61440\n" \
  "v_mfma_f32_16x16x32_bf16 v[28:31], a[108:111], v[92:95], v[28:31]\n" \
  "ds_read_b128 v[88:91], v10 offset:63488\n" \
  "v_mfma_f32_16x16x32_bf16 v[36:39], v[92:95], a[236:239], v[36:39]\n" \
  "v_mfma_f32_16x16x32_bf16 v[24:27], v[236:239], v[96:99], v[24:27]\n" \
  "s_add_u32 m0, s91, 24576\n" \
  "s_nop 0\n" \
  "buffer_load_dwordx4 v7, s[72:75], s87 offen lds\n" \
  "s_add_u32 s87, s87, 128\n" \
  "v_mfma_f32_16x16x32_bf16 v[32:35], a[108:111], v[96:99], v[32:35]\n" \
  "v_mfma_f32_16x16x32_bf16 v[40:43], v[96:99], a[236:239], v[40:43]\n" \
  "s_waitcnt lgkmcnt(4)\n" \
  "v_mfma_f32_16x16x32_bf16 v[20:23], v[240:243], v[68:71], v[20:23]\n" \
  "ds_read_b128 v[92:95], v11 offset:61440\n" \
  "v_mfma_f32_16x16x32_bf16 v[28:31], a[112:115], v[68:71], v[28:31]\n" \
  "ds_read_b128 v[96:99], v11 offset:63488\n" \
  "v_mfma_f32_16x16x32_bf16 v[36:39], v[68:71], a[240:243], v[36:39]\n" \
  "v_mfma_f32_16x16x32_bf16 v[24:27], v[240:243], v[72:75], v[24:27]\n" \
  "s_add_u32 m0, s91, 28672\n" \
  "s_nop 0\n" \
  "buffer_load_dwordx4 v7, s[72:75], s87 offen lds\n" \
  "s_add_u32 s87, s87, 128\n" \
  "v_mfma_f32_16x16x32_bf16 v[32:35], a[112:115], v[72:75], v[32:35]\n" \
  "v_mfma_f32_16x16x32_bf16 v[40:43], v[72:75], a[240:243], v[40:43]\n" \
  "s_waitcnt lgkmcnt(4)\n" \
  "v_mfma_f32_16x16x32_bf16 v[20:23], v[244:247], v[76:79], v[20:23]\n" \
  "ds_read_b128 v[68:71], v12 offset:0\n" \
  "v_mfma_f32_16x16x32_bf16 v[28:31], a[116:119], v[76:79], v[28:31]\n" \
  "ds_read_b128 v[72:75], v12 offset:2048\n" \
  "v_mfma_f32_16x16x32_bf16 v[36:39], v[76:79], a[244:247], v[36:39]\n" \
  "v_mfma_f32_16x16x32_bf16 v[24:27], v[244:247], v[80:83], v[24:27]\n" \
  "v_mfma_f32_16x16x32_bf16 v[32:35], a[116:119], v[80:83], v[32:35]\n" \
  "v_mfma_f32_16x16x32_bf16 v[40:43], v[80:83], a[244:247], v[40:43]\n" \
  "s_waitcnt lgkmcnt(4)\n" \
  "v_mfma_f32_16x16x32_bf16 v[20:23], v[248:251], v[84:87], v[20:23]\n" \
  "ds_read_b128 v[76:79], v13 offset:0\n" \
  "v_mfma_f32_16x16x32_bf16 v[28:31], a[120:123], v[84:87], v[28:31]\n" \
  "ds_read_b128 v[80:83], v13 offset:2048\n" \
  "v_mfma_f32_16x16x32_bf16 v[36:39], v[84:87], a[248:251], v[36:39]\n" \
  "v_mfma_f32_16x16x32_bf16 v[24:27], v[248:251], v[88:91], v[24:27]\n" \
  "v_mfma_f32_16x16x32_bf16 v[32:35], a[120:123], v[88:91], v[32:35]\n" \
  "v_mfma_f32_16x16x32_bf16 v[40:43], v[88:91], a[248:251], v[40:43]\n" \
  "s_waitcnt lgkmcnt(4)\n" \
  "v_mfma_f32_16x16x32_bf16 v[20:23], v[252:255], v[92:95], v[20:23]\n" \
  "ds_read_b128 v[84:87], v12 offset:4096\n" \
  "v_mfma_f32_16x16x32_bf16 v[28:31], a[124:127], v[92:95], v[28:31]\n" \
  "ds_read_b128 v[88:91], v12 offset:6144\n" \
  "v_mfma_f32_16x16x32_bf16 v[36:39], v[92:95], a[252:255], v[36:39]\n" \
  "v_mfma_f32_16x16x32_bf16 v[24:27], v[252:255], v[96:99], v[24:27]\n" \
  "v_mfma_f32_16x16x32_bf16 v[32:35], a[124:127], v[96:99], v[32:35]\n" \
  "v_mfma_f32_16x16x32_bf16 v[40:43], v[96:99], a[252:255], v[40:43]\n" \
  "s_cmp_eq_u32 s85, 64\n" \
  "s_cbranch_scc1 LX1_%=\n" \
  "s_waitcnt vmcnt(8)\n" \
  "s_barrier\n" \
  "s_waitcnt lgkmcnt(4)\n" \
  "v_mfma_f32_16x16x32_bf16 v[44:47], v[128:131], v[68:71], 0\n" \
  "ds_read_b128 v[92:95], v13 offset:4096\n" \
  "s_sub_u32 s89, s85, 1\n" \
  "s_and_b32 s89, s89, 63\n" \
  "v_mfma_f32_16x16x32_bf16 v[52:55], a[0:3], v[68:71], 0\n" \
  "ds_read_b128 v[96:99], v13 offset:6144\n" \
  "s_lshr_b32 s84, s89, 2\n" \
  "s_xnor_b32 s84, s84, %9\n" \
  "v_mfma_f32_16x16x32_bf16 v[60:63], v[68:71], a[128:131], 0\n" \
  "s_and_b32 s84, s84, 1\n" \
  "s_lshl_b32 s84, s84, 13\n" \
  "v_mfma_f32_16x16x32_bf16 v[48:51], v[128:131], v[72:75], 0\n" \
  "s_lshl_b32 s88, s89, 19\n" \
  "s_or_b32 s88, s88, s84\n" \
  "v_mfma_f32_16x16x32_bf16 v[56:59], a[0:3], v[72:75], 0\n" \
  "s_add_u32 s89, s88, " STR(QKV_SZ32) "\n" \
  "s_add_u32 s90, s89, " STR(QKV_SZ32) "\n" \
  "v_mfma_f32_16x16x32_bf16 v[64:67], v[72:75], a[128:131], 0\n" \
  "s_add_u32 s85, s85, 1\n" \
  "s_waitcnt lgkmcnt(4)\n" \
  "v_mfma_f32_16x16x32_bf16 v[44:47], v[132:135], v[76:79], v[44:47]\n" \
  "ds_read_b128 v[68:71], v12 offset:8192\n" \
  "v_cvt_pk_bf16_f32 v116, v20, v21\n" \
  "v_mfma_f32_16x16x32_bf16 v[52:55], a[4:7], v[76:79], v[52:55]\n" \
  "ds_read_b128 v[72:75], v12 offset:10240\n" \
  "v_cvt_pk_bf16_f32 v117, v22, v23\n" \
  "v_mfma_f32_16x16x32_bf16 v[60:63], v[76:79], a[132:135], v[60:63]\n" \
  "v_cvt_pk_bf16_f32 v118, v24, v25\n" \
  "v_mfma_f32_16x16x32_bf16 v[48:51], v[132:135], v[80:83], v[48:51]\n" \
  "v_cvt_pk_bf16_f32 v119, v26, v27\n" \
  "v_mfma_f32_16x16x32_bf16 v[56:59], a[4:7], v[80:83], v[56:59]\n" \
  "v_cvt_pk_bf16_f32 v120, v28, v29\n" \
  "v_mfma_f32_16x16x32_bf16 v[64:67], v[80:83], a[132:135], v[64:67]\n" \
  "v_cvt_pk_bf16_f32 v121, v30, v31\n" \
  "s_waitcnt lgkmcnt(4)\n" \
  "v_mfma_f32_16x16x32_bf16 v[44:47], v[136:139], v[84:87], v[44:47]\n" \
  "ds_read_b128 v[76:79], v13 offset:8192\n" \
  "v_cvt_pk_bf16_f32 v122, v32, v33\n" \
  "v_mfma_f32_16x16x32_bf16 v[52:55], a[8:11], v[84:87], v[52:55]\n" \
  "ds_read_b128 v[80:83], v13 offset:10240\n" \
  "v_cvt_pk_bf16_f32 v123, v34, v35\n" \
  "v_mfma_f32_16x16x32_bf16 v[60:63], v[84:87], a[136:139], v[60:63]\n" \
  "v_cvt_pk_bf16_f32 v124, v36, v37\n" \
  "v_mfma_f32_16x16x32_bf16 v[48:51], v[136:139], v[88:91], v[48:51]\n" \
  "v_cvt_pk_bf16_f32 v125, v38, v39\n" \
  "v_mfma_f32_16x16x32_bf16 v[56:59], a[8:11], v[88:91], v[56:59]\n" \
  "v_cvt_pk_bf16_f32 v126, v40, v41\n" \
  "v_mfma_f32_16x16x32_bf16 v[64:67], v[88:91], a[136:139], v[64:67]\n" \
  "v_cvt_pk_bf16_f32 v127, v42, v43\n" \
  "s_waitcnt lgkmcnt(4)\n" \
  "v_mfma_f32_16x16x32_bf16 v[44:47], v[140:143], v[92:95], v[44:47]\n" \
  "ds_read_b128 v[84:87], v12 offset:12288\n" \
  "v_permlane16_swap_b32 v116, v118\n" \
  "v_mfma_f32_16x16x32_bf16 v[52:55], a[12:15], v[92:95], v[52:55]\n" \
  "ds_read_b128 v[88:91], v12 offset:14336\n" \
  "v_permlane16_swap_b32 v117, v119\n" \
  "v_mfma_f32_16x16x32_bf16 v[60:63], v[92:95], a[140:143], v[60:63]\n" \
  "v_permlane16_swap_b32 v120, v122\n" \
  "v_mfma_f32_16x16x32_bf16 v[48:51], v[140:143], v[96:99], v[48:51]\n" \
  "v_permlane16_swap_b32 v121, v123\n" \
  "v_mfma_f32_16x16x32_bf16 v[56:59], a[12:15], v[96:99], v[56:59]\n" \
  "v_mfma_f32_16x16x32_bf16 v[64:67], v[96:99], a[140:143], v[64:67]\n" \
  "s_waitcnt lgkmcnt(4)\n" \
  "v_mfma_f32_16x16x32_bf16 v[44:47], v[144:147], v[68:71], v[44:47]\n" \
  "ds_read_b128 v[92:95], v13 offset:12288\n" \
  "buffer_store_dwordx4 v[116:119], v14, s[76:79], s88 offen sc1\n" \
  "v_mfma_f32_16x16x32_bf16 v[52:55], a[16:19], v[68:71], v[52:55]\n" \
  "ds_read_b128 v[96:99], v13 offset:14336\n" \
  "buffer_store_dwordx4 v[120:123], v14, s[76:79], s89 offen sc1\n" \
  "v_mfma_f32_16x16x32_bf16 v[60:63], v[68:71], a[144:147], v[60:63]\n" \
  "buffer_store_dwordx4 v[124:127], v15, s[76:79], s90 offen sc1\n" \
  "v_mfma_f32_16x16x32_bf16 v[48:51], v[144:147], v[72:75], v[48:51]\n" \
  "v_mfma_f32_16x16x32_bf16 v[56:59], a[16:19], v[72:75], v[56:59]\n" \
  "v_mfma_f32_16x16x32_bf16 v[64:67], v[72:75], a[144:147], v[64:67]\n" \
  "s_waitcnt lgkmcnt(4)\n" \
  "v_mfma_f32_16x16x32_bf16 v[44:47], v[148:151], v[76:79], v[44:47]\n" \
  "ds_read_b128 v[68:71], v12 offset:16384\n" \
  "v_mfma_f32_16x16x32_bf16 v[52:55], a[20:23], v[76:79], v[52:55]\n" \
  "ds_read_b128 v[72:75], v12 offset:18432\n" \
  "v_mfma_f32_16x16x32_bf16 v[60:63], v[76:79], a[148:151], v[60:63]\n" \
  "v_mfma_f32_16x16x32_bf16 v[48:51], v[148:151], v[80:83], v[48:51]\n" \
  "s_add_u32 m0, s91, 32768\n" \
  "s_nop 0\n" \
  "buffer_load_dwordx4 v7, s[72:75], s87 offen lds\n" \
  "s_add_u32 s87, s87, 128\n" \
  "v_mfma_f32_16x16x32_bf16 v[56:59], a[20:23], v[80:83], v[56:59]\n" \
  "v_mfma_f32_16x16x32_bf16 v[64:67], v[80:83], a[148:151], v[64:67]\n" \
  "s_waitcnt lgkmcnt(4)\n" \
  "v_mfma_f32_16x16x32_bf16 v[44:47], v[152:155], v[84:87], v[44:47]\n" \
  "ds_read_b128 v[76:79], v13 offset:16384\n" \
  "v_mfma_f32_16x16x32_bf16 v[52:55], a[24:27], v[84:87], v[52:55]\n" \
  "ds_read_b128 v[80:83], v13 offset:18432\n" \
  "v_mfma_f32_16x16x32_bf16 v[60:63], v[84:87], a[152:155], v[60:63]\n" \
  "v_mfma_f32_16x16x32_bf16 v[48:51], v[152:155], v[88:91], v[48:51]\n" \
  "s_add_u32 m0, s91, 36864\n" \
  "s_nop 0\n" \
  "buffer_load_dwordx4 v7, s[72:75], s87 offen lds\n" \
  "s_add_u32 s87, s87, 128\n" \
  "v_mfma_f32_16x16x32_bf16 v[56:59], a[24:27], v[88:91], v[56:59]\n" \
  "v_mfma_f32_16x16x32_bf16 v[64:67], v[88:91], a[152:155], v[64:67]\n" \
  "s_waitcnt lgkmcnt(4)\n" \
  "v_mfma_f32_16x16x32_bf16 v[44:47], v[156:159], v[92:95], v[44:47]\n" \
  "ds_read_b128 v[84:87], v12 offset:20480\n" \
  "v_mfma_f32_16x16x32_bf16 v[52:55], a[28:31], v[92:95], v[52:55]\n" \
  "ds_read_b128 v[88:91], v12 offset:22528\n" \
  "v_mfma_f32_16x16x32_bf16 v[60:63], v[92:95], a[156:159], v[60:63]\n" \
  "v_mfma_f32_16x16x32_bf16 v[48:51], v[156:159], v[96:99], v[48:51]\n" \
  "s_add_u32 m0, s91, 40960\n" \
  "s_nop 0\n" \
  "buffer_load_dwordx4 v7, s[72:75], s87 offen lds\n" \
  "s_add_u32 s87, s87, 128\n" \
  "v_mfma_f32_16x16x32_bf16 v[56:59], a[28:31], v[96:99], v[56:59]\n" \
  "v_mfma_f32_16x16x32_bf16 v[64:67], v[96:99], a[156:159], v[64:67]\n" \
  "s_waitcnt lgkmcnt(4)\n" \
  "v_mfma_f32_16x16x32_bf16 v[44:47], v[160:163], v[68:71], v[44:47]\n" \
  "ds_read_b128 v[92:95], v13 offset:20480\n" \
  "v_mfma_f32_16x16x32_bf16 v[52:55], a[32:35], v[68:71], v[52:55]\n" \
  "ds_read_b128 v[96:99], v13 offset:22528\n" \
  "v_mfma_f32_16x16x32_bf16 v[60:63], v[68:71], a[160:163], v[60:63]\n" \
  "v_mfma_f32_16x16x32_bf16 v[48:51], v[160:163], v[72:75], v[48:51]\n" \
  "s_add_u32 m0, s91, 45056\n" \
  "s_nop 0\n" \
  "buffer_load_dwordx4 v7, s[72:75], s87 offen lds\n" \
  "s_add_u32 s87, s87, 128\n" \
  "v_mfma_f32_16x16x32_bf16 v[56:59], a[32:35], v[72:75], v[56:59]\n" \
  "v_mfma_f32_16x16x32_bf16 v[64:67], v[72:75], a[160:163], v[64:67]\n" \
  "s_waitcnt lgkmcnt(4)\n" \
  "v_mfma_f32_16x16x32_bf16 v[44:47], v[164:167], v[76:79], v[44:47]\n" \
  "ds_read_b128 v[68:71], v12 offset:24576\n" \
  "v_mfma_f32_16x16x32_bf16 v[52:55], a[36:39], v[76:79], v[52:55]\n" \
  "ds_read_b128 v[72:75], v12 offset:26624\n" \
  "v_mfma_f32_16x16x32_bf16 v[60:63], v[76:79], a[164:167], v[60:63]\n" \
  "v_mfma_f32_16x16x32_bf16 v[48:51], v[164:167], v[80:83], v[48:51]\n" \
  "s_add_u32 m0, s91, 49152\n" \
  "s_nop 0\n" \
  "buffer_load_dwordx4 v7, s[72:75], s87 offen lds\n" \
  "s_add_u32 s87, s87, 128\n" \
  "v_mfma_f32_16x16x32_bf16 v[56:59], a[36:39], v[80:83], v[56:59]\n" \
  "v_mfma_f32_16x16x32_bf16 v[64:67], v[80:83], a[164:167], v[64:67]\n" \
  "s_waitcnt lgkmcnt(4)\n" \
  "v_mfma_f32_16x16x32_bf16 v[44:47], v[168:171], v[84:87], v[44:47]\n" \
  "ds_read_b128 v[76:79], v13 offset:24576\n" \
  "v_mfma_f32_16x16x32_bf16 v[52:55], a[40:43], v[84:87], v[52:55]\n" \
  "ds_read_b128 v[80:83], v13 offset:26624\n" \
  "v_mfma_f32_16x16x32_bf16 v[60:63], v[84:87], a[168:171], v[60:63]\n" \
  "v_mfma_f32_16x16x32_bf16 v[48:51], v[168:171], v[88:91], v[48:51]\n" \
  "s_add_u32 m0, s91, 53248\n" \
  "s_nop 0\n" \
  "buffer_load_dwordx4 v7, s[72:75], s87 offen lds\n" \
  "s_add_u32 s87, s87, 128\n" \
  "v_mfma_f32_16x16x32_bf16 v[56:59], a[40:43], v[88:91], v[56:59]\n" \
  "v_mfma_f32_16x16x32_bf16 v[64:67], v[88:91], a[168:171], v[64:67]\n" \
  "s_waitcnt lgkmcnt(4)\n" \
  "v_mfma_f32_16x16x32_bf16 v[44:47], v[172:175], v[92:95], v[44:47]\n" \
  "ds_read_b128 v[84:87], v12 offset:28672\n" \
  "v_mfma_f32_16x16x32_bf16 v[52:55], a[44:47], v[92:95], v[52:55]\n" \
  "ds_read_b128 v[88:91], v12 offset:30720\n" \
  "v_mfma_f32_16x16x32_bf16 v[60:63], v[92:95], a[172:175], v[60:63]\n" \
  "v_mfma_f32_16x16x32_bf16 v[48:51], v[172:175], v[96:99], v[48:51]\n" \
  "s_add_u32 m0, s91, 57344\n" \
  "s_nop 0\n" \
  "buffer_load_dwordx4 v7, s[72:75], s87 offen lds\n" \
  "s_add_u32 s87, s87, 128\n" \
  "v_mfma_f32_16x16x32_bf16 v[56:59], a[44:47], v[96:99], v[56:59]\n" \
  "v_mfma_f32_16x16x32_bf16 v[64:67], v[96:99], a[172:175], v[64:67]\n" \
  "s_waitcnt lgkmcnt(4)\n" \
  "v_mfma_f32_16x16x32_bf16 v[44:47], v[176:179], v[68:71], v[44:47]\n" \
  "ds_read_b128 v[92:95], v13 offset:28672\n" \
  "v_mfma_f32_16x16x32_bf16 v[52:55], a[48:51], v[68:71], v[52:55]\n" \
  "ds_read_b128 v[96:99], v13 offset:30720\n" \
  "v_mfma_f32_16x16x32_bf16 v[60:63], v[68:71], a[176:179], v[60:63]\n" \
  "v_mfma_f32_16x16x32_bf16 v[48:51], v[176:179], v[72:75], v[48:51]\n" \
  "s_add_u32 m0, s91, 61440\n" \
  "s_nop 0\n" \
  "buffer_load_dwordx4 v7, s[72:75], s87 offen lds\n" \
  "s_add_u32 s87, s87, 63616\n" \
  "s_min_u32 s87, s87, s96\n" \
  "v_mfma_f32_16x16x32_bf16 v[56:59], a[48:51], v[72:75], v[56:59]\n" \
  "v_mfma_f32_16x16x32_bf16 v[64:67], v[72:75], a[176:179], v[64:67]\n" \
  "s_waitcnt lgkmcnt(4)\n" \
  "v_mfma_f32_16x16x32_bf16 v[44:47], v[180:183], v[76:79], v[44:47]\n" \
  "ds_read_b128 v[68:71], v12 offset:32768\n" \
  "v_mfma_f32_16x16x32_bf16 v[52:55], a[52:55], v[76:79], v[52:55]\n" \
  "ds_read_b128 v[72:75], v12 offset:34816\n" \
  "v_mfma_f32_16x16x32_bf16 v[60:63], v[76:79], a[180:183], v[60:63]\n" \
  "v_mfma_f32_16x16x32_bf16 v[48:51], v[180:183], v[80:83], v[48:51]\n" \
  "v_mfma_f32_16x16x32_bf16 v[56:59], a[52:55], v[80:83], v[56:59]\n" \
  "v_mfma_f32_16x16x32_bf16 v[64:67], v[80:83], a[180:183], v[64:67]\n" \
  "s_waitcnt lgkmcnt(4)\n" \
  "v_mfma_f32_16x16x32_bf16 v[44:47], v[184:187], v[84:87], v[44:47]\n" \
  "ds_read_b128 v[76:79], v13 offset:32768\n" \
  "v_mfma_f32_16x16x32_bf16 v[52:55], a[56:59], v[84:87], v[52:55]\n" \
  "ds_read_b128 v[80:83], v13 offset:34816\n" \
  "v_mfma_f32_16x16x32_bf16 v[60:63], v[84:87], a[184:187], v[60:63]\n" \
  "v_mfma_f32_16x16x32_bf16 v[48:51], v[184:187], v[88:91], v[48:51]\n" \
  "v_mfma_f32_16x16x32_bf16 v[56:59], a[56:59], v[88:91], v[56:59]\n" \
  "v_mfma_f32_16x16x32_bf16 v[64:67], v[88:91], a[184:187], v[64:67]\n" \
  "s_waitcnt lgkmcnt(4)\n" \
  "v_mfma_f32_16x16x32_bf16 v[44:47], v[188:191], v[92:95], v[44:47]\n" \
  "ds_read_b128 v[84:87], v12 offset:36864\n" \
  "v_mfma_f32_16x16x32_bf16 v[52:55], a[60:63], v[92:95], v[52:55]\n" \
  "ds_read_b128 v[88:91], v12 offset:38912\n" \
  "v_mfma_f32_16x16x32_bf16 v[60:63], v[92:95], a[188:191], v[60:63]\n" \
  "v_mfma_f32_16x16x32_bf16 v[48:51], v[188:191], v[96:99], v[48:51]\n" \
  "v_mfma_f32_16x16x32_bf16 v[56:59], a[60:63], v[96:99], v[56:59]\n" \
  "v_mfma_f32_16x16x32_bf16 v[64:67], v[96:99], a[188:191], v[64:67]\n" \
  "s_waitcnt vmcnt(8)\n" \
  "s_barrier\n" \
  "s_waitcnt lgkmcnt(4)\n" \
  "v_mfma_f32_16x16x32_bf16 v[44:47], v[192:195], v[68:71], v[44:47]\n" \
  "ds_read_b128 v[92:95], v13 offset:36864\n" \
  "buffer_store_dword v17, v16, s[80:83], 0 offen sc1\n" \
  "v_mfma_f32_16x16x32_bf16 v[52:55], a[64:67], v[68:71], v[52:55]\n" \
  "ds_read_b128 v[96:99], v13 offset:38912\n" \
  "v_add_u32 v17, 1, v17\n" \
  "v_mfma_f32_16x16x32_bf16 v[60:63], v[68:71], a[192:195], v[60:63]\n" \
  "v_and_b32 v14, 0x7fffffff, v14\n" \
  "v_mfma_f32_16x16x32_bf16 v[48:51], v[192:195], v[72:75], v[48:51]\n" \
  "v_and_b32 v15, 0x7fffffff, v15\n" \
  "v_mfma_f32_16x16x32_bf16 v[56:59], a[64:67], v[72:75], v[56:59]\n" \
  "v_mfma_f32_16x16x32_bf16 v[64:67], v[72:75], a[192:195], v[64:67]\n" \
  "s_waitcnt lgkmcnt(4)\n" \
  "v_mfma_f32_16x16x32_bf16 v[44:47], v[196:199], v[76:79], v[44:47]\n" \
  "ds_read_b128 v[68:71], v12 offset:40960\n" \
  "v_mfma_f32_16x16x32_bf16 v[52:55], a[68:71], v[76:79], v[52:55]\n" \
  "ds_read_b128 v[72:75], v12 offset:43008\n" \
  "v_mfma_f32_16x16x32_bf16 v[60:63], v[76:79], a[196:199], v[60:63]\n" \
  "v_mfma_f32_16x16x32_bf16 v[48:51], v[196:199], v[80:83], v[48:51]\n" \
  "v_mfma_f32_16x16x32_bf16 v[56:59], a[68:71], v[80:83], v[56:59]\n" \
  "v_mfma_f32_16x16x32_bf16 v[64:67], v[80:83], a[196:199], v[64:67]\n" \
  "s_waitcnt lgkmcnt(4)\n" \
  "v_mfma_f32_16x16x32_bf16 v[44:47], v[200:203], v[84:87], v[44:47]\n" \
  "ds_read_b128 v[76:79], v13 offset:40960\n" \
  "v_mfma_f32_16x16x32_bf16 v[52:55], a[72:75], v[84:87], v[52:55]\n" \
  "ds_read_b128 v[80:83], v13 offset:43008\n" \
  "v_mfma_f32_16x16x32_bf16 v[60:63], v[84:87], a[200:203], v[60:63]\n" \
  "v_mfma_f32_16x16x32_bf16 v[48:51], v[200:203], v[88:91], v[48:51]\n" \
  "v_mfma_f32_16x16x32_bf16 v[56:59], a[72:75], v[88:91], v[56:59]\n" \
  "v_mfma_f32_16x16x32_bf16 v[64:67], v[88:91], a[200:203], v[64:67]\n" \
  "s_waitcnt lgkmcnt(4)\n" \
  "v_mfma_f32_16x16x32_bf16 v[44:47], v[204:207], v[92:95], v[44:47]\n" \
  "ds_read_b128 v[84:87], v12 offset:45056\n" \
  "v_mfma_f32_16x16x32_bf16 v[52:55], a[76:79], v[92:95], v[52:55]\n" \
  "ds_read_b128 v[88:91], v12 offset:47104\n" \
  "v_mfma_f32_16x16x32_bf16 v[60:63], v[92:95], a[204:207], v[60:63]\n" \
  "v_mfma_f32_16x16x32_bf16 v[48:51], v[204:207], v[96:99], v[48:51]\n" \
  "v_mfma_f32_16x16x32_bf16 v[56:59], a[76:79], v[96:99], v[56:59]\n" \
  "v_mfma_f32_16x16x32_bf16 v[64:67], v[96:99], a[204:207], v[64:67]\n" \
  "s_waitcnt lgkmcnt(4)\n" \
  "v_mfma_f32_16x16x32_bf16 v[44:47], v[208:211], v[68:71], v[44:47]\n" \
  "ds_read_b128 v[92:95], v13 offset:45056\n" \
  "v_mfma_f32_16x16x32_bf16 v[52:55], a[80:83], v[68:71], v[52:55]\n" \
  "ds_read_b128 v[96:99], v13 offset:47104\n" \
  "v_mfma_f32_16x16x32_bf16 v[60:63], v[68:71], a[208:211], v[60:63]\n" \
  "v_mfma_f32_16x16x32_bf16 v[48:51], v[208:211], v[72:75], v[48:51]\n" \
  "v_mfma_f32_16x16x32_bf16 v[56:59], a[80:83], v[72:75], v[56:59]\n" \
  "v_mfma_f32_16x16x32_bf16 v[64:67], v[72:75], a[208:211], v[64:67]\n" \
  "s_waitcnt lgkmcnt(4)\n" \
  "v_mfma_f32_16x16x32_bf16 v[44:47], v[212:215], v[76:79], v[44:47]\n" \
  "ds_read_b128 v[68:71], v12 offset:49152\n" \
  "v_mfma_f32_16x16x32_bf16 v[52:55], a[84:87], v[76:79], v[52:55]\n" \
  "ds_read_b128 v[72:75], v12 offset:51200\n" \
  "v_mfma_f32_16x16x32_bf16 v[60:63], v[76:79], a[212:215], v[60:63]\n" \
  "v_mfma_f32_16x16x32_bf16 v[48:51], v[212:215], v[80:83], v[48:51]\n" \
  "s_add_u32 m0, s91, 65536\n" \
  "s_nop 0\n" \
  "buffer_load_dwordx4 v7, s[72:75], s87 offen lds\n" \
  "s_add_u32 s87, s87, 128\n" \
  "v_mfma_f32_16x16x32_bf16 v[56:59], a[84:87], v[80:83], v[56:59]\n" \
  "v_mfma_f32_16x16x32_bf16 v[64:67], v[80:83], a[212:215], v[64:67]\n" \
  "s_waitcnt lgkmcnt(4)\n" \
  "v_mfma_f32_16x16x32_bf16 v[44:47], v[216:219], v[84:87], v[44:47]\n" \
  "ds_read_b128 v[76:79], v13 offset:49152\n" \
  "v_mfma_f32_16x16x32_bf16 v[52:55], a[88:91], v[84:87], v[52:55]\n" \
  "ds_read_b128 v[80:83], v13 offset:51200\n" \
  "v_mfma_f32_16x16x32_bf16 v[60:63], v[84:87], a[216:219], v[60:63]\n" \
  "v_mfma_f32_16x16x32_bf16 v[48:51], v[216:219], v[88:91], v[48:51]\n" \
  "s_add_u32 m0, s91, 69632\n" \
  "s_nop 0\n" \
  "buffer_load_dwordx4 v7, s[72:75], s87 offen lds\n" \
  "s_add_u32 s87, s87, 128\n" \
  "v_mfma_f32_16x16x32_bf16 v[56:59], a[88:91], v[88:91], v[56:59]\n" \
  "v_mfma_f32_16x16x32_bf16 v[64:67], v[88:91], a[216:219], v[64:67]\n" \
  "s_waitcnt lgkmcnt(4)\n" \
  "v_mfma_f32_16x16x32_bf16 v[44:47], v[220:223], v[92:95], v[44:47]\n" \
  "ds_read_b128 v[84:87], v12 offset:53248\n" \
  "v_mfma_f32_16x16x32_bf16 v[52:55], a[92:95], v[92:95], v[52:55]\n" \
  "ds_read_b128 v[88:91], v12 offset:55296\n" \
  "v_mfma_f32_16x16x32_bf16 v[60:63], v[92:95], a[220:223], v[60:63]\n" \
  "v_mfma_f32_16x16x32_bf16 v[48:51], v[220:223], v[96:99], v[48:51]\n" \
  "s_add_u32 m0, s91, 73728\n" \
  "s_nop 0\n" \
  "buffer_load_dwordx4 v7, s[72:75], s87 offen lds\n" \
  "s_add_u32 s87, s87, 128\n" \
  "v_mfma_f32_16x16x32_bf16 v[56:59], a[92:95], v[96:99], v[56:59]\n" \
  "v_mfma_f32_16x16x32_bf16 v[64:67], v[96:99], a[220:223], v[64:67]\n" \
  "s_waitcnt lgkmcnt(4)\n" \
  "v_mfma_f32_16x16x32_bf16 v[44:47], v[224:227], v[68:71], v[44:47]\n" \
  "ds_read_b128 v[92:95], v13 offset:53248\n" \
  "v_mfma_f32_16x16x32_bf16 v[52:55], a[96:99], v[68:71], v[52:55]\n" \
  "ds_read_b128 v[96:99], v13 offset:55296\n" \
  "v_mfma_f32_16x16x32_bf16 v[60:63], v[68:71], a[224:227], v[60:63]\n" \
  "v_mfma_f32_16x16x32_bf16 v[48:51], v[224:227], v[72:75], v[48:51]\n" \
  "s_add_u32 m0, s91, 77824\n" \
  "s_nop 0\n" \
  "buffer_load_dwordx4 v7, s[72:75], s87 offen lds\n" \
  "s_add_u32 s87, s87, 128\n" \
  "v_mfma_f32_16x16x32_bf16 v[56:59], a[96:99], v[72:75], v[56:59]\n" \
  "v_mfma_f32_16x16x32_bf16 v[64:67], v[72:75], a[224:227], v[64:67]\n" \
  "s_waitcnt lgkmcnt(4)\n" \
  "v_mfma_f32_16x16x32_bf16 v[44:47], v[228:231], v[76:79], v[44:47]\n" \
  "ds_read_b128 v[68:71], v12 offset:57344\n" \
  "v_mfma_f32_16x16x32_bf16 v[52:55], a[100:103], v[76:79], v[52:55]\n" \
  "ds_read_b128 v[72:75], v12 offset:59392\n" \
  "v_mfma_f32_16x16x32_bf16 v[60:63], v[76:79], a[228:231], v[60:63]\n" \
  "v_mfma_f32_16x16x32_bf16 v[48:51], v[228:231], v[80:83], v[48:51]\n" \
  "s_add_u32 m0, s91, 81920\n" \
  "s_nop 0\n" \
  "buffer_load_dwordx4 v7, s[72:75], s87 offen lds\n" \
  "s_add_u32 s87, s87, 128\n" \
  "v_mfma_f32_16x16x32_bf16 v[56:59], a[100:103], v[80:83], v[56:59]\n" \
  "v_mfma_f32_16x16x32_bf16 v[64:67], v[80:83], a[228:231], v[64:67]\n" \
  "s_waitcnt lgkmcnt(4)\n" \
  "v_mfma_f32_16x16x32_bf16 v[44:47], v[232:235], v[84:87], v[44:47]\n" \
  "ds_read_b128 v[76:79], v13 offset:57344\n" \
  "v_mfma_f32_16x16x32_bf16 v[52:55], a[104:107], v[84:87], v[52:55]\n" \
  "ds_read_b128 v[80:83], v13 offset:59392\n" \
  "v_mfma_f32_16x16x32_bf16 v[60:63], v[84:87], a[232:235], v[60:63]\n" \
  "v_mfma_f32_16x16x32_bf16 v[48:51], v[232:235], v[88:91], v[48:51]\n" \
  "s_add_u32 m0, s91, 86016\n" \
  "s_nop 0\n" \
  "buffer_load_dwordx4 v7, s[72:75], s87 offen lds\n" \
  "s_add_u32 s87, s87, 128\n" \
  "v_mfma_f32_16x16x32_bf16 v[56:59], a[104:107], v[88:91], v[56:59]\n" \
  "v_mfma_f32_16x16x32_bf16 v[64:67], v[88:91], a[232:235], v[64:67]\n" \
  "s_waitcnt lgkmcnt(4)\n" \
  "v_mfma_f32_16x16x32_bf16 v[44:47], v[236:239], v[92:95], v[44:47]\n" \
  "ds_read_b128 v[84:87], v12 offset:61440\n" \
  "v_mfma_f32_16x16x32_bf16 v[52:55], a[108:111], v[92:95], v[52:55]\n" \
  "ds_read_b128 v[88:91], v12 offset:63488\n" \
  "v_mfma_f32_16x16x32_bf16 v[60:63], v[92:95], a[236:239], v[60:63]\n" \
  "v_mfma_f32_16x16x32_bf16 v[48:51], v[236:239], v[96:99], v[48:51]\n" \
  "s_add_u32 m0, s91, 90112\n" \
  "s_nop 0\n" \
  "buffer_load_dwordx4 v7, s[72:75], s87 offen lds\n" \
  "s_add_u32 s87, s87, 128\n" \
  "v_mfma_f32_16x16x32_bf16 v[56:59], a[108:111], v[96:99], v[56:59]\n" \
  "v_mfma_f32_16x16x32_bf16 v[64:67], v[96:99], a[236:239], v[64:67]\n" \
  "s_waitcnt lgkmcnt(4)\n" \
  "v_mfma_f32_16x16x32_bf16 v[44:47], v[240:243], v[68:71], v[44:47]\n" \
  "ds_read_b128 v[92:95], v13 offset:61440\n" \
  "v_mfma_f32_16x16x32_bf16 v[52:55], a[112:115], v[68:71], v[52:55]\n" \
  "ds_read_b128 v[96:99], v13 offset:63488\n" \
  "v_mfma_f32_16x16x32_bf16 v[60:63], v[68:71], a[240:243], v[60:63]\n" \
  "v_mfma_f32_16x16x32_bf16 v[48:51], v[240:243], v[72:75], v[48:51]\n" \
  "s_add_u32 m0, s91, 94208\n" \
  "s_nop 0\n" \
  "buffer_load_dwordx4 v7, s[72:75], s87 offen lds\n" \
  "s_add_u32 s87, s87, 128\n" \
  "v_mfma_f32_16x16x32_bf16 v[56:59], a[112:115], v[72:75], v[56:59]\n" \
  "v_mfma_f32_16x16x32_bf16 v[64:67], v[72:75], a[240:243], v[64:67]\n" \
  "s_waitcnt lgkmcnt(4)\n" \
  "v_mfma_f32_16x16x32_bf16 v[44:47], v[244:247], v[76:79], v[44:47]\n" \
  "ds_read_b128 v[68:71], v10 offset:0\n" \
  "v_mfma_f32_16x16x32_bf16 v[52:55], a[116:119], v[76:79], v[52:55]\n" \
  "ds_read_b128 v[72:75], v10 offset:2048\n" \
  "v_mfma_f32_16x16x32_bf16 v[60:63], v[76:79], a[244:247], v[60:63]\n" \
  "v_mfma_f32_16x16x32_bf16 v[48:51], v[244:247], v[80:83], v[48:51]\n" \
  "v_mfma_f32_16x16x32_bf16 v[56:59], a[116:119], v[80:83], v[56:59]\n" \
  "v_mfma_f32_16x16x32_bf16 v[64:67], v[80:83], a[244:247], v[64:67]\n" \
  "s_waitcnt lgkmcnt(4)\n" \
  "v_mfma_f32_16x16x32_bf16 v[44:47], v[248:251], v[84:87], v[44:47]\n" \
  "ds_read_b128 v[76:79], v11 offset:0\n" \
  "v_mfma_f32_16x16x32_bf16 v[52:55], a[120:123], v[84:87], v[52:55]\n" \
  "ds_read_b128 v[80:83], v11 offset:2048\n" \
  "v_mfma_f32_16x16x32_bf16 v[60:63], v[84:87], a[248:251], v[60:63]\n" \
  "v_mfma_f32_16x16x32_bf16 v[48:51], v[248:251], v[88:91], v[48:51]\n" \
  "v_mfma_f32_16x16x32_bf16 v[56:59], a[120:123], v[88:91], v[56:59]\n" \
  "v_mfma_f32_16x16x32_bf16 v[64:67], v[88:91], a[248:251], v[64:67]\n" \
  "s_waitcnt lgkmcnt(4)\n" \
  "v_mfma_f32_16x16x32_bf16 v[44:47], v[252:255], v[92:95], v[44:47]\n" \
  "ds_read_b128 v[84:87], v10 offset:4096\n" \
  "v_mfma_f32_16x16x32_bf16 v[52:55], a[124:127], v[92:95], v[52:55]\n" \
  "ds_read_b128 v[88:91], v10 offset:6144\n" \
  "v_mfma_f32_16x16x32_bf16 v[60:63], v[92:95], a[252:255], v[60:63]\n" \
  "v_mfma_f32_16x16x32_bf16 v[48:51], v[252:255], v[96:99], v[48:51]\n" \
  "v_mfma_f32_16x16x32_bf16 v[56:59], a[124:127], v[96:99], v[56:59]\n" \
  "v_mfma_f32_16x16x32_bf16 v[64:67], v[96:99], a[252:255], v[64:67]\n" \
  "s_branch LG_%=\n" \
  "LX0_%=:\n" \
  "s_sub_u32 s89, s85, 1\n" \
  "s_and_b32 s89, s89, 63\n" \
  "s_lshr_b32 s84, s89, 2\n" \
  "s_xnor_b32 s84, s84, %9\n" \
  "s_and_b32 s84, s84, 1\n" \
  "s_lshl_b32 s84, s84, 13\n" \
  "s_lshl_b32 s88, s89, 19\n" \
  "s_or_b32 s88, s88, s84\n" \
  "s_add_u32 s89, s88, " STR(QKV_SZ32) "\n" \
  "s_add_u32 s90, s89, " STR(QKV_SZ32) "\n" \
  "s_add_u32 s85, s85, 1\n" \
  "v_cvt_pk_bf16_f32 v116, v44, v45\n" \
  "v_cvt_pk_bf16_f32 v117, v46, v47\n" \
  "v_cvt_pk_bf16_f32 v118, v48, v49\n" \
  "v_cvt_pk_bf16_f32 v119, v50, v51\n" \
  "v_cvt_pk_bf16_f32 v120, v52, v53\n" \
  "v_cvt_pk_bf16_f32 v121, v54, v55\n" \
  "v_cvt_pk_bf16_f32 v122, v56, v57\n" \
  "v_cvt_pk_bf16_f32 v123, v58, v59\n" \
  "v_cvt_pk_bf16_f32 v124, v60, v61\n" \
  "v_cvt_pk_bf16_f32 v125, v62, v63\n" \
  "v_cvt_pk_bf16_f32 v126, v64, v65\n" \
  "v_cvt_pk_bf16_f32 v127, v66, v67\n" \
  "s_nop 1\n" \
  "v_permlane16_swap_b32 v116, v118\n" \
  "v_permlane16_swap_b32 v117, v119\n" \
  "v_permlane16_swap_b32 v120, v122\n" \
  "v_permlane16_swap_b32 v121, v123\n" \
  "buffer_store_dwordx4 v[116:119], v14, s[76:79], s88 offen sc1\n" \
  "buffer_store_dwordx4 v[120:123], v14, s[76:79], s89 offen sc1\n" \
  "buffer_store_dwordx4 v[124:127], v15, s[76:79], s90 offen sc1\n" \
  "s_branch LZ_%=\n" \
  "LX1_%=:\n" \
  "s_sub_u32 s89, s85, 1\n" \
  "s_and_b32 s89, s89, 63\n" \
  "s_lshr_b32 s84, s89, 2\n" \
  "s_xnor_b32 s84, s84, %9\n" \
  "s_and_b32 s84, s84, 1\n" \
  "s_lshl_b32 s84, s84, 13\n" \
  "s_lshl_b32 s88, s89, 19\n" \
  "s_or_b32 s88, s88, s84\n" \
  "s_add_u32 s89, s88, " STR(QKV_SZ32) "\n" \
  "s_add_u32 s90, s89, " STR(QKV_SZ32) "\n" \
  "s_add_u32 s85, s85, 1\n" \
  "v_cvt_pk_bf16_f32 v116, v20, v21\n" \
  "v_cvt_pk_bf16_f32 v117, v22, v23\n" \
  "v_cvt_pk_bf16_f32 v118, v24, v25\n" \
  "v_cvt_pk_bf16_f32 v119, v26, v27\n" \
  "v_cvt_pk_bf16_f32 v120, v28, v29\n" \
  "v_cvt_pk_bf16_f32 v121, v30, v31\n" \
  "v_cvt_pk_bf16_f32 v122, v32, v33\n" \
  "v_cvt_pk_bf16_f32 v123, v34, v35\n" \
  "v_cvt_pk_bf16_f32 v124, v36, v37\n" \
  "v_cvt_pk_bf16_f32 v125, v38, v39\n" \
  "v_cvt_pk_bf16_f32 v126, v40, v41\n" \
  "v_cvt_pk_bf16_f32 v127, v42, v43\n" \
  "s_nop 1\n" \
  "v_permlane16_swap_b32 v116, v118\n" \
  "v_permlane16_swap_b32 v117, v119\n" \
  "v_permlane16_swap_b32 v120, v122\n" \
  "v_permlane16_swap_b32 v121, v123\n" \
  "buffer_store_dwordx4 v[116:119], v14, s[76:79], s88 offen sc1\n" \
  "buffer_store_dwordx4 v[120:123], v14, s[76:79], s89 offen sc1\n" \
  "buffer_store_dwordx4 v[124:127], v15, s[76:79], s90 offen sc1\n" \
  "s_branch LZ_%=\n" \
  "LZ_%=:\n" \
  "s_waitcnt vmcnt(0) lgkmcnt(0)\n" \
  "s_barrier\n" \
  "buffer_store_dword v17, v16, s[80:83], 0 offen sc1\n" \
  "s_waitcnt vmcnt(0)\n" \
  ""

#define XO_ASM \
  "s_mov_b64 s[68:69], %0\n" \
  "s_and_b32 s69, s69, 0xffff\n" \
  "s_mov_b32 s70, 8388608\n" \
  "s_mov_b32 s71, 0x00020000\n" \
  "s_mov_b64 s[72:73], %1\n" \
  "s_and_b32 s73, s73, 0xffff\n" \
  "s_mov_b32 s74, " STR(QKV_SZ32) "\n" \
  "s_mov_b32 s75, 0x00020000\n" \
  "s_mov_b64 s[76:77], %2\n" \
  "s_and_b32 s77, s77, 0xffff\n" \
  "s_mov_b32 s78, 8388608\n" \
  "s_mov_b32 s79, 0x00020000\n" \
  "s_mov_b64 s[80:81], %3\n" \
  "s_and_b32 s81, s81, 0xffff\n" \
  "s_mov_b32 s82, " STR(CTRL_BYTES) "\n" \
  "s_mov_b32 s83, 0x00020000\n" \
  "v_mbcnt_lo_u32_b32 v4, -1, 0\n" \
  "v_mbcnt_hi_u32_b32 v4, -1, v4\n" \
  "v_and_b32 v20, 15, v4\n" \
  "v_lshrrev_b32 v21, 4, v4\n" \
  "v_lshlrev_b32 v6, 4, v4\n" \
  "s_lshl_b32 s86, %5, 15\n" \
  "s_add_u32 s86, s86, %6\n" \
  "s_lshl_b32 s90, %5, 21\n" \
  "v_lshrrev_b32 v5, 3, v4\n" \
  "v_and_b32 v15, 7, v4\n" \
  "v_lshlrev_b32 v7, 11, v5\n" \
  "v_lshl_add_u32 v7, v15, 4, v7\n" \
  "v_lshlrev_b32 v5, 5, v21\n" \
  "v_xor_b32 v7, v7, v5\n" \
  "s_add_u32 m0, s86, 0\n" \
  "s_nop 0\n" \
  "buffer_load_dwordx4 v7, s[68:71], s90 offen lds\n" \
  "s_add_u32 s90, s90, 16384\n" \
  "s_add_u32 m0, s86, 1024\n" \
  "s_nop 0\n" \
  "buffer_load_dwordx4 v7, s[68:71], s90 offen lds\n" \
  "s_add_u32 s90, s90, 16384\n" \
  "s_add_u32 m0, s86, 2048\n" \
  "s_nop 0\n" \
  "buffer_load_dwordx4 v7, s[68:71], s90 offen lds\n" \
  "s_add_u32 s90, s90, 16384\n" \
  "s_add_u32 m0, s86, 3072\n" \
  "s_nop 0\n" \
  "buffer_load_dwordx4 v7, s[68:71], s90 offen lds\n" \
  "s_add_u32 s90, s90, 16384\n" \
  "s_add_u32 m0, s86, 4096\n" \
  "s_nop 0\n" \
  "buffer_load_dwordx4 v7, s[68:71], s90 offen lds\n" \
  "s_add_u32 s90, s90, 16384\n" \
  "s_add_u32 m0, s86, 5120\n" \
  "s_nop 0\n" \
  "buffer_load_dwordx4 v7, s[68:71], s90 offen lds\n" \
  "s_add_u32 s90, s90, 16384\n" \
  "s_add_u32 m0, s86, 6144\n" \
  "s_nop 0\n" \
  "buffer_load_dwordx4 v7, s[68:71], s90 offen lds\n" \
  "s_add_u32 s90, s90, 16384\n" \
  "s_add_u32 m0, s86, 7168\n" \
  "s_nop 0\n" \
  "buffer_load_dwordx4 v7, s[68:71], s90 offen lds\n" \
  "s_add_u32 s90, s90, 16384\n" \
  "s_add_u32 m0, s86, 8192\n" \
  "s_nop 0\n" \
  "buffer_load_dwordx4 v7, s[68:71], s90 offen lds\n" \
  "s_add_u32 s90, s90, 16384\n" \
  "s_add_u32 m0, s86, 9216\n" \
  "s_nop 0\n" \
  "buffer_load_dwordx4 v7, s[68:71], s90 offen lds\n" \
  "s_add_u32 s90, s90, 16384\n" \
  "s_add_u32 m0, s86, 10240\n" \
  "s_nop 0\n" \
  "buffer_load_dwordx4 v7, s[68:71], s90 offen lds\n" \
  "s_add_u32 s90, s90, 16384\n" \
  "s_add_u32 m0, s86, 11264\n" \
  "s_nop 0\n" \
  "buffer_load_dwordx4 v7, s[68:71], s90 offen lds\n" \
  "s_add_u32 s90, s90, 16384\n" \
  "s_add_u32 m0, s86, 12288\n" \
  "s_nop 0\n" \
  "buffer_load_dwordx4 v7, s[68:71], s90 offen lds\n" \
  "s_add_u32 s90, s90, 16384\n" \
  "s_add_u32 m0, s86, 13312\n" \
  "s_nop 0\n" \
  "buffer_load_dwordx4 v7, s[68:71], s90 offen lds\n" \
  "s_add_u32 s90, s90, 16384\n" \
  "s_add_u32 m0, s86, 14336\n" \
  "s_nop 0\n" \
  "buffer_load_dwordx4 v7, s[68:71], s90 offen lds\n" \
  "s_add_u32 s90, s90, 16384\n" \
  "s_add_u32 m0, s86, 15360\n" \
  "s_nop 0\n" \
  "buffer_load_dwordx4 v7, s[68:71], s90 offen lds\n" \
  "s_add_u32 s90, s90, 16384\n" \
  "s_add_u32 m0, s86, 16384\n" \
  "s_nop 0\n" \
  "buffer_load_dwordx4 v7, s[68:71], s90 offen lds\n" \
  "s_add_u32 s90, s90, 16384\n" \
  "s_add_u32 m0, s86, 17408\n" \
  "s_nop 0\n" \
  "buffer_load_dwordx4 v7, s[68:71], s90 offen lds\n" \
  "s_add_u32 s90, s90, 16384\n" \
  "s_add_u32 m0, s86, 18432\n" \
  "s_nop 0\n" \
  "buffer_load_dwordx4 v7, s[68:71], s90 offen lds\n" \
  "s_add_u32 s90, s90, 16384\n" \
  "s_add_u32 m0, s86, 19456\n" \
  "s_nop 0\n" \
  "buffer_load_dwordx4 v7, s[68:71], s90 offen lds\n" \
  "s_add_u32 s90, s90, 16384\n" \
  "s_add_u32 m0, s86, 20480\n" \
  "s_nop 0\n" \
  "buffer_load_dwordx4 v7, s[68:71], s90 offen lds\n" \
  "s_add_u32 s90, s90, 16384\n" \
  "s_add_u32 m0, s86, 21504\n" \
  "s_nop 0\n" \
  "buffer_load_dwordx4 v7, s[68:71], s90 offen lds\n" \
  "s_add_u32 s90, s90, 16384\n" \
  "s_add_u32 m0, s86, 22528\n" \
  "s_nop 0\n" \
  "buffer_load_dwordx4 v7, s[68:71], s90 offen lds\n" \
  "s_add_u32 s90, s90, 16384\n" \
  "s_add_u32 m0, s86, 23552\n" \
  "s_nop 0\n" \
  "buffer_load_dwordx4 v7, s[68:71], s90 offen lds\n" \
  "s_add_u32 s90, s90, 16384\n" \
  "s_add_u32 m0, s86, 24576\n" \
  "s_nop 0\n" \
  "buffer_load_dwordx4 v7, s[68:71], s90 offen lds\n" \
  "s_add_u32 s90, s90, 16384\n" \
  "s_add_u32 m0, s86, 25600\n" \
  "s_nop 0\n" \
  "buffer_load_dwordx4 v7, s[68:71], s90 offen lds\n" \
  "s_add_u32 s90, s90, 16384\n" \
  "s_add_u32 m0, s86, 26624\n" \
  "s_nop 0\n" \
  "buffer_load_dwordx4 v7, s[68:71], s90 offen lds\n" \
  "s_add_u32 s90, s90, 16384\n" \
  "s_add_u32 m0, s86, 27648\n" \
  "s_nop 0\n" \
  "buffer_load_dwordx4 v7, s[68:71], s90 offen lds\n" \
  "s_add_u32 s90, s90, 16384\n" \
  "s_add_u32 m0, s86, 28672\n" \
  "s_nop 0\n" \
  "buffer_load_dwordx4 v7, s[68:71], s90 offen lds\n" \
  "s_add_u32 s90, s90, 16384\n" \
  "s_add_u32 m0, s86, 29696\n" \
  "s_nop 0\n" \
  "buffer_load_dwordx4 v7, s[68:71], s90 offen lds\n" \
  "s_add_u32 s90, s90, 16384\n" \
  "s_add_u32 m0, s86, 30720\n" \
  "s_nop 0\n" \
  "buffer_load_dwordx4 v7, s[68:71], s90 offen lds\n" \
  "s_add_u32 s90, s90, 16384\n" \
  "s_add_u32 m0, s86, 31744\n" \
  "s_nop 0\n" \
  "buffer_load_dwordx4 v7, s[68:71], s90 offen lds\n" \
  "s_add_u32 s90, s90, 16384\n" \
  "v_lshrrev_b32 v5, 2, v20\n" \
  "v_lshl_add_u32 v5, v21, 2, v5\n" \
  "v_lshlrev_b32 v15, 7, v5\n" \
  "v_lshrrev_b32 v5, 1, v5\n" \
  "v_and_b32 v5, 3, v5\n" \
  "v_lshl_add_u32 v15, v5, 5, v15\n" \
  "v_and_b32 v5, 3, v4\n" \
  "v_lshl_add_u32 v15, v5, 3, v15\n" \
  "s_add_u32 s84, %5, 0\n" \
  "s_and_b32 s84, s84, 3\n" \
  "s_lshl_b32 s85, s84, 5\n" \
  "v_xor_b32 v5, s85, v15\n" \
  "v_add_u32 v16, s86, v5\n" \
  "s_mul_i32 s84, s84, 3072\n" \
  "s_add_u32 s84, s84, 131072\n" \
  "s_add_u32 s84, s84, %6\n" \
  "v_add_u32 v11, s84, v6\n" \
  "s_add_u32 s84, %5, 1\n" \
  "s_and_b32 s84, s84, 3\n" \
  "s_lshl_b32 s85, s84, 5\n" \
  "v_xor_b32 v5, s85, v15\n" \
  "v_add_u32 v17, s86, v5\n" \
  "s_mul_i32 s84, s84, 3072\n" \
  "s_add_u32 s84, s84, 131072\n" \
  "s_add_u32 s84, s84, %6\n" \
  "v_add_u32 v12, s84, v6\n" \
  "s_add_u32 s84, %5, 2\n" \
  "s_and_b32 s84, s84, 3\n" \
  "s_lshl_b32 s85, s84, 5\n" \
  "v_xor_b32 v5, s85, v15\n" \
  "v_add_u32 v18, s86, v5\n" \
  "s_mul_i32 s84, s84, 3072\n" \
  "s_add_u32 s84, s84, 131072\n" \
  "s_add_u32 s84, s84, %6\n" \
  "v_add_u32 v13, s84, v6\n" \
  "s_add_u32 s84, %5, 3\n" \
  "s_and_b32 s84, s84, 3\n" \
  "s_lshl_b32 s85, s84, 5\n" \
  "v_xor_b32 v5, s85, v15\n" \
  "v_add_u32 v19, s86, v5\n" \
  "s_mul_i32 s84, s84, 3072\n" \
  "s_add_u32 s84, s84, 131072\n" \
  "s_add_u32 s84, s84, %6\n" \
  "v_add_u32 v14, s84, v6\n" \
  "s_and_b32 s84, %4, 1\n" \
  "s_lshl_b32 s96, s84, 18\n" \
  "s_lshl_b32 s85, %5, 16\n" \
  "s_add_u32 s96, s96, s85\n" \
  "s_lshl_b32 s88, s84, 16\n" \
  "s_lshr_b32 s84, %4, 1\n" \
  "s_lshl_b32 s84, s84, 7\n" \
  "s_add_u32 s88, s88, s84\n" \
  "s_lshl_b32 s84, %5, 5\n" \
  "s_add_u32 s88, s88, s84\n" \
  "v_lshlrev_b32 v10, 12, v20\n" \
  "v_lshl_add_u32 v10, v21, 3, v10\n" \
  "v_and_b32 v8, 7, v4\n" \
  "v_lshlrev_b32 v8, 7, v8\n" \
  "s_lshl_b32 s84, %5, 10\n" \
  "s_add_u32 s84, s84, " STR(CW_XPROG) "*4\n" \
  "v_add_u32 v8, s84, v8\n" \
  "s_mov_b32 s91, 0\n" \
  "s_mov_b32 s89, 0\n" \
  "s_lshr_b32 s84, s89, 2\n" \
  "s_xnor_b32 s84, s84, %7\n" \
  "s_and_b32 s84, s84, 1\n" \
  "s_lshl_b32 s84, s84, 13\n" \
  "s_lshl_b32 s87, s89, 19\n" \
  "s_or_b32 s87, s87, s84\n" \
  "s_add_u32 s87, s87, s96\n" \
  "s_waitcnt vmcnt(16)\n" \
  "ds_read_b64_tr_b16 v[128:129], v16 offset:0\n" \
  "ds_read_b64_tr_b16 v[130:131], v16 offset:2048\n" \
  "ds_read_b64_tr_b16 v[132:133], v17 offset:0\n" \
  "ds_read_b64_tr_b16 v[134:135], v17 offset:2048\n" \
  "ds_read_b64_tr_b16 v[136:137], v18 offset:0\n" \
  "ds_read_b64_tr_b16 v[138:139], v18 offset:2048\n" \
  "ds_read_b64_tr_b16 v[140:141], v19 offset:0\n" \
  "ds_read_b64_tr_b16 v[142:143], v19 offset:2048\n" \
  "ds_read_b64_tr_b16 v[144:145], v16 offset:4096\n" \
  "ds_read_b64_tr_b16 v[146:147], v16 offset:6144\n" \
  "ds_read_b64_tr_b16 v[148:149], v17 offset:4096\n" \
  "ds_read_b64_tr_b16 v[150:151], v17 offset:6144\n" \
  "ds_read_b64_tr_b16 v[152:153], v18 offset:4096\n" \
  "ds_read_b64_tr_b16 v[154:155], v18 offset:6144\n" \
  "ds_read_b64_tr_b16 v[156:157], v19 offset:4096\n" \
  "ds_read_b64_tr_b16 v[158:159], v19 offset:6144\n" \
  "ds_read_b64_tr_b16 v[160:161], v16 offset:8192\n" \
  "ds_read_b64_tr_b16 v[162:163], v16 offset:10240\n" \
  "ds_read_b64_tr_b16 v[164:165], v17 offset:8192\n" \
  "ds_read_b64_tr_b16 v[166:167], v17 offset:10240\n" \
  "ds_read_b64_tr_b16 v[168:169], v18 offset:8192\n" \
  "ds_read_b64_tr_b16 v[170:171], v18 offset:10240\n" \
  "ds_read_b64_tr_b16 v[172:173], v19 offset:8192\n" \
  "ds_read_b64_tr_b16 v[174:175], v19 offset:10240\n" \
  "ds_read_b64_tr_b16 v[176:177], v16 offset:12288\n" \
  "ds_read_b64_tr_b16 v[178:179], v16 offset:14336\n" \
  "ds_read_b64_tr_b16 v[180:181], v17 offset:12288\n" \
  "ds_read_b64_tr_b16 v[182:183], v17 offset:14336\n" \
  "ds_read_b64_tr_b16 v[184:185], v18 offset:12288\n" \
  "ds_read_b64_tr_b16 v[186:187], v18 offset:14336\n" \
  "ds_read_b64_tr_b16 v[188:189], v19 offset:12288\n" \
  "ds_read_b64_tr_b16 v[190:191], v19 offset:14336\n" \
  "s_waitcnt lgkmcnt(0)\n" \
  "s_add_u32 m0, s86, 0\n" \
  "s_nop 0\n" \
  "buffer_load_dwordx4 v7, s[68:71], s90 offen lds\n" \
  "s_add_u32 s90, s90, 16384\n" \
  "s_add_u32 m0, s86, 1024\n" \
  "s_nop 0\n" \
  "buffer_load_dwordx4 v7, s[68:71], s90 offen lds\n" \
  "s_add_u32 s90, s90, 16384\n" \
  "s_add_u32 m0, s86, 2048\n" \
  "s_nop 0\n" \
  "buffer_load_dwordx4 v7, s[68:71], s90 offen lds\n" \
  "s_add_u32 s90, s90, 16384\n" \
  "s_add_u32 m0, s86, 3072\n" \
  "s_nop 0\n" \
  "buffer_load_dwordx4 v7, s[68:71], s90 offen lds\n" \
  "s_add_u32 s90, s90, 16384\n" \
  "s_add_u32 m0, s86, 4096\n" \
  "s_nop 0\n" \
  "buffer_load_dwordx4 v7, s[68:71], s90 offen lds\n" \
  "s_add_u32 s90, s90, 16384\n" \
  "s_add_u32 m0, s86, 5120\n" \
  "s_nop 0\n" \
  "buffer_load_dwordx4 v7, s[68:71], s90 offen lds\n" \
  "s_add_u32 s90, s90, 16384\n" \
  "s_add_u32 m0, s86, 6144\n" \
  "s_nop 0\n" \
  "buffer_load_dwordx4 v7, s[68:71], s90 offen lds\n" \
  "s_add_u32 s90, s90, 16384\n" \
  "s_add_u32 m0, s86, 7168\n" \
  "s_nop 0\n" \
  "buffer_load_dwordx4 v7, s[68:71], s90 offen lds\n" \
  "s_add_u32 s90, s90, 16384\n" \
  "s_add_u32 m0, s86, 8192\n" \
  "s_nop 0\n" \
  "buffer_load_dwordx4 v7, s[68:71], s90 offen lds\n" \
  "s_add_u32 s90, s90, 16384\n" \
  "s_add_u32 m0, s86, 9216\n" \
  "s_nop 0\n" \
  "buffer_load_dwordx4 v7, s[68:71], s90 offen lds\n" \
  "s_add_u32 s90, s90, 16384\n" \
  "s_add_u32 m0, s86, 10240\n" \
  "s_nop 0\n" \
  "buffer_load_dwordx4 v7, s[68:71], s90 offen lds\n" \
  "s_add_u32 s90, s90, 16384\n" \
  "s_add_u32 m0, s86, 11264\n" \
  "s_nop 0\n" \
  "buffer_load_dwordx4 v7, s[68:71], s90 offen lds\n" \
  "s_add_u32 s90, s90, 16384\n" \
  "s_add_u32 m0, s86, 12288\n" \
  "s_nop 0\n" \
  "buffer_load_dwordx4 v7, s[68:71], s90 offen lds\n" \
  "s_add_u32 s90, s90, 16384\n" \
  "s_add_u32 m0, s86, 13312\n" \
  "s_nop 0\n" \
  "buffer_load_dwordx4 v7, s[68:71], s90 offen lds\n" \
  "s_add_u32 s90, s90, 16384\n" \
  "s_add_u32 m0, s86, 14336\n" \
  "s_nop 0\n" \
  "buffer_load_dwordx4 v7, s[68:71], s90 offen lds\n" \
  "s_add_u32 s90, s90, 16384\n" \
  "s_add_u32 m0, s86, 15360\n" \
  "s_nop 0\n" \
  "buffer_load_dwordx4 v7, s[68:71], s90 offen lds\n" \
  "s_add_u32 s90, s90, 16384\n" \
  "s_waitcnt vmcnt(16)\n" \
  "ds_read_b64_tr_b16 v[192:193], v16 offset:16384\n" \
  "ds_read_b64_tr_b16 v[194:195], v16 offset:18432\n" \
  "ds_read_b64_tr_b16 v[196:197], v17 offset:16384\n" \
  "ds_read_b64_tr_b16 v[198:199], v17 offset:18432\n" \
  "ds_read_b64_tr_b16 v[200:201], v18 offset:16384\n" \
  "ds_read_b64_tr_b16 v[202:203], v18 offset:18432\n" \
  "ds_read_b64_tr_b16 v[204:205], v19 offset:16384\n" \
  "ds_read_b64_tr_b16 v[206:207], v19 offset:18432\n" \
  "ds_read_b64_tr_b16 v[208:209], v16 offset:20480\n" \
  "ds_read_b64_tr_b16 v[210:211], v16 offset:22528\n" \
  "ds_read_b64_tr_b16 v[212:213], v17 offset:20480\n" \
  "ds_read_b64_tr_b16 v[214:215], v17 offset:22528\n" \
  "ds_read_b64_tr_b16 v[216:217], v18 offset:20480\n" \
  "ds_read_b64_tr_b16 v[218:219], v18 offset:22528\n" \
  "ds_read_b64_tr_b16 v[220:221], v19 offset:20480\n" \
  "ds_read_b64_tr_b16 v[222:223], v19 offset:22528\n" \
  "ds_read_b64_tr_b16 v[224:225], v16 offset:24576\n" \
  "ds_read_b64_tr_b16 v[226:227], v16 offset:26624\n" \
  "ds_read_b64_tr_b16 v[228:229], v17 offset:24576\n" \
  "ds_read_b64_tr_b16 v[230:231], v17 offset:26624\n" \
  "ds_read_b64_tr_b16 v[232:233], v18 offset:24576\n" \
  "ds_read_b64_tr_b16 v[234:235], v18 offset:26624\n" \
  "ds_read_b64_tr_b16 v[236:237], v19 offset:24576\n" \
  "ds_read_b64_tr_b16 v[238:239], v19 offset:26624\n" \
  "ds_read_b64_tr_b16 v[240:241], v16 offset:28672\n" \
  "ds_read_b64_tr_b16 v[242:243], v16 offset:30720\n" \
  "ds_read_b64_tr_b16 v[244:245], v17 offset:28672\n" \
  "ds_read_b64_tr_b16 v[246:247], v17 offset:30720\n" \
  "ds_read_b64_tr_b16 v[248:249], v18 offset:28672\n" \
  "ds_read_b64_tr_b16 v[250:251], v18 offset:30720\n" \
  "ds_read_b64_tr_b16 v[252:253], v19 offset:28672\n" \
  "ds_read_b64_tr_b16 v[254:255], v19 offset:30720\n" \
  "s_waitcnt lgkmcnt(0)\n" \
  "s_add_u32 m0, s86, 16384\n" \
  "s_nop 0\n" \
  "buffer_load_dwordx4 v7, s[68:71], s90 offen lds\n" \
  "s_add_u32 s90, s90, 16384\n" \
  "s_add_u32 m0, s86, 17408\n" \
  "s_nop 0\n" \
  "buffer_load_dwordx4 v7, s[68:71], s90 offen lds\n" \
  "s_add_u32 s90, s90, 16384\n" \
  "s_add_u32 m0, s86, 18432\n" \
  "s_nop 0\n" \
  "buffer_load_dwordx4 v7, s[68:71], s90 offen lds\n" \
  "s_add_u32 s90, s90, 16384\n" \
  "s_add_u32 m0, s86, 19456\n" \
  "s_nop 0\n" \
  "buffer_load_dwordx4 v7, s[68:71], s90 offen lds\n" \
  "s_add_u32 s90, s90, 16384\n" \
  "s_add_u32 m0, s86, 20480\n" \
  "s_nop 0\n" \
  "buffer_load_dwordx4 v7, s[68:71], s90 offen lds\n" \
  "s_add_u32 s90, s90, 16384\n" \
  "s_add_u32 m0, s86, 21504\n" \
  "s_nop 0\n" \
  "buffer_load_dwordx4 v7, s[68:71], s90 offen lds\n" \
  "s_add_u32 s90, s90, 16384\n" \
  "s_add_u32 m0, s86, 22528\n" \
  "s_nop 0\n" \
  "buffer_load_dwordx4 v7, s[68:71], s90 offen lds\n" \
  "s_add_u32 s90, s90, 16384\n" \
  "s_add_u32 m0, s86, 23552\n" \
  "s_nop 0\n" \
  "buffer_load_dwordx4 v7, s[68:71], s90 offen lds\n" \
  "s_add_u32 s90, s90, 16384\n" \
  "s_add_u32 m0, s86, 24576\n" \
  "s_nop 0\n" \
  "buffer_load_dwordx4 v7, s[68:71], s90 offen lds\n" \
  "s_add_u32 s90, s90, 16384\n" \
  "s_add_u32 m0, s86, 25600\n" \
  "s_nop 0\n" \
  "buffer_load_dwordx4 v7, s[68:71], s90 offen lds\n" \
  "s_add_u32 s90, s90, 16384\n" \
  "s_add_u32 m0, s86, 26624\n" \
  "s_nop 0\n" \
  "buffer_load_dwordx4 v7, s[68:71], s90 offen lds\n" \
  "s_add_u32 s90, s90, 16384\n" \
  "s_add_u32 m0, s86, 27648\n" \
  "s_nop 0\n" \
  "buffer_load_dwordx4 v7, s[68:71], s90 offen lds\n" \
  "s_add_u32 s90, s90, 16384\n" \
  "s_add_u32 m0, s86, 28672\n" \
  "s_nop 0\n" \
  "buffer_load_dwordx4 v7, s[68:71], s90 offen lds\n" \
  "s_add_u32 s90, s90, 16384\n" \
  "s_add_u32 m0, s86, 29696\n" \
  "s_nop 0\n" \
  "buffer_load_dwordx4 v7, s[68:71], s90 offen lds\n" \
  "s_add_u32 s90, s90, 16384\n" \
  "s_add_u32 m0, s86, 30720\n" \
  "s_nop 0\n" \
  "buffer_load_dwordx4 v7, s[68:71], s90 offen lds\n" \
  "s_add_u32 s90, s90, 16384\n" \
  "s_add_u32 m0, s86, 31744\n" \
  "s_nop 0\n" \
  "buffer_load_dwordx4 v7, s[68:71], s90 offen lds\n" \
  "s_add_u32 s90, s90, 16384\n" \
  "s_waitcnt vmcnt(16)\n" \
  "ds_read_b64_tr_b16 a[0:1], v16 offset:0\n" \
  "ds_read_b64_tr_b16 a[2:3], v16 offset:2048\n" \
  "ds_read_b64_tr_b16 a[4:5], v17 offset:0\n" \
  "ds_read_b64_tr_b16 a[6:7], v17 offset:2048\n" \
  "ds_read_b64_tr_b16 a[8:9], v18 offset:0\n" \
  "ds_read_b64_tr_b16 a[10:11], v18 offset:2048\n" \
  "ds_read_b64_tr_b16 a[12:13], v19 offset:0\n" \
  "ds_read_b64_tr_b16 a[14:15], v19 offset:2048\n" \
  "ds_read_b64_tr_b16 a[16:17], v16 offset:4096\n" \
  "ds_read_b64_tr_b16 a[18:19], v16 offset:6144\n" \
  "ds_read_b64_tr_b16 a[20:21], v17 offset:4096\n" \
  "ds_read_b64_tr_b16 a[22:23], v17 offset:6144\n" \
  "ds_read_b64_tr_b16 a[24:25], v18 offset:4096\n" \
  "ds_read_b64_tr_b16 a[26:27], v18 offset:6144\n" \
  "ds_read_b64_tr_b16 a[28:29], v19 offset:4096\n" \
  "ds_read_b64_tr_b16 a[30:31], v19 offset:6144\n" \
  "ds_read_b64_tr_b16 a[32:33], v16 offset:8192\n" \
  "ds_read_b64_tr_b16 a[34:35], v16 offset:10240\n" \
  "ds_read_b64_tr_b16 a[36:37], v17 offset:8192\n" \
  "ds_read_b64_tr_b16 a[38:39], v17 offset:10240\n" \
  "ds_read_b64_tr_b16 a[40:41], v18 offset:8192\n" \
  "ds_read_b64_tr_b16 a[42:43], v18 offset:10240\n" \
  "ds_read_b64_tr_b16 a[44:45], v19 offset:8192\n" \
  "ds_read_b64_tr_b16 a[46:47], v19 offset:10240\n" \
  "ds_read_b64_tr_b16 a[48:49], v16 offset:12288\n" \
  "ds_read_b64_tr_b16 a[50:51], v16 offset:14336\n" \
  "ds_read_b64_tr_b16 a[52:53], v17 offset:12288\n" \
  "ds_read_b64_tr_b16 a[54:55], v17 offset:14336\n" \
  "ds_read_b64_tr_b16 a[56:57], v18 offset:12288\n" \
  "ds_read_b64_tr_b16 a[58:59], v18 offset:14336\n" \
  "ds_read_b64_tr_b16 a[60:61], v19 offset:12288\n" \
  "ds_read_b64_tr_b16 a[62:63], v19 offset:14336\n" \
  "s_waitcnt lgkmcnt(0)\n" \
  "s_add_u32 m0, s86, 0\n" \
  "s_nop 0\n" \
  "buffer_load_dwordx4 v7, s[68:71], s90 offen lds\n" \
  "s_add_u32 s90, s90, 16384\n" \
  "s_add_u32 m0, s86, 1024\n" \
  "s_nop 0\n" \
  "buffer_load_dwordx4 v7, s[68:71], s90 offen lds\n" \
  "s_add_u32 s90, s90, 16384\n" \
  "s_add_u32 m0, s86, 2048\n" \
  "s_nop 0\n" \
  "buffer_load_dwordx4 v7, s[68:71], s90 offen lds\n" \
  "s_add_u32 s90, s90, 16384\n" \
  "s_add_u32 m0, s86, 3072\n" \
  "s_nop 0\n" \
  "buffer_load_dwordx4 v7, s[68:71], s90 offen lds\n" \
  "s_add_u32 s90, s90, 16384\n" \
  "s_add_u32 m0, s86, 4096\n" \
  "s_nop 0\n" \
  "buffer_load_dwordx4 v7, s[68:71], s90 offen lds\n" \
  "s_add_u32 s90, s90, 16384\n" \
  "s_add_u32 m0, s86, 5120\n" \
  "s_nop 0\n" \
  "buffer_load_dwordx4 v7, s[68:71], s90 offen lds\n" \
  "s_add_u32 s90, s90, 16384\n" \
  "s_add_u32 m0, s86, 6144\n" \
  "s_nop 0\n" \
  "buffer_load_dwordx4 v7, s[68:71], s90 offen lds\n" \
  "s_add_u32 s90, s90, 16384\n" \
  "s_add_u32 m0, s86, 7168\n" \
  "s_nop 0\n" \
  "buffer_load_dwordx4 v7, s[68:71], s90 offen lds\n" \
  "s_add_u32 s90, s90, 16384\n" \
  "s_add_u32 m0, s86, 8192\n" \
  "s_nop 0\n" \
  "buffer_load_dwordx4 v7, s[68:71], s90 offen lds\n" \
  "s_add_u32 s90, s90, 16384\n" \
  "s_add_u32 m0, s86, 9216\n" \
  "s_nop 0\n" \
  "buffer_load_dwordx4 v7, s[68:71], s90 offen lds\n" \
  "s_add_u32 s90, s90, 16384\n" \
  "s_add_u32 m0, s86, 10240\n" \
  "s_nop 0\n" \
  "buffer_load_dwordx4 v7, s[68:71], s90 offen lds\n" \
  "s_add_u32 s90, s90, 16384\n" \
  "s_add_u32 m0, s86, 11264\n" \
  "s_nop 0\n" \
  "buffer_load_dwordx4 v7, s[68:71], s90 offen lds\n" \
  "s_add_u32 s90, s90, 16384\n" \
  "s_add_u32 m0, s86, 12288\n" \
  "s_nop 0\n" \
  "buffer_load_dwordx4 v7, s[68:71], s90 offen lds\n" \
  "s_add_u32 s90, s90, 16384\n" \
  "s_add_u32 m0, s86, 13312\n" \
  "s_nop 0\n" \
  "buffer_load_dwordx4 v7, s[68:71], s90 offen lds\n" \
  "s_add_u32 s90, s90, 16384\n" \
  "s_add_u32 m0, s86, 14336\n" \
  "s_nop 0\n" \
  "buffer_load_dwordx4 v7, s[68:71], s90 offen lds\n" \
  "s_add_u32 s90, s90, 16384\n" \
  "s_add_u32 m0, s86, 15360\n" \
  "s_nop 0\n" \
  "buffer_load_dwordx4 v7, s[68:71], s90 offen lds\n" \
  "s_add_u32 s90, s90, 16384\n" \
  "s_waitcnt vmcnt(16)\n" \
  "ds_read_b64_tr_b16 a[64:65], v16 offset:16384\n" \
  "ds_read_b64_tr_b16 a[66:67], v16 offset:18432\n" \
  "ds_read_b64_tr_b16 a[68:69], v17 offset:16384\n" \
  "ds_read_b64_tr_b16 a[70:71], v17 offset:18432\n" \
  "ds_read_b64_tr_b16 a[72:73], v18 offset:16384\n" \
  "ds_read_b64_tr_b16 a[74:75], v18 offset:18432\n" \
  "ds_read_b64_tr_b16 a[76:77], v19 offset:16384\n" \
  "ds_read_b64_tr_b16 a[78:79], v19 offset:18432\n" \
  "ds_read_b64_tr_b16 a[80:81], v16 offset:20480\n" \
  "ds_read_b64_tr_b16 a[82:83], v16 offset:22528\n" \
  "ds_read_b64_tr_b16 a[84:85], v17 offset:20480\n" \
  "ds_read_b64_tr_b16 a[86:87], v17 offset:22528\n" \
  "ds_read_b64_tr_b16 a[88:89], v18 offset:20480\n" \
  "ds_read_b64_tr_b16 a[90:91], v18 offset:22528\n" \
  "ds_read_b64_tr_b16 a[92:93], v19 offset:20480\n" \
  "ds_read_b64_tr_b16 a[94:95], v19 offset:22528\n" \
  "ds_read_b64_tr_b16 a[96:97], v16 offset:24576\n" \
  "ds_read_b64_tr_b16 a[98:99], v16 offset:26624\n" \
  "ds_read_b64_tr_b16 a[100:101], v17 offset:24576\n" \
  "ds_read_b64_tr_b16 a[102:103], v17 offset:26624\n" \
  "ds_read_b64_tr_b16 a[104:105], v18 offset:24576\n" \
  "ds_read_b64_tr_b16 a[106:107], v18 offset:26624\n" \
  "ds_read_b64_tr_b16 a[108:109], v19 offset:24576\n" \
  "ds_read_b64_tr_b16 a[110:111], v19 offset:26624\n" \
  "ds_read_b64_tr_b16 a[112:113], v16 offset:28672\n" \
  "ds_read_b64_tr_b16 a[114:115], v16 offset:30720\n" \
  "ds_read_b64_tr_b16 a[116:117], v17 offset:28672\n" \
  "ds_read_b64_tr_b16 a[118:119], v17 offset:30720\n" \
  "ds_read_b64_tr_b16 a[120:121], v18 offset:28672\n" \
  "ds_read_b64_tr_b16 a[122:123], v18 offset:30720\n" \
  "ds_read_b64_tr_b16 a[124:125], v19 offset:28672\n" \
  "ds_read_b64_tr_b16 a[126:127], v19 offset:30720\n" \
  "s_waitcnt lgkmcnt(0)\n" \
  "s_add_u32 m0, s86, 16384\n" \
  "s_nop 0\n" \
  "buffer_load_dwordx4 v7, s[68:71], s90 offen lds\n" \
  "s_add_u32 s90, s90, 16384\n" \
  "s_add_u32 m0, s86, 17408\n" \
  "s_nop 0\n" \
  "buffer_load_dwordx4 v7, s[68:71], s90 offen lds\n" \
  "s_add_u32 s90, s90, 16384\n" \
  "s_add_u32 m0, s86, 18432\n" \
  "s_nop 0\n" \
  "buffer_load_dwordx4 v7, s[68:71], s90 offen lds\n" \
  "s_add_u32 s90, s90, 16384\n" \
  "s_add_u32 m0, s86, 19456\n" \
  "s_nop 0\n" \
  "buffer_load_dwordx4 v7, s[68:71], s90 offen lds\n" \
  "s_add_u32 s90, s90, 16384\n" \
  "s_add_u32 m0, s86, 20480\n" \
  "s_nop 0\n" \
  "buffer_load_dwordx4 v7, s[68:71], s90 offen lds\n" \
  "s_add_u32 s90, s90, 16384\n" \
  "s_add_u32 m0, s86, 21504\n" \
  "s_nop 0\n" \
  "buffer_load_dwordx4 v7, s[68:71], s90 offen lds\n" \
  "s_add_u32 s90, s90, 16384\n" \
  "s_add_u32 m0, s86, 22528\n" \
  "s_nop 0\n" \
  "buffer_load_dwordx4 v7, s[68:71], s90 offen lds\n" \
  "s_add_u32 s90, s90, 16384\n" \
  "s_add_u32 m0, s86, 23552\n" \
  "s_nop 0\n" \
  "buffer_load_dwordx4 v7, s[68:71], s90 offen lds\n" \
  "s_add_u32 s90, s90, 16384\n" \
  "s_add_u32 m0, s86, 24576\n" \
  "s_nop 0\n" \
  "buffer_load_dwordx4 v7, s[68:71], s90 offen lds\n" \
  "s_add_u32 s90, s90, 16384\n" \
  "s_add_u32 m0, s86, 25600\n" \
  "s_nop 0\n" \
  "buffer_load_dwordx4 v7, s[68:71], s90 offen lds\n" \
  "s_add_u32 s90, s90, 16384\n" \
  "s_add_u32 m0, s86, 26624\n" \
  "s_nop 0\n" \
  "buffer_load_dwordx4 v7, s[68:71], s90 offen lds\n" \
  "s_add_u32 s90, s90, 16384\n" \
  "s_add_u32 m0, s86, 27648\n" \
  "s_nop 0\n" \
  "buffer_load_dwordx4 v7, s[68:71], s90 offen lds\n" \
  "s_add_u32 s90, s90, 16384\n" \
  "s_add_u32 m0, s86, 28672\n" \
  "s_nop 0\n" \
  "buffer_load_dwordx4 v7, s[68:71], s90 offen lds\n" \
  "s_add_u32 s90, s90, 16384\n" \
  "s_add_u32 m0, s86, 29696\n" \
  "s_nop 0\n" \
  "buffer_load_dwordx4 v7, s[68:71], s90 offen lds\n" \
  "s_add_u32 s90, s90, 16384\n" \
  "s_add_u32 m0, s86, 30720\n" \
  "s_nop 0\n" \
  "buffer_load_dwordx4 v7, s[68:71], s90 offen lds\n" \
  "s_add_u32 s90, s90, 16384\n" \
  "s_add_u32 m0, s86, 31744\n" \
  "s_nop 0\n" \
  "buffer_load_dwordx4 v7, s[68:71], s90 offen lds\n" \
  "s_add_u32 s90, s90, 16384\n" \
  "s_waitcnt vmcnt(16)\n" \
  "ds_read_b64_tr_b16 a[128:129], v16 offset:0\n" \
  "ds_read_b64_tr_b16 a[130:131], v16 offset:2048\n" \
  "ds_read_b64_tr_b16 a[132:133], v17 offset:0\n" \
  "ds_read_b64_tr_b16 a[134:135], v17 offset:2048\n" \
  "ds_read_b64_tr_b16 a[136:137], v18 offset:0\n" \
  "ds_read_b64_tr_b16 a[138:139], v18 offset:2048\n" \
  "ds_read_b64_tr_b16 a[140:141], v19 offset:0\n" \
  "ds_read_b64_tr_b16 a[142:143], v19 offset:2048\n" \
  "ds_read_b64_tr_b16 a[144:145], v16 offset:4096\n" \
  "ds_read_b64_tr_b16 a[146:147], v16 offset:6144\n" \
  "ds_read_b64_tr_b16 a[148:149], v17 offset:4096\n" \
  "ds_read_b64_tr_b16 a[150:151], v17 offset:6144\n" \
  "ds_read_b64_tr_b16 a[152:153], v18 offset:4096\n" \
  "ds_read_b64_tr_b16 a[154:155], v18 offset:6144\n" \
  "ds_read_b64_tr_b16 a[156:157], v19 offset:4096\n" \
  "ds_read_b64_tr_b16 a[158:159], v19 offset:6144\n" \
  "ds_read_b64_tr_b16 a[160:161], v16 offset:8192\n" \
  "ds_read_b64_tr_b16 a[162:163], v16 offset:10240\n" \
  "ds_read_b64_tr_b16 a[164:165], v17 offset:8192\n" \
  "ds_read_b64_tr_b16 a[166:167], v17 offset:10240\n" \
  "ds_read_b64_tr_b16 a[168:169], v18 offset:8192\n" \
  "ds_read_b64_tr_b16 a[170:171], v18 offset:10240\n" \
  "ds_read_b64_tr_b16 a[172:173], v19 offset:8192\n" \
  "ds_read_b64_tr_b16 a[174:175], v19 offset:10240\n" \
  "ds_read_b64_tr_b16 a[176:177], v16 offset:12288\n" \
  "ds_read_b64_tr_b16 a[178:179], v16 offset:14336\n" \
  "ds_read_b64_tr_b16 a[180:181], v17 offset:12288\n" \
  "ds_read_b64_tr_b16 a[182:183], v17 offset:14336\n" \
  "ds_read_b64_tr_b16 a[184:185], v18 offset:12288\n" \
  "ds_read_b64_tr_b16 a[186:187], v18 offset:14336\n" \
  "ds_read_b64_tr_b16 a[188:189], v19 offset:12288\n" \
  "ds_read_b64_tr_b16 a[190:191], v19 offset:14336\n" \
  "s_waitcnt lgkmcnt(0)\n" \
  "s_add_u32 m0, s86, 0\n" \
  "s_nop 0\n" \
  "buffer_load_dwordx4 v7, s[68:71], s90 offen lds\n" \
  "s_add_u32 s90, s90, 16384\n" \
  "s_add_u32 m0, s86, 1024\n" \
  "s_nop 0\n" \
  "buffer_load_dwordx4 v7, s[68:71], s90 offen lds\n" \
  "s_add_u32 s90, s90, 16384\n" \
  "s_add_u32 m0, s86, 2048\n" \
  "s_nop 0\n" \
  "buffer_load_dwordx4 v7, s[68:71], s90 offen lds\n" \
  "s_add_u32 s90, s90, 16384\n" \
  "s_add_u32 m0, s86, 3072\n" \
  "s_nop 0\n" \
  "buffer_load_dwordx4 v7, s[68:71], s90 offen lds\n" \
  "s_add_u32 s90, s90, 16384\n" \
  "s_add_u32 m0, s86, 4096\n" \
  "s_nop 0\n" \
  "buffer_load_dwordx4 v7, s[68:71], s90 offen lds\n" \
  "s_add_u32 s90, s90, 16384\n" \
  "s_add_u32 m0, s86, 5120\n" \
  "s_nop 0\n" \
  "buffer_load_dwordx4 v7, s[68:71], s90 offen lds\n" \
  "s_add_u32 s90, s90, 16384\n" \
  "s_add_u32 m0, s86, 6144\n" \
  "s_nop 0\n" \
  "buffer_load_dwordx4 v7, s[68:71], s90 offen lds\n" \
  "s_add_u32 s90, s90, 16384\n" \
  "s_add_u32 m0, s86, 7168\n" \
  "s_nop 0\n" \
  "buffer_load_dwordx4 v7, s[68:71], s90 offen lds\n" \
  "s_add_u32 s90, s90, 16384\n" \
  "s_add_u32 m0, s86, 8192\n" \
  "s_nop 0\n" \
  "buffer_load_dwordx4 v7, s[68:71], s90 offen lds\n" \
  "s_add_u32 s90, s90, 16384\n" \
  "s_add_u32 m0, s86, 9216\n" \
  "s_nop 0\n" \
  "buffer_load_dwordx4 v7, s[68:71], s90 offen lds\n" \
  "s_add_u32 s90, s90, 16384\n" \
  "s_add_u32 m0, s86, 10240\n" \
  "s_nop 0\n" \
  "buffer_load_dwordx4 v7, s[68:71], s90 offen lds\n" \
  "s_add_u32 s90, s90, 16384\n" \
  "s_add_u32 m0, s86, 11264\n" \
  "s_nop 0\n" \
  "buffer_load_dwordx4 v7, s[68:71], s90 offen lds\n" \
  "s_add_u32 s90, s90, 16384\n" \
  "s_add_u32 m0, s86, 12288\n" \
  "s_nop 0\n" \
  "buffer_load_dwordx4 v7, s[68:71], s90 offen lds\n" \
  "s_add_u32 s90, s90, 16384\n" \
  "s_add_u32 m0, s86, 13312\n" \
  "s_nop 0\n" \
  "buffer_load_dwordx4 v7, s[68:71], s90 offen lds\n" \
  "s_add_u32 s90, s90, 16384\n" \
  "s_add_u32 m0, s86, 14336\n" \
  "s_nop 0\n" \
  "buffer_load_dwordx4 v7, s[68:71], s90 offen lds\n" \
  "s_add_u32 s90, s90, 16384\n" \
  "s_add_u32 m0, s86, 15360\n" \
  "s_nop 0\n" \
  "buffer_load_dwordx4 v7, s[68:71], s90 offen lds\n" \
  "s_add_u32 s90, s90, 16384\n" \
  "s_waitcnt vmcnt(16)\n" \
  "ds_read_b64_tr_b16 a[192:193], v16 offset:16384\n" \
  "ds_read_b64_tr_b16 a[194:195], v16 offset:18432\n" \
  "ds_read_b64_tr_b16 a[196:197], v17 offset:16384\n" \
  "ds_read_b64_tr_b16 a[198:199], v17 offset:18432\n" \
  "ds_read_b64_tr_b16 a[200:201], v18 offset:16384\n" \
  "ds_read_b64_tr_b16 a[202:203], v18 offset:18432\n" \
  "ds_read_b64_tr_b16 a[204:205], v19 offset:16384\n" \
  "ds_read_b64_tr_b16 a[206:207], v19 offset:18432\n" \
  "ds_read_b64_tr_b16 a[208:209], v16 offset:20480\n" \
  "ds_read_b64_tr_b16 a[210:211], v16 offset:22528\n" \
  "ds_read_b64_tr_b16 a[212:213], v17 offset:20480\n" \
  "ds_read_b64_tr_b16 a[214:215], v17 offset:22528\n" \
  "ds_read_b64_tr_b16 a[216:217], v18 offset:20480\n" \
  "ds_read_b64_tr_b16 a[218:219], v18 offset:22528\n" \
  "ds_read_b64_tr_b16 a[220:221], v19 offset:20480\n" \
  "ds_read_b64_tr_b16 a[222:223], v19 offset:22528\n" \
  "ds_read_b64_tr_b16 a[224:225], v16 offset:24576\n" \
  "ds_read_b64_tr_b16 a[226:227], v16 offset:26624\n" \
  "ds_read_b64_tr_b16 a[228:229], v17 offset:24576\n" \
  "ds_read_b64_tr_b16 a[230:231], v17 offset:26624\n" \
  "ds_read_b64_tr_b16 a[232:233], v18 offset:24576\n" \
  "ds_read_b64_tr_b16 a[234:235], v18 offset:26624\n" \
  "ds_read_b64_tr_b16 a[236:237], v19 offset:24576\n" \
  "ds_read_b64_tr_b16 a[238:239], v19 offset:26624\n" \
  "ds_read_b64_tr_b16 a[240:241], v16 offset:28672\n" \
  "ds_read_b64_tr_b16 a[242:243], v16 offset:30720\n" \
  "ds_read_b64_tr_b16 a[244:245], v17 offset:28672\n" \
  "ds_read_b64_tr_b16 a[246:247], v17 offset:30720\n" \
  "ds_read_b64_tr_b16 a[248:249], v18 offset:28672\n" \
  "ds_read_b64_tr_b16 a[250:251], v18 offset:30720\n" \
  "ds_read_b64_tr_b16 a[252:253], v19 offset:28672\n" \
  "ds_read_b64_tr_b16 a[254:255], v19 offset:30720\n" \
  "s_waitcnt lgkmcnt(0)\n" \
  "s_add_u32 m0, s86, 16384\n" \
  "s_nop 0\n" \
  "buffer_load_dwordx4 v7, s[68:71], s90 offen lds\n" \
  "s_add_u32 s90, s90, 16384\n" \
  "s_add_u32 m0, s86, 17408\n" \
  "s_nop 0\n" \
  "buffer_load_dwordx4 v7, s[68:71], s90 offen lds\n" \
  "s_add_u32 s90, s90, 16384\n" \
  "s_add_u32 m0, s86, 18432\n" \
  "s_nop 0\n" \
  "buffer_load_dwordx4 v7, s[68:71], s90 offen lds\n" \
  "s_add_u32 s90, s90, 16384\n" \
  "s_add_u32 m0, s86, 19456\n" \
  "s_nop 0\n" \
  "buffer_load_dwordx4 v7, s[68:71], s90 offen lds\n" \
  "s_add_u32 s90, s90, 16384\n" \
  "s_add_u32 m0, s86, 20480\n" \
  "s_nop 0\n" \
  "buffer_load_dwordx4 v7, s[68:71], s90 offen lds\n" \
  "s_add_u32 s90, s90, 16384\n" \
  "s_add_u32 m0, s86, 21504\n" \
  "s_nop 0\n" \
  "buffer_load_dwordx4 v7, s[68:71], s90 offen lds\n" \
  "s_add_u32 s90, s90, 16384\n" \
  "s_add_u32 m0, s86, 22528\n" \
  "s_nop 0\n" \
  "buffer_load_dwordx4 v7, s[68:71], s90 offen lds\n" \
  "s_add_u32 s90, s90, 16384\n" \
  "s_add_u32 m0, s86, 23552\n" \
  "s_nop 0\n" \
  "buffer_load_dwordx4 v7, s[68:71], s90 offen lds\n" \
  "s_add_u32 s90, s90, 16384\n" \
  "s_add_u32 m0, s86, 24576\n" \
  "s_nop 0\n" \
  "buffer_load_dwordx4 v7, s[68:71], s90 offen lds\n" \
  "s_add_u32 s90, s90, 16384\n" \
  "s_add_u32 m0, s86, 25600\n" \
  "s_nop 0\n" \
  "buffer_load_dwordx4 v7, s[68:71], s90 offen lds\n" \
  "s_add_u32 s90, s90, 16384\n" \
  "s_add_u32 m0, s86, 26624\n" \
  "s_nop 0\n" \
  "buffer_load_dwordx4 v7, s[68:71], s90 offen lds\n" \
  "s_add_u32 s90, s90, 16384\n" \
  "s_add_u32 m0, s86, 27648\n" \
  "s_nop 0\n" \
  "buffer_load_dwordx4 v7, s[68:71], s90 offen lds\n" \
  "s_add_u32 s90, s90, 16384\n" \
  "s_add_u32 m0, s86, 28672\n" \
  "s_nop 0\n" \
  "buffer_load_dwordx4 v7, s[68:71], s90 offen lds\n" \
  "s_add_u32 s90, s90, 16384\n" \
  "s_add_u32 m0, s86, 29696\n" \
  "s_nop 0\n" \
  "buffer_load_dwordx4 v7, s[68:71], s90 offen lds\n" \
  "s_add_u32 s90, s90, 16384\n" \
  "s_add_u32 m0, s86, 30720\n" \
  "s_nop 0\n" \
  "buffer_load_dwordx4 v7, s[68:71], s90 offen lds\n" \
  "s_add_u32 s90, s90, 16384\n" \
  "s_add_u32 m0, s86, 31744\n" \
  "s_nop 0\n" \
  "buffer_load_dwordx4 v7, s[68:71], s90 offen lds\n" \
  "s_add_u32 s90, s90, 16384\n" \
  "s_mov_b32 s92, 1\n" \
  "LP0_%=:\n" \
  "buffer_load_dword v9, v8, s[80:83], 0 offen sc1\n" \
  "s_waitcnt vmcnt(0)\n" \
  "v_cmp_gt_u32 vcc, s92, v9\n" \
  "s_cbranch_vccz LP1_%=\n" \
  "s_sleep 8\n" \
  "s_add_u32 s91, s91, 1\n" \
  "s_cmp_lt_u32 s91, " STR(SPIN_CAP) "\n" \
  "s_cbranch_scc1 LP0_%=\n" \
  "v_mov_b32 v5, " STR(CW_DEAD) "*4\n" \
  "v_mov_b32 v15, 1\n" \
  "buffer_store_dword v15, v5, s[80:83], 0 offen sc1\n" \
  "s_branch LP1_%=\n" \
  "LP1_%=:\n" \
  "s_add_u32 s84, s87, 0\n" \
  "buffer_load_dwordx4 v[64:67], v6, s[72:75], s84 offen sc1\n" \
  "s_add_u32 s84, s87, 1024\n" \
  "buffer_load_dwordx4 v[68:71], v6, s[72:75], s84 offen sc1\n" \
  "s_add_u32 s84, s87, 2048\n" \
  "buffer_load_dwordx4 v[72:75], v6, s[72:75], s84 offen sc1\n" \
  "s_add_u32 s84, s87, 49152\n" \
  "buffer_load_dwordx4 v[76:79], v6, s[72:75], s84 offen sc1\n" \
  "s_add_u32 s84, s87, 3072\n" \
  "buffer_load_dwordx4 v[80:83], v6, s[72:75], s84 offen sc1\n" \
  "s_add_u32 s84, s87, 4096\n" \
  "buffer_load_dwordx4 v[84:87], v6, s[72:75], s84 offen sc1\n" \
  "s_add_u32 s84, s87, 5120\n" \
  "buffer_load_dwordx4 v[88:91], v6, s[72:75], s84 offen sc1\n" \
  "s_add_u32 s84, s87, 50176\n" \
  "buffer_load_dwordx4 v[92:95], v6, s[72:75], s84 offen sc1\n" \
  "s_add_u32 s84, s87, 6144\n" \
  "buffer_load_dwordx4 v[96:99], v6, s[72:75], s84 offen sc1\n" \
  "s_add_u32 s84, s87, 7168\n" \
  "buffer_load_dwordx4 v[100:103], v6, s[72:75], s84 offen sc1\n" \
  "s_add_u32 s84, s87, 16384\n" \
  "buffer_load_dwordx4 v[104:107], v6, s[72:75], s84 offen sc1\n" \
  "s_add_u32 s84, s87, 51200\n" \
  "buffer_load_dwordx4 v[108:111], v6, s[72:75], s84 offen sc1\n" \
  "s_add_u32 s84, s87, 17408\n" \
  "buffer_load_dwordx4 v[112:115], v6, s[72:75], s84 offen sc1\n" \
  "s_add_u32 s84, s87, 18432\n" \
  "buffer_load_dwordx4 v[116:119], v6, s[72:75], s84 offen sc1\n" \
  "s_add_u32 s84, s87, 19456\n" \
  "buffer_load_dwordx4 v[120:123], v6, s[72:75], s84 offen sc1\n" \
  "s_add_u32 s84, s87, 52224\n" \
  "buffer_load_dwordx4 v[124:127], v6, s[72:75], s84 offen sc1\n" \
  "v_or_b32 v10, 0x80000000, v10\n" \
  "s_sub_u32 s88, s88, 131072\n" \
  ".p2align 6\n" \
  "LX_%=:\n" \
  "s_waitcnt vmcnt(15)\n" \
  "v_mfma_f32_16x16x32_bf16 v[20:23], v[128:131], v[64:67], 0\n" \
  "v_mfma_f32_16x16x32_bf16 v[24:27], v[132:135], v[64:67], 0\n" \
  "v_mfma_f32_16x16x32_bf16 v[28:31], v[136:139], v[64:67], 0\n" \
  "v_mfma_f32_16x16x32_bf16 v[32:35], v[140:143], v[64:67], 0\n" \
  "s_add_u32 s84, s87, 20480\n" \
  "buffer_load_dwordx4 v[64:67], v6, s[72:75], s84 offen sc1\n" \
  "s_waitcnt vmcnt(15)\n" \
  "v_mfma_f32_16x16x32_bf16 v[20:23], v[144:147], v[68:71], v[20:23]\n" \
  "v_mfma_f32_16x16x32_bf16 v[24:27], v[148:151], v[68:71], v[24:27]\n" \
  "v_mfma_f32_16x16x32_bf16 v[28:31], v[152:155], v[68:71], v[28:31]\n" \
  "v_mfma_f32_16x16x32_bf16 v[32:35], v[156:159], v[68:71], v[32:35]\n" \
  "s_add_u32 s84, s87, 21504\n" \
  "buffer_load_dwordx4 v[68:71], v6, s[72:75], s84 offen sc1\n" \
  "s_waitcnt vmcnt(15)\n" \
  "v_mfma_f32_16x16x32_bf16 v[20:23], v[160:163], v[72:75], v[20:23]\n" \
  "ds_read_b64_tr_b16 v[40:41], v16 offset:0\n" \
  "ds_read_b64_tr_b16 v[42:43], v16 offset:2048\n" \
  "v_mfma_f32_16x16x32_bf16 v[24:27], v[164:167], v[72:75], v[24:27]\n" \
  "ds_read_b64_tr_b16 v[44:45], v17 offset:0\n" \
  "ds_read_b64_tr_b16 v[46:47], v17 offset:2048\n" \
  "v_mfma_f32_16x16x32_bf16 v[28:31], v[168:171], v[72:75], v[28:31]\n" \
  "ds_read_b64_tr_b16 v[48:49], v18 offset:0\n" \
  "ds_read_b64_tr_b16 v[50:51], v18 offset:2048\n" \
  "v_mfma_f32_16x16x32_bf16 v[32:35], v[172:175], v[72:75], v[32:35]\n" \
  "ds_read_b64_tr_b16 v[52:53], v19 offset:0\n" \
  "ds_read_b64_tr_b16 v[54:55], v19 offset:2048\n" \
  "s_add_u32 s84, s87, 22528\n" \
  "buffer_load_dwordx4 v[72:75], v6, s[72:75], s84 offen sc1\n" \
  "s_waitcnt vmcnt(15)\n" \
  "s_waitcnt lgkmcnt(6)\n" \
  "v_mfma_f32_16x16x32_bf16 v[20:23], v[40:43], v[76:79], v[20:23]\n" \
  "s_waitcnt lgkmcnt(4)\n" \
  "v_mfma_f32_16x16x32_bf16 v[24:27], v[44:47], v[76:79], v[24:27]\n" \
  "s_waitcnt lgkmcnt(2)\n" \
  "v_mfma_f32_16x16x32_bf16 v[28:31], v[48:51], v[76:79], v[28:31]\n" \
  "s_waitcnt lgkmcnt(0)\n" \
  "v_mfma_f32_16x16x32_bf16 v[32:35], v[52:55], v[76:79], v[32:35]\n" \
  "s_add_u32 s84, s87, 53248\n" \
  "buffer_load_dwordx4 v[76:79], v6, s[72:75], s84 offen sc1\n" \
  "s_waitcnt lgkmcnt(0)\n" \
  "s_barrier\n" \
  "ds_read_b128 v[52:55], v11 offset:0\n" \
  "ds_read_b128 v[56:59], v11 offset:1024\n" \
  "ds_read_b128 v[60:63], v11 offset:2048\n" \
  "s_waitcnt vmcnt(15)\n" \
  "v_mfma_f32_16x16x32_bf16 v[20:23], v[176:179], v[80:83], v[20:23]\n" \
  "v_mfma_f32_16x16x32_bf16 v[24:27], v[180:183], v[80:83], v[24:27]\n" \
  "v_mfma_f32_16x16x32_bf16 v[28:31], v[184:187], v[80:83], v[28:31]\n" \
  "v_mfma_f32_16x16x32_bf16 v[32:35], v[188:191], v[80:83], v[32:35]\n" \
  "s_add_u32 s84, s87, 23552\n" \
  "buffer_load_dwordx4 v[80:83], v6, s[72:75], s84 offen sc1\n" \
  "s_waitcnt lgkmcnt(0)\n" \
  "s_waitcnt vmcnt(15)\n" \
  "v_mfma_f32_16x16x32_bf16 v[20:23], v[192:195], v[84:87], v[20:23]\n" \
  "v_add_f32 v36, v36, v52\n" \
  "v_add_f32 v37, v37, v53\n" \
  "v_add_f32 v38, v38, v54\n" \
  "v_add_f32 v39, v39, v55\n" \
  "v_mfma_f32_16x16x32_bf16 v[24:27], v[196:199], v[84:87], v[24:27]\n" \
  "v_add_f32 v56, v56, v60\n" \
  "v_add_f32 v57, v57, v61\n" \
  "v_add_f32 v58, v58, v62\n" \
  "v_add_f32 v59, v59, v63\n" \
  "v_mfma_f32_16x16x32_bf16 v[28:31], v[200:203], v[84:87], v[28:31]\n" \
  "v_add_f32 v36, v36, v56\n" \
  "v_add_f32 v37, v37, v57\n" \
  "v_add_f32 v38, v38, v58\n" \
  "v_add_f32 v39, v39, v59\n" \
  "v_mfma_f32_16x16x32_bf16 v[32:35], v[204:207], v[84:87], v[32:35]\n" \
  "v_cvt_pk_bf16_f32 v36, v36, v37\n" \
  "v_cvt_pk_bf16_f32 v37, v38, v39\n" \
  "buffer_store_dwordx2 v[36:37], v10, s[76:79], s88 offen\n" \
  "s_add_u32 s88, s88, 131072\n" \
  "v_and_b32 v10, 0x7fffffff, v10\n" \
  "s_add_u32 s84, s87, 32768\n" \
  "buffer_load_dwordx4 v[84:87], v6, s[72:75], s84 offen sc1\n" \
  "s_waitcnt vmcnt(16)\n" \
  "v_mfma_f32_16x16x32_bf16 v[20:23], v[208:211], v[88:91], v[20:23]\n" \
  "ds_read_b64_tr_b16 v[40:41], v16 offset:4096\n" \
  "ds_read_b64_tr_b16 v[42:43], v16 offset:6144\n" \
  "v_mfma_f32_16x16x32_bf16 v[24:27], v[212:215], v[88:91], v[24:27]\n" \
  "ds_read_b64_tr_b16 v[44:45], v17 offset:4096\n" \
  "ds_read_b64_tr_b16 v[46:47], v17 offset:6144\n" \
  "v_mfma_f32_16x16x32_bf16 v[28:31], v[216:219], v[88:91], v[28:31]\n" \
  "ds_read_b64_tr_b16 v[48:49], v18 offset:4096\n" \
  "ds_read_b64_tr_b16 v[50:51], v18 offset:6144\n" \
  "v_mfma_f32_16x16x32_bf16 v[32:35], v[220:223], v[88:91], v[32:35]\n" \
  "ds_read_b64_tr_b16 v[52:53], v19 offset:4096\n" \
  "ds_read_b64_tr_b16 v[54:55], v19 offset:6144\n" \
  "s_add_u32 s84, s87, 33792\n" \
  "buffer_load_dwordx4 v[88:91], v6, s[72:75], s84 offen sc1\n" \
  "s_waitcnt vmcnt(16)\n" \
  "s_waitcnt lgkmcnt(6)\n" \
  "v_mfma_f32_16x16x32_bf16 v[20:23], v[40:43], v[92:95], v[20:23]\n" \
  "s_waitcnt lgkmcnt(4)\n" \
  "v_mfma_f32_16x16x32_bf16 v[24:27], v[44:47], v[92:95], v[24:27]\n" \
  "s_waitcnt lgkmcnt(2)\n" \
  "v_mfma_f32_16x16x32_bf16 v[28:31], v[48:51], v[92:95], v[28:31]\n" \
  "s_waitcnt lgkmcnt(0)\n" \
  "v_mfma_f32_16x16x32_bf16 v[32:35], v[52:55], v[92:95], v[32:35]\n" \
  "s_add_u32 s84, s87, 54272\n" \
  "buffer_load_dwordx4 v[92:95], v6, s[72:75], s84 offen sc1\n" \
  "s_waitcnt vmcnt(16)\n" \
  "v_mfma_f32_16x16x32_bf16 v[20:23], v[224:227], v[96:99], v[20:23]\n" \
  "buffer_load_dword v9, v8, s[80:83], 0 offen sc1\n" \
  "v_mfma_f32_16x16x32_bf16 v[24:27], v[228:231], v[96:99], v[24:27]\n" \
  "v_xor_b32 v11, 0x4000, v11\n" \
  "v_xor_b32 v12, 0x4000, v12\n" \
  "v_mfma_f32_16x16x32_bf16 v[28:31], v[232:235], v[96:99], v[28:31]\n" \
  "v_xor_b32 v13, 0x4000, v13\n" \
  "v_xor_b32 v14, 0x4000, v14\n" \
  "v_mfma_f32_16x16x32_bf16 v[32:35], v[236:239], v[96:99], v[32:35]\n" \
  "s_add_u32 s84, s87, 34816\n" \
  "buffer_load_dwordx4 v[96:99], v6, s[72:75], s84 offen sc1\n" \
  "s_waitcnt vmcnt(17)\n" \
  "v_mfma_f32_16x16x32_bf16 v[20:23], v[240:243], v[100:103], v[20:23]\n" \
  "v_mfma_f32_16x16x32_bf16 v[24:27], v[244:247], v[100:103], v[24:27]\n" \
  "v_mfma_f32_16x16x32_bf16 v[28:31], v[248:251], v[100:103], v[28:31]\n" \
  "v_mfma_f32_16x16x32_bf16 v[32:35], v[252:255], v[100:103], v[32:35]\n" \
  "s_add_u32 s84, s87, 35840\n" \
  "buffer_load_dwordx4 v[100:103], v6, s[72:75], s84 offen sc1\n" \
  "s_waitcnt vmcnt(17)\n" \
  "v_mfma_f32_16x16x32_bf16 v[20:23], a[0:3], v[104:107], v[20:23]\n" \
  "ds_read_b64_tr_b16 v[40:41], v16 offset:8192\n" \
  "ds_read_b64_tr_b16 v[42:43], v16 offset:10240\n" \
  "v_mfma_f32_16x16x32_bf16 v[24:27], a[4:7], v[104:107], v[24:27]\n" \
  "ds_read_b64_tr_b16 v[44:45], v17 offset:8192\n" \
  "ds_read_b64_tr_b16 v[46:47], v17 offset:10240\n" \
  "v_mfma_f32_16x16x32_bf16 v[28:31], a[8:11], v[104:107], v[28:31]\n" \
  "ds_read_b64_tr_b16 v[48:49], v18 offset:8192\n" \
  "ds_read_b64_tr_b16 v[50:51], v18 offset:10240\n" \
  "v_mfma_f32_16x16x32_bf16 v[32:35], a[12:15], v[104:107], v[32:35]\n" \
  "ds_read_b64_tr_b16 v[52:53], v19 offset:8192\n" \
  "ds_read_b64_tr_b16 v[54:55], v19 offset:10240\n" \
  "s_add_u32 s84, s87, 36864\n" \
  "buffer_load_dwordx4 v[104:107], v6, s[72:75], s84 offen sc1\n" \
  "s_waitcnt vmcnt(17)\n" \
  "s_waitcnt lgkmcnt(6)\n" \
  "v_mfma_f32_16x16x32_bf16 v[20:23], v[40:43], v[108:111], v[20:23]\n" \
  "s_waitcnt lgkmcnt(4)\n" \
  "v_mfma_f32_16x16x32_bf16 v[24:27], v[44:47], v[108:111], v[24:27]\n" \
  "s_waitcnt lgkmcnt(2)\n" \
  "v_mfma_f32_16x16x32_bf16 v[28:31], v[48:51], v[108:111], v[28:31]\n" \
  "s_waitcnt lgkmcnt(0)\n" \
  "v_mfma_f32_16x16x32_bf16 v[32:35], v[52:55], v[108:111], v[32:35]\n" \
  "s_add_u32 s84, s87, 55296\n" \
  "buffer_load_dwordx4 v[108:111], v6, s[72:75], s84 offen sc1\n" \
  "s_waitcnt vmcnt(17)\n" \
  "v_mfma_f32_16x16x32_bf16 v[20:23], a[16:19], v[112:115], v[20:23]\n" \
  "v_mfma_f32_16x16x32_bf16 v[24:27], a[20:23], v[112:115], v[24:27]\n" \
  "v_mfma_f32_16x16x32_bf16 v[28:31], a[24:27], v[112:115], v[28:31]\n" \
  "v_mfma_f32_16x16x32_bf16 v[32:35], a[28:31], v[112:115], v[32:35]\n" \
  "s_add_u32 s84, s87, 37888\n" \
  "buffer_load_dwordx4 v[112:115], v6, s[72:75], s84 offen sc1\n" \
  "s_waitcnt vmcnt(17)\n" \
  "v_mfma_f32_16x16x32_bf16 v[20:23], a[32:35], v[116:119], v[20:23]\n" \
  "v_mfma_f32_16x16x32_bf16 v[24:27], a[36:39], v[116:119], v[24:27]\n" \
  "v_mfma_f32_16x16x32_bf16 v[28:31], a[40:43], v[116:119], v[28:31]\n" \
  "v_mfma_f32_16x16x32_bf16 v[32:35], a[44:47], v[116:119], v[32:35]\n" \
  "s_add_u32 s84, s87, 38912\n" \
  "buffer_load_dwordx4 v[116:119], v6, s[72:75], s84 offen sc1\n" \
  "s_waitcnt vmcnt(17)\n" \
  "v_mfma_f32_16x16x32_bf16 v[20:23], a[48:51], v[120:123], v[20:23]\n" \
  "ds_read_b64_tr_b16 v[40:41], v16 offset:12288\n" \
  "ds_read_b64_tr_b16 v[42:43], v16 offset:14336\n" \
  "v_mfma_f32_16x16x32_bf16 v[24:27], a[52:55], v[120:123], v[24:27]\n" \
  "ds_read_b64_tr_b16 v[44:45], v17 offset:12288\n" \
  "ds_read_b64_tr_b16 v[46:47], v17 offset:14336\n" \
  "v_mfma_f32_16x16x32_bf16 v[28:31], a[56:59], v[120:123], v[28:31]\n" \
  "ds_read_b64_tr_b16 v[48:49], v18 offset:12288\n" \
  "ds_read_b64_tr_b16 v[50:51], v18 offset:14336\n" \
  "v_mfma_f32_16x16x32_bf16 v[32:35], a[60:63], v[120:123], v[32:35]\n" \
  "ds_read_b64_tr_b16 v[52:53], v19 offset:12288\n" \
  "ds_read_b64_tr_b16 v[54:55], v19 offset:14336\n" \
  "s_add_u32 s84, s87, 39936\n" \
  "buffer_load_dwordx4 v[120:123], v6, s[72:75], s84 offen sc1\n" \
  "s_waitcnt vmcnt(17)\n" \
  "s_waitcnt lgkmcnt(6)\n" \
  "v_mfma_f32_16x16x32_bf16 v[20:23], v[40:43], v[124:127], v[20:23]\n" \
  "s_waitcnt lgkmcnt(4)\n" \
  "v_mfma_f32_16x16x32_bf16 v[24:27], v[44:47], v[124:127], v[24:27]\n" \
  "s_waitcnt lgkmcnt(2)\n" \
  "v_mfma_f32_16x16x32_bf16 v[28:31], v[48:51], v[124:127], v[28:31]\n" \
  "s_waitcnt lgkmcnt(0)\n" \
  "v_mfma_f32_16x16x32_bf16 v[32:35], v[52:55], v[124:127], v[32:35]\n" \
  "s_add_u32 s84, s87, 56320\n" \
  "buffer_load_dwordx4 v[124:127], v6, s[72:75], s84 offen sc1\n" \
  "s_waitcnt vmcnt(8)\n" \
  "s_add_u32 s92, s89, 2\n" \
  "s_min_u32 s92, s92, 64\n" \
  "v_cmp_gt_u32 vcc, s92, v9\n" \
  "s_sub_u32 s85, s92, 1\n" \
  "s_lshr_b32 s84, s85, 2\n" \
  "s_xnor_b32 s84, s84, %7\n" \
  "s_and_b32 s84, s84, 1\n" \
  "s_lshl_b32 s84, s84, 13\n" \
  "s_lshl_b32 s87, s85, 19\n" \
  "s_or_b32 s87, s87, s84\n" \
  "s_add_u32 s87, s87, s96\n" \
  "s_cbranch_vccnz LP2_%=\n" \
  "LP3_%=:\n" \
  "s_waitcnt vmcnt(17)\n" \
  "v_mfma_f32_16x16x32_bf16 v[20:23], a[64:67], v[64:67], v[20:23]\n" \
  "v_mfma_f32_16x16x32_bf16 v[24:27], a[68:71], v[64:67], v[24:27]\n" \
  "v_mfma_f32_16x16x32_bf16 v[28:31], a[72:75], v[64:67], v[28:31]\n" \
  "v_mfma_f32_16x16x32_bf16 v[32:35], a[76:79], v[64:67], v[32:35]\n" \
  "s_add_u32 s84, s87, 0\n" \
  "buffer_load_dwordx4 v[64:67], v6, s[72:75], s84 offen sc1\n" \
  "s_waitcnt vmcnt(17)\n" \
  "v_mfma_f32_16x16x32_bf16 v[20:23], a[80:83], v[68:71], v[20:23]\n" \
  "v_mfma_f32_16x16x32_bf16 v[24:27], a[84:87], v[68:71], v[24:27]\n" \
  "v_mfma_f32_16x16x32_bf16 v[28:31], a[88:91], v[68:71], v[28:31]\n" \
  "v_mfma_f32_16x16x32_bf16 v[32:35], a[92:95], v[68:71], v[32:35]\n" \
  "s_add_u32 s84, s87, 1024\n" \
  "buffer_load_dwordx4 v[68:71], v6, s[72:75], s84 offen sc1\n" \
  "s_waitcnt vmcnt(17)\n" \
  "v_mfma_f32_16x16x32_bf16 v[20:23], a[96:99], v[72:75], v[20:23]\n" \
  "ds_read_b64_tr_b16 v[40:41], v16 offset:16384\n" \
  "ds_read_b64_tr_b16 v[42:43], v16 offset:18432\n" \
  "v_mfma_f32_16x16x32_bf16 v[24:27], a[100:103], v[72:75], v[24:27]\n" \
  "ds_read_b64_tr_b16 v[44:45], v17 offset:16384\n" \
  "ds_read_b64_tr_b16 v[46:47], v17 offset:18432\n" \
  "v_mfma_f32_16x16x32_bf16 v[28:31], a[104:107], v[72:75], v[28:31]\n" \
  "ds_read_b64_tr_b16 v[48:49], v18 offset:16384\n" \
  "ds_read_b64_tr_b16 v[50:51], v18 offset:18432\n" \
  "v_mfma_f32_16x16x32_bf16 v[32:35], a[108:111], v[72:75], v[32:35]\n" \
  "ds_read_b64_tr_b16 v[52:53], v19 offset:16384\n" \
  "ds_read_b64_tr_b16 v[54:55], v19 offset:18432\n" \
  "s_add_u32 s84, s87, 2048\n" \
  "buffer_load_dwordx4 v[72:75], v6, s[72:75], s84 offen sc1\n" \
  "s_waitcnt vmcnt(17)\n" \
  "s_waitcnt lgkmcnt(6)\n" \
  "v_mfma_f32_16x16x32_bf16 v[20:23], v[40:43], v[76:79], v[20:23]\n" \
  "s_waitcnt lgkmcnt(4)\n" \
  "v_mfma_f32_16x16x32_bf16 v[24:27], v[44:47], v[76:79], v[24:27]\n" \
  "s_waitcnt lgkmcnt(2)\n" \
  "v_mfma_f32_16x16x32_bf16 v[28:31], v[48:51], v[76:79], v[28:31]\n" \
  "s_waitcnt lgkmcnt(0)\n" \
  "v_mfma_f32_16x16x32_bf16 v[32:35], v[52:55], v[76:79], v[32:35]\n" \
  "s_add_u32 s84, s87, 49152\n" \
  "buffer_load_dwordx4 v[76:79], v6, s[72:75], s84 offen sc1\n" \
  "s_waitcnt vmcnt(17)\n" \
  "v_mfma_f32_16x16x32_bf16 v[20:23], a[112:115], v[80:83], v[20:23]\n" \
  "v_mfma_f32_16x16x32_bf16 v[24:27], a[116:119], v[80:83], v[24:27]\n" \
  "v_mfma_f32_16x16x32_bf16 v[28:31], a[120:123], v[80:83], v[28:31]\n" \
  "v_mfma_f32_16x16x32_bf16 v[32:35], a[124:127], v[80:83], v[32:35]\n" \
  "s_add_u32 s84, s87, 3072\n" \
  "buffer_load_dwordx4 v[80:83], v6, s[72:75], s84 offen sc1\n" \
  "s_waitcnt vmcnt(16)\n" \
  "v_mfma_f32_16x16x32_bf16 v[20:23], a[128:131], v[84:87], v[20:23]\n" \
  "v_mfma_f32_16x16x32_bf16 v[24:27], a[132:135], v[84:87], v[24:27]\n" \
  "v_mfma_f32_16x16x32_bf16 v[28:31], a[136:139], v[84:87], v[28:31]\n" \
  "v_mfma_f32_16x16x32_bf16 v[32:35], a[140:143], v[84:87], v[32:35]\n" \
  "s_add_u32 s84, s87, 4096\n" \
  "buffer_load_dwordx4 v[84:87], v6, s[72:75], s84 offen sc1\n" \
  "s_waitcnt vmcnt(16)\n" \
  "v_mfma_f32_16x16x32_bf16 v[20:23], a[144:147], v[88:91], v[20:23]\n" \
  "ds_read_b64_tr_b16 v[40:41], v16 offset:20480\n" \
  "ds_read_b64_tr_b16 v[42:43], v16 offset:22528\n" \
  "v_mfma_f32_16x16x32_bf16 v[24:27], a[148:151], v[88:91], v[24:27]\n" \
  "ds_read_b64_tr_b16 v[44:45], v17 offset:20480\n" \
  "ds_read_b64_tr_b16 v[46:47], v17 offset:22528\n" \
  "v_mfma_f32_16x16x32_bf16 v[28:31], a[152:155], v[88:91], v[28:31]\n" \
  "ds_read_b64_tr_b16 v[48:49], v18 offset:20480\n" \
  "ds_read_b64_tr_b16 v[50:51], v18 offset:22528\n" \
  "v_mfma_f32_16x16x32_bf16 v[32:35], a[156:159], v[88:91], v[32:35]\n" \
  "ds_read_b64_tr_b16 v[52:53], v19 offset:20480\n" \
  "ds_read_b64_tr_b16 v[54:55], v19 offset:22528\n" \
  "s_add_u32 s84, s87, 5120\n" \
  "buffer_load_dwordx4 v[88:91], v6, s[72:75], s84 offen sc1\n" \
  "s_waitcnt vmcnt(16)\n" \
  "s_waitcnt lgkmcnt(6)\n" \
  "v_mfma_f32_16x16x32_bf16 v[20:23], v[40:43], v[92:95], v[20:23]\n" \
  "s_waitcnt lgkmcnt(4)\n" \
  "v_mfma_f32_16x16x32_bf16 v[24:27], v[44:47], v[92:95], v[24:27]\n" \
  "s_waitcnt lgkmcnt(2)\n" \
  "v_mfma_f32_16x16x32_bf16 v[28:31], v[48:51], v[92:95], v[28:31]\n" \
  "s_waitcnt lgkmcnt(0)\n" \
  "v_mfma_f32_16x16x32_bf16 v[32:35], v[52:55], v[92:95], v[32:35]\n" \
  "s_add_u32 s84, s87, 50176\n" \
  "buffer_load_dwordx4 v[92:95], v6, s[72:75], s84 offen sc1\n" \
  "s_waitcnt vmcnt(15)\n" \
  "v_mfma_f32_16x16x32_bf16 v[20:23], a[160:163], v[96:99], v[20:23]\n" \
  "v_mfma_f32_16x16x32_bf16 v[24:27], a[164:167], v[96:99], v[24:27]\n" \
  "v_mfma_f32_16x16x32_bf16 v[28:31], a[168:171], v[96:99], v[28:31]\n" \
  "v_mfma_f32_16x16x32_bf16 v[32:35], a[172:175], v[96:99], v[32:35]\n" \
  "s_add_u32 s84, s87, 6144\n" \
  "buffer_load_dwordx4 v[96:99], v6, s[72:75], s84 offen sc1\n" \
  "s_waitcnt vmcnt(15)\n" \
  "v_mfma_f32_16x16x32_bf16 v[20:23], a[176:179], v[100:103], v[20:23]\n" \
  "v_mfma_f32_16x16x32_bf16 v[24:27], a[180:183], v[100:103], v[24:27]\n" \
  "v_mfma_f32_16x16x32_bf16 v[28:31], a[184:187], v[100:103], v[28:31]\n" \
  "v_mfma_f32_16x16x32_bf16 v[32:35], a[188:191], v[100:103], v[32:35]\n" \
  "s_add_u32 s84, s87, 7168\n" \
  "buffer_load_dwordx4 v[100:103], v6, s[72:75], s84 offen sc1\n" \
  "s_waitcnt vmcnt(15)\n" \
  "v_mfma_f32_16x16x32_bf16 v[20:23], a[192:195], v[104:107], v[20:23]\n" \
  "ds_read_b64_tr_b16 v[40:41], v16 offset:24576\n" \
  "ds_read_b64_tr_b16 v[42:43], v16 offset:26624\n" \
  "v_mfma_f32_16x16x32_bf16 v[24:27], a[196:199], v[104:107], v[24:27]\n" \
  "ds_read_b64_tr_b16 v[44:45], v17 offset:24576\n" \
  "ds_read_b64_tr_b16 v[46:47], v17 offset:26624\n" \
  "v_mfma_f32_16x16x32_bf16 v[28:31], a[200:203], v[104:107], v[28:31]\n" \
  "ds_read_b64_tr_b16 v[48:49], v18 offset:24576\n" \
  "ds_read_b64_tr_b16 v[50:51], v18 offset:26624\n" \
  "v_mfma_f32_16x16x32_bf16 v[32:35], a[204:207], v[104:107], v[32:35]\n" \
  "ds_read_b64_tr_b16 v[52:53], v19 offset:24576\n" \
  "ds_read_b64_tr_b16 v[54:55], v19 offset:26624\n" \
  "s_add_u32 s84, s87, 16384\n" \
  "buffer_load_dwordx4 v[104:107], v6, s[72:75], s84 offen sc1\n" \
  "s_waitcnt vmcnt(15)\n" \
  "s_waitcnt lgkmcnt(6)\n" \
  "v_mfma_f32_16x16x32_bf16 v[20:23], v[40:43], v[108:111], v[20:23]\n" \
  "s_waitcnt lgkmcnt(4)\n" \
  "v_mfma_f32_16x16x32_bf16 v[24:27], v[44:47], v[108:111], v[24:27]\n" \
  "s_waitcnt lgkmcnt(2)\n" \
  "v_mfma_f32_16x16x32_bf16 v[28:31], v[48:51], v[108:111], v[28:31]\n" \
  "s_waitcnt lgkmcnt(0)\n" \
  "v_mfma_f32_16x16x32_bf16 v[32:35], v[52:55], v[108:111], v[32:35]\n" \
  "s_add_u32 s84, s87, 51200\n" \
  "buffer_load_dwordx4 v[108:111], v6, s[72:75], s84 offen sc1\n" \
  "s_waitcnt vmcnt(15)\n" \
  "v_mfma_f32_16x16x32_bf16 v[20:23], a[208:211], v[112:115], v[20:23]\n" \
  "v_mfma_f32_16x16x32_bf16 v[24:27], a[212:215], v[112:115], v[24:27]\n" \
  "v_mfma_f32_16x16x32_bf16 v[28:31], a[216:219], v[112:115], v[28:31]\n" \
  "v_mfma_f32_16x16x32_bf16 v[32:35], a[220:223], v[112:115], v[32:35]\n" \
  "s_add_u32 s84, s87, 17408\n" \
  "buffer_load_dwordx4 v[112:115], v6, s[72:75], s84 offen sc1\n" \
  "s_waitcnt vmcnt(15)\n" \
  "v_mfma_f32_16x16x32_bf16 v[20:23], a[224:227], v[116:119], v[20:23]\n" \
  "v_mfma_f32_16x16x32_bf16 v[24:27], a[228:231], v[116:119], v[24:27]\n" \
  "v_mfma_f32_16x16x32_bf16 v[28:31], a[232:235], v[116:119], v[28:31]\n" \
  "v_mfma_f32_16x16x32_bf16 v[32:35], a[236:239], v[116:119], v[32:35]\n" \
  "s_add_u32 s84, s87, 18432\n" \
  "buffer_load_dwordx4 v[116:119], v6, s[72:75], s84 offen sc1\n" \
  "s_waitcnt vmcnt(15)\n" \
  "v_mfma_f32_16x16x32_bf16 v[20:23], a[240:243], v[120:123], v[20:23]\n" \
  "ds_read_b64_tr_b16 v[40:41], v16 offset:28672\n" \
  "ds_read_b64_tr_b16 v[42:43], v16 offset:30720\n" \
  "v_mfma_f32_16x16x32_bf16 v[24:27], a[244:247], v[120:123], v[24:27]\n" \
  "ds_read_b64_tr_b16 v[44:45], v17 offset:28672\n" \
  "ds_read_b64_tr_b16 v[46:47], v17 offset:30720\n" \
  "v_mfma_f32_16x16x32_bf16 v[28:31], a[248:251], v[120:123], v[28:31]\n" \
  "ds_read_b64_tr_b16 v[48:49], v18 offset:28672\n" \
  "ds_read_b64_tr_b16 v[50:51], v18 offset:30720\n" \
  "v_mfma_f32_16x16x32_bf16 v[32:35], a[252:255], v[120:123], v[32:35]\n" \
  "ds_read_b64_tr_b16 v[52:53], v19 offset:28672\n" \
  "ds_read_b64_tr_b16 v[54:55], v19 offset:30720\n" \
  "s_add_u32 s84, s87, 19456\n" \
  "buffer_load_dwordx4 v[120:123], v6, s[72:75], s84 offen sc1\n" \
  "s_waitcnt vmcnt(15)\n" \
  "s_waitcnt lgkmcnt(6)\n" \
  "v_mfma_f32_16x16x32_bf16 v[20:23], v[40:43], v[124:127], v[20:23]\n" \
  "s_waitcnt lgkmcnt(4)\n" \
  "v_mfma_f32_16x16x32_bf16 v[24:27], v[44:47], v[124:127], v[24:27]\n" \
  "s_waitcnt lgkmcnt(2)\n" \
  "v_mfma_f32_16x16x32_bf16 v[28:31], v[48:51], v[124:127], v[28:31]\n" \
  "s_waitcnt lgkmcnt(0)\n" \
  "v_mfma_f32_16x16x32_bf16 v[32:35], v[52:55], v[124:127], v[32:35]\n" \
  "s_add_u32 s84, s87, 52224\n" \
  "buffer_load_dwordx4 v[124:127], v6, s[72:75], s84 offen sc1\n" \
  "s_add_u32 s89, s89, 1\n" \
  "s_cmp_eq_u32 s89, 64\n" \
  "v_mov_b64 v[36:37], v[20:21]\n" \
  "v_mov_b64 v[38:39], v[22:23]\n" \
  "ds_write_b128 v12, v[24:27]\n" \
  "ds_write_b128 v13, v[28:31] offset:1024\n" \
  "ds_write_b128 v14, v[32:35] offset:2048\n" \
  "s_cbranch_scc0 LX_%=\n" \
  "s_waitcnt lgkmcnt(0)\n" \
  "s_barrier\n" \
  "ds_read_b128 v[52:55], v11 offset:0\n" \
  "ds_read_b128 v[56:59], v11 offset:1024\n" \
  "ds_read_b128 v[60:63], v11 offset:2048\n" \
  "s_waitcnt lgkmcnt(0)\n" \
  "v_add_f32 v36, v36, v52\n" \
  "v_add_f32 v37, v37, v53\n" \
  "v_add_f32 v38, v38, v54\n" \
  "v_add_f32 v39, v39, v55\n" \
  "v_add_f32 v56, v56, v60\n" \
  "v_add_f32 v57, v57, v61\n" \
  "v_add_f32 v58, v58, v62\n" \
  "v_add_f32 v59, v59, v63\n" \
  "v_add_f32 v36, v36, v56\n" \
  "v_add_f32 v37, v37, v57\n" \
  "v_add_f32 v38, v38, v58\n" \
  "v_add_f32 v39, v39, v59\n" \
  "v_cvt_pk_bf16_f32 v36, v36, v37\n" \
  "v_cvt_pk_bf16_f32 v37, v38, v39\n" \
  "buffer_store_dwordx2 v[36:37], v10, s[76:79], s88 offen\n" \
  "s_add_u32 s88, s88, 131072\n" \
  "v_and_b32 v10, 0x7fffffff, v10\n" \
  "s_waitcnt vmcnt(0)\n" \
  "s_branch LE_%=\n" \
  "LP2_%=:\n" \
  "buffer_load_dword v9, v8, s[80:83], 0 offen sc1\n" \
  "s_waitcnt vmcnt(0)\n" \
  "v_cmp_gt_u32 vcc, s92, v9\n" \
  "s_cbranch_vccz LP3_%=\n" \
  "s_sleep 2\n" \
  "s_add_u32 s91, s91, 1\n" \
  "s_cmp_lt_u32 s91, " STR(SPIN_CAP) "\n" \
  "s_cbranch_scc1 LP2_%=\n" \
  "v_mov_b32 v5, " STR(CW_DEAD) "*4\n" \
  "v_mov_b32 v15, 1\n" \
  "buffer_store_dword v15, v5, s[80:83], 0 offen sc1\n" \
  "s_branch LP3_%=\n" \
  "LE_%=:\n" \
  ""

#define XA_ASM \
  "s_mov_b64 s[68:69], %0\n" \
  "s_and_b32 s69, s69, 0xffff\n" \
  "s_mov_b32 s70, " STR(EX_BYTES32) "\n" \
  "s_mov_b32 s71, 0x00020000\n" \
  "s_mov_b64 s[72:73], %1\n" \
  "s_and_b32 s73, s73, 0xffff\n" \
  "s_mov_b32 s74, 4*" STR(QKV_SZ32) "\n" \
  "s_mov_b32 s75, 0x00020000\n" \
  "s_mov_b64 s[76:77], %2\n" \
  "s_and_b32 s77, s77, 0xffff\n" \
  "s_mov_b32 s78, " STR(CTRL_BYTES) "\n" \
  "s_mov_b32 s79, 0x00020000\n" \
  "v_mbcnt_lo_u32_b32 v4, -1, 0\n" \
  "v_mbcnt_hi_u32_b32 v4, -1, v4\n" \
  "v_and_b32 v116, 15, v4\n" \
  "v_lshrrev_b32 v117, 4, v4\n" \
  "v_lshlrev_b32 v7, 4, v4\n" \
  "v_add_u32 v8, 4096, v7\n" \
  "v_add_u32 v17, 0x40000, v7\n" \
  "v_add_u32 v11, %5, v7\n" \
  "v_lshrrev_b32 v5, 2, v116\n" \
  "v_and_b32 v5, 1, v5\n" \
  "v_lshl_add_u32 v5, v117, 2, v5\n" \
  "v_and_b32 v6, 2, v4\n" \
  "v_add_lshl_u32 v5, v5, v6, 4\n" \
  "v_and_b32 v6, 1, v4\n" \
  "v_lshl_add_u32 v5, v6, 3, v5\n" \
  "v_add_u32 v12, %5, v5\n" \
  "s_lshl_b32 s80, %3, 7\n" \
  "s_add_u32 s81, s80, " STR(CW_XPROG) "*4\n" \
  "v_lshl_add_u32 v15, v4, 24, s81\n" \
  "s_add_u32 s81, s80, " STR(CW_PROG) "*4\n" \
  "v_mov_b32 v13, s81\n" \
  "s_lshl_b32 s80, s80, 1\n" \
  "s_add_u32 s80, s80, " STR(CW_GPROG) "*4\n" \
  "v_writelane_b32 v13, s80, 1\n" \
  "s_add_u32 s80, s80, 128\n" \
  "v_writelane_b32 v13, s80, 2\n" \
  "v_mov_b32 v16, 0\n" \
  "v_mov_b32 v6, 0xff800000\n" \
  "v_lshlrev_b32 v5, 2, v117\n" \
  "v_cmp_gt_u32 vcc, v5, v116\n" \
  "v_mov_b32 v24, 0x3f803f80\n" \
  "v_add_u32 v5, 1, v5\n" \
  "v_cndmask_b32 v20, 0, v6, vcc\n" \
  "v_cmp_gt_u32 vcc, v5, v116\n" \
  "v_mov_b32 v25, 0x3f803f80\n" \
  "v_add_u32 v5, 1, v5\n" \
  "v_cndmask_b32 v21, 0, v6, vcc\n" \
  "v_cmp_gt_u32 vcc, v5, v116\n" \
  "v_mov_b32 v26, 0x3f803f80\n" \
  "v_add_u32 v5, 1, v5\n" \
  "v_cndmask_b32 v22, 0, v6, vcc\n" \
  "v_cmp_gt_u32 vcc, v5, v116\n" \
  "v_mov_b32 v27, 0x3f803f80\n" \
  "v_add_u32 v5, 1, v5\n" \
  "v_cndmask_b32 v23, 0, v6, vcc\n" \
  "v_mov_b64 v[130:131], 0\n" \
  "v_mov_b32 v156, 0\n" \
  "v_mov_b32 v158, 0\n" \
  "s_mov_b32 s89, 0x3e0293ee\n" \
  "s_mov_b32 s88, 0\n" \
  "s_mov_b32 s82, 0\n" \
  "s_mov_b32 s90, 0\n" \
  "s_lshr_b32 s80, s82, 2\n" \
  "s_xnor_b32 s80, s80, %4\n" \
  "s_and_b32 s80, s80, 1\n" \
  "s_lshl_b32 s80, s80, 13\n" \
  "s_lshl_b32 s83, s82, 19\n" \
  "s_or_b32 s83, s83, s80\n" \
  "s_lshl_b32 s80, %3, 14\n" \
  "s_add_u32 s83, s83, s80\n" \
  "s_add_u32 s84, s83, " STR(QKV_SZ32) "\n" \
  "s_add_u32 s85, s84, " STR(QKV_SZ32) "\n" \
  "s_mov_b32 s81, 1\n" \
  "LP0_%=:\n" \
  "buffer_load_dword v14, v13, s[76:79], 0 offen sc1\n" \
  "s_waitcnt vmcnt(0)\n" \
  "v_readlane_b32 s91, v14, 0\n" \
  "v_readlane_b32 s92, v14, 1\n" \
  "v_readlane_b32 s80, v14, 2\n" \
  "s_min_u32 s92, s92, s80\n" \
  "s_cmp_ge_u32 s92, s81\n" \
  "s_cbranch_scc1 LP1_%=\n" \
  "s_sleep 16\n" \
  "s_add_u32 s88, s88, 1\n" \
  "s_cmp_lt_u32 s88, " STR(SPIN_CAP) "\n" \
  "s_cbranch_scc1 LP0_%=\n" \
  "v_mov_b32 v5, " STR(CW_DEAD) "*4\n" \
  "v_mov_b32 v6, 1\n" \
  "buffer_store_dword v6, v5, s[76:79], 0 offen sc1\n" \
  "s_branch LP1_%=\n" \
  "LP1_%=:\n" \
  "buffer_load_dwordx4 a[64:67], v7, s[72:75], s84 offen offset:0 sc1\n" \
  "buffer_load_dwordx4 a[68:71], v7, s[72:75], s84 offen offset:1024 sc1\n" \
  "buffer_load_dwordx4 a[72:75], v7, s[72:75], s84 offen offset:2048 sc1\n" \
  "buffer_load_dwordx4 a[76:79], v7, s[72:75], s84 offen offset:3072 sc1\n" \
  "buffer_load_dwordx4 a[80:83], v8, s[72:75], s84 offen offset:0 sc1\n" \
  "buffer_load_dwordx4 a[84:87], v8, s[72:75], s84 offen offset:1024 sc1\n" \
  "buffer_load_dwordx4 a[88:91], v8, s[72:75], s84 offen offset:2048 sc1\n" \
  "buffer_load_dwordx4 a[92:95], v8, s[72:75], s84 offen offset:3072 sc1\n" \
  "buffer_load_dwordx4 a[0:3], v7, s[72:75], s83 offen offset:0 sc1\n" \
  "buffer_load_dwordx4 a[4:7], v7, s[72:75], s83 offen offset:1024 sc1\n" \
  "buffer_load_dwordx4 a[8:11], v7, s[72:75], s83 offen offset:2048 sc1\n" \
  "buffer_load_dwordx4 a[12:15], v7, s[72:75], s83 offen offset:3072 sc1\n" \
  "buffer_load_dwordx4 a[16:19], v8, s[72:75], s83 offen offset:0 sc1\n" \
  "buffer_load_dwordx4 a[20:23], v8, s[72:75], s83 offen offset:1024 sc1\n" \
  "buffer_load_dwordx4 a[24:27], v8, s[72:75], s83 offen offset:2048 sc1\n" \
  "buffer_load_dwordx4 a[28:31], v8, s[72:75], s83 offen offset:3072 sc1\n" \
  "buffer_load_dwordx4 a[96:99], v7, s[72:75], s85 offen offset:0 sc1\n" \
  "buffer_load_dwordx4 a[100:103], v7, s[72:75], s85 offen offset:1024 sc1\n" \
  "buffer_load_dwordx4 a[104:107], v7, s[72:75], s85 offen offset:2048 sc1\n" \
  "buffer_load_dwordx4 a[108:111], v7, s[72:75], s85 offen offset:3072 sc1\n" \
  "buffer_load_dwordx4 a[112:115], v8, s[72:75], s85 offen offset:0 sc1\n" \
  "buffer_load_dwordx4 a[116:119], v8, s[72:75], s85 offen offset:1024 sc1\n" \
  "buffer_load_dwordx4 a[120:123], v8, s[72:75], s85 offen offset:2048 sc1\n" \
  "buffer_load_dwordx4 a[124:127], v8, s[72:75], s85 offen offset:3072 sc1\n" \
  "s_add_u32 s80, s82, 0\n" \
  "s_lshl_b32 s80, s80, 5\n" \
  "s_add_u32 s80, s80, %3\n" \
  "s_lshl_b32 s87, s80, 14\n" \
  "s_add_u32 s87, s87, " STR(EXP_B) "<<21\n" \
  "s_lshr_b32 s80, s87, 21\n" \
  "s_lshr_b32 s80, %4, s80\n" \
  "s_and_b32 s80, s80, 1\n" \
  "s_lshl_b32 s80, s80, 13\n" \
  "s_or_b32 s87, s87, s80\n" \
  "s_waitcnt vmcnt(0)\n" \
  ".p2align 6\n" \
  "LA_%=:\n" \
  "s_waitcnt vmcnt(24)\n" \
  "s_add_u32 s81, s82, 2\n" \
  "s_min_u32 s81, s81, 64\n" \
  "s_sub_u32 s84, s81, 1\n" \
  "v_mfma_f32_16x16x32_bf16 v[116:119], a[64:67], a[0:3], v[20:23]\n" \
  "s_lshr_b32 s80, s84, 2\n" \
  "s_xnor_b32 s80, s80, %4\n" \
  "v_mfma_f32_16x16x32_bf16 v[120:123], a[64:67], a[16:19], 0\n" \
  "s_and_b32 s80, s80, 1\n" \
  "s_lshl_b32 s80, s80, 13\n" \
  "v_mfma_f32_16x16x32_bf16 v[124:127], a[80:83], a[16:19], v[20:23]\n" \
  "s_lshl_b32 s83, s84, 19\n" \
  "s_or_b32 s83, s83, s80\n" \
  "v_mfma_f32_16x16x32_bf16 v[116:119], a[68:71], a[4:7], v[116:119]\n" \
  "s_lshl_b32 s80, %3, 14\n" \
  "s_add_u32 s83, s83, s80\n" \
  "v_mfma_f32_16x16x32_bf16 v[120:123], a[68:71], a[20:23], v[120:123]\n" \
  "s_add_u32 s84, s83, " STR(QKV_SZ32) "\n" \
  "s_add_u32 s85, s84, " STR(QKV_SZ32) "\n" \
  "v_mfma_f32_16x16x32_bf16 v[124:127], a[84:87], a[20:23], v[124:127]\n" \
  "s_lshr_b32 s80, s82, 2\n" \
  "s_xnor_b32 s80, s80, %4\n" \
  "v_mfma_f32_16x16x32_bf16 v[116:119], a[72:75], a[8:11], v[116:119]\n" \
  "s_and_b32 s80, s80, 1\n" \
  "s_lshl_b32 s80, s80, 13\n" \
  "v_mfma_f32_16x16x32_bf16 v[120:123], a[72:75], a[24:27], v[120:123]\n" \
  "s_lshl_b32 s86, s82, 19\n" \
  "s_or_b32 s86, s86, s80\n" \
  "v_mfma_f32_16x16x32_bf16 v[124:127], a[88:91], a[24:27], v[124:127]\n" \
  "s_lshr_b32 s80, %3, 1\n" \
  "s_lshl_b32 s80, s80, 14\n" \
  "v_mfma_f32_16x16x32_bf16 v[116:119], a[76:79], a[12:15], v[116:119]\n" \
  "s_add_u32 s86, s86, s80\n" \
  "s_and_b32 s80, %3, 1\n" \
  "v_mfma_f32_16x16x32_bf16 v[120:123], a[76:79], a[28:31], v[120:123]\n" \
  "s_lshl_b32 s80, s80, 12\n" \
  "s_add_u32 s86, s86, s80\n" \
  "v_mfma_f32_16x16x32_bf16 v[124:127], a[92:95], a[28:31], v[124:127]\n" \
  "s_add_u32 s86, s86, 3*" STR(QKV_SZ32) "\n" \
  "s_cmp_lt_u32 s92, s81\n" \
  "s_cbranch_scc1 LG0_%=\n" \
  "LH0_%=:\n" \
  "buffer_load_dwordx4 a[64:67], v7, s[72:75], s84 offen offset:0 sc1\n" \
  "buffer_load_dwordx4 a[68:71], v7, s[72:75], s84 offen offset:1024 sc1\n" \
  "buffer_load_dwordx4 a[72:75], v7, s[72:75], s84 offen offset:2048 sc1\n" \
  "buffer_load_dwordx4 a[76:79], v7, s[72:75], s84 offen offset:3072 sc1\n" \
  "buffer_load_dwordx4 a[80:83], v8, s[72:75], s84 offen offset:0 sc1\n" \
  "buffer_load_dwordx4 a[84:87], v8, s[72:75], s84 offen offset:1024 sc1\n" \
  "v_max3_f32 v144, v116, v117, v118\n" \
  "v_max3_f32 v145, v120, v121, v122\n" \
  "v_max_f32 v144, v144, v119\n" \
  "v_max3_f32 v145, v145, v123, v124\n" \
  "v_max3_f32 v145, v145, v125, v126\n" \
  "v_max_f32 v145, v145, v127\n" \
  "v_mov_b32 v5, v144\n" \
  "v_mov_b32 v6, v145\n" \
  "s_nop 0\n" \
  "v_permlane16_swap_b32 v144, v5\n" \
  "v_permlane16_swap_b32 v145, v6\n" \
  "v_max_f32 v144, v144, v5\n" \
  "v_max_f32 v145, v145, v6\n" \
  "v_mov_b32 v5, v144\n" \
  "v_mov_b32 v6, v145\n" \
  "s_nop 0\n" \
  "v_permlane32_swap_b32 v144, v5\n" \
  "v_permlane32_swap_b32 v145, v6\n" \
  "v_max_f32 v144, v144, v5\n" \
  "v_max_f32 v145, v145, v6\n" \
  "v_mul_f32 v144, s89, v144\n" \
  "v_mul_f32 v145, s89, v145\n" \
  "v_fma_f32 v116, v116, s89, -v144\n" \
  "v_exp_f32 v116, v116\n" \
  "buffer_load_dwordx4 a[88:91], v8, s[72:75], s84 offen offset:2048 sc1\n" \
  "v_fma_f32 v117, v117, s89, -v144\n" \
  "v_exp_f32 v117, v117\n" \
  "buffer_load_dwordx4 a[92:95], v8, s[72:75], s84 offen offset:3072 sc1\n" \
  "v_fma_f32 v118, v118, s89, -v144\n" \
  "v_exp_f32 v118, v118\n" \
  "buffer_load_dwordx4 a[32:35], v7, s[72:75], s83 offen offset:0 sc1\n" \
  "v_fma_f32 v119, v119, s89, -v144\n" \
  "v_exp_f32 v119, v119\n" \
  "buffer_load_dwordx4 a[36:39], v7, s[72:75], s83 offen offset:1024 sc1\n" \
  "v_fma_f32 v120, v120, s89, -v145\n" \
  "v_exp_f32 v120, v120\n" \
  "buffer_load_dwordx4 a[40:43], v7, s[72:75], s83 offen offset:2048 sc1\n" \
  "v_fma_f32 v121, v121, s89, -v145\n" \
  "v_exp_f32 v121, v121\n" \
  "buffer_load_dwordx4 a[44:47], v7, s[72:75], s83 offen offset:3072 sc1\n" \
  "v_fma_f32 v122, v122, s89, -v145\n" \
  "v_exp_f32 v122, v122\n" \
  "buffer_load_dwordx4 a[48:51], v8, s[72:75], s83 offen offset:0 sc1\n" \
  "v_fma_f32 v123, v123, s89, -v145\n" \
  "v_exp_f32 v123, v123\n" \
  "buffer_load_dwordx4 a[52:55], v8, s[72:75], s83 offen offset:1024 sc1\n" \
  "v_fma_f32 v124, v124, s89, -v145\n" \
  "v_exp_f32 v124, v124\n" \
  "buffer_load_dwordx4 a[56:59], v8, s[72:75], s83 offen offset:2048 sc1\n" \
  "v_fma_f32 v125, v125, s89, -v145\n" \
  "v_exp_f32 v125, v125\n" \
  "buffer_load_dwordx4 a[60:63], v8, s[72:75], s83 offen offset:3072 sc1\n" \
  "v_fma_f32 v126, v126, s89, -v145\n" \
  "v_exp_f32 v126, v126\n" \
  "v_fma_f32 v127, v127, s89, -v145\n" \
  "v_exp_f32 v127, v127\n" \
  "s_nop 0\n" \
  "v_cvt_pk_bf16_f32 v128, v116, v117\n" \
  "v_cvt_pk_bf16_f32 v129, v118, v119\n" \
  "v_cvt_pk_bf16_f32 v132, v120, v121\n" \
  "v_cvt_pk_bf16_f32 v133, v122, v123\n" \
  "v_cvt_pk_bf16_f32 v134, v124, v125\n" \
  "v_cvt_pk_bf16_f32 v135, v126, v127\n" \
  "s_waitcnt vmcnt(22)\n" \
  "buffer_store_dword v16, v15, s[76:79], 0 offen sc1\n" \
  "v_mfma_f32_16x16x32_bf16 v[136:139], v[24:27], v[128:131], 0\n" \
  "v_mfma_f32_16x16x32_bf16 v[140:143], v[24:27], v[132:135], 0\n" \
  "v_add_u32 v16, 1, v16\n" \
  "v_mfma_f32_16x16x32_bf16 v[52:55], a[96:99], v[128:131], 0\n" \
  "v_mfma_f32_16x16x32_bf16 v[84:87], a[96:99], v[132:135], 0\n" \
  "buffer_load_dwordx4 a[96:99], v7, s[72:75], s85 offen offset:0 sc1\n" \
  "v_mfma_f32_16x16x32_bf16 v[56:59], a[100:103], v[128:131], 0\n" \
  "v_mfma_f32_16x16x32_bf16 v[88:91], a[100:103], v[132:135], 0\n" \
  "buffer_load_dwordx4 a[100:103], v7, s[72:75], s85 offen offset:1024 sc1\n" \
  "v_mfma_f32_16x16x32_bf16 v[60:63], a[104:107], v[128:131], 0\n" \
  "v_mfma_f32_16x16x32_bf16 v[92:95], a[104:107], v[132:135], 0\n" \
  "buffer_load_dwordx4 a[104:107], v7, s[72:75], s85 offen offset:2048 sc1\n" \
  "v_mfma_f32_16x16x32_bf16 v[64:67], a[108:111], v[128:131], 0\n" \
  "v_mfma_f32_16x16x32_bf16 v[96:99], a[108:111], v[132:135], 0\n" \
  "buffer_load_dwordx4 a[108:111], v7, s[72:75], s85 offen offset:3072 sc1\n" \
  "v_mfma_f32_16x16x32_bf16 v[68:71], a[112:115], v[128:131], 0\n" \
  "v_mfma_f32_16x16x32_bf16 v[100:103], a[112:115], v[132:135], 0\n" \
  "buffer_load_dwordx4 a[112:115], v8, s[72:75], s85 offen offset:0 sc1\n" \
  "v_mfma_f32_16x16x32_bf16 v[72:75], a[116:119], v[128:131], 0\n" \
  "v_mfma_f32_16x16x32_bf16 v[104:107], a[116:119], v[132:135], 0\n" \
  "buffer_load_dwordx4 a[116:119], v8, s[72:75], s85 offen offset:1024 sc1\n" \
  "v_mfma_f32_16x16x32_bf16 v[76:79], a[120:123], v[128:131], 0\n" \
  "v_mfma_f32_16x16x32_bf16 v[108:111], a[120:123], v[132:135], 0\n" \
  "buffer_load_dwordx4 a[120:123], v8, s[72:75], s85 offen offset:2048 sc1\n" \
  "v_mfma_f32_16x16x32_bf16 v[80:83], a[124:127], v[128:131], 0\n" \
  "v_mfma_f32_16x16x32_bf16 v[112:115], a[124:127], v[132:135], 0\n" \
  "buffer_load_dwordx4 a[124:127], v8, s[72:75], s85 offen offset:3072 sc1\n" \
  "buffer_load_dword v14, v13, s[76:79], 0 offen sc1\n" \
  "s_cmp_eq_u32 s90, 0\n" \
  "s_cbranch_scc1 LW0_%=\n" \
  "s_waitcnt vmcnt(26)\n" \
  "LM0_%=:\n" \
  "ds_write_b128 v11, v[44:47]\n" \
  "ds_write_b128 v11, v[48:51] offset:1024\n" \
  "v_mfma_f32_16x16x32_bf16 v[148:151], v[28:31], a[0:3], 0\n" \
  "v_max3_u32 v5, v28, v29, v30\n" \
  "v_max3_u32 v5, v5, v31, v32\n" \
  "v_mfma_f32_16x16x32_bf16 v[152:155], v[28:31], a[16:19], 0\n" \
  "v_max3_u32 v5, v5, v33, v34\n" \
  "v_max3_u32 v5, v5, v35, v36\n" \
  "v_mfma_f32_16x16x32_bf16 v[148:151], v[32:35], a[4:7], v[148:151]\n" \
  "v_max3_u32 v5, v5, v37, v38\n" \
  "v_max3_u32 v5, v5, v39, v40\n" \
  "v_mfma_f32_16x16x32_bf16 v[152:155], v[32:35], a[20:23], v[152:155]\n" \
  "v_max3_u32 v5, v5, v41, v42\n" \
  "v_max3_u32 v5, v5, v43, v44\n" \
  "v_mfma_f32_16x16x32_bf16 v[148:151], v[36:39], a[8:11], v[148:151]\n" \
  "v_max3_u32 v5, v5, v45, v46\n" \
  "v_max3_u32 v5, v5, v47, v48\n" \
  "v_mfma_f32_16x16x32_bf16 v[152:155], v[36:39], a[24:27], v[152:155]\n" \
  "v_max3_u32 v5, v5, v49, v50\n" \
  "v_max_u32 v5, v5, v51\n" \
  "v_mfma_f32_16x16x32_bf16 v[148:151], v[40:43], a[12:15], v[148:151]\n" \
  "v_cmp_eq_u32 vcc, -1, v5\n" \
  "v_mfma_f32_16x16x32_bf16 v[152:155], v[40:43], a[28:31], v[152:155]\n" \
  "ds_read_b64_tr_b16 a[128:129], v12 offset:0\n" \
  "ds_read_b64_tr_b16 a[130:131], v12 offset:256\n" \
  "ds_read_b64_tr_b16 a[132:133], v12 offset:512\n" \
  "ds_read_b64_tr_b16 a[134:135], v12 offset:768\n" \
  "ds_read_b64_tr_b16 a[136:137], v12 offset:1024\n" \
  "ds_read_b64_tr_b16 a[138:139], v12 offset:1280\n" \
  "ds_read_b64_tr_b16 a[140:141], v12 offset:1536\n" \
  "ds_read_b64_tr_b16 a[142:143], v12 offset:1792\n" \
  "s_cbranch_vccnz LR0_%=\n" \
  "v_max_f32 v146, v150, v151\n" \
  "v_max_f32 v147, v154, v155\n" \
  "v_mov_b32 v5, v146\n" \
  "v_mov_b32 v6, v147\n" \
  "s_nop 0\n" \
  "v_permlane16_swap_b32 v146, v5\n" \
  "v_permlane16_swap_b32 v147, v6\n" \
  "v_max_f32 v146, v146, v5\n" \
  "v_max_f32 v147, v147, v6\n" \
  "v_mov_b32 v5, v146\n" \
  "v_mov_b32 v6, v147\n" \
  "s_nop 0\n" \
  "v_permlane32_swap_b32 v146, v5\n" \
  "v_permlane32_swap_b32 v147, v6\n" \
  "v_max_f32 v146, v146, v5\n" \
  "v_max_f32 v147, v147, v6\n" \
  "v_mul_f32 v146, s89, v146\n" \
  "v_mul_f32 v147, s89, v147\n" \
  "v_max_f32 v146, v146, v144\n" \
  "v_max_f32 v147, v147, v145\n" \
  "v_sub_f32 v160, v144, v146\n" \
  "v_sub_f32 v162, v145, v147\n" \
  "v_fma_f32 v150, v150, s89, -v146\n" \
  "v_fma_f32 v151, v151, s89, -v146\n" \
  "v_fma_f32 v154, v154, s89, -v147\n" \
  "v_fma_f32 v155, v155, s89, -v147\n" \
  "v_exp_f32 v150, v150\n" \
  "v_exp_f32 v151, v151\n" \
  "v_exp_f32 v154, v154\n" \
  "v_exp_f32 v155, v155\n" \
  "v_exp_f32 v160, v160\n" \
  "v_exp_f32 v162, v162\n" \
  "v_add_f32 v146, v150, v151\n" \
  "v_add_f32 v147, v154, v155\n" \
  "v_mov_b32 v5, v146\n" \
  "v_mov_b32 v6, v147\n" \
  "s_nop 0\n" \
  "v_permlane16_swap_b32 v146, v5\n" \
  "v_permlane16_swap_b32 v147, v6\n" \
  "v_add_f32 v146, v146, v5\n" \
  "v_add_f32 v147, v147, v6\n" \
  "v_mov_b32 v5, v146\n" \
  "v_mov_b32 v6, v147\n" \
  "s_nop 0\n" \
  "v_permlane32_swap_b32 v146, v5\n" \
  "v_permlane32_swap_b32 v147, v6\n" \
  "v_add_f32 v146, v146, v5\n" \
  "v_add_f32 v147, v147, v6\n" \
  "v_fma_f32 v146, v160, v136, v146\n" \
  "v_fma_f32 v147, v162, v140, v147\n" \
  "v_rcp_f32 v146, v146\n" \
  "v_rcp_f32 v147, v147\n" \
  "v_mul_f32 v160, v160, v146\n" \
  "v_mul_f32 v162, v162, v147\n" \
  "v_mul_f32 v150, v150, v146\n" \
  "v_mul_f32 v151, v151, v146\n" \
  "v_mul_f32 v154, v154, v147\n" \
  "v_mul_f32 v155, v155, v147\n" \
  "v_cvt_pk_bf16_f32 v157, v150, v151\n" \
  "v_cvt_pk_bf16_f32 v159, v154, v155\n" \
  "v_pk_mul_f32 v[52:53], v[52:53], v[160:161] op_sel_hi:[1,0]\n" \
  "v_pk_mul_f32 v[54:55], v[54:55], v[160:161] op_sel_hi:[1,0]\n" \
  "v_pk_mul_f32 v[56:57], v[56:57], v[160:161] op_sel_hi:[1,0]\n" \
  "v_pk_mul_f32 v[58:59], v[58:59], v[160:161] op_sel_hi:[1,0]\n" \
  "v_pk_mul_f32 v[60:61], v[60:61], v[160:161] op_sel_hi:[1,0]\n" \
  "v_pk_mul_f32 v[62:63], v[62:63], v[160:161] op_sel_hi:[1,0]\n" \
  "v_pk_mul_f32 v[64:65], v[64:65], v[160:161] op_sel_hi:[1,0]\n" \
  "v_pk_mul_f32 v[66:67], v[66:67], v[160:161] op_sel_hi:[1,0]\n" \
  "v_pk_mul_f32 v[68:69], v[68:69], v[160:161] op_sel_hi:[1,0]\n" \
  "v_pk_mul_f32 v[70:71], v[70:71], v[160:161] op_sel_hi:[1,0]\n" \
  "v_pk_mul_f32 v[72:73], v[72:73], v[160:161] op_sel_hi:[1,0]\n" \
  "v_pk_mul_f32 v[74:75], v[74:75], v[160:161] op_sel_hi:[1,0]\n" \
  "v_pk_mul_f32 v[76:77], v[76:77], v[160:161] op_sel_hi:[1,0]\n" \
  "v_pk_mul_f32 v[78:79], v[78:79], v[160:161] op_sel_hi:[1,0]\n" \
  "v_pk_mul_f32 v[80:81], v[80:81], v[160:161] op_sel_hi:[1,0]\n" \
  "v_pk_mul_f32 v[82:83], v[82:83], v[160:161] op_sel_hi:[1,0]\n" \
  "v_pk_mul_f32 v[84:85], v[84:85], v[162:163] op_sel_hi:[1,0]\n" \
  "v_pk_mul_f32 v[86:87], v[86:87], v[162:163] op_sel_hi:[1,0]\n" \
  "v_pk_mul_f32 v[88:89], v[88:89], v[162:163] op_sel_hi:[1,0]\n" \
  "v_pk_mul_f32 v[90:91], v[90:91], v[162:163] op_sel_hi:[1,0]\n" \
  "v_pk_mul_f32 v[92:93], v[92:93], v[162:163] op_sel_hi:[1,0]\n" \
  "v_pk_mul_f32 v[94:95], v[94:95], v[162:163] op_sel_hi:[1,0]\n" \
  "v_pk_mul_f32 v[96:97], v[96:97], v[162:163] op_sel_hi:[1,0]\n" \
  "v_pk_mul_f32 v[98:99], v[98:99], v[162:163] op_sel_hi:[1,0]\n" \
  "v_pk_mul_f32 v[100:101], v[100:101], v[162:163] op_sel_hi:[1,0]\n" \
  "v_pk_mul_f32 v[102:103], v[102:103], v[162:163] op_sel_hi:[1,0]\n" \
  "v_pk_mul_f32 v[104:105], v[104:105], v[162:163] op_sel_hi:[1,0]\n" \
  "v_pk_mul_f32 v[106:107], v[106:107], v[162:163] op_sel_hi:[1,0]\n" \
  "v_pk_mul_f32 v[108:109], v[108:109], v[162:163] op_sel_hi:[1,0]\n" \
  "v_pk_mul_f32 v[110:111], v[110:111], v[162:163] op_sel_hi:[1,0]\n" \
  "v_pk_mul_f32 v[112:113], v[112:113], v[162:163] op_sel_hi:[1,0]\n" \
  "v_pk_mul_f32 v[114:115], v[114:115], v[162:163] op_sel_hi:[1,0]\n" \
  "s_waitcnt lgkmcnt(0)\n" \
  "v_mfma_f32_16x16x16_bf16 v[52:55], a[128:129], v[156:157], v[52:55]\n" \
  "v_mfma_f32_16x16x16_bf16 v[56:59], a[130:131], v[156:157], v[56:59]\n" \
  "v_mfma_f32_16x16x16_bf16 v[60:63], a[132:133], v[156:157], v[60:63]\n" \
  "v_mfma_f32_16x16x16_bf16 v[64:67], a[134:135], v[156:157], v[64:67]\n" \
  "v_mfma_f32_16x16x16_bf16 v[68:71], a[136:137], v[156:157], v[68:71]\n" \
  "v_cvt_pk_bf16_f32 v164, v52, v53\n" \
  "v_cvt_pk_bf16_f32 v165, v54, v55\n" \
  "v_mfma_f32_16x16x16_bf16 v[72:75], a[138:139], v[156:157], v[72:75]\n" \
  "v_cvt_pk_bf16_f32 v166, v56, v57\n" \
  "v_cvt_pk_bf16_f32 v167, v58, v59\n" \
  "buffer_store_dwordx4 v[164:167], v7, s[72:75], s86 offen offset:0 sc1\n" \
  "v_mfma_f32_16x16x16_bf16 v[76:79], a[140:141], v[156:157], v[76:79]\n" \
  "v_cvt_pk_bf16_f32 v168, v60, v61\n" \
  "v_cvt_pk_bf16_f32 v169, v62, v63\n" \
  "v_mfma_f32_16x16x16_bf16 v[80:83], a[142:143], v[156:157], v[80:83]\n" \
  "v_cvt_pk_bf16_f32 v170, v64, v65\n" \
  "v_cvt_pk_bf16_f32 v171, v66, v67\n" \
  "buffer_store_dwordx4 v[168:171], v7, s[72:75], s86 offen offset:1024 sc1\n" \
  "v_mfma_f32_16x16x16_bf16 v[84:87], a[128:129], v[158:159], v[84:87]\n" \
  "v_cvt_pk_bf16_f32 v172, v68, v69\n" \
  "v_cvt_pk_bf16_f32 v173, v70, v71\n" \
  "v_mfma_f32_16x16x16_bf16 v[88:91], a[130:131], v[158:159], v[88:91]\n" \
  "v_cvt_pk_bf16_f32 v174, v72, v73\n" \
  "v_cvt_pk_bf16_f32 v175, v74, v75\n" \
  "buffer_store_dwordx4 v[172:175], v7, s[72:75], s86 offen offset:2048 sc1\n" \
  "v_mfma_f32_16x16x16_bf16 v[92:95], a[132:133], v[158:159], v[92:95]\n" \
  "v_cvt_pk_bf16_f32 v176, v76, v77\n" \
  "v_cvt_pk_bf16_f32 v177, v78, v79\n" \
  "v_mfma_f32_16x16x16_bf16 v[96:99], a[134:135], v[158:159], v[96:99]\n" \
  "v_cvt_pk_bf16_f32 v178, v80, v81\n" \
  "v_cvt_pk_bf16_f32 v179, v82, v83\n" \
  "buffer_store_dwordx4 v[176:179], v7, s[72:75], s86 offen offset:3072 sc1\n" \
  "v_mfma_f32_16x16x16_bf16 v[100:103], a[136:137], v[158:159], v[100:103]\n" \
  "v_cvt_pk_bf16_f32 v180, v84, v85\n" \
  "v_cvt_pk_bf16_f32 v181, v86, v87\n" \
  "v_mfma_f32_16x16x16_bf16 v[104:107], a[138:139], v[158:159], v[104:107]\n" \
  "v_cvt_pk_bf16_f32 v182, v88, v89\n" \
  "v_cvt_pk_bf16_f32 v183, v90, v91\n" \
  "buffer_store_dwordx4 v[180:183], v17, s[72:75], s86 offen offset:0 sc1\n" \
  "v_mfma_f32_16x16x16_bf16 v[108:111], a[140:141], v[158:159], v[108:111]\n" \
  "v_cvt_pk_bf16_f32 v184, v92, v93\n" \
  "v_cvt_pk_bf16_f32 v185, v94, v95\n" \
  "v_mfma_f32_16x16x16_bf16 v[112:115], a[142:143], v[158:159], v[112:115]\n" \
  "v_cvt_pk_bf16_f32 v186, v96, v97\n" \
  "v_cvt_pk_bf16_f32 v187, v98, v99\n" \
  "buffer_store_dwordx4 v[184:187], v17, s[72:75], s86 offen offset:1024 sc1\n" \
  "v_cvt_pk_bf16_f32 v188, v100, v101\n" \
  "v_cvt_pk_bf16_f32 v189, v102, v103\n" \
  "v_cvt_pk_bf16_f32 v190, v104, v105\n" \
  "v_cvt_pk_bf16_f32 v191, v106, v107\n" \
  "buffer_store_dwordx4 v[188:191], v17, s[72:75], s86 offen offset:2048 sc1\n" \
  "s_nop 1\n" \
  "v_cvt_pk_bf16_f32 v192, v108, v109\n" \
  "v_cvt_pk_bf16_f32 v193, v110, v111\n" \
  "v_cvt_pk_bf16_f32 v194, v112, v113\n" \
  "v_cvt_pk_bf16_f32 v195, v114, v115\n" \
  "buffer_store_dwordx4 v[192:195], v17, s[72:75], s86 offen offset:3072 sc1\n" \
  "s_add_u32 s82, s82, 1\n" \
  "s_cmp_eq_u32 s82, 64\n" \
  "s_cbranch_scc1 LZ_%=\n" \
  "s_add_u32 s80, s82, 0\n" \
  "s_lshl_b32 s80, s80, 5\n" \
  "s_add_u32 s80, s80, %3\n" \
  "s_lshl_b32 s87, s80, 14\n" \
  "s_add_u32 s87, s87, " STR(EXP_B) "<<21\n" \
  "s_lshr_b32 s80, s87, 21\n" \
  "s_lshr_b32 s80, %4, s80\n" \
  "s_and_b32 s80, s80, 1\n" \
  "s_lshl_b32 s80, s80, 13\n" \
  "s_or_b32 s87, s87, s80\n" \
  "s_waitcnt vmcnt(8)\n" \
  "v_readlane_b32 s91, v14, 0\n" \
  "v_readlane_b32 s92, v14, 1\n" \
  "v_readlane_b32 s80, v14, 2\n" \
  "s_min_u32 s92, s92, s80\n" \
  "s_cmp_gt_u32 s91, s82\n" \
  "s_cselect_b32 s90, 1, 0\n" \
  "s_cselect_b32 s80, 0, 0x80000000\n" \
  "v_or_b32 v9, s80, v7\n" \
  "v_or_b32 v10, s80, v8\n" \
  "buffer_load_dwordx4 v[28:31], v9, s[68:71], s87 offen offset:0 sc1\n" \
  "buffer_load_dwordx4 v[32:35], v9, s[68:71], s87 offen offset:1024 sc1\n" \
  "buffer_load_dwordx4 v[36:39], v9, s[68:71], s87 offen offset:2048 sc1\n" \
  "buffer_load_dwordx4 v[40:43], v9, s[68:71], s87 offen offset:3072 sc1\n" \
  "buffer_load_dwordx4 v[44:47], v10, s[68:71], s87 offen offset:0 sc1\n" \
  "buffer_load_dwordx4 v[48:51], v10, s[68:71], s87 offen offset:1024 sc1\n" \
  "s_waitcnt vmcnt(24)\n" \
  "s_add_u32 s81, s82, 2\n" \
  "s_min_u32 s81, s81, 64\n" \
  "s_sub_u32 s84, s81, 1\n" \
  "v_mfma_f32_16x16x32_bf16 v[116:119], a[64:67], a[32:35], v[20:23]\n" \
  "s_lshr_b32 s80, s84, 2\n" \
  "s_xnor_b32 s80, s80, %4\n" \
  "v_mfma_f32_16x16x32_bf16 v[120:123], a[64:67], a[48:51], 0\n" \
  "s_and_b32 s80, s80, 1\n" \
  "s_lshl_b32 s80, s80, 13\n" \
  "v_mfma_f32_16x16x32_bf16 v[124:127], a[80:83], a[48:51], v[20:23]\n" \
  "s_lshl_b32 s83, s84, 19\n" \
  "s_or_b32 s83, s83, s80\n" \
  "v_mfma_f32_16x16x32_bf16 v[116:119], a[68:71], a[36:39], v[116:119]\n" \
  "s_lshl_b32 s80, %3, 14\n" \
  "s_add_u32 s83, s83, s80\n" \
  "v_mfma_f32_16x16x32_bf16 v[120:123], a[68:71], a[52:55], v[120:123]\n" \
  "s_add_u32 s84, s83, " STR(QKV_SZ32) "\n" \
  "s_add_u32 s85, s84, " STR(QKV_SZ32) "\n" \
  "v_mfma_f32_16x16x32_bf16 v[124:127], a[84:87], a[52:55], v[124:127]\n" \
  "s_lshr_b32 s80, s82, 2\n" \
  "s_xnor_b32 s80, s80, %4\n" \
  "v_mfma_f32_16x16x32_bf16 v[116:119], a[72:75], a[40:43], v[116:119]\n" \
  "s_and_b32 s80, s80, 1\n" \
  "s_lshl_b32 s80, s80, 13\n" \
  "v_mfma_f32_16x16x32_bf16 v[120:123], a[72:75], a[56:59], v[120:123]\n" \
  "s_lshl_b32 s86, s82, 19\n" \
  "s_or_b32 s86, s86, s80\n" \
  "v_mfma_f32_16x16x32_bf16 v[124:127], a[88:91], a[56:59], v[124:127]\n" \
  "s_lshr_b32 s80, %3, 1\n" \
  "s_lshl_b32 s80, s80, 14\n" \
  "v_mfma_f32_16x16x32_bf16 v[116:119], a[76:79], a[44:47], v[116:119]\n" \
  "s_add_u32 s86, s86, s80\n" \
  "s_and_b32 s80, %3, 1\n" \
  "v_mfma_f32_16x16x32_bf16 v[120:123], a[76:79], a[60:63], v[120:123]\n" \
  "s_lshl_b32 s80, s80, 12\n" \
  "s_add_u32 s86, s86, s80\n" \
  "v_mfma_f32_16x16x32_bf16 v[124:127], a[92:95], a[60:63], v[124:127]\n" \
  "s_add_u32 s86, s86, 3*" STR(QKV_SZ32) "\n" \
  "s_cmp_lt_u32 s92, s81\n" \
  "s_cbranch_scc1 LG1_%=\n" \
  "LH1_%=:\n" \
  "buffer_load_dwordx4 a[64:67], v7, s[72:75], s84 offen offset:0 sc1\n" \
  "buffer_load_dwordx4 a[68:71], v7, s[72:75], s84 offen offset:1024 sc1\n" \
  "buffer_load_dwordx4 a[72:75], v7, s[72:75], s84 offen offset:2048 sc1\n" \
  "buffer_load_dwordx4 a[76:79], v7, s[72:75], s84 offen offset:3072 sc1\n" \
  "buffer_load_dwordx4 a[80:83], v8, s[72:75], s84 offen offset:0 sc1\n" \
  "buffer_load_dwordx4 a[84:87], v8, s[72:75], s84 offen offset:1024 sc1\n" \
  "v_max3_f32 v144, v116, v117, v118\n" \
  "v_max3_f32 v145, v120, v121, v122\n" \
  "v_max_f32 v144, v144, v119\n" \
  "v_max3_f32 v145, v145, v123, v124\n" \
  "v_max3_f32 v145, v145, v125, v126\n" \
  "v_max_f32 v145, v145, v127\n" \
  "v_mov_b32 v5, v144\n" \
  "v_mov_b32 v6, v145\n" \
  "s_nop 0\n" \
  "v_permlane16_swap_b32 v144, v5\n" \
  "v_permlane16_swap_b32 v145, v6\n" \
  "v_max_f32 v144, v144, v5\n" \
  "v_max_f32 v145, v145, v6\n" \
  "v_mov_b32 v5, v144\n" \
  "v_mov_b32 v6, v145\n" \
  "s_nop 0\n" \
  "v_permlane32_swap_b32 v144, v5\n" \
  "v_permlane32_swap_b32 v145, v6\n" \
  "v_max_f32 v144, v144, v5\n" \
  "v_max_f32 v145, v145, v6\n" \
  "v_mul_f32 v144, s89, v144\n" \
  "v_mul_f32 v145, s89, v145\n" \
  "v_fma_f32 v116, v116, s89, -v144\n" \
  "v_exp_f32 v116, v116\n" \
  "buffer_load_dwordx4 a[88:91], v8, s[72:75], s84 offen offset:2048 sc1\n" \
  "v_fma_f32 v117, v117, s89, -v144\n" \
  "v_exp_f32 v117, v117\n" \
  "buffer_load_dwordx4 a[92:95], v8, s[72:75], s84 offen offset:3072 sc1\n" \
  "v_fma_f32 v118, v118, s89, -v144\n" \
  "v_exp_f32 v118, v118\n" \
  "buffer_load_dwordx4 a[0:3], v7, s[72:75], s83 offen offset:0 sc1\n" \
  "v_fma_f32 v119, v119, s89, -v144\n" \
  "v_exp_f32 v119, v119\n" \
  "buffer_load_dwordx4 a[4:7], v7, s[72:75], s83 offen offset:1024 sc1\n" \
  "v_fma_f32 v120, v120, s89, -v145\n" \
  "v_exp_f32 v120, v120\n" \
  "buffer_load_dwordx4 a[8:11], v7, s[72:75], s83 offen offset:2048 sc1\n" \
  "v_fma_f32 v121, v121, s89, -v145\n" \
  "v_exp_f32 v121, v121\n" \
  "buffer_load_dwordx4 a[12:15], v7, s[72:75], s83 offen offset:3072 sc1\n" \
  "v_fma_f32 v122, v122, s89, -v145\n" \
  "v_exp_f32 v122, v122\n" \
  "buffer_load_dwordx4 a[16:19], v8, s[72:75], s83 offen offset:0 sc1\n" \
  "v_fma_f32 v123, v123, s89, -v145\n" \
  "v_exp_f32 v123, v123\n" \
  "buffer_load_dwordx4 a[20:23], v8, s[72:75], s83 offen offset:1024 sc1\n" \
  "v_fma_f32 v124, v124, s89, -v145\n" \
  "v_exp_f32 v124, v124\n" \
  "buffer_load_dwordx4 a[24:27], v8, s[72:75], s83 offen offset:2048 sc1\n" \
  "v_fma_f32 v125, v125, s89, -v145\n" \
  "v_exp_f32 v125, v125\n" \
  "buffer_load_dwordx4 a[28:31], v8, s[72:75], s83 offen offset:3072 sc1\n" \
  "v_fma_f32 v126, v126, s89, -v145\n" \
  "v_exp_f32 v126, v126\n" \
  "v_fma_f32 v127, v127, s89, -v145\n" \
  "v_exp_f32 v127, v127\n" \
  "s_nop 0\n" \
  "v_cvt_pk_bf16_f32 v128, v116, v117\n" \
  "v_cvt_pk_bf16_f32 v129, v118, v119\n" \
  "v_cvt_pk_bf16_f32 v132, v120, v121\n" \
  "v_cvt_pk_bf16_f32 v133, v122, v123\n" \
  "v_cvt_pk_bf16_f32 v134, v124, v125\n" \
  "v_cvt_pk_bf16_f32 v135, v126, v127\n" \
  "s_waitcnt vmcnt(22)\n" \
  "buffer_store_dword v16, v15, s[76:79], 0 offen sc1\n" \
  "v_mfma_f32_16x16x32_bf16 v[136:139], v[24:27], v[128:131], 0\n" \
  "v_mfma_f32_16x16x32_bf16 v[140:143], v[24:27], v[132:135], 0\n" \
  "v_add_u32 v16, 1, v16\n" \
  "v_mfma_f32_16x16x32_bf16 v[52:55], a[96:99], v[128:131], 0\n" \
  "v_mfma_f32_16x16x32_bf16 v[84:87], a[96:99], v[132:135], 0\n" \
  "buffer_load_dwordx4 a[96:99], v7, s[72:75], s85 offen offset:0 sc1\n" \
  "v_mfma_f32_16x16x32_bf16 v[56:59], a[100:103], v[128:131], 0\n" \
  "v_mfma_f32_16x16x32_bf16 v[88:91], a[100:103], v[132:135], 0\n" \
  "buffer_load_dwordx4 a[100:103], v7, s[72:75], s85 offen offset:1024 sc1\n" \
  "v_mfma_f32_16x16x32_bf16 v[60:63], a[104:107], v[128:131], 0\n" \
  "v_mfma_f32_16x16x32_bf16 v[92:95], a[104:107], v[132:135], 0\n" \
  "buffer_load_dwordx4 a[104:107], v7, s[72:75], s85 offen offset:2048 sc1\n" \
  "v_mfma_f32_16x16x32_bf16 v[64:67], a[108:111], v[128:131], 0\n" \
  "v_mfma_f32_16x16x32_bf16 v[96:99], a[108:111], v[132:135], 0\n" \
  "buffer_load_dwordx4 a[108:111], v7, s[72:75], s85 offen offset:3072 sc1\n" \
  "v_mfma_f32_16x16x32_bf16 v[68:71], a[112:115], v[128:131], 0\n" \
  "v_mfma_f32_16x16x32_bf16 v[100:103], a[112:115], v[132:135], 0\n" \
  "buffer_load_dwordx4 a[112:115], v8, s[72:75], s85 offen offset:0 sc1\n" \
  "v_mfma_f32_16x16x32_bf16 v[72:75], a[116:119], v[128:131], 0\n" \
  "v_mfma_f32_16x16x32_bf16 v[104:107], a[116:119], v[132:135], 0\n" \
  "buffer_load_dwordx4 a[116:119], v8, s[72:75], s85 offen offset:1024 sc1\n" \
  "v_mfma_f32_16x16x32_bf16 v[76:79], a[120:123], v[128:131], 0\n" \
  "v_mfma_f32_16x16x32_bf16 v[108:111], a[120:123], v[132:135], 0\n" \
  "buffer_load_dwordx4 a[120:123], v8, s[72:75], s85 offen offset:2048 sc1\n" \
  "v_mfma_f32_16x16x32_bf16 v[80:83], a[124:127], v[128:131], 0\n" \
  "v_mfma_f32_16x16x32_bf16 v[112:115], a[124:127], v[132:135], 0\n" \
  "buffer_load_dwordx4 a[124:127], v8, s[72:75], s85 offen offset:3072 sc1\n" \
  "buffer_load_dword v14, v13, s[76:79], 0 offen sc1\n" \
  "s_cmp_eq_u32 s90, 0\n" \
  "s_cbranch_scc1 LW1_%=\n" \
  "s_waitcnt vmcnt(26)\n" \
  "LM1_%=:\n" \
  "ds_write_b128 v11, v[44:47]\n" \
  "ds_write_b128 v11, v[48:51] offset:1024\n" \
  "v_mfma_f32_16x16x32_bf16 v[148:151], v[28:31], a[32:35], 0\n" \
  "v_max3_u32 v5, v28, v29, v30\n" \
  "v_max3_u32 v5, v5, v31, v32\n" \
  "v_mfma_f32_16x16x32_bf16 v[152:155], v[28:31], a[48:51], 0\n" \
  "v_max3_u32 v5, v5, v33, v34\n" \
  "v_max3_u32 v5, v5, v35, v36\n" \
  "v_mfma_f32_16x16x32_bf16 v[148:151], v[32:35], a[36:39], v[148:151]\n" \
  "v_max3_u32 v5, v5, v37, v38\n" \
  "v_max3_u32 v5, v5, v39, v40\n" \
  "v_mfma_f32_16x16x32_bf16 v[152:155], v[32:35], a[52:55], v[152:155]\n" \
  "v_max3_u32 v5, v5, v41, v42\n" \
  "v_max3_u32 v5, v5, v43, v44\n" \
  "v_mfma_f32_16x16x32_bf16 v[148:151], v[36:39], a[40:43], v[148:151]\n" \
  "v_max3_u32 v5, v5, v45, v46\n" \
  "v_max3_u32 v5, v5, v47, v48\n" \
  "v_mfma_f32_16x16x32_bf16 v[152:155], v[36:39], a[56:59], v[152:155]\n" \
  "v_max3_u32 v5, v5, v49, v50\n" \
  "v_max_u32 v5, v5, v51\n" \
  "v_mfma_f32_16x16x32_bf16 v[148:151], v[40:43], a[44:47], v[148:151]\n" \
  "v_cmp_eq_u32 vcc, -1, v5\n" \
  "v_mfma_f32_16x16x32_bf16 v[152:155], v[40:43], a[60:63], v[152:155]\n" \
  "ds_read_b64_tr_b16 a[128:129], v12 offset:0\n" \
  "ds_read_b64_tr_b16 a[130:131], v12 offset:256\n" \
  "ds_read_b64_tr_b16 a[132:133], v12 offset:512\n" \
  "ds_read_b64_tr_b16 a[134:135], v12 offset:768\n" \
  "ds_read_b64_tr_b16 a[136:137], v12 offset:1024\n" \
  "ds_read_b64_tr_b16 a[138:139], v12 offset:1280\n" \
  "ds_read_b64_tr_b16 a[140:141], v12 offset:1536\n" \
  "ds_read_b64_tr_b16 a[142:143], v12 offset:1792\n" \
  "s_cbranch_vccnz LR1_%=\n" \
  "v_max_f32 v146, v150, v151\n" \
  "v_max_f32 v147, v154, v155\n" \
  "v_mov_b32 v5, v146\n" \
  "v_mov_b32 v6, v147\n" \
  "s_nop 0\n" \
  "v_permlane16_swap_b32 v146, v5\n" \
  "v_permlane16_swap_b32 v147, v6\n" \
  "v_max_f32 v146, v146, v5\n" \
  "v_max_f32 v147, v147, v6\n" \
  "v_mov_b32 v5, v146\n" \
  "v_mov_b32 v6, v147\n" \
  "s_nop 0\n" \
  "v_permlane32_swap_b32 v146, v5\n" \
  "v_permlane32_swap_b32 v147, v6\n" \
  "v_max_f32 v146, v146, v5\n" \
  "v_max_f32 v147, v147, v6\n" \
  "v_mul_f32 v146, s89, v146\n" \
  "v_mul_f32 v147, s89, v147\n" \
  "v_max_f32 v146, v146, v144\n" \
  "v_max_f32 v147, v147, v145\n" \
  "v_sub_f32 v160, v144, v146\n" \
  "v_sub_f32 v162, v145, v147\n" \
  "v_fma_f32 v150, v150, s89, -v146\n" \
  "v_fma_f32 v151, v151, s89, -v146\n" \
  "v_fma_f32 v154, v154, s89, -v147\n" \
  "v_fma_f32 v155, v155, s89, -v147\n" \
  "v_exp_f32 v150, v150\n" \
  "v_exp_f32 v151, v151\n" \
  "v_exp_f32 v154, v154\n" \
  "v_exp_f32 v155, v155\n" \
  "v_exp_f32 v160, v160\n" \
  "v_exp_f32 v162, v162\n" \
  "v_add_f32 v146, v150, v151\n" \
  "v_add_f32 v147, v154, v155\n" \
  "v_mov_b32 v5, v146\n" \
  "v_mov_b32 v6, v147\n" \
  "s_nop 0\n" \
  "v_permlane16_swap_b32 v146, v5\n" \
  "v_permlane16_swap_b32 v147, v6\n" \
  "v_add_f32 v146, v146, v5\n" \
  "v_add_f32 v147, v147, v6\n" \
  "v_mov_b32 v5, v146\n" \
  "v_mov_b32 v6, v147\n" \
  "s_nop 0\n" \
  "v_permlane32_swap_b32 v146, v5\n" \
  "v_permlane32_swap_b32 v147, v6\n" \
  "v_add_f32 v146, v146, v5\n" \
  "v_add_f32 v147, v147, v6\n" \
  "v_fma_f32 v146, v160, v136, v146\n" \
  "v_fma_f32 v147, v162, v140, v147\n" \
  "v_rcp_f32 v146, v146\n" \
  "v_rcp_f32 v147, v147\n" \
  "v_mul_f32 v160, v160, v146\n" \
  "v_mul_f32 v162, v162, v147\n" \
  "v_mul_f32 v150, v150, v146\n" \
  "v_mul_f32 v151, v151, v146\n" \
  "v_mul_f32 v154, v154, v147\n" \
  "v_mul_f32 v155, v155, v147\n" \
  "v_cvt_pk_bf16_f32 v157, v150, v151\n" \
  "v_cvt_pk_bf16_f32 v159, v154, v155\n" \
  "v_pk_mul_f32 v[52:53], v[52:53], v[160:161] op_sel_hi:[1,0]\n" \
  "v_pk_mul_f32 v[54:55], v[54:55], v[160:161] op_sel_hi:[1,0]\n" \
  "v_pk_mul_f32 v[56:57], v[56:57], v[160:161] op_sel_hi:[1,0]\n" \
  "v_pk_mul_f32 v[58:59], v[58:59], v[160:161] op_sel_hi:[1,0]\n" \
  "v_pk_mul_f32 v[60:61], v[60:61], v[160:161] op_sel_hi:[1,0]\n" \
  "v_pk_mul_f32 v[62:63], v[62:63], v[160:161] op_sel_hi:[1,0]\n" \
  "v_pk_mul_f32 v[64:65], v[64:65], v[160:161] op_sel_hi:[1,0]\n" \
  "v_pk_mul_f32 v[66:67], v[66:67], v[160:161] op_sel_hi:[1,0]\n" \
  "v_pk_mul_f32 v[68:69], v[68:69], v[160:161] op_sel_hi:[1,0]\n" \
  "v_pk_mul_f32 v[70:71], v[70:71], v[160:161] op_sel_hi:[1,0]\n" \
  "v_pk_mul_f32 v[72:73], v[72:73], v[160:161] op_sel_hi:[1,0]\n" \
  "v_pk_mul_f32 v[74:75], v[74:75], v[160:161] op_sel_hi:[1,0]\n" \
  "v_pk_mul_f32 v[76:77], v[76:77], v[160:161] op_sel_hi:[1,0]\n" \
  "v_pk_mul_f32 v[78:79], v[78:79], v[160:161] op_sel_hi:[1,0]\n" \
  "v_pk_mul_f32 v[80:81], v[80:81], v[160:161] op_sel_hi:[1,0]\n" \
  "v_pk_mul_f32 v[82:83], v[82:83], v[160:161] op_sel_hi:[1,0]\n" \
  "v_pk_mul_f32 v[84:85], v[84:85], v[162:163] op_sel_hi:[1,0]\n" \
  "v_pk_mul_f32 v[86:87], v[86:87], v[162:163] op_sel_hi:[1,0]\n" \
  "v_pk_mul_f32 v[88:89], v[88:89], v[162:163] op_sel_hi:[1,0]\n" \
  "v_pk_mul_f32 v[90:91], v[90:91], v[162:163] op_sel_hi:[1,0]\n" \
  "v_pk_mul_f32 v[92:93], v[92:93], v[162:163] op_sel_hi:[1,0]\n" \
  "v_pk_mul_f32 v[94:95], v[94:95], v[162:163] op_sel_hi:[1,0]\n" \
  "v_pk_mul_f32 v[96:97], v[96:97], v[162:163] op_sel_hi:[1,0]\n" \
  "v_pk_mul_f32 v[98:99], v[98:99], v[162:163] op_sel_hi:[1,0]\n" \
  "v_pk_mul_f32 v[100:101], v[100:101], v[162:163] op_sel_hi:[1,0]\n" \
  "v_pk_mul_f32 v[102:103], v[102:103], v[162:163] op_sel_hi:[1,0]\n" \
  "v_pk_mul_f32 v[104:105], v[104:105], v[162:163] op_sel_hi:[1,0]\n" \
  "v_pk_mul_f32 v[106:107], v[106:107], v[162:163] op_sel_hi:[1,0]\n" \
  "v_pk_mul_f32 v[108:109], v[108:109], v[162:163] op_sel_hi:[1,0]\n" \
  "v_pk_mul_f32 v[110:111], v[110:111], v[162:163] op_sel_hi:[1,0]\n" \
  "v_pk_mul_f32 v[112:113], v[112:113], v[162:163] op_sel_hi:[1,0]\n" \
  "v_pk_mul_f32 v[114:115], v[114:115], v[162:163] op_sel_hi:[1,0]\n" \
  "s_waitcnt lgkmcnt(0)\n" \
  "v_mfma_f32_16x16x16_bf16 v[52:55], a[128:129], v[156:157], v[52:55]\n" \
  "v_mfma_f32_16x16x16_bf16 v[56:59], a[130:131], v[156:157], v[56:59]\n" \
  "v_mfma_f32_16x16x16_bf16 v[60:63], a[132:133], v[156:157], v[60:63]\n" \
  "v_mfma_f32_16x16x16_bf16 v[64:67], a[134:135], v[156:157], v[64:67]\n" \
  "v_mfma_f32_16x16x16_bf16 v[68:71], a[136:137], v[156:157], v[68:71]\n" \
  "v_cvt_pk_bf16_f32 v164, v52, v53\n" \
  "v_cvt_pk_bf16_f32 v165, v54, v55\n" \
  "v_mfma_f32_16x16x16_bf16 v[72:75], a[138:139], v[156:157], v[72:75]\n" \
  "v_cvt_pk_bf16_f32 v166, v56, v57\n" \
  "v_cvt_pk_bf16_f32 v167, v58, v59\n" \
  "buffer_store_dwordx4 v[164:167], v7, s[72:75], s86 offen offset:0 sc1\n" \
  "v_mfma_f32_16x16x16_bf16 v[76:79], a[140:141], v[156:157], v[76:79]\n" \
  "v_cvt_pk_bf16_f32 v168, v60, v61\n" \
  "v_cvt_pk_bf16_f32 v169, v62, v63\n" \
  "v_mfma_f32_16x16x16_bf16 v[80:83], a[142:143], v[156:157], v[80:83]\n" \
  "v_cvt_pk_bf16_f32 v170, v64, v65\n" \
  "v_cvt_pk_bf16_f32 v171, v66, v67\n" \
  "buffer_store_dwordx4 v[168:171], v7, s[72:75], s86 offen offset:1024 sc1\n" \
  "v_mfma_f32_16x16x16_bf16 v[84:87], a[128:129], v[158:159], v[84:87]\n" \
  "v_cvt_pk_bf16_f32 v172, v68, v69\n" \
  "v_cvt_pk_bf16_f32 v173, v70, v71\n" \
  "v_mfma_f32_16x16x16_bf16 v[88:91], a[130:131], v[158:159], v[88:91]\n" \
  "v_cvt_pk_bf16_f32 v174, v72, v73\n" \
  "v_cvt_pk_bf16_f32 v175, v74, v75\n" \
  "buffer_store_dwordx4 v[172:175], v7, s[72:75], s86 offen offset:2048 sc1\n" \
  "v_mfma_f32_16x16x16_bf16 v[92:95], a[132:133], v[158:159], v[92:95]\n" \
  "v_cvt_pk_bf16_f32 v176, v76, v77\n" \
  "v_cvt_pk_bf16_f32 v177, v78, v79\n" \
  "v_mfma_f32_16x16x16_bf16 v[96:99], a[134:135], v[158:159], v[96:99]\n" \
  "v_cvt_pk_bf16_f32 v178, v80, v81\n" \
  "v_cvt_pk_bf16_f32 v179, v82, v83\n" \
  "buffer_store_dwordx4 v[176:179], v7, s[72:75], s86 offen offset:3072 sc1\n" \
  "v_mfma_f32_16x16x16_bf16 v[100:103], a[136:137], v[158:159], v[100:103]\n" \
  "v_cvt_pk_bf16_f32 v180, v84, v85\n" \
  "v_cvt_pk_bf16_f32 v181, v86, v87\n" \
  "v_mfma_f32_16x16x16_bf16 v[104:107], a[138:139], v[158:159], v[104:107]\n" \
  "v_cvt_pk_bf16_f32 v182, v88, v89\n" \
  "v_cvt_pk_bf16_f32 v183, v90, v91\n" \
  "buffer_store_dwordx4 v[180:183], v17, s[72:75], s86 offen offset:0 sc1\n" \
  "v_mfma_f32_16x16x16_bf16 v[108:111], a[140:141], v[158:159], v[108:111]\n" \
  "v_cvt_pk_bf16_f32 v184, v92, v93\n" \
  "v_cvt_pk_bf16_f32 v185, v94, v95\n" \
  "v_mfma_f32_16x16x16_bf16 v[112:115], a[142:143], v[158:159], v[112:115]\n" \
  "v_cvt_pk_bf16_f32 v186, v96, v97\n" \
  "v_cvt_pk_bf16_f32 v187, v98, v99\n" \
  "buffer_store_dwordx4 v[184:187], v17, s[72:75], s86 offen offset:1024 sc1\n" \
  "v_cvt_pk_bf16_f32 v188, v100, v101\n" \
  "v_cvt_pk_bf16_f32 v189, v102, v103\n" \
  "v_cvt_pk_bf16_f32 v190, v104, v105\n" \
  "v_cvt_pk_bf16_f32 v191, v106, v107\n" \
  "buffer_store_dwordx4 v[188:191], v17, s[72:75], s86 offen offset:2048 sc1\n" \
  "s_nop 1\n" \
  "v_cvt_pk_bf16_f32 v192, v108, v109\n" \
  "v_cvt_pk_bf16_f32 v193, v110, v111\n" \
  "v_cvt_pk_bf16_f32 v194, v112, v113\n" \
  "v_cvt_pk_bf16_f32 v195, v114, v115\n" \
  "buffer_store_dwordx4 v[192:195], v17, s[72:75], s86 offen offset:3072 sc1\n" \
  "s_add_u32 s82, s82, 1\n" \
  "s_cmp_eq_u32 s82, 64\n" \
  "s_cbranch_scc1 LZ_%=\n" \
  "s_add_u32 s80, s82, 0\n" \
  "s_lshl_b32 s80, s80, 5\n" \
  "s_add_u32 s80, s80, %3\n" \
  "s_lshl_b32 s87, s80, 14\n" \
  "s_add_u32 s87, s87, " STR(EXP_B) "<<21\n" \
  "s_lshr_b32 s80, s87, 21\n" \
  "s_lshr_b32 s80, %4, s80\n" \
  "s_and_b32 s80, s80, 1\n" \
  "s_lshl_b32 s80, s80, 13\n" \
  "s_or_b32 s87, s87, s80\n" \
  "s_waitcnt vmcnt(8)\n" \
  "v_readlane_b32 s91, v14, 0\n" \
  "v_readlane_b32 s92, v14, 1\n" \
  "v_readlane_b32 s80, v14, 2\n" \
  "s_min_u32 s92, s92, s80\n" \
  "s_cmp_gt_u32 s91, s82\n" \
  "s_cselect_b32 s90, 1, 0\n" \
  "s_cselect_b32 s80, 0, 0x80000000\n" \
  "v_or_b32 v9, s80, v7\n" \
  "v_or_b32 v10, s80, v8\n" \
  "buffer_load_dwordx4 v[28:31], v9, s[68:71], s87 offen offset:0 sc1\n" \
  "buffer_load_dwordx4 v[32:35], v9, s[68:71], s87 offen offset:1024 sc1\n" \
  "buffer_load_dwordx4 v[36:39], v9, s[68:71], s87 offen offset:2048 sc1\n" \
  "buffer_load_dwordx4 v[40:43], v9, s[68:71], s87 offen offset:3072 sc1\n" \
  "buffer_load_dwordx4 v[44:47], v10, s[68:71], s87 offen offset:0 sc1\n" \
  "buffer_load_dwordx4 v[48:51], v10, s[68:71], s87 offen offset:1024 sc1\n" \
  "s_branch LA_%=\n" \
  "LG0_%=:\n" \
  "buffer_load_dword v14, v13, s[76:79], 0 offen sc1\n" \
  "s_waitcnt vmcnt(0)\n" \
  "v_readlane_b32 s91, v14, 0\n" \
  "v_readlane_b32 s92, v14, 1\n" \
  "v_readlane_b32 s80, v14, 2\n" \
  "s_min_u32 s92, s92, s80\n" \
  "s_cmp_ge_u32 s92, s81\n" \
  "s_cbranch_scc1 LH0_%=\n" \
  "s_sleep 4\n" \
  "s_add_u32 s88, s88, 1\n" \
  "s_cmp_lt_u32 s88, " STR(SPIN_CAP) "\n" \
  "s_cbranch_scc1 LG0_%=\n" \
  "v_mov_b32 v5, " STR(CW_DEAD) "*4\n" \
  "v_mov_b32 v6, 1\n" \
  "buffer_store_dword v6, v5, s[76:79], 0 offen sc1\n" \
  "s_branch LH0_%=\n" \
  "LW0_%=:\n" \
  "s_add_u32 s81, s82, 1\n" \
  "s_min_u32 s81, s81, 63\n" \
  "LQ0_%=:\n" \
  "buffer_load_dword v14, v13, s[76:79], 0 offen sc1\n" \
  "s_waitcnt vmcnt(0)\n" \
  "v_readlane_b32 s91, v14, 0\n" \
  "v_readlane_b32 s92, v14, 1\n" \
  "v_readlane_b32 s80, v14, 2\n" \
  "s_min_u32 s92, s92, s80\n" \
  "s_cmp_ge_u32 s91, s81\n" \
  "s_cbranch_scc1 LR0_%=\n" \
  "s_sleep 2\n" \
  "s_add_u32 s88, s88, 1\n" \
  "s_cmp_lt_u32 s88, " STR(SPIN_CAP) "\n" \
  "s_cbranch_scc1 LQ0_%=\n" \
  "v_mov_b32 v5, " STR(CW_DEAD) "*4\n" \
  "v_mov_b32 v6, 1\n" \
  "buffer_store_dword v6, v5, s[76:79], 0 offen sc1\n" \
  "s_branch LR0_%=\n" \
  "LR0_%=:\n" \
  "buffer_load_dwordx4 v[28:31], v7, s[68:71], s87 offen offset:0 sc1\n" \
  "buffer_load_dwordx4 v[32:35], v7, s[68:71], s87 offen offset:1024 sc1\n" \
  "buffer_load_dwordx4 v[36:39], v7, s[68:71], s87 offen offset:2048 sc1\n" \
  "buffer_load_dwordx4 v[40:43], v7, s[68:71], s87 offen offset:3072 sc1\n" \
  "buffer_load_dwordx4 v[44:47], v8, s[68:71], s87 offen offset:0 sc1\n" \
  "buffer_load_dwordx4 v[48:51], v8, s[68:71], s87 offen offset:1024 sc1\n" \
  "s_waitcnt vmcnt(0)\n" \
  "v_max3_u32 v5, v28, v29, v30\n" \
  "v_max3_u32 v5, v5, v31, v32\n" \
  "v_max3_u32 v5, v5, v33, v34\n" \
  "v_max3_u32 v5, v5, v35, v36\n" \
  "v_max3_u32 v5, v5, v37, v38\n" \
  "v_max3_u32 v5, v5, v39, v40\n" \
  "v_max3_u32 v5, v5, v41, v42\n" \
  "v_max3_u32 v5, v5, v43, v44\n" \
  "v_max3_u32 v5, v5, v45, v46\n" \
  "v_max3_u32 v5, v5, v47, v48\n" \
  "v_max3_u32 v5, v5, v49, v50\n" \
  "v_max_u32 v5, v5, v51\n" \
  "v_cmp_eq_u32 vcc, -1, v5\n" \
  "s_cbranch_vccz LU0_%=\n" \
  "s_sleep 1\n" \
  "s_add_u32 s88, s88, 1\n" \
  "s_cmp_lt_u32 s88, " STR(SPIN_CAP) "\n" \
  "s_cbranch_scc1 LR0_%=\n" \
  "v_mov_b32 v5, " STR(CW_DEAD) "*4\n" \
  "v_mov_b32 v6, 1\n" \
  "buffer_store_dword v6, v5, s[76:79], 0 offen sc1\n" \
  "s_branch LV0_%=\n" \
  "LV0_%=:\n" \
  "v_mov_b64 v[28:29], 0\n" \
  "v_mov_b64 v[30:31], 0\n" \
  "v_mov_b64 v[32:33], 0\n" \
  "v_mov_b64 v[34:35], 0\n" \
  "v_mov_b64 v[36:37], 0\n" \
  "v_mov_b64 v[38:39], 0\n" \
  "v_mov_b64 v[40:41], 0\n" \
  "v_mov_b64 v[42:43], 0\n" \
  "v_mov_b64 v[44:45], 0\n" \
  "v_mov_b64 v[46:47], 0\n" \
  "v_mov_b64 v[48:49], 0\n" \
  "v_mov_b64 v[50:51], 0\n" \
  "LU0_%=:\n" \
  "s_nop 1\n" \
  "s_branch LM0_%=\n" \
  "LG1_%=:\n" \
  "buffer_load_dword v14, v13, s[76:79], 0 offen sc1\n" \
  "s_waitcnt vmcnt(0)\n" \
  "v_readlane_b32 s91, v14, 0\n" \
  "v_readlane_b32 s92, v14, 1\n" \
  "v_readlane_b32 s80, v14, 2\n" \
  "s_min_u32 s92, s92, s80\n" \
  "s_cmp_ge_u32 s92, s81\n" \
  "s_cbranch_scc1 LH1_%=\n" \
  "s_sleep 4\n" \
  "s_add_u32 s88, s88, 1\n" \
  "s_cmp_lt_u32 s88, " STR(SPIN_CAP) "\n" \
  "s_cbranch_scc1 LG1_%=\n" \
  "v_mov_b32 v5, " STR(CW_DEAD) "*4\n" \
  "v_mov_b32 v6, 1\n" \
  "buffer_store_dword v6, v5, s[76:79], 0 offen sc1\n" \
  "s_branch LH1_%=\n" \
  "LW1_%=:\n" \
  "s_add_u32 s81, s82, 1\n" \
  "s_min_u32 s81, s81, 63\n" \
  "LQ1_%=:\n" \
  "buffer_load_dword v14, v13, s[76:79], 0 offen sc1\n" \
  "s_waitcnt vmcnt(0)\n" \
  "v_readlane_b32 s91, v14, 0\n" \
  "v_readlane_b32 s92, v14, 1\n" \
  "v_readlane_b32 s80, v14, 2\n" \
  "s_min_u32 s92, s92, s80\n" \
  "s_cmp_ge_u32 s91, s81\n" \
  "s_cbranch_scc1 LR1_%=\n" \
  "s_sleep 2\n" \
  "s_add_u32 s88, s88, 1\n" \
  "s_cmp_lt_u32 s88, " STR(SPIN_CAP) "\n" \
  "s_cbranch_scc1 LQ1_%=\n" \
  "v_mov_b32 v5, " STR(CW_DEAD) "*4\n" \
  "v_mov_b32 v6, 1\n" \
  "buffer_store_dword v6, v5, s[76:79], 0 offen sc1\n" \
  "s_branch LR1_%=\n" \
  "LR1_%=:\n" \
  "buffer_load_dwordx4 v[28:31], v7, s[68:71], s87 offen offset:0 sc1\n" \
  "buffer_load_dwordx4 v[32:35], v7, s[68:71], s87 offen offset:1024 sc1\n" \
  "buffer_load_dwordx4 v[36:39], v7, s[68:71], s87 offen offset:2048 sc1\n" \
  "buffer_load_dwordx4 v[40:43], v7, s[68:71], s87 offen offset:3072 sc1\n" \
  "buffer_load_dwordx4 v[44:47], v8, s[68:71], s87 offen offset:0 sc1\n" \
  "buffer_load_dwordx4 v[48:51], v8, s[68:71], s87 offen offset:1024 sc1\n" \
  "s_waitcnt vmcnt(0)\n" \
  "v_max3_u32 v5, v28, v29, v30\n" \
  "v_max3_u32 v5, v5, v31, v32\n" \
  "v_max3_u32 v5, v5, v33, v34\n" \
  "v_max3_u32 v5, v5, v35, v36\n" \
  "v_max3_u32 v5, v5, v37, v38\n" \
  "v_max3_u32 v5, v5, v39, v40\n" \
  "v_max3_u32 v5, v5, v41, v42\n" \
  "v_max3_u32 v5, v5, v43, v44\n" \
  "v_max3_u32 v5, v5, v45, v46\n" \
  "v_max3_u32 v5, v5, v47, v48\n" \
  "v_max3_u32 v5, v5, v49, v50\n" \
  "v_max_u32 v5, v5, v51\n" \
  "v_cmp_eq_u32 vcc, -1, v5\n" \
  "s_cbranch_vccz LU1_%=\n" \
  "s_sleep 1\n" \
  "s_add_u32 s88, s88, 1\n" \
  "s_cmp_lt_u32 s88, " STR(SPIN_CAP) "\n" \
  "s_cbranch_scc1 LR1_%=\n" \
  "v_mov_b32 v5, " STR(CW_DEAD) "*4\n" \
  "v_mov_b32 v6, 1\n" \
  "buffer_store_dword v6, v5, s[76:79], 0 offen sc1\n" \
  "s_branch LV1_%=\n" \
  "LV1_%=:\n" \
  "v_mov_b64 v[28:29], 0\n" \
  "v_mov_b64 v[30:31], 0\n" \
  "v_mov_b64 v[32:33], 0\n" \
  "v_mov_b64 v[34:35], 0\n" \
  "v_mov_b64 v[36:37], 0\n" \
  "v_mov_b64 v[38:39], 0\n" \
  "v_mov_b64 v[40:41], 0\n" \
  "v_mov_b64 v[42:43], 0\n" \
  "v_mov_b64 v[44:45], 0\n" \
  "v_mov_b64 v[46:47], 0\n" \
  "v_mov_b64 v[48:49], 0\n" \
  "v_mov_b64 v[50:51], 0\n" \
  "LU1_%=:\n" \
  "s_nop 1\n" \
  "s_branch LM1_%=\n" \
  "LZ_%=:\n" \
  "s_waitcnt vmcnt(0)\n" \
  "buffer_store_dword v16, v15, s[76:79], 0 offen sc1\n" \
  "s_waitcnt vmcnt(0)\n" \
  ""

#define CONV_ASM \
  "s_waitcnt lgkmcnt(0)\n" \
  "ds_read_b64 a[0:1], %2 offset:32\n" \
  "ds_read_b64 a[2:3], %2 offset:40\n" \
  "ds_read_b128 a[4:7], %3 offset:5040\n" \
  "ds_read_b128 a[8:11], %3 offset:2480\n" \
  "ds_read_b64 a[12:13], %2 offset:2080\n" \
  "ds_read_b64 a[14:15], %2 offset:2088\n" \
  "ds_read_b64 a[16:17], %2 offset:0\n" \
  "ds_read_b64 a[18:19], %2 offset:8\n" \
  "ds_read_b128 a[20:23], %3 offset:5072\n" \
  "ds_read_b128 a[24:27], %3 offset:2512\n" \
  "ds_read_b64 a[28:29], %2 offset:2048\n" \
  "ds_read_b64 a[30:31], %2 offset:2056\n" \
  "s_waitcnt lgkmcnt(6)\n" \
  "v_mfma_f32_32x32x16_bf16 %1, a[0:3], a[4:7], 0\n" \
  "ds_read_b64 a[32:33], %2 offset:96\n" \
  "ds_read_b64 a[34:35], %2 offset:104\n" \
  "v_mfma_f32_32x32x16_bf16 %0, a[0:3], a[8:11], 0\n" \
  "ds_read_b128 a[36:39], %3 offset:4960\n" \
  "ds_read_b128 a[40:43], %3 offset:2400\n" \
  "v_mfma_f32_32x32x16_bf16 %1, a[12:15], a[8:11], %1\n" \
  "ds_read_b64 a[44:45], %2 offset:2144\n" \
  "ds_read_b64 a[46:47], %2 offset:2152\n" \
  "s_waitcnt lgkmcnt(6)\n" \
  "v_mfma_f32_32x32x16_bf16 %1, a[16:19], a[20:23], %1\n" \
  "ds_read_b64 a[0:1], %2 offset:64\n" \
  "ds_read_b64 a[2:3], %2 offset:72\n" \
  "v_mfma_f32_32x32x16_bf16 %0, a[16:19], a[24:27], %0\n" \
  "ds_read_b128 a[4:7], %3 offset:4992\n" \
  "ds_read_b128 a[8:11], %3 offset:2432\n" \
  "v_mfma_f32_32x32x16_bf16 %1, a[28:31], a[24:27], %1\n" \
  "ds_read_b64 a[12:13], %2 offset:2112\n" \
  "ds_read_b64 a[14:15], %2 offset:2120\n" \
  "s_waitcnt lgkmcnt(6)\n" \
  "v_mfma_f32_32x32x16_bf16 %1, a[32:35], a[36:39], %1\n" \
  "ds_read_b64 a[16:17], %2 offset:160\n" \
  "ds_read_b64 a[18:19], %2 offset:168\n" \
  "v_mfma_f32_32x32x16_bf16 %0, a[32:35], a[40:43], %0\n" \
  "ds_read_b128 a[20:23], %3 offset:4880\n" \
  "ds_read_b128 a[24:27], %3 offset:2320\n" \
  "v_mfma_f32_32x32x16_bf16 %1, a[44:47], a[40:43], %1\n" \
  "ds_read_b64 a[28:29], %2 offset:2208\n" \
  "ds_read_b64 a[30:31], %2 offset:2216\n" \
  "s_waitcnt lgkmcnt(6)\n" \
  "v_mfma_f32_32x32x16_bf16 %1, a[0:3], a[4:7], %1\n" \
  "ds_read_b64 a[32:33], %2 offset:128\n" \
  "ds_read_b64 a[34:35], %2 offset:136\n" \
  "v_mfma_f32_32x32x16_bf16 %0, a[0:3], a[8:11], %0\n" \
  "ds_read_b128 a[36:39], %3 offset:4912\n" \
  "ds_read_b128 a[40:43], %3 offset:2352\n" \
  "v_mfma_f32_32x32x16_bf16 %1, a[12:15], a[8:11], %1\n" \
  "ds_read_b64 a[44:45], %2 offset:2176\n" \
  "ds_read_b64 a[46:47], %2 offset:2184\n" \
  "s_waitcnt lgkmcnt(6)\n" \
  "v_mfma_f32_32x32x16_bf16 %1, a[16:19], a[20:23], %1\n" \
  "ds_read_b64 a[0:1], %2 offset:224\n" \
  "ds_read_b64 a[2:3], %2 offset:232\n" \
  "v_mfma_f32_32x32x16_bf16 %0, a[16:19], a[24:27], %0\n" \
  "ds_read_b128 a[4:7], %3 offset:4800\n" \
  "ds_read_b128 a[8:11], %3 offset:2240\n" \
  "v_mfma_f32_32x32x16_bf16 %1, a[28:31], a[24:27], %1\n" \
  "ds_read_b64 a[12:13], %2 offset:2272\n" \
  "ds_read_b64 a[14:15], %2 offset:2280\n" \
  "s_waitcnt lgkmcnt(6)\n" \
  "v_mfma_f32_32x32x16_bf16 %1, a[32:35], a[36:39], %1\n" \
  "ds_read_b64 a[16:17], %2 offset:192\n" \
  "ds_read_b64 a[18:19], %2 offset:200\n" \
  "v_mfma_f32_32x32x16_bf16 %0, a[32:35], a[40:43], %0\n" \
  "ds_read_b128 a[20:23], %3 offset:4832\n" \
  "ds_read_b128 a[24:27], %3 offset:2272\n" \
  "v_mfma_f32_32x32x16_bf16 %1, a[44:47], a[40:43], %1\n" \
  "ds_read_b64 a[28:29], %2 offset:2240\n" \
  "ds_read_b64 a[30:31], %2 offset:2248\n" \
  "s_waitcnt lgkmcnt(6)\n" \
  "v_mfma_f32_32x32x16_bf16 %1, a[0:3], a[4:7], %1\n" \
  "ds_read_b64 a[32:33], %2 offset:288\n" \
  "ds_read_b64 a[34:35], %2 offset:296\n" \
  "v_mfma_f32_32x32x16_bf16 %0, a[0:3], a[8:11], %0\n" \
  "ds_read_b128 a[36:39], %3 offset:4720\n" \
  "ds_read_b128 a[40:43], %3 offset:2160\n" \
  "v_mfma_f32_32x32x16_bf16 %1, a[12:15], a[8:11], %1\n" \
  "ds_read_b64 a[44:45], %2 offset:2336\n" \
  "ds_read_b64 a[46:47], %2 offset:2344\n" \
  "s_waitcnt lgkmcnt(6)\n" \
  "v_mfma_f32_32x32x16_bf16 %1, a[16:19], a[20:23], %1\n" \
  "ds_read_b64 a[0:1], %2 offset:256\n" \
  "ds_read_b64 a[2:3], %2 offset:264\n" \
  "v_mfma_f32_32x32x16_bf16 %0, a[16:19], a[24:27], %0\n" \
  "ds_read_b128 a[4:7], %3 offset:4752\n" \
  "ds_read_b128 a[8:11], %3 offset:2192\n" \
  "v_mfma_f32_32x32x16_bf16 %1, a[28:31], a[24:27], %1\n" \
  "ds_read_b64 a[12:13], %2 offset:2304\n" \
  "ds_read_b64 a[14:15], %2 offset:2312\n" \
  "s_waitcnt lgkmcnt(6)\n" \
  "v_mfma_f32_32x32x16_bf16 %1, a[32:35], a[36:39], %1\n" \
  "ds_read_b64 a[16:17], %2 offset:352\n" \
  "ds_read_b64 a[18:19], %2 offset:360\n" \
  "v_mfma_f32_32x32x16_bf16 %0, a[32:35], a[40:43], %0\n" \
  "ds_read_b128 a[20:23], %3 offset:4640\n" \
  "ds_read_b128 a[24:27], %3 offset:2080\n" \
  "v_mfma_f32_32x32x16_bf16 %1, a[44:47], a[40:43], %1\n" \
  "ds_read_b64 a[28:29], %2 offset:2400\n" \
  "ds_read_b64 a[30:31], %2 offset:2408\n" \
  "s_waitcnt lgkmcnt(6)\n" \
  "v_mfma_f32_32x32x16_bf16 %1, a[0:3], a[4:7], %1\n" \
  "ds_read_b64 a[32:33], %2 offset:320\n" \
  "ds_read_b64 a[34:35], %2 offset:328\n" \
  "v_mfma_f32_32x32x16_bf16 %0, a[0:3], a[8:11], %0\n" \
  "ds_read_b128 a[36:39], %3 offset:4672\n" \
  "ds_read_b128 a[40:43], %3 offset:2112\n" \
  "v_mfma_f32_32x32x16_bf16 %1, a[12:15], a[8:11], %1\n" \
  "ds_read_b64 a[44:45], %2 offset:2368\n" \
  "ds_read_b64 a[46:47], %2 offset:2376\n" \
  "s_waitcnt lgkmcnt(6)\n" \
  "v_mfma_f32_32x32x16_bf16 %1, a[16:19], a[20:23], %1\n" \
  "ds_read_b64 a[0:1], %2 offset:416\n" \
  "ds_read_b64 a[2:3], %2 offset:424\n" \
  "v_mfma_f32_32x32x16_bf16 %0, a[16:19], a[24:27], %0\n" \
  "ds_read_b128 a[4:7], %3 offset:4560\n" \
  "ds_read_b128 a[8:11], %3 offset:2000\n" \
  "v_mfma_f32_32x32x16_bf16 %1, a[28:31], a[24:27], %1\n" \
  "ds_read_b64 a[12:13], %2 offset:2464\n" \
  "ds_read_b64 a[14:15], %2 offset:2472\n" \
  "s_waitcnt lgkmcnt(6)\n" \
  "v_mfma_f32_32x32x16_bf16 %1, a[32:35], a[36:39], %1\n" \
  "ds_read_b64 a[16:17], %2 offset:384\n" \
  "ds_read_b64 a[18:19], %2 offset:392\n" \
  "v_mfma_f32_32x32x16_bf16 %0, a[32:35], a[40:43], %0\n" \
  "ds_read_b128 a[20:23], %3 offset:4592\n" \
  "ds_read_b128 a[24:27], %3 offset:2032\n" \
  "v_mfma_f32_32x32x16_bf16 %1, a[44:47], a[40:43], %1\n" \
  "ds_read_b64 a[28:29], %2 offset:2432\n" \
  "ds_read_b64 a[30:31], %2 offset:2440\n" \
  "s_waitcnt lgkmcnt(6)\n" \
  "v_mfma_f32_32x32x16_bf16 %1, a[0:3], a[4:7], %1\n" \
  "ds_read_b64 a[32:33], %2 offset:480\n" \
  "ds_read_b64 a[34:35], %2 offset:488\n" \
  "v_mfma_f32_32x32x16_bf16 %0, a[0:3], a[8:11], %0\n" \
  "ds_read_b128 a[36:39], %3 offset:4480\n" \
  "ds_read_b128 a[40:43], %3 offset:1920\n" \
  "v_mfma_f32_32x32x16_bf16 %1, a[12:15], a[8:11], %1\n" \
  "ds_read_b64 a[44:45], %2 offset:2528\n" \
  "ds_read_b64 a[46:47], %2 offset:2536\n" \
  "s_waitcnt lgkmcnt(6)\n" \
  "v_mfma_f32_32x32x16_bf16 %1, a[16:19], a[20:23], %1\n" \
  "ds_read_b64 a[0:1], %2 offset:448\n" \
  "ds_read_b64 a[2:3], %2 offset:456\n" \
  "v_mfma_f32_32x32x16_bf16 %0, a[16:19], a[24:27], %0\n" \
  "ds_read_b128 a[4:7], %3 offset:4512\n" \
  "ds_read_b128 a[8:11], %3 offset:1952\n" \
  "v_mfma_f32_32x32x16_bf16 %1, a[28:31], a[24:27], %1\n" \
  "ds_read_b64 a[12:13], %2 offset:2496\n" \
  "ds_read_b64 a[14:15], %2 offset:2504\n" \
  "s_waitcnt lgkmcnt(6)\n" \
  "v_mfma_f32_32x32x16_bf16 %1, a[32:35], a[36:39], %1\n" \
  "ds_read_b64 a[16:17], %2 offset:544\n" \
  "ds_read_b64 a[18:19], %2 offset:552\n" \
  "v_mfma_f32_32x32x16_bf16 %0, a[32:35], a[40:43], %0\n" \
  "ds_read_b128 a[20:23], %3 offset:4400\n" \
  "ds_read_b128 a[24:27], %3 offset:1840\n" \
  "v_mfma_f32_32x32x16_bf16 %1, a[44:47], a[40:43], %1\n" \
  "ds_read_b64 a[28:29], %2 offset:2592\n" \
  "ds_read_b64 a[30:31], %2 offset:2600\n" \
  "s_waitcnt lgkmcnt(6)\n" \
  "v_mfma_f32_32x32x16_bf16 %1, a[0:3], a[4:7], %1\n" \
  "ds_read_b64 a[32:33], %2 offset:512\n" \
  "ds_read_b64 a[34:35], %2 offset:520\n" \
  "v_mfma_f32_32x32x16_bf16 %0, a[0:3], a[8:11], %0\n" \
  "ds_read_b128 a[36:39], %3 offset:4432\n" \
  "ds_read_b128 a[40:43], %3 offset:1872\n" \
  "v_mfma_f32_32x32x16_bf16 %1, a[12:15], a[8:11], %1\n" \
  "ds_read_b64 a[44:45], %2 offset:2560\n" \
  "ds_read_b64 a[46:47], %2 offset:2568\n" \
  "s_waitcnt lgkmcnt(6)\n" \
  "v_mfma_f32_32x32x16_bf16 %1, a[16:19], a[20:23], %1\n" \
  "ds_read_b64 a[0:1], %2 offset:608\n" \
  "ds_read_b64 a[2:3], %2 offset:616\n" \
  "v_mfma_f32_32x32x16_bf16 %0, a[16:19], a[24:27], %0\n" \
  "ds_read_b128 a[4:7], %3 offset:4320\n" \
  "ds_read_b128 a[8:11], %3 offset:1760\n" \
  "v_mfma_f32_32x32x16_bf16 %1, a[28:31], a[24:27], %1\n" \
  "ds_read_b64 a[12:13], %2 offset:2656\n" \
  "ds_read_b64 a[14:15], %2 offset:2664\n" \
  "s_waitcnt lgkmcnt(6)\n" \
  "v_mfma_f32_32x32x16_bf16 %1, a[32:35], a[36:39], %1\n" \
  "ds_read_b64 a[16:17], %2 offset:576\n" \
  "ds_read_b64 a[18:19], %2 offset:584\n" \
  "v_mfma_f32_32x32x16_bf16 %0, a[32:35], a[40:43], %0\n" \
  "ds_read_b128 a[20:23], %3 offset:4352\n" \
  "ds_read_b128 a[24:27], %3 offset:1792\n" \
  "v_mfma_f32_32x32x16_bf16 %1, a[44:47], a[40:43], %1\n" \
  "ds_read_b64 a[28:29], %2 offset:2624\n" \
  "ds_read_b64 a[30:31], %2 offset:2632\n" \
  "s_waitcnt lgkmcnt(6)\n" \
  "v_mfma_f32_32x32x16_bf16 %1, a[0:3], a[4:7], %1\n" \
  "ds_read_b64 a[32:33], %2 offset:672\n" \
  "ds_read_b64 a[34:35], %2 offset:680\n" \
  "v_mfma_f32_32x32x16_bf16 %0, a[0:3], a[8:11], %0\n" \
  "ds_read_b128 a[36:39], %3 offset:4240\n" \
  "ds_read_b128 a[40:43], %3 offset:1680\n" \
  "v_mfma_f32_32x32x16_bf16 %1, a[12:15], a[8:11], %1\n" \
  "ds_read_b64 a[44:45], %2 offset:2720\n" \
  "ds_read_b64 a[46:47], %2 offset:2728\n" \
  "s_waitcnt lgkmcnt(6)\n" \
  "v_mfma_f32_32x32x16_bf16 %1, a[16:19], a[20:23], %1\n" \
  "ds_read_b64 a[0:1], %2 offset:640\n" \
  "ds_read_b64 a[2:3], %2 offset:648\n" \
  "v_mfma_f32_32x32x16_bf16 %0, a[16:19], a[24:27], %0\n" \
  "ds_read_b128 a[4:7], %3 offset:4272\n" \
  "ds_read_b128 a[8:11], %3 offset:1712\n" \
  "v_mfma_f32_32x32x16_bf16 %1, a[28:31], a[24:27], %1\n" \
  "ds_read_b64 a[12:13], %2 offset:2688\n" \
  "ds_read_b64 a[14:15], %2 offset:2696\n" \
  "s_waitcnt lgkmcnt(6)\n" \
  "v_mfma_f32_32x32x16_bf16 %1, a[32:35], a[36:39], %1\n" \
  "ds_read_b64 a[16:17], %2 offset:736\n" \
  "ds_read_b64 a[18:19], %2 offset:744\n" \
  "v_mfma_f32_32x32x16_bf16 %0, a[32:35], a[40:43], %0\n" \
  "ds_read_b128 a[20:23], %3 offset:4160\n" \
  "ds_read_b128 a[24:27], %3 offset:1600\n" \
  "v_mfma_f32_32x32x16_bf16 %1, a[44:47], a[40:43], %1\n" \
  "ds_read_b64 a[28:29], %2 offset:2784\n" \
  "ds_read_b64 a[30:31], %2 offset:2792\n" \
  "s_waitcnt lgkmcnt(6)\n" \
  "v_mfma_f32_32x32x16_bf16 %1, a[0:3], a[4:7], %1\n" \
  "ds_read_b64 a[32:33], %2 offset:704\n" \
  "ds_read_b64 a[34:35], %2 offset:712\n" \
  "v_mfma_f32_32x32x16_bf16 %0, a[0:3], a[8:11], %0\n" \
  "ds_read_b128 a[36:39], %3 offset:4192\n" \
  "ds_read_b128 a[40:43], %3 offset:1632\n" \
  "v_mfma_f32_32x32x16_bf16 %1, a[12:15], a[8:11], %1\n" \
  "ds_read_b64 a[44:45], %2 offset:2752\n" \
  "ds_read_b64 a[46:47], %2 offset:2760\n" \
  "s_waitcnt lgkmcnt(6)\n" \
  "v_mfma_f32_32x32x16_bf16 %1, a[16:19], a[20:23], %1\n" \
  "ds_read_b64 a[0:1], %2 offset:800\n" \
  "ds_read_b64 a[2:3], %2 offset:808\n" \
  "v_mfma_f32_32x32x16_bf16 %0, a[16:19], a[24:27], %0\n" \
  "ds_read_b128 a[4:7], %3 offset:4080\n" \
  "ds_read_b128 a[8:11], %3 offset:1520\n" \
  "v_mfma_f32_32x32x16_bf16 %1, a[28:31], a[24:27], %1\n" \
  "ds_read_b64 a[12:13], %2 offset:2848\n" \
  "ds_read_b64 a[14:15], %2 offset:2856\n" \
  "s_waitcnt lgkmcnt(6)\n" \
  "v_mfma_f32_32x32x16_bf16 %1, a[32:35], a[36:39], %1\n" \
  "ds_read_b64 a[16:17], %2 offset:768\n" \
  "ds_read_b64 a[18:19], %2 offset:776\n" \
  "v_mfma_f32_32x32x16_bf16 %0, a[32:35], a[40:43], %0\n" \
  "ds_read_b128 a[20:23], %3 offset:4112\n" \
  "ds_read_b128 a[24:27], %3 offset:1552\n" \
  "v_mfma_f32_32x32x16_bf16 %1, a[44:47], a[40:43], %1\n" \
  "ds_read_b64 a[28:29], %2 offset:2816\n" \
  "ds_read_b64 a[30:31], %2 offset:2824\n" \
  "s_waitcnt lgkmcnt(6)\n" \
  "v_mfma_f32_32x32x16_bf16 %1, a[0:3], a[4:7], %1\n" \
  "ds_read_b64 a[32:33], %2 offset:864\n" \
  "ds_read_b64 a[34:35], %2 offset:872\n" \
  "v_mfma_f32_32x32x16_bf16 %0, a[0:3], a[8:11], %0\n" \
  "ds_read_b128 a[36:39], %3 offset:4000\n" \
  "ds_read_b128 a[40:43], %3 offset:1440\n" \
  "v_mfma_f32_32x32x16_bf16 %1, a[12:15], a[8:11], %1\n" \
  "ds_read_b64 a[44:45], %2 offset:2912\n" \
  "ds_read_b64 a[46:47], %2 offset:2920\n" \
  "s_waitcnt lgkmcnt(6)\n" \
  "v_mfma_f32_32x32x16_bf16 %1, a[16:19], a[20:23], %1\n" \
  "ds_read_b64 a[0:1], %2 offset:832\n" \
  "ds_read_b64 a[2:3], %2 offset:840\n" \
  "v_mfma_f32_32x32x16_bf16 %0, a[16:19], a[24:27], %0\n" \
  "ds_read_b128 a[4:7], %3 offset:4032\n" \
  "ds_read_b128 a[8:11], %3 offset:1472\n" \
  "v_mfma_f32_32x32x16_bf16 %1, a[28:31], a[24:27], %1\n" \
  "ds_read_b64 a[12:13], %2 offset:2880\n" \
  "ds_read_b64 a[14:15], %2 offset:2888\n" \
  "s_waitcnt lgkmcnt(6)\n" \
  "v_mfma_f32_32x32x16_bf16 %1, a[32:35], a[36:39], %1\n" \
  "ds_read_b64 a[16:17], %2 offset:928\n" \
  "ds_read_b64 a[18:19], %2 offset:936\n" \
  "v_mfma_f32_32x32x16_bf16 %0, a[32:35], a[40:43], %0\n" \
  "ds_read_b128 a[20:23], %3 offset:3920\n" \
  "ds_read_b128 a[24:27], %3 offset:1360\n" \
  "v_mfma_f32_32x32x16_bf16 %1, a[44:47], a[40:43], %1\n" \
  "ds_read_b64 a[28:29], %2 offset:2976\n" \
  "ds_read_b64 a[30:31], %2 offset:2984\n" \
  "s_waitcnt lgkmcnt(6)\n" \
  "v_mfma_f32_32x32x16_bf16 %1, a[0:3], a[4:7], %1\n" \
  "ds_read_b64 a[32:33], %2 offset:896\n" \
  "ds_read_b64 a[34:35], %2 offset:904\n" \
  "v_mfma_f32_32x32x16_bf16 %0, a[0:3], a[8:11], %0\n" \
  "ds_read_b128 a[36:39], %3 offset:3952\n" \
  "ds_read_b128 a[40:43], %3 offset:1392\n" \
  "v_mfma_f32_32x32x16_bf16 %1, a[12:15], a[8:11], %1\n" \
  "ds_read_b64 a[44:45], %2 offset:2944\n" \
  "ds_read_b64 a[46:47], %2 offset:2952\n" \
  "s_waitcnt lgkmcnt(6)\n" \
  "v_mfma_f32_32x32x16_bf16 %1, a[16:19], a[20:23], %1\n" \
  "ds_read_b64 a[0:1], %2 offset:992\n" \
  "ds_read_b64 a[2:3], %2 offset:1000\n" \
  "v_mfma_f32_32x32x16_bf16 %0, a[16:19], a[24:27], %0\n" \
  "ds_read_b128 a[4:7], %3 offset:3840\n" \
  "ds_read_b128 a[8:11], %3 offset:1280\n" \
  "v_mfma_f32_32x32x16_bf16 %1, a[28:31], a[24:27], %1\n" \
  "ds_read_b64 a[12:13], %2 offset:3040\n" \
  "ds_read_b64 a[14:15], %2 offset:3048\n" \
  "s_waitcnt lgkmcnt(6)\n" \
  "v_mfma_f32_32x32x16_bf16 %1, a[32:35], a[36:39], %1\n" \
  "ds_read_b64 a[16:17], %2 offset:960\n" \
  "ds_read_b64 a[18:19], %2 offset:968\n" \
  "v_mfma_f32_32x32x16_bf16 %0, a[32:35], a[40:43], %0\n" \
  "ds_read_b128 a[20:23], %3 offset:3872\n" \
  "ds_read_b128 a[24:27], %3 offset:1312\n" \
  "v_mfma_f32_32x32x16_bf16 %1, a[44:47], a[40:43], %1\n" \
  "ds_read_b64 a[28:29], %2 offset:3008\n" \
  "ds_read_b64 a[30:31], %2 offset:3016\n" \
  "s_waitcnt lgkmcnt(6)\n" \
  "v_mfma_f32_32x32x16_bf16 %1, a[0:3], a[4:7], %1\n" \
  "ds_read_b64 a[32:33], %2 offset:1056\n" \
  "ds_read_b64 a[34:35], %2 offset:1064\n" \
  "v_mfma_f32_32x32x16_bf16 %0, a[0:3], a[8:11], %0\n" \
  "ds_read_b128 a[36:39], %3 offset:3760\n" \
  "ds_read_b128 a[40:43], %3 offset:1200\n" \
  "v_mfma_f32_32x32x16_bf16 %1, a[12:15], a[8:11], %1\n" \
  "ds_read_b64 a[44:45], %2 offset:3104\n" \
  "ds_read_b64 a[46:47], %2 offset:3112\n" \
  "s_waitcnt lgkmcnt(6)\n" \
  "v_mfma_f32_32x32x16_bf16 %1, a[16:19], a[20:23], %1\n" \
  "ds_read_b64 a[0:1], %2 offset:1024\n" \
  "ds_read_b64 a[2:3], %2 offset:1032\n" \
  "v_mfma_f32_32x32x16_bf16 %0, a[16:19], a[24:27], %0\n" \
  "ds_read_b128 a[4:7], %3 offset:3792\n" \
  "ds_read_b128 a[8:11], %3 offset:1232\n" \
  "v_mfma_f32_32x32x16_bf16 %1, a[28:31], a[24:27], %1\n" \
  "ds_read_b64 a[12:13], %2 offset:3072\n" \
  "ds_read_b64 a[14:15], %2 offset:3080\n" \
  "s_waitcnt lgkmcnt(6)\n" \
  "v_mfma_f32_32x32x16_bf16 %1, a[32:35], a[36:39], %1\n" \
  "ds_read_b64 a[16:17], %2 offset:1120\n" \
  "ds_read_b64 a[18:19], %2 offset:1128\n" \
  "v_mfma_f32_32x32x16_bf16 %0, a[32:35], a[40:43], %0\n" \
  "ds_read_b128 a[20:23], %3 offset:3680\n" \
  "ds_read_b128 a[24:27], %3 offset:1120\n" \
  "v_mfma_f32_32x32x16_bf16 %1, a[44:47], a[40:43], %1\n" \
  "ds_read_b64 a[28:29], %2 offset:3168\n" \
  "ds_read_b64 a[30:31], %2 offset:3176\n" \
  "s_waitcnt lgkmcnt(6)\n" \
  "v_mfma_f32_32x32x16_bf16 %1, a[0:3], a[4:7], %1\n" \
  "ds_read_b64 a[32:33], %2 offset:1088\n" \
  "ds_read_b64 a[34:35], %2 offset:1096\n" \
  "v_mfma_f32_32x32x16_bf16 %0, a[0:3], a[8:11], %0\n" \
  "ds_read_b128 a[36:39], %3 offset:3712\n" \
  "ds_read_b128 a[40:43], %3 offset:1152\n" \
  "v_mfma_f32_32x32x16_bf16 %1, a[12:15], a[8:11], %1\n" \
  "ds_read_b64 a[44:45], %2 offset:3136\n" \
  "ds_read_b64 a[46:47], %2 offset:3144\n" \
  "s_waitcnt lgkmcnt(6)\n" \
  "v_mfma_f32_32x32x16_bf16 %1, a[16:19], a[20:23], %1\n" \
  "ds_read_b64 a[0:1], %2 offset:1184\n" \
  "ds_read_b64 a[2:3], %2 offset:1192\n" \
  "v_mfma_f32_32x32x16_bf16 %0, a[16:19], a[24:27], %0\n" \
  "ds_read_b128 a[4:7], %3 offset:3600\n" \
  "ds_read_b128 a[8:11], %3 offset:1040\n" \
  "v_mfma_f32_32x32x16_bf16 %1, a[28:31], a[24:27], %1\n" \
  "ds_read_b64 a[12:13], %2 offset:3232\n" \
  "ds_read_b64 a[14:15], %2 offset:3240\n" \
  "s_waitcnt lgkmcnt(6)\n" \
  "v_mfma_f32_32x32x16_bf16 %1, a[32:35], a[36:39], %1\n" \
  "ds_read_b64 a[16:17], %2 offset:1152\n" \
  "ds_read_b64 a[18:19], %2 offset:1160\n" \
  "v_mfma_f32_32x32x16_bf16 %0, a[32:35], a[40:43], %0\n" \
  "ds_read_b128 a[20:23], %3 offset:3632\n" \
  "ds_read_b128 a[24:27], %3 offset:1072\n" \
  "v_mfma_f32_32x32x16_bf16 %1, a[44:47], a[40:43], %1\n" \
  "ds_read_b64 a[28:29], %2 offset:3200\n" \
  "ds_read_b64 a[30:31], %2 offset:3208\n" \
  "s_waitcnt lgkmcnt(6)\n" \
  "v_mfma_f32_32x32x16_bf16 %1, a[0:3], a[4:7], %1\n" \
  "ds_read_b64 a[32:33], %2 offset:1248\n" \
  "ds_read_b64 a[34:35], %2 offset:1256\n" \
  "v_mfma_f32_32x32x16_bf16 %0, a[0:3], a[8:11], %0\n" \
  "ds_read_b128 a[36:39], %3 offset:3520\n" \
  "ds_read_b128 a[40:43], %3 offset:960\n" \
  "v_mfma_f32_32x32x16_bf16 %1, a[12:15], a[8:11], %1\n" \
  "ds_read_b64 a[44:45], %2 offset:3296\n" \
  "ds_read_b64 a[46:47], %2 offset:3304\n" \
  "s_waitcnt lgkmcnt(6)\n" \
  "v_mfma_f32_32x32x16_bf16 %1, a[16:19], a[20:23], %1\n" \
  "ds_read_b64 a[0:1], %2 offset:1216\n" \
  "ds_read_b64 a[2:3], %2 offset:1224\n" \
  "v_mfma_f32_32x32x16_bf16 %0, a[16:19], a[24:27], %0\n" \
  "ds_read_b128 a[4:7], %3 offset:3552\n" \
  "ds_read_b128 a[8:11], %3 offset:992\n" \
  "v_mfma_f32_32x32x16_bf16 %1, a[28:31], a[24:27], %1\n" \
  "ds_read_b64 a[12:13], %2 offset:3264\n" \
  "ds_read_b64 a[14:15], %2 offset:3272\n" \
  "s_waitcnt lgkmcnt(6)\n" \
  "v_mfma_f32_32x32x16_bf16 %1, a[32:35], a[36:39], %1\n" \
  "ds_read_b64 a[16:17], %2 offset:1312\n" \
  "ds_read_b64 a[18:19], %2 offset:1320\n" \
  "v_mfma_f32_32x32x16_bf16 %0, a[32:35], a[40:43], %0\n" \
  "ds_read_b128 a[20:23], %3 offset:3440\n" \
  "ds_read_b128 a[24:27], %3 offset:880\n" \
  "v_mfma_f32_32x32x16_bf16 %1, a[44:47], a[40:43], %1\n" \
  "ds_read_b64 a[28:29], %2 offset:3360\n" \
  "ds_read_b64 a[30:31], %2 offset:3368\n" \
  "s_waitcnt lgkmcnt(6)\n" \
  "v_mfma_f32_32x32x16_bf16 %1, a[0:3], a[4:7], %1\n" \
  "ds_read_b64 a[32:33], %2 offset:1280\n" \
  "ds_read_b64 a[34:35], %2 offset:1288\n" \
  "v_mfma_f32_32x32x16_bf16 %0, a[0:3], a[8:11], %0\n" \
  "ds_read_b128 a[36:39], %3 offset:3472\n" \
  "ds_read_b128 a[40:43], %3 offset:912\n" \
  "v_mfma_f32_32x32x16_bf16 %1, a[12:15], a[8:11], %1\n" \
  "ds_read_b64 a[44:45], %2 offset:3328\n" \
  "ds_read_b64 a[46:47], %2 offset:3336\n" \
  "s_waitcnt lgkmcnt(6)\n" \
  "v_mfma_f32_32x32x16_bf16 %1, a[16:19], a[20:23], %1\n" \
  "ds_read_b64 a[0:1], %2 offset:1376\n" \
  "ds_read_b64 a[2:3], %2 offset:1384\n" \
  "v_mfma_f32_32x32x16_bf16 %0, a[16:19], a[24:27], %0\n" \
  "ds_read_b128 a[4:7], %3 offset:3360\n" \
  "ds_read_b128 a[8:11], %3 offset:800\n" \
  "v_mfma_f32_32x32x16_bf16 %1, a[28:31], a[24:27], %1\n" \
  "ds_read_b64 a[12:13], %2 offset:3424\n" \
  "ds_read_b64 a[14:15], %2 offset:3432\n" \
  "s_waitcnt lgkmcnt(6)\n" \
  "v_mfma_f32_32x32x16_bf16 %1, a[32:35], a[36:39], %1\n" \
  "ds_read_b64 a[16:17], %2 offset:1344\n" \
  "ds_read_b64 a[18:19], %2 offset:1352\n" \
  "v_mfma_f32_32x32x16_bf16 %0, a[32:35], a[40:43], %0\n" \
  "ds_read_b128 a[20:23], %3 offset:3392\n" \
  "ds_read_b128 a[24:27], %3 offset:832\n" \
  "v_mfma_f32_32x32x16_bf16 %1, a[44:47], a[40:43], %1\n" \
  "ds_read_b64 a[28:29], %2 offset:3392\n" \
  "ds_read_b64 a[30:31], %2 offset:3400\n" \
  "s_waitcnt lgkmcnt(6)\n" \
  "v_mfma_f32_32x32x16_bf16 %1, a[0:3], a[4:7], %1\n" \
  "ds_read_b64 a[32:33], %2 offset:1440\n" \
  "ds_read_b64 a[34:35], %2 offset:1448\n" \
  "v_mfma_f32_32x32x16_bf16 %0, a[0:3], a[8:11], %0\n" \
  "ds_read_b128 a[36:39], %3 offset:3280\n" \
  "ds_read_b128 a[40:43], %3 offset:720\n" \
  "v_mfma_f32_32x32x16_bf16 %1, a[12:15], a[8:11], %1\n" \
  "ds_read_b64 a[44:45], %2 offset:3488\n" \
  "ds_read_b64 a[46:47], %2 offset:3496\n" \
  "s_waitcnt lgkmcnt(6)\n" \
  "v_mfma_f32_32x32x16_bf16 %1, a[16:19], a[20:23], %1\n" \
  "ds_read_b64 a[0:1], %2 offset:1408\n" \
  "ds_read_b64 a[2:3], %2 offset:1416\n" \
  "v_mfma_f32_32x32x16_bf16 %0, a[16:19], a[24:27], %0\n" \
  "ds_read_b128 a[4:7], %3 offset:3312\n" \
  "ds_read_b128 a[8:11], %3 offset:752\n" \
  "v_mfma_f32_32x32x16_bf16 %1, a[28:31], a[24:27], %1\n" \
  "ds_read_b64 a[12:13], %2 offset:3456\n" \
  "ds_read_b64 a[14:15], %2 offset:3464\n" \
  "s_waitcnt lgkmcnt(6)\n" \
  "v_mfma_f32_32x32x16_bf16 %1, a[32:35], a[36:39], %1\n" \
  "ds_read_b64 a[16:17], %2 offset:1504\n" \
  "ds_read_b64 a[18:19], %2 offset:1512\n" \
  "v_mfma_f32_32x32x16_bf16 %0, a[32:35], a[40:43], %0\n" \
  "ds_read_b128 a[20:23], %3 offset:3200\n" \
  "ds_read_b128 a[24:27], %3 offset:640\n" \
  "v_mfma_f32_32x32x16_bf16 %1, a[44:47], a[40:43], %1\n" \
  "ds_read_b64 a[28:29], %2 offset:3552\n" \
  "ds_read_b64 a[30:31], %2 offset:3560\n" \
  "s_waitcnt lgkmcnt(6)\n" \
  "v_mfma_f32_32x32x16_bf16 %1, a[0:3], a[4:7], %1\n" \
  "ds_read_b64 a[32:33], %2 offset:1472\n" \
  "ds_read_b64 a[34:35], %2 offset:1480\n" \
  "v_mfma_f32_32x32x16_bf16 %0, a[0:3], a[8:11], %0\n" \
  "ds_read_b128 a[36:39], %3 offset:3232\n" \
  "ds_read_b128 a[40:43], %3 offset:672\n" \
  "v_mfma_f32_32x32x16_bf16 %1, a[12:15], a[8:11], %1\n" \
  "ds_read_b64 a[44:45], %2 offset:3520\n" \
  "ds_read_b64 a[46:47], %2 offset:3528\n" \
  "s_waitcnt lgkmcnt(6)\n" \
  "v_mfma_f32_32x32x16_bf16 %1, a[16:19], a[20:23], %1\n" \
  "ds_read_b64 a[0:1], %2 offset:1568\n" \
  "ds_read_b64 a[2:3], %2 offset:1576\n" \
  "v_mfma_f32_32x32x16_bf16 %0, a[16:19], a[24:27], %0\n" \
  "ds_read_b128 a[4:7], %3 offset:3120\n" \
  "ds_read_b128 a[8:11], %3 offset:560\n" \
  "v_mfma_f32_32x32x16_bf16 %1, a[28:31], a[24:27], %1\n" \
  "ds_read_b64 a[12:13], %2 offset:3616\n" \
  "ds_read_b64 a[14:15], %2 offset:3624\n" \
  "s_waitcnt lgkmcnt(6)\n" \
  "v_mfma_f32_32x32x16_bf16 %1, a[32:35], a[36:39], %1\n" \
  "ds_read_b64 a[16:17], %2 offset:1536\n" \
  "ds_read_b64 a[18:19], %2 offset:1544\n" \
  "v_mfma_f32_32x32x16_bf16 %0, a[32:35], a[40:43], %0\n" \
  "ds_read_b128 a[20:23], %3 offset:3152\n" \
  "ds_read_b128 a[24:27], %3 offset:592\n" \
  "v_mfma_f32_32x32x16_bf16 %1, a[44:47], a[40:43], %1\n" \
  "ds_read_b64 a[28:29], %2 offset:3584\n" \
  "ds_read_b64 a[30:31], %2 offset:3592\n" \
  "s_waitcnt lgkmcnt(6)\n" \
  "v_mfma_f32_32x32x16_bf16 %1, a[0:3], a[4:7], %1\n" \
  "ds_read_b64 a[32:33], %2 offset:1632\n" \
  "ds_read_b64 a[34:35], %2 offset:1640\n" \
  "v_mfma_f32_32x32x16_bf16 %0, a[0:3], a[8:11], %0\n" \
  "ds_read_b128 a[36:39], %3 offset:3040\n" \
  "ds_read_b128 a[40:43], %3 offset:480\n" \
  "v_mfma_f32_32x32x16_bf16 %1, a[12:15], a[8:11], %1\n" \
  "ds_read_b64 a[44:45], %2 offset:3680\n" \
  "ds_read_b64 a[46:47], %2 offset:3688\n" \
  "s_waitcnt lgkmcnt(6)\n" \
  "v_mfma_f32_32x32x16_bf16 %1, a[16:19], a[20:23], %1\n" \
  "ds_read_b64 a[0:1], %2 offset:1600\n" \
  "ds_read_b64 a[2:3], %2 offset:1608\n" \
  "v_mfma_f32_32x32x16_bf16 %0, a[16:19], a[24:27], %0\n" \
  "ds_read_b128 a[4:7], %3 offset:3072\n" \
  "ds_read_b128 a[8:11], %3 offset:512\n" \
  "v_mfma_f32_32x32x16_bf16 %1, a[28:31], a[24:27], %1\n" \
  "ds_read_b64 a[12:13], %2 offset:3648\n" \
  "ds_read_b64 a[14:15], %2 offset:3656\n" \
  "s_waitcnt lgkmcnt(6)\n" \
  "v_mfma_f32_32x32x16_bf16 %1, a[32:35], a[36:39], %1\n" \
  "ds_read_b64 a[16:17], %2 offset:1696\n" \
  "ds_read_b64 a[18:19], %2 offset:1704\n" \
  "v_mfma_f32_32x32x16_bf16 %0, a[32:35], a[40:43], %0\n" \
  "ds_read_b128 a[20:23], %3 offset:2960\n" \
  "ds_read_b128 a[24:27], %3 offset:400\n" \
  "v_mfma_f32_32x32x16_bf16 %1, a[44:47], a[40:43], %1\n" \
  "ds_read_b64 a[28:29], %2 offset:3744\n" \
  "ds_read_b64 a[30:31], %2 offset:3752\n" \
  "s_waitcnt lgkmcnt(6)\n" \
  "v_mfma_f32_32x32x16_bf16 %1, a[0:3], a[4:7], %1\n" \
  "ds_read_b64 a[32:33], %2 offset:1664\n" \
  "ds_read_b64 a[34:35], %2 offset:1672\n" \
  "v_mfma_f32_32x32x16_bf16 %0, a[0:3], a[8:11], %0\n" \
  "ds_read_b128 a[36:39], %3 offset:2992\n" \
  "ds_read_b128 a[40:43], %3 offset:432\n" \
  "v_mfma_f32_32x32x16_bf16 %1, a[12:15], a[8:11], %1\n" \
  "ds_read_b64 a[44:45], %2 offset:3712\n" \
  "ds_read_b64 a[46:47], %2 offset:3720\n" \
  "s_waitcnt lgkmcnt(6)\n" \
  "v_mfma_f32_32x32x16_bf16 %1, a[16:19], a[20:23], %1\n" \
  "ds_read_b64 a[0:1], %2 offset:1760\n" \
  "ds_read_b64 a[2:3], %2 offset:1768\n" \
  "v_mfma_f32_32x32x16_bf16 %0, a[16:19], a[24:27], %0\n" \
  "ds_read_b128 a[4:7], %3 offset:2880\n" \
  "ds_read_b128 a[8:11], %3 offset:320\n" \
  "v_mfma_f32_32x32x16_bf16 %1, a[28:31], a[24:27], %1\n" \
  "ds_read_b64 a[12:13], %2 offset:3808\n" \
  "ds_read_b64 a[14:15], %2 offset:3816\n" \
  "s_waitcnt lgkmcnt(6)\n" \
  "v_mfma_f32_32x32x16_bf16 %1, a[32:35], a[36:39], %1\n" \
  "ds_read_b64 a[16:17], %2 offset:1728\n" \
  "ds_read_b64 a[18:19], %2 offset:1736\n" \
  "v_mfma_f32_32x32x16_bf16 %0, a[32:35], a[40:43], %0\n" \
  "ds_read_b128 a[20:23], %3 offset:2912\n" \
  "ds_read_b128 a[24:27], %3 offset:352\n" \
  "v_mfma_f32_32x32x16_bf16 %1, a[44:47], a[40:43], %1\n" \
  "ds_read_b64 a[28:29], %2 offset:3776\n" \
  "ds_read_b64 a[30:31], %2 offset:3784\n" \
  "s_waitcnt lgkmcnt(6)\n" \
  "v_mfma_f32_32x32x16_bf16 %1, a[0:3], a[4:7], %1\n" \
  "ds_read_b64 a[32:33], %2 offset:1824\n" \
  "ds_read_b64 a[34:35], %2 offset:1832\n" \
  "v_mfma_f32_32x32x16_bf16 %0, a[0:3], a[8:11], %0\n" \
  "ds_read_b128 a[36:39], %3 offset:2800\n" \
  "ds_read_b128 a[40:43], %3 offset:240\n" \
  "v_mfma_f32_32x32x16_bf16 %1, a[12:15], a[8:11], %1\n" \
  "ds_read_b64 a[44:45], %2 offset:3872\n" \
  "ds_read_b64 a[46:47], %2 offset:3880\n" \
  "s_waitcnt lgkmcnt(6)\n" \
  "v_mfma_f32_32x32x16_bf16 %1, a[16:19], a[20:23], %1\n" \
  "ds_read_b64 a[0:1], %2 offset:1792\n" \
  "ds_read_b64 a[2:3], %2 offset:1800\n" \
  "v_mfma_f32_32x32x16_bf16 %0, a[16:19], a[24:27], %0\n" \
  "ds_read_b128 a[4:7], %3 offset:2832\n" \
  "ds_read_b128 a[8:11], %3 offset:272\n" \
  "v_mfma_f32_32x32x16_bf16 %1, a[28:31], a[24:27], %1\n" \
  "ds_read_b64 a[12:13], %2 offset:3840\n" \
  "ds_read_b64 a[14:15], %2 offset:3848\n" \
  "s_waitcnt lgkmcnt(6)\n" \
  "v_mfma_f32_32x32x16_bf16 %1, a[32:35], a[36:39], %1\n" \
  "ds_read_b64 a[16:17], %2 offset:1888\n" \
  "ds_read_b64 a[18:19], %2 offset:1896\n" \
  "v_mfma_f32_32x32x16_bf16 %0, a[32:35], a[40:43], %0\n" \
  "ds_read_b128 a[20:23], %3 offset:2720\n" \
  "ds_read_b128 a[24:27], %3 offset:160\n" \
  "v_mfma_f32_32x32x16_bf16 %1, a[44:47], a[40:43], %1\n" \
  "ds_read_b64 a[28:29], %2 offset:3936\n" \
  "ds_read_b64 a[30:31], %2 offset:3944\n" \
  "s_waitcnt lgkmcnt(6)\n" \
  "v_mfma_f32_32x32x16_bf16 %1, a[0:3], a[4:7], %1\n" \
  "ds_read_b64 a[32:33], %2 offset:1856\n" \
  "ds_read_b64 a[34:35], %2 offset:1864\n" \
  "v_mfma_f32_32x32x16_bf16 %0, a[0:3], a[8:11], %0\n" \
  "ds_read_b128 a[36:39], %3 offset:2752\n" \
  "ds_read_b128 a[40:43], %3 offset:192\n" \
  "v_mfma_f32_32x32x16_bf16 %1, a[12:15], a[8:11], %1\n" \
  "ds_read_b64 a[44:45], %2 offset:3904\n" \
  "ds_read_b64 a[46:47], %2 offset:3912\n" \
  "s_waitcnt lgkmcnt(6)\n" \
  "v_mfma_f32_32x32x16_bf16 %1, a[16:19], a[20:23], %1\n" \
  "ds_read_b64 a[0:1], %2 offset:1952\n" \
  "ds_read_b64 a[2:3], %2 offset:1960\n" \
  "v_mfma_f32_32x32x16_bf16 %0, a[16:19], a[24:27], %0\n" \
  "ds_read_b128 a[4:7], %3 offset:2640\n" \
  "ds_read_b128 a[8:11], %3 offset:80\n" \
  "v_mfma_f32_32x32x16_bf16 %1, a[28:31], a[24:27], %1\n" \
  "ds_read_b64 a[12:13], %2 offset:4000\n" \
  "ds_read_b64 a[14:15], %2 offset:4008\n" \
  "s_waitcnt lgkmcnt(6)\n" \
  "v_mfma_f32_32x32x16_bf16 %1, a[32:35], a[36:39], %1\n" \
  "ds_read_b64 a[16:17], %2 offset:1920\n" \
  "ds_read_b64 a[18:19], %2 offset:1928\n" \
  "v_mfma_f32_32x32x16_bf16 %0, a[32:35], a[40:43], %0\n" \
  "ds_read_b128 a[20:23], %3 offset:2672\n" \
  "ds_read_b128 a[24:27], %3 offset:112\n" \
  "v_mfma_f32_32x32x16_bf16 %1, a[44:47], a[40:43], %1\n" \
  "ds_read_b64 a[28:29], %2 offset:3968\n" \
  "ds_read_b64 a[30:31], %2 offset:3976\n" \
  "s_waitcnt lgkmcnt(6)\n" \
  "v_mfma_f32_32x32x16_bf16 %1, a[0:3], a[4:7], %1\n" \
  "ds_read_b64 a[32:33], %2 offset:2016\n" \
  "ds_read_b64 a[34:35], %2 offset:2024\n" \
  "v_mfma_f32_32x32x16_bf16 %0, a[0:3], a[8:11], %0\n" \
  "ds_read_b128 a[36:39], %3 offset:2560\n" \
  "ds_read_b128 a[40:43], %3 offset:0\n" \
  "v_mfma_f32_32x32x16_bf16 %1, a[12:15], a[8:11], %1\n" \
  "ds_read_b64 a[44:45], %2 offset:4064\n" \
  "ds_read_b64 a[46:47], %2 offset:4072\n" \
  "s_waitcnt lgkmcnt(6)\n" \
  "v_mfma_f32_32x32x16_bf16 %1, a[16:19], a[20:23], %1\n" \
  "ds_read_b64 a[0:1], %2 offset:1984\n" \
  "ds_read_b64 a[2:3], %2 offset:1992\n" \
  "v_mfma_f32_32x32x16_bf16 %0, a[16:19], a[24:27], %0\n" \
  "ds_read_b128 a[4:7], %3 offset:2592\n" \
  "ds_read_b128 a[8:11], %3 offset:32\n" \
  "v_mfma_f32_32x32x16_bf16 %1, a[28:31], a[24:27], %1\n" \
  "ds_read_b64 a[12:13], %2 offset:4032\n" \
  "ds_read_b64 a[14:15], %2 offset:4040\n" \
  "s_waitcnt lgkmcnt(6)\n" \
  "v_mfma_f32_32x32x16_bf16 %1, a[32:35], a[36:39], %1\n" \
  "v_mfma_f32_32x32x16_bf16 %0, a[32:35], a[40:43], %0\n" \
  "v_mfma_f32_32x32x16_bf16 %1, a[44:47], a[40:43], %1\n" \
  "s_waitcnt lgkmcnt(0)\n" \
  "v_mfma_f32_32x32x16_bf16 %1, a[0:3], a[4:7], %1\n" \
  "v_mfma_f32_32x32x16_bf16 %0, a[0:3], a[8:11], %0\n" \
  "v_mfma_f32_32x32x16_bf16 %1, a[12:15], a[8:11], %1\n" \
  "s_nop 15\n" \
  ""

#define CONV_CLOBBERS "a0", "a1", "a2", "a3", "a4", "a5", "a6", "a7", "a8", "a9", "a10", "a11", "a12", "a13", "a14", "a15", "a16", "a17", "a18", "a19", "a20", "a21", "a22", "a23", "a24", "a25", "a26", "a27", "a28", "a29", "a30", "a31", "a32", "a33", "a34", "a35", "a36", "a37", "a38", "a39", "a40", "a41", "a42", "a43", "a44", "a45", "a46", "a47", "memory"
#define REC_CLOBBERS \
  "v0", "v1", "v2", "v3", "v4", "v5", "v6", "v7", "v8", "v9", "v10", "v11", "v12", "v13", "v14", "v15", "v16", "v17", "v18", "v19", "v20", "v21", "v22",\
  "v23", "v24", "v25", "v26", "v27", "v28", "v29", "v30", "v31", "v32", "v33", "v34", "v35", "v36", "v37", "v38", "v39", "v40", "v41", "v42", "v43",\
  "v44", "v45", "v46", "v47", "v48", "v49", "v50", "v51", "v52", "v53", "v54", "v55", "v56", "v57", "v58", "v59", "v60", "v61", "v62", "v63", "v64",\
  "v65", "v66", "v67", "v68", "v69", "v70", "v71", "v72", "v73", "v74", "v75", "v76", "v77", "v78", "v79", "v80", "v81", "v82", "v83", "v84", "v85",\
  "v86", "v87", "v88", "v89", "v90", "v91", "v92", "v93", "v94", "v95", "v96", "v97", "v98", "v99", "v100", "v101", "v102", "v103", "v104", "v105",\
  "v106", "v107", "v108", "v109", "v110", "v111", "v112", "v113", "v114", "v115", "v116", "v117", "v118", "v119", "v120", "v121", "v122", "v123",\
  "v124", "v125", "v126", "v127", "v128", "v129", "v130", "v131", "v132", "v133", "v134", "v135", "v136", "v137", "v138", "v139", "v140", "v141",\
  "v142", "v143", "v144", "v145", "v146", "v147", "v148", "v149", "v150", "v151", "v152", "v153", "v154", "v155", "v156", "v157", "v158", "v159",\
  "v160", "v161", "v162", "v163", "v164", "v165", "v166", "v167", "v168", "v169", "v170", "v171", "v172", "v173", "v174", "v175", "v176", "v177",\
  "v178", "v179", "v180", "v181", "v182", "v183", "v184", "v185", "v186", "v187", "v188", "v189", "v190", "v191", "v192", "v193", "v194", "v195",\
  "v196", "v197", "v198", "v199", "v200", "v201", "v202", "v203", "v204", "v205", "v206", "v207", "v208", "v209", "v210", "v211", "v212", "v213",\
  "v214", "v215", "v216", "v217", "v218", "v219", "v220", "v221", "v222", "v223", "v224", "v225", "v226", "v227", "v228", "v229", "v230", "v231",\
  "v232", "v233", "v234", "v235", "v236", "v237", "v238", "v239", "v240", "v241", "v242", "v243", "v244", "v245", "v246", "v247", "v248", "v249",\
  "v250", "v251", "v252", "v253", "v254", "v255", "a0", "a1", "a2", "a3", "a4", "a5", "a6", "a7", "a8", "a9", "a10", "a11", "a12", "a13", "a14", "a15",\
  "a16", "a17", "a18", "a19", "a20", "a21", "a22", "a23", "a24", "a25", "a26", "a27", "a28", "a29", "a30", "a31", "a32", "a33", "a34", "a35", "a36",\
  "a37", "a38", "a39", "a40", "a41", "a42", "a43", "a44", "a45", "a46", "a47", "a48", "a49", "a50", "a51", "a52", "a53", "a54", "a55", "a56", "a57",\
  "a58", "a59", "a60", "a61", "a62", "a63", "a64", "a65", "a66", "a67", "a68", "a69", "a70", "a71", "a72", "a73", "a74", "a75", "a76", "a77", "a78",\
  "a79", "a80", "a81", "a82", "a83", "a84", "a85", "a86", "a87", "a88", "a89", "a90", "a91", "a92", "a93", "a94", "a95", "a96", "a97", "a98", "a99",\
  "a100", "a101", "a102", "a103", "a104", "a105", "a106", "a107", "a108", "a109", "a110", "a111", "a112", "a113", "a114", "a115", "a116", "a117",\
  "a118", "a119", "a120", "a121", "a122", "a123", "a124", "a125", "a126", "a127", "a128", "a129", "a130", "a131", "a132", "a133", "a134", "a135",\
  "a136", "a137", "a138", "a139", "a140", "a141", "a142", "a143", "a144", "a145", "a146", "a147", "a148", "a149", "a150", "a151", "a152", "a153",\
  "a154", "a155", "a156", "a157", "a158", "a159", "a160", "a161", "a162", "a163", "a164", "a165", "a166", "a167", "a168", "a169", "a170", "a171",\
  "a172", "a173", "a174", "a175", "a176", "a177", "a178", "a179", "a180", "a181", "a182", "a183", "a184", "a185", "a186", "a187", "a188", "a189",\
  "a190", "a191", "a192", "a193", "a194", "a195", "a196", "a197", "a198", "a199", "a200", "a201", "a202", "a203", "a204", "a205", "a206", "a207",\
  "a208", "a209", "a210", "a211", "a212", "a213", "a214", "a215", "a216", "a217", "a218", "a219", "a220", "a221", "a222", "a223", "a224", "a225",\
  "a226", "a227", "a228", "a229", "a230", "a231", "a232", "a233", "a234", "a235", "a236", "a237", "a238", "a239", "a240", "a241", "a242", "a243",\
  "a244", "a245", "a246", "a247", "a248", "a249", "a250", "a251", "a252", "a253", "a254", "a255", "s36", "s37", "s38", "s39", "s40", "s41", "s42",\
  "s43", "s44", "s45", "s46", "s47", "s48", "s49", "s50", "s51", "s52", "s53", "s54", "s55", "s56", "s57", "s58", "s59", "s60", "s61", "s62", "s63",\
  "s64", "s65", "s66", "s67", "s68", "s69", "s70", "s71", "s72", "s73", "s74", "s75", "s76", "s77", "s78", "s79", "s80", "s81", "s82", "s83", "s84",\
  "s85", "s86", "s87", "s88", "s89", "s90", "s91", "s92", "s93", "s94", "s95", "s96", "s97", "s98", "s99", "vcc", "scc", "memory"
__device__ __forceinline__ void role_rec(const Args& a, char LAS* smem, const int cu) {
  const int tid = threadIdx.x, lane = tid & 63, w = __builtin_amdgcn_readfirstlane(tid >> 6);
  const int head = cu >> 2, j = cu & 3;
  const __amdgpu_buffer_rsrc_t ex = __builtin_amdgcn_make_buffer_rsrc(a.ex, 0, (int)EX_BYTES, 0x00020000);
  u32x4 v0 = *(const u32x4*)(a.mem0 + (size_t)(4 * (cu & 1) + 2 * ((tid >> 2) & 1) + (tid & 1)) * DM + 16 * (cu >> 1) + 8 * ((tid >> 1) & 1));
  const u32 pol = wait_pol(a.ctrl);
  {
#pragma unroll
    for (int e = 0; e < 4; ++e) v0[e] = nosent(v0[e]);
    const u32 oa = exoff(pol, EXP_A + w, cu >> 6) + (cu & 63) * 128;
    __builtin_amdgcn_raw_buffer_store_b128(v0, ex, (lane < 8 ? 0u : 0x80000000u) + lane * 16, oa, SC1);
    const u32x4 sv = (u32x4){SENT, SENT, SENT, SENT};
    const int ln = lane >> 3;
    u32 vb = ln < 2 ? (u32)((4 * j + w) * 256 + ln * 128) : (ln == 2 ? (u32)(4096 + (4 * j + w) * 128) : 6144u);
    vb += (lane & 7) * 16;
    if (ln > 3 || (ln == 3 && ((cu & 31) != 0 || w != 1))) vb = 0x80000000u;
#pragma unroll
    for (int s = 0; s < 4; ++s) __builtin_amdgcn_raw_buffer_store_b128(sv, ex, vb, offB(pol, s, head), SC1);
#pragma unroll
    for (int i = 0; i < 2; ++i) __builtin_amdgcn_raw_buffer_store_b128(sv, ex, (cu & 1) * 4096 + tid * 16, exoff(pol, EXP_C, i * 64 + (cu >> 1)), SC1);
#pragma unroll
    for (int i = 0; i < 8; ++i) {
      const int s = i * 8 + ln;
      __builtin_amdgcn_raw_buffer_store_b128(sv, ex, (s == 0 ? 0x80000000u : 0u) + s * 32768 + (lane & 7) * 16, oa, SC1);
    }
  }
  const u16* pq = a.Wq + head * DH + 32 * j;
  const u16* pk = a.Wk + head * DH + 32 * j;
  const u16* pv = a.Wv + head * DH + 32 * j;
  const u16* po = a.Wo + (size_t)head * DH * DM + 256 * j;
  asm volatile(REC_ASM
               :: "s"(pq), "s"(pk), "s"(pv), "s"(po), "s"(a.ex), "s"(a.ctrl), "s"(cu), "s"(w), "s"(pol), "s"((u32)(uintptr_t)smem)
               : REC_CLOBBERS);
  __builtin_unreachable();
}
#define BULK_CLOBBERS \
  "v4", "v5", "v6", "v7", "v8", "v9", "v10", "v11", "v12", "v13", "v14", "v15", "v16", "v17", "v18", "v19", "v20", "v21", "v22", "v23", "v24", "v25",\
  "v26", "v27", "v28", "v29", "v30", "v31", "v32", "v33", "v34", "v35", "v36", "v37", "v38", "v39", "v40", "v41", "v42", "v43", "v44", "v45", "v46",\
  "v47", "v48", "v49", "v50", "v51", "v52", "v53", "v54", "v55", "v56", "v57", "v58", "v59", "v60", "v61", "v62", "v63", "v64", "v65", "v66", "v67",\
  "v68", "v69", "v70", "v71", "v72", "v73", "v74", "v75", "v76", "v77", "v78", "v79", "v80", "v81", "v82", "v83", "v84", "v85", "v86", "v87", "v88",\
  "v89", "v90", "v91", "v92", "v93", "v94", "v95", "v96", "v97", "v98", "v99", "v100", "v101", "v102", "v103", "v104", "v105", "v106", "v107", "v108",\
  "v109", "v110", "v111", "v112", "v113", "v114", "v115", "v116", "v117", "v118", "v119", "v120", "v121", "v122", "v123", "v124", "v125", "v126",\
  "v127", "v128", "v129", "v130", "v131", "v132", "v133", "v134", "v135", "v136", "v137", "v138", "v139", "v140", "v141", "v142", "v143", "v144",\
  "v145", "v146", "v147", "v148", "v149", "v150", "v151", "v152", "v153", "v154", "v155", "v156", "v157", "v158", "v159", "v160", "v161", "v162",\
  "v163", "v164", "v165", "v166", "v167", "v168", "v169", "v170", "v171", "v172", "v173", "v174", "v175", "v176", "v177", "v178", "v179", "v180",\
  "v181", "v182", "v183", "v184", "v185", "v186", "v187", "v188", "v189", "v190", "v191", "v192", "v193", "v194", "v195", "v196", "v197", "v198",\
  "v199", "v200", "v201", "v202", "v203", "v204", "v205", "v206", "v207", "v208", "v209", "v210", "v211", "v212", "v213", "v214", "v215", "v216",\
  "v217", "v218", "v219", "v220", "v221", "v222", "v223", "v224", "v225", "v226", "v227", "v228", "v229", "v230", "v231", "v232", "v233", "v234",\
  "v235", "v236", "v237", "v238", "v239", "v240", "v241", "v242", "v243", "v244", "v245", "v246", "v247", "v248", "v249", "v250", "v251", "v252",\
  "v253", "v254", "v255", "a0", "a1", "a2", "a3", "a4", "a5", "a6", "a7", "a8", "a9", "a10", "a11", "a12", "a13", "a14", "a15", "a16", "a17", "a18",\
  "a19", "a20", "a21", "a22", "a23", "a24", "a25", "a26", "a27", "a28", "a29", "a30", "a31", "a32", "a33", "a34", "a35", "a36", "a37", "a38", "a39",\
  "a40", "a41", "a42", "a43", "a44", "a45", "a46", "a47", "a48", "a49", "a50", "a51", "a52", "a53", "a54", "a55", "a56", "a57", "a58", "a59", "a60",\
  "a61", "a62", "a63", "a64", "a65", "a66", "a67", "a68", "a69", "a70", "a71", "a72", "a73", "a74", "a75", "a76", "a77", "a78", "a79", "a80", "a81",\
  "a82", "a83", "a84", "a85", "a86", "a87", "a88", "a89", "a90", "a91", "a92", "a93", "a94", "a95", "a96", "a97", "a98", "a99", "a100", "a101", "a102",\
  "a103", "a104", "a105", "a106", "a107", "a108", "a109", "a110", "a111", "a112", "a113", "a114", "a115", "a116", "a117", "a118", "a119", "a120",\
  "a121", "a122", "a123", "a124", "a125", "a126", "a127", "a128", "a129", "a130", "a131", "a132", "a133", "a134", "a135", "a136", "a137", "a138",\
  "a139", "a140", "a141", "a142", "a143", "a144", "a145", "a146", "a147", "a148", "a149", "a150", "a151", "a152", "a153", "a154", "a155", "a156",\
  "a157", "a158", "a159", "a160", "a161", "a162", "a163", "a164", "a165", "a166", "a167", "a168", "a169", "a170", "a171", "a172", "a173", "a174",\
  "a175", "a176", "a177", "a178", "a179", "a180", "a181", "a182", "a183", "a184", "a185", "a186", "a187", "a188", "a189", "a190", "a191", "a192",\
  "a193", "a194", "a195", "a196", "a197", "a198", "a199", "a200", "a201", "a202", "a203", "a204", "a205", "a206", "a207", "a208", "a209", "a210",\
  "a211", "a212", "a213", "a214", "a215", "a216", "a217", "a218", "a219", "a220", "a221", "a222", "a223", "a224", "a225", "a226", "a227", "a228",\
  "a229", "a230", "a231", "a232", "a233", "a234", "a235", "a236", "a237", "a238", "a239", "a240", "a241", "a242", "a243", "a244", "a245", "a246",\
  "a247", "a248", "a249", "a250", "a251", "a252", "a253", "a254", "a255", "s68", "s69", "s70", "s71", "s72", "s73", "s74", "s75", "s76", "s77", "s78",\
  "s79", "s80", "s81", "s82", "s83", "s84", "s85", "s86", "s87", "s88", "s89", "s90", "s91", "s92", "s93", "s94", "s95", "s96", "vcc", "scc", "memory"
__device__ __forceinline__ void role_gemm(const Args& a, char LAS* smem, const int gi) {
  const int w = __builtin_amdgcn_readfirstlane(threadIdx.x >> 6);
  asm volatile(GEMM_ASM
               :: "s"(a.Wq + 64 * gi), "s"(a.Wk + 64 * gi), "s"(a.Wv + 64 * gi), "s"(a.x), "s"(a.ex + WS_Q_OFF), "s"(a.ctrl), "s"(gi), "s"(w), "s"((u32)(uintptr_t)smem), "s"(wait_pol(a.ctrl))
               : BULK_CLOBBERS);
}
__device__ __forceinline__ void role_xo(const Args& a, char LAS* smem, const int xi) {
  const int w = __builtin_amdgcn_readfirstlane(threadIdx.x >> 6);
  sleepn(XO_SLEEP);
  asm volatile(XO_ASM
               :: "s"(a.Wo + 64 * (xi >> 1)), "s"(a.ex + WS_OX_OFF), "s"(a.out), "s"(a.ctrl), "s"(xi), "s"(w), "s"((u32)(uintptr_t)smem), "s"(wait_pol(a.ctrl))
               : BULK_CLOBBERS);
}
__device__ __forceinline__ void role_xa(const Args& a, char LAS* smem, const int idx) {
  const int w = __builtin_amdgcn_readfirstlane(threadIdx.x >> 6);
  const u32 pol = wait_pol(a.ctrl);
  sleepn(250);
  asm volatile(XA_ASM
               :: "s"(a.ex), "s"(a.ex + WS_Q_OFF), "s"(a.ctrl), "s"(idx * 4 + w), "s"(pol), "s"((u32)(uintptr_t)smem + w * 2048)
               : BULK_CLOBBERS);
}
#define S_ROW 4160
#define S_LW (8 * S_ROW + 64)
#define S_WSZ (3 * S_ROW + 96 * 80 + 2048 + 128)
__device__ __forceinline__ void split2(float a, float b, u32& hi, u32& lo) {
  hi = pk2(a, b);
  lo = pk2(a - __uint_as_float(hi << 16), b - __uint_as_float(hi & 0xffff0000u));
}
__device__ __forceinline__ void cexp1(float ar, float ai, float l, float& xr, float& xi) {
  const float e = __builtin_amdgcn_exp2f(ar * l);
  const float rv = __builtin_amdgcn_fractf(ai * l);
  xr = e * __builtin_amdgcn_cosf(rv); xi = e * __builtin_amdgcn_sinf(rv);
}
__device__ __forceinline__ u32 lo_hi(u32 lo, u32 hi) { return __builtin_amdgcn_perm(hi, lo, 0x07060100u); }
__device__ __forceinline__ void role_ssm(const Args& a, char LAS* smem, const int first, const int last, const int stride) {
  const int tid = threadIdx.x, lane = tid & 63, w = __builtin_amdgcn_readfirstlane(tid >> 6);
  const int c = lane & 15, g = lane >> 4, b = lane & 31, h = lane >> 5;
  char LAS* lw = smem + S_LW + w * S_WSZ;
  char LAS* lk = lw + 3 * S_ROW + 31 * 80;
  char LAS* lp = lk + 65 * 80;
  if (tid < 32) *(LAS u32x4*)(smem + (tid >> 2) * S_ROW + (tid & 3) * 16) = (u32x4){0, 0, 0, 0};
  for (int i = lane; i < 160; i += 64) *(LAS u32x4*)(lw + 3 * S_ROW + i * 16) = (u32x4){0, 0, 0, 0};
  float la[3];
  la[0] = (float)c;
  la[1] = c < 8 ? (float)(256 + 2 * c) : (float)(512 + 256 * ((c - 8) >> 2) + 4 * ((c - 8) & 3));
  la[2] = c < 8 ? (float)(1024 + 256 * (c >> 1) + 8 * (c & 1)) : (c == 8 ? 2048.f : 0.f);
  const float ly = (float)(16 * c);
  u32x4 un[8];
  auto fetch_u = [&](int grp) {
#pragma unroll
    for (int i = 0; i < 4; ++i) {
      const u16* p = a.u + (size_t)(2 * (tid + 256 * i)) * SSM_D + grp * 8;
      un[2 * i] = *(const u32x4*)p; un[2 * i + 1] = *(const u32x4*)(p + SSM_D);
    }
  };
  u32 pr[6]; u16 pdt, pds;
  auto fetch_p = [&](int d) {
    const size_t o = (size_t)d * SSM_N + 2 * lane;
    pr[0] = *(const u32*)(a.A_re + o); pr[1] = *(const u32*)(a.A_im + o); pr[2] = *(const u32*)(a.B_re + o);
    pr[3] = *(const u32*)(a.B_im + o); pr[4] = *(const u32*)(a.C_re + o); pr[5] = *(const u32*)(a.C_im + o);
    pdt = a.log_dt[d]; pds = a.Dskip[d];
  };
#define SST(i)
  fetch_u(first); fetch_p(first * 8 + w);
  for (int grp = first; grp < last; grp += stride) {
    __syncthreads();
#pragma unroll
    for (int i = 0; i < 4; ++i)
#pragma unroll
      for (int e = 0; e < 4; ++e) {
        const u32 x0 = un[2 * i][e], x1 = un[2 * i + 1][e];
        *(LAS u32*)(smem + (2 * e) * S_ROW + 64 + (tid + 256 * i) * 4) = __builtin_amdgcn_perm(x1, x0, 0x05040100u);
        *(LAS u32*)(smem + (2 * e + 1) * S_ROW + 64 + (tid + 256 * i) * 4) = __builtin_amdgcn_perm(x1, x0, 0x07060302u);
      }
    __syncthreads();
    const int nxt = grp + stride < last ? grp + stride : grp;
    fetch_u(nxt);
    for (int half = 0; half < 2; ++half) {
      const int ch = w + 4 * half;
      char LAS* lu = smem + ch * S_ROW;
      SST(0);
      const float dt = rbf(expf(bf2f(pdt))), ds = bf2f(pds);
#pragma unroll
      for (int i = 0; i < 2; ++i) {
        auto el = [&](int k) { return __uint_as_float(i ? (pr[k] & 0xffff0000u) : (pr[k] << 16)); };
        const float br = dt * el(2), bi = dt * el(3), cr = el(4), ci = el(5);
        *(LAS f32x4*)(lp + (2 * lane + i) * 16) = (f32x4){dt * el(0) * 1.4426950408889634f, dt * el(1) * 0.15915494309189535f, cr * br - ci * bi, cr * bi + ci * br};
      }
      fetch_p(half == 0 ? grp * 8 + w + 4 : nxt * 8 + w);
#pragma unroll
      for (int i = 0; i < 5; ++i) {
        const int q = lane + 64 * i;
        if (i < 4 || lane < 4) {
          const u32x4 x = *(const LAS u32x4*)(lu + q * 16);
          u32x2 y = *(const LAS u32x2*)(lu + q * 16 + 16);
          *(LAS u32x4*)(lw + q * 16) = (u32x4){__builtin_amdgcn_alignbit(x[1], x[0], 16), __builtin_amdgcn_alignbit(x[2], x[1], 16), __builtin_amdgcn_alignbit(x[3], x[2], 16), __builtin_amdgcn_alignbit(y[0], x[3], 16)};
          *(LAS u32x4*)(lw + S_ROW + q * 16) = (u32x4){x[1], x[2], x[3], y[0]};
          *(LAS u32x4*)(lw + 2 * S_ROW + q * 16) = (u32x4){__builtin_amdgcn_alignbit(x[2], x[1], 16), __builtin_amdgcn_alignbit(x[3], x[2], 16), __builtin_amdgcn_alignbit(y[0], x[3], 16), __builtin_amdgcn_alignbit(y[1], y[0], 16)};
        }
      }
      SST(1);
      f32x4 ka[3];
#pragma unroll
      for (int t = 0; t < 3; ++t) ka[t] = (f32x4){0, 0, 0, 0};
      f32x4 pn[4];
#pragma unroll
      for (int i = 0; i < 4; ++i) pn[i] = *(const LAS f32x4*)(lp + (4 * g + i) * 16);
      for (int k = 0; k < 8; ++k) {
        f32x4 p[4];
#pragma unroll
        for (int i = 0; i < 4; ++i) { p[i] = pn[i]; pn[i] = *(const LAS f32x4*)(lp + (16 * ((k + 1) & 7) + 4 * g + i) * 16); }
        u32x4 yh, yl, xh[3], xl[3];
#pragma unroll
        for (int i = 0; i < 4; ++i) {
          float er, ei; u32 hi, lo;
          cexp1(p[i][0], p[i][1], ly, er, ei);
          split2(p[i][2] * er - p[i][3] * ei, -(p[i][2] * ei + p[i][3] * er), hi, lo); yh[i] = hi; yl[i] = lo;
#pragma unroll
          for (int t = 0; t < 3; ++t) { cexp1(p[i][0], p[i][1], la[t], er, ei); split2(er, ei, hi, lo); xh[t][i] = hi; xl[t][i] = lo; }
        }
        __builtin_amdgcn_sched_barrier(0);
#pragma unroll
        for (int t = 0; t < 3; ++t) ka[t] = MFMA32(asb(xh[t]), asb(yh), ka[t]);
#pragma unroll
        for (int t = 0; t < 3; ++t) ka[t] = MFMA32(asb(xh[t]), asb(yl), ka[t]);
#pragma unroll
        for (int t = 0; t < 3; ++t) ka[t] = MFMA32(asb(xl[t]), asb(yh), ka[t]);
        __builtin_amdgcn_sched_barrier(0);
      }
      SST(2);
      {
        const int l = 16 * c + 4 * g;
        *(LAS u32x2*)(lk + (1 + (l >> 5)) * 80 + ((l & 31) ^ 4) * 2) = (u32x2){pk2(ka[0][3], ka[0][2]), pk2(ka[0][1], ka[0][0])};
        const int j1 = g < 2 ? 8 * c + 4 * g : 128 + 64 * (g - 2) + 4 * c;
        *(LAS u32x4*)(lp + j1 * 4) = (u32x4){pk2(ka[1][0], ka[1][0]), pk2(ka[1][1], ka[1][1]), pk2(ka[1][2], ka[1][2]), pk2(ka[1][3], ka[1][3])};
        if (g < 2) {
          *(LAS u32x2*)(lp + (256 + 64 * g + 2 * c) * 4) = (u32x2){pk2(ka[2][0], ka[2][0]), pk2(ka[2][1], ka[2][1])};
          *(LAS u32x2*)(lp + (256 + 64 * g + 32 + 2 * c) * 4) = (u32x2){pk2(ka[2][2], ka[2][2]), pk2(ka[2][3], ka[2][3])};
        } else if (lane == 32) *(LAS u32*)(lp + 384 * 4) = pk2(ka[2][0], ka[2][0]);
        if (lane < 32) {
          const u32x4 v = *(const LAS u32x4*)(lp + lane * 16);
          const u32 v4 = *(const LAS u32*)(lp + lane * 16 + 16);
          *(LAS u32x4*)(lk + (9 + (lane >> 2)) * 80 + (lane & 3) * 16) = (u32x4){lo_hi(v4, v[3]), v[2], lo_hi(v[2], v[1]), v[0]};
        }
        {
          const u32x2 v = *(const LAS u32x2*)(lp + 512 + lane * 8);
          const u32 v2 = *(const LAS u32*)(lp + 512 + lane * 8 + 8);
          *(LAS u32x4*)(lk + (17 + (lane >> 2)) * 80 + (lane & 3) * 16) = (u32x4){v2, v[1], lo_hi(v[1], v[0]), v[0]};
        }
        {
          const u32x2 v = *(const LAS u32x2*)(lp + 1024 + lane * 8);
          const u32 v2 = *(const LAS u32*)(lp + 1024 + lane * 8 + 8);
          char LAS* o = lk + (33 + (lane >> 1)) * 80 + (lane & 1) * 32;
          *(LAS u32x4*)o = (u32x4){v[1], lo_hi(v[1], v[0]), v[0], v[0]};
          *(LAS u32x4*)(o + 16) = (u32x4){v2, v2, v[1], v[1]};
        }
      }
      SST(3);
      const int p0 = 32 + b - 8 * h - 7, cp = p0 & 3;
      const char LAS* ra = (cp == 0 ? lu : lw + (cp - 1) * S_ROW) + (p0 - cp) * 2 - 32;
      const char LAS* rb = lk + 16 * h + 80 * (b - 30);
      f32x16 y0, y1;
      asm volatile(CONV_ASM : "=&v"(y0), "=&v"(y1) : "v"((u32)(uintptr_t)ra), "v"((u32)(uintptr_t)rb) : CONV_CLOBBERS);
      SST(4);
#pragma unroll
      for (int cg = 0; cg < 2; ++cg)
#pragma unroll
        for (int i4 = 0; i4 < 4; ++i4) {
          LAS u32x2* up = (LAS u32x2*)(lu + 64 + (32 * (32 * cg + b) + 8 * i4 + 4 * h) * 2);
          const u32x2 uv = *up;
          float v[4];
#pragma unroll
          for (int e = 0; e < 4; ++e) v[e] = cg ? y1[4 * i4 + e] : y0[4 * i4 + e];
          const u32 ya = pk2(v[0], v[1]), yb = pk2(v[2], v[3]);
          const u32 sa = pk2(__uint_as_float(uv[0] << 16) * ds, __uint_as_float(uv[0] & 0xffff0000u) * ds);
          const u32 sb = pk2(__uint_as_float(uv[1] << 16) * ds, __uint_as_float(uv[1] & 0xffff0000u) * ds);
          *up = (u32x2){pk2(__uint_as_float(ya << 16) + __uint_as_float(sa << 16), __uint_as_float(ya & 0xffff0000u) + __uint_as_float(sa & 0xffff0000u)),
                        pk2(__uint_as_float(yb << 16) + __uint_as_float(sb << 16), __uint_as_float(yb & 0xffff0000u) + __uint_as_float(sb & 0xffff0000u))};
        }
      SST(5);
    }
    __syncthreads();
#pragma unroll
    for (int i = 0; i < 4; ++i) {
      const int r = tid + 256 * i;
      u32x4 v0, v1;
#pragma unroll
      for (int e = 0; e < 4; ++e) {
        const u32 x0 = *(const LAS u32*)(smem + (2 * e) * S_ROW + 64 + r * 4), x1 = *(const LAS u32*)(smem + (2 * e + 1) * S_ROW + 64 + r * 4);
        v0[e] = __builtin_amdgcn_perm(x1, x0, 0x05040100u); v1[e] = __builtin_amdgcn_perm(x1, x0, 0x07060302u);
      }
      *(u32x4*)(a.out + (size_t)(2 * r) * OUTLD + DM + grp * 8) = v0;
      *(u32x4*)(a.out + (size_t)(2 * r + 1) * OUTLD + DM + grp * 8) = v1;
    }
  }
}
__global__ void __launch_bounds__(128) prep_kernel(u32* ctrl, char* ex) {
  __shared__ u32 best[2];
  const int b = blockIdx.x, tid = threadIdx.x, w = tid >> 6;
  for (int i = b * 128 + tid; i < CTRL_BYTES / 4; i += EX_PAGES * 128)
    if ((i < CW_POL || i >= CW_POL + 32) && (i < 6000 || i >= 6064)) ctrl[i] = 0;
  if (w == 0) {
    u32* p0 = (u32*)(ex + ((size_t)b << 21));
    u32 b0 = 0xffffffffu, b1 = 0xffffffffu;
    for (int rep = 0; rep < NPROBE; ++rep) {
      u32* pp = p0 + ((rep * 5) & 31) * 64;
      u64 t0 = __builtin_amdgcn_s_memtime();
      u32 r0 = __hip_atomic_fetch_or(pp, 0u, RLX_AGENT);
      asm volatile("s_waitcnt vmcnt(0)" :: "v"(r0) : "memory");
      u64 t1 = __builtin_amdgcn_s_memtime();
      u32 r1 = __hip_atomic_fetch_or(pp + 2048, 0u, RLX_AGENT);
      asm volatile("s_waitcnt vmcnt(0)" :: "v"(r1) : "memory");
      u64 t2 = __builtin_amdgcn_s_memtime();
      b0 = min(b0, (u32)(t1 - t0)); b1 = min(b1, (u32)(t2 - t1));
    }
    if (tid == 0) { best[0] = b0; best[1] = b1; }
  }
  if (tid == 0) {
    const u32 xcc = (u32)__builtin_amdgcn_s_getreg(20 | (3 << 11)) & 7u;
    const u32 fast1 = best[1] < best[0] ? 1u : 0u;
    ctrl[CW_POL + b] = xcc < 4 ? fast1 : (fast1 ^ 1u);
  }
}

__global__ void __launch_bounds__(256, 1) mega_kernel(Args a) {
  __shared__ __attribute__((aligned(16))) char smem_raw[163840];
  char LAS* smem = (char LAS*)smem_raw;
  const int tid = threadIdx.x;
  if (tid == 0) {
    const u32 xcc = (u32)__builtin_amdgcn_s_getreg(20 | (3 << 11)) & 7u;
    int role = -1;
    for (int k = 0; k < 8 && role < 0; ++k) {
      const u32 cc = (xcc + k) & 7u;
      const u32 t = __hip_atomic_fetch_add(a.ctrl + CW_TICKET + cc * 32, 1u, RLX_AGENT);
      if (t < 32) role = (int)(cc * 32 + t);
    }
    *(LAS int*)smem = role;
  }
  __syncthreads();
  const int role = __builtin_amdgcn_readfirstlane(*(LAS int*)smem);
  __syncthreads();
  if (role < 0) return;
  if (role < 128) role_rec(a, smem, role);
  else {
    const int cx = (role - 128) >> 5, t = role & 31;
    if (t < 8) role_xo(a, smem, cx * 8 + t);
    else if (t < 10) role_xa(a, smem, cx * 2 + t - 8);
    else if (t < 16) role_ssm(a, smem, cx * 32 + t - 10, cx * 32 + 32, 6);
    else role_gemm(a, smem, cx * 16 + t - 16);
  }
}

extern "C" void kernel_launch(void* const* d_in, const int* in_sizes, int n_in, void* d_out, int out_size,
                              void* d_ws, size_t ws_size, hipStream_t stream) {
  Args a;
  a.x = (const u16*)d_in[0]; a.mem0 = (const u16*)d_in[1];
  a.Wq = (const u16*)d_in[2]; a.Wk = (const u16*)d_in[3]; a.Wv = (const u16*)d_in[4]; a.Wo = (const u16*)d_in[5];
  a.u = (const u16*)d_in[7]; a.A_re = (const u16*)d_in[8]; a.A_im = (const u16*)d_in[9];
  a.B_re = (const u16*)d_in[10]; a.B_im = (const u16*)d_in[11]; a.C_re = (const u16*)d_in[12]; a.C_im = (const u16*)d_in[13];
  a.log_dt = (const u16*)d_in[14]; a.Dskip = (const u16*)d_in[15];
  a.out = (u16*)d_out;
  char* ws = (char*)d_ws;
  a.ctrl = (u32*)ws;
  a.ex = (char*)(((uintptr_t)ws + CTRL_BYTES + (2u << 20) - 1) & ~(uintptr_t)((2u << 20) - 1));
  prep_kernel<<<EX_PAGES, 128, 0, stream>>>(a.ctrl, a.ex);
  mega_kernel<<<256, 256, 0, stream>>>(a);
}
```

```cpp
#define XO_SLEEP 0
#define T_Q 1300
#include <hip/hip_runtime.h>
#include <hip/hip_bf16.h>
#include <stdint.h>

#define S_LEN 2048
#define DM 1024
#define NH 32
#define DH 128
#define HD 4096
#define NSEG 64
#define SSM_L 2048
#define SSM_D 1024
#define SSM_N 128
#define OUTLD 2048

typedef unsigned short u16;
typedef unsigned int u32;
typedef unsigned long long u64;
typedef __attribute__((ext_vector_type(8))) short bf16x8;
typedef __attribute__((ext_vector_type(4))) short bf16x4;
typedef __attribute__((ext_vector_type(4))) float f32x4;
typedef __attribute__((ext_vector_type(16))) float f32x16;
typedef __attribute__((ext_vector_type(4))) unsigned int u32x4;
typedef __attribute__((ext_vector_type(2))) unsigned int u32x2;
typedef __attribute__((ext_vector_type(2))) float f32x2;
typedef __attribute__((ext_vector_type(2))) __bf16 bf2_t;
typedef short v4i16_t __attribute__((ext_vector_type(4)));
#define LAS __attribute__((address_space(3)))

#define SENT 0xFFFFFFFFu
#define RLX_AGENT __ATOMIC_RELAXED, __HIP_MEMORY_SCOPE_AGENT
#define SC1 16

__device__ __forceinline__ float bf2f(u16 v) { return __uint_as_float(((u32)v) << 16); }
__device__ __forceinline__ u32 pk2(float a, float b) {
  f32x2 f = {a, b};
  bf2_t h = __builtin_convertvector(f, bf2_t);
  return __builtin_bit_cast(u32, h);
}
__device__ __forceinline__ float rbf(float f) { return __uint_as_float(pk2(f, 0.f) << 16); }
__device__ __forceinline__ u32 nosent(u32 v) { return v < 0xFFFFFFFEu ? v : 0xFFFFFFFEu; }
__device__ __forceinline__ bool okv(u32x4 v) { return v[0] != SENT && v[1] != SENT && v[2] != SENT && v[3] != SENT; }
__device__ __forceinline__ bf16x8 asb(u32x4 v) { return __builtin_bit_cast(bf16x8, v); }
__device__ __forceinline__ void sleepn(int n) { for (int k = 0; k < n; ++k) __builtin_amdgcn_s_sleep(1); }
#define WAIT_VM0() asm volatile("s_waitcnt vmcnt(0)" ::: "memory")
#define BAR() do { asm volatile("s_waitcnt lgkmcnt(0)" ::: "memory"); __builtin_amdgcn_s_barrier(); asm volatile("" ::: "memory"); } while (0)

__device__ __forceinline__ float xmax16(float x) {
  auto r = __builtin_amdgcn_permlane16_swap(__float_as_uint(x), __float_as_uint(x), false, false);
  return fmaxf(__uint_as_float(r[0]), __uint_as_float(r[1]));
}
__device__ __forceinline__ float xmax32(float x) {
  auto r = __builtin_amdgcn_permlane32_swap(__float_as_uint(x), __float_as_uint(x), false, false);
  return fmaxf(__uint_as_float(r[0]), __uint_as_float(r[1]));
}
__device__ __forceinline__ float xadd16(float x) {
  auto r = __builtin_amdgcn_permlane16_swap(__float_as_uint(x), __float_as_uint(x), false, false);
  return __uint_as_float(r[0]) + __uint_as_float(r[1]);
}
__device__ __forceinline__ float xadd32(float x) {
  auto r = __builtin_amdgcn_permlane32_swap(__float_as_uint(x), __float_as_uint(x), false, false);
  return __uint_as_float(r[0]) + __uint_as_float(r[1]);
}
__device__ __forceinline__ u32 ror8(u32 x) { return (u32)__builtin_amdgcn_update_dpp(0, (int)x, 0x128, 0xf, 0xf, true); }
template <int N> __device__ __forceinline__ float rorf(float x) {
  return __uint_as_float((u32)__builtin_amdgcn_update_dpp(0, (int)__float_as_uint(x), 0x120 + N, 0xf, 0xf, true));
}
__device__ __forceinline__ float ror8f(float x) { return __uint_as_float(ror8(__float_as_uint(x))); }

#define MFMA32(a, b, c) __builtin_amdgcn_mfma_f32_16x16x32_bf16(a, b, c, 0, 0, 0)
#define MFMA16(a, b, c) __builtin_amdgcn_mfma_f32_16x16x16bf16_1k(a, b, c, 0, 0, 0)
#define MFMA_AV0(acc, wa, xb) asm volatile("v_mfma_f32_16x16x32_bf16 %0, %1, %2, 0" : "=&v"(acc) : "a"(wa), "v"(xb))
#define MFMA_AV(acc, wa, xb) asm volatile("v_mfma_f32_16x16x32_bf16 %0, %1, %2, %0" : "+v"(acc) : "a"(wa), "v"(xb))
#define MFMA_VA0(acc, xa, wb) asm volatile("v_mfma_f32_16x16x32_bf16 %0, %1, %2, 0" : "=&v"(acc) : "v"(xa), "a"(wb))
#define MFMA_VA(acc, xa, wb) asm volatile("v_mfma_f32_16x16x32_bf16 %0, %1, %2, %0" : "+v"(acc) : "v"(xa), "a"(wb))
#define MFMA_VV0(acc, xa, xb) asm volatile("v_mfma_f32_16x16x32_bf16 %0, %1, %2, 0" : "=&v"(acc) : "v"(xa), "v"(xb))
#define MFMA16_VV(acc, xa, xb) asm volatile("v_mfma_f32_16x16x16_bf16 %0, %1, %2, %0" : "+v"(acc) : "v"(xa), "v"(xb))
#define MFMA_ACC(acc, xa, xb) asm volatile("v_mfma_f32_16x16x32_bf16 %0, %1, %2, %0" : "+a"(acc) : "v"(xa), "v"(xb))
#define NOP_IN() asm volatile("s_nop 1" ::: "memory")
#define PIN(x) asm volatile("" : "+v"(x))
#define PINA(x) asm volatile("" : "+a"(x))

__device__ __forceinline__ bf16x4 trread(const char LAS* p) {
  return __builtin_amdgcn_ds_read_tr16_b64_v4i16((LAS v4i16_t*)p);
}
__device__ __forceinline__ bf16x8 cat8(bf16x4 a, bf16x4 b) {
  return __builtin_shufflevector(a, b, 0, 1, 2, 3, 4, 5, 6, 7);
}

#define CW_TICKET 0
#define CW_POL 256
#define CW_INIT 288
#define CW_SSMQ 352
#define CW_DEAD 320
#define CW_PROG 384
#define CW_XPROG 1408
#define CW_GPROG 3456
#define CW_UIN 2432
#define CW_YOUT 8192
#define CTRL_BYTES 49152
#define EX_PAGES 24
#define EX_BYTES ((u64)EX_PAGES << 21)
#define EXP_C 0
#define EXP_A 20
#define EXP_B 2
#define EXP_DBG 19
#define QKV_SZ ((u64)S_LEN * HD * 4)
#define WS_Q_OFF (EX_BYTES)
#define WS_K_OFF (WS_Q_OFF + QKV_SZ)
#define WS_VT_OFF (WS_K_OFF + QKV_SZ)
#define WS_OX_OFF (WS_VT_OFF + QKV_SZ)
#define WS_UT_OFF (WS_OX_OFF + QKV_SZ)
#define EX_BYTES32 50331648
#define QKV_SZ32 33554432
#define WS_K_OFF32 83886080
static_assert(EX_BYTES32 == EX_BYTES && QKV_SZ32 == QKV_SZ && WS_K_OFF32 == WS_K_OFF, "layout");

#define SPIN_CAP 300000
#define NPROBE 5
#define F_B 1520
#define F_C 3390
#define F_P 4510
#ifndef M_C
#define M_C 150
#endif
#ifndef M_A
#define M_A 150
#endif
#define T_TOL 100
#define D_K 0
#define DECAY 0.998
#define RI0 1.06
#define T_LATE 50
#define O_A 130
#define O_B 150
#define O_C 60
#define GAP_B 4
#define GAP_V 4
#define GAP_0 3
#define D_PF (-200)
#define STR_(x) #x
#define STR(x) STR_(x)

struct Args {
  const u16 *x, *mem0, *Wq, *Wk, *Wv, *Wo;
  const u16 *u, *A_re, *A_im, *B_re, *B_im, *C_re, *C_im, *log_dt, *Dskip;
  u16* out;
  u32* ctrl;
  char* ex;
};

__device__ __forceinline__ bool is_dead(u32* ctrl) { return __hip_atomic_load(ctrl + CW_DEAD, RLX_AGENT) != 0; }
__device__ __forceinline__ void set_dead(u32* ctrl) { __hip_atomic_store(ctrl + CW_DEAD, 1u, RLX_AGENT); }
__device__ __forceinline__ void wait_ge(u32* ctrl, int word, u32 want, int slp) {
  for (int spins = 0;; ++spins) {
    asm volatile("" ::: "memory");
    u32 v = __builtin_amdgcn_readfirstlane(__hip_atomic_load(ctrl + word, RLX_AGENT));
    if (v >= want) break;
    if (spins > SPIN_CAP) { set_dead(ctrl); break; }
    if ((spins & 63) == 63 && __builtin_amdgcn_readfirstlane((int)is_dead(ctrl))) break;
    sleepn(slp);
  }
}
__device__ __forceinline__ u32 wait_pol(u32* ctrl) {
  const int lane = threadIdx.x & 63;
  u32 v = lane < EX_PAGES ? (ctrl[CW_POL + lane] & 1u) : 0u;
  u32 m = (u32)__builtin_amdgcn_readfirstlane((u32)__ballot(v != 0));
  const u32 full = (1u << EX_PAGES) - 1u;
  m = __builtin_popcount((m ^ 0xAAAAAAAAu) & full) <= EX_PAGES / 2 ? 0xAAAAAAAAu : 0x55555555u;
  return m;
}
__device__ __forceinline__ u32 exoff(u32 pol, u32 page, u32 pair) { return (page << 21) + (pair << 14) + (((pol >> page) & 1u) << 13); }
__device__ __forceinline__ u32 offB(u32 pol, int s, int h) { u32 pr = 32 * s + h; return exoff(pol, EXP_B + (pr >> 7), pr & 127); }
__device__ __forceinline__ u32 max4(u32x4 v) { return max(max(v[0], v[1]), max(v[2], v[3])); }
#define POLL_FAIL(spins) ((++(spins) > SPIN_CAP) ? (set_dead(ctrl), true) : (((spins) & 255) == 255 && __builtin_amdgcn_readfirstlane((int)is_dead(ctrl))))
#define REC_ASM \
  "s_mov_b64 exec, -1\n" \
  "v_mbcnt_lo_u32_b32 v0, -1, 0\n" \
  "v_mbcnt_hi_u32_b32 v0, -1, v0\n" \
  "s_lshr_b32 s70, %6, 2\n" \
  "s_and_b32 s71, %6, 3\n" \
  "v_lshlrev_b32 v1, 4, v0\n" \
  "v_and_b32 v27, 7, v0\n" \
  "v_lshrrev_b32 v33, 2, v0\n" \
  "v_lshlrev_b32 v33, 13, v33\n" \
  "v_and_b32 v28, 3, v0\n" \
  "v_lshl_add_u32 v33, v28, 4, v33\n" \
  "v_lshrrev_b32 v34, 3, v0\n" \
  "v_lshlrev_b32 v34, 11, v34\n" \
  "v_lshl_add_u32 v34, v27, 4, v34\n" \
  "s_lshl_b32 s60, %7, 21\n" \
  "s_mov_b64 s[36:37], %0\n" \
  "s_and_b32 s37, s37, 0xffff\n" \
  "s_mov_b32 s38, 8388608\n" \
  "s_mov_b32 s39, 0x00020000\n" \
  "s_mov_b64 s[40:41], %1\n" \
  "s_and_b32 s41, s41, 0xffff\n" \
  "s_mov_b32 s42, 8388608\n" \
  "s_mov_b32 s43, 0x00020000\n" \
  "s_mov_b64 s[44:45], %2\n" \
  "s_and_b32 s45, s45, 0xffff\n" \
  "s_mov_b32 s46, 8388608\n" \
  "s_mov_b32 s47, 0x00020000\n" \
  "s_add_u32 s59, %7, %6\n" \
  "s_add_u32 s59, s59, 0\n" \
  "s_and_b32 s59, s59, 3\n" \
  "s_lshl_b32 s59, s59, 19\n" \
  "s_add_u32 s59, s59, s60\n" \
  "buffer_load_dwordx4 a[0:3], v33, s[36:39], s59 offen\n" \
  "s_add_u32 s59, s59, 131072\n" \
  "buffer_load_dwordx4 a[4:7], v33, s[36:39], s59 offen\n" \
  "s_add_u32 s59, s59, 131072\n" \
  "buffer_load_dwordx4 a[8:11], v33, s[36:39], s59 offen\n" \
  "s_add_u32 s59, s59, 131072\n" \
  "buffer_load_dwordx4 a[12:15], v33, s[36:39], s59 offen\n" \
  "s_add_u32 s59, %7, %6\n" \
  "s_add_u32 s59, s59, 1\n" \
  "s_and_b32 s59, s59, 3\n" \
  "s_lshl_b32 s59, s59, 19\n" \
  "s_add_u32 s59, s59, s60\n" \
  "buffer_load_dwordx4 a[16:19], v33, s[36:39], s59 offen\n" \
  "s_add_u32 s59, s59, 131072\n" \
  "buffer_load_dwordx4 a[20:23], v33, s[36:39], s59 offen\n" \
  "s_add_u32 s59, s59, 131072\n" \
  "buffer_load_dwordx4 a[24:27], v33, s[36:39], s59 offen\n" \
  "s_add_u32 s59, s59, 131072\n" \
  "buffer_load_dwordx4 a[28:31], v33, s[36:39], s59 offen\n" \
  "s_add_u32 s59, %7, %6\n" \
  "s_add_u32 s59, s59, 2\n" \
  "s_and_b32 s59, s59, 3\n" \
  "s_lshl_b32 s59, s59, 19\n" \
  "s_add_u32 s59, s59, s60\n" \
  "buffer_load_dwordx4 a[32:35], v33, s[36:39], s59 offen\n" \
  "s_add_u32 s59, s59, 131072\n" \
  "buffer_load_dwordx4 a[36:39], v33, s[36:39], s59 offen\n" \
  "s_add_u32 s59, s59, 131072\n" \
  "buffer_load_dwordx4 a[40:43], v33, s[36:39], s59 offen\n" \
  "s_add_u32 s59, s59, 131072\n" \
  "buffer_load_dwordx4 a[44:47], v33, s[36:39], s59 offen\n" \
  "s_add_u32 s59, %7, %6\n" \
  "s_add_u32 s59, s59, 3\n" \
  "s_and_b32 s59, s59, 3\n" \
  "s_lshl_b32 s59, s59, 19\n" \
  "s_add_u32 s59, s59, s60\n" \
  "buffer_load_dwordx4 a[48:51], v33, s[36:39], s59 offen\n" \
  "s_add_u32 s59, s59, 131072\n" \
  "buffer_load_dwordx4 a[52:55], v33, s[36:39], s59 offen\n" \
  "s_add_u32 s59, s59, 131072\n" \
  "buffer_load_dwordx4 a[56:59], v33, s[36:39], s59 offen\n" \
  "s_add_u32 s59, s59, 131072\n" \
  "buffer_load_dwordx4 a[60:63], v33, s[36:39], s59 offen\n" \
  "s_add_u32 s59, %7, %6\n" \
  "s_add_u32 s59, s59, 0\n" \
  "s_and_b32 s59, s59, 3\n" \
  "s_lshl_b32 s59, s59, 19\n" \
  "s_add_u32 s59, s59, s60\n" \
  "buffer_load_dwordx4 a[64:67], v33, s[40:43], s59 offen\n" \
  "s_add_u32 s59, s59, 131072\n" \
  "buffer_load_dwordx4 a[68:71], v33, s[40:43], s59 offen\n" \
  "s_add_u32 s59, s59, 131072\n" \
  "buffer_load_dwordx4 a[72:75], v33, s[40:43], s59 offen\n" \
  "s_add_u32 s59, s59, 131072\n" \
  "buffer_load_dwordx4 a[76:79], v33, s[40:43], s59 offen\n" \
  "s_add_u32 s59, %7, %6\n" \
  "s_add_u32 s59, s59, 1\n" \
  "s_and_b32 s59, s59, 3\n" \
  "s_lshl_b32 s59, s59, 19\n" \
  "s_add_u32 s59, s59, s60\n" \
  "buffer_load_dwordx4 a[80:83], v33, s[40:43], s59 offen\n" \
  "s_add_u32 s59, s59, 131072\n" \
  "buffer_load_dwordx4 a[84:87], v33, s[40:43], s59 offen\n" \
  "s_add_u32 s59, s59, 131072\n" \
  "buffer_load_dwordx4 a[88:91], v33, s[40:43], s59 offen\n" \
  "s_add_u32 s59, s59, 131072\n" \
  "buffer_load_dwordx4 a[92:95], v33, s[40:43], s59 offen\n" \
  "s_add_u32 s59, %7, %6\n" \
  "s_add_u32 s59, s59, 2\n" \
  "s_and_b32 s59, s59, 3\n" \
  "s_lshl_b32 s59, s59, 19\n" \
  "s_add_u32 s59, s59, s60\n" \
  "buffer_load_dwordx4 a[96:99], v33, s[40:43], s59 offen\n" \
  "s_add_u32 s59, s59, 131072\n" \
  "buffer_load_dwordx4 a[100:103], v33, s[40:43], s59 offen\n" \
  "s_add_u32 s59, s59, 131072\n" \
  "buffer_load_dwordx4 a[104:107], v33, s[40:43], s59 offen\n" \
  "s_add_u32 s59, s59, 131072\n" \
  "buffer_load_dwordx4 a[108:111], v33, s[40:43], s59 offen\n" \
  "s_add_u32 s59, %7, %6\n" \
  "s_add_u32 s59, s59, 3\n" \
  "s_and_b32 s59, s59, 3\n" \
  "s_lshl_b32 s59, s59, 19\n" \
  "s_add_u32 s59, s59, s60\n" \
  "buffer_load_dwordx4 a[112:115], v33, s[40:43], s59 offen\n" \
  "s_add_u32 s59, s59, 131072\n" \
  "buffer_load_dwordx4 a[116:119], v33, s[40:43], s59 offen\n" \
  "s_add_u32 s59, s59, 131072\n" \
  "buffer_load_dwordx4 a[120:123], v33, s[40:43], s59 offen\n" \
  "s_add_u32 s59, s59, 131072\n" \
  "buffer_load_dwordx4 a[124:127], v33, s[40:43], s59 offen\n" \
  "s_add_u32 s59, %7, %6\n" \
  "s_add_u32 s59, s59, 0\n" \
  "s_and_b32 s59, s59, 3\n" \
  "s_lshl_b32 s59, s59, 19\n" \
  "s_add_u32 s59, s59, s60\n" \
  "buffer_load_dwordx4 a[128:131], v33, s[44:47], s59 offen\n" \
  "s_add_u32 s59, s59, 131072\n" \
  "buffer_load_dwordx4 a[132:135], v33, s[44:47], s59 offen\n" \
  "s_add_u32 s59, s59, 131072\n" \
  "buffer_load_dwordx4 a[136:139], v33, s[44:47], s59 offen\n" \
  "s_add_u32 s59, s59, 131072\n" \
  "buffer_load_dwordx4 a[140:143], v33, s[44:47], s59 offen\n" \
  "s_add_u32 s59, %7, %6\n" \
  "s_add_u32 s59, s59, 1\n" \
  "s_and_b32 s59, s59, 3\n" \
  "s_lshl_b32 s59, s59, 19\n" \
  "s_add_u32 s59, s59, s60\n" \
  "buffer_load_dwordx4 a[144:147], v33, s[44:47], s59 offen\n" \
  "s_add_u32 s59, s59, 131072\n" \
  "buffer_load_dwordx4 a[148:151], v33, s[44:47], s59 offen\n" \
  "s_add_u32 s59, s59, 131072\n" \
  "buffer_load_dwordx4 a[152:155], v33, s[44:47], s59 offen\n" \
  "s_add_u32 s59, s59, 131072\n" \
  "buffer_load_dwordx4 a[156:159], v33, s[44:47], s59 offen\n" \
  "s_add_u32 s59, %7, %6\n" \
  "s_add_u32 s59, s59, 2\n" \
  "s_and_b32 s59, s59, 3\n" \
  "s_lshl_b32 s59, s59, 19\n" \
  "s_add_u32 s59, s59, s60\n" \
  "buffer_load_dwordx4 a[160:163], v33, s[44:47], s59 offen\n" \
  "s_add_u32 s59, s59, 131072\n" \
  "buffer_load_dwordx4 a[164:167], v33, s[44:47], s59 offen\n" \
  "s_add_u32 s59, s59, 131072\n" \
  "buffer_load_dwordx4 a[168:171], v33, s[44:47], s59 offen\n" \
  "s_add_u32 s59, s59, 131072\n" \
  "buffer_load_dwordx4 a[172:175], v33, s[44:47], s59 offen\n" \
  "s_add_u32 s59, %7, %6\n" \
  "s_add_u32 s59, s59, 3\n" \
  "s_and_b32 s59, s59, 3\n" \
  "s_lshl_b32 s59, s59, 19\n" \
  "s_add_u32 s59, s59, s60\n" \
  "buffer_load_dwordx4 a[176:179], v33, s[44:47], s59 offen\n" \
  "s_add_u32 s59, s59, 131072\n" \
  "buffer_load_dwordx4 a[180:183], v33, s[44:47], s59 offen\n" \
  "s_add_u32 s59, s59, 131072\n" \
  "buffer_load_dwordx4 a[184:187], v33, s[44:47], s59 offen\n" \
  "s_add_u32 s59, s59, 131072\n" \
  "buffer_load_dwordx4 a[188:191], v33, s[44:47], s59 offen\n" \
  "s_mov_b64 s[36:37], %3\n" \
  "s_and_b32 s37, s37, 0xffff\n" \
  "s_mov_b32 s38, 262144\n" \
  "s_mov_b32 s39, 0x00020000\n" \
  "s_waitcnt vmcnt(48)\n" \
  "s_barrier\n" \
  "s_lshl_b32 s59, %7, 7\n" \
  "s_waitcnt vmcnt(46)\n" \
  "buffer_load_dwordx4 a[192:195], v34, s[36:39], s59 offen\n" \
  "s_add_u32 s59, s59, 16384\n" \
  "buffer_load_dwordx4 a[196:199], v34, s[36:39], s59 offen\n" \
  "s_add_u32 s59, s59, 16384\n" \
  "buffer_load_dwordx4 a[200:203], v34, s[36:39], s59 offen\n" \
  "s_add_u32 s59, s59, 16384\n" \
  "buffer_load_dwordx4 a[204:207], v34, s[36:39], s59 offen\n" \
  "s_add_u32 s59, s59, 16384\n" \
  "buffer_load_dwordx4 a[208:211], v34, s[36:39], s59 offen\n" \
  "s_add_u32 s59, s59, 16384\n" \
  "buffer_load_dwordx4 a[212:215], v34, s[36:39], s59 offen\n" \
  "s_add_u32 s59, s59, 16384\n" \
  "buffer_load_dwordx4 a[216:219], v34, s[36:39], s59 offen\n" \
  "s_add_u32 s59, s59, 16384\n" \
  "buffer_load_dwordx4 a[220:223], v34, s[36:39], s59 offen\n" \
  "s_add_u32 s59, s59, 16384\n" \
  "buffer_load_dwordx4 a[224:227], v34, s[36:39], s59 offen\n" \
  "s_add_u32 s59, s59, 16384\n" \
  "buffer_load_dwordx4 a[228:231], v34, s[36:39], s59 offen\n" \
  "s_add_u32 s59, s59, 16384\n" \
  "buffer_load_dwordx4 a[232:235], v34, s[36:39], s59 offen\n" \
  "s_add_u32 s59, s59, 16384\n" \
  "buffer_load_dwordx4 a[236:239], v34, s[36:39], s59 offen\n" \
  "s_add_u32 s59, s59, 16384\n" \
  "buffer_load_dwordx4 a[240:243], v34, s[36:39], s59 offen\n" \
  "s_add_u32 s59, s59, 16384\n" \
  "buffer_load_dwordx4 a[244:247], v34, s[36:39], s59 offen\n" \
  "s_add_u32 s59, s59, 16384\n" \
  "buffer_load_dwordx4 a[248:251], v34, s[36:39], s59 offen\n" \
  "s_add_u32 s59, s59, 16384\n" \
  "buffer_load_dwordx4 a[252:255], v34, s[36:39], s59 offen\n" \
  "s_mov_b64 s[44:45], %5\n" \
  "s_and_b32 s45, s45, 0xffff\n" \
  "s_mov_b32 s46, " STR(CTRL_BYTES) "\n" \
  "s_mov_b32 s47, 0x00020000\n" \
  "v_mov_b32 v27, " STR(CW_INIT) "*4\n" \
  "v_mov_b32 v28, 1\n" \
  "v_or_b32 v25, %7, v0\n" \
  "v_lshl_add_u32 v27, v25, 24, v27\n" \
  "s_waitcnt vmcnt(60)\n" \
  "buffer_atomic_add v28, v27, s[44:47], 0 offen\n" \
  "s_nop 1\n" \
  "s_memtime s[72:73]\n" \
  "s_memrealtime s[74:75]\n" \
  "s_nop 1\n" \
  "s_memrealtime s[76:77]\n" \
  "s_nop 1\n" \
  "s_memrealtime s[78:79]\n" \
  "s_nop 1\n" \
  "s_memrealtime s[80:81]\n" \
  "s_nop 1\n" \
  "s_memrealtime s[82:83]\n" \
  "s_nop 1\n" \
  "s_memrealtime s[84:85]\n" \
  "s_nop 1\n" \
  "s_memrealtime s[86:87]\n" \
  "s_nop 1\n" \
  "s_memrealtime s[88:89]\n" \
  "s_nop 1\n" \
  "s_memrealtime s[90:91]\n" \
  "s_mov_b32 s59, 0\n" \
  "s_waitcnt lgkmcnt(0)\n" \
  "s_cmp_eq_u32 s76, s74\n" \
  "s_addc_u32 s59, s59, 0\n" \
  "s_cmp_eq_u32 s78, s74\n" \
  "s_addc_u32 s59, s59, 0\n" \
  "s_cmp_eq_u32 s80, s74\n" \
  "s_addc_u32 s59, s59, 0\n" \
  "s_cmp_eq_u32 s82, s74\n" \
  "s_addc_u32 s59, s59, 0\n" \
  "s_cmp_eq_u32 s84, s74\n" \
  "s_addc_u32 s59, s59, 0\n" \
  "s_cmp_eq_u32 s86, s74\n" \
  "s_addc_u32 s59, s59, 0\n" \
  "s_cmp_eq_u32 s88, s74\n" \
  "s_addc_u32 s59, s59, 0\n" \
  "s_cmp_eq_u32 s90, s74\n" \
  "s_addc_u32 s59, s59, 0\n" \
  "s_mul_i32 s59, s59, 12\n" \
  "s_add_u32 s68, s72, s59\n" \
  "s_add_u32 s59, s74, 4\n" \
  "s_mul_i32 s69, s59, 24\n" \
  "s_mov_b32 s62, s69\n" \
  "s_mov_b32 s56, s68\n" \
  "s_mul_i32 s59, %7, 40960\n" \
  "s_add_u32 s94, s59, %9\n" \
  "v_and_b32 v36, 15, v0\n" \
  "v_lshrrev_b32 v37, 4, v0\n" \
  "v_lshrrev_b32 v38, 2, v36\n" \
  "v_and_b32 v39, 3, v0\n" \
  "v_lshrrev_b32 v27, 5, v0\n" \
  "v_lshlrev_b32 v27, 7, v27\n" \
  "v_xor_b32 v40, v1, v27\n" \
  "v_and_b32 v27, 1, v37\n" \
  "v_lshlrev_b32 v27, 5, v27\n" \
  "v_xor_b32 v40, v40, v27\n" \
  "v_add_u32 v40, s94, v40\n" \
  "v_add_u32 v35, s94, v1\n" \
  "v_lshrrev_b32 v46, 1, v37\n" \
  "v_lshlrev_b32 v46, 4, v46\n" \
  "v_and_b32 v27, 1, v37\n" \
  "v_lshl_add_u32 v46, v27, 2, v46\n" \
  "v_and_b32 v28, 1, v38\n" \
  "v_add_u32 v46, v46, v28\n" \
  "v_lshrrev_b32 v28, 1, v38\n" \
  "v_lshl_add_u32 v46, v28, 3, v46\n" \
  "v_lshlrev_b32 v27, 5, v27\n" \
  "v_lshl_add_u32 v27, v28, 7, v27\n" \
  "v_lshlrev_b32 v28, 6, v46\n" \
  "v_lshl_add_u32 v28, v39, 3, v28\n" \
  "v_xor_b32 v27, v27, v28\n" \
  "v_xor_b32 v28, 0, v27\n" \
  "v_add_u32 v42, s94, v28\n" \
  "v_xor_b32 v28, 128, v27\n" \
  "v_add_u32 v43, s94, v28\n" \
  "v_xor_b32 v28, 32, v27\n" \
  "v_add_u32 v44, s94, v28\n" \
  "v_xor_b32 v28, 160, v27\n" \
  "v_add_u32 v45, s94, v28\n" \
  "v_and_b32 v27, 0xffffffef, v27\n" \
  "v_lshrrev_b32 v28, 1, v39\n" \
  "v_lshl_add_u32 v27, v28, 14, v27\n" \
  "s_add_u32 s59, %7, 0\n" \
  "s_and_b32 s59, s59, 3\n" \
  "s_lshl_b32 s59, s59, 4\n" \
  "v_xor_b32 v28, s59, v27\n" \
  "v_add_u32 v52, s94, v28\n" \
  "v_xor_b32 v28, 128, v28\n" \
  "v_add_u32 v53, s94, v28\n" \
  "s_add_u32 s59, %7, 1\n" \
  "s_and_b32 s59, s59, 3\n" \
  "s_lshl_b32 s59, s59, 4\n" \
  "v_xor_b32 v28, s59, v27\n" \
  "v_add_u32 v54, s94, v28\n" \
  "v_xor_b32 v28, 128, v28\n" \
  "v_add_u32 v55, s94, v28\n" \
  "s_add_u32 s59, %7, 2\n" \
  "s_and_b32 s59, s59, 3\n" \
  "s_lshl_b32 s59, s59, 4\n" \
  "v_xor_b32 v28, s59, v27\n" \
  "v_add_u32 v56, s94, v28\n" \
  "v_xor_b32 v28, 128, v28\n" \
  "v_add_u32 v57, s94, v28\n" \
  "s_add_u32 s59, %7, 3\n" \
  "s_and_b32 s59, s59, 3\n" \
  "s_lshl_b32 s59, s59, 4\n" \
  "v_xor_b32 v28, s59, v27\n" \
  "v_add_u32 v58, s94, v28\n" \
  "v_xor_b32 v28, 128, v28\n" \
  "v_add_u32 v59, s94, v28\n" \
  "v_lshlrev_b32 v49, 7, v46\n" \
  "v_lshl_add_u32 v49, v39, 5, v49\n" \
  "v_add_u32 v49, s94, v49\n" \
  "s_waitcnt vmcnt(61)\n" \
  "ds_write_b128 v40, a[0:3] offset:0\n" \
  "ds_write_b128 v40, a[4:7] offset:1024\n" \
  "ds_write_b128 v40, a[8:11] offset:2048\n" \
  "ds_write_b128 v40, a[12:15] offset:3072\n" \
  "s_waitcnt vmcnt(57)\n" \
  "ds_write_b128 v40, a[16:19] offset:4096\n" \
  "ds_write_b128 v40, a[20:23] offset:5120\n" \
  "ds_write_b128 v40, a[24:27] offset:6144\n" \
  "ds_write_b128 v40, a[28:31] offset:7168\n" \
  "s_waitcnt vmcnt(53)\n" \
  "ds_write_b128 v40, a[32:35] offset:8192\n" \
  "ds_write_b128 v40, a[36:39] offset:9216\n" \
  "ds_write_b128 v40, a[40:43] offset:10240\n" \
  "ds_write_b128 v40, a[44:47] offset:11264\n" \
  "s_waitcnt vmcnt(49)\n" \
  "ds_write_b128 v40, a[48:51] offset:12288\n" \
  "ds_write_b128 v40, a[52:55] offset:13312\n" \
  "ds_write_b128 v40, a[56:59] offset:14336\n" \
  "ds_write_b128 v40, a[60:63] offset:15360\n" \
  "s_waitcnt vmcnt(45)\n" \
  "ds_write_b128 v40, a[64:67] offset:16384\n" \
  "ds_write_b128 v40, a[68:71] offset:17408\n" \
  "ds_write_b128 v40, a[72:75] offset:18432\n" \
  "ds_write_b128 v40, a[76:79] offset:19456\n" \
  "s_waitcnt vmcnt(41)\n" \
  "ds_write_b128 v40, a[80:83] offset:20480\n" \
  "ds_write_b128 v40, a[84:87] offset:21504\n" \
  "ds_write_b128 v40, a[88:91] offset:22528\n" \
  "ds_write_b128 v40, a[92:95] offset:23552\n" \
  "s_waitcnt vmcnt(37)\n" \
  "ds_write_b128 v40, a[96:99] offset:24576\n" \
  "ds_write_b128 v40, a[100:103] offset:25600\n" \
  "ds_write_b128 v40, a[104:107] offset:26624\n" \
  "ds_write_b128 v40, a[108:111] offset:27648\n" \
  "s_waitcnt vmcnt(33)\n" \
  "ds_write_b128 v40, a[112:115] offset:28672\n" \
  "ds_write_b128 v40, a[116:119] offset:29696\n" \
  "ds_write_b128 v40, a[120:123] offset:30720\n" \
  "ds_write_b128 v40, a[124:127] offset:31744\n" \
  "s_waitcnt lgkmcnt(0)\n" \
  "ds_read_b64_tr_b16 a[0:1], v52 offset:0\n" \
  "ds_read_b64_tr_b16 a[2:3], v53 offset:0\n" \
  "ds_read_b64_tr_b16 a[4:5], v52 offset:2048\n" \
  "ds_read_b64_tr_b16 a[6:7], v53 offset:2048\n" \
  "ds_read_b64_tr_b16 a[8:9], v54 offset:0\n" \
  "ds_read_b64_tr_b16 a[10:11], v55 offset:0\n" \
  "ds_read_b64_tr_b16 a[12:13], v54 offset:2048\n" \
  "ds_read_b64_tr_b16 a[14:15], v55 offset:2048\n" \
  "ds_read_b64_tr_b16 a[16:17], v56 offset:0\n" \
  "ds_read_b64_tr_b16 a[18:19], v57 offset:0\n" \
  "ds_read_b64_tr_b16 a[20:21], v56 offset:2048\n" \
  "ds_read_b64_tr_b16 a[22:23], v57 offset:2048\n" \
  "ds_read_b64_tr_b16 a[24:25], v58 offset:0\n" \
  "ds_read_b64_tr_b16 a[26:27], v59 offset:0\n" \
  "ds_read_b64_tr_b16 a[28:29], v58 offset:2048\n" \
  "ds_read_b64_tr_b16 a[30:31], v59 offset:2048\n" \
  "ds_read_b64_tr_b16 a[32:33], v52 offset:4096\n" \
  "ds_read_b64_tr_b16 a[34:35], v53 offset:4096\n" \
  "ds_read_b64_tr_b16 a[36:37], v52 offset:6144\n" \
  "ds_read_b64_tr_b16 a[38:39], v53 offset:6144\n" \
  "ds_read_b64_tr_b16 a[40:41], v54 offset:4096\n" \
  "ds_read_b64_tr_b16 a[42:43], v55 offset:4096\n" \
  "ds_read_b64_tr_b16 a[44:45], v54 offset:6144\n" \
  "ds_read_b64_tr_b16 a[46:47], v55 offset:6144\n" \
  "ds_read_b64_tr_b16 a[48:49], v56 offset:4096\n" \
  "ds_read_b64_tr_b16 a[50:51], v57 offset:4096\n" \
  "ds_read_b64_tr_b16 a[52:53], v56 offset:6144\n" \
  "ds_read_b64_tr_b16 a[54:55], v57 offset:6144\n" \
  "ds_read_b64_tr_b16 a[56:57], v58 offset:4096\n" \
  "ds_read_b64_tr_b16 a[58:59], v59 offset:4096\n" \
  "ds_read_b64_tr_b16 a[60:61], v58 offset:6144\n" \
  "ds_read_b64_tr_b16 a[62:63], v59 offset:6144\n" \
  "ds_read_b64_tr_b16 a[64:65], v52 offset:8192\n" \
  "ds_read_b64_tr_b16 a[66:67], v53 offset:8192\n" \
  "ds_read_b64_tr_b16 a[68:69], v52 offset:10240\n" \
  "ds_read_b64_tr_b16 a[70:71], v53 offset:10240\n" \
  "ds_read_b64_tr_b16 a[72:73], v54 offset:8192\n" \
  "ds_read_b64_tr_b16 a[74:75], v55 offset:8192\n" \
  "ds_read_b64_tr_b16 a[76:77], v54 offset:10240\n" \
  "ds_read_b64_tr_b16 a[78:79], v55 offset:10240\n" \
  "ds_read_b64_tr_b16 a[80:81], v56 offset:8192\n" \
  "ds_read_b64_tr_b16 a[82:83], v57 offset:8192\n" \
  "ds_read_b64_tr_b16 a[84:85], v56 offset:10240\n" \
  "ds_read_b64_tr_b16 a[86:87], v57 offset:10240\n" \
  "ds_read_b64_tr_b16 a[88:89], v58 offset:8192\n" \
  "ds_read_b64_tr_b16 a[90:91], v59 offset:8192\n" \
  "ds_read_b64_tr_b16 a[92:93], v58 offset:10240\n" \
  "ds_read_b64_tr_b16 a[94:95], v59 offset:10240\n" \
  "ds_read_b64_tr_b16 a[96:97], v52 offset:12288\n" \
  "ds_read_b64_tr_b16 a[98:99], v53 offset:12288\n" \
  "ds_read_b64_tr_b16 a[100:101], v52 offset:14336\n" \
  "ds_read_b64_tr_b16 a[102:103], v53 offset:14336\n" \
  "ds_read_b64_tr_b16 a[104:105], v54 offset:12288\n" \
  "ds_read_b64_tr_b16 a[106:107], v55 offset:12288\n" \
  "ds_read_b64_tr_b16 a[108:109], v54 offset:14336\n" \
  "ds_read_b64_tr_b16 a[110:111], v55 offset:14336\n" \
  "ds_read_b64_tr_b16 a[112:113], v56 offset:12288\n" \
  "ds_read_b64_tr_b16 a[114:115], v57 offset:12288\n" \
  "ds_read_b64_tr_b16 a[116:117], v56 offset:14336\n" \
  "ds_read_b64_tr_b16 a[118:119], v57 offset:14336\n" \
  "ds_read_b64_tr_b16 a[120:121], v58 offset:12288\n" \
  "ds_read_b64_tr_b16 a[122:123], v59 offset:12288\n" \
  "ds_read_b64_tr_b16 a[124:125], v58 offset:14336\n" \
  "ds_read_b64_tr_b16 a[126:127], v59 offset:14336\n" \
  "s_waitcnt lgkmcnt(0)\n" \
  "s_waitcnt vmcnt(17)\n" \
  "ds_write_b128 v40, a[128:131] offset:0\n" \
  "ds_write_b128 v40, a[132:135] offset:1024\n" \
  "ds_write_b128 v40, a[136:139] offset:2048\n" \
  "ds_write_b128 v40, a[140:143] offset:3072\n" \
  "ds_write_b128 v40, a[144:147] offset:4096\n" \
  "ds_write_b128 v40, a[148:151] offset:5120\n" \
  "ds_write_b128 v40, a[152:155] offset:6144\n" \
  "ds_write_b128 v40, a[156:159] offset:7168\n" \
  "ds_write_b128 v40, a[160:163] offset:8192\n" \
  "ds_write_b128 v40, a[164:167] offset:9216\n" \
  "ds_write_b128 v40, a[168:171] offset:10240\n" \
  "ds_write_b128 v40, a[172:175] offset:11264\n" \
  "ds_write_b128 v40, a[176:179] offset:12288\n" \
  "ds_write_b128 v40, a[180:183] offset:13312\n" \
  "ds_write_b128 v40, a[184:187] offset:14336\n" \
  "ds_write_b128 v40, a[188:191] offset:15360\n" \
  "s_waitcnt lgkmcnt(0)\n" \
  "ds_read_b64_tr_b16 a[128:129], v42 offset:0\n" \
  "ds_read_b64_tr_b16 a[130:131], v43 offset:0\n" \
  "ds_read_b64_tr_b16 a[132:133], v42 offset:2048\n" \
  "ds_read_b64_tr_b16 a[134:135], v43 offset:2048\n" \
  "ds_read_b64_tr_b16 a[136:137], v44 offset:0\n" \
  "ds_read_b64_tr_b16 a[138:139], v45 offset:0\n" \
  "ds_read_b64_tr_b16 a[140:141], v44 offset:2048\n" \
  "ds_read_b64_tr_b16 a[142:143], v45 offset:2048\n" \
  "ds_read_b64_tr_b16 a[144:145], v42 offset:4096\n" \
  "ds_read_b64_tr_b16 a[146:147], v43 offset:4096\n" \
  "ds_read_b64_tr_b16 a[148:149], v42 offset:6144\n" \
  "ds_read_b64_tr_b16 a[150:151], v43 offset:6144\n" \
  "ds_read_b64_tr_b16 a[152:153], v44 offset:4096\n" \
  "ds_read_b64_tr_b16 a[154:155], v45 offset:4096\n" \
  "ds_read_b64_tr_b16 a[156:157], v44 offset:6144\n" \
  "ds_read_b64_tr_b16 a[158:159], v45 offset:6144\n" \
  "ds_read_b64_tr_b16 a[160:161], v42 offset:8192\n" \
  "ds_read_b64_tr_b16 a[162:163], v43 offset:8192\n" \
  "ds_read_b64_tr_b16 a[164:165], v42 offset:10240\n" \
  "ds_read_b64_tr_b16 a[166:167], v43 offset:10240\n" \
  "ds_read_b64_tr_b16 a[168:169], v44 offset:8192\n" \
  "ds_read_b64_tr_b16 a[170:171], v45 offset:8192\n" \
  "ds_read_b64_tr_b16 a[172:173], v44 offset:10240\n" \
  "ds_read_b64_tr_b16 a[174:175], v45 offset:10240\n" \
  "ds_read_b64_tr_b16 a[176:177], v42 offset:12288\n" \
  "ds_read_b64_tr_b16 a[178:179], v43 offset:12288\n" \
  "ds_read_b64_tr_b16 a[180:181], v42 offset:14336\n" \
  "ds_read_b64_tr_b16 a[182:183], v43 offset:14336\n" \
  "ds_read_b64_tr_b16 a[184:185], v44 offset:12288\n" \
  "ds_read_b64_tr_b16 a[186:187], v45 offset:12288\n" \
  "ds_read_b64_tr_b16 a[188:189], v44 offset:14336\n" \
  "ds_read_b64_tr_b16 a[190:191], v45 offset:14336\n" \
  "s_waitcnt vmcnt(0)\n" \
  "ds_write_b128 v35, a[192:195] offset:16384\n" \
  "ds_write_b128 v35, a[196:199] offset:17408\n" \
  "ds_write_b128 v35, a[200:203] offset:18432\n" \
  "ds_write_b128 v35, a[204:207] offset:19456\n" \
  "ds_write_b128 v35, a[208:211] offset:20480\n" \
  "ds_write_b128 v35, a[212:215] offset:21504\n" \
  "ds_write_b128 v35, a[216:219] offset:22528\n" \
  "ds_write_b128 v35, a[220:223] offset:23552\n" \
  "ds_write_b128 v35, a[224:227] offset:24576\n" \
  "ds_write_b128 v35, a[228:231] offset:25600\n" \
  "ds_write_b128 v35, a[232:235] offset:26624\n" \
  "ds_write_b128 v35, a[236:239] offset:27648\n" \
  "ds_write_b128 v35, a[240:243] offset:28672\n" \
  "ds_write_b128 v35, a[244:247] offset:29696\n" \
  "ds_write_b128 v35, a[248:251] offset:30720\n" \
  "ds_write_b128 v35, a[252:255] offset:31744\n" \
  "s_waitcnt lgkmcnt(0)\n" \
  "ds_read_b64_tr_b16 a[192:193], v49 offset:16384\n" \
  "ds_read_b64_tr_b16 a[194:195], v49 offset:16640\n" \
  "ds_read_b64_tr_b16 a[196:197], v49 offset:20480\n" \
  "ds_read_b64_tr_b16 a[198:199], v49 offset:20736\n" \
  "ds_read_b64_tr_b16 a[200:201], v49 offset:24576\n" \
  "ds_read_b64_tr_b16 a[202:203], v49 offset:24832\n" \
  "ds_read_b64_tr_b16 a[204:205], v49 offset:28672\n" \
  "ds_read_b64_tr_b16 a[206:207], v49 offset:28928\n" \
  "ds_read_b64_tr_b16 a[208:209], v49 offset:16400\n" \
  "ds_read_b64_tr_b16 a[210:211], v49 offset:16656\n" \
  "ds_read_b64_tr_b16 a[212:213], v49 offset:20496\n" \
  "ds_read_b64_tr_b16 a[214:215], v49 offset:20752\n" \
  "ds_read_b64_tr_b16 a[216:217], v49 offset:24592\n" \
  "ds_read_b64_tr_b16 a[218:219], v49 offset:24848\n" \
  "ds_read_b64_tr_b16 a[220:221], v49 offset:28688\n" \
  "ds_read_b64_tr_b16 a[222:223], v49 offset:28944\n" \
  "ds_read_b64_tr_b16 a[224:225], v49 offset:16392\n" \
  "ds_read_b64_tr_b16 a[226:227], v49 offset:16648\n" \
  "ds_read_b64_tr_b16 a[228:229], v49 offset:20488\n" \
  "ds_read_b64_tr_b16 a[230:231], v49 offset:20744\n" \
  "ds_read_b64_tr_b16 a[232:233], v49 offset:24584\n" \
  "ds_read_b64_tr_b16 a[234:235], v49 offset:24840\n" \
  "ds_read_b64_tr_b16 a[236:237], v49 offset:28680\n" \
  "ds_read_b64_tr_b16 a[238:239], v49 offset:28936\n" \
  "ds_read_b64_tr_b16 a[240:241], v49 offset:16408\n" \
  "ds_read_b64_tr_b16 a[242:243], v49 offset:16664\n" \
  "ds_read_b64_tr_b16 a[244:245], v49 offset:20504\n" \
  "ds_read_b64_tr_b16 a[246:247], v49 offset:20760\n" \
  "ds_read_b64_tr_b16 a[248:249], v49 offset:24600\n" \
  "ds_read_b64_tr_b16 a[250:251], v49 offset:24856\n" \
  "ds_read_b64_tr_b16 a[252:253], v49 offset:28696\n" \
  "ds_read_b64_tr_b16 a[254:255], v49 offset:28952\n" \
  "s_mov_b64 s[36:37], %4\n" \
  "s_and_b32 s37, s37, 0xffff\n" \
  "s_mov_b32 s38, " STR(EX_BYTES32) "\n" \
  "s_mov_b32 s39, 0x00020000\n" \
  "s_mov_b64 s[40:41], %4\n" \
  "s_add_u32 s40, s40, " STR(WS_K_OFF32) "\n" \
  "s_addc_u32 s41, s41, 0\n" \
  "s_and_b32 s41, s41, 0xffff\n" \
  "s_mov_b32 s42, 3*" STR(QKV_SZ32) "\n" \
  "s_mov_b32 s43, 0x00020000\n" \
  "v_lshrrev_b32 v50, 5, v0\n" \
  "v_and_b32 v51, 31, v0\n" \
  "v_mov_b32 v2, 0x4444\n" \
  "v_mov_b32 v27, 0xeeee\n" \
  "v_cmp_lt_u32 vcc, 1, v39\n" \
  "s_mul_i32 s59, %7, 1280\n" \
  "s_add_u32 s59, s59, %9\n" \
  "v_cndmask_b32 v2, v2, v27, vcc\n" \
  "v_cndmask_b32 v29, 0, -1, vcc\n" \
  "v_cndmask_b32 v4, -1, 0, vcc\n" \
  "v_mul_u32_u24 v27, 80, v37\n" \
  "v_add_lshl_u32 v27, v27, v36, 2\n" \
  "v_add_u32 v5, s59, v27\n" \
  "v_lshlrev_b32 v27, 3, v0\n" \
  "s_add_u32 s60, %9, 8192\n" \
  "v_add_u32 v27, s60, v27\n" \
  "s_mul_i32 s59, %7, 1536\n" \
  "v_add_u32 v6, s59, v27\n" \
  "s_add_u32 s59, %7, 1\n" \
  "s_and_b32 s59, s59, 3\n" \
  "s_mul_i32 s59, s59, 1536\n" \
  "v_add_u32 v3, s59, v27\n" \
  "s_add_u32 s59, %7, 2\n" \
  "s_and_b32 s59, s59, 3\n" \
  "s_mul_i32 s59, s59, 1536\n" \
  "v_add_u32 v244, s59, v27\n" \
  "s_add_u32 s59, %7, 3\n" \
  "s_and_b32 s59, s59, 3\n" \
  "s_mul_i32 s59, s59, 1536\n" \
  "v_add_u32 v245, s59, v27\n" \
  "v_and_b32 v27, 1, v37\n" \
  "v_lshlrev_b32 v27, 1, v27\n" \
  "v_and_b32 v28, 1, v38\n" \
  "v_add_u32 v27, v27, v28\n" \
  "v_mul_u32_u24 v27, 160, v27\n" \
  "v_lshrrev_b32 v28, 1, v38\n" \
  "v_lshl_add_u32 v27, v28, 5, v27\n" \
  "v_lshl_add_u32 v27, v39, 3, v27\n" \
  "s_and_b32 s59, %7, 1\n" \
  "s_mul_i32 s59, s59, 640\n" \
  "s_lshr_b32 s60, %7, 1\n" \
  "s_lshl_b32 s60, s60, 6\n" \
  "s_add_u32 s59, s59, s60\n" \
  "s_add_u32 s59, s59, %9\n" \
  "v_add_u32 v7, s59, v27\n" \
  "v_mov_b32 v24, %9\n" \
  "s_lshl_b32 s94, s70, 14\n" \
  "v_add_u32 v17, s94, v1\n" \
  "v_add_u32 v17, 4096, v17\n" \
  "s_lshl_b32 s59, s71, 2\n" \
  "s_add_u32 s59, s59, %7\n" \
  "s_lshl_b32 s59, s59, 8\n" \
  "s_add_u32 s59, s59, s94\n" \
  "v_and_b32 v8, 0x38, v0\n" \
  "v_lshlrev_b32 v8, 2, v8\n" \
  "v_and_b32 v27, 1, v0\n" \
  "v_lshl_add_u32 v8, v27, 4, v8\n" \
  "v_and_b32 v27, 6, v0\n" \
  "v_lshl_add_u32 v8, v27, 1, v8\n" \
  "v_add_u32 v8, s59, v8\n" \
  "v_and_b32 v27, 7, v0\n" \
  "v_lshlrev_b32 v27, 4, v27\n" \
  "v_lshrrev_b32 v28, 3, v0\n" \
  "v_add_u32 v15, s59, v1\n" \
  "s_lshl_b32 s59, s71, 2\n" \
  "s_add_u32 s59, s59, %7\n" \
  "s_lshl_b32 s59, s59, 7\n" \
  "s_add_u32 s59, s59, 4096\n" \
  "s_add_u32 s59, s59, s94\n" \
  "v_lshlrev_b32 v9, 2, v51\n" \
  "v_add_u32 v9, s59, v9\n" \
  "v_lshl_add_u32 v9, v50, 31, v9\n" \
  "v_add_u32 v25, s59, v27\n" \
  "v_cmp_eq_u32 vcc, 2, v28\n" \
  "s_add_u32 s59, s94, 6144\n" \
  "s_and_b32 s60, %6, 31\n" \
  "s_or_b32 s60, s60, %7\n" \
  "v_cndmask_b32 v15, v15, v25, vcc\n" \
  "s_cmp_eq_u32 s60, 1\n" \
  "s_cselect_b32 s60, 0, 0x80000000\n" \
  "s_and_b32 s61, %6, 1\n" \
  "s_lshl_b32 s61, s61, 31\n" \
  "s_or_b32 s60, s60, s61\n" \
  "s_or_b32 s59, s59, s60\n" \
  "v_mov_b32 v25, 0x80000000\n" \
  "v_cmp_eq_u32 vcc, 3, v28\n" \
  "v_lshl_add_u32 v14, v0, 3, s59\n" \
  "s_nop 0\n" \
  "v_cndmask_b32 v15, v15, v25, vcc\n" \
  "v_lshl_add_u32 v25, v51, 4, s59\n" \
  "v_cmp_eq_u32 vcc, 1, v50\n" \
  "s_lshr_b32 s59, %6, 5\n" \
  "s_lshl_b32 s59, s59, 7\n" \
  "s_add_u32 s59, s59, 6144\n" \
  "v_cndmask_b32 v15, v15, v25, vcc\n" \
  "v_lshl_add_u32 v13, v39, 17, s59\n" \
  "s_lshl_b32 s59, s71, 2\n" \
  "s_add_u32 s59, s59, %7\n" \
  "s_lshl_b32 s59, s59, 16\n" \
  "v_lshlrev_b32 v10, 14, v37\n" \
  "v_and_b32 v25, 1, v28\n" \
  "v_lshl_add_u32 v10, v25, 12, v10\n" \
  "v_add3_u32 v10, v10, v27, s59\n" \
  "s_lshl_b32 s60, %7, 4\n" \
  "s_add_u32 s60, s60, s70\n" \
  "v_lshl_add_u32 v25, v28, 1, s60\n" \
  "v_and_b32 v25, 31, v25\n" \
  "v_lshl_add_u32 v10, v25, 7, v10\n" \
  "s_lshr_b32 s59, %6, 1\n" \
  "s_lshl_b32 s59, s59, 14\n" \
  "s_and_b32 s60, %6, 1\n" \
  "s_lshl_b32 s60, s60, 12\n" \
  "s_add_u32 s59, s59, s60\n" \
  "s_lshl_b32 s60, %7, 10\n" \
  "s_add_u32 s59, s59, s60\n" \
  "v_add_u32 v11, s59, v1\n" \
  "s_lshr_b32 s59, %6, 6\n" \
  "s_lshl_b32 s59, s59, 14\n" \
  "s_and_b32 s60, %6, 63\n" \
  "s_lshl_b32 s60, s60, 7\n" \
  "s_add_u32 s59, s59, s60\n" \
  "v_add_u32 v12, s59, v27\n" \
  "v_lshrrev_b32 v25, 3, v36\n" \
  "v_lshl_add_u32 v25, v50, 1, v25\n" \
  "v_add_u32 v25, " STR(EXP_A) ", v25\n" \
  "v_lshl_add_u32 v12, v25, 21, v12\n" \
  "v_lshrrev_b32 v25, v25, %8\n" \
  "v_and_b32 v25, 1, v25\n" \
  "v_lshl_add_u32 v12, v25, 13, v12\n" \
  "v_lshl_add_u32 v12, v37, 31, v12\n" \
  "v_and_b32 v27, 1, v37\n" \
  "v_lshlrev_b32 v27, 5, v27\n" \
  "v_xor_b32 v27, v1, v27\n" \
  "s_add_u32 s59, %9, 32768\n" \
  "v_add_u32 v19, s59, v27\n" \
  "s_and_b32 s60, %7, 1\n" \
  "s_lshl_b32 s60, s60, 13\n" \
  "v_add_u32 v18, s60, v19\n" \
  "v_and_b32 v20, 15, v46\n" \
  "v_lshlrev_b32 v20, 4, v20\n" \
  "v_lshl_add_u32 v20, v50, 10, v20\n" \
  "v_lshl_add_u32 v20, v39, 8, v20\n" \
  "v_and_b32 v27, 1, v39\n" \
  "v_lshlrev_b32 v27, 5, v27\n" \
  "v_xor_b32 v20, v20, v27\n" \
  "v_xor_b32 v21, 32, v20\n" \
  "s_add_u32 s59, s59, 8192\n" \
  "v_add_u32 v20, s59, v20\n" \
  "v_add_u32 v21, s59, v21\n" \
  "v_and_b32 v22, 1, v0\n" \
  "v_lshlrev_b32 v22, 7, v22\n" \
  "s_lshl_b32 s59, s70, 8\n" \
  "s_add_u32 s59, s59, " STR(CW_GPROG) "*4\n" \
  "v_add_u32 v22, s59, v22\n" \
  "s_add_u32 s59, %7, %6\n" \
  "s_add_u32 s59, s59, 0\n" \
  "s_and_b32 s59, s59, 3\n" \
  "s_lshl_b32 s59, s59, 10\n" \
  "v_add_u32 v80, s59, v1\n" \
  "v_add_u32 v84, s59, v19\n" \
  "v_mov_b32 v88, -1\n" \
  "s_add_u32 s59, %7, %6\n" \
  "s_add_u32 s59, s59, 1\n" \
  "s_and_b32 s59, s59, 3\n" \
  "s_lshl_b32 s59, s59, 10\n" \
  "v_add_u32 v81, s59, v1\n" \
  "v_add_u32 v85, s59, v19\n" \
  "v_mov_b32 v89, -1\n" \
  "s_add_u32 s59, %7, %6\n" \
  "s_add_u32 s59, s59, 2\n" \
  "s_and_b32 s59, s59, 3\n" \
  "s_lshl_b32 s59, s59, 10\n" \
  "v_add_u32 v82, s59, v1\n" \
  "v_add_u32 v86, s59, v19\n" \
  "v_mov_b32 v90, -1\n" \
  "s_add_u32 s59, %7, %6\n" \
  "s_add_u32 s59, s59, 3\n" \
  "s_and_b32 s59, s59, 3\n" \
  "s_lshl_b32 s59, s59, 10\n" \
  "v_add_u32 v83, s59, v1\n" \
  "v_add_u32 v87, s59, v19\n" \
  "v_mov_b32 v91, -1\n" \
  "v_mov_b32 v248, 0\n" \
  "v_mov_b32 v27, 0x3f80\n" \
  "v_and_b32 v25, 7, v36\n" \
  "v_cmp_eq_u32 vcc, 0, v25\n" \
  "v_mov_b32 v28, 1.0\n" \
  "s_nop 0\n" \
  "v_cndmask_b32 v248, v248, v27, vcc\n" \
  "v_cmp_eq_u32 vcc, 1, v25\n" \
  "s_nop 1\n" \
  "v_cndmask_b32 v248, v248, v28, vcc\n" \
  "v_mov_b32 v249, 0\n" \
  "v_mov_b32 v27, 0x3f80\n" \
  "v_and_b32 v25, 7, v36\n" \
  "v_cmp_eq_u32 vcc, 2, v25\n" \
  "v_mov_b32 v28, 1.0\n" \
  "s_nop 0\n" \
  "v_cndmask_b32 v249, v249, v27, vcc\n" \
  "v_cmp_eq_u32 vcc, 3, v25\n" \
  "s_nop 1\n" \
  "v_cndmask_b32 v249, v249, v28, vcc\n" \
  "v_mov_b32 v250, 0\n" \
  "v_mov_b32 v27, 0x3f80\n" \
  "v_and_b32 v25, 7, v36\n" \
  "v_cmp_eq_u32 vcc, 4, v25\n" \
  "v_mov_b32 v28, 1.0\n" \
  "s_nop 0\n" \
  "v_cndmask_b32 v250, v250, v27, vcc\n" \
  "v_cmp_eq_u32 vcc, 5, v25\n" \
  "s_nop 1\n" \
  "v_cndmask_b32 v250, v250, v28, vcc\n" \
  "v_mov_b32 v251, 0\n" \
  "v_mov_b32 v27, 0x3f80\n" \
  "v_and_b32 v25, 7, v36\n" \
  "v_cmp_eq_u32 vcc, 6, v25\n" \
  "v_mov_b32 v28, 1.0\n" \
  "s_nop 0\n" \
  "v_cndmask_b32 v251, v251, v27, vcc\n" \
  "v_cmp_eq_u32 vcc, 7, v25\n" \
  "s_nop 1\n" \
  "v_cndmask_b32 v251, v251, v28, vcc\n" \
  "v_and_b32 v27, 1, v0\n" \
  "v_cmp_eq_u32 vcc, 1, v27\n" \
  "v_mov_b32 v246, 0x05040100\n" \
  "v_mov_b32 v28, 0x03020706\n" \
  "v_cndmask_b32 v246, v246, v28, vcc\n" \
  "v_mov_b32 v252, 0x3f803f80\n" \
  "v_mov_b32 v253, 0x3f803f80\n" \
  "v_mov_b32 v254, 0x3f803f80\n" \
  "v_mov_b32 v255, 0\n" \
  "v_mov_b32 v30, 0\n" \
  "v_mov_b32 v31, 0\n" \
  "s_mov_b32 s92, 0x3e0293ee\n" \
  "s_mov_b32 s58, 0\n" \
  "s_mov_b32 s48, 0\n" \
  "s_lshr_b32 s59, %7, 1\n" \
  "s_lshl_b32 s59, s59, 14\n" \
  "s_and_b32 s60, %7, 1\n" \
  "s_lshl_b32 s60, s60, 12\n" \
  "s_add_u32 s93, s59, s60\n" \
  "s_lshr_b32 s60, %6, 5\n" \
  "s_add_u32 s60, s60, " STR(EXP_A) "\n" \
  "s_lshl_b32 s60, s60, 21\n" \
  "s_add_u32 s60, s60, s93\n" \
  "s_mov_b32 s53, 0\n" \
  "s_lshr_b32 s59, s60, 21\n" \
  "s_lshr_b32 s59, %8, s59\n" \
  "s_and_b32 s59, s59, 1\n" \
  "s_lshl_b32 s59, s59, 13\n" \
  "s_or_b32 s93, s60, s59\n" \
  "s_mov_b32 s60, " STR(EXP_C) "<<21\n" \
  "s_lshr_b32 s59, s60, 21\n" \
  "s_lshr_b32 s59, %8, s59\n" \
  "s_and_b32 s59, s59, 1\n" \
  "s_lshl_b32 s59, s59, 13\n" \
  "s_or_b32 s52, s60, s59\n" \
  "s_add_u32 s51, s52, 1<<20\n" \
  "s_and_b32 s59, %7, 1\n" \
  "s_mul_i32 s59, s59, " STR(QKV_SZ32) "\n" \
  "s_lshl_b32 s60, s70, 14\n" \
  "s_add_u32 s59, s59, s60\n" \
  "v_add_u32 v247, s59, v1\n" \
  "v_add_u32 v16, 4096, v247\n" \
  "s_lshr_b32 s60, %7, 1\n" \
  "s_lshr_b32 s59, s60, 2\n" \
  "s_xnor_b32 s59, s59, %8\n" \
  "s_and_b32 s59, s59, 1\n" \
  "s_lshl_b32 s59, s59, 13\n" \
  "s_lshl_b32 s55, s60, 19\n" \
  "s_or_b32 s55, s55, s59\n" \
  "s_waitcnt lgkmcnt(0)\n" \
  "v_mov_b32 v25, " STR(CW_INIT) "*4\n" \
  "LI_%=:\n" \
  "buffer_load_dword v23, v25, s[44:47], 0 offen sc1\n" \
  "s_waitcnt vmcnt(0)\n" \
  "v_cmp_gt_u32 vcc, 128, v23\n" \
  "s_cbranch_vccz LI2_%=\n" \
  "s_sleep 6\n" \
  "s_add_u32 s58, s58, 1\n" \
  "s_cmp_lt_u32 s58, " STR(SPIN_CAP) "\n" \
  "s_cbranch_scc1 LI_%=\n" \
  "v_mov_b32 v27, " STR(CW_DEAD) "*4\n" \
  "v_mov_b32 v28, 1\n" \
  "buffer_store_dword v28, v27, s[44:47], 0 offen sc1\n" \
  "s_branch LI2_%=\n" \
  "LI2_%=:\n" \
  "buffer_load_dword v23, v22, s[44:47], 0 offen sc1\n" \
  "s_waitcnt vmcnt(0)\n" \
  "v_cmp_gt_u32 vcc, 2, v23\n" \
  "s_cbranch_vccz LI3_%=\n" \
  "s_sleep 4\n" \
  "s_add_u32 s58, s58, 1\n" \
  "s_cmp_lt_u32 s58, " STR(SPIN_CAP) "\n" \
  "s_cbranch_scc1 LI2_%=\n" \
  "v_mov_b32 v27, " STR(CW_DEAD) "*4\n" \
  "v_mov_b32 v28, 1\n" \
  "buffer_store_dword v28, v27, s[44:47], 0 offen sc1\n" \
  "s_branch LI3_%=\n" \
  "LI3_%=:\n" \
  "buffer_load_dwordx4 v[200:203], v247, s[40:43], s55 offen offset:0 sc1\n" \
  "buffer_load_dwordx4 v[204:207], v247, s[40:43], s55 offen offset:1024 sc1\n" \
  "buffer_load_dwordx4 v[208:211], v247, s[40:43], s55 offen offset:2048 sc1\n" \
  "buffer_load_dwordx4 v[212:215], v247, s[40:43], s55 offen offset:3072 sc1\n" \
  "buffer_load_dwordx4 v[216:219], v16, s[40:43], s55 offen offset:0 sc1\n" \
  "buffer_load_dwordx4 v[220:223], v16, s[40:43], s55 offen offset:1024 sc1\n" \
  "buffer_load_dwordx4 v[224:227], v16, s[40:43], s55 offen offset:2048 sc1\n" \
  "buffer_load_dwordx4 v[228:231], v16, s[40:43], s55 offen offset:3072 sc1\n" \
  "s_barrier\n" \
  "s_cmp_gt_u32 %7, 1\n" \
  "s_cbranch_scc1 LI4_%=\n" \
  "s_waitcnt vmcnt(0)\n" \
  "ds_write_b128 v18, v[200:203] offset:0\n" \
  "ds_write_b128 v18, v[204:207] offset:1024\n" \
  "ds_write_b128 v18, v[208:211] offset:2048\n" \
  "ds_write_b128 v18, v[212:215] offset:3072\n" \
  "ds_write_b128 v18, v[216:219] offset:4096\n" \
  "ds_write_b128 v18, v[220:223] offset:5120\n" \
  "ds_write_b128 v18, v[224:227] offset:6144\n" \
  "ds_write_b128 v18, v[228:231] offset:7168\n" \
  "LI4_%=:\n" \
  "s_waitcnt lgkmcnt(0)\n" \
  "s_barrier\n" \
  "ds_read_b64_tr_b16 v[136:137], v20 offset:0\n" \
  "ds_read_b64_tr_b16 v[138:139], v21 offset:0\n" \
  "ds_read_b64_tr_b16 v[152:153], v20 offset:8\n" \
  "ds_read_b64_tr_b16 v[154:155], v21 offset:8\n" \
  "ds_read_b64_tr_b16 v[140:141], v20 offset:2048\n" \
  "ds_read_b64_tr_b16 v[142:143], v21 offset:2048\n" \
  "ds_read_b64_tr_b16 v[156:157], v20 offset:2056\n" \
  "ds_read_b64_tr_b16 v[158:159], v21 offset:2056\n" \
  "ds_read_b64_tr_b16 v[144:145], v20 offset:4096\n" \
  "ds_read_b64_tr_b16 v[146:147], v21 offset:4096\n" \
  "ds_read_b64_tr_b16 v[160:161], v20 offset:4104\n" \
  "ds_read_b64_tr_b16 v[162:163], v21 offset:4104\n" \
  "ds_read_b64_tr_b16 v[148:149], v20 offset:6144\n" \
  "ds_read_b64_tr_b16 v[150:151], v21 offset:6144\n" \
  "ds_read_b64_tr_b16 v[164:165], v20 offset:6152\n" \
  "ds_read_b64_tr_b16 v[166:167], v21 offset:6152\n" \
  "ds_read_b128 v[104:107], v84 offset:0\n" \
  "ds_read_b128 v[108:111], v85 offset:0\n" \
  "ds_read_b128 v[112:115], v86 offset:0\n" \
  "ds_read_b128 v[116:119], v87 offset:0\n" \
  "ds_read_b128 v[120:123], v84 offset:4096\n" \
  "ds_read_b128 v[124:127], v85 offset:4096\n" \
  "ds_read_b128 v[128:131], v86 offset:4096\n" \
  "ds_read_b128 v[132:135], v87 offset:4096\n" \
  "s_waitcnt lgkmcnt(0)\n" \
  "v_mfma_f32_16x16x32_bf16 v[184:187], v[136:139], a[224:227], 0\n" \
  "v_mfma_f32_16x16x32_bf16 v[188:191], v[152:155], a[224:227], 0\n" \
  "v_mfma_f32_16x16x32_bf16 v[192:195], v[136:139], a[240:243], 0\n" \
  "v_mfma_f32_16x16x32_bf16 v[196:199], v[152:155], a[240:243], 0\n" \
  "v_mfma_f32_16x16x32_bf16 v[184:187], v[140:143], a[228:231], v[184:187]\n" \
  "v_mfma_f32_16x16x32_bf16 v[188:191], v[156:159], a[228:231], v[188:191]\n" \
  "v_mfma_f32_16x16x32_bf16 v[192:195], v[140:143], a[244:247], v[192:195]\n" \
  "v_mfma_f32_16x16x32_bf16 v[196:199], v[156:159], a[244:247], v[196:199]\n" \
  "v_mfma_f32_16x16x32_bf16 v[184:187], v[144:147], a[232:235], v[184:187]\n" \
  "v_mfma_f32_16x16x32_bf16 v[188:191], v[160:163], a[232:235], v[188:191]\n" \
  "v_mfma_f32_16x16x32_bf16 v[192:195], v[144:147], a[248:251], v[192:195]\n" \
  "v_mfma_f32_16x16x32_bf16 v[196:199], v[160:163], a[248:251], v[196:199]\n" \
  "v_mfma_f32_16x16x32_bf16 v[184:187], v[148:151], a[236:239], v[184:187]\n" \
  "v_mfma_f32_16x16x32_bf16 v[188:191], v[164:167], a[236:239], v[188:191]\n" \
  "v_mfma_f32_16x16x32_bf16 v[192:195], v[148:151], a[252:255], v[192:195]\n" \
  "v_mfma_f32_16x16x32_bf16 v[196:199], v[164:167], a[252:255], v[196:199]\n" \
  "s_nop 7\n" \
  "v_cvt_pk_bf16_f32 v176, v184, v185\n" \
  "v_cvt_pk_bf16_f32 v177, v186, v187\n" \
  "v_cvt_pk_bf16_f32 v178, v188, v189\n" \
  "v_cvt_pk_bf16_f32 v179, v190, v191\n" \
  "v_cvt_pk_bf16_f32 v180, v192, v193\n" \
  "v_cvt_pk_bf16_f32 v181, v194, v195\n" \
  "v_cvt_pk_bf16_f32 v182, v196, v197\n" \
  "v_cvt_pk_bf16_f32 v183, v198, v199\n" \
  "v_mfma_f32_16x16x32_bf16 v[184:187], v[136:139], a[192:195], 0\n" \
  "v_mfma_f32_16x16x32_bf16 v[188:191], v[152:155], a[192:195], 0\n" \
  "v_mfma_f32_16x16x32_bf16 v[192:195], v[136:139], a[208:211], 0\n" \
  "v_mfma_f32_16x16x32_bf16 v[196:199], v[152:155], a[208:211], 0\n" \
  "v_mfma_f32_16x16x32_bf16 v[184:187], v[140:143], a[196:199], v[184:187]\n" \
  "v_mfma_f32_16x16x32_bf16 v[188:191], v[156:159], a[196:199], v[188:191]\n" \
  "v_mfma_f32_16x16x32_bf16 v[192:195], v[140:143], a[212:215], v[192:195]\n" \
  "v_mfma_f32_16x16x32_bf16 v[196:199], v[156:159], a[212:215], v[196:199]\n" \
  "v_mfma_f32_16x16x32_bf16 v[184:187], v[144:147], a[200:203], v[184:187]\n" \
  "v_mfma_f32_16x16x32_bf16 v[188:191], v[160:163], a[200:203], v[188:191]\n" \
  "v_mfma_f32_16x16x32_bf16 v[192:195], v[144:147], a[216:219], v[192:195]\n" \
  "v_mfma_f32_16x16x32_bf16 v[196:199], v[160:163], a[216:219], v[196:199]\n" \
  "v_mfma_f32_16x16x32_bf16 v[184:187], v[148:151], a[204:207], v[184:187]\n" \
  "v_mfma_f32_16x16x32_bf16 v[188:191], v[164:167], a[204:207], v[188:191]\n" \
  "v_mfma_f32_16x16x32_bf16 v[192:195], v[148:151], a[220:223], v[192:195]\n" \
  "v_mfma_f32_16x16x32_bf16 v[196:199], v[164:167], a[220:223], v[196:199]\n" \
  "s_memtime s[72:73]\n" \
  "s_memrealtime s[74:75]\n" \
  "s_nop 1\n" \
  "s_memrealtime s[76:77]\n" \
  "s_nop 1\n" \
  "s_memrealtime s[78:79]\n" \
  "s_nop 1\n" \
  "s_memrealtime s[80:81]\n" \
  "s_nop 1\n" \
  "s_memrealtime s[82:83]\n" \
  "s_nop 1\n" \
  "s_memrealtime s[84:85]\n" \
  "s_nop 1\n" \
  "s_memrealtime s[86:87]\n" \
  "s_nop 1\n" \
  "s_memrealtime s[88:89]\n" \
  "s_nop 1\n" \
  "s_memrealtime s[90:91]\n" \
  "s_mov_b32 s59, 0\n" \
  "s_waitcnt lgkmcnt(0)\n" \
  "s_cmp_eq_u32 s76, s74\n" \
  "s_addc_u32 s59, s59, 0\n" \
  "s_cmp_eq_u32 s78, s74\n" \
  "s_addc_u32 s59, s59, 0\n" \
  "s_cmp_eq_u32 s80, s74\n" \
  "s_addc_u32 s59, s59, 0\n" \
  "s_cmp_eq_u32 s82, s74\n" \
  "s_addc_u32 s59, s59, 0\n" \
  "s_cmp_eq_u32 s84, s74\n" \
  "s_addc_u32 s59, s59, 0\n" \
  "s_cmp_eq_u32 s86, s74\n" \
  "s_addc_u32 s59, s59, 0\n" \
  "s_cmp_eq_u32 s88, s74\n" \
  "s_addc_u32 s59, s59, 0\n" \
  "s_cmp_eq_u32 s90, s74\n" \
  "s_addc_u32 s59, s59, 0\n" \
  "s_mul_i32 s59, s59, 12\n" \
  "s_add_u32 s68, s72, s59\n" \
  "s_add_u32 s59, s74, 4\n" \
  "s_mul_i32 s69, s59, 24\n" \
  "v_cvt_pk_bf16_f32 v168, v184, v185\n" \
  "v_cvt_pk_bf16_f32 v169, v186, v187\n" \
  "v_cvt_pk_bf16_f32 v170, v188, v189\n" \
  "v_cvt_pk_bf16_f32 v171, v190, v191\n" \
  "v_cvt_pk_bf16_f32 v172, v192, v193\n" \
  "v_cvt_pk_bf16_f32 v173, v194, v195\n" \
  "v_cvt_pk_bf16_f32 v174, v196, v197\n" \
  "v_cvt_pk_bf16_f32 v175, v198, v199\n" \
  "v_mov_b32 v95, " STR(RI0) "\n" \
  "s_sub_u32 s60, s69, s62\n" \
  "s_sub_u32 s59, s68, s56\n" \
  "s_mov_b32 s62, s69\n" \
  "s_mov_b32 s56, s68\n" \
  "v_cvt_f32_u32 v27, s59\n" \
  "v_cvt_f32_u32 v28, s60\n" \
  "v_rcp_f32 v26, v27\n" \
  "v_rcp_f32 v19, v28\n" \
  "v_mul_f32 v26, v26, v28\n" \
  "v_mul_f32 v95, " STR(DECAY) ", v95\n" \
  "v_mul_f32 v19, v19, v27\n" \
  "v_max_f32 v95, v95, v26\n" \
  "v_cvt_f32_u32 v28, " STR(F_P) "\n" \
  "v_mul_f32 v27, v95, v28\n" \
  "v_cvt_u32_f32 v27, v27\n" \
  "v_min_u32 v27, 0x7fff, v27\n" \
  "s_nop 0\n" \
  "v_readfirstlane_b32 s63, v27\n" \
  "v_mov_b32 v27, 0\n" \
  "ds_write_b32 v24, v27 offset:14344\n" \
  "ds_write_b32 v24, v27 offset:14356\n" \
  "s_mov_b32 s95, " STR(T_Q) "\n" \
  "v_mov_b32 v92, s68\n" \
  "v_mov_b32 v93, 0\n" \
  "ds_write_b64 v24, v[92:93] offset:14336\n" \
  "s_waitcnt lgkmcnt(0)\n" \
  "s_add_u32 s49, s53, s93\n" \
  "v_mov_b64 v[48:49], 0\n" \
  "v_mov_b64 v[50:51], 0\n" \
  "v_mov_b64 v[52:53], 0\n" \
  "v_mov_b64 v[54:55], 0\n" \
  "v_mov_b64 v[56:57], 0\n" \
  "v_mov_b64 v[58:59], 0\n" \
  "v_mov_b64 v[60:61], 0\n" \
  "v_mov_b64 v[62:63], 0\n" \
  "v_mov_b64 v[64:65], 0\n" \
  "v_mov_b64 v[66:67], 0\n" \
  "v_mov_b64 v[68:69], 0\n" \
  "v_mov_b64 v[70:71], 0\n" \
  "s_cmp_gt_u32 %7, 1\n" \
  "s_cbranch_scc1 LM_%=\n" \
  ".p2align 6\n" \
  "LL_%=:\n" \
  "s_barrier\n" \
  "buffer_load_dwordx4 v[32:35], v80, s[36:39], s49 offen sc1\n" \
  "v_mfma_f32_16x16x32_bf16 v[184:187], v[136:139], a[224:227], 0\n" \
  "s_xor_b32 s51, s51, 1<<20\n" \
  "s_xor_b32 s52, s52, 1<<20\n" \
  "v_mfma_f32_16x16x32_bf16 v[188:191], v[152:155], a[224:227], 0\n" \
  "s_add_u32 s53, s53, 32768\n" \
  "s_lshl_b32 s60, s48, 19\n" \
  "v_mfma_f32_16x16x32_bf16 v[192:195], v[136:139], a[240:243], 0\n" \
  "s_add_u32 s60, s60, " STR(EXP_B) "<<21\n" \
  "s_lshr_b32 s59, s60, 21\n" \
  "buffer_load_dwordx4 v[36:39], v81, s[36:39], s49 offen sc1\n" \
  "v_mfma_f32_16x16x32_bf16 v[196:199], v[152:155], a[240:243], 0\n" \
  "s_lshr_b32 s59, %8, s59\n" \
  "s_and_b32 s94, s59, 1\n" \
  "v_mfma_f32_16x16x32_bf16 v[184:187], v[140:143], a[228:231], v[184:187]\n" \
  "s_lshl_b32 s94, s94, 13\n" \
  "s_or_b32 s50, s60, s94\n" \
  "v_mfma_f32_16x16x32_bf16 v[188:191], v[156:159], a[228:231], v[188:191]\n" \
  "s_lshl_b32 s94, s70, 14\n" \
  "s_add_u32 s91, s50, s94\n" \
  "buffer_load_dwordx4 v[40:43], v82, s[36:39], s49 offen sc1\n" \
  "v_mfma_f32_16x16x32_bf16 v[192:195], v[140:143], a[244:247], v[192:195]\n" \
  "s_and_b32 s94, s59, 2\n" \
  "s_lshl_b32 s94, s94, 12\n" \
  "v_mfma_f32_16x16x32_bf16 v[196:199], v[156:159], a[244:247], v[196:199]\n" \
  "s_add_u32 s60, s60, 1<<21\n" \
  "s_or_b32 s54, s60, s94\n" \
  "v_mfma_f32_16x16x32_bf16 v[184:187], v[144:147], a[232:235], v[184:187]\n" \
  "buffer_load_dwordx4 v[44:47], v83, s[36:39], s49 offen sc1\n" \
  "v_mfma_f32_16x16x32_bf16 v[188:191], v[160:163], a[232:235], v[188:191]\n" \
  "v_mfma_f32_16x16x32_bf16 v[192:195], v[144:147], a[248:251], v[192:195]\n" \
  "v_mfma_f32_16x16x32_bf16 v[196:199], v[160:163], a[248:251], v[196:199]\n" \
  "v_mfma_f32_16x16x32_bf16 v[184:187], v[148:151], a[236:239], v[184:187]\n" \
  "v_mfma_f32_16x16x32_bf16 v[188:191], v[164:167], a[236:239], v[188:191]\n" \
  "buffer_store_dwordx4 v[88:91], v11, s[36:39], s52 offen sc1\n" \
  "buffer_store_dwordx4 v[88:91], v15, s[36:39], s54 offen sc1\n" \
  "v_mfma_f32_16x16x32_bf16 v[192:195], v[148:151], a[252:255], v[192:195]\n" \
  "v_mfma_f32_16x16x32_bf16 v[196:199], v[164:167], a[252:255], v[196:199]\n" \
  "s_nop 6\n" \
  "v_cvt_pk_bf16_f32 v176, v184, v185\n" \
  "v_cvt_pk_bf16_f32 v177, v186, v187\n" \
  "v_cvt_pk_bf16_f32 v178, v188, v189\n" \
  "v_cvt_pk_bf16_f32 v179, v190, v191\n" \
  "v_cvt_pk_bf16_f32 v180, v192, v193\n" \
  "v_cvt_pk_bf16_f32 v181, v194, v195\n" \
  "v_cvt_pk_bf16_f32 v182, v196, v197\n" \
  "v_cvt_pk_bf16_f32 v183, v198, v199\n" \
  "LS1_%=:\n" \
  "s_waitcnt vmcnt(5)\n" \
  "v_smfmac_f32_16x16x64_bf16 v[52:55], v[32:35], a[8:15], v2\n" \
  "v_smfmac_f32_16x16x64_bf16 v[56:59], v[32:35], a[16:23], v2\n" \
  "v_smfmac_f32_16x16x64_bf16 v[60:63], v[32:35], a[24:31], v2\n" \
  "v_smfmac_f32_16x16x64_bf16 v[48:51], v[32:35], a[0:7], v2\n" \
  "s_waitcnt vmcnt(4)\n" \
  "v_smfmac_f32_16x16x64_bf16 v[52:55], v[36:39], a[40:47], v2\n" \
  "v_smfmac_f32_16x16x64_bf16 v[56:59], v[36:39], a[48:55], v2\n" \
  "v_smfmac_f32_16x16x64_bf16 v[60:63], v[36:39], a[56:63], v2\n" \
  "v_smfmac_f32_16x16x64_bf16 v[48:51], v[36:39], a[32:39], v2\n" \
  "s_waitcnt vmcnt(3)\n" \
  "v_smfmac_f32_16x16x64_bf16 v[52:55], v[40:43], a[72:79], v2\n" \
  "v_smfmac_f32_16x16x64_bf16 v[56:59], v[40:43], a[80:87], v2\n" \
  "v_smfmac_f32_16x16x64_bf16 v[60:63], v[40:43], a[88:95], v2\n" \
  "v_smfmac_f32_16x16x64_bf16 v[48:51], v[40:43], a[64:71], v2\n" \
  "s_waitcnt vmcnt(2)\n" \
  "v_smfmac_f32_16x16x64_bf16 v[52:55], v[44:47], a[104:111], v2\n" \
  "v_smfmac_f32_16x16x64_bf16 v[56:59], v[44:47], a[112:119], v2\n" \
  "v_smfmac_f32_16x16x64_bf16 v[60:63], v[44:47], a[120:127], v2\n" \
  "v_add_f32 v52, v52, v54\n" \
  "v_add_f32 v53, v53, v55\n" \
  "v_smfmac_f32_16x16x64_bf16 v[48:51], v[44:47], a[96:103], v2\n" \
  "v_add_f32 v56, v56, v58\n" \
  "v_add_f32 v57, v57, v59\n" \
  "ds_write_b64 v3, v[52:53] offset:0\n" \
  "v_add_f32 v60, v60, v62\n" \
  "v_add_f32 v61, v61, v63\n" \
  "v_smfmac_f32_16x16x64_bf16 v[64:67], v[32:35], a[128:135], v2\n" \
  "ds_write_b64 v244, v[56:57] offset:512\n" \
  "v_smfmac_f32_16x16x64_bf16 v[68:71], v[32:35], a[136:143], v2\n" \
  "ds_write_b64 v245, v[60:61] offset:1024\n" \
  "v_cmp_u_f32 vcc, v52, v53\n" \
  "v_smfmac_f32_16x16x64_bf16 v[64:67], v[36:39], a[144:151], v2\n" \
  "v_add_f32 v48, v48, v50\n" \
  "v_add_f32 v49, v49, v51\n" \
  "v_smfmac_f32_16x16x64_bf16 v[68:71], v[36:39], a[152:159], v2\n" \
  "v_smfmac_f32_16x16x64_bf16 v[64:67], v[40:43], a[160:167], v2\n" \
  "s_cbranch_vccnz LA_%=\n" \
  "LA4_%=:\n" \
  "s_waitcnt lgkmcnt(0)\n" \
  "s_barrier\n" \
  "ds_read_b64 v[72:73], v6 offset:0\n" \
  "ds_read_b64 v[74:75], v6 offset:512\n" \
  "ds_read_b64 v[76:77], v6 offset:1024\n" \
  "ds_read_b96 v[92:94], v24 offset:14336\n" \
  "v_smfmac_f32_16x16x64_bf16 v[68:71], v[40:43], a[168:175], v2\n" \
  "v_smfmac_f32_16x16x64_bf16 v[64:67], v[44:47], a[176:183], v2\n" \
  "s_waitcnt lgkmcnt(3)\n" \
  "v_add_f32 v48, v48, v72\n" \
  "v_add_f32 v49, v49, v73\n" \
  "s_waitcnt lgkmcnt(1)\n" \
  "v_add_f32 v74, v74, v76\n" \
  "v_add_f32 v75, v75, v77\n" \
  "v_add_f32 v48, v48, v74\n" \
  "v_add_f32 v49, v49, v75\n" \
  "v_cvt_pk_bf16_f32 v27, v48, v49\n" \
  "v_smfmac_f32_16x16x64_bf16 v[68:71], v[44:47], a[184:191], v2\n" \
  "v_mov_b32_dpp v28, v27 quad_perm:[1,0,3,2] row_mask:0xf bank_mask:0xf\n" \
  "v_perm_b32 v27, v28, v27, v246\n" \
  "buffer_store_dword v27, v8, s[36:39], s50 offen sc1\n" \
  "v_add_f32 v64, v64, v66\n" \
  "v_add_f32 v65, v65, v67\n" \
  "s_nop 2\n" \
  "v_add_f32 v68, v68, v70\n" \
  "v_add_f32 v69, v69, v71\n" \
  "ds_write2_b32 v5, v64, v65 offset0:0 offset1:40\n" \
  "ds_write2_b32 v5, v68, v69 offset0:16 offset1:56\n" \
  "v_mov_b64 v[184:185], 0\n" \
  "s_add_u32 s49, s53, s93\n" \
  "s_nop 1\n" \
  "v_mov_b64 v[186:187], 0\n" \
  "s_nop 1\n" \
  "v_mov_b64 v[188:189], 0\n" \
  "s_waitcnt lgkmcnt(0)\n" \
  "s_barrier\n" \
  "s_memtime s[66:67]\n" \
  "ds_read_b64 v[72:73], v7 offset:0\n" \
  "ds_read_b64 v[74:75], v7 offset:1280\n" \
  "ds_read_b64 v[76:77], v7 offset:2560\n" \
  "ds_read_b64 v[78:79], v7 offset:3840\n" \
  "v_readfirstlane_b32 s64, v92\n" \
  "v_readfirstlane_b32 s59, v93\n" \
  "v_readfirstlane_b32 s60, v94\n" \
  "v_mov_b64 v[190:191], 0\n" \
  "s_add_u32 s64, s64, s59\n" \
  "s_add_u32 s61, s64, " STR(F_B) "\n" \
  "v_mov_b64 v[192:193], 0\n" \
  "s_cmp_eq_u32 s48, 63\n" \
  "s_waitcnt lgkmcnt(0)\n" \
  "v_pk_add_f32 v[72:73], v[72:73], v[74:75]\n" \
  "v_pk_add_f32 v[76:77], v[76:77], v[78:79]\n" \
  "v_pk_add_f32 v[72:73], v[72:73], v[76:77]\n" \
  "v_cvt_pk_bf16_f32 v27, v72, v73\n" \
  "buffer_store_dword v27, v9, s[36:39], s50 offen sc1\n" \
  "s_cbranch_scc1 LE_%=\n" \
  "s_sub_i32 s61, s61, s66\n" \
  "s_cmp_gt_i32 s61, 24\n" \
  "s_cbranch_scc1 LW_b_%=\n" \
  "LB_b_%=:\n" \
  "buffer_load_dwordx4 v[32:35], v80, s[36:39], s91 offen sc1\n" \
  "s_lshr_b32 s95, s60, 2\n" \
  "s_sub_u32 s96, s60, s95\n" \
  "s_add_u32 s95, s95, " STR(T_Q) "\n" \
  "s_add_u32 s96, s96, " STR(F_C) "\n" \
  "s_sub_u32 s59, s64, s56\n" \
  "s_add_u32 s59, s59, " STR(F_P) "\n" \
  "v_mov_b64 v[194:195], 0\n" \
  "v_mov_b64 v[196:197], 0\n" \
  "v_mov_b64 v[198:199], 0\n" \
  "s_nop " STR(GAP_0) "\n" \
  "buffer_load_dwordx4 v[36:39], v81, s[36:39], s91 offen sc1\n" \
  "ds_read_b64_tr_b16 v[136:137], v20 offset:0\n" \
  "ds_read_b64_tr_b16 v[138:139], v21 offset:0\n" \
  "ds_read_b64_tr_b16 v[152:153], v20 offset:8\n" \
  "ds_read_b64_tr_b16 v[154:155], v21 offset:8\n" \
  "s_nop " STR(GAP_B) "\n" \
  "buffer_load_dwordx4 v[40:43], v82, s[36:39], s91 offen sc1\n" \
  "ds_read_b64_tr_b16 v[140:141], v20 offset:2048\n" \
  "ds_read_b64_tr_b16 v[142:143], v21 offset:2048\n" \
  "ds_read_b64_tr_b16 v[156:157], v20 offset:2056\n" \
  "ds_read_b64_tr_b16 v[158:159], v21 offset:2056\n" \
  "s_nop " STR(GAP_B) "\n" \
  "buffer_load_dwordx4 v[44:47], v83, s[36:39], s91 offen sc1\n" \
  "ds_read_b64_tr_b16 v[144:145], v20 offset:4096\n" \
  "ds_read_b64_tr_b16 v[146:147], v21 offset:4096\n" \
  "ds_read_b64_tr_b16 v[160:161], v20 offset:4104\n" \
  "ds_read_b64_tr_b16 v[162:163], v21 offset:4104\n" \
  "s_nop " STR(GAP_V) "\n" \
  "buffer_load_dwordx4 v[96:99], v17, s[36:39], s50 offen offset:0 sc1\n" \
  "ds_read_b64_tr_b16 v[148:149], v20 offset:6144\n" \
  "ds_read_b64_tr_b16 v[150:151], v21 offset:6144\n" \
  "ds_read_b64_tr_b16 v[164:165], v20 offset:6152\n" \
  "ds_read_b64_tr_b16 v[166:167], v21 offset:6152\n" \
  "s_nop " STR(GAP_B) "\n" \
  "buffer_load_dwordx4 v[100:103], v17, s[36:39], s50 offen offset:1024 sc1\n" \
  "v_cvt_f32_i32 v92, s59\n" \
  "v_mov_b32 v93, s63\n" \
  "v_mul_f32 v92, v92, v95\n" \
  "v_cvt_i32_f32 v92, v92\n" \
  "v_add_u32 v92, s62, v92\n" \
  "v_min_u32 v92, -2, v92\n" \
  "buffer_store_dwordx2 v[92:93], v14, s[36:39], s50 offen sc1\n" \
  "s_mov_b32 vcc_lo, 0x33333333\n" \
  "s_mov_b32 vcc_hi, 0x33333333\n" \
  "LS2_%=:\n" \
  "s_waitcnt vmcnt(6)\n" \
  "v_mfma_f32_16x16x32_bf16 v[228:231], v[120:123], v[32:35], 0\n" \
  "v_mfma_f32_16x16x32_bf16 v[224:227], v[104:107], v[32:35], 0\n" \
  "v_mfma_f32_16x16x32_bf16 v[232:235], v[32:35], v[32:35], 0\n" \
  "s_waitcnt vmcnt(5)\n" \
  "v_mfma_f32_16x16x32_bf16 v[228:231], v[124:127], v[36:39], v[228:231]\n" \
  "v_mfma_f32_16x16x32_bf16 v[224:227], v[108:111], v[36:39], v[224:227]\n" \
  "v_mfma_f32_16x16x32_bf16 v[232:235], v[36:39], v[36:39], v[232:235]\n" \
  "s_waitcnt vmcnt(4)\n" \
  "v_mfma_f32_16x16x32_bf16 v[228:231], v[128:131], v[40:43], v[228:231]\n" \
  "v_mfma_f32_16x16x32_bf16 v[224:227], v[112:115], v[40:43], v[224:227]\n" \
  "v_mfma_f32_16x16x32_bf16 v[232:235], v[40:43], v[40:43], v[232:235]\n" \
  "s_waitcnt vmcnt(3)\n" \
  "v_mfma_f32_16x16x32_bf16 v[228:231], v[132:135], v[44:47], v[228:231]\n" \
  "v_mfma_f32_16x16x32_bf16 v[224:227], v[116:119], v[44:47], v[224:227]\n" \
  "v_mfma_f32_16x16x32_bf16 v[232:235], v[44:47], v[44:47], v[232:235]\n" \
  "s_nop 3\n" \
  "v_cndmask_b32_dpp v224, v228, v224, vcc quad_perm:[2,3,0,1] row_mask:0xf bank_mask:0xf\n" \
  "v_cndmask_b32_dpp v225, v229, v225, vcc quad_perm:[2,3,0,1] row_mask:0xf bank_mask:0xf\n" \
  "v_cndmask_b32_dpp v226, v230, v226, vcc quad_perm:[2,3,0,1] row_mask:0xf bank_mask:0xf\n" \
  "v_cndmask_b32_dpp v227, v231, v227, vcc quad_perm:[2,3,0,1] row_mask:0xf bank_mask:0xf\n" \
  "v_cndmask_b32_dpp v234, v235, v234, vcc quad_perm:[2,3,0,1] row_mask:0xf bank_mask:0xf\n" \
  "v_cmp_u_f32 vcc, v228, v228\n" \
  "v_max3_f32 v25, v224, v225, v226\n" \
  "v_max3_f32 v25, v25, v227, v234\n" \
  "s_waitcnt vmcnt(2)\n" \
  "v_smfmac_f32_16x16x64_bf16 v[184:187], v[96:99], a[192:199], v2\n" \
  "v_max_f32_dpp v26, v25, v25 quad_perm:[2,3,0,1] row_mask:0xf bank_mask:0xf\n" \
  "v_max_f32_dpp v25, v25, v25 quad_perm:[2,3,0,1] row_mask:0xf bank_mask:0xf\n" \
  "v_smfmac_f32_16x16x64_bf16 v[188:191], v[96:99], a[208:215], v2\n" \
  "v_permlane16_swap_b32 v25, v26\n" \
  "v_max_f32 v27, v25, v26\n" \
  "v_max_f32 v25, v25, v26\n" \
  "v_smfmac_f32_16x16x64_bf16 v[192:195], v[96:99], a[224:231], v2\n" \
  "s_cbranch_vccnz LBE_%=\n" \
  "LBH_%=:\n" \
  "v_permlane32_swap_b32 v25, v27\n" \
  "v_max_f32 v25, v25, v27\n" \
  "v_mul_f32 v25, s92, v25\n" \
  "v_fma_f32 v224, v224, s92, -v25\n" \
  "v_smfmac_f32_16x16x64_bf16 v[196:199], v[96:99], a[240:247], v2\n" \
  "v_fma_f32 v225, v225, s92, -v25\n" \
  "v_exp_f32 v224, v224\n" \
  "v_exp_f32 v225, v225\n" \
  "s_waitcnt vmcnt(1)\n" \
  "v_smfmac_f32_16x16x64_bf16 v[184:187], v[100:103], a[200:207], v2\n" \
  "v_fma_f32 v226, v226, s92, -v25\n" \
  "v_smfmac_f32_16x16x64_bf16 v[188:191], v[100:103], a[216:223], v2\n" \
  "v_fma_f32 v227, v227, s92, -v25\n" \
  "v_smfmac_f32_16x16x64_bf16 v[192:195], v[100:103], a[232:239], v2\n" \
  "v_exp_f32 v226, v226\n" \
  "v_smfmac_f32_16x16x64_bf16 v[196:199], v[100:103], a[248:255], v2\n" \
  "v_exp_f32 v227, v227\n" \
  "v_cvt_pk_bf16_f32 v228, v224, v225\n" \
  "v_cvt_pk_bf16_f32 v229, v226, v227\n" \
  "v_and_b32 v236, v228, v4\n" \
  "v_and_b32 v237, v229, v4\n" \
  "v_and_b32_dpp v238, v228, v4 quad_perm:[2,3,0,1] row_mask:0xf bank_mask:0xf\n" \
  "v_and_b32_dpp v239, v229, v4 quad_perm:[2,3,0,1] row_mask:0xf bank_mask:0xf\n" \
  "v_fma_f32 v234, v234, s92, -v25\n" \
  "v_and_b32_dpp v240, v228, v29 quad_perm:[2,3,0,1] row_mask:0xf bank_mask:0xf\n" \
  "v_mfma_f32_16x16x32_bf16 v[48:51], v[168:171], v[236:239], 0\n" \
  "v_and_b32_dpp v241, v229, v29 quad_perm:[2,3,0,1] row_mask:0xf bank_mask:0xf\n" \
  "v_and_b32 v242, v228, v29\n" \
  "v_mfma_f32_16x16x32_bf16 v[52:55], v[176:179], v[236:239], 0\n" \
  "v_and_b32 v243, v229, v29\n" \
  "v_exp_f32 v234, v234\n" \
  "v_cvt_pk_bf16_f32 v216, v184, v185\n" \
  "v_mfma_f32_16x16x32_bf16 v[48:51], v[172:175], v[240:243], v[48:51]\n" \
  "v_mov_b32_dpp v25, v234 quad_perm:[0,1,0,1] row_mask:0xf bank_mask:0xf\n" \
  "v_mov_b32_dpp v26, v234 quad_perm:[2,3,2,3] row_mask:0xf bank_mask:0xf\n" \
  "v_mfma_f32_16x16x32_bf16 v[52:55], v[180:183], v[240:243], v[52:55]\n" \
  "v_cvt_pk_bf16_f32 v25, v25, v26\n" \
  "v_and_b32 v230, v25, v4\n" \
  "v_and_b32 v231, v25, v4\n" \
  "v_and_b32 v232, v25, v29\n" \
  "v_and_b32 v233, v25, v29\n" \
  "v_mfma_f32_16x16x32_bf16 v[56:59], v[252:255], v[228:231], 0\n" \
  "v_cvt_pk_bf16_f32 v217, v186, v187\n" \
  "v_cvt_pk_bf16_f32 v218, v188, v189\n" \
  "v_cvt_pk_bf16_f32 v219, v190, v191\n" \
  "v_cvt_pk_bf16_f32 v220, v192, v193\n" \
  "v_cvt_pk_bf16_f32 v221, v194, v195\n" \
  "v_mfma_f32_16x16x32_bf16 v[48:51], v[216:219], v[230:233], v[48:51]\n" \
  "v_cvt_pk_bf16_f32 v222, v196, v197\n" \
  "v_cvt_pk_bf16_f32 v223, v198, v199\n" \
  "v_add_f32_dpp v56, v56, v56 quad_perm:[2,3,0,1] row_mask:0xf bank_mask:0xf\n" \
  "v_rcp_f32 v56, v56\n" \
  "v_mfma_f32_16x16x32_bf16 v[52:55], v[220:223], v[230:233], v[52:55]\n" \
  "s_nop 2\n" \
  "v_cmp_u_f32 vcc, v48, v48\n" \
  "v_mul_f32 v48, v48, v56\n" \
  "v_mul_f32 v49, v49, v56\n" \
  "v_mul_f32 v50, v50, v56\n" \
  "v_mul_f32 v51, v51, v56\n" \
  "v_pk_mul_f32 v[52:53], v[52:53], v[56:57] op_sel_hi:[1,0]\n" \
  "v_pk_mul_f32 v[54:55], v[54:55], v[56:57] op_sel_hi:[1,0]\n" \
  "v_cvt_pk_bf16_f32 v72, v48, v49\n" \
  "v_cvt_pk_bf16_f32 v73, v50, v51\n" \
  "v_cvt_pk_bf16_f32 v74, v52, v53\n" \
  "v_cvt_pk_bf16_f32 v75, v54, v55\n" \
  "s_cbranch_vccnz LBF_%=\n" \
  "LBG_%=:\n" \
  "buffer_store_dwordx4 v[72:75], v10, s[36:39], s51 offen sc1\n" \
  "s_memtime s[66:67]\n" \
  "s_add_u32 s61, s64, s96\n" \
  "s_cmp_lg_u32 %7, 0\n" \
  "s_cbranch_scc1 LO_%=\n" \
  "s_waitcnt lgkmcnt(0)\n" \
  "s_sub_i32 s61, s61, s66\n" \
  "s_max_i32 s61, s61, " STR(M_C) "\n" \
  "s_cmp_gt_i32 s61, 24\n" \
  "s_cbranch_scc1 LW_c_%=\n" \
  "LB_c_%=:\n" \
  "buffer_load_dwordx4 v[32:35], v11, s[36:39], s51 offen offset:0 sc1\n" \
  "buffer_load_dwordx4 v[36:39], v11, s[36:39], s51 offen offset:1024 sc1\n" \
  "buffer_load_dwordx4 v[40:43], v11, s[36:39], s51 offen offset:2048 sc1\n" \
  "buffer_load_dwordx4 v[44:47], v11, s[36:39], s51 offen offset:3072 sc1\n" \
  "v_mfma_f32_16x16x32_bf16 v[184:187], v[136:139], a[192:195], 0\n" \
  "v_mov_b64 v[48:49], 0\n" \
  "v_mfma_f32_16x16x32_bf16 v[188:191], v[152:155], a[192:195], 0\n" \
  "v_mov_b64 v[50:51], 0\n" \
  "v_mfma_f32_16x16x32_bf16 v[192:195], v[136:139], a[208:211], 0\n" \
  "v_mov_b64 v[52:53], 0\n" \
  "v_mfma_f32_16x16x32_bf16 v[196:199], v[152:155], a[208:211], 0\n" \
  "v_mov_b64 v[54:55], 0\n" \
  "v_mfma_f32_16x16x32_bf16 v[184:187], v[140:143], a[196:199], v[184:187]\n" \
  "v_mov_b64 v[56:57], 0\n" \
  "v_mfma_f32_16x16x32_bf16 v[188:191], v[156:159], a[196:199], v[188:191]\n" \
  "v_mov_b64 v[58:59], 0\n" \
  "v_mfma_f32_16x16x32_bf16 v[192:195], v[140:143], a[212:215], v[192:195]\n" \
  "v_mov_b64 v[60:61], 0\n" \
  "v_mfma_f32_16x16x32_bf16 v[196:199], v[156:159], a[212:215], v[196:199]\n" \
  "v_mov_b64 v[62:63], 0\n" \
  "v_mfma_f32_16x16x32_bf16 v[184:187], v[144:147], a[200:203], v[184:187]\n" \
  "v_mov_b64 v[64:65], 0\n" \
  "ds_read_b128 v[104:107], v84 offset:0\n" \
  "v_mfma_f32_16x16x32_bf16 v[188:191], v[160:163], a[200:203], v[188:191]\n" \
  "v_mov_b64 v[66:67], 0\n" \
  "ds_read_b128 v[108:111], v85 offset:0\n" \
  "v_mfma_f32_16x16x32_bf16 v[192:195], v[144:147], a[216:219], v[192:195]\n" \
  "v_mov_b64 v[68:69], 0\n" \
  "ds_read_b128 v[112:115], v86 offset:0\n" \
  "v_mfma_f32_16x16x32_bf16 v[196:199], v[160:163], a[216:219], v[196:199]\n" \
  "v_mov_b64 v[70:71], 0\n" \
  "ds_read_b128 v[116:119], v87 offset:0\n" \
  "v_mfma_f32_16x16x32_bf16 v[184:187], v[148:151], a[204:207], v[184:187]\n" \
  "ds_read_b128 v[120:123], v84 offset:4096\n" \
  "v_mfma_f32_16x16x32_bf16 v[188:191], v[164:167], a[204:207], v[188:191]\n" \
  "ds_read_b128 v[124:127], v85 offset:4096\n" \
  "v_mfma_f32_16x16x32_bf16 v[192:195], v[148:151], a[220:223], v[192:195]\n" \
  "ds_read_b128 v[128:131], v86 offset:4096\n" \
  "v_mfma_f32_16x16x32_bf16 v[196:199], v[164:167], a[220:223], v[196:199]\n" \
  "ds_read_b128 v[132:135], v87 offset:4096\n" \
  "s_add_u32 s48, s48, 1\n" \
  "s_nop 5\n" \
  "v_cvt_pk_bf16_f32 v168, v184, v185\n" \
  "v_cvt_pk_bf16_f32 v169, v186, v187\n" \
  "v_cvt_pk_bf16_f32 v170, v188, v189\n" \
  "v_cvt_pk_bf16_f32 v171, v190, v191\n" \
  "v_cvt_pk_bf16_f32 v172, v192, v193\n" \
  "v_cvt_pk_bf16_f32 v173, v194, v195\n" \
  "v_cvt_pk_bf16_f32 v174, v196, v197\n" \
  "v_cvt_pk_bf16_f32 v175, v198, v199\n" \
  "LS3_%=:\n" \
  "s_waitcnt vmcnt(3)\n" \
  "v_mfma_f32_16x16x32_bf16 v[224:227], v[248:251], v[32:35], 0\n" \
  "s_waitcnt vmcnt(2)\n" \
  "v_mfma_f32_16x16x32_bf16 v[224:227], v[248:251], v[36:39], v[224:227]\n" \
  "s_waitcnt vmcnt(1)\n" \
  "v_mfma_f32_16x16x32_bf16 v[224:227], v[248:251], v[40:43], v[224:227]\n" \
  "s_waitcnt vmcnt(0)\n" \
  "v_mfma_f32_16x16x32_bf16 v[224:227], v[248:251], v[44:47], v[224:227]\n" \
  "s_nop 7\n" \
  "v_cmp_u_f32 vcc, v224, v224\n" \
  "v_add_f32_dpp v224, v224, v224 row_ror:8 row_mask:0xf bank_mask:0xf\n" \
  "v_add_f32_dpp v225, v225, v225 row_ror:8 row_mask:0xf bank_mask:0xf\n" \
  "v_add_f32_dpp v226, v226, v226 row_ror:8 row_mask:0xf bank_mask:0xf\n" \
  "v_add_f32_dpp v227, v227, v227 row_ror:8 row_mask:0xf bank_mask:0xf\n" \
  "v_cvt_pk_bf16_f32 v228, v224, v225\n" \
  "v_cvt_pk_bf16_f32 v229, v226, v227\n" \
  "s_cbranch_vccnz LC_%=\n" \
  "LC2_%=:\n" \
  "v_permlane16_swap_b32 v228, v230\n" \
  "v_permlane16_swap_b32 v229, v231\n" \
  "buffer_store_dwordx4 v[228:231], v12, s[36:39], s53 offen sc1\n" \
  "s_memtime s[84:85]\n" \
  "LK4_%=:\n" \
  "ds_read_b64 v[92:93], v24 offset:14352\n" \
  "s_memtime s[66:67]\n" \
  "s_waitcnt lgkmcnt(0)\n" \
  "v_readfirstlane_b32 s59, v93\n" \
  "v_readfirstlane_b32 s63, v92\n" \
  "s_cmp_lg_u32 s59, s48\n" \
  "s_cbranch_scc1 LK4_%=\n" \
  "s_sub_i32 s59, s63, s64\n" \
  "s_sub_i32 s59, s59, " STR(F_P) "-300\n" \
  "s_sub_i32 s61, s63, s66\n" \
  "s_sub_i32 s60, s63, s84\n" \
  "s_add_i32 s60, s60, " STR(T_LATE) "\n" \
  "s_min_i32 s59, s59, s60\n" \
  "s_cmp_lt_i32 s59, 0\n" \
  "s_cbranch_scc1 LN_%=\n" \
  "LN2_%=:\n" \
  "v_mov_b32 v92, s63\n" \
  "v_mov_b32 v93, 0\n" \
  "ds_write_b64 v24, v[92:93] offset:14336\n" \
  "s_sub_i32 s59, s84, s66\n" \
  "s_add_i32 s59, s59, " STR(M_A) "-50\n" \
  "s_max_i32 s61, s61, s59\n" \
  "s_cmp_gt_i32 s61, 24\n" \
  "s_cbranch_scc1 LW_a_%=\n" \
  "LB_a_%=:\n" \
  "s_waitcnt lgkmcnt(0)\n" \
  "s_branch LL_%=\n" \
  "LO_%=:\n" \
  "s_cmp_eq_u32 %7, 1\n" \
  "s_waitcnt lgkmcnt(0)\n" \
  "s_cbranch_scc1 LK_%=\n" \
  "s_sub_i32 s61, s61, s66\n" \
  "s_max_i32 s61, s61, " STR(M_C) "\n" \
  "s_add_i32 s61, s61, " STR(D_PF) "\n" \
  "s_add_u32 s56, s48, 3\n" \
  "s_min_u32 s56, s56, 64\n" \
  "v_cmp_gt_u32 vcc, s56, v23\n" \
  "s_lshl_b32 s59, s70, 7\n" \
  "s_add_u32 s59, s59, " STR(CW_PROG) "*4\n" \
  "s_lshl_b32 s60, s71, 2\n" \
  "s_or_b32 s60, s60, %7\n" \
  "s_cmp_eq_u32 s60, 3\n" \
  "s_cselect_b32 s59, s59, 0x80000000\n" \
  "v_lshl_add_u32 v27, v0, 24, s59\n" \
  "v_add_u32 v28, s48, v255\n" \
  "v_add_u32 v28, 1, v28\n" \
  "buffer_store_dword v28, v27, s[44:47], 0 offen sc1\n" \
  "v_mfma_f32_16x16x32_bf16 v[184:187], v[136:139], a[192:195], 0\n" \
  "v_mov_b64 v[48:49], 0\n" \
  "v_mfma_f32_16x16x32_bf16 v[188:191], v[152:155], a[192:195], 0\n" \
  "v_mov_b64 v[50:51], 0\n" \
  "v_mfma_f32_16x16x32_bf16 v[192:195], v[136:139], a[208:211], 0\n" \
  "v_mov_b64 v[52:53], 0\n" \
  "v_mfma_f32_16x16x32_bf16 v[196:199], v[152:155], a[208:211], 0\n" \
  "v_mov_b64 v[54:55], 0\n" \
  "v_mfma_f32_16x16x32_bf16 v[184:187], v[140:143], a[196:199], v[184:187]\n" \
  "v_mov_b64 v[56:57], 0\n" \
  "v_mfma_f32_16x16x32_bf16 v[188:191], v[156:159], a[196:199], v[188:191]\n" \
  "v_mov_b64 v[58:59], 0\n" \
  "v_mfma_f32_16x16x32_bf16 v[192:195], v[140:143], a[212:215], v[192:195]\n" \
  "v_mov_b64 v[60:61], 0\n" \
  "v_mfma_f32_16x16x32_bf16 v[196:199], v[156:159], a[212:215], v[196:199]\n" \
  "v_mov_b64 v[62:63], 0\n" \
  "v_mfma_f32_16x16x32_bf16 v[184:187], v[144:147], a[200:203], v[184:187]\n" \
  "v_mov_b64 v[64:65], 0\n" \
  "ds_read_b128 v[104:107], v84 offset:0\n" \
  "v_mfma_f32_16x16x32_bf16 v[188:191], v[160:163], a[200:203], v[188:191]\n" \
  "v_mov_b64 v[66:67], 0\n" \
  "ds_read_b128 v[108:111], v85 offset:0\n" \
  "v_mfma_f32_16x16x32_bf16 v[192:195], v[144:147], a[216:219], v[192:195]\n" \
  "v_mov_b64 v[68:69], 0\n" \
  "ds_read_b128 v[112:115], v86 offset:0\n" \
  "v_mfma_f32_16x16x32_bf16 v[196:199], v[160:163], a[216:219], v[196:199]\n" \
  "v_mov_b64 v[70:71], 0\n" \
  "ds_read_b128 v[116:119], v87 offset:0\n" \
  "v_mfma_f32_16x16x32_bf16 v[184:187], v[148:151], a[204:207], v[184:187]\n" \
  "ds_read_b128 v[120:123], v84 offset:4096\n" \
  "v_mfma_f32_16x16x32_bf16 v[188:191], v[164:167], a[204:207], v[188:191]\n" \
  "ds_read_b128 v[124:127], v85 offset:4096\n" \
  "v_mfma_f32_16x16x32_bf16 v[192:195], v[148:151], a[220:223], v[192:195]\n" \
  "ds_read_b128 v[128:131], v86 offset:4096\n" \
  "v_mfma_f32_16x16x32_bf16 v[196:199], v[164:167], a[220:223], v[196:199]\n" \
  "ds_read_b128 v[132:135], v87 offset:4096\n" \
  "s_add_u32 s48, s48, 1\n" \
  "s_sub_u32 s60, s56, 1\n" \
  "s_lshr_b32 s59, s60, 2\n" \
  "s_xnor_b32 s59, s59, %8\n" \
  "s_and_b32 s59, s59, 1\n" \
  "s_lshl_b32 s59, s59, 13\n" \
  "s_lshl_b32 s55, s60, 19\n" \
  "s_or_b32 s55, s55, s59\n" \
  "s_cbranch_vccnz LF_%=\n" \
  "LF2_%=:\n" \
  "s_cmp_gt_i32 s61, 24\n" \
  "s_cbranch_scc1 LW_p_%=\n" \
  "LB_p_%=:\n" \
  "buffer_load_dwordx4 v[200:203], v247, s[40:43], s55 offen offset:0 sc1\n" \
  "buffer_load_dwordx4 v[204:207], v247, s[40:43], s55 offen offset:1024 sc1\n" \
  "buffer_load_dwordx4 v[208:211], v247, s[40:43], s55 offen offset:2048 sc1\n" \
  "buffer_load_dwordx4 v[212:215], v247, s[40:43], s55 offen offset:3072 sc1\n" \
  "buffer_load_dwordx4 v[216:219], v16, s[40:43], s55 offen offset:0 sc1\n" \
  "buffer_load_dwordx4 v[220:223], v16, s[40:43], s55 offen offset:1024 sc1\n" \
  "buffer_load_dwordx4 v[224:227], v16, s[40:43], s55 offen offset:2048 sc1\n" \
  "buffer_load_dwordx4 v[228:231], v16, s[40:43], s55 offen offset:3072 sc1\n" \
  "buffer_load_dword v23, v22, s[44:47], 0 offen sc1\n" \
  "v_cvt_pk_bf16_f32 v168, v184, v185\n" \
  "v_cvt_pk_bf16_f32 v169, v186, v187\n" \
  "v_cvt_pk_bf16_f32 v170, v188, v189\n" \
  "v_cvt_pk_bf16_f32 v171, v190, v191\n" \
  "v_cvt_pk_bf16_f32 v172, v192, v193\n" \
  "v_cvt_pk_bf16_f32 v173, v194, v195\n" \
  "v_cvt_pk_bf16_f32 v174, v196, v197\n" \
  "v_cvt_pk_bf16_f32 v175, v198, v199\n" \
  "v_mfma_f32_16x16x32_bf16 v[184:187], v[136:139], a[224:227], 0\n" \
  "v_mfma_f32_16x16x32_bf16 v[188:191], v[152:155], a[224:227], 0\n" \
  "v_mfma_f32_16x16x32_bf16 v[192:195], v[136:139], a[240:243], 0\n" \
  "v_mfma_f32_16x16x32_bf16 v[196:199], v[152:155], a[240:243], 0\n" \
  "v_mfma_f32_16x16x32_bf16 v[184:187], v[140:143], a[228:231], v[184:187]\n" \
  "v_mfma_f32_16x16x32_bf16 v[188:191], v[156:159], a[228:231], v[188:191]\n" \
  "v_mfma_f32_16x16x32_bf16 v[192:195], v[140:143], a[244:247], v[192:195]\n" \
  "v_mfma_f32_16x16x32_bf16 v[196:199], v[156:159], a[244:247], v[196:199]\n" \
  "v_mfma_f32_16x16x32_bf16 v[184:187], v[144:147], a[232:235], v[184:187]\n" \
  "v_mfma_f32_16x16x32_bf16 v[188:191], v[160:163], a[232:235], v[188:191]\n" \
  "v_mfma_f32_16x16x32_bf16 v[192:195], v[144:147], a[248:251], v[192:195]\n" \
  "v_mfma_f32_16x16x32_bf16 v[196:199], v[160:163], a[248:251], v[196:199]\n" \
  "v_mfma_f32_16x16x32_bf16 v[184:187], v[148:151], a[236:239], v[184:187]\n" \
  "v_mfma_f32_16x16x32_bf16 v[188:191], v[164:167], a[236:239], v[188:191]\n" \
  "v_mfma_f32_16x16x32_bf16 v[192:195], v[148:151], a[252:255], v[192:195]\n" \
  "v_mfma_f32_16x16x32_bf16 v[196:199], v[164:167], a[252:255], v[196:199]\n" \
  "s_nop 7\n" \
  "v_cvt_pk_bf16_f32 v176, v184, v185\n" \
  "v_cvt_pk_bf16_f32 v177, v186, v187\n" \
  "v_cvt_pk_bf16_f32 v178, v188, v189\n" \
  "v_cvt_pk_bf16_f32 v179, v190, v191\n" \
  "v_cvt_pk_bf16_f32 v180, v192, v193\n" \
  "v_cvt_pk_bf16_f32 v181, v194, v195\n" \
  "v_cvt_pk_bf16_f32 v182, v196, v197\n" \
  "v_cvt_pk_bf16_f32 v183, v198, v199\n" \
  "s_waitcnt lgkmcnt(0)\n" \
  "s_branch LM_%=\n" \
  "LK_%=:\n" \
  "buffer_load_dwordx2 v[92:93], v13, s[36:39], s50 offen sc1\n" \
  "s_mov_b32 s94, s63\n" \
  "v_mfma_f32_16x16x32_bf16 v[184:187], v[136:139], a[192:195], 0\n" \
  "v_mov_b64 v[48:49], 0\n" \
  "v_mfma_f32_16x16x32_bf16 v[188:191], v[152:155], a[192:195], 0\n" \
  "v_mov_b64 v[50:51], 0\n" \
  "v_mfma_f32_16x16x32_bf16 v[192:195], v[136:139], a[208:211], 0\n" \
  "v_mov_b64 v[52:53], 0\n" \
  "v_mfma_f32_16x16x32_bf16 v[196:199], v[152:155], a[208:211], 0\n" \
  "v_mov_b64 v[54:55], 0\n" \
  "v_mfma_f32_16x16x32_bf16 v[184:187], v[140:143], a[196:199], v[184:187]\n" \
  "v_mov_b64 v[56:57], 0\n" \
  "v_mfma_f32_16x16x32_bf16 v[188:191], v[156:159], a[196:199], v[188:191]\n" \
  "v_mov_b64 v[58:59], 0\n" \
  "v_mfma_f32_16x16x32_bf16 v[192:195], v[140:143], a[212:215], v[192:195]\n" \
  "v_mov_b64 v[60:61], 0\n" \
  "v_mfma_f32_16x16x32_bf16 v[196:199], v[156:159], a[212:215], v[196:199]\n" \
  "v_mov_b64 v[62:63], 0\n" \
  "v_mfma_f32_16x16x32_bf16 v[184:187], v[144:147], a[200:203], v[184:187]\n" \
  "v_mov_b64 v[64:65], 0\n" \
  "ds_read_b128 v[104:107], v84 offset:0\n" \
  "v_mfma_f32_16x16x32_bf16 v[188:191], v[160:163], a[200:203], v[188:191]\n" \
  "v_mov_b64 v[66:67], 0\n" \
  "ds_read_b128 v[108:111], v85 offset:0\n" \
  "v_mfma_f32_16x16x32_bf16 v[192:195], v[144:147], a[216:219], v[192:195]\n" \
  "v_mov_b64 v[68:69], 0\n" \
  "ds_read_b128 v[112:115], v86 offset:0\n" \
  "v_mfma_f32_16x16x32_bf16 v[196:199], v[160:163], a[216:219], v[196:199]\n" \
  "v_mov_b64 v[70:71], 0\n" \
  "ds_read_b128 v[116:119], v87 offset:0\n" \
  "v_mfma_f32_16x16x32_bf16 v[184:187], v[148:151], a[204:207], v[184:187]\n" \
  "ds_read_b128 v[120:123], v84 offset:4096\n" \
  "v_mfma_f32_16x16x32_bf16 v[188:191], v[164:167], a[204:207], v[188:191]\n" \
  "ds_read_b128 v[124:127], v85 offset:4096\n" \
  "v_mfma_f32_16x16x32_bf16 v[192:195], v[148:151], a[220:223], v[192:195]\n" \
  "ds_read_b128 v[128:131], v86 offset:4096\n" \
  "v_mfma_f32_16x16x32_bf16 v[196:199], v[164:167], a[220:223], v[196:199]\n" \
  "ds_read_b128 v[132:135], v87 offset:4096\n" \
  "s_add_u32 s48, s48, 1\n" \
  "s_nop 5\n" \
  "v_cvt_pk_bf16_f32 v168, v184, v185\n" \
  "v_cvt_pk_bf16_f32 v169, v186, v187\n" \
  "v_cvt_pk_bf16_f32 v170, v188, v189\n" \
  "v_cvt_pk_bf16_f32 v171, v190, v191\n" \
  "v_cvt_pk_bf16_f32 v172, v192, v193\n" \
  "v_cvt_pk_bf16_f32 v173, v194, v195\n" \
  "v_cvt_pk_bf16_f32 v174, v196, v197\n" \
  "v_cvt_pk_bf16_f32 v175, v198, v199\n" \
  "s_nop " STR(D_K) "\n" \
  "s_waitcnt lgkmcnt(0)\n" \
  "s_memtime s[72:73]\n" \
  "s_memrealtime s[74:75]\n" \
  "s_nop 1\n" \
  "s_memrealtime s[76:77]\n" \
  "s_nop 1\n" \
  "s_memrealtime s[78:79]\n" \
  "s_nop 1\n" \
  "s_memrealtime s[80:81]\n" \
  "s_nop 1\n" \
  "s_memrealtime s[82:83]\n" \
  "s_nop 1\n" \
  "s_memrealtime s[84:85]\n" \
  "s_nop 1\n" \
  "s_memrealtime s[86:87]\n" \
  "s_nop 1\n" \
  "s_memrealtime s[88:89]\n" \
  "s_nop 1\n" \
  "s_memrealtime s[90:91]\n" \
  "s_mov_b32 s59, 0\n" \
  "s_waitcnt lgkmcnt(0)\n" \
  "s_cmp_eq_u32 s76, s74\n" \
  "s_addc_u32 s59, s59, 0\n" \
  "s_cmp_eq_u32 s78, s74\n" \
  "s_addc_u32 s59, s59, 0\n" \
  "s_cmp_eq_u32 s80, s74\n" \
  "s_addc_u32 s59, s59, 0\n" \
  "s_cmp_eq_u32 s82, s74\n" \
  "s_addc_u32 s59, s59, 0\n" \
  "s_cmp_eq_u32 s84, s74\n" \
  "s_addc_u32 s59, s59, 0\n" \
  "s_cmp_eq_u32 s86, s74\n" \
  "s_addc_u32 s59, s59, 0\n" \
  "s_cmp_eq_u32 s88, s74\n" \
  "s_addc_u32 s59, s59, 0\n" \
  "s_cmp_eq_u32 s90, s74\n" \
  "s_addc_u32 s59, s59, 0\n" \
  "s_mul_i32 s59, s59, 12\n" \
  "s_add_u32 s68, s72, s59\n" \
  "s_add_u32 s59, s74, 4\n" \
  "s_mul_i32 s69, s59, 24\n" \
  "s_sub_u32 s60, s69, s62\n" \
  "s_sub_u32 s59, s68, s56\n" \
  "s_mov_b32 s62, s69\n" \
  "s_mov_b32 s56, s68\n" \
  "v_cvt_f32_u32 v27, s59\n" \
  "v_cvt_f32_u32 v28, s60\n" \
  "v_rcp_f32 v26, v27\n" \
  "v_rcp_f32 v19, v28\n" \
  "v_mul_f32 v26, v26, v28\n" \
  "v_mul_f32 v95, " STR(DECAY) ", v95\n" \
  "v_mul_f32 v19, v19, v27\n" \
  "v_max_f32 v95, v95, v26\n" \
  "v_cvt_f32_u32 v28, " STR(F_P) "\n" \
  "v_mul_f32 v27, v95, v28\n" \
  "v_cvt_u32_f32 v27, v27\n" \
  "v_min_u32 v27, 0x7fff, v27\n" \
  "s_nop 0\n" \
  "v_readfirstlane_b32 s63, v27\n" \
  "LK2_%=:\n" \
  "s_waitcnt vmcnt(0)\n" \
  "v_max_u32 v26, v92, v93\n" \
  "v_cmp_eq_u32 vcc, -1, v26\n" \
  "v_subrev_u32 v25, s62, v92\n" \
  "v_subrev_u32 v26, s94, v93\n" \
  "s_nop 0\n" \
  "v_max_i32_dpp v25, v25, v25 quad_perm:[1,0,3,2] row_mask:0xf bank_mask:0xf\n" \
  "v_max_i32_dpp v26, v26, v26 quad_perm:[1,0,3,2] row_mask:0xf bank_mask:0xf\n" \
  "s_cbranch_vccnz LK3_%=\n" \
  "v_max_i32_dpp v25, v25, v25 quad_perm:[2,3,0,1] row_mask:0xf bank_mask:0xf\n" \
  "v_max_i32_dpp v26, v26, v26 quad_perm:[2,3,0,1] row_mask:0xf bank_mask:0xf\n" \
  "LK5_%=:\n" \
  "v_cvt_f32_i32 v25, v25\n" \
  "v_mov_b32 v93, s48\n" \
  "v_mul_f32 v25, v25, v19\n" \
  "v_cvt_i32_f32 v25, v25\n" \
  "v_add_u32 v92, s68, v25\n" \
  "ds_write_b64 v24, v[92:93] offset:14352\n" \
  "v_max_i32 v26, 0, v26\n" \
  "v_min_i32 v26, 400, v26\n" \
  "ds_write_b32 v24, v26 offset:14344\n" \
  "s_waitcnt lgkmcnt(0)\n" \
  "s_branch LL_%=\n" \
  ".p2align 6\n" \
  "LM_%=:\n" \
  "s_barrier\n" \
  "buffer_load_dwordx4 v[32:35], v80, s[36:39], s49 offen sc1\n" \
  "s_xor_b32 s51, s51, 1<<20\n" \
  "s_xor_b32 s52, s52, 1<<20\n" \
  "s_add_u32 s53, s53, 32768\n" \
  "s_lshl_b32 s60, s48, 19\n" \
  "s_add_u32 s60, s60, " STR(EXP_B) "<<21\n" \
  "s_lshr_b32 s59, s60, 21\n" \
  "s_nop 5\n" \
  "buffer_load_dwordx4 v[36:39], v81, s[36:39], s49 offen sc1\n" \
  "s_lshr_b32 s59, %8, s59\n" \
  "s_and_b32 s94, s59, 1\n" \
  "s_lshl_b32 s94, s94, 13\n" \
  "s_or_b32 s50, s60, s94\n" \
  "s_lshl_b32 s94, s70, 14\n" \
  "s_add_u32 s91, s50, s94\n" \
  "s_nop 5\n" \
  "buffer_load_dwordx4 v[40:43], v82, s[36:39], s49 offen sc1\n" \
  "s_and_b32 s94, s59, 2\n" \
  "s_lshl_b32 s94, s94, 12\n" \
  "s_add_u32 s60, s60, 1<<21\n" \
  "s_or_b32 s54, s60, s94\n" \
  "s_nop 7\n" \
  "buffer_load_dwordx4 v[44:47], v83, s[36:39], s49 offen sc1\n" \
  "s_waitcnt vmcnt(4)\n" \
  "ds_write_b128 v18, v[200:203] offset:0\n" \
  "ds_write_b128 v18, v[204:207] offset:1024\n" \
  "ds_write_b128 v18, v[208:211] offset:2048\n" \
  "ds_write_b128 v18, v[212:215] offset:3072\n" \
  "buffer_store_dwordx4 v[88:91], v11, s[36:39], s52 offen sc1\n" \
  "buffer_store_dwordx4 v[88:91], v15, s[36:39], s54 offen sc1\n" \
  "ds_write_b128 v18, v[216:219] offset:4096\n" \
  "ds_write_b128 v18, v[220:223] offset:5120\n" \
  "ds_write_b128 v18, v[224:227] offset:6144\n" \
  "ds_write_b128 v18, v[228:231] offset:7168\n" \
  "s_branch LS1_%=\n" \
  "LA_%=:\n" \
  "ds_read_b64 v[92:93], v24 offset:14336\n" \
  "v_max3_u32 v25, v32, v33, v34\n" \
  "v_max3_u32 v25, v25, v35, v36\n" \
  "v_max3_u32 v25, v25, v37, v38\n" \
  "v_max3_u32 v25, v25, v39, v40\n" \
  "v_max3_u32 v25, v25, v41, v42\n" \
  "v_max3_u32 v25, v25, v43, v44\n" \
  "v_max3_u32 v25, v25, v45, v46\n" \
  "v_max_u32 v25, v25, v47\n" \
  "v_cmp_eq_u32 vcc, -1, v25\n" \
  "s_cbranch_vccz LA4_%=\n" \
  "s_add_u32 s58, s58, 1\n" \
  "s_cmp_lt_u32 s58, " STR(SPIN_CAP) "\n" \
  "s_cbranch_scc1 LA1_%=\n" \
  "v_mov_b32 v27, " STR(CW_DEAD) "*4\n" \
  "v_mov_b32 v28, 1\n" \
  "buffer_store_dword v28, v27, s[44:47], 0 offen sc1\n" \
  "s_branch LA4_%=\n" \
  "LA1_%=:\n" \
  "s_waitcnt lgkmcnt(0)\n" \
  "v_readfirstlane_b32 s64, v92\n" \
  "s_memtime s[66:67]\n" \
  "s_waitcnt lgkmcnt(0)\n" \
  "s_sub_u32 s59, " STR(O_A) ", s66\n" \
  "LQ_ra_%=:\n" \
  "s_add_u32 s64, s64, s95\n" \
  "s_add_i32 s61, s64, s59\n" \
  "s_cmp_lt_i32 s61, -" STR(T_TOL) "\n" \
  "s_cbranch_scc1 LQ_ra_%=\n" \
  "s_max_i32 s61, s61, 0\n" \
  "s_min_i32 s61, s61, 4000\n" \
  "s_bitcmp0_b32 s61, 4\n" \
  "s_cbranch_scc1 LX_ra_%=\n" \
  "s_nop 8\n" \
  "LX_ra_%=:\n" \
  "s_lshr_b32 s61, s61, 5\n" \
  "s_cbranch_scc0 LB_ra_%=\n" \
  ".p2align 4\n" \
  "LY_ra_%=:\n" \
  "s_sub_u32 s61, s61, 1\n" \
  "s_cmp_lg_u32 s61, 0\n" \
  "s_cbranch_scc1 LY_ra_%=\n" \
  "LB_ra_%=:\n" \
  "buffer_load_dwordx4 v[32:35], v80, s[36:39], s49 offen sc1\n" \
  "s_nop 11\n" \
  "buffer_load_dwordx4 v[36:39], v81, s[36:39], s49 offen sc1\n" \
  "s_nop 11\n" \
  "buffer_load_dwordx4 v[40:43], v82, s[36:39], s49 offen sc1\n" \
  "s_nop 11\n" \
  "buffer_load_dwordx4 v[44:47], v83, s[36:39], s49 offen sc1\n" \
  "v_mov_b64 v[48:49], 0\n" \
  "v_mov_b64 v[50:51], 0\n" \
  "v_mov_b64 v[52:53], 0\n" \
  "v_mov_b64 v[54:55], 0\n" \
  "v_mov_b64 v[56:57], 0\n" \
  "v_mov_b64 v[58:59], 0\n" \
  "v_mov_b64 v[60:61], 0\n" \
  "v_mov_b64 v[62:63], 0\n" \
  "v_mov_b64 v[64:65], 0\n" \
  "v_mov_b64 v[66:67], 0\n" \
  "v_mov_b64 v[68:69], 0\n" \
  "v_mov_b64 v[70:71], 0\n" \
  "buffer_store_dword v27, v88, s[36:39], 0 offen sc1\n" \
  "buffer_store_dword v27, v88, s[36:39], 0 offen sc1\n" \
  "v_sub_u32 v27, s64, v92\n" \
  "ds_max_u32 v24, v27 offset:14340\n" \
  "s_branch LS1_%=\n" \
  "LW_b_%=:\n" \
  "s_min_i32 s61, s61, 4000\n" \
  "s_sub_i32 s61, s61, 72\n" \
  "s_max_i32 s61, s61, 0\n" \
  "s_bitcmp0_b32 s61, 4\n" \
  "s_cbranch_scc1 LX_b_%=\n" \
  "s_nop 8\n" \
  "LX_b_%=:\n" \
  "s_lshr_b32 s61, s61, 5\n" \
  "s_cbranch_scc0 LB_b_%=\n" \
  ".p2align 4\n" \
  "LY_b_%=:\n" \
  "s_sub_u32 s61, s61, 1\n" \
  "s_cmp_lg_u32 s61, 0\n" \
  "s_cbranch_scc1 LY_b_%=\n" \
  "s_branch LB_b_%=\n" \
  "LBE_%=:\n" \
  "v_max3_u32 v26, v32, v33, v34\n" \
  "v_max3_u32 v26, v26, v35, v36\n" \
  "v_max3_u32 v26, v26, v37, v38\n" \
  "v_max3_u32 v26, v26, v39, v40\n" \
  "v_max3_u32 v26, v26, v41, v42\n" \
  "v_max3_u32 v26, v26, v43, v44\n" \
  "v_max3_u32 v26, v26, v45, v46\n" \
  "v_max_u32 v26, v26, v47\n" \
  "v_cmp_eq_u32 vcc, -1, v26\n" \
  "s_cbranch_vccz LBH_%=\n" \
  "s_add_u32 s58, s58, 1\n" \
  "s_cmp_lt_u32 s58, " STR(SPIN_CAP) "\n" \
  "s_cbranch_scc1 LB1_%=\n" \
  "v_mov_b32 v27, " STR(CW_DEAD) "*4\n" \
  "v_mov_b32 v28, 1\n" \
  "buffer_store_dword v28, v27, s[44:47], 0 offen sc1\n" \
  "s_branch LBI_%=\n" \
  "LBI_%=:\n" \
  "v_mov_b32 v27, v25\n" \
  "s_nop 1\n" \
  "s_branch LBH_%=\n" \
  "LBF_%=:\n" \
  "v_max3_u32 v25, v32, v33, v34\n" \
  "v_max3_u32 v25, v25, v35, v36\n" \
  "v_max3_u32 v25, v25, v37, v38\n" \
  "v_max3_u32 v25, v25, v39, v40\n" \
  "v_max3_u32 v25, v25, v41, v42\n" \
  "v_max3_u32 v25, v25, v43, v44\n" \
  "v_max3_u32 v25, v25, v45, v46\n" \
  "v_max_u32 v25, v25, v47\n" \
  "v_max3_u32 v25, v25, v96, v97\n" \
  "v_max3_u32 v25, v25, v98, v99\n" \
  "v_max3_u32 v25, v25, v100, v101\n" \
  "v_max3_u32 v25, v25, v102, v103\n" \
  "v_cmp_eq_u32 vcc, -1, v25\n" \
  "s_cbranch_vccz LBG_%=\n" \
  "s_add_u32 s58, s58, 1\n" \
  "s_cmp_lt_u32 s58, " STR(SPIN_CAP) "\n" \
  "s_cbranch_scc1 LB1_%=\n" \
  "v_mov_b32 v27, " STR(CW_DEAD) "*4\n" \
  "v_mov_b32 v28, 1\n" \
  "buffer_store_dword v28, v27, s[44:47], 0 offen sc1\n" \
  "s_branch LBG_%=\n" \
  "LB1_%=:\n" \
  "s_waitcnt vmcnt(0)\n" \
  "v_mov_b64 v[184:185], 0\n" \
  "v_mov_b64 v[186:187], 0\n" \
  "v_mov_b64 v[188:189], 0\n" \
  "v_mov_b64 v[190:191], 0\n" \
  "v_mov_b64 v[192:193], 0\n" \
  "v_mov_b64 v[194:195], 0\n" \
  "v_mov_b64 v[196:197], 0\n" \
  "v_mov_b64 v[198:199], 0\n" \
  "s_memtime s[66:67]\n" \
  "s_waitcnt lgkmcnt(0)\n" \
  "s_sub_u32 s59, " STR(F_B) "+" STR(O_B) ", s66\n" \
  "LQ_rb_%=:\n" \
  "s_add_u32 s64, s64, s95\n" \
  "s_add_i32 s61, s64, s59\n" \
  "s_cmp_lt_i32 s61, -" STR(T_TOL) "\n" \
  "s_cbranch_scc1 LQ_rb_%=\n" \
  "s_max_i32 s61, s61, 0\n" \
  "s_min_i32 s61, s61, 4000\n" \
  "s_bitcmp0_b32 s61, 4\n" \
  "s_cbranch_scc1 LX_rb_%=\n" \
  "s_nop 8\n" \
  "LX_rb_%=:\n" \
  "s_lshr_b32 s61, s61, 5\n" \
  "s_cbranch_scc0 LB_rb_%=\n" \
  ".p2align 4\n" \
  "LY_rb_%=:\n" \
  "s_sub_u32 s61, s61, 1\n" \
  "s_cmp_lg_u32 s61, 0\n" \
  "s_cbranch_scc1 LY_rb_%=\n" \
  "LB_rb_%=:\n" \
  "buffer_load_dwordx4 v[32:35], v80, s[36:39], s91 offen sc1\n" \
  "s_nop 11\n" \
  "buffer_load_dwordx4 v[36:39], v81, s[36:39], s91 offen sc1\n" \
  "s_nop 11\n" \
  "buffer_load_dwordx4 v[40:43], v82, s[36:39], s91 offen sc1\n" \
  "s_nop 11\n" \
  "buffer_load_dwordx4 v[44:47], v83, s[36:39], s91 offen sc1\n" \
  "s_nop 11\n" \
  "buffer_load_dwordx4 v[96:99], v17, s[36:39], s50 offen offset:0 sc1\n" \
  "s_nop 11\n" \
  "buffer_load_dwordx4 v[100:103], v17, s[36:39], s50 offen offset:1024 sc1\n" \
  "buffer_store_dword v27, v88, s[36:39], 0 offen sc1\n" \
  "s_mov_b32 vcc_lo, 0x33333333\n" \
  "s_mov_b32 vcc_hi, 0x33333333\n" \
  "s_branch LS2_%=\n" \
  "LW_c_%=:\n" \
  "s_min_i32 s61, s61, 4000\n" \
  "s_sub_i32 s61, s61, 72\n" \
  "s_max_i32 s61, s61, 0\n" \
  "s_bitcmp0_b32 s61, 4\n" \
  "s_cbranch_scc1 LX_c_%=\n" \
  "s_nop 8\n" \
  "LX_c_%=:\n" \
  "s_lshr_b32 s61, s61, 5\n" \
  "s_cbranch_scc0 LB_c_%=\n" \
  ".p2align 4\n" \
  "LY_c_%=:\n" \
  "s_sub_u32 s61, s61, 1\n" \
  "s_cmp_lg_u32 s61, 0\n" \
  "s_cbranch_scc1 LY_c_%=\n" \
  "s_branch LB_c_%=\n" \
  "LW_a_%=:\n" \
  "s_min_i32 s61, s61, 4000\n" \
  "s_sub_i32 s61, s61, 72\n" \
  "s_max_i32 s61, s61, 0\n" \
  "s_bitcmp0_b32 s61, 4\n" \
  "s_cbranch_scc1 LX_a_%=\n" \
  "s_nop 8\n" \
  "LX_a_%=:\n" \
  "s_lshr_b32 s61, s61, 5\n" \
  "s_cbranch_scc0 LB_a_%=\n" \
  ".p2align 4\n" \
  "LY_a_%=:\n" \
  "s_sub_u32 s61, s61, 1\n" \
  "s_cmp_lg_u32 s61, 0\n" \
  "s_cbranch_scc1 LY_a_%=\n" \
  "s_branch LB_a_%=\n" \
  "LN_%=:\n" \
  "LN3_%=:\n" \
  "s_add_u32 s63, s63, s95\n" \
  "s_add_i32 s61, s61, s95\n" \
  "s_add_i32 s59, s59, s95\n" \
  "s_cmp_lt_i32 s59, 0\n" \
  "s_cbranch_scc1 LN3_%=\n" \
  "s_branch LN2_%=\n" \
  "LC_%=:\n" \
  "v_max3_u32 v25, v32, v33, v34\n" \
  "v_max3_u32 v25, v25, v35, v36\n" \
  "v_max3_u32 v25, v25, v37, v38\n" \
  "v_max3_u32 v25, v25, v39, v40\n" \
  "v_max3_u32 v25, v25, v41, v42\n" \
  "v_max3_u32 v25, v25, v43, v44\n" \
  "v_max3_u32 v25, v25, v45, v46\n" \
  "v_max_u32 v25, v25, v47\n" \
  "v_cmp_eq_u32 vcc, -1, v25\n" \
  "s_cbranch_vccz LC2_%=\n" \
  "s_add_u32 s58, s58, 1\n" \
  "s_cmp_lt_u32 s58, " STR(SPIN_CAP) "\n" \
  "s_cbranch_scc1 LC1_%=\n" \
  "v_mov_b32 v27, " STR(CW_DEAD) "*4\n" \
  "v_mov_b32 v28, 1\n" \
  "buffer_store_dword v28, v27, s[44:47], 0 offen sc1\n" \
  "s_branch LC2_%=\n" \
  "LC1_%=:\n" \
  "s_add_u32 s60, s96, " STR(O_C) "\n" \
  "s_memtime s[66:67]\n" \
  "s_waitcnt lgkmcnt(0)\n" \
  "s_sub_u32 s59, s60, s66\n" \
  "LQ_rc_%=:\n" \
  "s_add_u32 s64, s64, s95\n" \
  "s_add_i32 s61, s64, s59\n" \
  "s_cmp_lt_i32 s61, -" STR(T_TOL) "\n" \
  "s_cbranch_scc1 LQ_rc_%=\n" \
  "s_max_i32 s61, s61, 0\n" \
  "s_min_i32 s61, s61, 4000\n" \
  "s_bitcmp0_b32 s61, 4\n" \
  "s_cbranch_scc1 LX_rc_%=\n" \
  "s_nop 8\n" \
  "LX_rc_%=:\n" \
  "s_lshr_b32 s61, s61, 5\n" \
  "s_cbranch_scc0 LB_rc_%=\n" \
  ".p2align 4\n" \
  "LY_rc_%=:\n" \
  "s_sub_u32 s61, s61, 1\n" \
  "s_cmp_lg_u32 s61, 0\n" \
  "s_cbranch_scc1 LY_rc_%=\n" \
  "LB_rc_%=:\n" \
  "buffer_load_dwordx4 v[32:35], v11, s[36:39], s51 offen offset:0 sc1\n" \
  "buffer_load_dwordx4 v[36:39], v11, s[36:39], s51 offen offset:1024 sc1\n" \
  "buffer_load_dwordx4 v[40:43], v11, s[36:39], s51 offen offset:2048 sc1\n" \
  "buffer_load_dwordx4 v[44:47], v11, s[36:39], s51 offen offset:3072 sc1\n" \
  "s_branch LS3_%=\n" \
  "LW_p_%=:\n" \
  "s_min_i32 s61, s61, 4000\n" \
  "s_sub_i32 s61, s61, 72\n" \
  "s_max_i32 s61, s61, 0\n" \
  "s_bitcmp0_b32 s61, 4\n" \
  "s_cbranch_scc1 LX_p_%=\n" \
  "s_nop 8\n" \
  "LX_p_%=:\n" \
  "s_lshr_b32 s61, s61, 5\n" \
  "s_cbranch_scc0 LB_p_%=\n" \
  ".p2align 4\n" \
  "LY_p_%=:\n" \
  "s_sub_u32 s61, s61, 1\n" \
  "s_cmp_lg_u32 s61, 0\n" \
  "s_cbranch_scc1 LY_p_%=\n" \
  "s_branch LB_p_%=\n" \
  "LF_%=:\n" \
  "buffer_load_dword v23, v22, s[44:47], 0 offen sc1\n" \
  "s_mov_b32 s61, 0\n" \
  "s_waitcnt vmcnt(0)\n" \
  "v_cmp_gt_u32 vcc, s56, v23\n" \
  "s_cbranch_vccz LF2_%=\n" \
  "s_sleep 2\n" \
  "s_add_u32 s58, s58, 1\n" \
  "s_cmp_lt_u32 s58, " STR(SPIN_CAP) "\n" \
  "s_cbranch_scc1 LF_%=\n" \
  "v_mov_b32 v27, " STR(CW_DEAD) "*4\n" \
  "v_mov_b32 v28, 1\n" \
  "buffer_store_dword v28, v27, s[44:47], 0 offen sc1\n" \
  "s_branch LF2_%=\n" \
  "LK3_%=:\n" \
  "s_sleep 2\n" \
  "buffer_load_dwordx2 v[92:93], v13, s[36:39], s50 offen sc1\n" \
  "s_add_u32 s58, s58, 1\n" \
  "s_cmp_lt_u32 s58, " STR(SPIN_CAP) "\n" \
  "s_cbranch_scc1 LK2_%=\n" \
  "v_mov_b32 v27, " STR(CW_DEAD) "*4\n" \
  "v_mov_b32 v28, 1\n" \
  "buffer_store_dword v28, v27, s[44:47], 0 offen sc1\n" \
  "s_branch LK6_%=\n" \
  "LK6_%=:\n" \
  "v_mov_b32 v25, 0\n" \
  "s_branch LK5_%=\n" \
  "LE_%=:\n" \
  "s_endpgm\n" \
  ""

#define GEMM_ASM \
  "v_mbcnt_lo_u32_b32 v4, -1, 0\n" \
  "v_mbcnt_hi_u32_b32 v4, -1, v4\n" \
  "v_and_b32 v116, 15, v4\n" \
  "v_lshrrev_b32 v117, 4, v4\n" \
  "s_lshl_b32 s86, %7, 13\n" \
  "s_add_u32 s86, s86, %8\n" \
  "v_lshrrev_b32 v5, 3, v4\n" \
  "v_and_b32 v6, 7, v4\n" \
  "v_lshlrev_b32 v8, 13, v5\n" \
  "v_lshl_add_u32 v8, v6, 4, v8\n" \
  "v_and_b32 v9, 1, v117\n" \
  "v_lshlrev_b32 v9, 5, v9\n" \
  "v_xor_b32 v8, v8, v9\n" \
  "v_xor_b32 v9, 64, v8\n" \
  "s_mov_b64 s[68:69], %1\n" \
  "s_and_b32 s69, s69, 0xffff\n" \
  "s_mov_b32 s70, 8388608\n" \
  "s_mov_b32 s71, 0x00020000\n" \
  "s_lshl_b32 s92, %7, 19\n" \
  "s_add_u32 s92, s92, 0\n" \
  "s_add_u32 m0, s86, 98304\n" \
  "s_nop 0\n" \
  "buffer_load_dwordx4 v8, s[68:71], s92 offen lds\n" \
  "s_add_u32 m0, s86, 99328\n" \
  "s_add_u32 s92, s92, 65536\n" \
  "buffer_load_dwordx4 v9, s[68:71], s92 offen lds\n" \
  "s_add_u32 m0, s86, 100352\n" \
  "s_add_u32 s92, s92, 65536\n" \
  "buffer_load_dwordx4 v8, s[68:71], s92 offen lds\n" \
  "s_add_u32 m0, s86, 101376\n" \
  "s_add_u32 s92, s92, 65536\n" \
  "buffer_load_dwordx4 v9, s[68:71], s92 offen lds\n" \
  "s_add_u32 m0, s86, 102400\n" \
  "s_add_u32 s92, s92, 65536\n" \
  "buffer_load_dwordx4 v8, s[68:71], s92 offen lds\n" \
  "s_add_u32 m0, s86, 103424\n" \
  "s_add_u32 s92, s92, 65536\n" \
  "buffer_load_dwordx4 v9, s[68:71], s92 offen lds\n" \
  "s_add_u32 m0, s86, 104448\n" \
  "s_add_u32 s92, s92, 65536\n" \
  "buffer_load_dwordx4 v8, s[68:71], s92 offen lds\n" \
  "s_add_u32 m0, s86, 105472\n" \
  "s_add_u32 s92, s92, 65536\n" \
  "buffer_load_dwordx4 v9, s[68:71], s92 offen lds\n" \
  "s_lshl_b32 s92, %7, 19\n" \
  "s_add_u32 s92, s92, 2097152\n" \
  "s_add_u32 m0, s86, 131072\n" \
  "s_nop 0\n" \
  "buffer_load_dwordx4 v8, s[68:71], s92 offen lds\n" \
  "s_add_u32 m0, s86, 132096\n" \
  "s_add_u32 s92, s92, 65536\n" \
  "buffer_load_dwordx4 v9, s[68:71], s92 offen lds\n" \
  "s_add_u32 m0, s86, 133120\n" \
  "s_add_u32 s92, s92, 65536\n" \
  "buffer_load_dwordx4 v8, s[68:71], s92 offen lds\n" \
  "s_add_u32 m0, s86, 134144\n" \
  "s_add_u32 s92, s92, 65536\n" \
  "buffer_load_dwordx4 v9, s[68:71], s92 offen lds\n" \
  "s_add_u32 m0, s86, 135168\n" \
  "s_add_u32 s92, s92, 65536\n" \
  "buffer_load_dwordx4 v8, s[68:71], s92 offen lds\n" \
  "s_add_u32 m0, s86, 136192\n" \
  "s_add_u32 s92, s92, 65536\n" \
  "buffer_load_dwordx4 v9, s[68:71], s92 offen lds\n" \
  "s_add_u32 m0, s86, 137216\n" \
  "s_add_u32 s92, s92, 65536\n" \
  "buffer_load_dwordx4 v8, s[68:71], s92 offen lds\n" \
  "s_add_u32 m0, s86, 138240\n" \
  "s_add_u32 s92, s92, 65536\n" \
  "buffer_load_dwordx4 v9, s[68:71], s92 offen lds\n" \
  "s_lshl_b32 s92, %7, 19\n" \
  "s_add_u32 s92, s92, 4194304\n" \
  "s_add_u32 m0, s86, 0\n" \
  "s_nop 0\n" \
  "buffer_load_dwordx4 v8, s[68:71], s92 offen lds\n" \
  "s_add_u32 m0, s86, 1024\n" \
  "s_add_u32 s92, s92, 65536\n" \
  "buffer_load_dwordx4 v9, s[68:71], s92 offen lds\n" \
  "s_add_u32 m0, s86, 2048\n" \
  "s_add_u32 s92, s92, 65536\n" \
  "buffer_load_dwordx4 v8, s[68:71], s92 offen lds\n" \
  "s_add_u32 m0, s86, 3072\n" \
  "s_add_u32 s92, s92, 65536\n" \
  "buffer_load_dwordx4 v9, s[68:71], s92 offen lds\n" \
  "s_add_u32 m0, s86, 4096\n" \
  "s_add_u32 s92, s92, 65536\n" \
  "buffer_load_dwordx4 v8, s[68:71], s92 offen lds\n" \
  "s_add_u32 m0, s86, 5120\n" \
  "s_add_u32 s92, s92, 65536\n" \
  "buffer_load_dwordx4 v9, s[68:71], s92 offen lds\n" \
  "s_add_u32 m0, s86, 6144\n" \
  "s_add_u32 s92, s92, 65536\n" \
  "buffer_load_dwordx4 v8, s[68:71], s92 offen lds\n" \
  "s_add_u32 m0, s86, 7168\n" \
  "s_add_u32 s92, s92, 65536\n" \
  "buffer_load_dwordx4 v9, s[68:71], s92 offen lds\n" \
  "s_lshl_b32 s92, %7, 19\n" \
  "s_add_u32 s92, s92, 6291456\n" \
  "s_add_u32 m0, s86, 32768\n" \
  "s_nop 0\n" \
  "buffer_load_dwordx4 v8, s[68:71], s92 offen lds\n" \
  "s_add_u32 m0, s86, 33792\n" \
  "s_add_u32 s92, s92, 65536\n" \
  "buffer_load_dwordx4 v9, s[68:71], s92 offen lds\n" \
  "s_add_u32 m0, s86, 34816\n" \
  "s_add_u32 s92, s92, 65536\n" \
  "buffer_load_dwordx4 v8, s[68:71], s92 offen lds\n" \
  "s_add_u32 m0, s86, 35840\n" \
  "s_add_u32 s92, s92, 65536\n" \
  "buffer_load_dwordx4 v9, s[68:71], s92 offen lds\n" \
  "s_add_u32 m0, s86, 36864\n" \
  "s_add_u32 s92, s92, 65536\n" \
  "buffer_load_dwordx4 v8, s[68:71], s92 offen lds\n" \
  "s_add_u32 m0, s86, 37888\n" \
  "s_add_u32 s92, s92, 65536\n" \
  "buffer_load_dwordx4 v9, s[68:71], s92 offen lds\n" \
  "s_add_u32 m0, s86, 38912\n" \
  "s_add_u32 s92, s92, 65536\n" \
  "buffer_load_dwordx4 v8, s[68:71], s92 offen lds\n" \
  "s_add_u32 m0, s86, 39936\n" \
  "s_add_u32 s92, s92, 65536\n" \
  "buffer_load_dwordx4 v9, s[68:71], s92 offen lds\n" \
  "s_mov_b64 s[72:73], %3\n" \
  "s_and_b32 s73, s73, 0xffff\n" \
  "s_mov_b32 s74, 4194304\n" \
  "s_mov_b32 s75, 0x00020000\n" \
  "s_mov_b64 s[76:77], %4\n" \
  "s_and_b32 s77, s77, 0xffff\n" \
  "s_mov_b32 s78, 3*" STR(QKV_SZ32) "\n" \
  "s_mov_b32 s79, 0x00020000\n" \
  "s_mov_b64 s[80:81], %5\n" \
  "s_and_b32 s81, s81, 0xffff\n" \
  "s_mov_b32 s82, " STR(CTRL_BYTES) "\n" \
  "s_mov_b32 s83, 0x00020000\n" \
  "v_xor_b32 v6, v6, v117\n" \
  "s_and_b32 s84, %7, 1\n" \
  "s_lshl_b32 s84, s84, 2\n" \
  "v_xor_b32 v6, s84, v6\n" \
  "v_lshlrev_b32 v7, 11, v5\n" \
  "v_lshl_add_u32 v7, v6, 4, v7\n" \
  "s_lshl_b32 s87, %7, 14\n" \
  "s_add_u32 s96, s87, 63*65536\n" \
  "v_lshrrev_b32 v5, 1, v116\n" \
  "v_xor_b32 v5, v5, v117\n" \
  "v_lshlrev_b32 v5, 4, v5\n" \
  "v_lshl_add_u32 v5, v116, 7, v5\n" \
  "v_add_u32 v10, %8, v5\n" \
  "v_xor_b32 v5, 64, v5\n" \
  "v_add_u32 v11, %8, v5\n" \
  "v_add_u32 v12, 0x10000, v10\n" \
  "v_add_u32 v13, 0x10000, v11\n" \
  "s_lshr_b32 s84, %6, 1\n" \
  "s_lshl_b32 s84, s84, 14\n" \
  "s_and_b32 s85, %6, 1\n" \
  "s_lshl_b32 s85, s85, 2\n" \
  "s_add_u32 s85, s85, %7\n" \
  "s_lshl_b32 s91, s85, 9\n" \
  "s_add_u32 s91, s91, s84\n" \
  "v_and_b32 v5, 1, v117\n" \
  "v_lshlrev_b32 v14, 12, v5\n" \
  "v_lshrrev_b32 v5, 1, v117\n" \
  "v_lshl_add_u32 v14, v5, 8, v14\n" \
  "v_lshl_add_u32 v14, v116, 4, v14\n" \
  "v_add_u32 v14, s91, v14\n" \
  "s_lshl_b32 s91, s85, 10\n" \
  "s_add_u32 s91, s91, s84\n" \
  "v_lshlrev_b32 v15, 4, v4\n" \
  "v_add_u32 v15, s91, v15\n" \
  "s_lshl_b32 s84, %6, 7\n" \
  "s_add_u32 s84, s84, " STR(CW_GPROG) "*4\n" \
  "v_or_b32 v5, %7, v4\n" \
  "v_lshl_add_u32 v16, v5, 24, s84\n" \
  "v_mov_b32 v17, 0\n" \
  "v_or_b32 v14, 0x80000000, v14\n" \
  "v_or_b32 v15, 0x80000000, v15\n" \
  "v_lshrrev_b32 v5, 2, v116\n" \
  "v_lshl_add_u32 v6, v117, 3, v5\n" \
  "v_lshlrev_b32 v6, 7, v6\n" \
  "v_lshrrev_b32 v5, 1, v5\n" \
  "v_and_b32 v119, 1, v117\n" \
  "v_lshl_add_u32 v5, v119, 1, v5\n" \
  "v_xor_b32 v5, %7, v5\n" \
  "v_lshl_add_u32 v6, v5, 5, v6\n" \
  "v_and_b32 v5, 3, v4\n" \
  "v_lshl_add_u32 v6, v5, 3, v6\n" \
  "v_add_u32 v118, %8, v6\n" \
  "v_add_u32 v119, 0x10000, v118\n" \
  "v_add_u32 v120, 0x20000, v118\n" \
  "s_lshl_b32 s84, %7, 10\n" \
  "s_add_u32 s91, s84, %8\n" \
  "s_waitcnt vmcnt(24) lgkmcnt(0)\n" \
  "s_barrier\n" \
  "s_mov_b64 s[68:69], %2\n" \
  "s_and_b32 s69, s69, 0xffff\n" \
  "s_mov_b32 s70, 8388608\n" \
  "s_mov_b32 s71, 0x00020000\n" \
  "s_lshl_b32 s92, %7, 19\n" \
  "s_add_u32 s92, s92, 0\n" \
  "s_add_u32 m0, s86, 65536\n" \
  "s_nop 0\n" \
  "buffer_load_dwordx4 v8, s[68:71], s92 offen lds\n" \
  "s_add_u32 m0, s86, 66560\n" \
  "s_add_u32 s92, s92, 65536\n" \
  "buffer_load_dwordx4 v9, s[68:71], s92 offen lds\n" \
  "s_add_u32 m0, s86, 67584\n" \
  "s_add_u32 s92, s92, 65536\n" \
  "buffer_load_dwordx4 v8, s[68:71], s92 offen lds\n" \
  "s_add_u32 m0, s86, 68608\n" \
  "s_add_u32 s92, s92, 65536\n" \
  "buffer_load_dwordx4 v9, s[68:71], s92 offen lds\n" \
  "s_add_u32 m0, s86, 69632\n" \
  "s_add_u32 s92, s92, 65536\n" \
  "buffer_load_dwordx4 v8, s[68:71], s92 offen lds\n" \
  "s_add_u32 m0, s86, 70656\n" \
  "s_add_u32 s92, s92, 65536\n" \
  "buffer_load_dwordx4 v9, s[68:71], s92 offen lds\n" \
  "s_add_u32 m0, s86, 71680\n" \
  "s_add_u32 s92, s92, 65536\n" \
  "buffer_load_dwordx4 v8, s[68:71], s92 offen lds\n" \
  "s_add_u32 m0, s86, 72704\n" \
  "s_add_u32 s92, s92, 65536\n" \
  "buffer_load_dwordx4 v9, s[68:71], s92 offen lds\n" \
  "ds_read_b64_tr_b16 a[0:1], v119 offset:32768\n" \
  "ds_read_b64_tr_b16 a[2:3], v119 offset:33280\n" \
  "ds_read_b64_tr_b16 a[4:5], v119 offset:36864\n" \
  "ds_read_b64_tr_b16 a[6:7], v119 offset:37376\n" \
  "ds_read_b64_tr_b16 a[8:9], v119 offset:40960\n" \
  "ds_read_b64_tr_b16 a[10:11], v119 offset:41472\n" \
  "ds_read_b64_tr_b16 a[12:13], v119 offset:45056\n" \
  "ds_read_b64_tr_b16 a[14:15], v119 offset:45568\n" \
  "ds_read_b64_tr_b16 a[16:17], v119 offset:49152\n" \
  "ds_read_b64_tr_b16 a[18:19], v119 offset:49664\n" \
  "ds_read_b64_tr_b16 a[20:21], v119 offset:53248\n" \
  "ds_read_b64_tr_b16 a[22:23], v119 offset:53760\n" \
  "ds_read_b64_tr_b16 a[24:25], v119 offset:57344\n" \
  "ds_read_b64_tr_b16 a[26:27], v119 offset:57856\n" \
  "ds_read_b64_tr_b16 a[28:29], v119 offset:61440\n" \
  "ds_read_b64_tr_b16 a[30:31], v119 offset:61952\n" \
  "s_waitcnt vmcnt(24) lgkmcnt(0)\n" \
  "s_barrier\n" \
  "s_lshl_b32 s92, %7, 19\n" \
  "s_add_u32 s92, s92, 2097152\n" \
  "s_add_u32 m0, s86, 98304\n" \
  "s_nop 0\n" \
  "buffer_load_dwordx4 v8, s[68:71], s92 offen lds\n" \
  "s_add_u32 m0, s86, 99328\n" \
  "s_add_u32 s92, s92, 65536\n" \
  "buffer_load_dwordx4 v9, s[68:71], s92 offen lds\n" \
  "s_add_u32 m0, s86, 100352\n" \
  "s_add_u32 s92, s92, 65536\n" \
  "buffer_load_dwordx4 v8, s[68:71], s92 offen lds\n" \
  "s_add_u32 m0, s86, 101376\n" \
  "s_add_u32 s92, s92, 65536\n" \
  "buffer_load_dwordx4 v9, s[68:71], s92 offen lds\n" \
  "s_add_u32 m0, s86, 102400\n" \
  "s_add_u32 s92, s92, 65536\n" \
  "buffer_load_dwordx4 v8, s[68:71], s92 offen lds\n" \
  "s_add_u32 m0, s86, 103424\n" \
  "s_add_u32 s92, s92, 65536\n" \
  "buffer_load_dwordx4 v9, s[68:71], s92 offen lds\n" \
  "s_add_u32 m0, s86, 104448\n" \
  "s_add_u32 s92, s92, 65536\n" \
  "buffer_load_dwordx4 v8, s[68:71], s92 offen lds\n" \
  "s_add_u32 m0, s86, 105472\n" \
  "s_add_u32 s92, s92, 65536\n" \
  "buffer_load_dwordx4 v9, s[68:71], s92 offen lds\n" \
  "ds_read_b64_tr_b16 a[32:33], v120 offset:0\n" \
  "ds_read_b64_tr_b16 a[34:35], v120 offset:512\n" \
  "ds_read_b64_tr_b16 a[36:37], v120 offset:4096\n" \
  "ds_read_b64_tr_b16 a[38:39], v120 offset:4608\n" \
  "ds_read_b64_tr_b16 a[40:41], v120 offset:8192\n" \
  "ds_read_b64_tr_b16 a[42:43], v120 offset:8704\n" \
  "ds_read_b64_tr_b16 a[44:45], v120 offset:12288\n" \
  "ds_read_b64_tr_b16 a[46:47], v120 offset:12800\n" \
  "ds_read_b64_tr_b16 a[48:49], v120 offset:16384\n" \
  "ds_read_b64_tr_b16 a[50:51], v120 offset:16896\n" \
  "ds_read_b64_tr_b16 a[52:53], v120 offset:20480\n" \
  "ds_read_b64_tr_b16 a[54:55], v120 offset:20992\n" \
  "ds_read_b64_tr_b16 a[56:57], v120 offset:24576\n" \
  "ds_read_b64_tr_b16 a[58:59], v120 offset:25088\n" \
  "ds_read_b64_tr_b16 a[60:61], v120 offset:28672\n" \
  "ds_read_b64_tr_b16 a[62:63], v120 offset:29184\n" \
  "s_waitcnt vmcnt(24) lgkmcnt(0)\n" \
  "s_barrier\n" \
  "s_lshl_b32 s92, %7, 19\n" \
  "s_add_u32 s92, s92, 4194304\n" \
  "s_add_u32 m0, s86, 131072\n" \
  "s_nop 0\n" \
  "buffer_load_dwordx4 v8, s[68:71], s92 offen lds\n" \
  "s_add_u32 m0, s86, 132096\n" \
  "s_add_u32 s92, s92, 65536\n" \
  "buffer_load_dwordx4 v9, s[68:71], s92 offen lds\n" \
  "s_add_u32 m0, s86, 133120\n" \
  "s_add_u32 s92, s92, 65536\n" \
  "buffer_load_dwordx4 v8, s[68:71], s92 offen lds\n" \
  "s_add_u32 m0, s86, 134144\n" \
  "s_add_u32 s92, s92, 65536\n" \
  "buffer_load_dwordx4 v9, s[68:71], s92 offen lds\n" \
  "s_add_u32 m0, s86, 135168\n" \
  "s_add_u32 s92, s92, 65536\n" \
  "buffer_load_dwordx4 v8, s[68:71], s92 offen lds\n" \
  "s_add_u32 m0, s86, 136192\n" \
  "s_add_u32 s92, s92, 65536\n" \
  "buffer_load_dwordx4 v9, s[68:71], s92 offen lds\n" \
  "s_add_u32 m0, s86, 137216\n" \
  "s_add_u32 s92, s92, 65536\n" \
  "buffer_load_dwordx4 v8, s[68:71], s92 offen lds\n" \
  "s_add_u32 m0, s86, 138240\n" \
  "s_add_u32 s92, s92, 65536\n" \
  "buffer_load_dwordx4 v9, s[68:71], s92 offen lds\n" \
  "ds_read_b64_tr_b16 a[64:65], v118 offset:0\n" \
  "ds_read_b64_tr_b16 a[66:67], v118 offset:512\n" \
  "ds_read_b64_tr_b16 a[68:69], v118 offset:4096\n" \
  "ds_read_b64_tr_b16 a[70:71], v118 offset:4608\n" \
  "ds_read_b64_tr_b16 a[72:73], v118 offset:8192\n" \
  "ds_read_b64_tr_b16 a[74:75], v118 offset:8704\n" \
  "ds_read_b64_tr_b16 a[76:77], v118 offset:12288\n" \
  "ds_read_b64_tr_b16 a[78:79], v118 offset:12800\n" \
  "ds_read_b64_tr_b16 a[80:81], v118 offset:16384\n" \
  "ds_read_b64_tr_b16 a[82:83], v118 offset:16896\n" \
  "ds_read_b64_tr_b16 a[84:85], v118 offset:20480\n" \
  "ds_read_b64_tr_b16 a[86:87], v118 offset:20992\n" \
  "ds_read_b64_tr_b16 a[88:89], v118 offset:24576\n" \
  "ds_read_b64_tr_b16 a[90:91], v118 offset:25088\n" \
  "ds_read_b64_tr_b16 a[92:93], v118 offset:28672\n" \
  "ds_read_b64_tr_b16 a[94:95], v118 offset:29184\n" \
  "s_waitcnt vmcnt(24) lgkmcnt(0)\n" \
  "s_barrier\n" \
  "s_lshl_b32 s92, %7, 19\n" \
  "s_add_u32 s92, s92, 6291456\n" \
  "s_add_u32 m0, s86, 0\n" \
  "s_nop 0\n" \
  "buffer_load_dwordx4 v8, s[68:71], s92 offen lds\n" \
  "s_add_u32 m0, s86, 1024\n" \
  "s_add_u32 s92, s92, 65536\n" \
  "buffer_load_dwordx4 v9, s[68:71], s92 offen lds\n" \
  "s_add_u32 m0, s86, 2048\n" \
  "s_add_u32 s92, s92, 65536\n" \
  "buffer_load_dwordx4 v8, s[68:71], s92 offen lds\n" \
  "s_add_u32 m0, s86, 3072\n" \
  "s_add_u32 s92, s92, 65536\n" \
  "buffer_load_dwordx4 v9, s[68:71], s92 offen lds\n" \
  "s_add_u32 m0, s86, 4096\n" \
  "s_add_u32 s92, s92, 65536\n" \
  "buffer_load_dwordx4 v8, s[68:71], s92 offen lds\n" \
  "s_add_u32 m0, s86, 5120\n" \
  "s_add_u32 s92, s92, 65536\n" \
  "buffer_load_dwordx4 v9, s[68:71], s92 offen lds\n" \
  "s_add_u32 m0, s86, 6144\n" \
  "s_add_u32 s92, s92, 65536\n" \
  "buffer_load_dwordx4 v8, s[68:71], s92 offen lds\n" \
  "s_add_u32 m0, s86, 7168\n" \
  "s_add_u32 s92, s92, 65536\n" \
  "buffer_load_dwordx4 v9, s[68:71], s92 offen lds\n" \
  "ds_read_b64_tr_b16 a[96:97], v118 offset:32768\n" \
  "ds_read_b64_tr_b16 a[98:99], v118 offset:33280\n" \
  "ds_read_b64_tr_b16 a[100:101], v118 offset:36864\n" \
  "ds_read_b64_tr_b16 a[102:103], v118 offset:37376\n" \
  "ds_read_b64_tr_b16 a[104:105], v118 offset:40960\n" \
  "ds_read_b64_tr_b16 a[106:107], v118 offset:41472\n" \
  "ds_read_b64_tr_b16 a[108:109], v118 offset:45056\n" \
  "ds_read_b64_tr_b16 a[110:111], v118 offset:45568\n" \
  "ds_read_b64_tr_b16 a[112:113], v118 offset:49152\n" \
  "ds_read_b64_tr_b16 a[114:115], v118 offset:49664\n" \
  "ds_read_b64_tr_b16 a[116:117], v118 offset:53248\n" \
  "ds_read_b64_tr_b16 a[118:119], v118 offset:53760\n" \
  "ds_read_b64_tr_b16 a[120:121], v118 offset:57344\n" \
  "ds_read_b64_tr_b16 a[122:123], v118 offset:57856\n" \
  "ds_read_b64_tr_b16 a[124:125], v118 offset:61440\n" \
  "ds_read_b64_tr_b16 a[126:127], v118 offset:61952\n" \
  "s_waitcnt vmcnt(24) lgkmcnt(0)\n" \
  "s_barrier\n" \
  "s_mov_b64 s[68:69], %0\n" \
  "s_and_b32 s69, s69, 0xffff\n" \
  "s_mov_b32 s70, 8388608\n" \
  "s_mov_b32 s71, 0x00020000\n" \
  "s_lshl_b32 s92, %7, 19\n" \
  "s_add_u32 s92, s92, 0\n" \
  "s_add_u32 m0, s86, 32768\n" \
  "s_nop 0\n" \
  "buffer_load_dwordx4 v8, s[68:71], s92 offen lds\n" \
  "s_add_u32 m0, s86, 33792\n" \
  "s_add_u32 s92, s92, 65536\n" \
  "buffer_load_dwordx4 v9, s[68:71], s92 offen lds\n" \
  "s_add_u32 m0, s86, 34816\n" \
  "s_add_u32 s92, s92, 65536\n" \
  "buffer_load_dwordx4 v8, s[68:71], s92 offen lds\n" \
  "s_add_u32 m0, s86, 35840\n" \
  "s_add_u32 s92, s92, 65536\n" \
  "buffer_load_dwordx4 v9, s[68:71], s92 offen lds\n" \
  "s_add_u32 m0, s86, 36864\n" \
  "s_add_u32 s92, s92, 65536\n" \
  "buffer_load_dwordx4 v8, s[68:71], s92 offen lds\n" \
  "s_add_u32 m0, s86, 37888\n" \
  "s_add_u32 s92, s92, 65536\n" \
  "buffer_load_dwordx4 v9, s[68:71], s92 offen lds\n" \
  "s_add_u32 m0, s86, 38912\n" \
  "s_add_u32 s92, s92, 65536\n" \
  "buffer_load_dwordx4 v8, s[68:71], s92 offen lds\n" \
  "s_add_u32 m0, s86, 39936\n" \
  "s_add_u32 s92, s92, 65536\n" \
  "buffer_load_dwordx4 v9, s[68:71], s92 offen lds\n" \
  "ds_read_b64_tr_b16 a[128:129], v119 offset:0\n" \
  "ds_read_b64_tr_b16 a[130:131], v119 offset:512\n" \
  "ds_read_b64_tr_b16 a[132:133], v119 offset:4096\n" \
  "ds_read_b64_tr_b16 a[134:135], v119 offset:4608\n" \
  "ds_read_b64_tr_b16 a[136:137], v119 offset:8192\n" \
  "ds_read_b64_tr_b16 a[138:139], v119 offset:8704\n" \
  "ds_read_b64_tr_b16 a[140:141], v119 offset:12288\n" \
  "ds_read_b64_tr_b16 a[142:143], v119 offset:12800\n" \
  "ds_read_b64_tr_b16 a[144:145], v119 offset:16384\n" \
  "ds_read_b64_tr_b16 a[146:147], v119 offset:16896\n" \
  "ds_read_b64_tr_b16 a[148:149], v119 offset:20480\n" \
  "ds_read_b64_tr_b16 a[150:151], v119 offset:20992\n" \
  "ds_read_b64_tr_b16 a[152:153], v119 offset:24576\n" \
  "ds_read_b64_tr_b16 a[154:155], v119 offset:25088\n" \
  "ds_read_b64_tr_b16 a[156:157], v119 offset:28672\n" \
  "ds_read_b64_tr_b16 a[158:159], v119 offset:29184\n" \
  "s_waitcnt vmcnt(24) lgkmcnt(0)\n" \
  "s_barrier\n" \
  "s_lshl_b32 s92, %7, 19\n" \
  "s_add_u32 s92, s92, 2097152\n" \
  "s_add_u32 m0, s86, 65536\n" \
  "s_nop 0\n" \
  "buffer_load_dwordx4 v8, s[68:71], s92 offen lds\n" \
  "s_add_u32 m0, s86, 66560\n" \
  "s_add_u32 s92, s92, 65536\n" \
  "buffer_load_dwordx4 v9, s[68:71], s92 offen lds\n" \
  "s_add_u32 m0, s86, 67584\n" \
  "s_add_u32 s92, s92, 65536\n" \
  "buffer_load_dwordx4 v8, s[68:71], s92 offen lds\n" \
  "s_add_u32 m0, s86, 68608\n" \
  "s_add_u32 s92, s92, 65536\n" \
  "buffer_load_dwordx4 v9, s[68:71], s92 offen lds\n" \
  "s_add_u32 m0, s86, 69632\n" \
  "s_add_u32 s92, s92, 65536\n" \
  "buffer_load_dwordx4 v8, s[68:71], s92 offen lds\n" \
  "s_add_u32 m0, s86, 70656\n" \
  "s_add_u32 s92, s92, 65536\n" \
  "buffer_load_dwordx4 v9, s[68:71], s92 offen lds\n" \
  "s_add_u32 m0, s86, 71680\n" \
  "s_add_u32 s92, s92, 65536\n" \
  "buffer_load_dwordx4 v8, s[68:71], s92 offen lds\n" \
  "s_add_u32 m0, s86, 72704\n" \
  "s_add_u32 s92, s92, 65536\n" \
  "buffer_load_dwordx4 v9, s[68:71], s92 offen lds\n" \
  "ds_read_b64_tr_b16 a[160:161], v119 offset:32768\n" \
  "ds_read_b64_tr_b16 a[162:163], v119 offset:33280\n" \
  "ds_read_b64_tr_b16 a[164:165], v119 offset:36864\n" \
  "ds_read_b64_tr_b16 a[166:167], v119 offset:37376\n" \
  "ds_read_b64_tr_b16 a[168:169], v119 offset:40960\n" \
  "ds_read_b64_tr_b16 a[170:171], v119 offset:41472\n" \
  "ds_read_b64_tr_b16 a[172:173], v119 offset:45056\n" \
  "ds_read_b64_tr_b16 a[174:175], v119 offset:45568\n" \
  "ds_read_b64_tr_b16 a[176:177], v119 offset:49152\n" \
  "ds_read_b64_tr_b16 a[178:179], v119 offset:49664\n" \
  "ds_read_b64_tr_b16 a[180:181], v119 offset:53248\n" \
  "ds_read_b64_tr_b16 a[182:183], v119 offset:53760\n" \
  "ds_read_b64_tr_b16 a[184:185], v119 offset:57344\n" \
  "ds_read_b64_tr_b16 a[186:187], v119 offset:57856\n" \
  "ds_read_b64_tr_b16 a[188:189], v119 offset:61440\n" \
  "ds_read_b64_tr_b16 a[190:191], v119 offset:61952\n" \
  "s_waitcnt vmcnt(24) lgkmcnt(0)\n" \
  "s_barrier\n" \
  "s_lshl_b32 s92, %7, 19\n" \
  "s_add_u32 s92, s92, 4194304\n" \
  "s_add_u32 m0, s86, 98304\n" \
  "s_nop 0\n" \
  "buffer_load_dwordx4 v8, s[68:71], s92 offen lds\n" \
  "s_add_u32 m0, s86, 99328\n" \
  "s_add_u32 s92, s92, 65536\n" \
  "buffer_load_dwordx4 v9, s[68:71], s92 offen lds\n" \
  "s_add_u32 m0, s86, 100352\n" \
  "s_add_u32 s92, s92, 65536\n" \
  "buffer_load_dwordx4 v8, s[68:71], s92 offen lds\n" \
  "s_add_u32 m0, s86, 101376\n" \
  "s_add_u32 s92, s92, 65536\n" \
  "buffer_load_dwordx4 v9, s[68:71], s92 offen lds\n" \
  "s_add_u32 m0, s86, 102400\n" \
  "s_add_u32 s92, s92, 65536\n" \
  "buffer_load_dwordx4 v8, s[68:71], s92 offen lds\n" \
  "s_add_u32 m0, s86, 103424\n" \
  "s_add_u32 s92, s92, 65536\n" \
  "buffer_load_dwordx4 v9, s[68:71], s92 offen lds\n" \
  "s_add_u32 m0, s86, 104448\n" \
  "s_add_u32 s92, s92, 65536\n" \
  "buffer_load_dwordx4 v8, s[68:71], s92 offen lds\n" \
  "s_add_u32 m0, s86, 105472\n" \
  "s_add_u32 s92, s92, 65536\n" \
  "buffer_load_dwordx4 v9, s[68:71], s92 offen lds\n" \
  "ds_read_b64_tr_b16 a[192:193], v120 offset:0\n" \
  "ds_read_b64_tr_b16 a[194:195], v120 offset:512\n" \
  "ds_read_b64_tr_b16 a[196:197], v120 offset:4096\n" \
  "ds_read_b64_tr_b16 a[198:199], v120 offset:4608\n" \
  "ds_read_b64_tr_b16 a[200:201], v120 offset:8192\n" \
  "ds_read_b64_tr_b16 a[202:203], v120 offset:8704\n" \
  "ds_read_b64_tr_b16 a[204:205], v120 offset:12288\n" \
  "ds_read_b64_tr_b16 a[206:207], v120 offset:12800\n" \
  "ds_read_b64_tr_b16 a[208:209], v120 offset:16384\n" \
  "ds_read_b64_tr_b16 a[210:211], v120 offset:16896\n" \
  "ds_read_b64_tr_b16 a[212:213], v120 offset:20480\n" \
  "ds_read_b64_tr_b16 a[214:215], v120 offset:20992\n" \
  "ds_read_b64_tr_b16 a[216:217], v120 offset:24576\n" \
  "ds_read_b64_tr_b16 a[218:219], v120 offset:25088\n" \
  "ds_read_b64_tr_b16 a[220:221], v120 offset:28672\n" \
  "ds_read_b64_tr_b16 a[222:223], v120 offset:29184\n" \
  "s_waitcnt vmcnt(24) lgkmcnt(0)\n" \
  "s_barrier\n" \
  "s_lshl_b32 s92, %7, 19\n" \
  "s_add_u32 s92, s92, 6291456\n" \
  "s_add_u32 m0, s86, 131072\n" \
  "s_nop 0\n" \
  "buffer_load_dwordx4 v8, s[68:71], s92 offen lds\n" \
  "s_add_u32 m0, s86, 132096\n" \
  "s_add_u32 s92, s92, 65536\n" \
  "buffer_load_dwordx4 v9, s[68:71], s92 offen lds\n" \
  "s_add_u32 m0, s86, 133120\n" \
  "s_add_u32 s92, s92, 65536\n" \
  "buffer_load_dwordx4 v8, s[68:71], s92 offen lds\n" \
  "s_add_u32 m0, s86, 134144\n" \
  "s_add_u32 s92, s92, 65536\n" \
  "buffer_load_dwordx4 v9, s[68:71], s92 offen lds\n" \
  "s_add_u32 m0, s86, 135168\n" \
  "s_add_u32 s92, s92, 65536\n" \
  "buffer_load_dwordx4 v8, s[68:71], s92 offen lds\n" \
  "s_add_u32 m0, s86, 136192\n" \
  "s_add_u32 s92, s92, 65536\n" \
  "buffer_load_dwordx4 v9, s[68:71], s92 offen lds\n" \
  "s_add_u32 m0, s86, 137216\n" \
  "s_add_u32 s92, s92, 65536\n" \
  "buffer_load_dwordx4 v8, s[68:71], s92 offen lds\n" \
  "s_add_u32 m0, s86, 138240\n" \
  "s_add_u32 s92, s92, 65536\n" \
  "buffer_load_dwordx4 v9, s[68:71], s92 offen lds\n" \
  "ds_read_b64_tr_b16 a[224:225], v118 offset:0\n" \
  "ds_read_b64_tr_b16 a[226:227], v118 offset:512\n" \
  "ds_read_b64_tr_b16 a[228:229], v118 offset:4096\n" \
  "ds_read_b64_tr_b16 a[230:231], v118 offset:4608\n" \
  "ds_read_b64_tr_b16 a[232:233], v118 offset:8192\n" \
  "ds_read_b64_tr_b16 a[234:235], v118 offset:8704\n" \
  "ds_read_b64_tr_b16 a[236:237], v118 offset:12288\n" \
  "ds_read_b64_tr_b16 a[238:239], v118 offset:12800\n" \
  "ds_read_b64_tr_b16 a[240:241], v118 offset:16384\n" \
  "ds_read_b64_tr_b16 a[242:243], v118 offset:16896\n" \
  "ds_read_b64_tr_b16 a[244:245], v118 offset:20480\n" \
  "ds_read_b64_tr_b16 a[246:247], v118 offset:20992\n" \
  "ds_read_b64_tr_b16 a[248:249], v118 offset:24576\n" \
  "ds_read_b64_tr_b16 a[250:251], v118 offset:25088\n" \
  "ds_read_b64_tr_b16 a[252:253], v118 offset:28672\n" \
  "ds_read_b64_tr_b16 a[254:255], v118 offset:29184\n" \
  "s_waitcnt vmcnt(24) lgkmcnt(0)\n" \
  "s_barrier\n" \
  "s_add_u32 m0, s91, 0\n" \
  "s_nop 0\n" \
  "buffer_load_dwordx4 v7, s[72:75], s87 offen lds\n" \
  "s_add_u32 s87, s87, 128\n" \
  "s_add_u32 m0, s91, 4096\n" \
  "s_nop 0\n" \
  "buffer_load_dwordx4 v7, s[72:75], s87 offen lds\n" \
  "s_add_u32 s87, s87, 128\n" \
  "s_add_u32 m0, s91, 8192\n" \
  "s_nop 0\n" \
  "buffer_load_dwordx4 v7, s[72:75], s87 offen lds\n" \
  "s_add_u32 s87, s87, 128\n" \
  "s_add_u32 m0, s91, 12288\n" \
  "s_nop 0\n" \
  "buffer_load_dwordx4 v7, s[72:75], s87 offen lds\n" \
  "s_add_u32 s87, s87, 128\n" \
  "s_add_u32 m0, s91, 16384\n" \
  "s_nop 0\n" \
  "buffer_load_dwordx4 v7, s[72:75], s87 offen lds\n" \
  "s_add_u32 s87, s87, 128\n" \
  "s_add_u32 m0, s91, 20480\n" \
  "s_nop 0\n" \
  "buffer_load_dwordx4 v7, s[72:75], s87 offen lds\n" \
  "s_add_u32 s87, s87, 128\n" \
  "s_add_u32 m0, s91, 24576\n" \
  "s_nop 0\n" \
  "buffer_load_dwordx4 v7, s[72:75], s87 offen lds\n" \
  "s_add_u32 s87, s87, 128\n" \
  "s_add_u32 m0, s91, 28672\n" \
  "s_nop 0\n" \
  "buffer_load_dwordx4 v7, s[72:75], s87 offen lds\n" \
  "s_add_u32 s87, s87, 128\n" \
  "ds_read_b64_tr_b16 v[128:129], v118 offset:32768\n" \
  "ds_read_b64_tr_b16 v[130:131], v118 offset:33280\n" \
  "ds_read_b64_tr_b16 v[132:133], v118 offset:36864\n" \
  "ds_read_b64_tr_b16 v[134:135], v118 offset:37376\n" \
  "ds_read_b64_tr_b16 v[136:137], v118 offset:40960\n" \
  "ds_read_b64_tr_b16 v[138:139], v118 offset:41472\n" \
  "ds_read_b64_tr_b16 v[140:141], v118 offset:45056\n" \
  "ds_read_b64_tr_b16 v[142:143], v118 offset:45568\n" \
  "ds_read_b64_tr_b16 v[144:145], v118 offset:49152\n" \
  "ds_read_b64_tr_b16 v[146:147], v118 offset:49664\n" \
  "ds_read_b64_tr_b16 v[148:149], v118 offset:53248\n" \
  "ds_read_b64_tr_b16 v[150:151], v118 offset:53760\n" \
  "ds_read_b64_tr_b16 v[152:153], v118 offset:57344\n" \
  "ds_read_b64_tr_b16 v[154:155], v118 offset:57856\n" \
  "ds_read_b64_tr_b16 v[156:157], v118 offset:61440\n" \
  "ds_read_b64_tr_b16 v[158:159], v118 offset:61952\n" \
  "s_waitcnt vmcnt(24) lgkmcnt(0)\n" \
  "s_barrier\n" \
  "s_add_u32 m0, s91, 32768\n" \
  "s_nop 0\n" \
  "buffer_load_dwordx4 v7, s[72:75], s87 offen lds\n" \
  "s_add_u32 s87, s87, 128\n" \
  "s_add_u32 m0, s91, 36864\n" \
  "s_nop 0\n" \
  "buffer_load_dwordx4 v7, s[72:75], s87 offen lds\n" \
  "s_add_u32 s87, s87, 128\n" \
  "s_add_u32 m0, s91, 40960\n" \
  "s_nop 0\n" \
  "buffer_load_dwordx4 v7, s[72:75], s87 offen lds\n" \
  "s_add_u32 s87, s87, 128\n" \
  "s_add_u32 m0, s91, 45056\n" \
  "s_nop 0\n" \
  "buffer_load_dwordx4 v7, s[72:75], s87 offen lds\n" \
  "s_add_u32 s87, s87, 128\n" \
  "s_add_u32 m0, s91, 49152\n" \
  "s_nop 0\n" \
  "buffer_load_dwordx4 v7, s[72:75], s87 offen lds\n" \
  "s_add_u32 s87, s87, 128\n" \
  "s_add_u32 m0, s91, 53248\n" \
  "s_nop 0\n" \
  "buffer_load_dwordx4 v7, s[72:75], s87 offen lds\n" \
  "s_add_u32 s87, s87, 128\n" \
  "s_add_u32 m0, s91, 57344\n" \
  "s_nop 0\n" \
  "buffer_load_dwordx4 v7, s[72:75], s87 offen lds\n" \
  "s_add_u32 s87, s87, 128\n" \
  "s_add_u32 m0, s91, 61440\n" \
  "s_nop 0\n" \
  "buffer_load_dwordx4 v7, s[72:75], s87 offen lds\n" \
  "s_add_u32 s87, s87, 63616\n" \
  "s_min_u32 s87, s87, s96\n" \
  "ds_read_b64_tr_b16 v[160:161], v119 offset:0\n" \
  "ds_read_b64_tr_b16 v[162:163], v119 offset:512\n" \
  "ds_read_b64_tr_b16 v[164:165], v119 offset:4096\n" \
  "ds_read_b64_tr_b16 v[166:167], v119 offset:4608\n" \
  "ds_read_b64_tr_b16 v[168:169], v119 offset:8192\n" \
  "ds_read_b64_tr_b16 v[170:171], v119 offset:8704\n" \
  "ds_read_b64_tr_b16 v[172:173], v119 offset:12288\n" \
  "ds_read_b64_tr_b16 v[174:175], v119 offset:12800\n" \
  "ds_read_b64_tr_b16 v[176:177], v119 offset:16384\n" \
  "ds_read_b64_tr_b16 v[178:179], v119 offset:16896\n" \
  "ds_read_b64_tr_b16 v[180:181], v119 offset:20480\n" \
  "ds_read_b64_tr_b16 v[182:183], v119 offset:20992\n" \
  "ds_read_b64_tr_b16 v[184:185], v119 offset:24576\n" \
  "ds_read_b64_tr_b16 v[186:187], v119 offset:25088\n" \
  "ds_read_b64_tr_b16 v[188:189], v119 offset:28672\n" \
  "ds_read_b64_tr_b16 v[190:191], v119 offset:29184\n" \
  "s_waitcnt vmcnt(24) lgkmcnt(0)\n" \
  "s_barrier\n" \
  "s_add_u32 m0, s91, 65536\n" \
  "s_nop 0\n" \
  "buffer_load_dwordx4 v7, s[72:75], s87 offen lds\n" \
  "s_add_u32 s87, s87, 128\n" \
  "s_add_u32 m0, s91, 69632\n" \
  "s_nop 0\n" \
  "buffer_load_dwordx4 v7, s[72:75], s87 offen lds\n" \
  "s_add_u32 s87, s87, 128\n" \
  "s_add_u32 m0, s91, 73728\n" \
  "s_nop 0\n" \
  "buffer_load_dwordx4 v7, s[72:75], s87 offen lds\n" \
  "s_add_u32 s87, s87, 128\n" \
  "s_add_u32 m0, s91, 77824\n" \
  "s_nop 0\n" \
  "buffer_load_dwordx4 v7, s[72:75], s87 offen lds\n" \
  "s_add_u32 s87, s87, 128\n" \
  "s_add_u32 m0, s91, 81920\n" \
  "s_nop 0\n" \
  "buffer_load_dwordx4 v7, s[72:75], s87 offen lds\n" \
  "s_add_u32 s87, s87, 128\n" \
  "s_add_u32 m0, s91, 86016\n" \
  "s_nop 0\n" \
  "buffer_load_dwordx4 v7, s[72:75], s87 offen lds\n" \
  "s_add_u32 s87, s87, 128\n" \
  "s_add_u32 m0, s91, 90112\n" \
  "s_nop 0\n" \
  "buffer_load_dwordx4 v7, s[72:75], s87 offen lds\n" \
  "s_add_u32 s87, s87, 128\n" \
  "s_add_u32 m0, s91, 94208\n" \
  "s_nop 0\n" \
  "buffer_load_dwordx4 v7, s[72:75], s87 offen lds\n" \
  "s_add_u32 s87, s87, 128\n" \
  "ds_read_b64_tr_b16 v[192:193], v119 offset:32768\n" \
  "ds_read_b64_tr_b16 v[194:195], v119 offset:33280\n" \
  "ds_read_b64_tr_b16 v[196:197], v119 offset:36864\n" \
  "ds_read_b64_tr_b16 v[198:199], v119 offset:37376\n" \
  "ds_read_b64_tr_b16 v[200:201], v119 offset:40960\n" \
  "ds_read_b64_tr_b16 v[202:203], v119 offset:41472\n" \
  "ds_read_b64_tr_b16 v[204:205], v119 offset:45056\n" \
  "ds_read_b64_tr_b16 v[206:207], v119 offset:45568\n" \
  "ds_read_b64_tr_b16 v[208:209], v119 offset:49152\n" \
  "ds_read_b64_tr_b16 v[210:211], v119 offset:49664\n" \
  "ds_read_b64_tr_b16 v[212:213], v119 offset:53248\n" \
  "ds_read_b64_tr_b16 v[214:215], v119 offset:53760\n" \
  "ds_read_b64_tr_b16 v[216:217], v119 offset:57344\n" \
  "ds_read_b64_tr_b16 v[218:219], v119 offset:57856\n" \
  "ds_read_b64_tr_b16 v[220:221], v119 offset:61440\n" \
  "ds_read_b64_tr_b16 v[222:223], v119 offset:61952\n" \
  "s_waitcnt vmcnt(24) lgkmcnt(0)\n" \
  "s_barrier\n" \
  "ds_read_b64_tr_b16 v[224:225], v120 offset:0\n" \
  "ds_read_b64_tr_b16 v[226:227], v120 offset:512\n" \
  "ds_read_b64_tr_b16 v[228:229], v120 offset:4096\n" \
  "ds_read_b64_tr_b16 v[230:231], v120 offset:4608\n" \
  "ds_read_b64_tr_b16 v[232:233], v120 offset:8192\n" \
  "ds_read_b64_tr_b16 v[234:235], v120 offset:8704\n" \
  "ds_read_b64_tr_b16 v[236:237], v120 offset:12288\n" \
  "ds_read_b64_tr_b16 v[238:239], v120 offset:12800\n" \
  "ds_read_b64_tr_b16 v[240:241], v120 offset:16384\n" \
  "ds_read_b64_tr_b16 v[242:243], v120 offset:16896\n" \
  "ds_read_b64_tr_b16 v[244:245], v120 offset:20480\n" \
  "ds_read_b64_tr_b16 v[246:247], v120 offset:20992\n" \
  "ds_read_b64_tr_b16 v[248:249], v120 offset:24576\n" \
  "ds_read_b64_tr_b16 v[250:251], v120 offset:25088\n" \
  "ds_read_b64_tr_b16 v[252:253], v120 offset:28672\n" \
  "ds_read_b64_tr_b16 v[254:255], v120 offset:29184\n" \
  "s_waitcnt vmcnt(8) lgkmcnt(0)\n" \
  "s_barrier\n" \
  "ds_read_b128 v[68:71], v10 offset:0\n" \
  "ds_read_b128 v[72:75], v10 offset:2048\n" \
  "ds_read_b128 v[76:79], v11 offset:0\n" \
  "ds_read_b128 v[80:83], v11 offset:2048\n" \
  "ds_read_b128 v[84:87], v10 offset:4096\n" \
  "ds_read_b128 v[88:91], v10 offset:6144\n" \
  "s_mov_b32 s85, 0\n" \
  ".p2align 6\n" \
  "LG_%=:\n" \
  "s_cmp_eq_u32 s85, 64\n" \
  "s_cbranch_scc1 LX0_%=\n" \
  "s_waitcnt vmcnt(8)\n" \
  "s_barrier\n" \
  "s_waitcnt lgkmcnt(4)\n" \
  "v_mfma_f32_16x16x32_bf16 v[20:23], v[128:131], v[68:71], 0\n" \
  "ds_read_b128 v[92:95], v11 offset:4096\n" \
  "s_sub_u32 s89, s85, 1\n" \
  "s_and_b32 s89, s89, 63\n" \
  "v_mfma_f32_16x16x32_bf16 v[28:31], a[0:3], v[68:71], 0\n" \
  "ds_read_b128 v[96:99], v11 offset:6144\n" \
  "s_lshr_b32 s84, s89, 2\n" \
  "s_xnor_b32 s84, s84, %9\n" \
  "v_mfma_f32_16x16x32_bf16 v[36:39], v[68:71], a[128:131], 0\n" \
  "s_and_b32 s84, s84, 1\n" \
  "s_lshl_b32 s84, s84, 13\n" \
  "v_mfma_f32_16x16x32_bf16 v[24:27], v[128:131], v[72:75], 0\n" \
  "s_lshl_b32 s88, s89, 19\n" \
  "s_or_b32 s88, s88, s84\n" \
  "v_mfma_f32_16x16x32_bf16 v[32:35], a[0:3], v[72:75], 0\n" \
  "s_add_u32 s89, s88, " STR(QKV_SZ32) "\n" \
  "s_add_u32 s90, s89, " STR(QKV_SZ32) "\n" \
  "v_mfma_f32_16x16x32_bf16 v[40:43], v[72:75], a[128:131], 0\n" \
  "s_add_u32 s85, s85, 1\n" \
  "s_waitcnt lgkmcnt(4)\n" \
  "v_mfma_f32_16x16x32_bf16 v[20:23], v[132:135], v[76:79], v[20:23]\n" \
  "ds_read_b128 v[68:71], v10 offset:8192\n" \
  "v_cvt_pk_bf16_f32 v116, v44, v45\n" \
  "v_mfma_f32_16x16x32_bf16 v[28:31], a[4:7], v[76:79], v[28:31]\n" \
  "ds_read_b128 v[72:75], v10 offset:10240\n" \
  "v_cvt_pk_bf16_f32 v117, v46, v47\n" \
  "v_mfma_f32_16x16x32_bf16 v[36:39], v[76:79], a[132:135], v[36:39]\n" \
  "v_cvt_pk_bf16_f32 v118, v48, v49\n" \
  "v_mfma_f32_16x16x32_bf16 v[24:27], v[132:135], v[80:83], v[24:27]\n" \
  "v_cvt_pk_bf16_f32 v119, v50, v51\n" \
  "v_mfma_f32_16x16x32_bf16 v[32:35], a[4:7], v[80:83], v[32:35]\n" \
  "v_cvt_pk_bf16_f32 v120, v52, v53\n" \
  "v_mfma_f32_16x16x32_bf16 v[40:43], v[80:83], a[132:135], v[40:43]\n" \
  "v_cvt_pk_bf16_f32 v121, v54, v55\n" \
  "s_waitcnt lgkmcnt(4)\n" \
  "v_mfma_f32_16x16x32_bf16 v[20:23], v[136:139], v[84:87], v[20:23]\n" \
  "ds_read_b128 v[76:79], v11 offset:8192\n" \
  "v_cvt_pk_bf16_f32 v122, v56, v57\n" \
  "v_mfma_f32_16x16x32_bf16 v[28:31], a[8:11], v[84:87], v[28:31]\n" \
  "ds_read_b128 v[80:83], v11 offset:10240\n" \
  "v_cvt_pk_bf16_f32 v123, v58, v59\n" \
  "v_mfma_f32_16x16x32_bf16 v[36:39], v[84:87], a[136:139], v[36:39]\n" \
  "v_cvt_pk_bf16_f32 v124, v60, v61\n" \
  "v_mfma_f32_16x16x32_bf16 v[24:27], v[136:139], v[88:91], v[24:27]\n" \
  "v_cvt_pk_bf16_f32 v125, v62, v63\n" \
  "v_mfma_f32_16x16x32_bf16 v[32:35], a[8:11], v[88:91], v[32:35]\n" \
  "v_cvt_pk_bf16_f32 v126, v64, v65\n" \
  "v_mfma_f32_16x16x32_bf16 v[40:43], v[88:91], a[136:139], v[40:43]\n" \
  "v_cvt_pk_bf16_f32 v127, v66, v67\n" \
  "s_waitcnt lgkmcnt(4)\n" \
  "v_mfma_f32_16x16x32_bf16 v[20:23], v[140:143], v[92:95], v[20:23]\n" \
  "ds_read_b128 v[84:87], v10 offset:12288\n" \
  "v_permlane16_swap_b32 v116, v118\n" \
  "v_mfma_f32_16x16x32_bf16 v[28:31], a[12:15], v[92:95], v[28:31]\n" \
  "ds_read_b128 v[88:91], v10 offset:14336\n" \
  "v_permlane16_swap_b32 v117, v119\n" \
  "v_mfma_f32_16x16x32_bf16 v[36:39], v[92:95], a[140:143], v[36:39]\n" \
  "v_permlane16_swap_b32 v120, v122\n" \
  "v_mfma_f32_16x16x32_bf16 v[24:27], v[140:143], v[96:99], v[24:27]\n" \
  "v_permlane16_swap_b32 v121, v123\n" \
  "v_mfma_f32_16x16x32_bf16 v[32:35], a[12:15], v[96:99], v[32:35]\n" \
  "v_mfma_f32_16x16x32_bf16 v[40:43], v[96:99], a[140:143], v[40:43]\n" \
  "s_waitcnt lgkmcnt(4)\n" \
  "v_mfma_f32_16x16x32_bf16 v[20:23], v[144:147], v[68:71], v[20:23]\n" \
  "ds_read_b128 v[92:95], v11 offset:12288\n" \
  "buffer_store_dwordx4 v[116:119], v14, s[76:79], s88 offen sc1\n" \
  "v_mfma_f32_16x16x32_bf16 v[28:31], a[16:19], v[68:71], v[28:31]\n" \
  "ds_read_b128 v[96:99], v11 offset:14336\n" \
  "buffer_store_dwordx4 v[120:123], v14, s[76:79], s89 offen sc1\n" \
  "v_mfma_f32_16x16x32_bf16 v[36:39], v[68:71], a[144:147], v[36:39]\n" \
  "buffer_store_dwordx4 v[124:127], v15, s[76:79], s90 offen sc1\n" \
  "v_mfma_f32_16x16x32_bf16 v[24:27], v[144:147], v[72:75], v[24:27]\n" \
  "v_mfma_f32_16x16x32_bf16 v[32:35], a[16:19], v[72:75], v[32:35]\n" \
  "v_mfma_f32_16x16x32_bf16 v[40:43], v[72:75], a[144:147], v[40:43]\n" \
  "s_waitcnt lgkmcnt(4)\n" \
  "v_mfma_f32_16x16x32_bf16 v[20:23], v[148:151], v[76:79], v[20:23]\n" \
  "ds_read_b128 v[68:71], v10 offset:16384\n" \
  "v_mfma_f32_16x16x32_bf16 v[28:31], a[20:23], v[76:79], v[28:31]\n" \
  "ds_read_b128 v[72:75], v10 offset:18432\n" \
  "v_mfma_f32_16x16x32_bf16 v[36:39], v[76:79], a[148:151], v[36:39]\n" \
  "v_mfma_f32_16x16x32_bf16 v[24:27], v[148:151], v[80:83], v[24:27]\n" \
  "s_add_u32 m0, s91, 98304\n" \
  "s_nop 0\n" \
  "buffer_load_dwordx4 v7, s[72:75], s87 offen lds\n" \
  "s_add_u32 s87, s87, 128\n" \
  "v_mfma_f32_16x16x32_bf16 v[32:35], a[20:23], v[80:83], v[32:35]\n" \
  "v_mfma_f32_16x16x32_bf16 v[40:43], v[80:83], a[148:151], v[40:43]\n" \
  "s_waitcnt lgkmcnt(4)\n" \
  "v_mfma_f32_16x16x32_bf16 v[20:23], v[152:155], v[84:87], v[20:23]\n" \
  "ds_read_b128 v[76:79], v11 offset:16384\n" \
  "v_mfma_f32_16x16x32_bf16 v[28:31], a[24:27], v[84:87], v[28:31]\n" \
  "ds_read_b128 v[80:83], v11 offset:18432\n" \
  "v_mfma_f32_16x16x32_bf16 v[36:39], v[84:87], a[152:155], v[36:39]\n" \
  "v_mfma_f32_16x16x32_bf16 v[24:27], v[152:155], v[88:91], v[24:27]\n" \
  "s_add_u32 m0, s91, 102400\n" \
  "s_nop 0\n" \
  "buffer_load_dwordx4 v7, s[72:75], s87 offen lds\n" \
  "s_add_u32 s87, s87, 128\n" \
  "v_mfma_f32_16x16x32_bf16 v[32:35], a[24:27], v[88:91], v[32:35]\n" \
  "v_mfma_f32_16x16x32_bf16 v[40:43], v[88:91], a[152:155], v[40:43]\n" \
  "s_waitcnt lgkmcnt(4)\n" \
  "v_mfma_f32_16x16x32_bf16 v[20:23], v[156:159], v[92:95], v[20:23]\n" \
  "ds_read_b128 v[84:87], v10 offset:20480\n" \
  "v_mfma_f32_16x16x32_bf16 v[28:31], a[28:31], v[92:95], v[28:31]\n" \
  "ds_read_b128 v[88:91], v10 offset:22528\n" \
  "v_mfma_f32_16x16x32_bf16 v[36:39], v[92:95], a[156:159], v[36:39]\n" \
  "v_mfma_f32_16x16x32_bf16 v[24:27], v[156:159], v[96:99], v[24:27]\n" \
  "s_add_u32 m0, s91, 106496\n" \
  "s_nop 0\n" \
  "buffer_load_dwordx4 v7, s[72:75], s87 offen lds\n" \
  "s_add_u32 s87, s87, 128\n" \
  "v_mfma_f32_16x16x32_bf16 v[32:35], a[28:31], v[96:99], v[32:35]\n" \
  "v_mfma_f32_16x16x32_bf16 v[40:43], v[96:99], a[156:159], v[40:43]\n" \
  "s_waitcnt lgkmcnt(4)\n" \
  "v_mfma_f32_16x16x32_bf16 v[20:23], v[160:163], v[68:71], v[20:23]\n" \
  "ds_read_b128 v[92:95], v11 offset:20480\n" \
  "v_mfma_f32_16x16x32_bf16 v[28:31], a[32:35], v[68:71], v[28:31]\n" \
  "ds_read_b128 v[96:99], v11 offset:22528\n" \
  "v_mfma_f32_16x16x32_bf16 v[36:39], v[68:71], a[160:163], v[36:39]\n" \
  "v_mfma_f32_16x16x32_bf16 v[24:27], v[160:163], v[72:75], v[24:27]\n" \
  "s_add_u32 m0, s91, 110592\n" \
  "s_nop 0\n" \
  "buffer_load_dwordx4 v7, s[72:75], s87 offen lds\n" \
  "s_add_u32 s87, s87, 128\n" \
  "v_mfma_f32_16x16x32_bf16 v[32:35], a[32:35], v[72:75], v[32:35]\n" \
  "v_mfma_f32_16x16x32_bf16 v[40:43], v[72:75], a[160:163], v[40:43]\n" \
  "s_waitcnt lgkmcnt(4)\n" \
  "v_mfma_f32_16x16x32_bf16 v[20:23], v[164:167], v[76:79], v[20:23]\n" \
  "ds_read_b128 v[68:71], v10 offset:24576\n" \
  "v_mfma_f32_16x16x32_bf16 v[28:31], a[36:39], v[76:79], v[28:31]\n" \
  "ds_read_b128 v[72:75], v10 offset:26624\n" \
  "v_mfma_f32_16x16x32_bf16 v[36:39], v[76:79], a[164:167], v[36:39]\n" \
  "v_mfma_f32_16x16x32_bf16 v[24:27], v[164:167], v[80:83], v[24:27]\n" \
  "s_add_u32 m0, s91, 114688\n" \
  "s_nop 0\n" \
  "buffer_load_dwordx4 v7, s[72:75], s87 offen lds\n" \
  "s_add_u32 s87, s87, 128\n" \
  "v_mfma_f32_16x16x32_bf16 v[32:35], a[36:39], v[80:83], v[32:35]\n" \
  "v_mfma_f32_16x16x32_bf16 v[40:43], v[80:83], a[164:167], v[40:43]\n" \
  "s_waitcnt lgkmcnt(4)\n" \
  "v_mfma_f32_16x16x32_bf16 v[20:23], v[168:171], v[84:87], v[20:23]\n" \
  "ds_read_b128 v[76:79], v11 offset:24576\n" \
  "v_mfma_f32_16x16x32_bf16 v[28:31], a[40:43], v[84:87], v[28:31]\n" \
  "ds_read_b128 v[80:83], v11 offset:26624\n" \
  "v_mfma_f32_16x16x32_bf16 v[36:39], v[84:87], a[168:171], v[36:39]\n" \
  "v_mfma_f32_16x16x32_bf16 v[24:27], v[168:171], v[88:91], v[24:27]\n" \
  "s_add_u32 m0, s91, 118784\n" \
  "s_nop 0\n" \
  "buffer_load_dwordx4 v7, s[72:75], s87 offen lds\n" \
  "s_add_u32 s87, s87, 128\n" \
  "v_mfma_f32_16x16x32_bf16 v[32:35], a[40:43], v[88:91], v[32:35]\n" \
  "v_mfma_f32_16x16x32_bf16 v[40:43], v[88:91], a[168:171], v[40:43]\n" \
  "s_waitcnt lgkmcnt(4)\n" \
  "v_mfma_f32_16x16x32_bf16 v[20:23], v[172:175], v[92:95], v[20:23]\n" \
  "ds_read_b128 v[84:87], v10 offset:28672\n" \
  "v_mfma_f32_16x16x32_bf16 v[28:31], a[44:47], v[92:95], v[28:31]\n" \
  "ds_read_b128 v[88:91], v10 offset:30720\n" \
  "v_mfma_f32_16x16x32_bf16 v[36:39], v[92:95], a[172:175], v[36:39]\n" \
  "v_mfma_f32_16x16x32_bf16 v[24:27], v[172:175], v[96:99], v[24:27]\n" \
  "s_add_u32 m0, s91, 122880\n" \
  "s_nop 0\n" \
  "buffer_load_dwordx4 v7, s[72:75], s87 offen lds\n" \
  "s_add_u32 s87, s87, 128\n" \
  "v_mfma_f32_16x16x32_bf16 v[32:35], a[44:47], v[96:99], v[32:35]\n" \
  "v_mfma_f32_16x16x32_bf16 v[40:43], v[96:99], a[172:175], v[40:43]\n" \
  "s_waitcnt lgkmcnt(4)\n" \
  "v_mfma_f32_16x16x32_bf16 v[20:23], v[176:179], v[68:71], v[20:23]\n" \
  "ds_read_b128 v[92:95], v11 offset:28672\n" \
  "v_mfma_f32_16x16x32_bf16 v[28:31], a[48:51], v[68:71], v[28:31]\n" \
  "ds_read_b128 v[96:99], v11 offset:30720\n" \
  "v_mfma_f32_16x16x32_bf16 v[36:39], v[68:71], a[176:179], v[36:39]\n" \
  "v_mfma_f32_16x16x32_bf16 v[24:27], v[176:179], v[72:75], v[24:27]\n" \
  "s_add_u32 m0, s91, 126976\n" \
  "s_nop 0\n" \
  "buffer_load_dwordx4 v7, s[72:75], s87 offen lds\n" \
  "s_add_u32 s87, s87, 63616\n" \
  "s_min_u32 s87, s87, s96\n" \
  "v_mfma_f32_16x16x32_bf16 v[32:35], a[48:51], v[72:75], v[32:35]\n" \
  "v_mfma_f32_16x16x32_bf16 v[40:43], v[72:75], a[176:179], v[40:43]\n" \
  "s_waitcnt lgkmcnt(4)\n" \
  "v_mfma_f32_16x16x32_bf16 v[20:23], v[180:183], v[76:79], v[20:23]\n" \
  "ds_read_b128 v[68:71], v10 offset:32768\n" \
  "v_mfma_f32_16x16x32_bf16 v[28:31], a[52:55], v[76:79], v[28:31]\n" \
  "ds_read_b128 v[72:75], v10 offset:34816\n" \
  "v_mfma_f32_16x16x32_bf16 v[36:39], v[76:79], a[180:183], v[36:39]\n" \
  "v_mfma_f32_16x16x32_bf16 v[24:27], v[180:183], v[80:83], v[24:27]\n" \
  "v_mfma_f32_16x16x32_bf16 v[32:35], a[52:55], v[80:83], v[32:35]\n" \
  "v_mfma_f32_16x16x32_bf16 v[40:43], v[80:83], a[180:183], v[40:43]\n" \
  "s_waitcnt lgkmcnt(4)\n" \
  "v_mfma_f32_16x16x32_bf16 v[20:23], v[184:187], v[84:87], v[20:23]\n" \
  "ds_read_b128 v[76:79], v11 offset:32768\n" \
  "v_mfma_f32_16x16x32_bf16 v[28:31], a[56:59], v[84:87], v[28:31]\n" \
  "ds_read_b128 v[80:83], v11 offset:34816\n" \
  "v_mfma_f32_16x16x32_bf16 v[36:39], v[84:87], a[184:187], v[36:39]\n" \
  "v_mfma_f32_16x16x32_bf16 v[24:27], v[184:187], v[88:91], v[24:27]\n" \
  "v_mfma_f32_16x16x32_bf16 v[32:35], a[56:59], v[88:91], v[32:35]\n" \
  "v_mfma_f32_16x16x32_bf16 v[40:43], v[88:91], a[184:187], v[40:43]\n" \
  "s_waitcnt lgkmcnt(4)\n" \
  "v_mfma_f32_16x16x32_bf16 v[20:23], v[188:191], v[92:95], v[20:23]\n" \
  "ds_read_b128 v[84:87], v10 offset:36864\n" \
  "v_mfma_f32_16x16x32_bf16 v[28:31], a[60:63], v[92:95], v[28:31]\n" \
  "ds_read_b128 v[88:91], v10 offset:38912\n" \
  "v_mfma_f32_16x16x32_bf16 v[36:39], v[92:95], a[188:191], v[36:39]\n" \
  "v_mfma_f32_16x16x32_bf16 v[24:27], v[188:191], v[96:99], v[24:27]\n" \
  "v_mfma_f32_16x16x32_bf16 v[32:35], a[60:63], v[96:99], v[32:35]\n" \
  "v_mfma_f32_16x16x32_bf16 v[40:43], v[96:99], a[188:191], v[40:43]\n" \
  "s_waitcnt vmcnt(8)\n" \
  "s_barrier\n" \
  "s_waitcnt lgkmcnt(4)\n" \
  "v_mfma_f32_16x16x32_bf16 v[20:23], v[192:195], v[68:71], v[20:23]\n" \
  "ds_read_b128 v[92:95], v11 offset:36864\n" \
  "buffer_store_dword v17, v16, s[80:83], 0 offen sc1\n" \
  "v_mfma_f32_16x16x32_bf16 v[28:31], a[64:67], v[68:71], v[28:31]\n" \
  "ds_read_b128 v[96:99], v11 offset:38912\n" \
  "v_add_u32 v17, 1, v17\n" \
  "v_mfma_f32_16x16x32_bf16 v[36:39], v[68:71], a[192:195], v[36:39]\n" \
  "v_and_b32 v14, 0x7fffffff, v14\n" \
  "v_mfma_f32_16x16x32_bf16 v[24:27], v[192:195], v[72:75], v[24:27]\n" \
  "v_and_b32 v15, 0x7fffffff, v15\n" \
  "v_mfma_f32_16x16x32_bf16 v[32:35], a[64:67], v[72:75], v[32:35]\n" \
  "v_mfma_f32_16x16x32_bf16 v[40:43], v[72:75], a[192:195], v[40:43]\n" \
  "s_waitcnt lgkmcnt(4)\n" \
  "v_mfma_f32_16x16x32_bf16 v[20:23], v[196:199], v[76:79], v[20:23]\n" \
  "ds_read_b128 v[68:71], v10 offset:40960\n" \
  "v_mfma_f32_16x16x32_bf16 v[28:31], a[68:71], v[76:79], v[28:31]\n" \
  "ds_read_b128 v[72:75], v10 offset:43008\n" \
  "v_mfma_f32_16x16x32_bf16 v[36:39], v[76:79], a[196:199], v[36:39]\n" \
  "v_mfma_f32_16x16x32_bf16 v[24:27], v[196:199], v[80:83], v[24:27]\n" \
  "v_mfma_f32_16x16x32_bf16 v[32:35], a[68:71], v[80:83], v[32:35]\n" \
  "v_mfma_f32_16x16x32_bf16 v[40:43], v[80:83], a[196:199], v[40:43]\n" \
  "s_waitcnt lgkmcnt(4)\n" \
  "v_mfma_f32_16x16x32_bf16 v[20:23], v[200:203], v[84:87], v[20:23]\n" \
  "ds_read_b128 v[76:79], v11 offset:40960\n" \
  "v_mfma_f32_16x16x32_bf16 v[28:31], a[72:75], v[84:87], v[28:31]\n" \
  "ds_read_b128 v[80:83], v11 offset:43008\n" \
  "v_mfma_f32_16x16x32_bf16 v[36:39], v[84:87], a[200:203], v[36:39]\n" \
  "v_mfma_f32_16x16x32_bf16 v[24:27], v[200:203], v[88:91], v[24:27]\n" \
  "v_mfma_f32_16x16x32_bf16 v[32:35], a[72:75], v[88:91], v[32:35]\n" \
  "v_mfma_f32_16x16x32_bf16 v[40:43], v[88:91], a[200:203], v[40:43]\n" \
  "s_waitcnt lgkmcnt(4)\n" \
  "v_mfma_f32_16x16x32_bf16 v[20:23], v[204:207], v[92:95], v[20:23]\n" \
  "ds_read_b128 v[84:87], v10 offset:45056\n" \
  "v_mfma_f32_16x16x32_bf16 v[28:31], a[76:79], v[92:95], v[28:31]\n" \
  "ds_read_b128 v[88:91], v10 offset:47104\n" \
  "v_mfma_f32_16x16x32_bf16 v[36:39], v[92:95], a[204:207], v[36:39]\n" \
  "v_mfma_f32_16x16x32_bf16 v[24:27], v[204:207], v[96:99], v[24:27]\n" \
  "v_mfma_f32_16x16x32_bf16 v[32:35], a[76:79], v[96:99], v[32:35]\n" \
  "v_mfma_f32_16x16x32_bf16 v[40:43], v[96:99], a[204:207], v[40:43]\n" \
  "s_waitcnt lgkmcnt(4)\n" \
  "v_mfma_f32_16x16x32_bf16 v[20:23], v[208:211], v[68:71], v[20:23]\n" \
  "ds_read_b128 v[92:95], v11 offset:45056\n" \
  "v_mfma_f32_16x16x32_bf16 v[28:31], a[80:83], v[68:71], v[28:31]\n" \
  "ds_read_b128 v[96:99], v11 offset:47104\n" \
  "v_mfma_f32_16x16x32_bf16 v[36:39], v[68:71], a[208:211], v[36:39]\n" \
  "v_mfma_f32_16x16x32_bf16 v[24:27], v[208:211], v[72:75], v[24:27]\n" \
  "v_mfma_f32_16x16x32_bf16 v[32:35], a[80:83], v[72:75], v[32:35]\n" \
  "v_mfma_f32_16x16x32_bf16 v[40:43], v[72:75], a[208:211], v[40:43]\n" \
  "s_waitcnt lgkmcnt(4)\n" \
  "v_mfma_f32_16x16x32_bf16 v[20:23], v[212:215], v[76:79], v[20:23]\n" \
  "ds_read_b128 v[68:71], v10 offset:49152\n" \
  "v_mfma_f32_16x16x32_bf16 v[28:31], a[84:87], v[76:79], v[28:31]\n" \
  "ds_read_b128 v[72:75], v10 offset:51200\n" \
  "v_mfma_f32_16x16x32_bf16 v[36:39], v[76:79], a[212:215], v[36:39]\n" \
  "v_mfma_f32_16x16x32_bf16 v[24:27], v[212:215], v[80:83], v[24:27]\n" \
  "s_add_u32 m0, s91, 0\n" \
  "s_nop 0\n" \
  "buffer_load_dwordx4 v7, s[72:75], s87 offen lds\n" \
  "s_add_u32 s87, s87, 128\n" \
  "v_mfma_f32_16x16x32_bf16 v[32:35], a[84:87], v[80:83], v[32:35]\n" \
  "v_mfma_f32_16x16x32_bf16 v[40:43], v[80:83], a[212:215], v[40:43]\n" \
  "s_waitcnt lgkmcnt(4)\n" \
  "v_mfma_f32_16x16x32_bf16 v[20:23], v[216:219], v[84:87], v[20:23]\n" \
  "ds_read_b128 v[76:79], v11 offset:49152\n" \
  "v_mfma_f32_16x16x32_bf16 v[28:31], a[88:91], v[84:87], v[28:31]\n" \
  "ds_read_b128 v[80:83], v11 offset:51200\n" \
  "v_mfma_f32_16x16x32_bf16 v[36:39], v[84:87], a[216:219], v[36:39]\n" \
  "v_mfma_f32_16x16x32_bf16 v[24:27], v[216:219], v[88:91], v[24:27]\n" \
  "s_add_u32 m0, s91, 4096\n" \
  "s_nop 0\n" \
  "buffer_load_dwordx4 v7, s[72:75], s87 offen lds\n" \
  "s_add_u32 s87, s87, 128\n" \
  "v_mfma_f32_16x16x32_bf16 v[32:35], a[88:91], v[88:91], v[32:35]\n" \
  "v_mfma_f32_16x16x32_bf16 v[40:43], v[88:91], a[216:219], v[40:43]\n" \
  "s_waitcnt lgkmcnt(4)\n" \
  "v_mfma_f32_16x16x32_bf16 v[20:23], v[220:223], v[92:95], v[20:23]\n" \
  "ds_read_b128 v[84:87], v10 offset:53248\n" \
  "v_mfma_f32_16x16x32_bf16 v[28:31], a[92:95], v[92:95], v[28:31]\n" \
  "ds_read_b128 v[88:91], v10 offset:55296\n" \
  "v_mfma_f32_16x16x32_bf16 v[36:39], v[92:95], a[220:223], v[36:39]\n" \
  "v_mfma_f32_16x16x32_bf16 v[24:27], v[220:223], v[96:99], v[24:27]\n" \
  "s_add_u32 m0, s91, 8192\n" \
  "s_nop 0\n" \
  "buffer_load_dwordx4 v7, s[72:75], s87 offen lds\n" \
  "s_add_u32 s87, s87, 128\n" \
  "v_mfma_f32_16x16x32_bf16 v[32:35], a[92:95], v[96:99], v[32:35]\n" \
  "v_mfma_f32_16x16x32_bf16 v[40:43], v[96:99], a[220:223], v[40:43]\n" \
  "s_waitcnt lgkmcnt(4)\n" \
  "v_mfma_f32_16x16x32_bf16 v[20:23], v[224:227], v[68:71], v[20:23]\n" \
  "ds_read_b128 v[92:95], v11 offset:53248\n" \
  "v_mfma_f32_16x16x32_bf16 v[28:31], a[96:99], v[68:71], v[28:31]\n" \
  "ds_read_b128 v[96:99], v11 offset:55296\n" \
  "v_mfma_f32_16x16x32_bf16 v[36:39], v[68:71], a[224:227], v[36:39]\n" \
  "v_mfma_f32_16x16x32_bf16 v[24:27], v[224:227], v[72:75], v[24:27]\n" \
  "s_add_u32 m0, s91, 12288\n" \
  "s_nop 0\n" \
  "buffer_load_dwordx4 v7, s[72:75], s87 offen lds\n" \
  "s_add_u32 s87, s87, 128\n" \
  "v_mfma_f32_16x16x32_bf16 v[32:35], a[96:99], v[72:75], v[32:35]\n" \
  "v_mfma_f32_16x16x32_bf16 v[40:43], v[72:75], a[224:227], v[40:43]\n" \
  "s_waitcnt lgkmcnt(4)\n" \
  "v_mfma_f32_16x16x32_bf16 v[20:23], v[228:231], v[76:79], v[20:23]\n" \
  "ds_read_b128 v[68:71], v10 offset:57344\n" \
  "v_mfma_f32_16x16x32_bf16 v[28:31], a[100:103], v[76:79], v[28:31]\n" \
  "ds_read_b128 v[72:75], v10 offset:59392\n" \
  "v_mfma_f32_16x16x32_bf16 v[36:39], v[76:79], a[228:231], v[36:39]\n" \
  "v_mfma_f32_16x16x32_bf16 v[24:27], v[228:231], v[80:83], v[24:27]\n" \
  "s_add_u32 m0, s91, 16384\n" \
  "s_nop 0\n" \
  "buffer_load_dwordx4 v7, s[72:75], s87 offen lds\n" \
  "s_add_u32 s87, s87, 128\n" \
  "v_mfma_f32_16x16x32_bf16 v[32:35], a[100:103], v[80:83], v[32:35]\n" \
  "v_mfma_f32_16x16x32_bf16 v[40:43], v[80:83], a[228:231], v[40:43]\n" \
  "s_waitcnt lgkmcnt(4)\n" \
  "v_mfma_f32_16x16x32_bf16 v[20:23], v[232:235], v[84:87], v[20:23]\n" \
  "ds_read_b128 v[76:79], v11 offset:57344\n" \
  "v_mfma_f32_16x16x32_bf16 v[28:31], a[104:107], v[84:87], v[28:31]\n" \
  "ds_read_b128 v[80:83], v11 offset:59392\n" \
  "v_mfma_f32_16x16x32_bf16 v[36:39], v[84:87], a[232:235], v[36:39]\n" \
  "v_mfma_f32_16x16x32_bf16 v[24:27], v[232:235], v[88:91], v[24:27]\n" \
  "s_add_u32 m0, s91, 20480\n" \
  "s_nop 0\n" \
  "buffer_load_dwordx4 v7, s[72:75], s87 offen lds\n" \
  "s_add_u32 s87, s87, 128\n" \
  "v_mfma_f32_16x16x32_bf16 v[32:35], a[104:107], v[88:91], v[32:35]\n" \
  "v_mfma_f32_16x16x32_bf16 v[40:43], v[88:91], a[232:235], v[40:43]\n" \
  "s_waitcnt lgkmcnt(4)\n" \
  "v_mfma_f32_16x16x32_bf16 v[20:23], v[236:239], v[92:95], v[20:23]\n" \
  "ds_read_b128 v[84:87], v10 offset:61440\n" \
  "v_mfma_f32_16x16x32_bf16 v[28:31], a[108:111], v[92:95], v[28:31]\n" \
  "ds_read_b128 v[88:91], v10 offset:63488\n" \
  "v_mfma_f32_16x16x32_bf16 v[36:39], v[92:95], a[236:239], v[36:39]\n" \
  "v_mfma_f32_16x16x32_bf16 v[24:27], v[236:239], v[96:99], v[24:27]\n" \
  "s_add_u32 m0, s91, 24576\n" \
  "s_nop 0\n" \
  "buffer_load_dwordx4 v7, s[72:75], s87 offen lds\n" \
  "s_add_u32 s87, s87, 128\n" \
  "v_mfma_f32_16x16x32_bf16 v[32:35], a[108:111], v[96:99], v[32:35]\n" \
  "v_mfma_f32_16x16x32_bf16 v[40:43], v[96:99], a[236:239], v[40:43]\n" \
  "s_waitcnt lgkmcnt(4)\n" \
  "v_mfma_f32_16x16x32_bf16 v[20:23], v[240:243], v[68:71], v[20:23]\n" \
  "ds_read_b128 v[92:95], v11 offset:61440\n" \
  "v_mfma_f32_16x16x32_bf16 v[28:31], a[112:115], v[68:71], v[28:31]\n" \
  "ds_read_b128 v[96:99], v11 offset:63488\n" \
  "v_mfma_f32_16x16x32_bf16 v[36:39], v[68:71], a[240:243], v[36:39]\n" \
  "v_mfma_f32_16x16x32_bf16 v[24:27], v[240:243], v[72:75], v[24:27]\n" \
  "s_add_u32 m0, s91, 28672\n" \
  "s_nop 0\n" \
  "buffer_load_dwordx4 v7, s[72:75], s87 offen lds\n" \
  "s_add_u32 s87, s87, 128\n" \
  "v_mfma_f32_16x16x32_bf16 v[32:35], a[112:115], v[72:75], v[32:35]\n" \
  "v_mfma_f32_16x16x32_bf16 v[40:43], v[72:75], a[240:243], v[40:43]\n" \
  "s_waitcnt lgkmcnt(4)\n" \
  "v_mfma_f32_16x16x32_bf16 v[20:23], v[244:247], v[76:79], v[20:23]\n" \
  "ds_read_b128 v[68:71], v12 offset:0\n" \
  "v_mfma_f32_16x16x32_bf16 v[28:31], a[116:119], v[76:79], v[28:31]\n" \
  "ds_read_b128 v[72:75], v12 offset:2048\n" \
  "v_mfma_f32_16x16x32_bf16 v[36:39], v[76:79], a[244:247], v[36:39]\n" \
  "v_mfma_f32_16x16x32_bf16 v[24:27], v[244:247], v[80:83], v[24:27]\n" \
  "v_mfma_f32_16x16x32_bf16 v[32:35], a[116:119], v[80:83], v[32:35]\n" \
  "v_mfma_f32_16x16x32_bf16 v[40:43], v[80:83], a[244:247], v[40:43]\n" \
  "s_waitcnt lgkmcnt(4)\n" \
  "v_mfma_f32_16x16x32_bf16 v[20:23], v[248:251], v[84:87], v[20:23]\n" \
  "ds_read_b128 v[76:79], v13 offset:0\n" \
  "v_mfma_f32_16x16x32_bf16 v[28:31], a[120:123], v[84:87], v[28:31]\n" \
  "ds_read_b128 v[80:83], v13 offset:2048\n" \
  "v_mfma_f32_16x16x32_bf16 v[36:39], v[84:87], a[248:251], v[36:39]\n" \
  "v_mfma_f32_16x16x32_bf16 v[24:27], v[248:251], v[88:91], v[24:27]\n" \
  "v_mfma_f32_16x16x32_bf16 v[32:35], a[120:123], v[88:91], v[32:35]\n" \
  "v_mfma_f32_16x16x32_bf16 v[40:43], v[88:91], a[248:251], v[40:43]\n" \
  "s_waitcnt lgkmcnt(4)\n" \
  "v_mfma_f32_16x16x32_bf16 v[20:23], v[252:255], v[92:95], v[20:23]\n" \
  "ds_read_b128 v[84:87], v12 offset:4096\n" \
  "v_mfma_f32_16x16x32_bf16 v[28:31], a[124:127], v[92:95], v[28:31]\n" \
  "ds_read_b128 v[88:91], v12 offset:6144\n" \
  "v_mfma_f32_16x16x32_bf16 v[36:39], v[92:95], a[252:255], v[36:39]\n" \
  "v_mfma_f32_16x16x32_bf16 v[24:27], v[252:255], v[96:99], v[24:27]\n" \
  "v_mfma_f32_16x16x32_bf16 v[32:35], a[124:127], v[96:99], v[32:35]\n" \
  "v_mfma_f32_16x16x32_bf16 v[40:43], v[96:99], a[252:255], v[40:43]\n" \
  "s_cmp_eq_u32 s85, 64\n" \
  "s_cbranch_scc1 LX1_%=\n" \
  "s_waitcnt vmcnt(8)\n" \
  "s_barrier\n" \
  "s_waitcnt lgkmcnt(4)\n" \
  "v_mfma_f32_16x16x32_bf16 v[44:47], v[128:131], v[68:71], 0\n" \
  "ds_read_b128 v[92:95], v13 offset:4096\n" \
  "s_sub_u32 s89, s85, 1\n" \
  "s_and_b32 s89, s89, 63\n" \
  "v_mfma_f32_16x16x32_bf16 v[52:55], a[0:3], v[68:71], 0\n" \
  "ds_read_b128 v[96:99], v13 offset:6144\n" \
  "s_lshr_b32 s84, s89, 2\n" \
  "s_xnor_b32 s84, s84, %9\n" \
  "v_mfma_f32_16x16x32_bf16 v[60:63], v[68:71], a[128:131], 0\n" \
  "s_and_b32 s84, s84, 1\n" \
  "s_lshl_b32 s84, s84, 13\n" \
  "v_mfma_f32_16x16x32_bf16 v[48:51], v[128:131], v[72:75], 0\n" \
  "s_lshl_b32 s88, s89, 19\n" \
  "s_or_b32 s88, s88, s84\n" \
  "v_mfma_f32_16x16x32_bf16 v[56:59], a[0:3], v[72:75], 0\n" \
  "s_add_u32 s89, s88, " STR(QKV_SZ32) "\n" \
  "s_add_u32 s90, s89, " STR(QKV_SZ32) "\n" \
  "v_mfma_f32_16x16x32_bf16 v[64:67], v[72:75], a[128:131], 0\n" \
  "s_add_u32 s85, s85, 1\n" \
  "s_waitcnt lgkmcnt(4)\n" \
  "v_mfma_f32_16x16x32_bf16 v[44:47], v[132:135], v[76:79], v[44:47]\n" \
  "ds_read_b128 v[68:71], v12 offset:8192\n" \
  "v_cvt_pk_bf16_f32 v116, v20, v21\n" \
  "v_mfma_f32_16x16x32_bf16 v[52:55], a[4:7], v[76:79], v[52:55]\n" \
  "ds_read_b128 v[72:75], v12 offset:10240\n" \
  "v_cvt_pk_bf16_f32 v117, v22, v23\n" \
  "v_mfma_f32_16x16x32_bf16 v[60:63], v[76:79], a[132:135], v[60:63]\n" \
  "v_cvt_pk_bf16_f32 v118, v24, v25\n" \
  "v_mfma_f32_16x16x32_bf16 v[48:51], v[132:135], v[80:83], v[48:51]\n" \
  "v_cvt_pk_bf16_f32 v119, v26, v27\n" \
  "v_mfma_f32_16x16x32_bf16 v[56:59], a[4:7], v[80:83], v[56:59]\n" \
  "v_cvt_pk_bf16_f32 v120, v28, v29\n" \
  "v_mfma_f32_16x16x32_bf16 v[64:67], v[80:83], a[132:135], v[64:67]\n" \
  "v_cvt_pk_bf16_f32 v121, v30, v31\n" \
  "s_waitcnt lgkmcnt(4)\n" \
  "v_mfma_f32_16x16x32_bf16 v[44:47], v[136:139], v[84:87], v[44:47]\n" \
  "ds_read_b128 v[76:79], v13 offset:8192\n" \
  "v_cvt_pk_bf16_f32 v122, v32, v33\n" \
  "v_mfma_f32_16x16x32_bf16 v[52:55], a[8:11], v[84:87], v[52:55]\n" \
  "ds_read_b128 v[80:83], v13 offset:10240\n" \
  "v_cvt_pk_bf16_f32 v123, v34, v35\n" \
  "v_mfma_f32_16x16x32_bf16 v[60:63], v[84:87], a[136:139], v[60:63]\n" \
  "v_cvt_pk_bf16_f32 v124, v36, v37\n" \
  "v_mfma_f32_16x16x32_bf16 v[48:51], v[136:139], v[88:91], v[48:51]\n" \
  "v_cvt_pk_bf16_f32 v125, v38, v39\n" \
  "v_mfma_f32_16x16x32_bf16 v[56:59], a[8:11], v[88:91], v[56:59]\n" \
  "v_cvt_pk_bf16_f32 v126, v40, v41\n" \
  "v_mfma_f32_16x16x32_bf16 v[64:67], v[88:91], a[136:139], v[64:67]\n" \
  "v_cvt_pk_bf16_f32 v127, v42, v43\n" \
  "s_waitcnt lgkmcnt(4)\n" \
  "v_mfma_f32_16x16x32_bf16 v[44:47], v[140:143], v[92:95], v[44:47]\n" \
  "ds_read_b128 v[84:87], v12 offset:12288\n" \
  "v_permlane16_swap_b32 v116, v118\n" \
  "v_mfma_f32_16x16x32_bf16 v[52:55], a[12:15], v[92:95], v[52:55]\n" \
  "ds_read_b128 v[88:91], v12 offset:14336\n" \
  "v_permlane16_swap_b32 v117, v119\n" \
  "v_mfma_f32_16x16x32_bf16 v[60:63], v[92:95], a[140:143], v[60:63]\n" \
  "v_permlane16_swap_b32 v120, v122\n" \
  "v_mfma_f32_16x16x32_bf16 v[48:51], v[140:143], v[96:99], v[48:51]\n" \
  "v_permlane16_swap_b32 v121, v123\n" \
  "v_mfma_f32_16x16x32_bf16 v[56:59], a[12:15], v[96:99], v[56:59]\n" \
  "v_mfma_f32_16x16x32_bf16 v[64:67], v[96:99], a[140:143], v[64:67]\n" \
  "s_waitcnt lgkmcnt(4)\n" \
  "v_mfma_f32_16x16x32_bf16 v[44:47], v[144:147], v[68:71], v[44:47]\n" \
  "ds_read_b128 v[92:95], v13 offset:12288\n" \
  "buffer_store_dwordx4 v[116:119], v14, s[76:79], s88 offen sc1\n" \
  "v_mfma_f32_16x16x32_bf16 v[52:55], a[16:19], v[68:71], v[52:55]\n" \
  "ds_read_b128 v[96:99], v13 offset:14336\n" \
  "buffer_store_dwordx4 v[120:123], v14, s[76:79], s89 offen sc1\n" \
  "v_mfma_f32_16x16x32_bf16 v[60:63], v[68:71], a[144:147], v[60:63]\n" \
  "buffer_store_dwordx4 v[124:127], v15, s[76:79], s90 offen sc1\n" \
  "v_mfma_f32_16x16x32_bf16 v[48:51], v[144:147], v[72:75], v[48:51]\n" \
  "v_mfma_f32_16x16x32_bf16 v[56:59], a[16:19], v[72:75], v[56:59]\n" \
  "v_mfma_f32_16x16x32_bf16 v[64:67], v[72:75], a[144:147], v[64:67]\n" \
  "s_waitcnt lgkmcnt(4)\n" \
  "v_mfma_f32_16x16x32_bf16 v[44:47], v[148:151], v[76:79], v[44:47]\n" \
  "ds_read_b128 v[68:71], v12 offset:16384\n" \
  "v_mfma_f32_16x16x32_bf16 v[52:55], a[20:23], v[76:79], v[52:55]\n" \
  "ds_read_b128 v[72:75], v12 offset:18432\n" \
  "v_mfma_f32_16x16x32_bf16 v[60:63], v[76:79], a[148:151], v[60:63]\n" \
  "v_mfma_f32_16x16x32_bf16 v[48:51], v[148:151], v[80:83], v[48:51]\n" \
  "s_add_u32 m0, s91, 32768\n" \
  "s_nop 0\n" \
  "buffer_load_dwordx4 v7, s[72:75], s87 offen lds\n" \
  "s_add_u32 s87, s87, 128\n" \
  "v_mfma_f32_16x16x32_bf16 v[56:59], a[20:23], v[80:83], v[56:59]\n" \
  "v_mfma_f32_16x16x32_bf16 v[64:67], v[80:83], a[148:151], v[64:67]\n" \
  "s_waitcnt lgkmcnt(4)\n" \
  "v_mfma_f32_16x16x32_bf16 v[44:47], v[152:155], v[84:87], v[44:47]\n" \
  "ds_read_b128 v[76:79], v13 offset:16384\n" \
  "v_mfma_f32_16x16x32_bf16 v[52:55], a[24:27], v[84:87], v[52:55]\n" \
  "ds_read_b128 v[80:83], v13 offset:18432\n" \
  "v_mfma_f32_16x16x32_bf16 v[60:63], v[84:87], a[152:155], v[60:63]\n" \
  "v_mfma_f32_16x16x32_bf16 v[48:51], v[152:155], v[88:91], v[48:51]\n" \
  "s_add_u32 m0, s91, 36864\n" \
  "s_nop 0\n" \
  "buffer_load_dwordx4 v7, s[72:75], s87 offen lds\n" \
  "s_add_u32 s87, s87, 128\n" \
  "v_mfma_f32_16x16x32_bf16 v[56:59], a[24:27], v[88:91], v[56:59]\n" \
  "v_mfma_f32_16x16x32_bf16 v[64:67], v[88:91], a[152:155], v[64:67]\n" \
  "s_waitcnt lgkmcnt(4)\n" \
  "v_mfma_f32_16x16x32_bf16 v[44:47], v[156:159], v[92:95], v[44:47]\n" \
  "ds_read_b128 v[84:87], v12 offset:20480\n" \
  "v_mfma_f32_16x16x32_bf16 v[52:55], a[28:31], v[92:95], v[52:55]\n" \
  "ds_read_b128 v[88:91], v12 offset:22528\n" \
  "v_mfma_f32_16x16x32_bf16 v[60:63], v[92:95], a[156:159], v[60:63]\n" \
  "v_mfma_f32_16x16x32_bf16 v[48:51], v[156:159], v[96:99], v[48:51]\n" \
  "s_add_u32 m0, s91, 40960\n" \
  "s_nop 0\n" \
  "buffer_load_dwordx4 v7, s[72:75], s87 offen lds\n" \
  "s_add_u32 s87, s87, 128\n" \
  "v_mfma_f32_16x16x32_bf16 v[56:59], a[28:31], v[96:99], v[56:59]\n" \
  "v_mfma_f32_16x16x32_bf16 v[64:67], v[96:99], a[156:159], v[64:67]\n" \
  "s_waitcnt lgkmcnt(4)\n" \
  "v_mfma_f32_16x16x32_bf16 v[44:47], v[160:163], v[68:71], v[44:47]\n" \
  "ds_read_b128 v[92:95], v13 offset:20480\n" \
  "v_mfma_f32_16x16x32_bf16 v[52:55], a[32:35], v[68:71], v[52:55]\n" \
  "ds_read_b128 v[96:99], v13 offset:22528\n" \
  "v_mfma_f32_16x16x32_bf16 v[60:63], v[68:71], a[160:163], v[60:63]\n" \
  "v_mfma_f32_16x16x32_bf16 v[48:51], v[160:163], v[72:75], v[48:51]\n" \
  "s_add_u32 m0, s91, 45056\n" \
  "s_nop 0\n" \
  "buffer_load_dwordx4 v7, s[72:75], s87 offen lds\n" \
  "s_add_u32 s87, s87, 128\n" \
  "v_mfma_f32_16x16x32_bf16 v[56:59], a[32:35], v[72:75], v[56:59]\n" \
  "v_mfma_f32_16x16x32_bf16 v[64:67], v[72:75], a[160:163], v[64:67]\n" \
  "s_waitcnt lgkmcnt(4)\n" \
  "v_mfma_f32_16x16x32_bf16 v[44:47], v[164:167], v[76:79], v[44:47]\n" \
  "ds_read_b128 v[68:71], v12 offset:24576\n" \
  "v_mfma_f32_16x16x32_bf16 v[52:55], a[36:39], v[76:79], v[52:55]\n" \
  "ds_read_b128 v[72:75], v12 offset:26624\n" \
  "v_mfma_f32_16x16x32_bf16 v[60:63], v[76:79], a[164:167], v[60:63]\n" \
  "v_mfma_f32_16x16x32_bf16 v[48:51], v[164:167], v[80:83], v[48:51]\n" \
  "s_add_u32 m0, s91, 49152\n" \
  "s_nop 0\n" \
  "buffer_load_dwordx4 v7, s[72:75], s87 offen lds\n" \
  "s_add_u32 s87, s87, 128\n" \
  "v_mfma_f32_16x16x32_bf16 v[56:59], a[36:39], v[80:83], v[56:59]\n" \
  "v_mfma_f32_16x16x32_bf16 v[64:67], v[80:83], a[164:167], v[64:67]\n" \
  "s_waitcnt lgkmcnt(4)\n" \
  "v_mfma_f32_16x16x32_bf16 v[44:47], v[168:171], v[84:87], v[44:47]\n" \
  "ds_read_b128 v[76:79], v13 offset:24576\n" \
  "v_mfma_f32_16x16x32_bf16 v[52:55], a[40:43], v[84:87], v[52:55]\n" \
  "ds_read_b128 v[80:83], v13 offset:26624\n" \
  "v_mfma_f32_16x16x32_bf16 v[60:63], v[84:87], a[168:171], v[60:63]\n" \
  "v_mfma_f32_16x16x32_bf16 v[48:51], v[168:171], v[88:91], v[48:51]\n" \
  "s_add_u32 m0, s91, 53248\n" \
  "s_nop 0\n" \
  "buffer_load_dwordx4 v7, s[72:75], s87 offen lds\n" \
  "s_add_u32 s87, s87, 128\n" \
  "v_mfma_f32_16x16x32_bf16 v[56:59], a[40:43], v[88:91], v[56:59]\n" \
  "v_mfma_f32_16x16x32_bf16 v[64:67], v[88:91], a[168:171], v[64:67]\n" \
  "s_waitcnt lgkmcnt(4)\n" \
  "v_mfma_f32_16x16x32_bf16 v[44:47], v[172:175], v[92:95], v[44:47]\n" \
  "ds_read_b128 v[84:87], v12 offset:28672\n" \
  "v_mfma_f32_16x16x32_bf16 v[52:55], a[44:47], v[92:95], v[52:55]\n" \
  "ds_read_b128 v[88:91], v12 offset:30720\n" \
  "v_mfma_f32_16x16x32_bf16 v[60:63], v[92:95], a[172:175], v[60:63]\n" \
  "v_mfma_f32_16x16x32_bf16 v[48:51], v[172:175], v[96:99], v[48:51]\n" \
  "s_add_u32 m0, s91, 57344\n" \
  "s_nop 0\n" \
  "buffer_load_dwordx4 v7, s[72:75], s87 offen lds\n" \
  "s_add_u32 s87, s87, 128\n" \
  "v_mfma_f32_16x16x32_bf16 v[56:59], a[44:47], v[96:99], v[56:59]\n" \
  "v_mfma_f32_16x16x32_bf16 v[64:67], v[96:99], a[172:175], v[64:67]\n" \
  "s_waitcnt lgkmcnt(4)\n" \
  "v_mfma_f32_16x16x32_bf16 v[44:47], v[176:179], v[68:71], v[44:47]\n" \
  "ds_read_b128 v[92:95], v13 offset:28672\n" \
  "v_mfma_f32_16x16x32_bf16 v[52:55], a[48:51], v[68:71], v[52:55]\n" \
  "ds_read_b128 v[96:99], v13 offset:30720\n" \
  "v_mfma_f32_16x16x32_bf16 v[60:63], v[68:71], a[176:179], v[60:63]\n" \
  "v_mfma_f32_16x16x32_bf16 v[48:51], v[176:179], v[72:75], v[48:51]\n" \
  "s_add_u32 m0, s91, 61440\n" \
  "s_nop 0\n" \
  "buffer_load_dwordx4 v7, s[72:75], s87 offen lds\n" \
  "s_add_u32 s87, s87, 63616\n" \
  "s_min_u32 s87, s87, s96\n" \
  "v_mfma_f32_16x16x32_bf16 v[56:59], a[48:51], v[72:75], v[56:59]\n" \
  "v_mfma_f32_16x16x32_bf16 v[64:67], v[72:75], a[176:179], v[64:67]\n" \
  "s_waitcnt lgkmcnt(4)\n" \
  "v_mfma_f32_16x16x32_bf16 v[44:47], v[180:183], v[76:79], v[44:47]\n" \
  "ds_read_b128 v[68:71], v12 offset:32768\n" \
  "v_mfma_f32_16x16x32_bf16 v[52:55], a[52:55], v[76:79], v[52:55]\n" \
  "ds_read_b128 v[72:75], v12 offset:34816\n" \
  "v_mfma_f32_16x16x32_bf16 v[60:63], v[76:79], a[180:183], v[60:63]\n" \
  "v_mfma_f32_16x16x32_bf16 v[48:51], v[180:183], v[80:83], v[48:51]\n" \
  "v_mfma_f32_16x16x32_bf16 v[56:59], a[52:55], v[80:83], v[56:59]\n" \
  "v_mfma_f32_16x16x32_bf16 v[64:67], v[80:83], a[180:183], v[64:67]\n" \
  "s_waitcnt lgkmcnt(4)\n" \
  "v_mfma_f32_16x16x32_bf16 v[44:47], v[184:187], v[84:87], v[44:47]\n" \
  "ds_read_b128 v[76:79], v13 offset:32768\n" \
  "v_mfma_f32_16x16x32_bf16 v[52:55], a[56:59], v[84:87], v[52:55]\n" \
  "ds_read_b128 v[80:83], v13 offset:34816\n" \
  "v_mfma_f32_16x16x32_bf16 v[60:63], v[84:87], a[184:187], v[60:63]\n" \
  "v_mfma_f32_16x16x32_bf16 v[48:51], v[184:187], v[88:91], v[48:51]\n" \
  "v_mfma_f32_16x16x32_bf16 v[56:59], a[56:59], v[88:91], v[56:59]\n" \
  "v_mfma_f32_16x16x32_bf16 v[64:67], v[88:91], a[184:187], v[64:67]\n" \
  "s_waitcnt lgkmcnt(4)\n" \
  "v_mfma_f32_16x16x32_bf16 v[44:47], v[188:191], v[92:95], v[44:47]\n" \
  "ds_read_b128 v[84:87], v12 offset:36864\n" \
  "v_mfma_f32_16x16x32_bf16 v[52:55], a[60:63], v[92:95], v[52:55]\n" \
  "ds_read_b128 v[88:91], v12 offset:38912\n" \
  "v_mfma_f32_16x16x32_bf16 v[60:63], v[92:95], a[188:191], v[60:63]\n" \
  "v_mfma_f32_16x16x32_bf16 v[48:51], v[188:191], v[96:99], v[48:51]\n" \
  "v_mfma_f32_16x16x32_bf16 v[56:59], a[60:63], v[96:99], v[56:59]\n" \
  "v_mfma_f32_16x16x32_bf16 v[64:67], v[96:99], a[188:191], v[64:67]\n" \
  "s_waitcnt vmcnt(8)\n" \
  "s_barrier\n" \
  "s_waitcnt lgkmcnt(4)\n" \
  "v_mfma_f32_16x16x32_bf16 v[44:47], v[192:195], v[68:71], v[44:47]\n" \
  "ds_read_b128 v[92:95], v13 offset:36864\n" \
  "buffer_store_dword v17, v16, s[80:83], 0 offen sc1\n" \
  "v_mfma_f32_16x16x32_bf16 v[52:55], a[64:67], v[68:71], v[52:55]\n" \
  "ds_read_b128 v[96:99], v13 offset:38912\n" \
  "v_add_u32 v17, 1, v17\n" \
  "v_mfma_f32_16x16x32_bf16 v[60:63], v[68:71], a[192:195], v[60:63]\n" \
  "v_and_b32 v14, 0x7fffffff, v14\n" \
  "v_mfma_f32_16x16x32_bf16 v[48:51], v[192:195], v[72:75], v[48:51]\n" \
  "v_and_b32 v15, 0x7fffffff, v15\n" \
  "v_mfma_f32_16x16x32_bf16 v[56:59], a[64:67], v[72:75], v[56:59]\n" \
  "v_mfma_f32_16x16x32_bf16 v[64:67], v[72:75], a[192:195], v[64:67]\n" \
  "s_waitcnt lgkmcnt(4)\n" \
  "v_mfma_f32_16x16x32_bf16 v[44:47], v[196:199], v[76:79], v[44:47]\n" \
  "ds_read_b128 v[68:71], v12 offset:40960\n" \
  "v_mfma_f32_16x16x32_bf16 v[52:55], a[68:71], v[76:79], v[52:55]\n" \
  "ds_read_b128 v[72:75], v12 offset:43008\n" \
  "v_mfma_f32_16x16x32_bf16 v[60:63], v[76:79], a[196:199], v[60:63]\n" \
  "v_mfma_f32_16x16x32_bf16 v[48:51], v[196:199], v[80:83], v[48:51]\n" \
  "v_mfma_f32_16x16x32_bf16 v[56:59], a[68:71], v[80:83], v[56:59]\n" \
  "v_mfma_f32_16x16x32_bf16 v[64:67], v[80:83], a[196:199], v[64:67]\n" \
  "s_waitcnt lgkmcnt(4)\n" \
  "v_mfma_f32_16x16x32_bf16 v[44:47], v[200:203], v[84:87], v[44:47]\n" \
  "ds_read_b128 v[76:79], v13 offset:40960\n" \
  "v_mfma_f32_16x16x32_bf16 v[52:55], a[72:75], v[84:87], v[52:55]\n" \
  "ds_read_b128 v[80:83], v13 offset:43008\n" \
  "v_mfma_f32_16x16x32_bf16 v[60:63], v[84:87], a[200:203], v[60:63]\n" \
  "v_mfma_f32_16x16x32_bf16 v[48:51], v[200:203], v[88:91], v[48:51]\n" \
  "v_mfma_f32_16x16x32_bf16 v[56:59], a[72:75], v[88:91], v[56:59]\n" \
  "v_mfma_f32_16x16x32_bf16 v[64:67], v[88:91], a[200:203], v[64:67]\n" \
  "s_waitcnt lgkmcnt(4)\n" \
  "v_mfma_f32_16x16x32_bf16 v[44:47], v[204:207], v[92:95], v[44:47]\n" \
  "ds_read_b128 v[84:87], v12 offset:45056\n" \
  "v_mfma_f32_16x16x32_bf16 v[52:55], a[76:79], v[92:95], v[52:55]\n" \
  "ds_read_b128 v[88:91], v12 offset:47104\n" \
  "v_mfma_f32_16x16x32_bf16 v[60:63], v[92:95], a[204:207], v[60:63]\n" \
  "v_mfma_f32_16x16x32_bf16 v[48:51], v[204:207], v[96:99], v[48:51]\n" \
  "v_mfma_f32_16x16x32_bf16 v[56:59], a[76:79], v[96:99], v[56:59]\n" \
  "v_mfma_f32_16x16x32_bf16 v[64:67], v[96:99], a[204:207], v[64:67]\n" \
  "s_waitcnt lgkmcnt(4)\n" \
  "v_mfma_f32_16x16x32_bf16 v[44:47], v[208:211], v[68:71], v[44:47]\n" \
  "ds_read_b128 v[92:95], v13 offset:45056\n" \
  "v_mfma_f32_16x16x32_bf16 v[52:55], a[80:83], v[68:71], v[52:55]\n" \
  "ds_read_b128 v[96:99], v13 offset:47104\n" \
  "v_mfma_f32_16x16x32_bf16 v[60:63], v[68:71], a[208:211], v[60:63]\n" \
  "v_mfma_f32_16x16x32_bf16 v[48:51], v[208:211], v[72:75], v[48:51]\n" \
  "v_mfma_f32_16x16x32_bf16 v[56:59], a[80:83], v[72:75], v[56:59]\n" \
  "v_mfma_f32_16x16x32_bf16 v[64:67], v[72:75], a[208:211], v[64:67]\n" \
  "s_waitcnt lgkmcnt(4)\n" \
  "v_mfma_f32_16x16x32_bf16 v[44:47], v[212:215], v[76:79], v[44:47]\n" \
  "ds_read_b128 v[68:71], v12 offset:49152\n" \
  "v_mfma_f32_16x16x32_bf16 v[52:55], a[84:87], v[76:79], v[52:55]\n" \
  "ds_read_b128 v[72:75], v12 offset:51200\n" \
  "v_mfma_f32_16x16x32_bf16 v[60:63], v[76:79], a[212:215], v[60:63]\n" \
  "v_mfma_f32_16x16x32_bf16 v[48:51], v[212:215], v[80:83], v[48:51]\n" \
  "s_add_u32 m0, s91, 65536\n" \
  "s_nop 0\n" \
  "buffer_load_dwordx4 v7, s[72:75], s87 offen lds\n" \
  "s_add_u32 s87, s87, 128\n" \
  "v_mfma_f32_16x16x32_bf16 v[56:59], a[84:87], v[80:83], v[56:59]\n" \
  "v_mfma_f32_16x16x32_bf16 v[64:67], v[80:83], a[212:215], v[64:67]\n" \
  "s_waitcnt lgkmcnt(4)\n" \
  "v_mfma_f32_16x16x32_bf16 v[44:47], v[216:219], v[84:87], v[44:47]\n" \
  "ds_read_b128 v[76:79], v13 offset:49152\n" \
  "v_mfma_f32_16x16x32_bf16 v[52:55], a[88:91], v[84:87], v[52:55]\n" \
  "ds_read_b128 v[80:83], v13 offset:51200\n" \
  "v_mfma_f32_16x16x32_bf16 v[60:63], v[84:87], a[216:219], v[60:63]\n" \
  "v_mfma_f32_16x16x32_bf16 v[48:51], v[216:219], v[88:91], v[48:51]\n" \
  "s_add_u32 m0, s91, 69632\n" \
  "s_nop 0\n" \
  "buffer_load_dwordx4 v7, s[72:75], s87 offen lds\n" \
  "s_add_u32 s87, s87, 128\n" \
  "v_mfma_f32_16x16x32_bf16 v[56:59], a[88:91], v[88:91], v[56:59]\n" \
  "v_mfma_f32_16x16x32_bf16 v[64:67], v[88:91], a[216:219], v[64:67]\n" \
  "s_waitcnt lgkmcnt(4)\n" \
  "v_mfma_f32_16x16x32_bf16 v[44:47], v[220:223], v[92:95], v[44:47]\n" \
  "ds_read_b128 v[84:87], v12 offset:53248\n" \
  "v_mfma_f32_16x16x32_bf16 v[52:55], a[92:95], v[92:95], v[52:55]\n" \
  "ds_read_b128 v[88:91], v12 offset:55296\n" \
  "v_mfma_f32_16x16x32_bf16 v[60:63], v[92:95], a[220:223], v[60:63]\n" \
  "v_mfma_f32_16x16x32_bf16 v[48:51], v[220:223], v[96:99], v[48:51]\n" \
  "s_add_u32 m0, s91, 73728\n" \
  "s_nop 0\n" \
  "buffer_load_dwordx4 v7, s[72:75], s87 offen lds\n" \
  "s_add_u32 s87, s87, 128\n" \
  "v_mfma_f32_16x16x32_bf16 v[56:59], a[92:95], v[96:99], v[56:59]\n" \
  "v_mfma_f32_16x16x32_bf16 v[64:67], v[96:99], a[220:223], v[64:67]\n" \
  "s_waitcnt lgkmcnt(4)\n" \
  "v_mfma_f32_16x16x32_bf16 v[44:47], v[224:227], v[68:71], v[44:47]\n" \
  "ds_read_b128 v[92:95], v13 offset:53248\n" \
  "v_mfma_f32_16x16x32_bf16 v[52:55], a[96:99], v[68:71], v[52:55]\n" \
  "ds_read_b128 v[96:99], v13 offset:55296\n" \
  "v_mfma_f32_16x16x32_bf16 v[60:63], v[68:71], a[224:227], v[60:63]\n" \
  "v_mfma_f32_16x16x32_bf16 v[48:51], v[224:227], v[72:75], v[48:51]\n" \
  "s_add_u32 m0, s91, 77824\n" \
  "s_nop 0\n" \
  "buffer_load_dwordx4 v7, s[72:75], s87 offen lds\n" \
  "s_add_u32 s87, s87, 128\n" \
  "v_mfma_f32_16x16x32_bf16 v[56:59], a[96:99], v[72:75], v[56:59]\n" \
  "v_mfma_f32_16x16x32_bf16 v[64:67], v[72:75], a[224:227], v[64:67]\n" \
  "s_waitcnt lgkmcnt(4)\n" \
  "v_mfma_f32_16x16x32_bf16 v[44:47], v[228:231], v[76:79], v[44:47]\n" \
  "ds_read_b128 v[68:71], v12 offset:57344\n" \
  "v_mfma_f32_16x16x32_bf16 v[52:55], a[100:103], v[76:79], v[52:55]\n" \
  "ds_read_b128 v[72:75], v12 offset:59392\n" \
  "v_mfma_f32_16x16x32_bf16 v[60:63], v[76:79], a[228:231], v[60:63]\n" \
  "v_mfma_f32_16x16x32_bf16 v[48:51], v[228:231], v[80:83], v[48:51]\n" \
  "s_add_u32 m0, s91, 81920\n" \
  "s_nop 0\n" \
  "buffer_load_dwordx4 v7, s[72:75], s87 offen lds\n" \
  "s_add_u32 s87, s87, 128\n" \
  "v_mfma_f32_16x16x32_bf16 v[56:59], a[100:103], v[80:83], v[56:59]\n" \
  "v_mfma_f32_16x16x32_bf16 v[64:67], v[80:83], a[228:231], v[64:67]\n" \
  "s_waitcnt lgkmcnt(4)\n" \
  "v_mfma_f32_16x16x32_bf16 v[44:47], v[232:235], v[84:87], v[44:47]\n" \
  "ds_read_b128 v[76:79], v13 offset:57344\n" \
  "v_mfma_f32_16x16x32_bf16 v[52:55], a[104:107], v[84:87], v[52:55]\n" \
  "ds_read_b128 v[80:83], v13 offset:59392\n" \
  "v_mfma_f32_16x16x32_bf16 v[60:63], v[84:87], a[232:235], v[60:63]\n" \
  "v_mfma_f32_16x16x32_bf16 v[48:51], v[232:235], v[88:91], v[48:51]\n" \
  "s_add_u32 m0, s91, 86016\n" \
  "s_nop 0\n" \
  "buffer_load_dwordx4 v7, s[72:75], s87 offen lds\n" \
  "s_add_u32 s87, s87, 128\n" \
  "v_mfma_f32_16x16x32_bf16 v[56:59], a[104:107], v[88:91], v[56:59]\n" \
  "v_mfma_f32_16x16x32_bf16 v[64:67], v[88:91], a[232:235], v[64:67]\n" \
  "s_waitcnt lgkmcnt(4)\n" \
  "v_mfma_f32_16x16x32_bf16 v[44:47], v[236:239], v[92:95], v[44:47]\n" \
  "ds_read_b128 v[84:87], v12 offset:61440\n" \
  "v_mfma_f32_16x16x32_bf16 v[52:55], a[108:111], v[92:95], v[52:55]\n" \
  "ds_read_b128 v[88:91], v12 offset:63488\n" \
  "v_mfma_f32_16x16x32_bf16 v[60:63], v[92:95], a[236:239], v[60:63]\n" \
  "v_mfma_f32_16x16x32_bf16 v[48:51], v[236:239], v[96:99], v[48:51]\n" \
  "s_add_u32 m0, s91, 90112\n" \
  "s_nop 0\n" \
  "buffer_load_dwordx4 v7, s[72:75], s87 offen lds\n" \
  "s_add_u32 s87, s87, 128\n" \
  "v_mfma_f32_16x16x32_bf16 v[56:59], a[108:111], v[96:99], v[56:59]\n" \
  "v_mfma_f32_16x16x32_bf16 v[64:67], v[96:99], a[236:239], v[64:67]\n" \
  "s_waitcnt lgkmcnt(4)\n" \
  "v_mfma_f32_16x16x32_bf16 v[44:47], v[240:243], v[68:71], v[44:47]\n" \
  "ds_read_b128 v[92:95], v13 offset:61440\n" \
  "v_mfma_f32_16x16x32_bf16 v[52:55], a[112:115], v[68:71], v[52:55]\n" \
  "ds_read_b128 v[96:99], v13 offset:63488\n" \
  "v_mfma_f32_16x16x32_bf16 v[60:63], v[68:71], a[240:243], v[60:63]\n" \
  "v_mfma_f32_16x16x32_bf16 v[48:51], v[240:243], v[72:75], v[48:51]\n" \
  "s_add_u32 m0, s91, 94208\n" \
  "s_nop 0\n" \
  "buffer_load_dwordx4 v7, s[72:75], s87 offen lds\n" \
  "s_add_u32 s87, s87, 128\n" \
  "v_mfma_f32_16x16x32_bf16 v[56:59], a[112:115], v[72:75], v[56:59]\n" \
  "v_mfma_f32_16x16x32_bf16 v[64:67], v[72:75], a[240:243], v[64:67]\n" \
  "s_waitcnt lgkmcnt(4)\n" \
  "v_mfma_f32_16x16x32_bf16 v[44:47], v[244:247], v[76:79], v[44:47]\n" \
  "ds_read_b128 v[68:71], v10 offset:0\n" \
  "v_mfma_f32_16x16x32_bf16 v[52:55], a[116:119], v[76:79], v[52:55]\n" \
  "ds_read_b128 v[72:75], v10 offset:2048\n" \
  "v_mfma_f32_16x16x32_bf16 v[60:63], v[76:79], a[244:247], v[60:63]\n" \
  "v_mfma_f32_16x16x32_bf16 v[48:51], v[244:247], v[80:83], v[48:51]\n" \
  "v_mfma_f32_16x16x32_bf16 v[56:59], a[116:119], v[80:83], v[56:59]\n" \
  "v_mfma_f32_16x16x32_bf16 v[64:67], v[80:83], a[244:247], v[64:67]\n" \
  "s_waitcnt lgkmcnt(4)\n" \
  "v_mfma_f32_16x16x32_bf16 v[44:47], v[248:251], v[84:87], v[44:47]\n" \
  "ds_read_b128 v[76:79], v11 offset:0\n" \
  "v_mfma_f32_16x16x32_bf16 v[52:55], a[120:123], v[84:87], v[52:55]\n" \
  "ds_read_b128 v[80:83], v11 offset:2048\n" \
  "v_mfma_f32_16x16x32_bf16 v[60:63], v[84:87], a[248:251], v[60:63]\n" \
  "v_mfma_f32_16x16x32_bf16 v[48:51], v[248:251], v[88:91], v[48:51]\n" \
  "v_mfma_f32_16x16x32_bf16 v[56:59], a[120:123], v[88:91], v[56:59]\n" \
  "v_mfma_f32_16x16x32_bf16 v[64:67], v[88:91], a[248:251], v[64:67]\n" \
  "s_waitcnt lgkmcnt(4)\n" \
  "v_mfma_f32_16x16x32_bf16 v[44:47], v[252:255], v[92:95], v[44:47]\n" \
  "ds_read_b128 v[84:87], v10 offset:4096\n" \
  "v_mfma_f32_16x16x32_bf16 v[52:55], a[124:127], v[92:95], v[52:55]\n" \
  "ds_read_b128 v[88:91], v10 offset:6144\n" \
  "v_mfma_f32_16x16x32_bf16 v[60:63], v[92:95], a[252:255], v[60:63]\n" \
  "v_mfma_f32_16x16x32_bf16 v[48:51], v[252:255], v[96:99], v[48:51]\n" \
  "v_mfma_f32_16x16x32_bf16 v[56:59], a[124:127], v[96:99], v[56:59]\n" \
  "v_mfma_f32_16x16x32_bf16 v[64:67], v[96:99], a[252:255], v[64:67]\n" \
  "s_branch LG_%=\n" \
  "LX0_%=:\n" \
  "s_sub_u32 s89, s85, 1\n" \
  "s_and_b32 s89, s89, 63\n" \
  "s_lshr_b32 s84, s89, 2\n" \
  "s_xnor_b32 s84, s84, %9\n" \
  "s_and_b32 s84, s84, 1\n" \
  "s_lshl_b32 s84, s84, 13\n" \
  "s_lshl_b32 s88, s89, 19\n" \
  "s_or_b32 s88, s88, s84\n" \
  "s_add_u32 s89, s88, " STR(QKV_SZ32) "\n" \
  "s_add_u32 s90, s89, " STR(QKV_SZ32) "\n" \
  "s_add_u32 s85, s85, 1\n" \
  "v_cvt_pk_bf16_f32 v116, v44, v45\n" \
  "v_cvt_pk_bf16_f32 v117, v46, v47\n" \
  "v_cvt_pk_bf16_f32 v118, v48, v49\n" \
  "v_cvt_pk_bf16_f32 v119, v50, v51\n" \
  "v_cvt_pk_bf16_f32 v120, v52, v53\n" \
  "v_cvt_pk_bf16_f32 v121, v54, v55\n" \
  "v_cvt_pk_bf16_f32 v122, v56, v57\n" \
  "v_cvt_pk_bf16_f32 v123, v58, v59\n" \
  "v_cvt_pk_bf16_f32 v124, v60, v61\n" \
  "v_cvt_pk_bf16_f32 v125, v62, v63\n" \
  "v_cvt_pk_bf16_f32 v126, v64, v65\n" \
  "v_cvt_pk_bf16_f32 v127, v66, v67\n" \
  "s_nop 1\n" \
  "v_permlane16_swap_b32 v116, v118\n" \
  "v_permlane16_swap_b32 v117, v119\n" \
  "v_permlane16_swap_b32 v120, v122\n" \
  "v_permlane16_swap_b32 v121, v123\n" \
  "buffer_store_dwordx4 v[116:119], v14, s[76:79], s88 offen sc1\n" \
  "buffer_store_dwordx4 v[120:123], v14, s[76:79], s89 offen sc1\n" \
  "buffer_store_dwordx4 v[124:127], v15, s[76:79], s90 offen sc1\n" \
  "s_branch LZ_%=\n" \
  "LX1_%=:\n" \
  "s_sub_u32 s89, s85, 1\n" \
  "s_and_b32 s89, s89, 63\n" \
  "s_lshr_b32 s84, s89, 2\n" \
  "s_xnor_b32 s84, s84, %9\n" \
  "s_and_b32 s84, s84, 1\n" \
  "s_lshl_b32 s84, s84, 13\n" \
  "s_lshl_b32 s88, s89, 19\n" \
  "s_or_b32 s88, s88, s84\n" \
  "s_add_u32 s89, s88, " STR(QKV_SZ32) "\n" \
  "s_add_u32 s90, s89, " STR(QKV_SZ32) "\n" \
  "s_add_u32 s85, s85, 1\n" \
  "v_cvt_pk_bf16_f32 v116, v20, v21\n" \
  "v_cvt_pk_bf16_f32 v117, v22, v23\n" \
  "v_cvt_pk_bf16_f32 v118, v24, v25\n" \
  "v_cvt_pk_bf16_f32 v119, v26, v27\n" \
  "v_cvt_pk_bf16_f32 v120, v28, v29\n" \
  "v_cvt_pk_bf16_f32 v121, v30, v31\n" \
  "v_cvt_pk_bf16_f32 v122, v32, v33\n" \
  "v_cvt_pk_bf16_f32 v123, v34, v35\n" \
  "v_cvt_pk_bf16_f32 v124, v36, v37\n" \
  "v_cvt_pk_bf16_f32 v125, v38, v39\n" \
  "v_cvt_pk_bf16_f32 v126, v40, v41\n" \
  "v_cvt_pk_bf16_f32 v127, v42, v43\n" \
  "s_nop 1\n" \
  "v_permlane16_swap_b32 v116, v118\n" \
  "v_permlane16_swap_b32 v117, v119\n" \
  "v_permlane16_swap_b32 v120, v122\n" \
  "v_permlane16_swap_b32 v121, v123\n" \
  "buffer_store_dwordx4 v[116:119], v14, s[76:79], s88 offen sc1\n" \
  "buffer_store_dwordx4 v[120:123], v14, s[76:79], s89 offen sc1\n" \
  "buffer_store_dwordx4 v[124:127], v15, s[76:79], s90 offen sc1\n" \
  "s_branch LZ_%=\n" \
  "LZ_%=:\n" \
  "s_waitcnt vmcnt(0) lgkmcnt(0)\n" \
  "s_barrier\n" \
  "buffer_store_dword v17, v16, s[80:83], 0 offen sc1\n" \
  "s_waitcnt vmcnt(0)\n" \
  ""

#define XO_ASM \
  "s_mov_b64 s[68:69], %0\n" \
  "s_and_b32 s69, s69, 0xffff\n" \
  "s_mov_b32 s70, 8388608\n" \
  "s_mov_b32 s71, 0x00020000\n" \
  "s_mov_b64 s[72:73], %1\n" \
  "s_and_b32 s73, s73, 0xffff\n" \
  "s_mov_b32 s74, " STR(QKV_SZ32) "\n" \
  "s_mov_b32 s75, 0x00020000\n" \
  "s_mov_b64 s[76:77], %2\n" \
  "s_and_b32 s77, s77, 0xffff\n" \
  "s_mov_b32 s78, 8388608\n" \
  "s_mov_b32 s79, 0x00020000\n" \
  "s_mov_b64 s[80:81], %3\n" \
  "s_and_b32 s81, s81, 0xffff\n" \
  "s_mov_b32 s82, " STR(CTRL_BYTES) "\n" \
  "s_mov_b32 s83, 0x00020000\n" \
  "v_mbcnt_lo_u32_b32 v4, -1, 0\n" \
  "v_mbcnt_hi_u32_b32 v4, -1, v4\n" \
  "v_and_b32 v20, 15, v4\n" \
  "v_lshrrev_b32 v21, 4, v4\n" \
  "v_lshlrev_b32 v6, 4, v4\n" \
  "s_lshl_b32 s86, %5, 15\n" \
  "s_add_u32 s86, s86, %6\n" \
  "s_lshl_b32 s90, %5, 21\n" \
  "v_lshrrev_b32 v5, 3, v4\n" \
  "v_and_b32 v15, 7, v4\n" \
  "v_lshlrev_b32 v7, 11, v5\n" \
  "v_lshl_add_u32 v7, v15, 4, v7\n" \
  "v_lshlrev_b32 v5, 5, v21\n" \
  "v_xor_b32 v7, v7, v5\n" \
  "s_add_u32 m0, s86, 0\n" \
  "s_nop 0\n" \
  "buffer_load_dwordx4 v7, s[68:71], s90 offen lds\n" \
  "s_add_u32 s90, s90, 16384\n" \
  "s_add_u32 m0, s86, 1024\n" \
  "s_nop 0\n" \
  "buffer_load_dwordx4 v7, s[68:71], s90 offen lds\n" \
  "s_add_u32 s90, s90, 16384\n" \
  "s_add_u32 m0, s86, 2048\n" \
  "s_nop 0\n" \
  "buffer_load_dwordx4 v7, s[68:71], s90 offen lds\n" \
  "s_add_u32 s90, s90, 16384\n" \
  "s_add_u32 m0, s86, 3072\n" \
  "s_nop 0\n" \
  "buffer_load_dwordx4 v7, s[68:71], s90 offen lds\n" \
  "s_add_u32 s90, s90, 16384\n" \
  "s_add_u32 m0, s86, 4096\n" \
  "s_nop 0\n" \
  "buffer_load_dwordx4 v7, s[68:71], s90 offen lds\n" \
  "s_add_u32 s90, s90, 16384\n" \
  "s_add_u32 m0, s86, 5120\n" \
  "s_nop 0\n" \
  "buffer_load_dwordx4 v7, s[68:71], s90 offen lds\n" \
  "s_add_u32 s90, s90, 16384\n" \
  "s_add_u32 m0, s86, 6144\n" \
  "s_nop 0\n" \
  "buffer_load_dwordx4 v7, s[68:71], s90 offen lds\n" \
  "s_add_u32 s90, s90, 16384\n" \
  "s_add_u32 m0, s86, 7168\n" \
  "s_nop 0\n" \
  "buffer_load_dwordx4 v7, s[68:71], s90 offen lds\n" \
  "s_add_u32 s90, s90, 16384\n" \
  "s_add_u32 m0, s86, 8192\n" \
  "s_nop 0\n" \
  "buffer_load_dwordx4 v7, s[68:71], s90 offen lds\n" \
  "s_add_u32 s90, s90, 16384\n" \
  "s_add_u32 m0, s86, 9216\n" \
  "s_nop 0\n" \
  "buffer_load_dwordx4 v7, s[68:71], s90 offen lds\n" \
  "s_add_u32 s90, s90, 16384\n" \
  "s_add_u32 m0, s86, 10240\n" \
  "s_nop 0\n" \
  "buffer_load_dwordx4 v7, s[68:71], s90 offen lds\n" \
  "s_add_u32 s90, s90, 16384\n" \
  "s_add_u32 m0, s86, 11264\n" \
  "s_nop 0\n" \
  "buffer_load_dwordx4 v7, s[68:71], s90 offen lds\n" \
  "s_add_u32 s90, s90, 16384\n" \
  "s_add_u32 m0, s86, 12288\n" \
  "s_nop 0\n" \
  "buffer_load_dwordx4 v7, s[68:71], s90 offen lds\n" \
  "s_add_u32 s90, s90, 16384\n" \
  "s_add_u32 m0, s86, 13312\n" \
  "s_nop 0\n" \
  "buffer_load_dwordx4 v7, s[68:71], s90 offen lds\n" \
  "s_add_u32 s90, s90, 16384\n" \
  "s_add_u32 m0, s86, 14336\n" \
  "s_nop 0\n" \
  "buffer_load_dwordx4 v7, s[68:71], s90 offen lds\n" \
  "s_add_u32 s90, s90, 16384\n" \
  "s_add_u32 m0, s86, 15360\n" \
  "s_nop 0\n" \
  "buffer_load_dwordx4 v7, s[68:71], s90 offen lds\n" \
  "s_add_u32 s90, s90, 16384\n" \
  "s_add_u32 m0, s86, 16384\n" \
  "s_nop 0\n" \
  "buffer_load_dwordx4 v7, s[68:71], s90 offen lds\n" \
  "s_add_u32 s90, s90, 16384\n" \
  "s_add_u32 m0, s86, 17408\n" \
  "s_nop 0\n" \
  "buffer_load_dwordx4 v7, s[68:71], s90 offen lds\n" \
  "s_add_u32 s90, s90, 16384\n" \
  "s_add_u32 m0, s86, 18432\n" \
  "s_nop 0\n" \
  "buffer_load_dwordx4 v7, s[68:71], s90 offen lds\n" \
  "s_add_u32 s90, s90, 16384\n" \
  "s_add_u32 m0, s86, 19456\n" \
  "s_nop 0\n" \
  "buffer_load_dwordx4 v7, s[68:71], s90 offen lds\n" \
  "s_add_u32 s90, s90, 16384\n" \
  "s_add_u32 m0, s86, 20480\n" \
  "s_nop 0\n" \
  "buffer_load_dwordx4 v7, s[68:71], s90 offen lds\n" \
  "s_add_u32 s90, s90, 16384\n" \
  "s_add_u32 m0, s86, 21504\n" \
  "s_nop 0\n" \
  "buffer_load_dwordx4 v7, s[68:71], s90 offen lds\n" \
  "s_add_u32 s90, s90, 16384\n" \
  "s_add_u32 m0, s86, 22528\n" \
  "s_nop 0\n" \
  "buffer_load_dwordx4 v7, s[68:71], s90 offen lds\n" \
  "s_add_u32 s90, s90, 16384\n" \
  "s_add_u32 m0, s86, 23552\n" \
  "s_nop 0\n" \
  "buffer_load_dwordx4 v7, s[68:71], s90 offen lds\n" \
  "s_add_u32 s90, s90, 16384\n" \
  "s_add_u32 m0, s86, 24576\n" \
  "s_nop 0\n" \
  "buffer_load_dwordx4 v7, s[68:71], s90 offen lds\n" \
  "s_add_u32 s90, s90, 16384\n" \
  "s_add_u32 m0, s86, 25600\n" \
  "s_nop 0\n" \
  "buffer_load_dwordx4 v7, s[68:71], s90 offen lds\n" \
  "s_add_u32 s90, s90, 16384\n" \
  "s_add_u32 m0, s86, 26624\n" \
  "s_nop 0\n" \
  "buffer_load_dwordx4 v7, s[68:71], s90 offen lds\n" \
  "s_add_u32 s90, s90, 16384\n" \
  "s_add_u32 m0, s86, 27648\n" \
  "s_nop 0\n" \
  "buffer_load_dwordx4 v7, s[68:71], s90 offen lds\n" \
  "s_add_u32 s90, s90, 16384\n" \
  "s_add_u32 m0, s86, 28672\n" \
  "s_nop 0\n" \
  "buffer_load_dwordx4 v7, s[68:71], s90 offen lds\n" \
  "s_add_u32 s90, s90, 16384\n" \
  "s_add_u32 m0, s86, 29696\n" \
  "s_nop 0\n" \
  "buffer_load_dwordx4 v7, s[68:71], s90 offen lds\n" \
  "s_add_u32 s90, s90, 16384\n" \
  "s_add_u32 m0, s86, 30720\n" \
  "s_nop 0\n" \
  "buffer_load_dwordx4 v7, s[68:71], s90 offen lds\n" \
  "s_add_u32 s90, s90, 16384\n" \
  "s_add_u32 m0, s86, 31744\n" \
  "s_nop 0\n" \
  "buffer_load_dwordx4 v7, s[68:71], s90 offen lds\n" \
  "s_add_u32 s90, s90, 16384\n" \
  "v_lshrrev_b32 v5, 2, v20\n" \
  "v_lshl_add_u32 v5, v21, 2, v5\n" \
  "v_lshlrev_b32 v15, 7, v5\n" \
  "v_lshrrev_b32 v5, 1, v5\n" \
  "v_and_b32 v5, 3, v5\n" \
  "v_lshl_add_u32 v15, v5, 5, v15\n" \
  "v_and_b32 v5, 3, v4\n" \
  "v_lshl_add_u32 v15, v5, 3, v15\n" \
  "s_add_u32 s84, %5, 0\n" \
  "s_and_b32 s84, s84, 3\n" \
  "s_lshl_b32 s85, s84, 5\n" \
  "v_xor_b32 v5, s85, v15\n" \
  "v_add_u32 v16, s86, v5\n" \
  "s_mul_i32 s84, s84, 3072\n" \
  "s_add_u32 s84, s84, 131072\n" \
  "s_add_u32 s84, s84, %6\n" \
  "v_add_u32 v11, s84, v6\n" \
  "s_add_u32 s84, %5, 1\n" \
  "s_and_b32 s84, s84, 3\n" \
  "s_lshl_b32 s85, s84, 5\n" \
  "v_xor_b32 v5, s85, v15\n" \
  "v_add_u32 v17, s86, v5\n" \
  "s_mul_i32 s84, s84, 3072\n" \
  "s_add_u32 s84, s84, 131072\n" \
  "s_add_u32 s84, s84, %6\n" \
  "v_add_u32 v12, s84, v6\n" \
  "s_add_u32 s84, %5, 2\n" \
  "s_and_b32 s84, s84, 3\n" \
  "s_lshl_b32 s85, s84, 5\n" \
  "v_xor_b32 v5, s85, v15\n" \
  "v_add_u32 v18, s86, v5\n" \
  "s_mul_i32 s84, s84, 3072\n" \
  "s_add_u32 s84, s84, 131072\n" \
  "s_add_u32 s84, s84, %6\n" \
  "v_add_u32 v13, s84, v6\n" \
  "s_add_u32 s84, %5, 3\n" \
  "s_and_b32 s84, s84, 3\n" \
  "s_lshl_b32 s85, s84, 5\n" \
  "v_xor_b32 v5, s85, v15\n" \
  "v_add_u32 v19, s86, v5\n" \
  "s_mul_i32 s84, s84, 3072\n" \
  "s_add_u32 s84, s84, 131072\n" \
  "s_add_u32 s84, s84, %6\n" \
  "v_add_u32 v14, s84, v6\n" \
  "s_and_b32 s84, %4, 1\n" \
  "s_lshl_b32 s96, s84, 18\n" \
  "s_lshl_b32 s85, %5, 16\n" \
  "s_add_u32 s96, s96, s85\n" \
  "s_lshl_b32 s88, s84, 16\n" \
  "s_lshr_b32 s84, %4, 1\n" \
  "s_lshl_b32 s84, s84, 7\n" \
  "s_add_u32 s88, s88, s84\n" \
  "s_lshl_b32 s84, %5, 5\n" \
  "s_add_u32 s88, s88, s84\n" \
  "v_lshlrev_b32 v10, 12, v20\n" \
  "v_lshl_add_u32 v10, v21, 3, v10\n" \
  "v_and_b32 v8, 7, v4\n" \
  "v_lshlrev_b32 v8, 7, v8\n" \
  "s_lshl_b32 s84, %5, 10\n" \
  "s_add_u32 s84, s84, " STR(CW_XPROG) "*4\n" \
  "v_add_u32 v8, s84, v8\n" \
  "s_mov_b32 s91, 0\n" \
  "s_mov_b32 s89, 0\n" \
  "s_lshr_b32 s84, s89, 2\n" \
  "s_xnor_b32 s84, s84, %7\n" \
  "s_and_b32 s84, s84, 1\n" \
  "s_lshl_b32 s84, s84, 13\n" \
  "s_lshl_b32 s87, s89, 19\n" \
  "s_or_b32 s87, s87, s84\n" \
  "s_add_u32 s87, s87, s96\n" \
  "s_waitcnt vmcnt(16)\n" \
  "ds_read_b64_tr_b16 v[128:129], v16 offset:0\n" \
  "ds_read_b64_tr_b16 v[130:131], v16 offset:2048\n" \
  "ds_read_b64_tr_b16 v[132:133], v17 offset:0\n" \
  "ds_read_b64_tr_b16 v[134:135], v17 offset:2048\n" \
  "ds_read_b64_tr_b16 v[136:137], v18 offset:0\n" \
  "ds_read_b64_tr_b16 v[138:139], v18 offset:2048\n" \
  "ds_read_b64_tr_b16 v[140:141], v19 offset:0\n" \
  "ds_read_b64_tr_b16 v[142:143], v19 offset:2048\n" \
  "ds_read_b64_tr_b16 v[144:145], v16 offset:4096\n" \
  "ds_read_b64_tr_b16 v[146:147], v16 offset:6144\n" \
  "ds_read_b64_tr_b16 v[148:149], v17 offset:4096\n" \
  "ds_read_b64_tr_b16 v[150:151], v17 offset:6144\n" \
  "ds_read_b64_tr_b16 v[152:153], v18 offset:4096\n" \
  "ds_read_b64_tr_b16 v[154:155], v18 offset:6144\n" \
  "ds_read_b64_tr_b16 v[156:157], v19 offset:4096\n" \
  "ds_read_b64_tr_b16 v[158:159], v19 offset:6144\n" \
  "ds_read_b64_tr_b16 v[160:161], v16 offset:8192\n" \
  "ds_read_b64_tr_b16 v[162:163], v16 offset:10240\n" \
  "ds_read_b64_tr_b16 v[164:165], v17 offset:8192\n" \
  "ds_read_b64_tr_b16 v[166:167], v17 offset:10240\n" \
  "ds_read_b64_tr_b16 v[168:169], v18 offset:8192\n" \
  "ds_read_b64_tr_b16 v[170:171], v18 offset:10240\n" \
  "ds_read_b64_tr_b16 v[172:173], v19 offset:8192\n" \
  "ds_read_b64_tr_b16 v[174:175], v19 offset:10240\n" \
  "ds_read_b64_tr_b16 v[176:177], v16 offset:12288\n" \
  "ds_read_b64_tr_b16 v[178:179], v16 offset:14336\n" \
  "ds_read_b64_tr_b16 v[180:181], v17 offset:12288\n" \
  "ds_read_b64_tr_b16 v[182:183], v17 offset:14336\n" \
  "ds_read_b64_tr_b16 v[184:185], v18 offset:12288\n" \
  "ds_read_b64_tr_b16 v[186:187], v18 offset:14336\n" \
  "ds_read_b64_tr_b16 v[188:189], v19 offset:12288\n" \
  "ds_read_b64_tr_b16 v[190:191], v19 offset:14336\n" \
  "s_waitcnt lgkmcnt(0)\n" \
  "s_add_u32 m0, s86, 0\n" \
  "s_nop 0\n" \
  "buffer_load_dwordx4 v7, s[68:71], s90 offen lds\n" \
  "s_add_u32 s90, s90, 16384\n" \
  "s_add_u32 m0, s86, 1024\n" \
  "s_nop 0\n" \
  "buffer_load_dwordx4 v7, s[68:71], s90 offen lds\n" \
  "s_add_u32 s90, s90, 16384\n" \
  "s_add_u32 m0, s86, 2048\n" \
  "s_nop 0\n" \
  "buffer_load_dwordx4 v7, s[68:71], s90 offen lds\n" \
  "s_add_u32 s90, s90, 16384\n" \
  "s_add_u32 m0, s86, 3072\n" \
  "s_nop 0\n" \
  "buffer_load_dwordx4 v7, s[68:71], s90 offen lds\n" \
  "s_add_u32 s90, s90, 16384\n" \
  "s_add_u32 m0, s86, 4096\n" \
  "s_nop 0\n" \
  "buffer_load_dwordx4 v7, s[68:71], s90 offen lds\n" \
  "s_add_u32 s90, s90, 16384\n" \
  "s_add_u32 m0, s86, 5120\n" \
  "s_nop 0\n" \
  "buffer_load_dwordx4 v7, s[68:71], s90 offen lds\n" \
  "s_add_u32 s90, s90, 16384\n" \
  "s_add_u32 m0, s86, 6144\n" \
  "s_nop 0\n" \
  "buffer_load_dwordx4 v7, s[68:71], s90 offen lds\n" \
  "s_add_u32 s90, s90, 16384\n" \
  "s_add_u32 m0, s86, 7168\n" \
  "s_nop 0\n" \
  "buffer_load_dwordx4 v7, s[68:71], s90 offen lds\n" \
  "s_add_u32 s90, s90, 16384\n" \
  "s_add_u32 m0, s86, 8192\n" \
  "s_nop 0\n" \
  "buffer_load_dwordx4 v7, s[68:71], s90 offen lds\n" \
  "s_add_u32 s90, s90, 16384\n" \
  "s_add_u32 m0, s86, 9216\n" \
  "s_nop 0\n" \
  "buffer_load_dwordx4 v7, s[68:71], s90 offen lds\n" \
  "s_add_u32 s90, s90, 16384\n" \
  "s_add_u32 m0, s86, 10240\n" \
  "s_nop 0\n" \
  "buffer_load_dwordx4 v7, s[68:71], s90 offen lds\n" \
  "s_add_u32 s90, s90, 16384\n" \
  "s_add_u32 m0, s86, 11264\n" \
  "s_nop 0\n" \
  "buffer_load_dwordx4 v7, s[68:71], s90 offen lds\n" \
  "s_add_u32 s90, s90, 16384\n" \
  "s_add_u32 m0, s86, 12288\n" \
  "s_nop 0\n" \
  "buffer_load_dwordx4 v7, s[68:71], s90 offen lds\n" \
  "s_add_u32 s90, s90, 16384\n" \
  "s_add_u32 m0, s86, 13312\n" \
  "s_nop 0\n" \
  "buffer_load_dwordx4 v7, s[68:71], s90 offen lds\n" \
  "s_add_u32 s90, s90, 16384\n" \
  "s_add_u32 m0, s86, 14336\n" \
  "s_nop 0\n" \
  "buffer_load_dwordx4 v7, s[68:71], s90 offen lds\n" \
  "s_add_u32 s90, s90, 16384\n" \
  "s_add_u32 m0, s86, 15360\n" \
  "s_nop 0\n" \
  "buffer_load_dwordx4 v7, s[68:71], s90 offen lds\n" \
  "s_add_u32 s90, s90, 16384\n" \
  "s_waitcnt vmcnt(16)\n" \
  "ds_read_b64_tr_b16 v[192:193], v16 offset:16384\n" \
  "ds_read_b64_tr_b16 v[194:195], v16 offset:18432\n" \
  "ds_read_b64_tr_b16 v[196:197], v17 offset:16384\n" \
  "ds_read_b64_tr_b16 v[198:199], v17 offset:18432\n" \
  "ds_read_b64_tr_b16 v[200:201], v18 offset:16384\n" \
  "ds_read_b64_tr_b16 v[202:203], v18 offset:18432\n" \
  "ds_read_b64_tr_b16 v[204:205], v19 offset:16384\n" \
  "ds_read_b64_tr_b16 v[206:207], v19 offset:18432\n" \
  "ds_read_b64_tr_b16 v[208:209], v16 offset:20480\n" \
  "ds_read_b64_tr_b16 v[210:211], v16 offset:22528\n" \
  "ds_read_b64_tr_b16 v[212:213], v17 offset:20480\n" \
  "ds_read_b64_tr_b16 v[214:215], v17 offset:22528\n" \
  "ds_read_b64_tr_b16 v[216:217], v18 offset:20480\n" \
  "ds_read_b64_tr_b16 v[218:219], v18 offset:22528\n" \
  "ds_read_b64_tr_b16 v[220:221], v19 offset:20480\n" \
  "ds_read_b64_tr_b16 v[222:223], v19 offset:22528\n" \
  "ds_read_b64_tr_b16 v[224:225], v16 offset:24576\n" \
  "ds_read_b64_tr_b16 v[226:227], v16 offset:26624\n" \
  "ds_read_b64_tr_b16 v[228:229], v17 offset:24576\n" \
  "ds_read_b64_tr_b16 v[230:231], v17 offset:26624\n" \
  "ds_read_b64_tr_b16 v[232:233], v18 offset:24576\n" \
  "ds_read_b64_tr_b16 v[234:235], v18 offset:26624\n" \
  "ds_read_b64_tr_b16 v[236:237], v19 offset:24576\n" \
  "ds_read_b64_tr_b16 v[238:239], v19 offset:26624\n" \
  "ds_read_b64_tr_b16 v[240:241], v16 offset:28672\n" \
  "ds_read_b64_tr_b16 v[242:243], v16 offset:30720\n" \
  "ds_read_b64_tr_b16 v[244:245], v17 offset:28672\n" \
  "ds_read_b64_tr_b16 v[246:247], v17 offset:30720\n" \
  "ds_read_b64_tr_b16 v[248:249], v18 offset:28672\n" \
  "ds_read_b64_tr_b16 v[250:251], v18 offset:30720\n" \
  "ds_read_b64_tr_b16 v[252:253], v19 offset:28672\n" \
  "ds_read_b64_tr_b16 v[254:255], v19 offset:30720\n" \
  "s_waitcnt lgkmcnt(0)\n" \
  "s_add_u32 m0, s86, 16384\n" \
  "s_nop 0\n" \
  "buffer_load_dwordx4 v7, s[68:71], s90 offen lds\n" \
  "s_add_u32 s90, s90, 16384\n" \
  "s_add_u32 m0, s86, 17408\n" \
  "s_nop 0\n" \
  "buffer_load_dwordx4 v7, s[68:71], s90 offen lds\n" \
  "s_add_u32 s90, s90, 16384\n" \
  "s_add_u32 m0, s86, 18432\n" \
  "s_nop 0\n" \
  "buffer_load_dwordx4 v7, s[68:71], s90 offen lds\n" \
  "s_add_u32 s90, s90, 16384\n" \
  "s_add_u32 m0, s86, 19456\n" \
  "s_nop 0\n" \
  "buffer_load_dwordx4 v7, s[68:71], s90 offen lds\n" \
  "s_add_u32 s90, s90, 16384\n" \
  "s_add_u32 m0, s86, 20480\n" \
  "s_nop 0\n" \
  "buffer_load_dwordx4 v7, s[68:71], s90 offen lds\n" \
  "s_add_u32 s90, s90, 16384\n" \
  "s_add_u32 m0, s86, 21504\n" \
  "s_nop 0\n" \
  "buffer_load_dwordx4 v7, s[68:71], s90 offen lds\n" \
  "s_add_u32 s90, s90, 16384\n" \
  "s_add_u32 m0, s86, 22528\n" \
  "s_nop 0\n" \
  "buffer_load_dwordx4 v7, s[68:71], s90 offen lds\n" \
  "s_add_u32 s90, s90, 16384\n" \
  "s_add_u32 m0, s86, 23552\n" \
  "s_nop 0\n" \
  "buffer_load_dwordx4 v7, s[68:71], s90 offen lds\n" \
  "s_add_u32 s90, s90, 16384\n" \
  "s_add_u32 m0, s86, 24576\n" \
  "s_nop 0\n" \
  "buffer_load_dwordx4 v7, s[68:71], s90 offen lds\n" \
  "s_add_u32 s90, s90, 16384\n" \
  "s_add_u32 m0, s86, 25600\n" \
  "s_nop 0\n" \
  "buffer_load_dwordx4 v7, s[68:71], s90 offen lds\n" \
  "s_add_u32 s90, s90, 16384\n" \
  "s_add_u32 m0, s86, 26624\n" \
  "s_nop 0\n" \
  "buffer_load_dwordx4 v7, s[68:71], s90 offen lds\n" \
  "s_add_u32 s90, s90, 16384\n" \
  "s_add_u32 m0, s86, 27648\n" \
  "s_nop 0\n" \
  "buffer_load_dwordx4 v7, s[68:71], s90 offen lds\n" \
  "s_add_u32 s90, s90, 16384\n" \
  "s_add_u32 m0, s86, 28672\n" \
  "s_nop 0\n" \
  "buffer_load_dwordx4 v7, s[68:71], s90 offen lds\n" \
  "s_add_u32 s90, s90, 16384\n" \
  "s_add_u32 m0, s86, 29696\n" \
  "s_nop 0\n" \
  "buffer_load_dwordx4 v7, s[68:71], s90 offen lds\n" \
  "s_add_u32 s90, s90, 16384\n" \
  "s_add_u32 m0, s86, 30720\n" \
  "s_nop 0\n" \
  "buffer_load_dwordx4 v7, s[68:71], s90 offen lds\n" \
  "s_add_u32 s90, s90, 16384\n" \
  "s_add_u32 m0, s86, 31744\n" \
  "s_nop 0\n" \
  "buffer_load_dwordx4 v7, s[68:71], s90 offen lds\n" \
  "s_add_u32 s90, s90, 16384\n" \
  "s_waitcnt vmcnt(16)\n" \
  "ds_read_b64_tr_b16 a[0:1], v16 offset:0\n" \
  "ds_read_b64_tr_b16 a[2:3], v16 offset:2048\n" \
  "ds_read_b64_tr_b16 a[4:5], v17 offset:0\n" \
  "ds_read_b64_tr_b16 a[6:7], v17 offset:2048\n" \
  "ds_read_b64_tr_b16 a[8:9], v18 offset:0\n" \
  "ds_read_b64_tr_b16 a[10:11], v18 offset:2048\n" \
  "ds_read_b64_tr_b16 a[12:13], v19 offset:0\n" \
  "ds_read_b64_tr_b16 a[14:15], v19 offset:2048\n" \
  "ds_read_b64_tr_b16 a[16:17], v16 offset:4096\n" \
  "ds_read_b64_tr_b16 a[18:19], v16 offset:6144\n" \
  "ds_read_b64_tr_b16 a[20:21], v17 offset:4096\n" \
  "ds_read_b64_tr_b16 a[22:23], v17 offset:6144\n" \
  "ds_read_b64_tr_b16 a[24:25], v18 offset:4096\n" \
  "ds_read_b64_tr_b16 a[26:27], v18 offset:6144\n" \
  "ds_read_b64_tr_b16 a[28:29], v19 offset:4096\n" \
  "ds_read_b64_tr_b16 a[30:31], v19 offset:6144\n" \
  "ds_read_b64_tr_b16 a[32:33], v16 offset:8192\n" \
  "ds_read_b64_tr_b16 a[34:35], v16 offset:10240\n" \
  "ds_read_b64_tr_b16 a[36:37], v17 offset:8192\n" \
  "ds_read_b64_tr_b16 a[38:39], v17 offset:10240\n" \
  "ds_read_b64_tr_b16 a[40:41], v18 offset:8192\n" \
  "ds_read_b64_tr_b16 a[42:43], v18 offset:10240\n" \
  "ds_read_b64_tr_b16 a[44:45], v19 offset:8192\n" \
  "ds_read_b64_tr_b16 a[46:47], v19 offset:10240\n" \
  "ds_read_b64_tr_b16 a[48:49], v16 offset:12288\n" \
  "ds_read_b64_tr_b16 a[50:51], v16 offset:14336\n" \
  "ds_read_b64_tr_b16 a[52:53], v17 offset:12288\n" \
  "ds_read_b64_tr_b16 a[54:55], v17 offset:14336\n" \
  "ds_read_b64_tr_b16 a[56:57], v18 offset:12288\n" \
  "ds_read_b64_tr_b16 a[58:59], v18 offset:14336\n" \
  "ds_read_b64_tr_b16 a[60:61], v19 offset:12288\n" \
  "ds_read_b64_tr_b16 a[62:63], v19 offset:14336\n" \
  "s_waitcnt lgkmcnt(0)\n" \
  "s_add_u32 m0, s86, 0\n" \
  "s_nop 0\n" \
  "buffer_load_dwordx4 v7, s[68:71], s90 offen lds\n" \
  "s_add_u32 s90, s90, 16384\n" \
  "s_add_u32 m0, s86, 1024\n" \
  "s_nop 0\n" \
  "buffer_load_dwordx4 v7, s[68:71], s90 offen lds\n" \
  "s_add_u32 s90, s90, 16384\n" \
  "s_add_u32 m0, s86, 2048\n" \
  "s_nop 0\n" \
  "buffer_load_dwordx4 v7, s[68:71], s90 offen lds\n" \
  "s_add_u32 s90, s90, 16384\n" \
  "s_add_u32 m0, s86, 3072\n" \
  "s_nop 0\n" \
  "buffer_load_dwordx4 v7, s[68:71], s90 offen lds\n" \
  "s_add_u32 s90, s90, 16384\n" \
  "s_add_u32 m0, s86, 4096\n" \
  "s_nop 0\n" \
  "buffer_load_dwordx4 v7, s[68:71], s90 offen lds\n" \
  "s_add_u32 s90, s90, 16384\n" \
  "s_add_u32 m0, s86, 5120\n" \
  "s_nop 0\n" \
  "buffer_load_dwordx4 v7, s[68:71], s90 offen lds\n" \
  "s_add_u32 s90, s90, 16384\n" \
  "s_add_u32 m0, s86, 6144\n" \
  "s_nop 0\n" \
  "buffer_load_dwordx4 v7, s[68:71], s90 offen lds\n" \
  "s_add_u32 s90, s90, 16384\n" \
  "s_add_u32 m0, s86, 7168\n" \
  "s_nop 0\n" \
  "buffer_load_dwordx4 v7, s[68:71], s90 offen lds\n" \
  "s_add_u32 s90, s90, 16384\n" \
  "s_add_u32 m0, s86, 8192\n" \
  "s_nop 0\n" \
  "buffer_load_dwordx4 v7, s[68:71], s90 offen lds\n" \
  "s_add_u32 s90, s90, 16384\n" \
  "s_add_u32 m0, s86, 9216\n" \
  "s_nop 0\n" \
  "buffer_load_dwordx4 v7, s[68:71], s90 offen lds\n" \
  "s_add_u32 s90, s90, 16384\n" \
  "s_add_u32 m0, s86, 10240\n" \
  "s_nop 0\n" \
  "buffer_load_dwordx4 v7, s[68:71], s90 offen lds\n" \
  "s_add_u32 s90, s90, 16384\n" \
  "s_add_u32 m0, s86, 11264\n" \
  "s_nop 0\n" \
  "buffer_load_dwordx4 v7, s[68:71], s90 offen lds\n" \
  "s_add_u32 s90, s90, 16384\n" \
  "s_add_u32 m0, s86, 12288\n" \
  "s_nop 0\n" \
  "buffer_load_dwordx4 v7, s[68:71], s90 offen lds\n" \
  "s_add_u32 s90, s90, 16384\n" \
  "s_add_u32 m0, s86, 13312\n" \
  "s_nop 0\n" \
  "buffer_load_dwordx4 v7, s[68:71], s90 offen lds\n" \
  "s_add_u32 s90, s90, 16384\n" \
  "s_add_u32 m0, s86, 14336\n" \
  "s_nop 0\n" \
  "buffer_load_dwordx4 v7, s[68:71], s90 offen lds\n" \
  "s_add_u32 s90, s90, 16384\n" \
  "s_add_u32 m0, s86, 15360\n" \
  "s_nop 0\n" \
  "buffer_load_dwordx4 v7, s[68:71], s90 offen lds\n" \
  "s_add_u32 s90, s90, 16384\n" \
  "s_waitcnt vmcnt(16)\n" \
  "ds_read_b64_tr_b16 a[64:65], v16 offset:16384\n" \
  "ds_read_b64_tr_b16 a[66:67], v16 offset:18432\n" \
  "ds_read_b64_tr_b16 a[68:69], v17 offset:16384\n" \
  "ds_read_b64_tr_b16 a[70:71], v17 offset:18432\n" \
  "ds_read_b64_tr_b16 a[72:73], v18 offset:16384\n" \
  "ds_read_b64_tr_b16 a[74:75], v18 offset:18432\n" \
  "ds_read_b64_tr_b16 a[76:77], v19 offset:16384\n" \
  "ds_read_b64_tr_b16 a[78:79], v19 offset:18432\n" \
  "ds_read_b64_tr_b16 a[80:81], v16 offset:20480\n" \
  "ds_read_b64_tr_b16 a[82:83], v16 offset:22528\n" \
  "ds_read_b64_tr_b16 a[84:85], v17 offset:20480\n" \
  "ds_read_b64_tr_b16 a[86:87], v17 offset:22528\n" \
  "ds_read_b64_tr_b16 a[88:89], v18 offset:20480\n" \
  "ds_read_b64_tr_b16 a[90:91], v18 offset:22528\n" \
  "ds_read_b64_tr_b16 a[92:93], v19 offset:20480\n" \
  "ds_read_b64_tr_b16 a[94:95], v19 offset:22528\n" \
  "ds_read_b64_tr_b16 a[96:97], v16 offset:24576\n" \
  "ds_read_b64_tr_b16 a[98:99], v16 offset:26624\n" \
  "ds_read_b64_tr_b16 a[100:101], v17 offset:24576\n" \
  "ds_read_b64_tr_b16 a[102:103], v17 offset:26624\n" \
  "ds_read_b64_tr_b16 a[104:105], v18 offset:24576\n" \
  "ds_read_b64_tr_b16 a[106:107], v18 offset:26624\n" \
  "ds_read_b64_tr_b16 a[108:109], v19 offset:24576\n" \
  "ds_read_b64_tr_b16 a[110:111], v19 offset:26624\n" \
  "ds_read_b64_tr_b16 a[112:113], v16 offset:28672\n" \
  "ds_read_b64_tr_b16 a[114:115], v16 offset:30720\n" \
  "ds_read_b64_tr_b16 a[116:117], v17 offset:28672\n" \
  "ds_read_b64_tr_b16 a[118:119], v17 offset:30720\n" \
  "ds_read_b64_tr_b16 a[120:121], v18 offset:28672\n" \
  "ds_read_b64_tr_b16 a[122:123], v18 offset:30720\n" \
  "ds_read_b64_tr_b16 a[124:125], v19 offset:28672\n" \
  "ds_read_b64_tr_b16 a[126:127], v19 offset:30720\n" \
  "s_waitcnt lgkmcnt(0)\n" \
  "s_add_u32 m0, s86, 16384\n" \
  "s_nop 0\n" \
  "buffer_load_dwordx4 v7, s[68:71], s90 offen lds\n" \
  "s_add_u32 s90, s90, 16384\n" \
  "s_add_u32 m0, s86, 17408\n" \
  "s_nop 0\n" \
  "buffer_load_dwordx4 v7, s[68:71], s90 offen lds\n" \
  "s_add_u32 s90, s90, 16384\n" \
  "s_add_u32 m0, s86, 18432\n" \
  "s_nop 0\n" \
  "buffer_load_dwordx4 v7, s[68:71], s90 offen lds\n" \
  "s_add_u32 s90, s90, 16384\n" \
  "s_add_u32 m0, s86, 19456\n" \
  "s_nop 0\n" \
  "buffer_load_dwordx4 v7, s[68:71], s90 offen lds\n" \
  "s_add_u32 s90, s90, 16384\n" \
  "s_add_u32 m0, s86, 20480\n" \
  "s_nop 0\n" \
  "buffer_load_dwordx4 v7, s[68:71], s90 offen lds\n" \
  "s_add_u32 s90, s90, 16384\n" \
  "s_add_u32 m0, s86, 21504\n" \
  "s_nop 0\n" \
  "buffer_load_dwordx4 v7, s[68:71], s90 offen lds\n" \
  "s_add_u32 s90, s90, 16384\n" \
  "s_add_u32 m0, s86, 22528\n" \
  "s_nop 0\n" \
  "buffer_load_dwordx4 v7, s[68:71], s90 offen lds\n" \
  "s_add_u32 s90, s90, 16384\n" \
  "s_add_u32 m0, s86, 23552\n" \
  "s_nop 0\n" \
  "buffer_load_dwordx4 v7, s[68:71], s90 offen lds\n" \
  "s_add_u32 s90, s90, 16384\n" \
  "s_add_u32 m0, s86, 24576\n" \
  "s_nop 0\n" \
  "buffer_load_dwordx4 v7, s[68:71], s90 offen lds\n" \
  "s_add_u32 s90, s90, 16384\n" \
  "s_add_u32 m0, s86, 25600\n" \
  "s_nop 0\n" \
  "buffer_load_dwordx4 v7, s[68:71], s90 offen lds\n" \
  "s_add_u32 s90, s90, 16384\n" \
  "s_add_u32 m0, s86, 26624\n" \
  "s_nop 0\n" \
  "buffer_load_dwordx4 v7, s[68:71], s90 offen lds\n" \
  "s_add_u32 s90, s90, 16384\n" \
  "s_add_u32 m0, s86, 27648\n" \
  "s_nop 0\n" \
  "buffer_load_dwordx4 v7, s[68:71], s90 offen lds\n" \
  "s_add_u32 s90, s90, 16384\n" \
  "s_add_u32 m0, s86, 28672\n" \
  "s_nop 0\n" \
  "buffer_load_dwordx4 v7, s[68:71], s90 offen lds\n" \
  "s_add_u32 s90, s90, 16384\n" \
  "s_add_u32 m0, s86, 29696\n" \
  "s_nop 0\n" \
  "buffer_load_dwordx4 v7, s[68:71], s90 offen lds\n" \
  "s_add_u32 s90, s90, 16384\n" \
  "s_add_u32 m0, s86, 30720\n" \
  "s_nop 0\n" \
  "buffer_load_dwordx4 v7, s[68:71], s90 offen lds\n" \
  "s_add_u32 s90, s90, 16384\n" \
  "s_add_u32 m0, s86, 31744\n" \
  "s_nop 0\n" \
  "buffer_load_dwordx4 v7, s[68:71], s90 offen lds\n" \
  "s_add_u32 s90, s90, 16384\n" \
  "s_waitcnt vmcnt(16)\n" \
  "ds_read_b64_tr_b16 a[128:129], v16 offset:0\n" \
  "ds_read_b64_tr_b16 a[130:131], v16 offset:2048\n" \
  "ds_read_b64_tr_b16 a[132:133], v17 offset:0\n" \
  "ds_read_b64_tr_b16 a[134:135], v17 offset:2048\n" \
  "ds_read_b64_tr_b16 a[136:137], v18 offset:0\n" \
  "ds_read_b64_tr_b16 a[138:139], v18 offset:2048\n" \
  "ds_read_b64_tr_b16 a[140:141], v19 offset:0\n" \
  "ds_read_b64_tr_b16 a[142:143], v19 offset:2048\n" \
  "ds_read_b64_tr_b16 a[144:145], v16 offset:4096\n" \
  "ds_read_b64_tr_b16 a[146:147], v16 offset:6144\n" \
  "ds_read_b64_tr_b16 a[148:149], v17 offset:4096\n" \
  "ds_read_b64_tr_b16 a[150:151], v17 offset:6144\n" \
  "ds_read_b64_tr_b16 a[152:153], v18 offset:4096\n" \
  "ds_read_b64_tr_b16 a[154:155], v18 offset:6144\n" \
  "ds_read_b64_tr_b16 a[156:157], v19 offset:4096\n" \
  "ds_read_b64_tr_b16 a[158:159], v19 offset:6144\n" \
  "ds_read_b64_tr_b16 a[160:161], v16 offset:8192\n" \
  "ds_read_b64_tr_b16 a[162:163], v16 offset:10240\n" \
  "ds_read_b64_tr_b16 a[164:165], v17 offset:8192\n" \
  "ds_read_b64_tr_b16 a[166:167], v17 offset:10240\n" \
  "ds_read_b64_tr_b16 a[168:169], v18 offset:8192\n" \
  "ds_read_b64_tr_b16 a[170:171], v18 offset:10240\n" \
  "ds_read_b64_tr_b16 a[172:173], v19 offset:8192\n" \
  "ds_read_b64_tr_b16 a[174:175], v19 offset:10240\n" \
  "ds_read_b64_tr_b16 a[176:177], v16 offset:12288\n" \
  "ds_read_b64_tr_b16 a[178:179], v16 offset:14336\n" \
  "ds_read_b64_tr_b16 a[180:181], v17 offset:12288\n" \
  "ds_read_b64_tr_b16 a[182:183], v17 offset:14336\n" \
  "ds_read_b64_tr_b16 a[184:185], v18 offset:12288\n" \
  "ds_read_b64_tr_b16 a[186:187], v18 offset:14336\n" \
  "ds_read_b64_tr_b16 a[188:189], v19 offset:12288\n" \
  "ds_read_b64_tr_b16 a[190:191], v19 offset:14336\n" \
  "s_waitcnt lgkmcnt(0)\n" \
  "s_add_u32 m0, s86, 0\n" \
  "s_nop 0\n" \
  "buffer_load_dwordx4 v7, s[68:71], s90 offen lds\n" \
  "s_add_u32 s90, s90, 16384\n" \
  "s_add_u32 m0, s86, 1024\n" \
  "s_nop 0\n" \
  "buffer_load_dwordx4 v7, s[68:71], s90 offen lds\n" \
  "s_add_u32 s90, s90, 16384\n" \
  "s_add_u32 m0, s86, 2048\n" \
  "s_nop 0\n" \
  "buffer_load_dwordx4 v7, s[68:71], s90 offen lds\n" \
  "s_add_u32 s90, s90, 16384\n" \
  "s_add_u32 m0, s86, 3072\n" \
  "s_nop 0\n" \
  "buffer_load_dwordx4 v7, s[68:71], s90 offen lds\n" \
  "s_add_u32 s90, s90, 16384\n" \
  "s_add_u32 m0, s86, 4096\n" \
  "s_nop 0\n" \
  "buffer_load_dwordx4 v7, s[68:71], s90 offen lds\n" \
  "s_add_u32 s90, s90, 16384\n" \
  "s_add_u32 m0, s86, 5120\n" \
  "s_nop 0\n" \
  "buffer_load_dwordx4 v7, s[68:71], s90 offen lds\n" \
  "s_add_u32 s90, s90, 16384\n" \
  "s_add_u32 m0, s86, 6144\n" \
  "s_nop 0\n" \
  "buffer_load_dwordx4 v7, s[68:71], s90 offen lds\n" \
  "s_add_u32 s90, s90, 16384\n" \
  "s_add_u32 m0, s86, 7168\n" \
  "s_nop 0\n" \
  "buffer_load_dwordx4 v7, s[68:71], s90 offen lds\n" \
  "s_add_u32 s90, s90, 16384\n" \
  "s_add_u32 m0, s86, 8192\n" \
  "s_nop 0\n" \
  "buffer_load_dwordx4 v7, s[68:71], s90 offen lds\n" \
  "s_add_u32 s90, s90, 16384\n" \
  "s_add_u32 m0, s86, 9216\n" \
  "s_nop 0\n" \
  "buffer_load_dwordx4 v7, s[68:71], s90 offen lds\n" \
  "s_add_u32 s90, s90, 16384\n" \
  "s_add_u32 m0, s86, 10240\n" \
  "s_nop 0\n" \
  "buffer_load_dwordx4 v7, s[68:71], s90 offen lds\n" \
  "s_add_u32 s90, s90, 16384\n" \
  "s_add_u32 m0, s86, 11264\n" \
  "s_nop 0\n" \
  "buffer_load_dwordx4 v7, s[68:71], s90 offen lds\n" \
  "s_add_u32 s90, s90, 16384\n" \
  "s_add_u32 m0, s86, 12288\n" \
  "s_nop 0\n" \
  "buffer_load_dwordx4 v7, s[68:71], s90 offen lds\n" \
  "s_add_u32 s90, s90, 16384\n" \
  "s_add_u32 m0, s86, 13312\n" \
  "s_nop 0\n" \
  "buffer_load_dwordx4 v7, s[68:71], s90 offen lds\n" \
  "s_add_u32 s90, s90, 16384\n" \
  "s_add_u32 m0, s86, 14336\n" \
  "s_nop 0\n" \
  "buffer_load_dwordx4 v7, s[68:71], s90 offen lds\n" \
  "s_add_u32 s90, s90, 16384\n" \
  "s_add_u32 m0, s86, 15360\n" \
  "s_nop 0\n" \
  "buffer_load_dwordx4 v7, s[68:71], s90 offen lds\n" \
  "s_add_u32 s90, s90, 16384\n" \
  "s_waitcnt vmcnt(16)\n" \
  "ds_read_b64_tr_b16 a[192:193], v16 offset:16384\n" \
  "ds_read_b64_tr_b16 a[194:195], v16 offset:18432\n" \
  "ds_read_b64_tr_b16 a[196:197], v17 offset:16384\n" \
  "ds_read_b64_tr_b16 a[198:199], v17 offset:18432\n" \
  "ds_read_b64_tr_b16 a[200:201], v18 offset:16384\n" \
  "ds_read_b64_tr_b16 a[202:203], v18 offset:18432\n" \
  "ds_read_b64_tr_b16 a[204:205], v19 offset:16384\n" \
  "ds_read_b64_tr_b16 a[206:207], v19 offset:18432\n" \
  "ds_read_b64_tr_b16 a[208:209], v16 offset:20480\n" \
  "ds_read_b64_tr_b16 a[210:211], v16 offset:22528\n" \
  "ds_read_b64_tr_b16 a[212:213], v17 offset:20480\n" \
  "ds_read_b64_tr_b16 a[214:215], v17 offset:22528\n" \
  "ds_read_b64_tr_b16 a[216:217], v18 offset:20480\n" \
  "ds_read_b64_tr_b16 a[218:219], v18 offset:22528\n" \
  "ds_read_b64_tr_b16 a[220:221], v19 offset:20480\n" \
  "ds_read_b64_tr_b16 a[222:223], v19 offset:22528\n" \
  "ds_read_b64_tr_b16 a[224:225], v16 offset:24576\n" \
  "ds_read_b64_tr_b16 a[226:227], v16 offset:26624\n" \
  "ds_read_b64_tr_b16 a[228:229], v17 offset:24576\n" \
  "ds_read_b64_tr_b16 a[230:231], v17 offset:26624\n" \
  "ds_read_b64_tr_b16 a[232:233], v18 offset:24576\n" \
  "ds_read_b64_tr_b16 a[234:235], v18 offset:26624\n" \
  "ds_read_b64_tr_b16 a[236:237], v19 offset:24576\n" \
  "ds_read_b64_tr_b16 a[238:239], v19 offset:26624\n" \
  "ds_read_b64_tr_b16 a[240:241], v16 offset:28672\n" \
  "ds_read_b64_tr_b16 a[242:243], v16 offset:30720\n" \
  "ds_read_b64_tr_b16 a[244:245], v17 offset:28672\n" \
  "ds_read_b64_tr_b16 a[246:247], v17 offset:30720\n" \
  "ds_read_b64_tr_b16 a[248:249], v18 offset:28672\n" \
  "ds_read_b64_tr_b16 a[250:251], v18 offset:30720\n" \
  "ds_read_b64_tr_b16 a[252:253], v19 offset:28672\n" \
  "ds_read_b64_tr_b16 a[254:255], v19 offset:30720\n" \
  "s_waitcnt lgkmcnt(0)\n" \
  "s_add_u32 m0, s86, 16384\n" \
  "s_nop 0\n" \
  "buffer_load_dwordx4 v7, s[68:71], s90 offen lds\n" \
  "s_add_u32 s90, s90, 16384\n" \
  "s_add_u32 m0, s86, 17408\n" \
  "s_nop 0\n" \
  "buffer_load_dwordx4 v7, s[68:71], s90 offen lds\n" \
  "s_add_u32 s90, s90, 16384\n" \
  "s_add_u32 m0, s86, 18432\n" \
  "s_nop 0\n" \
  "buffer_load_dwordx4 v7, s[68:71], s90 offen lds\n" \
  "s_add_u32 s90, s90, 16384\n" \
  "s_add_u32 m0, s86, 19456\n" \
  "s_nop 0\n" \
  "buffer_load_dwordx4 v7, s[68:71], s90 offen lds\n" \
  "s_add_u32 s90, s90, 16384\n" \
  "s_add_u32 m0, s86, 20480\n" \
  "s_nop 0\n" \
  "buffer_load_dwordx4 v7, s[68:71], s90 offen lds\n" \
  "s_add_u32 s90, s90, 16384\n" \
  "s_add_u32 m0, s86, 21504\n" \
  "s_nop 0\n" \
  "buffer_load_dwordx4 v7, s[68:71], s90 offen lds\n" \
  "s_add_u32 s90, s90, 16384\n" \
  "s_add_u32 m0, s86, 22528\n" \
  "s_nop 0\n" \
  "buffer_load_dwordx4 v7, s[68:71], s90 offen lds\n" \
  "s_add_u32 s90, s90, 16384\n" \
  "s_add_u32 m0, s86, 23552\n" \
  "s_nop 0\n" \
  "buffer_load_dwordx4 v7, s[68:71], s90 offen lds\n" \
  "s_add_u32 s90, s90, 16384\n" \
  "s_add_u32 m0, s86, 24576\n" \
  "s_nop 0\n" \
  "buffer_load_dwordx4 v7, s[68:71], s90 offen lds\n" \
  "s_add_u32 s90, s90, 16384\n" \
  "s_add_u32 m0, s86, 25600\n" \
  "s_nop 0\n" \
  "buffer_load_dwordx4 v7, s[68:71], s90 offen lds\n" \
  "s_add_u32 s90, s90, 16384\n" \
  "s_add_u32 m0, s86, 26624\n" \
  "s_nop 0\n" \
  "buffer_load_dwordx4 v7, s[68:71], s90 offen lds\n" \
  "s_add_u32 s90, s90, 16384\n" \
  "s_add_u32 m0, s86, 27648\n" \
  "s_nop 0\n" \
  "buffer_load_dwordx4 v7, s[68:71], s90 offen lds\n" \
  "s_add_u32 s90, s90, 16384\n" \
  "s_add_u32 m0, s86, 28672\n" \
  "s_nop 0\n" \
  "buffer_load_dwordx4 v7, s[68:71], s90 offen lds\n" \
  "s_add_u32 s90, s90, 16384\n" \
  "s_add_u32 m0, s86, 29696\n" \
  "s_nop 0\n" \
  "buffer_load_dwordx4 v7, s[68:71], s90 offen lds\n" \
  "s_add_u32 s90, s90, 16384\n" \
  "s_add_u32 m0, s86, 30720\n" \
  "s_nop 0\n" \
  "buffer_load_dwordx4 v7, s[68:71], s90 offen lds\n" \
  "s_add_u32 s90, s90, 16384\n" \
  "s_add_u32 m0, s86, 31744\n" \
  "s_nop 0\n" \
  "buffer_load_dwordx4 v7, s[68:71], s90 offen lds\n" \
  "s_add_u32 s90, s90, 16384\n" \
  "s_mov_b32 s92, 1\n" \
  "LP0_%=:\n" \
  "buffer_load_dword v9, v8, s[80:83], 0 offen sc1\n" \
  "s_waitcnt vmcnt(0)\n" \
  "v_cmp_gt_u32 vcc, s92, v9\n" \
  "s_cbranch_vccz LP1_%=\n" \
  "s_sleep 8\n" \
  "s_add_u32 s91, s91, 1\n" \
  "s_cmp_lt_u32 s91, " STR(SPIN_CAP) "\n" \
  "s_cbranch_scc1 LP0_%=\n" \
  "v_mov_b32 v5, " STR(CW_DEAD) "*4\n" \
  "v_mov_b32 v15, 1\n" \
  "buffer_store_dword v15, v5, s[80:83], 0 offen sc1\n" \
  "s_branch LP1_%=\n" \
  "LP1_%=:\n" \
  "s_add_u32 s84, s87, 0\n" \
  "buffer_load_dwordx4 v[64:67], v6, s[72:75], s84 offen sc1\n" \
  "s_add_u32 s84, s87, 1024\n" \
  "buffer_load_dwordx4 v[68:71], v6, s[72:75], s84 offen sc1\n" \
  "s_add_u32 s84, s87, 2048\n" \
  "buffer_load_dwordx4 v[72:75], v6, s[72:75], s84 offen sc1\n" \
  "s_add_u32 s84, s87, 49152\n" \
  "buffer_load_dwordx4 v[76:79], v6, s[72:75], s84 offen sc1\n" \
  "s_add_u32 s84, s87, 3072\n" \
  "buffer_load_dwordx4 v[80:83], v6, s[72:75], s84 offen sc1\n" \
  "s_add_u32 s84, s87, 4096\n" \
  "buffer_load_dwordx4 v[84:87], v6, s[72:75], s84 offen sc1\n" \
  "s_add_u32 s84, s87, 5120\n" \
  "buffer_load_dwordx4 v[88:91], v6, s[72:75], s84 offen sc1\n" \
  "s_add_u32 s84, s87, 50176\n" \
  "buffer_load_dwordx4 v[92:95], v6, s[72:75], s84 offen sc1\n" \
  "s_add_u32 s84, s87, 6144\n" \
  "buffer_load_dwordx4 v[96:99], v6, s[72:75], s84 offen sc1\n" \
  "s_add_u32 s84, s87, 7168\n" \
  "buffer_load_dwordx4 v[100:103], v6, s[72:75], s84 offen sc1\n" \
  "s_add_u32 s84, s87, 16384\n" \
  "buffer_load_dwordx4 v[104:107], v6, s[72:75], s84 offen sc1\n" \
  "s_add_u32 s84, s87, 51200\n" \
  "buffer_load_dwordx4 v[108:111], v6, s[72:75], s84 offen sc1\n" \
  "s_add_u32 s84, s87, 17408\n" \
  "buffer_load_dwordx4 v[112:115], v6, s[72:75], s84 offen sc1\n" \
  "s_add_u32 s84, s87, 18432\n" \
  "buffer_load_dwordx4 v[116:119], v6, s[72:75], s84 offen sc1\n" \
  "s_add_u32 s84, s87, 19456\n" \
  "buffer_load_dwordx4 v[120:123], v6, s[72:75], s84 offen sc1\n" \
  "s_add_u32 s84, s87, 52224\n" \
  "buffer_load_dwordx4 v[124:127], v6, s[72:75], s84 offen sc1\n" \
  "v_or_b32 v10, 0x80000000, v10\n" \
  "s_sub_u32 s88, s88, 131072\n" \
  ".p2align 6\n" \
  "LX_%=:\n" \
  "s_waitcnt vmcnt(15)\n" \
  "v_mfma_f32_16x16x32_bf16 v[20:23], v[128:131], v[64:67], 0\n" \
  "v_mfma_f32_16x16x32_bf16 v[24:27], v[132:135], v[64:67], 0\n" \
  "v_mfma_f32_16x16x32_bf16 v[28:31], v[136:139], v[64:67], 0\n" \
  "v_mfma_f32_16x16x32_bf16 v[32:35], v[140:143], v[64:67], 0\n" \
  "s_add_u32 s84, s87, 20480\n" \
  "buffer_load_dwordx4 v[64:67], v6, s[72:75], s84 offen sc1\n" \
  "s_waitcnt vmcnt(15)\n" \
  "v_mfma_f32_16x16x32_bf16 v[20:23], v[144:147], v[68:71], v[20:23]\n" \
  "v_mfma_f32_16x16x32_bf16 v[24:27], v[148:151], v[68:71], v[24:27]\n" \
  "v_mfma_f32_16x16x32_bf16 v[28:31], v[152:155], v[68:71], v[28:31]\n" \
  "v_mfma_f32_16x16x32_bf16 v[32:35], v[156:159], v[68:71], v[32:35]\n" \
  "s_add_u32 s84, s87, 21504\n" \
  "buffer_load_dwordx4 v[68:71], v6, s[72:75], s84 offen sc1\n" \
  "s_waitcnt vmcnt(15)\n" \
  "v_mfma_f32_16x16x32_bf16 v[20:23], v[160:163], v[72:75], v[20:23]\n" \
  "ds_read_b64_tr_b16 v[40:41], v16 offset:0\n" \
  "ds_read_b64_tr_b16 v[42:43], v16 offset:2048\n" \
  "v_mfma_f32_16x16x32_bf16 v[24:27], v[164:167], v[72:75], v[24:27]\n" \
  "ds_read_b64_tr_b16 v[44:45], v17 offset:0\n" \
  "ds_read_b64_tr_b16 v[46:47], v17 offset:2048\n" \
  "v_mfma_f32_16x16x32_bf16 v[28:31], v[168:171], v[72:75], v[28:31]\n" \
  "ds_read_b64_tr_b16 v[48:49], v18 offset:0\n" \
  "ds_read_b64_tr_b16 v[50:51], v18 offset:2048\n" \
  "v_mfma_f32_16x16x32_bf16 v[32:35], v[172:175], v[72:75], v[32:35]\n" \
  "ds_read_b64_tr_b16 v[52:53], v19 offset:0\n" \
  "ds_read_b64_tr_b16 v[54:55], v19 offset:2048\n" \
  "s_add_u32 s84, s87, 22528\n" \
  "buffer_load_dwordx4 v[72:75], v6, s[72:75], s84 offen sc1\n" \
  "s_waitcnt vmcnt(15)\n" \
  "s_waitcnt lgkmcnt(6)\n" \
  "v_mfma_f32_16x16x32_bf16 v[20:23], v[40:43], v[76:79], v[20:23]\n" \
  "s_waitcnt lgkmcnt(4)\n" \
  "v_mfma_f32_16x16x32_bf16 v[24:27], v[44:47], v[76:79], v[24:27]\n" \
  "s_waitcnt lgkmcnt(2)\n" \
  "v_mfma_f32_16x16x32_bf16 v[28:31], v[48:51], v[76:79], v[28:31]\n" \
  "s_waitcnt lgkmcnt(0)\n" \
  "v_mfma_f32_16x16x32_bf16 v[32:35], v[52:55], v[76:79], v[32:35]\n" \
  "s_add_u32 s84, s87, 53248\n" \
  "buffer_load_dwordx4 v[76:79], v6, s[72:75], s84 offen sc1\n" \
  "s_waitcnt lgkmcnt(0)\n" \
  "s_barrier\n" \
  "ds_read_b128 v[52:55], v11 offset:0\n" \
  "ds_read_b128 v[56:59], v11 offset:1024\n" \
  "ds_read_b128 v[60:63], v11 offset:2048\n" \
  "s_waitcnt vmcnt(15)\n" \
  "v_mfma_f32_16x16x32_bf16 v[20:23], v[176:179], v[80:83], v[20:23]\n" \
  "v_mfma_f32_16x16x32_bf16 v[24:27], v[180:183], v[80:83], v[24:27]\n" \
  "v_mfma_f32_16x16x32_bf16 v[28:31], v[184:187], v[80:83], v[28:31]\n" \
  "v_mfma_f32_16x16x32_bf16 v[32:35], v[188:191], v[80:83], v[32:35]\n" \
  "s_add_u32 s84, s87, 23552\n" \
  "buffer_load_dwordx4 v[80:83], v6, s[72:75], s84 offen sc1\n" \
  "s_waitcnt lgkmcnt(0)\n" \
  "s_waitcnt vmcnt(15)\n" \
  "v_mfma_f32_16x16x32_bf16 v[20:23], v[192:195], v[84:87], v[20:23]\n" \
  "v_add_f32 v36, v36, v52\n" \
  "v_add_f32 v37, v37, v53\n" \
  "v_add_f32 v38, v38, v54\n" \
  "v_add_f32 v39, v39, v55\n" \
  "v_mfma_f32_16x16x32_bf16 v[24:27], v[196:199], v[84:87], v[24:27]\n" \
  "v_add_f32 v56, v56, v60\n" \
  "v_add_f32 v57, v57, v61\n" \
  "v_add_f32 v58, v58, v62\n" \
  "v_add_f32 v59, v59, v63\n" \
  "v_mfma_f32_16x16x32_bf16 v[28:31], v[200:203], v[84:87], v[28:31]\n" \
  "v_add_f32 v36, v36, v56\n" \
  "v_add_f32 v37, v37, v57\n" \
  "v_add_f32 v38, v38, v58\n" \
  "v_add_f32 v39, v39, v59\n" \
  "v_mfma_f32_16x16x32_bf16 v[32:35], v[204:207], v[84:87], v[32:35]\n" \
  "v_cvt_pk_bf16_f32 v36, v36, v37\n" \
  "v_cvt_pk_bf16_f32 v37, v38, v39\n" \
  "buffer_store_dwordx2 v[36:37], v10, s[76:79], s88 offen\n" \
  "s_add_u32 s88, s88, 131072\n" \
  "v_and_b32 v10, 0x7fffffff, v10\n" \
  "s_add_u32 s84, s87, 32768\n" \
  "buffer_load_dwordx4 v[84:87], v6, s[72:75], s84 offen sc1\n" \
  "s_waitcnt vmcnt(16)\n" \
  "v_mfma_f32_16x16x32_bf16 v[20:23], v[208:211], v[88:91], v[20:23]\n" \
  "ds_read_b64_tr_b16 v[40:41], v16 offset:4096\n" \
  "ds_read_b64_tr_b16 v[42:43], v16 offset:6144\n" \
  "v_mfma_f32_16x16x32_bf16 v[24:27], v[212:215], v[88:91], v[24:27]\n" \
  "ds_read_b64_tr_b16 v[44:45], v17 offset:4096\n" \
  "ds_read_b64_tr_b16 v[46:47], v17 offset:6144\n" \
  "v_mfma_f32_16x16x32_bf16 v[28:31], v[216:219], v[88:91], v[28:31]\n" \
  "ds_read_b64_tr_b16 v[48:49], v18 offset:4096\n" \
  "ds_read_b64_tr_b16 v[50:51], v18 offset:6144\n" \
  "v_mfma_f32_16x16x32_bf16 v[32:35], v[220:223], v[88:91], v[32:35]\n" \
  "ds_read_b64_tr_b16 v[52:53], v19 offset:4096\n" \
  "ds_read_b64_tr_b16 v[54:55], v19 offset:6144\n" \
  "s_add_u32 s84, s87, 33792\n" \
  "buffer_load_dwordx4 v[88:91], v6, s[72:75], s84 offen sc1\n" \
  "s_waitcnt vmcnt(16)\n" \
  "s_waitcnt lgkmcnt(6)\n" \
  "v_mfma_f32_16x16x32_bf16 v[20:23], v[40:43], v[92:95], v[20:23]\n" \
  "s_waitcnt lgkmcnt(4)\n" \
  "v_mfma_f32_16x16x32_bf16 v[24:27], v[44:47], v[92:95], v[24:27]\n" \
  "s_waitcnt lgkmcnt(2)\n" \
  "v_mfma_f32_16x16x32_bf16 v[28:31], v[48:51], v[92:95], v[28:31]\n" \
  "s_waitcnt lgkmcnt(0)\n" \
  "v_mfma_f32_16x16x32_bf16 v[32:35], v[52:55], v[92:95], v[32:35]\n" \
  "s_add_u32 s84, s87, 54272\n" \
  "buffer_load_dwordx4 v[92:95], v6, s[72:75], s84 offen sc1\n" \
  "s_waitcnt vmcnt(16)\n" \
  "v_mfma_f32_16x16x32_bf16 v[20:23], v[224:227], v[96:99], v[20:23]\n" \
  "buffer_load_dword v9, v8, s[80:83], 0 offen sc1\n" \
  "v_mfma_f32_16x16x32_bf16 v[24:27], v[228:231], v[96:99], v[24:27]\n" \
  "v_xor_b32 v11, 0x4000, v11\n" \
  "v_xor_b32 v12, 0x4000, v12\n" \
  "v_mfma_f32_16x16x32_bf16 v[28:31], v[232:235], v[96:99], v[28:31]\n" \
  "v_xor_b32 v13, 0x4000, v13\n" \
  "v_xor_b32 v14, 0x4000, v14\n" \
  "v_mfma_f32_16x16x32_bf16 v[32:35], v[236:239], v[96:99], v[32:35]\n" \
  "s_add_u32 s84, s87, 34816\n" \
  "buffer_load_dwordx4 v[96:99], v6, s[72:75], s84 offen sc1\n" \
  "s_waitcnt vmcnt(17)\n" \
  "v_mfma_f32_16x16x32_bf16 v[20:23], v[240:243], v[100:103], v[20:23]\n" \
  "v_mfma_f32_16x16x32_bf16 v[24:27], v[244:247], v[100:103], v[24:27]\n" \
  "v_mfma_f32_16x16x32_bf16 v[28:31], v[248:251], v[100:103], v[28:31]\n" \
  "v_mfma_f32_16x16x32_bf16 v[32:35], v[252:255], v[100:103], v[32:35]\n" \
  "s_add_u32 s84, s87, 35840\n" \
  "buffer_load_dwordx4 v[100:103], v6, s[72:75], s84 offen sc1\n" \
  "s_waitcnt vmcnt(17)\n" \
  "v_mfma_f32_16x16x32_bf16 v[20:23], a[0:3], v[104:107], v[20:23]\n" \
  "ds_read_b64_tr_b16 v[40:41], v16 offset:8192\n" \
  "ds_read_b64_tr_b16 v[42:43], v16 offset:10240\n" \
  "v_mfma_f32_16x16x32_bf16 v[24:27], a[4:7], v[104:107], v[24:27]\n" \
  "ds_read_b64_tr_b16 v[44:45], v17 offset:8192\n" \
  "ds_read_b64_tr_b16 v[46:47], v17 offset:10240\n" \
  "v_mfma_f32_16x16x32_bf16 v[28:31], a[8:11], v[104:107], v[28:31]\n" \
  "ds_read_b64_tr_b16 v[48:49], v18 offset:8192\n" \
  "ds_read_b64_tr_b16 v[50:51], v18 offset:10240\n" \
  "v_mfma_f32_16x16x32_bf16 v[32:35], a[12:15], v[104:107], v[32:35]\n" \
  "ds_read_b64_tr_b16 v[52:53], v19 offset:8192\n" \
  "ds_read_b64_tr_b16 v[54:55], v19 offset:10240\n" \
  "s_add_u32 s84, s87, 36864\n" \
  "buffer_load_dwordx4 v[104:107], v6, s[72:75], s84 offen sc1\n" \
  "s_waitcnt vmcnt(17)\n" \
  "s_waitcnt lgkmcnt(6)\n" \
  "v_mfma_f32_16x16x32_bf16 v[20:23], v[40:43], v[108:111], v[20:23]\n" \
  "s_waitcnt lgkmcnt(4)\n" \
  "v_mfma_f32_16x16x32_bf16 v[24:27], v[44:47], v[108:111], v[24:27]\n" \
  "s_waitcnt lgkmcnt(2)\n" \
  "v_mfma_f32_16x16x32_bf16 v[28:31], v[48:51], v[108:111], v[28:31]\n" \
  "s_waitcnt lgkmcnt(0)\n" \
  "v_mfma_f32_16x16x32_bf16 v[32:35], v[52:55], v[108:111], v[32:35]\n" \
  "s_add_u32 s84, s87, 55296\n" \
  "buffer_load_dwordx4 v[108:111], v6, s[72:75], s84 offen sc1\n" \
  "s_waitcnt vmcnt(17)\n" \
  "v_mfma_f32_16x16x32_bf16 v[20:23], a[16:19], v[112:115], v[20:23]\n" \
  "v_mfma_f32_16x16x32_bf16 v[24:27], a[20:23], v[112:115], v[24:27]\n" \
  "v_mfma_f32_16x16x32_bf16 v[28:31], a[24:27], v[112:115], v[28:31]\n" \
  "v_mfma_f32_16x16x32_bf16 v[32:35], a[28:31], v[112:115], v[32:35]\n" \
  "s_add_u32 s84, s87, 37888\n" \
  "buffer_load_dwordx4 v[112:115], v6, s[72:75], s84 offen sc1\n" \
  "s_waitcnt vmcnt(17)\n" \
  "v_mfma_f32_16x16x32_bf16 v[20:23], a[32:35], v[116:119], v[20:23]\n" \
  "v_mfma_f32_16x16x32_bf16 v[24:27], a[36:39], v[116:119], v[24:27]\n" \
  "v_mfma_f32_16x16x32_bf16 v[28:31], a[40:43], v[116:119], v[28:31]\n" \
  "v_mfma_f32_16x16x32_bf16 v[32:35], a[44:47], v[116:119], v[32:35]\n" \
  "s_add_u32 s84, s87, 38912\n" \
  "buffer_load_dwordx4 v[116:119], v6, s[72:75], s84 offen sc1\n" \
  "s_waitcnt vmcnt(17)\n" \
  "v_mfma_f32_16x16x32_bf16 v[20:23], a[48:51], v[120:123], v[20:23]\n" \
  "ds_read_b64_tr_b16 v[40:41], v16 offset:12288\n" \
  "ds_read_b64_tr_b16 v[42:43], v16 offset:14336\n" \
  "v_mfma_f32_16x16x32_bf16 v[24:27], a[52:55], v[120:123], v[24:27]\n" \
  "ds_read_b64_tr_b16 v[44:45], v17 offset:12288\n" \
  "ds_read_b64_tr_b16 v[46:47], v17 offset:14336\n" \
  "v_mfma_f32_16x16x32_bf16 v[28:31], a[56:59], v[120:123], v[28:31]\n" \
  "ds_read_b64_tr_b16 v[48:49], v18 offset:12288\n" \
  "ds_read_b64_tr_b16 v[50:51], v18 offset:14336\n" \
  "v_mfma_f32_16x16x32_bf16 v[32:35], a[60:63], v[120:123], v[32:35]\n" \
  "ds_read_b64_tr_b16 v[52:53], v19 offset:12288\n" \
  "ds_read_b64_tr_b16 v[54:55], v19 offset:14336\n" \
  "s_add_u32 s84, s87, 39936\n" \
  "buffer_load_dwordx4 v[120:123], v6, s[72:75], s84 offen sc1\n" \
  "s_waitcnt vmcnt(17)\n" \
  "s_waitcnt lgkmcnt(6)\n" \
  "v_mfma_f32_16x16x32_bf16 v[20:23], v[40:43], v[124:127], v[20:23]\n" \
  "s_waitcnt lgkmcnt(4)\n" \
  "v_mfma_f32_16x16x32_bf16 v[24:27], v[44:47], v[124:127], v[24:27]\n" \
  "s_waitcnt lgkmcnt(2)\n" \
  "v_mfma_f32_16x16x32_bf16 v[28:31], v[48:51], v[124:127], v[28:31]\n" \
  "s_waitcnt lgkmcnt(0)\n" \
  "v_mfma_f32_16x16x32_bf16 v[32:35], v[52:55], v[124:127], v[32:35]\n" \
  "s_add_u32 s84, s87, 56320\n" \
  "buffer_load_dwordx4 v[124:127], v6, s[72:75], s84 offen sc1\n" \
  "s_waitcnt vmcnt(8)\n" \
  "s_add_u32 s92, s89, 2\n" \
  "s_min_u32 s92, s92, 64\n" \
  "v_cmp_gt_u32 vcc, s92, v9\n" \
  "s_sub_u32 s85, s92, 1\n" \
  "s_lshr_b32 s84, s85, 2\n" \
  "s_xnor_b32 s84, s84, %7\n" \
  "s_and_b32 s84, s84, 1\n" \
  "s_lshl_b32 s84, s84, 13\n" \
  "s_lshl_b32 s87, s85, 19\n" \
  "s_or_b32 s87, s87, s84\n" \
  "s_add_u32 s87, s87, s96\n" \
  "s_cbranch_vccnz LP2_%=\n" \
  "LP3_%=:\n" \
  "s_waitcnt vmcnt(17)\n" \
  "v_mfma_f32_16x16x32_bf16 v[20:23], a[64:67], v[64:67], v[20:23]\n" \
  "v_mfma_f32_16x16x32_bf16 v[24:27], a[68:71], v[64:67], v[24:27]\n" \
  "v_mfma_f32_16x16x32_bf16 v[28:31], a[72:75], v[64:67], v[28:31]\n" \
  "v_mfma_f32_16x16x32_bf16 v[32:35], a[76:79], v[64:67], v[32:35]\n" \
  "s_add_u32 s84, s87, 0\n" \
  "buffer_load_dwordx4 v[64:67], v6, s[72:75], s84 offen sc1\n" \
  "s_waitcnt vmcnt(17)\n" \
  "v_mfma_f32_16x16x32_bf16 v[20:23], a[80:83], v[68:71], v[20:23]\n" \
  "v_mfma_f32_16x16x32_bf16 v[24:27], a[84:87], v[68:71], v[24:27]\n" \
  "v_mfma_f32_16x16x32_bf16 v[28:31], a[88:91], v[68:71], v[28:31]\n" \
  "v_mfma_f32_16x16x32_bf16 v[32:35], a[92:95], v[68:71], v[32:35]\n" \
  "s_add_u32 s84, s87, 1024\n" \
  "buffer_load_dwordx4 v[68:71], v6, s[72:75], s84 offen sc1\n" \
  "s_waitcnt vmcnt(17)\n" \
  "v_mfma_f32_16x16x32_bf16 v[20:23], a[96:99], v[72:75], v[20:23]\n" \
  "ds_read_b64_tr_b16 v[40:41], v16 offset:16384\n" \
  "ds_read_b64_tr_b16 v[42:43], v16 offset:18432\n" \
  "v_mfma_f32_16x16x32_bf16 v[24:27], a[100:103], v[72:75], v[24:27]\n" \
  "ds_read_b64_tr_b16 v[44:45], v17 offset:16384\n" \
  "ds_read_b64_tr_b16 v[46:47], v17 offset:18432\n" \
  "v_mfma_f32_16x16x32_bf16 v[28:31], a[104:107], v[72:75], v[28:31]\n" \
  "ds_read_b64_tr_b16 v[48:49], v18 offset:16384\n" \
  "ds_read_b64_tr_b16 v[50:51], v18 offset:18432\n" \
  "v_mfma_f32_16x16x32_bf16 v[32:35], a[108:111], v[72:75], v[32:35]\n" \
  "ds_read_b64_tr_b16 v[52:53], v19 offset:16384\n" \
  "ds_read_b64_tr_b16 v[54:55], v19 offset:18432\n" \
  "s_add_u32 s84, s87, 2048\n" \
  "buffer_load_dwordx4 v[72:75], v6, s[72:75], s84 offen sc1\n" \
  "s_waitcnt vmcnt(17)\n" \
  "s_waitcnt lgkmcnt(6)\n" \
  "v_mfma_f32_16x16x32_bf16 v[20:23], v[40:43], v[76:79], v[20:23]\n" \
  "s_waitcnt lgkmcnt(4)\n" \
  "v_mfma_f32_16x16x32_bf16 v[24:27], v[44:47], v[76:79], v[24:27]\n" \
  "s_waitcnt lgkmcnt(2)\n" \
  "v_mfma_f32_16x16x32_bf16 v[28:31], v[48:51], v[76:79], v[28:31]\n" \
  "s_waitcnt lgkmcnt(0)\n" \
  "v_mfma_f32_16x16x32_bf16 v[32:35], v[52:55], v[76:79], v[32:35]\n" \
  "s_add_u32 s84, s87, 49152\n" \
  "buffer_load_dwordx4 v[76:79], v6, s[72:75], s84 offen sc1\n" \
  "s_waitcnt vmcnt(17)\n" \
  "v_mfma_f32_16x16x32_bf16 v[20:23], a[112:115], v[80:83], v[20:23]\n" \
  "v_mfma_f32_16x16x32_bf16 v[24:27], a[116:119], v[80:83], v[24:27]\n" \
  "v_mfma_f32_16x16x32_bf16 v[28:31], a[120:123], v[80:83], v[28:31]\n" \
  "v_mfma_f32_16x16x32_bf16 v[32:35], a[124:127], v[80:83], v[32:35]\n" \
  "s_add_u32 s84, s87, 3072\n" \
  "buffer_load_dwordx4 v[80:83], v6, s[72:75], s84 offen sc1\n" \
  "s_waitcnt vmcnt(16)\n" \
  "v_mfma_f32_16x16x32_bf16 v[20:23], a[128:131], v[84:87], v[20:23]\n" \
  "v_mfma_f32_16x16x32_bf16 v[24:27], a[132:135], v[84:87], v[24:27]\n" \
  "v_mfma_f32_16x16x32_bf16 v[28:31], a[136:139], v[84:87], v[28:31]\n" \
  "v_mfma_f32_16x16x32_bf16 v[32:35], a[140:143], v[84:87], v[32:35]\n" \
  "s_add_u32 s84, s87, 4096\n" \
  "buffer_load_dwordx4 v[84:87], v6, s[72:75], s84 offen sc1\n" \
  "s_waitcnt vmcnt(16)\n" \
  "v_mfma_f32_16x16x32_bf16 v[20:23], a[144:147], v[88:91], v[20:23]\n" \
  "ds_read_b64_tr_b16 v[40:41], v16 offset:20480\n" \
  "ds_read_b64_tr_b16 v[42:43], v16 offset:22528\n" \
  "v_mfma_f32_16x16x32_bf16 v[24:27], a[148:151], v[88:91], v[24:27]\n" \
  "ds_read_b64_tr_b16 v[44:45], v17 offset:20480\n" \
  "ds_read_b64_tr_b16 v[46:47], v17 offset:22528\n" \
  "v_mfma_f32_16x16x32_bf16 v[28:31], a[152:155], v[88:91], v[28:31]\n" \
  "ds_read_b64_tr_b16 v[48:49], v18 offset:20480\n" \
  "ds_read_b64_tr_b16 v[50:51], v18 offset:22528\n" \
  "v_mfma_f32_16x16x32_bf16 v[32:35], a[156:159], v[88:91], v[32:35]\n" \
  "ds_read_b64_tr_b16 v[52:53], v19 offset:20480\n" \
  "ds_read_b64_tr_b16 v[54:55], v19 offset:22528\n" \
  "s_add_u32 s84, s87, 5120\n" \
  "buffer_load_dwordx4 v[88:91], v6, s[72:75], s84 offen sc1\n" \
  "s_waitcnt vmcnt(16)\n" \
  "s_waitcnt lgkmcnt(6)\n" \
  "v_mfma_f32_16x16x32_bf16 v[20:23], v[40:43], v[92:95], v[20:23]\n" \
  "s_waitcnt lgkmcnt(4)\n" \
  "v_mfma_f32_16x16x32_bf16 v[24:27], v[44:47], v[92:95], v[24:27]\n" \
  "s_waitcnt lgkmcnt(2)\n" \
  "v_mfma_f32_16x16x32_bf16 v[28:31], v[48:51], v[92:95], v[28:31]\n" \
  "s_waitcnt lgkmcnt(0)\n" \
  "v_mfma_f32_16x16x32_bf16 v[32:35], v[52:55], v[92:95], v[32:35]\n" \
  "s_add_u32 s84, s87, 50176\n" \
  "buffer_load_dwordx4 v[92:95], v6, s[72:75], s84 offen sc1\n" \
  "s_waitcnt vmcnt(15)\n" \
  "v_mfma_f32_16x16x32_bf16 v[20:23], a[160:163], v[96:99], v[20:23]\n" \
  "v_mfma_f32_16x16x32_bf16 v[24:27], a[164:167], v[96:99], v[24:27]\n" \
  "v_mfma_f32_16x16x32_bf16 v[28:31], a[168:171], v[96:99], v[28:31]\n" \
  "v_mfma_f32_16x16x32_bf16 v[32:35], a[172:175], v[96:99], v[32:35]\n" \
  "s_add_u32 s84, s87, 6144\n" \
  "buffer_load_dwordx4 v[96:99], v6, s[72:75], s84 offen sc1\n" \
  "s_waitcnt vmcnt(15)\n" \
  "v_mfma_f32_16x16x32_bf16 v[20:23], a[176:179], v[100:103], v[20:23]\n" \
  "v_mfma_f32_16x16x32_bf16 v[24:27], a[180:183], v[100:103], v[24:27]\n" \
  "v_mfma_f32_16x16x32_bf16 v[28:31], a[184:187], v[100:103], v[28:31]\n" \
  "v_mfma_f32_16x16x32_bf16 v[32:35], a[188:191], v[100:103], v[32:35]\n" \
  "s_add_u32 s84, s87, 7168\n" \
  "buffer_load_dwordx4 v[100:103], v6, s[72:75], s84 offen sc1\n" \
  "s_waitcnt vmcnt(15)\n" \
  "v_mfma_f32_16x16x32_bf16 v[20:23], a[192:195], v[104:107], v[20:23]\n" \
  "ds_read_b64_tr_b16 v[40:41], v16 offset:24576\n" \
  "ds_read_b64_tr_b16 v[42:43], v16 offset:26624\n" \
  "v_mfma_f32_16x16x32_bf16 v[24:27], a[196:199], v[104:107], v[24:27]\n" \
  "ds_read_b64_tr_b16 v[44:45], v17 offset:24576\n" \
  "ds_read_b64_tr_b16 v[46:47], v17 offset:26624\n" \
  "v_mfma_f32_16x16x32_bf16 v[28:31], a[200:203], v[104:107], v[28:31]\n" \
  "ds_read_b64_tr_b16 v[48:49], v18 offset:24576\n" \
  "ds_read_b64_tr_b16 v[50:51], v18 offset:26624\n" \
  "v_mfma_f32_16x16x32_bf16 v[32:35], a[204:207], v[104:107], v[32:35]\n" \
  "ds_read_b64_tr_b16 v[52:53], v19 offset:24576\n" \
  "ds_read_b64_tr_b16 v[54:55], v19 offset:26624\n" \
  "s_add_u32 s84, s87, 16384\n" \
  "buffer_load_dwordx4 v[104:107], v6, s[72:75], s84 offen sc1\n" \
  "s_waitcnt vmcnt(15)\n" \
  "s_waitcnt lgkmcnt(6)\n" \
  "v_mfma_f32_16x16x32_bf16 v[20:23], v[40:43], v[108:111], v[20:23]\n" \
  "s_waitcnt lgkmcnt(4)\n" \
  "v_mfma_f32_16x16x32_bf16 v[24:27], v[44:47], v[108:111], v[24:27]\n" \
  "s_waitcnt lgkmcnt(2)\n" \
  "v_mfma_f32_16x16x32_bf16 v[28:31], v[48:51], v[108:111], v[28:31]\n" \
  "s_waitcnt lgkmcnt(0)\n" \
  "v_mfma_f32_16x16x32_bf16 v[32:35], v[52:55], v[108:111], v[32:35]\n" \
  "s_add_u32 s84, s87, 51200\n" \
  "buffer_load_dwordx4 v[108:111], v6, s[72:75], s84 offen sc1\n" \
  "s_waitcnt vmcnt(15)\n" \
  "v_mfma_f32_16x16x32_bf16 v[20:23], a[208:211], v[112:115], v[20:23]\n" \
  "v_mfma_f32_16x16x32_bf16 v[24:27], a[212:215], v[112:115], v[24:27]\n" \
  "v_mfma_f32_16x16x32_bf16 v[28:31], a[216:219], v[112:115], v[28:31]\n" \
  "v_mfma_f32_16x16x32_bf16 v[32:35], a[220:223], v[112:115], v[32:35]\n" \
  "s_add_u32 s84, s87, 17408\n" \
  "buffer_load_dwordx4 v[112:115], v6, s[72:75], s84 offen sc1\n" \
  "s_waitcnt vmcnt(15)\n" \
  "v_mfma_f32_16x16x32_bf16 v[20:23], a[224:227], v[116:119], v[20:23]\n" \
  "v_mfma_f32_16x16x32_bf16 v[24:27], a[228:231], v[116:119], v[24:27]\n" \
  "v_mfma_f32_16x16x32_bf16 v[28:31], a[232:235], v[116:119], v[28:31]\n" \
  "v_mfma_f32_16x16x32_bf16 v[32:35], a[236:239], v[116:119], v[32:35]\n" \
  "s_add_u32 s84, s87, 18432\n" \
  "buffer_load_dwordx4 v[116:119], v6, s[72:75], s84 offen sc1\n" \
  "s_waitcnt vmcnt(15)\n" \
  "v_mfma_f32_16x16x32_bf16 v[20:23], a[240:243], v[120:123], v[20:23]\n" \
  "ds_read_b64_tr_b16 v[40:41], v16 offset:28672\n" \
  "ds_read_b64_tr_b16 v[42:43], v16 offset:30720\n" \
  "v_mfma_f32_16x16x32_bf16 v[24:27], a[244:247], v[120:123], v[24:27]\n" \
  "ds_read_b64_tr_b16 v[44:45], v17 offset:28672\n" \
  "ds_read_b64_tr_b16 v[46:47], v17 offset:30720\n" \
  "v_mfma_f32_16x16x32_bf16 v[28:31], a[248:251], v[120:123], v[28:31]\n" \
  "ds_read_b64_tr_b16 v[48:49], v18 offset:28672\n" \
  "ds_read_b64_tr_b16 v[50:51], v18 offset:30720\n" \
  "v_mfma_f32_16x16x32_bf16 v[32:35], a[252:255], v[120:123], v[32:35]\n" \
  "ds_read_b64_tr_b16 v[52:53], v19 offset:28672\n" \
  "ds_read_b64_tr_b16 v[54:55], v19 offset:30720\n" \
  "s_add_u32 s84, s87, 19456\n" \
  "buffer_load_dwordx4 v[120:123], v6, s[72:75], s84 offen sc1\n" \
  "s_waitcnt vmcnt(15)\n" \
  "s_waitcnt lgkmcnt(6)\n" \
  "v_mfma_f32_16x16x32_bf16 v[20:23], v[40:43], v[124:127], v[20:23]\n" \
  "s_waitcnt lgkmcnt(4)\n" \
  "v_mfma_f32_16x16x32_bf16 v[24:27], v[44:47], v[124:127], v[24:27]\n" \
  "s_waitcnt lgkmcnt(2)\n" \
  "v_mfma_f32_16x16x32_bf16 v[28:31], v[48:51], v[124:127], v[28:31]\n" \
  "s_waitcnt lgkmcnt(0)\n" \
  "v_mfma_f32_16x16x32_bf16 v[32:35], v[52:55], v[124:127], v[32:35]\n" \
  "s_add_u32 s84, s87, 52224\n" \
  "buffer_load_dwordx4 v[124:127], v6, s[72:75], s84 offen sc1\n" \
  "s_add_u32 s89, s89, 1\n" \
  "s_cmp_eq_u32 s89, 64\n" \
  "v_mov_b64 v[36:37], v[20:21]\n" \
  "v_mov_b64 v[38:39], v[22:23]\n" \
  "ds_write_b128 v12, v[24:27]\n" \
  "ds_write_b128 v13, v[28:31] offset:1024\n" \
  "ds_write_b128 v14, v[32:35] offset:2048\n" \
  "s_cbranch_scc0 LX_%=\n" \
  "s_waitcnt lgkmcnt(0)\n" \
  "s_barrier\n" \
  "ds_read_b128 v[52:55], v11 offset:0\n" \
  "ds_read_b128 v[56:59], v11 offset:1024\n" \
  "ds_read_b128 v[60:63], v11 offset:2048\n" \
  "s_waitcnt lgkmcnt(0)\n" \
  "v_add_f32 v36, v36, v52\n" \
  "v_add_f32 v37, v37, v53\n" \
  "v_add_f32 v38, v38, v54\n" \
  "v_add_f32 v39, v39, v55\n" \
  "v_add_f32 v56, v56, v60\n" \
  "v_add_f32 v57, v57, v61\n" \
  "v_add_f32 v58, v58, v62\n" \
  "v_add_f32 v59, v59, v63\n" \
  "v_add_f32 v36, v36, v56\n" \
  "v_add_f32 v37, v37, v57\n" \
  "v_add_f32 v38, v38, v58\n" \
  "v_add_f32 v39, v39, v59\n" \
  "v_cvt_pk_bf16_f32 v36, v36, v37\n" \
  "v_cvt_pk_bf16_f32 v37, v38, v39\n" \
  "buffer_store_dwordx2 v[36:37], v10, s[76:79], s88 offen\n" \
  "s_add_u32 s88, s88, 131072\n" \
  "v_and_b32 v10, 0x7fffffff, v10\n" \
  "s_waitcnt vmcnt(0)\n" \
  "s_branch LE_%=\n" \
  "LP2_%=:\n" \
  "buffer_load_dword v9, v8, s[80:83], 0 offen sc1\n" \
  "s_waitcnt vmcnt(0)\n" \
  "v_cmp_gt_u32 vcc, s92, v9\n" \
  "s_cbranch_vccz LP3_%=\n" \
  "s_sleep 2\n" \
  "s_add_u32 s91, s91, 1\n" \
  "s_cmp_lt_u32 s91, " STR(SPIN_CAP) "\n" \
  "s_cbranch_scc1 LP2_%=\n" \
  "v_mov_b32 v5, " STR(CW_DEAD) "*4\n" \
  "v_mov_b32 v15, 1\n" \
  "buffer_store_dword v15, v5, s[80:83], 0 offen sc1\n" \
  "s_branch LP3_%=\n" \
  "LE_%=:\n" \
  ""

#define XA_ASM \
  "s_mov_b64 s[68:69], %0\n" \
  "s_and_b32 s69, s69, 0xffff\n" \
  "s_mov_b32 s70, " STR(EX_BYTES32) "\n" \
  "s_mov_b32 s71, 0x00020000\n" \
  "s_mov_b64 s[72:73], %1\n" \
  "s_and_b32 s73, s73, 0xffff\n" \
  "s_mov_b32 s74, 4*" STR(QKV_SZ32) "\n" \
  "s_mov_b32 s75, 0x00020000\n" \
  "s_mov_b64 s[76:77], %2\n" \
  "s_and_b32 s77, s77, 0xffff\n" \
  "s_mov_b32 s78, " STR(CTRL_BYTES) "\n" \
  "s_mov_b32 s79, 0x00020000\n" \
  "v_mbcnt_lo_u32_b32 v4, -1, 0\n" \
  "v_mbcnt_hi_u32_b32 v4, -1, v4\n" \
  "v_and_b32 v116, 15, v4\n" \
  "v_lshrrev_b32 v117, 4, v4\n" \
  "v_lshlrev_b32 v7, 4, v4\n" \
  "v_add_u32 v8, 4096, v7\n" \
  "v_add_u32 v17, 0x40000, v7\n" \
  "v_add_u32 v11, %5, v7\n" \
  "v_lshrrev_b32 v5, 2, v116\n" \
  "v_and_b32 v5, 1, v5\n" \
  "v_lshl_add_u32 v5, v117, 2, v5\n" \
  "v_and_b32 v6, 2, v4\n" \
  "v_add_lshl_u32 v5, v5, v6, 4\n" \
  "v_and_b32 v6, 1, v4\n" \
  "v_lshl_add_u32 v5, v6, 3, v5\n" \
  "v_add_u32 v12, %5, v5\n" \
  "s_lshl_b32 s80, %3, 7\n" \
  "s_add_u32 s81, s80, " STR(CW_XPROG) "*4\n" \
  "v_lshl_add_u32 v15, v4, 24, s81\n" \
  "s_add_u32 s81, s80, " STR(CW_PROG) "*4\n" \
  "v_mov_b32 v13, s81\n" \
  "s_lshl_b32 s80, s80, 1\n" \
  "s_add_u32 s80, s80, " STR(CW_GPROG) "*4\n" \
  "v_writelane_b32 v13, s80, 1\n" \
  "s_add_u32 s80, s80, 128\n" \
  "v_writelane_b32 v13, s80, 2\n" \
  "v_mov_b32 v16, 0\n" \
  "v_mov_b32 v6, 0xff800000\n" \
  "v_lshlrev_b32 v5, 2, v117\n" \
  "v_cmp_gt_u32 vcc, v5, v116\n" \
  "v_mov_b32 v24, 0x3f803f80\n" \
  "v_add_u32 v5, 1, v5\n" \
  "v_cndmask_b32 v20, 0, v6, vcc\n" \
  "v_cmp_gt_u32 vcc, v5, v116\n" \
  "v_mov_b32 v25, 0x3f803f80\n" \
  "v_add_u32 v5, 1, v5\n" \
  "v_cndmask_b32 v21, 0, v6, vcc\n" \
  "v_cmp_gt_u32 vcc, v5, v116\n" \
  "v_mov_b32 v26, 0x3f803f80\n" \
  "v_add_u32 v5, 1, v5\n" \
  "v_cndmask_b32 v22, 0, v6, vcc\n" \
  "v_cmp_gt_u32 vcc, v5, v116\n" \
  "v_mov_b32 v27, 0x3f803f80\n" \
  "v_add_u32 v5, 1, v5\n" \
  "v_cndmask_b32 v23, 0, v6, vcc\n" \
  "v_mov_b64 v[130:131], 0\n" \
  "v_mov_b32 v156, 0\n" \
  "v_mov_b32 v158, 0\n" \
  "s_mov_b32 s89, 0x3e0293ee\n" \
  "s_mov_b32 s88, 0\n" \
  "s_mov_b32 s82, 0\n" \
  "s_mov_b32 s90, 0\n" \
  "s_lshr_b32 s80, s82, 2\n" \
  "s_xnor_b32 s80, s80, %4\n" \
  "s_and_b32 s80, s80, 1\n" \
  "s_lshl_b32 s80, s80, 13\n" \
  "s_lshl_b32 s83, s82, 19\n" \
  "s_or_b32 s83, s83, s80\n" \
  "s_lshl_b32 s80, %3, 14\n" \
  "s_add_u32 s83, s83, s80\n" \
  "s_add_u32 s84, s83, " STR(QKV_SZ32) "\n" \
  "s_add_u32 s85, s84, " STR(QKV_SZ32) "\n" \
  "s_mov_b32 s81, 1\n" \
  "LP0_%=:\n" \
  "buffer_load_dword v14, v13, s[76:79], 0 offen sc1\n" \
  "s_waitcnt vmcnt(0)\n" \
  "v_readlane_b32 s91, v14, 0\n" \
  "v_readlane_b32 s92, v14, 1\n" \
  "v_readlane_b32 s80, v14, 2\n" \
  "s_min_u32 s92, s92, s80\n" \
  "s_cmp_ge_u32 s92, s81\n" \
  "s_cbranch_scc1 LP1_%=\n" \
  "s_sleep 16\n" \
  "s_add_u32 s88, s88, 1\n" \
  "s_cmp_lt_u32 s88, " STR(SPIN_CAP) "\n" \
  "s_cbranch_scc1 LP0_%=\n" \
  "v_mov_b32 v5, " STR(CW_DEAD) "*4\n" \
  "v_mov_b32 v6, 1\n" \
  "buffer_store_dword v6, v5, s[76:79], 0 offen sc1\n" \
  "s_branch LP1_%=\n" \
  "LP1_%=:\n" \
  "buffer_load_dwordx4 a[64:67], v7, s[72:75], s84 offen offset:0 sc1\n" \
  "buffer_load_dwordx4 a[68:71], v7, s[72:75], s84 offen offset:1024 sc1\n" \
  "buffer_load_dwordx4 a[72:75], v7, s[72:75], s84 offen offset:2048 sc1\n" \
  "buffer_load_dwordx4 a[76:79], v7, s[72:75], s84 offen offset:3072 sc1\n" \
  "buffer_load_dwordx4 a[80:83], v8, s[72:75], s84 offen offset:0 sc1\n" \
  "buffer_load_dwordx4 a[84:87], v8, s[72:75], s84 offen offset:1024 sc1\n" \
  "buffer_load_dwordx4 a[88:91], v8, s[72:75], s84 offen offset:2048 sc1\n" \
  "buffer_load_dwordx4 a[92:95], v8, s[72:75], s84 offen offset:3072 sc1\n" \
  "buffer_load_dwordx4 a[0:3], v7, s[72:75], s83 offen offset:0 sc1\n" \
  "buffer_load_dwordx4 a[4:7], v7, s[72:75], s83 offen offset:1024 sc1\n" \
  "buffer_load_dwordx4 a[8:11], v7, s[72:75], s83 offen offset:2048 sc1\n" \
  "buffer_load_dwordx4 a[12:15], v7, s[72:75], s83 offen offset:3072 sc1\n" \
  "buffer_load_dwordx4 a[16:19], v8, s[72:75], s83 offen offset:0 sc1\n" \
  "buffer_load_dwordx4 a[20:23], v8, s[72:75], s83 offen offset:1024 sc1\n" \
  "buffer_load_dwordx4 a[24:27], v8, s[72:75], s83 offen offset:2048 sc1\n" \
  "buffer_load_dwordx4 a[28:31], v8, s[72:75], s83 offen offset:3072 sc1\n" \
  "buffer_load_dwordx4 a[96:99], v7, s[72:75], s85 offen offset:0 sc1\n" \
  "buffer_load_dwordx4 a[100:103], v7, s[72:75], s85 offen offset:1024 sc1\n" \
  "buffer_load_dwordx4 a[104:107], v7, s[72:75], s85 offen offset:2048 sc1\n" \
  "buffer_load_dwordx4 a[108:111], v7, s[72:75], s85 offen offset:3072 sc1\n" \
  "buffer_load_dwordx4 a[112:115], v8, s[72:75], s85 offen offset:0 sc1\n" \
  "buffer_load_dwordx4 a[116:119], v8, s[72:75], s85 offen offset:1024 sc1\n" \
  "buffer_load_dwordx4 a[120:123], v8, s[72:75], s85 offen offset:2048 sc1\n" \
  "buffer_load_dwordx4 a[124:127], v8, s[72:75], s85 offen offset:3072 sc1\n" \
  "s_add_u32 s80, s82, 0\n" \
  "s_lshl_b32 s80, s80, 5\n" \
  "s_add_u32 s80, s80, %3\n" \
  "s_lshl_b32 s87, s80, 14\n" \
  "s_add_u32 s87, s87, " STR(EXP_B) "<<21\n" \
  "s_lshr_b32 s80, s87, 21\n" \
  "s_lshr_b32 s80, %4, s80\n" \
  "s_and_b32 s80, s80, 1\n" \
  "s_lshl_b32 s80, s80, 13\n" \
  "s_or_b32 s87, s87, s80\n" \
  "s_waitcnt vmcnt(0)\n" \
  ".p2align 6\n" \
  "LA_%=:\n" \
  "s_waitcnt vmcnt(24)\n" \
  "s_add_u32 s81, s82, 2\n" \
  "s_min_u32 s81, s81, 64\n" \
  "s_sub_u32 s84, s81, 1\n" \
  "v_mfma_f32_16x16x32_bf16 v[116:119], a[64:67], a[0:3], v[20:23]\n" \
  "s_lshr_b32 s80, s84, 2\n" \
  "s_xnor_b32 s80, s80, %4\n" \
  "v_mfma_f32_16x16x32_bf16 v[120:123], a[64:67], a[16:19], 0\n" \
  "s_and_b32 s80, s80, 1\n" \
  "s_lshl_b32 s80, s80, 13\n" \
  "v_mfma_f32_16x16x32_bf16 v[124:127], a[80:83], a[16:19], v[20:23]\n" \
  "s_lshl_b32 s83, s84, 19\n" \
  "s_or_b32 s83, s83, s80\n" \
  "v_mfma_f32_16x16x32_bf16 v[116:119], a[68:71], a[4:7], v[116:119]\n" \
  "s_lshl_b32 s80, %3, 14\n" \
  "s_add_u32 s83, s83, s80\n" \
  "v_mfma_f32_16x16x32_bf16 v[120:123], a[68:71], a[20:23], v[120:123]\n" \
  "s_add_u32 s84, s83, " STR(QKV_SZ32) "\n" \
  "s_add_u32 s85, s84, " STR(QKV_SZ32) "\n" \
  "v_mfma_f32_16x16x32_bf16 v[124:127], a[84:87], a[20:23], v[124:127]\n" \
  "s_lshr_b32 s80, s82, 2\n" \
  "s_xnor_b32 s80, s80, %4\n" \
  "v_mfma_f32_16x16x32_bf16 v[116:119], a[72:75], a[8:11], v[116:119]\n" \
  "s_and_b32 s80, s80, 1\n" \
  "s_lshl_b32 s80, s80, 13\n" \
  "v_mfma_f32_16x16x32_bf16 v[120:123], a[72:75], a[24:27], v[120:123]\n" \
  "s_lshl_b32 s86, s82, 19\n" \
  "s_or_b32 s86, s86, s80\n" \
  "v_mfma_f32_16x16x32_bf16 v[124:127], a[88:91], a[24:27], v[124:127]\n" \
  "s_lshr_b32 s80, %3, 1\n" \
  "s_lshl_b32 s80, s80, 14\n" \
  "v_mfma_f32_16x16x32_bf16 v[116:119], a[76:79], a[12:15], v[116:119]\n" \
  "s_add_u32 s86, s86, s80\n" \
  "s_and_b32 s80, %3, 1\n" \
  "v_mfma_f32_16x16x32_bf16 v[120:123], a[76:79], a[28:31], v[120:123]\n" \
  "s_lshl_b32 s80, s80, 12\n" \
  "s_add_u32 s86, s86, s80\n" \
  "v_mfma_f32_16x16x32_bf16 v[124:127], a[92:95], a[28:31], v[124:127]\n" \
  "s_add_u32 s86, s86, 3*" STR(QKV_SZ32) "\n" \
  "s_cmp_lt_u32 s92, s81\n" \
  "s_cbranch_scc1 LG0_%=\n" \
  "LH0_%=:\n" \
  "buffer_load_dwordx4 a[64:67], v7, s[72:75], s84 offen offset:0 sc1\n" \
  "buffer_load_dwordx4 a[68:71], v7, s[72:75], s84 offen offset:1024 sc1\n" \
  "buffer_load_dwordx4 a[72:75], v7, s[72:75], s84 offen offset:2048 sc1\n" \
  "buffer_load_dwordx4 a[76:79], v7, s[72:75], s84 offen offset:3072 sc1\n" \
  "buffer_load_dwordx4 a[80:83], v8, s[72:75], s84 offen offset:0 sc1\n" \
  "buffer_load_dwordx4 a[84:87], v8, s[72:75], s84 offen offset:1024 sc1\n" \
  "v_max3_f32 v144, v116, v117, v118\n" \
  "v_max3_f32 v145, v120, v121, v122\n" \
  "v_max_f32 v144, v144, v119\n" \
  "v_max3_f32 v145, v145, v123, v124\n" \
  "v_max3_f32 v145, v145, v125, v126\n" \
  "v_max_f32 v145, v145, v127\n" \
  "v_mov_b32 v5, v144\n" \
  "v_mov_b32 v6, v145\n" \
  "s_nop 0\n" \
  "v_permlane16_swap_b32 v144, v5\n" \
  "v_permlane16_swap_b32 v145, v6\n" \
  "v_max_f32 v144, v144, v5\n" \
  "v_max_f32 v145, v145, v6\n" \
  "v_mov_b32 v5, v144\n" \
  "v_mov_b32 v6, v145\n" \
  "s_nop 0\n" \
  "v_permlane32_swap_b32 v144, v5\n" \
  "v_permlane32_swap_b32 v145, v6\n" \
  "v_max_f32 v144, v144, v5\n" \
  "v_max_f32 v145, v145, v6\n" \
  "v_mul_f32 v144, s89, v144\n" \
  "v_mul_f32 v145, s89, v145\n" \
  "v_fma_f32 v116, v116, s89, -v144\n" \
  "v_exp_f32 v116, v116\n" \
  "buffer_load_dwordx4 a[88:91], v8, s[72:75], s84 offen offset:2048 sc1\n" \
  "v_fma_f32 v117, v117, s89, -v144\n" \
  "v_exp_f32 v117, v117\n" \
  "buffer_load_dwordx4 a[92:95], v8, s[72:75], s84 offen offset:3072 sc1\n" \
  "v_fma_f32 v118, v118, s89, -v144\n" \
  "v_exp_f32 v118, v118\n" \
  "buffer_load_dwordx4 a[32:35], v7, s[72:75], s83 offen offset:0 sc1\n" \
  "v_fma_f32 v119, v119, s89, -v144\n" \
  "v_exp_f32 v119, v119\n" \
  "buffer_load_dwordx4 a[36:39], v7, s[72:75], s83 offen offset:1024 sc1\n" \
  "v_fma_f32 v120, v120, s89, -v145\n" \
  "v_exp_f32 v120, v120\n" \
  "buffer_load_dwordx4 a[40:43], v7, s[72:75], s83 offen offset:2048 sc1\n" \
  "v_fma_f32 v121, v121, s89, -v145\n" \
  "v_exp_f32 v121, v121\n" \
  "buffer_load_dwordx4 a[44:47], v7, s[72:75], s83 offen offset:3072 sc1\n" \
  "v_fma_f32 v122, v122, s89, -v145\n" \
  "v_exp_f32 v122, v122\n" \
  "buffer_load_dwordx4 a[48:51], v8, s[72:75], s83 offen offset:0 sc1\n" \
  "v_fma_f32 v123, v123, s89, -v145\n" \
  "v_exp_f32 v123, v123\n" \
  "buffer_load_dwordx4 a[52:55], v8, s[72:75], s83 offen offset:1024 sc1\n" \
  "v_fma_f32 v124, v124, s89, -v145\n" \
  "v_exp_f32 v124, v124\n" \
  "buffer_load_dwordx4 a[56:59], v8, s[72:75], s83 offen offset:2048 sc1\n" \
  "v_fma_f32 v125, v125, s89, -v145\n" \
  "v_exp_f32 v125, v125\n" \
  "buffer_load_dwordx4 a[60:63], v8, s[72:75], s83 offen offset:3072 sc1\n" \
  "v_fma_f32 v126, v126, s89, -v145\n" \
  "v_exp_f32 v126, v126\n" \
  "v_fma_f32 v127, v127, s89, -v145\n" \
  "v_exp_f32 v127, v127\n" \
  "s_nop 0\n" \
  "v_cvt_pk_bf16_f32 v128, v116, v117\n" \
  "v_cvt_pk_bf16_f32 v129, v118, v119\n" \
  "v_cvt_pk_bf16_f32 v132, v120, v121\n" \
  "v_cvt_pk_bf16_f32 v133, v122, v123\n" \
  "v_cvt_pk_bf16_f32 v134, v124, v125\n" \
  "v_cvt_pk_bf16_f32 v135, v126, v127\n" \
  "s_waitcnt vmcnt(22)\n" \
  "buffer_store_dword v16, v15, s[76:79], 0 offen sc1\n" \
  "v_mfma_f32_16x16x32_bf16 v[136:139], v[24:27], v[128:131], 0\n" \
  "v_mfma_f32_16x16x32_bf16 v[140:143], v[24:27], v[132:135], 0\n" \
  "v_add_u32 v16, 1, v16\n" \
  "v_mfma_f32_16x16x32_bf16 v[52:55], a[96:99], v[128:131], 0\n" \
  "v_mfma_f32_16x16x32_bf16 v[84:87], a[96:99], v[132:135], 0\n" \
  "buffer_load_dwordx4 a[96:99], v7, s[72:75], s85 offen offset:0 sc1\n" \
  "v_mfma_f32_16x16x32_bf16 v[56:59], a[100:103], v[128:131], 0\n" \
  "v_mfma_f32_16x16x32_bf16 v[88:91], a[100:103], v[132:135], 0\n" \
  "buffer_load_dwordx4 a[100:103], v7, s[72:75], s85 offen offset:1024 sc1\n" \
  "v_mfma_f32_16x16x32_bf16 v[60:63], a[104:107], v[128:131], 0\n" \
  "v_mfma_f32_16x16x32_bf16 v[92:95], a[104:107], v[132:135], 0\n" \
  "buffer_load_dwordx4 a[104:107], v7, s[72:75], s85 offen offset:2048 sc1\n" \
  "v_mfma_f32_16x16x32_bf16 v[64:67], a[108:111], v[128:131], 0\n" \
  "v_mfma_f32_16x16x32_bf16 v[96:99], a[108:111], v[132:135], 0\n" \
  "buffer_load_dwordx4 a[108:111], v7, s[72:75], s85 offen offset:3072 sc1\n" \
  "v_mfma_f32_16x16x32_bf16 v[68:71], a[112:115], v[128:131], 0\n" \
  "v_mfma_f32_16x16x32_bf16 v[100:103], a[112:115], v[132:135], 0\n" \
  "buffer_load_dwordx4 a[112:115], v8, s[72:75], s85 offen offset:0 sc1\n" \
  "v_mfma_f32_16x16x32_bf16 v[72:75], a[116:119], v[128:131], 0\n" \
  "v_mfma_f32_16x16x32_bf16 v[104:107], a[116:119], v[132:135], 0\n" \
  "buffer_load_dwordx4 a[116:119], v8, s[72:75], s85 offen offset:1024 sc1\n" \
  "v_mfma_f32_16x16x32_bf16 v[76:79], a[120:123], v[128:131], 0\n" \
  "v_mfma_f32_16x16x32_bf16 v[108:111], a[120:123], v[132:135], 0\n" \
  "buffer_load_dwordx4 a[120:123], v8, s[72:75], s85 offen offset:2048 sc1\n" \
  "v_mfma_f32_16x16x32_bf16 v[80:83], a[124:127], v[128:131], 0\n" \
  "v_mfma_f32_16x16x32_bf16 v[112:115], a[124:127], v[132:135], 0\n" \
  "buffer_load_dwordx4 a[124:127], v8, s[72:75], s85 offen offset:3072 sc1\n" \
  "buffer_load_dword v14, v13, s[76:79], 0 offen sc1\n" \
  "s_cmp_eq_u32 s90, 0\n" \
  "s_cbranch_scc1 LW0_%=\n" \
  "s_waitcnt vmcnt(26)\n" \
  "LM0_%=:\n" \
  "ds_write_b128 v11, v[44:47]\n" \
  "ds_write_b128 v11, v[48:51] offset:1024\n" \
  "v_mfma_f32_16x16x32_bf16 v[148:151], v[28:31], a[0:3], 0\n" \
  "v_max3_u32 v5, v28, v29, v30\n" \
  "v_max3_u32 v5, v5, v31, v32\n" \
  "v_mfma_f32_16x16x32_bf16 v[152:155], v[28:31], a[16:19], 0\n" \
  "v_max3_u32 v5, v5, v33, v34\n" \
  "v_max3_u32 v5, v5, v35, v36\n" \
  "v_mfma_f32_16x16x32_bf16 v[148:151], v[32:35], a[4:7], v[148:151]\n" \
  "v_max3_u32 v5, v5, v37, v38\n" \
  "v_max3_u32 v5, v5, v39, v40\n" \
  "v_mfma_f32_16x16x32_bf16 v[152:155], v[32:35], a[20:23], v[152:155]\n" \
  "v_max3_u32 v5, v5, v41, v42\n" \
  "v_max3_u32 v5, v5, v43, v44\n" \
  "v_mfma_f32_16x16x32_bf16 v[148:151], v[36:39], a[8:11], v[148:151]\n" \
  "v_max3_u32 v5, v5, v45, v46\n" \
  "v_max3_u32 v5, v5, v47, v48\n" \
  "v_mfma_f32_16x16x32_bf16 v[152:155], v[36:39], a[24:27], v[152:155]\n" \
  "v_max3_u32 v5, v5, v49, v50\n" \
  "v_max_u32 v5, v5, v51\n" \
  "v_mfma_f32_16x16x32_bf16 v[148:151], v[40:43], a[12:15], v[148:151]\n" \
  "v_cmp_eq_u32 vcc, -1, v5\n" \
  "v_mfma_f32_16x16x32_bf16 v[152:155], v[40:43], a[28:31], v[152:155]\n" \
  "ds_read_b64_tr_b16 a[128:129], v12 offset:0\n" \
  "ds_read_b64_tr_b16 a[130:131], v12 offset:256\n" \
  "ds_read_b64_tr_b16 a[132:133], v12 offset:512\n" \
  "ds_read_b64_tr_b16 a[134:135], v12 offset:768\n" \
  "ds_read_b64_tr_b16 a[136:137], v12 offset:1024\n" \
  "ds_read_b64_tr_b16 a[138:139], v12 offset:1280\n" \
  "ds_read_b64_tr_b16 a[140:141], v12 offset:1536\n" \
  "ds_read_b64_tr_b16 a[142:143], v12 offset:1792\n" \
  "s_cbranch_vccnz LR0_%=\n" \
  "v_max_f32 v146, v150, v151\n" \
  "v_max_f32 v147, v154, v155\n" \
  "v_mov_b32 v5, v146\n" \
  "v_mov_b32 v6, v147\n" \
  "s_nop 0\n" \
  "v_permlane16_swap_b32 v146, v5\n" \
  "v_permlane16_swap_b32 v147, v6\n" \
  "v_max_f32 v146, v146, v5\n" \
  "v_max_f32 v147, v147, v6\n" \
  "v_mov_b32 v5, v146\n" \
  "v_mov_b32 v6, v147\n" \
  "s_nop 0\n" \
  "v_permlane32_swap_b32 v146, v5\n" \
  "v_permlane32_swap_b32 v147, v6\n" \
  "v_max_f32 v146, v146, v5\n" \
  "v_max_f32 v147, v147, v6\n" \
  "v_mul_f32 v146, s89, v146\n" \
  "v_mul_f32 v147, s89, v147\n" \
  "v_max_f32 v146, v146, v144\n" \
  "v_max_f32 v147, v147, v145\n" \
  "v_sub_f32 v160, v144, v146\n" \
  "v_sub_f32 v162, v145, v147\n" \
  "v_fma_f32 v150, v150, s89, -v146\n" \
  "v_fma_f32 v151, v151, s89, -v146\n" \
  "v_fma_f32 v154, v154, s89, -v147\n" \
  "v_fma_f32 v155, v155, s89, -v147\n" \
  "v_exp_f32 v150, v150\n" \
  "v_exp_f32 v151, v151\n" \
  "v_exp_f32 v154, v154\n" \
  "v_exp_f32 v155, v155\n" \
  "v_exp_f32 v160, v160\n" \
  "v_exp_f32 v162, v162\n" \
  "v_add_f32 v146, v150, v151\n" \
  "v_add_f32 v147, v154, v155\n" \
  "v_mov_b32 v5, v146\n" \
  "v_mov_b32 v6, v147\n" \
  "s_nop 0\n" \
  "v_permlane16_swap_b32 v146, v5\n" \
  "v_permlane16_swap_b32 v147, v6\n" \
  "v_add_f32 v146, v146, v5\n" \
  "v_add_f32 v147, v147, v6\n" \
  "v_mov_b32 v5, v146\n" \
  "v_mov_b32 v6, v147\n" \
  "s_nop 0\n" \
  "v_permlane32_swap_b32 v146, v5\n" \
  "v_permlane32_swap_b32 v147, v6\n" \
  "v_add_f32 v146, v146, v5\n" \
  "v_add_f32 v147, v147, v6\n" \
  "v_fma_f32 v146, v160, v136, v146\n" \
  "v_fma_f32 v147, v162, v140, v147\n" \
  "v_rcp_f32 v146, v146\n" \
  "v_rcp_f32 v147, v147\n" \
  "v_mul_f32 v160, v160, v146\n" \
  "v_mul_f32 v162, v162, v147\n" \
  "v_mul_f32 v150, v150, v146\n" \
  "v_mul_f32 v151, v151, v146\n" \
  "v_mul_f32 v154, v154, v147\n" \
  "v_mul_f32 v155, v155, v147\n" \
  "v_cvt_pk_bf16_f32 v157, v150, v151\n" \
  "v_cvt_pk_bf16_f32 v159, v154, v155\n" \
  "v_pk_mul_f32 v[52:53], v[52:53], v[160:161] op_sel_hi:[1,0]\n" \
  "v_pk_mul_f32 v[54:55], v[54:55], v[160:161] op_sel_hi:[1,0]\n" \
  "v_pk_mul_f32 v[56:57], v[56:57], v[160:161] op_sel_hi:[1,0]\n" \
  "v_pk_mul_f32 v[58:59], v[58:59], v[160:161] op_sel_hi:[1,0]\n" \
  "v_pk_mul_f32 v[60:61], v[60:61], v[160:161] op_sel_hi:[1,0]\n" \
  "v_pk_mul_f32 v[62:63], v[62:63], v[160:161] op_sel_hi:[1,0]\n" \
  "v_pk_mul_f32 v[64:65], v[64:65], v[160:161] op_sel_hi:[1,0]\n" \
  "v_pk_mul_f32 v[66:67], v[66:67], v[160:161] op_sel_hi:[1,0]\n" \
  "v_pk_mul_f32 v[68:69], v[68:69], v[160:161] op_sel_hi:[1,0]\n" \
  "v_pk_mul_f32 v[70:71], v[70:71], v[160:161] op_sel_hi:[1,0]\n" \
  "v_pk_mul_f32 v[72:73], v[72:73], v[160:161] op_sel_hi:[1,0]\n" \
  "v_pk_mul_f32 v[74:75], v[74:75], v[160:161] op_sel_hi:[1,0]\n" \
  "v_pk_mul_f32 v[76:77], v[76:77], v[160:161] op_sel_hi:[1,0]\n" \
  "v_pk_mul_f32 v[78:79], v[78:79], v[160:161] op_sel_hi:[1,0]\n" \
  "v_pk_mul_f32 v[80:81], v[80:81], v[160:161] op_sel_hi:[1,0]\n" \
  "v_pk_mul_f32 v[82:83], v[82:83], v[160:161] op_sel_hi:[1,0]\n" \
  "v_pk_mul_f32 v[84:85], v[84:85], v[162:163] op_sel_hi:[1,0]\n" \
  "v_pk_mul_f32 v[86:87], v[86:87], v[162:163] op_sel_hi:[1,0]\n" \
  "v_pk_mul_f32 v[88:89], v[88:89], v[162:163] op_sel_hi:[1,0]\n" \
  "v_pk_mul_f32 v[90:91], v[90:91], v[162:163] op_sel_hi:[1,0]\n" \
  "v_pk_mul_f32 v[92:93], v[92:93], v[162:163] op_sel_hi:[1,0]\n" \
  "v_pk_mul_f32 v[94:95], v[94:95], v[162:163] op_sel_hi:[1,0]\n" \
  "v_pk_mul_f32 v[96:97], v[96:97], v[162:163] op_sel_hi:[1,0]\n" \
  "v_pk_mul_f32 v[98:99], v[98:99], v[162:163] op_sel_hi:[1,0]\n" \
  "v_pk_mul_f32 v[100:101], v[100:101], v[162:163] op_sel_hi:[1,0]\n" \
  "v_pk_mul_f32 v[102:103], v[102:103], v[162:163] op_sel_hi:[1,0]\n" \
  "v_pk_mul_f32 v[104:105], v[104:105], v[162:163] op_sel_hi:[1,0]\n" \
  "v_pk_mul_f32 v[106:107], v[106:107], v[162:163] op_sel_hi:[1,0]\n" \
  "v_pk_mul_f32 v[108:109], v[108:109], v[162:163] op_sel_hi:[1,0]\n" \
  "v_pk_mul_f32 v[110:111], v[110:111], v[162:163] op_sel_hi:[1,0]\n" \
  "v_pk_mul_f32 v[112:113], v[112:113], v[162:163] op_sel_hi:[1,0]\n" \
  "v_pk_mul_f32 v[114:115], v[114:115], v[162:163] op_sel_hi:[1,0]\n" \
  "s_waitcnt lgkmcnt(0)\n" \
  "v_mfma_f32_16x16x16_bf16 v[52:55], a[128:129], v[156:157], v[52:55]\n" \
  "v_mfma_f32_16x16x16_bf16 v[56:59], a[130:131], v[156:157], v[56:59]\n" \
  "v_mfma_f32_16x16x16_bf16 v[60:63], a[132:133], v[156:157], v[60:63]\n" \
  "v_mfma_f32_16x16x16_bf16 v[64:67], a[134:135], v[156:157], v[64:67]\n" \
  "v_mfma_f32_16x16x16_bf16 v[68:71], a[136:137], v[156:157], v[68:71]\n" \
  "v_cvt_pk_bf16_f32 v164, v52, v53\n" \
  "v_cvt_pk_bf16_f32 v165, v54, v55\n" \
  "v_mfma_f32_16x16x16_bf16 v[72:75], a[138:139], v[156:157], v[72:75]\n" \
  "v_cvt_pk_bf16_f32 v166, v56, v57\n" \
  "v_cvt_pk_bf16_f32 v167, v58, v59\n" \
  "buffer_store_dwordx4 v[164:167], v7, s[72:75], s86 offen offset:0 sc1\n" \
  "v_mfma_f32_16x16x16_bf16 v[76:79], a[140:141], v[156:157], v[76:79]\n" \
  "v_cvt_pk_bf16_f32 v168, v60, v61\n" \
  "v_cvt_pk_bf16_f32 v169, v62, v63\n" \
  "v_mfma_f32_16x16x16_bf16 v[80:83], a[142:143], v[156:157], v[80:83]\n" \
  "v_cvt_pk_bf16_f32 v170, v64, v65\n" \
  "v_cvt_pk_bf16_f32 v171, v66, v67\n" \
  "buffer_store_dwordx4 v[168:171], v7, s[72:75], s86 offen offset:1024 sc1\n" \
  "v_mfma_f32_16x16x16_bf16 v[84:87], a[128:129], v[158:159], v[84:87]\n" \
  "v_cvt_pk_bf16_f32 v172, v68, v69\n" \
  "v_cvt_pk_bf16_f32 v173, v70, v71\n" \
  "v_mfma_f32_16x16x16_bf16 v[88:91], a[130:131], v[158:159], v[88:91]\n" \
  "v_cvt_pk_bf16_f32 v174, v72, v73\n" \
  "v_cvt_pk_bf16_f32 v175, v74, v75\n" \
  "buffer_store_dwordx4 v[172:175], v7, s[72:75], s86 offen offset:2048 sc1\n" \
  "v_mfma_f32_16x16x16_bf16 v[92:95], a[132:133], v[158:159], v[92:95]\n" \
  "v_cvt_pk_bf16_f32 v176, v76, v77\n" \
  "v_cvt_pk_bf16_f32 v177, v78, v79\n" \
  "v_mfma_f32_16x16x16_bf16 v[96:99], a[134:135], v[158:159], v[96:99]\n" \
  "v_cvt_pk_bf16_f32 v178, v80, v81\n" \
  "v_cvt_pk_bf16_f32 v179, v82, v83\n" \
  "buffer_store_dwordx4 v[176:179], v7, s[72:75], s86 offen offset:3072 sc1\n" \
  "v_mfma_f32_16x16x16_bf16 v[100:103], a[136:137], v[158:159], v[100:103]\n" \
  "v_cvt_pk_bf16_f32 v180, v84, v85\n" \
  "v_cvt_pk_bf16_f32 v181, v86, v87\n" \
  "v_mfma_f32_16x16x16_bf16 v[104:107], a[138:139], v[158:159], v[104:107]\n" \
  "v_cvt_pk_bf16_f32 v182, v88, v89\n" \
  "v_cvt_pk_bf16_f32 v183, v90, v91\n" \
  "buffer_store_dwordx4 v[180:183], v17, s[72:75], s86 offen offset:0 sc1\n" \
  "v_mfma_f32_16x16x16_bf16 v[108:111], a[140:141], v[158:159], v[108:111]\n" \
  "v_cvt_pk_bf16_f32 v184, v92, v93\n" \
  "v_cvt_pk_bf16_f32 v185, v94, v95\n" \
  "v_mfma_f32_16x16x16_bf16 v[112:115], a[142:143], v[158:159], v[112:115]\n" \
  "v_cvt_pk_bf16_f32 v186, v96, v97\n" \
  "v_cvt_pk_bf16_f32 v187, v98, v99\n" \
  "buffer_store_dwordx4 v[184:187], v17, s[72:75], s86 offen offset:1024 sc1\n" \
  "v_cvt_pk_bf16_f32 v188, v100, v101\n" \
  "v_cvt_pk_bf16_f32 v189, v102, v103\n" \
  "v_cvt_pk_bf16_f32 v190, v104, v105\n" \
  "v_cvt_pk_bf16_f32 v191, v106, v107\n" \
  "buffer_store_dwordx4 v[188:191], v17, s[72:75], s86 offen offset:2048 sc1\n" \
  "s_nop 1\n" \
  "v_cvt_pk_bf16_f32 v192, v108, v109\n" \
  "v_cvt_pk_bf16_f32 v193, v110, v111\n" \
  "v_cvt_pk_bf16_f32 v194, v112, v113\n" \
  "v_cvt_pk_bf16_f32 v195, v114, v115\n" \
  "buffer_store_dwordx4 v[192:195], v17, s[72:75], s86 offen offset:3072 sc1\n" \
  "s_add_u32 s82, s82, 1\n" \
  "s_cmp_eq_u32 s82, 64\n" \
  "s_cbranch_scc1 LZ_%=\n" \
  "s_add_u32 s80, s82, 0\n" \
  "s_lshl_b32 s80, s80, 5\n" \
  "s_add_u32 s80, s80, %3\n" \
  "s_lshl_b32 s87, s80, 14\n" \
  "s_add_u32 s87, s87, " STR(EXP_B) "<<21\n" \
  "s_lshr_b32 s80, s87, 21\n" \
  "s_lshr_b32 s80, %4, s80\n" \
  "s_and_b32 s80, s80, 1\n" \
  "s_lshl_b32 s80, s80, 13\n" \
  "s_or_b32 s87, s87, s80\n" \
  "s_waitcnt vmcnt(8)\n" \
  "v_readlane_b32 s91, v14, 0\n" \
  "v_readlane_b32 s92, v14, 1\n" \
  "v_readlane_b32 s80, v14, 2\n" \
  "s_min_u32 s92, s92, s80\n" \
  "s_cmp_gt_u32 s91, s82\n" \
  "s_cselect_b32 s90, 1, 0\n" \
  "s_cselect_b32 s80, 0, 0x80000000\n" \
  "v_or_b32 v9, s80, v7\n" \
  "v_or_b32 v10, s80, v8\n" \
  "buffer_load_dwordx4 v[28:31], v9, s[68:71], s87 offen offset:0 sc1\n" \
  "buffer_load_dwordx4 v[32:35], v9, s[68:71], s87 offen offset:1024 sc1\n" \
  "buffer_load_dwordx4 v[36:39], v9, s[68:71], s87 offen offset:2048 sc1\n" \
  "buffer_load_dwordx4 v[40:43], v9, s[68:71], s87 offen offset:3072 sc1\n" \
  "buffer_load_dwordx4 v[44:47], v10, s[68:71], s87 offen offset:0 sc1\n" \
  "buffer_load_dwordx4 v[48:51], v10, s[68:71], s87 offen offset:1024 sc1\n" \
  "s_waitcnt vmcnt(24)\n" \
  "s_add_u32 s81, s82, 2\n" \
  "s_min_u32 s81, s81, 64\n" \
  "s_sub_u32 s84, s81, 1\n" \
  "v_mfma_f32_16x16x32_bf16 v[116:119], a[64:67], a[32:35], v[20:23]\n" \
  "s_lshr_b32 s80, s84, 2\n" \
  "s_xnor_b32 s80, s80, %4\n" \
  "v_mfma_f32_16x16x32_bf16 v[120:123], a[64:67], a[48:51], 0\n" \
  "s_and_b32 s80, s80, 1\n" \
  "s_lshl_b32 s80, s80, 13\n" \
  "v_mfma_f32_16x16x32_bf16 v[124:127], a[80:83], a[48:51], v[20:23]\n" \
  "s_lshl_b32 s83, s84, 19\n" \
  "s_or_b32 s83, s83, s80\n" \
  "v_mfma_f32_16x16x32_bf16 v[116:119], a[68:71], a[36:39], v[116:119]\n" \
  "s_lshl_b32 s80, %3, 14\n" \
  "s_add_u32 s83, s83, s80\n" \
  "v_mfma_f32_16x16x32_bf16 v[120:123], a[68:71], a[52:55], v[120:123]\n" \
  "s_add_u32 s84, s83, " STR(QKV_SZ32) "\n" \
  "s_add_u32 s85, s84, " STR(QKV_SZ32) "\n" \
  "v_mfma_f32_16x16x32_bf16 v[124:127], a[84:87], a[52:55], v[124:127]\n" \
  "s_lshr_b32 s80, s82, 2\n" \
  "s_xnor_b32 s80, s80, %4\n" \
  "v_mfma_f32_16x16x32_bf16 v[116:119], a[72:75], a[40:43], v[116:119]\n" \
  "s_and_b32 s80, s80, 1\n" \
  "s_lshl_b32 s80, s80, 13\n" \
  "v_mfma_f32_16x16x32_bf16 v[120:123], a[72:75], a[56:59], v[120:123]\n" \
  "s_lshl_b32 s86, s82, 19\n" \
  "s_or_b32 s86, s86, s80\n" \
  "v_mfma_f32_16x16x32_bf16 v[124:127], a[88:91], a[56:59], v[124:127]\n" \
  "s_lshr_b32 s80, %3, 1\n" \
  "s_lshl_b32 s80, s80, 14\n" \
  "v_mfma_f32_16x16x32_bf16 v[116:119], a[76:79], a[44:47], v[116:119]\n" \
  "s_add_u32 s86, s86, s80\n" \
  "s_and_b32 s80, %3, 1\n" \
  "v_mfma_f32_16x16x32_bf16 v[120:123], a[76:79], a[60:63], v[120:123]\n" \
  "s_lshl_b32 s80, s80, 12\n" \
  "s_add_u32 s86, s86, s80\n" \
  "v_mfma_f32_16x16x32_bf16 v[124:127], a[92:95], a[60:63], v[124:127]\n" \
  "s_add_u32 s86, s86, 3*" STR(QKV_SZ32) "\n" \
  "s_cmp_lt_u32 s92, s81\n" \
  "s_cbranch_scc1 LG1_%=\n" \
  "LH1_%=:\n" \
  "buffer_load_dwordx4 a[64:67], v7, s[72:75], s84 offen offset:0 sc1\n" \
  "buffer_load_dwordx4 a[68:71], v7, s[72:75], s84 offen offset:1024 sc1\n" \
  "buffer_load_dwordx4 a[72:75], v7, s[72:75], s84 offen offset:2048 sc1\n" \
  "buffer_load_dwordx4 a[76:79], v7, s[72:75], s84 offen offset:3072 sc1\n" \
  "buffer_load_dwordx4 a[80:83], v8, s[72:75], s84 offen offset:0 sc1\n" \
  "buffer_load_dwordx4 a[84:87], v8, s[72:75], s84 offen offset:1024 sc1\n" \
  "v_max3_f32 v144, v116, v117, v118\n" \
  "v_max3_f32 v145, v120, v121, v122\n" \
  "v_max_f32 v144, v144, v119\n" \
  "v_max3_f32 v145, v145, v123, v124\n" \
  "v_max3_f32 v145, v145, v125, v126\n" \
  "v_max_f32 v145, v145, v127\n" \
  "v_mov_b32 v5, v144\n" \
  "v_mov_b32 v6, v145\n" \
  "s_nop 0\n" \
  "v_permlane16_swap_b32 v144, v5\n" \
  "v_permlane16_swap_b32 v145, v6\n" \
  "v_max_f32 v144, v144, v5\n" \
  "v_max_f32 v145, v145, v6\n" \
  "v_mov_b32 v5, v144\n" \
  "v_mov_b32 v6, v145\n" \
  "s_nop 0\n" \
  "v_permlane32_swap_b32 v144, v5\n" \
  "v_permlane32_swap_b32 v145, v6\n" \
  "v_max_f32 v144, v144, v5\n" \
  "v_max_f32 v145, v145, v6\n" \
  "v_mul_f32 v144, s89, v144\n" \
  "v_mul_f32 v145, s89, v145\n" \
  "v_fma_f32 v116, v116, s89, -v144\n" \
  "v_exp_f32 v116, v116\n" \
  "buffer_load_dwordx4 a[88:91], v8, s[72:75], s84 offen offset:2048 sc1\n" \
  "v_fma_f32 v117, v117, s89, -v144\n" \
  "v_exp_f32 v117, v117\n" \
  "buffer_load_dwordx4 a[92:95], v8, s[72:75], s84 offen offset:3072 sc1\n" \
  "v_fma_f32 v118, v118, s89, -v144\n" \
  "v_exp_f32 v118, v118\n" \
  "buffer_load_dwordx4 a[0:3], v7, s[72:75], s83 offen offset:0 sc1\n" \
  "v_fma_f32 v119, v119, s89, -v144\n" \
  "v_exp_f32 v119, v119\n" \
  "buffer_load_dwordx4 a[4:7], v7, s[72:75], s83 offen offset:1024 sc1\n" \
  "v_fma_f32 v120, v120, s89, -v145\n" \
  "v_exp_f32 v120, v120\n" \
  "buffer_load_dwordx4 a[8:11], v7, s[72:75], s83 offen offset:2048 sc1\n" \
  "v_fma_f32 v121, v121, s89, -v145\n" \
  "v_exp_f32 v121, v121\n" \
  "buffer_load_dwordx4 a[12:15], v7, s[72:75], s83 offen offset:3072 sc1\n" \
  "v_fma_f32 v122, v122, s89, -v145\n" \
  "v_exp_f32 v122, v122\n" \
  "buffer_load_dwordx4 a[16:19], v8, s[72:75], s83 offen offset:0 sc1\n" \
  "v_fma_f32 v123, v123, s89, -v145\n" \
  "v_exp_f32 v123, v123\n" \
  "buffer_load_dwordx4 a[20:23], v8, s[72:75], s83 offen offset:1024 sc1\n" \
  "v_fma_f32 v124, v124, s89, -v145\n" \
  "v_exp_f32 v124, v124\n" \
  "buffer_load_dwordx4 a[24:27], v8, s[72:75], s83 offen offset:2048 sc1\n" \
  "v_fma_f32 v125, v125, s89, -v145\n" \
  "v_exp_f32 v125, v125\n" \
  "buffer_load_dwordx4 a[28:31], v8, s[72:75], s83 offen offset:3072 sc1\n" \
  "v_fma_f32 v126, v126, s89, -v145\n" \
  "v_exp_f32 v126, v126\n" \
  "v_fma_f32 v127, v127, s89, -v145\n" \
  "v_exp_f32 v127, v127\n" \
  "s_nop 0\n" \
  "v_cvt_pk_bf16_f32 v128, v116, v117\n" \
  "v_cvt_pk_bf16_f32 v129, v118, v119\n" \
  "v_cvt_pk_bf16_f32 v132, v120, v121\n" \
  "v_cvt_pk_bf16_f32 v133, v122, v123\n" \
  "v_cvt_pk_bf16_f32 v134, v124, v125\n" \
  "v_cvt_pk_bf16_f32 v135, v126, v127\n" \
  "s_waitcnt vmcnt(22)\n" \
  "buffer_store_dword v16, v15, s[76:79], 0 offen sc1\n" \
  "v_mfma_f32_16x16x32_bf16 v[136:139], v[24:27], v[128:131], 0\n" \
  "v_mfma_f32_16x16x32_bf16 v[140:143], v[24:27], v[132:135], 0\n" \
  "v_add_u32 v16, 1, v16\n" \
  "v_mfma_f32_16x16x32_bf16 v[52:55], a[96:99], v[128:131], 0\n" \
  "v_mfma_f32_16x16x32_bf16 v[84:87], a[96:99], v[132:135], 0\n" \
  "buffer_load_dwordx4 a[96:99], v7, s[72:75], s85 offen offset:0 sc1\n" \
  "v_mfma_f32_16x16x32_bf16 v[56:59], a[100:103], v[128:131], 0\n" \
  "v_mfma_f32_16x16x32_bf16 v[88:91], a[100:103], v[132:135], 0\n" \
  "buffer_load_dwordx4 a[100:103], v7, s[72:75], s85 offen offset:1024 sc1\n" \
  "v_mfma_f32_16x16x32_bf16 v[60:63], a[104:107], v[128:131], 0\n" \
  "v_mfma_f32_16x16x32_bf16 v[92:95], a[104:107], v[132:135], 0\n" \
  "buffer_load_dwordx4 a[104:107], v7, s[72:75], s85 offen offset:2048 sc1\n" \
  "v_mfma_f32_16x16x32_bf16 v[64:67], a[108:111], v[128:131], 0\n" \
  "v_mfma_f32_16x16x32_bf16 v[96:99], a[108:111], v[132:135], 0\n" \
  "buffer_load_dwordx4 a[108:111], v7, s[72:75], s85 offen offset:3072 sc1\n" \
  "v_mfma_f32_16x16x32_bf16 v[68:71], a[112:115], v[128:131], 0\n" \
  "v_mfma_f32_16x16x32_bf16 v[100:103], a[112:115], v[132:135], 0\n" \
  "buffer_load_dwordx4 a[112:115], v8, s[72:75], s85 offen offset:0 sc1\n" \
  "v_mfma_f32_16x16x32_bf16 v[72:75], a[116:119], v[128:131], 0\n" \
  "v_mfma_f32_16x16x32_bf16 v[104:107], a[116:119], v[132:135], 0\n" \
  "buffer_load_dwordx4 a[116:119], v8, s[72:75], s85 offen offset:1024 sc1\n" \
  "v_mfma_f32_16x16x32_bf16 v[76:79], a[120:123], v[128:131], 0\n" \
  "v_mfma_f32_16x16x32_bf16 v[108:111], a[120:123], v[132:135], 0\n" \
  "buffer_load_dwordx4 a[120:123], v8, s[72:75], s85 offen offset:2048 sc1\n" \
  "v_mfma_f32_16x16x32_bf16 v[80:83], a[124:127], v[128:131], 0\n" \
  "v_mfma_f32_16x16x32_bf16 v[112:115], a[124:127], v[132:135], 0\n" \
  "buffer_load_dwordx4 a[124:127], v8, s[72:75], s85 offen offset:3072 sc1\n" \
  "buffer_load_dword v14, v13, s[76:79], 0 offen sc1\n" \
  "s_cmp_eq_u32 s90, 0\n" \
  "s_cbranch_scc1 LW1_%=\n" \
  "s_waitcnt vmcnt(26)\n" \
  "LM1_%=:\n" \
  "ds_write_b128 v11, v[44:47]\n" \
  "ds_write_b128 v11, v[48:51] offset:1024\n" \
  "v_mfma_f32_16x16x32_bf16 v[148:151], v[28:31], a[32:35], 0\n" \
  "v_max3_u32 v5, v28, v29, v30\n" \
  "v_max3_u32 v5, v5, v31, v32\n" \
  "v_mfma_f32_16x16x32_bf16 v[152:155], v[28:31], a[48:51], 0\n" \
  "v_max3_u32 v5, v5, v33, v34\n" \
  "v_max3_u32 v5, v5, v35, v36\n" \
  "v_mfma_f32_16x16x32_bf16 v[148:151], v[32:35], a[36:39], v[148:151]\n" \
  "v_max3_u32 v5, v5, v37, v38\n" \
  "v_max3_u32 v5, v5, v39, v40\n" \
  "v_mfma_f32_16x16x32_bf16 v[152:155], v[32:35], a[52:55], v[152:155]\n" \
  "v_max3_u32 v5, v5, v41, v42\n" \
  "v_max3_u32 v5, v5, v43, v44\n" \
  "v_mfma_f32_16x16x32_bf16 v[148:151], v[36:39], a[40:43], v[148:151]\n" \
  "v_max3_u32 v5, v5, v45, v46\n" \
  "v_max3_u32 v5, v5, v47, v48\n" \
  "v_mfma_f32_16x16x32_bf16 v[152:155], v[36:39], a[56:59], v[152:155]\n" \
  "v_max3_u32 v5, v5, v49, v50\n" \
  "v_max_u32 v5, v5, v51\n" \
  "v_mfma_f32_16x16x32_bf16 v[148:151], v[40:43], a[44:47], v[148:151]\n" \
  "v_cmp_eq_u32 vcc, -1, v5\n" \
  "v_mfma_f32_16x16x32_bf16 v[152:155], v[40:43], a[60:63], v[152:155]\n" \
  "ds_read_b64_tr_b16 a[128:129], v12 offset:0\n" \
  "ds_read_b64_tr_b16 a[130:131], v12 offset:256\n" \
  "ds_read_b64_tr_b16 a[132:133], v12 offset:512\n" \
  "ds_read_b64_tr_b16 a[134:135], v12 offset:768\n" \
  "ds_read_b64_tr_b16 a[136:137], v12 offset:1024\n" \
  "ds_read_b64_tr_b16 a[138:139], v12 offset:1280\n" \
  "ds_read_b64_tr_b16 a[140:141], v12 offset:1536\n" \
  "ds_read_b64_tr_b16 a[142:143], v12 offset:1792\n" \
  "s_cbranch_vccnz LR1_%=\n" \
  "v_max_f32 v146, v150, v151\n" \
  "v_max_f32 v147, v154, v155\n" \
  "v_mov_b32 v5, v146\n" \
  "v_mov_b32 v6, v147\n" \
  "s_nop 0\n" \
  "v_permlane16_swap_b32 v146, v5\n" \
  "v_permlane16_swap_b32 v147, v6\n" \
  "v_max_f32 v146, v146, v5\n" \
  "v_max_f32 v147, v147, v6\n" \
  "v_mov_b32 v5, v146\n" \
  "v_mov_b32 v6, v147\n" \
  "s_nop 0\n" \
  "v_permlane32_swap_b32 v146, v5\n" \
  "v_permlane32_swap_b32 v147, v6\n" \
  "v_max_f32 v146, v146, v5\n" \
  "v_max_f32 v147, v147, v6\n" \
  "v_mul_f32 v146, s89, v146\n" \
  "v_mul_f32 v147, s89, v147\n" \
  "v_max_f32 v146, v146, v144\n" \
  "v_max_f32 v147, v147, v145\n" \
  "v_sub_f32 v160, v144, v146\n" \
  "v_sub_f32 v162, v145, v147\n" \
  "v_fma_f32 v150, v150, s89, -v146\n" \
  "v_fma_f32 v151, v151, s89, -v146\n" \
  "v_fma_f32 v154, v154, s89, -v147\n" \
  "v_fma_f32 v155, v155, s89, -v147\n" \
  "v_exp_f32 v150, v150\n" \
  "v_exp_f32 v151, v151\n" \
  "v_exp_f32 v154, v154\n" \
  "v_exp_f32 v155, v155\n" \
  "v_exp_f32 v160, v160\n" \
  "v_exp_f32 v162, v162\n" \
  "v_add_f32 v146, v150, v151\n" \
  "v_add_f32 v147, v154, v155\n" \
  "v_mov_b32 v5, v146\n" \
  "v_mov_b32 v6, v147\n" \
  "s_nop 0\n" \
  "v_permlane16_swap_b32 v146, v5\n" \
  "v_permlane16_swap_b32 v147, v6\n" \
  "v_add_f32 v146, v146, v5\n" \
  "v_add_f32 v147, v147, v6\n" \
  "v_mov_b32 v5, v146\n" \
  "v_mov_b32 v6, v147\n" \
  "s_nop 0\n" \
  "v_permlane32_swap_b32 v146, v5\n" \
  "v_permlane32_swap_b32 v147, v6\n" \
  "v_add_f32 v146, v146, v5\n" \
  "v_add_f32 v147, v147, v6\n" \
  "v_fma_f32 v146, v160, v136, v146\n" \
  "v_fma_f32 v147, v162, v140, v147\n" \
  "v_rcp_f32 v146, v146\n" \
  "v_rcp_f32 v147, v147\n" \
  "v_mul_f32 v160, v160, v146\n" \
  "v_mul_f32 v162, v162, v147\n" \
  "v_mul_f32 v150, v150, v146\n" \
  "v_mul_f32 v151, v151, v146\n" \
  "v_mul_f32 v154, v154, v147\n" \
  "v_mul_f32 v155, v155, v147\n" \
  "v_cvt_pk_bf16_f32 v157, v150, v151\n" \
  "v_cvt_pk_bf16_f32 v159, v154, v155\n" \
  "v_pk_mul_f32 v[52:53], v[52:53], v[160:161] op_sel_hi:[1,0]\n" \
  "v_pk_mul_f32 v[54:55], v[54:55], v[160:161] op_sel_hi:[1,0]\n" \
  "v_pk_mul_f32 v[56:57], v[56:57], v[160:161] op_sel_hi:[1,0]\n" \
  "v_pk_mul_f32 v[58:59], v[58:59], v[160:161] op_sel_hi:[1,0]\n" \
  "v_pk_mul_f32 v[60:61], v[60:61], v[160:161] op_sel_hi:[1,0]\n" \
  "v_pk_mul_f32 v[62:63], v[62:63], v[160:161] op_sel_hi:[1,0]\n" \
  "v_pk_mul_f32 v[64:65], v[64:65], v[160:161] op_sel_hi:[1,0]\n" \
  "v_pk_mul_f32 v[66:67], v[66:67], v[160:161] op_sel_hi:[1,0]\n" \
  "v_pk_mul_f32 v[68:69], v[68:69], v[160:161] op_sel_hi:[1,0]\n" \
  "v_pk_mul_f32 v[70:71], v[70:71], v[160:161] op_sel_hi:[1,0]\n" \
  "v_pk_mul_f32 v[72:73], v[72:73], v[160:161] op_sel_hi:[1,0]\n" \
  "v_pk_mul_f32 v[74:75], v[74:75], v[160:161] op_sel_hi:[1,0]\n" \
  "v_pk_mul_f32 v[76:77], v[76:77], v[160:161] op_sel_hi:[1,0]\n" \
  "v_pk_mul_f32 v[78:79], v[78:79], v[160:161] op_sel_hi:[1,0]\n" \
  "v_pk_mul_f32 v[80:81], v[80:81], v[160:161] op_sel_hi:[1,0]\n" \
  "v_pk_mul_f32 v[82:83], v[82:83], v[160:161] op_sel_hi:[1,0]\n" \
  "v_pk_mul_f32 v[84:85], v[84:85], v[162:163] op_sel_hi:[1,0]\n" \
  "v_pk_mul_f32 v[86:87], v[86:87], v[162:163] op_sel_hi:[1,0]\n" \
  "v_pk_mul_f32 v[88:89], v[88:89], v[162:163] op_sel_hi:[1,0]\n" \
  "v_pk_mul_f32 v[90:91], v[90:91], v[162:163] op_sel_hi:[1,0]\n" \
  "v_pk_mul_f32 v[92:93], v[92:93], v[162:163] op_sel_hi:[1,0]\n" \
  "v_pk_mul_f32 v[94:95], v[94:95], v[162:163] op_sel_hi:[1,0]\n" \
  "v_pk_mul_f32 v[96:97], v[96:97], v[162:163] op_sel_hi:[1,0]\n" \
  "v_pk_mul_f32 v[98:99], v[98:99], v[162:163] op_sel_hi:[1,0]\n" \
  "v_pk_mul_f32 v[100:101], v[100:101], v[162:163] op_sel_hi:[1,0]\n" \
  "v_pk_mul_f32 v[102:103], v[102:103], v[162:163] op_sel_hi:[1,0]\n" \
  "v_pk_mul_f32 v[104:105], v[104:105], v[162:163] op_sel_hi:[1,0]\n" \
  "v_pk_mul_f32 v[106:107], v[106:107], v[162:163] op_sel_hi:[1,0]\n" \
  "v_pk_mul_f32 v[108:109], v[108:109], v[162:163] op_sel_hi:[1,0]\n" \
  "v_pk_mul_f32 v[110:111], v[110:111], v[162:163] op_sel_hi:[1,0]\n" \
  "v_pk_mul_f32 v[112:113], v[112:113], v[162:163] op_sel_hi:[1,0]\n" \
  "v_pk_mul_f32 v[114:115], v[114:115], v[162:163] op_sel_hi:[1,0]\n" \
  "s_waitcnt lgkmcnt(0)\n" \
  "v_mfma_f32_16x16x16_bf16 v[52:55], a[128:129], v[156:157], v[52:55]\n" \
  "v_mfma_f32_16x16x16_bf16 v[56:59], a[130:131], v[156:157], v[56:59]\n" \
  "v_mfma_f32_16x16x16_bf16 v[60:63], a[132:133], v[156:157], v[60:63]\n" \
  "v_mfma_f32_16x16x16_bf16 v[64:67], a[134:135], v[156:157], v[64:67]\n" \
  "v_mfma_f32_16x16x16_bf16 v[68:71], a[136:137], v[156:157], v[68:71]\n" \
  "v_cvt_pk_bf16_f32 v164, v52, v53\n" \
  "v_cvt_pk_bf16_f32 v165, v54, v55\n" \
  "v_mfma_f32_16x16x16_bf16 v[72:75], a[138:139], v[156:157], v[72:75]\n" \
  "v_cvt_pk_bf16_f32 v166, v56, v57\n" \
  "v_cvt_pk_bf16_f32 v167, v58, v59\n" \
  "buffer_store_dwordx4 v[164:167], v7, s[72:75], s86 offen offset:0 sc1\n" \
  "v_mfma_f32_16x16x16_bf16 v[76:79], a[140:141], v[156:157], v[76:79]\n" \
  "v_cvt_pk_bf16_f32 v168, v60, v61\n" \
  "v_cvt_pk_bf16_f32 v169, v62, v63\n" \
  "v_mfma_f32_16x16x16_bf16 v[80:83], a[142:143], v[156:157], v[80:83]\n" \
  "v_cvt_pk_bf16_f32 v170, v64, v65\n" \
  "v_cvt_pk_bf16_f32 v171, v66, v67\n" \
  "buffer_store_dwordx4 v[168:171], v7, s[72:75], s86 offen offset:1024 sc1\n" \
  "v_mfma_f32_16x16x16_bf16 v[84:87], a[128:129], v[158:159], v[84:87]\n" \
  "v_cvt_pk_bf16_f32 v172, v68, v69\n" \
  "v_cvt_pk_bf16_f32 v173, v70, v71\n" \
  "v_mfma_f32_16x16x16_bf16 v[88:91], a[130:131], v[158:159], v[88:91]\n" \
  "v_cvt_pk_bf16_f32 v174, v72, v73\n" \
  "v_cvt_pk_bf16_f32 v175, v74, v75\n" \
  "buffer_store_dwordx4 v[172:175], v7, s[72:75], s86 offen offset:2048 sc1\n" \
  "v_mfma_f32_16x16x16_bf16 v[92:95], a[132:133], v[158:159], v[92:95]\n" \
  "v_cvt_pk_bf16_f32 v176, v76, v77\n" \
  "v_cvt_pk_bf16_f32 v177, v78, v79\n" \
  "v_mfma_f32_16x16x16_bf16 v[96:99], a[134:135], v[158:159], v[96:99]\n" \
  "v_cvt_pk_bf16_f32 v178, v80, v81\n" \
  "v_cvt_pk_bf16_f32 v179, v82, v83\n" \
  "buffer_store_dwordx4 v[176:179], v7, s[72:75], s86 offen offset:3072 sc1\n" \
  "v_mfma_f32_16x16x16_bf16 v[100:103], a[136:137], v[158:159], v[100:103]\n" \
  "v_cvt_pk_bf16_f32 v180, v84, v85\n" \
  "v_cvt_pk_bf16_f32 v181, v86, v87\n" \
  "v_mfma_f32_16x16x16_bf16 v[104:107], a[138:139], v[158:159], v[104:107]\n" \
  "v_cvt_pk_bf16_f32 v182, v88, v89\n" \
  "v_cvt_pk_bf16_f32 v183, v90, v91\n" \
  "buffer_store_dwordx4 v[180:183], v17, s[72:75], s86 offen offset:0 sc1\n" \
  "v_mfma_f32_16x16x16_bf16 v[108:111], a[140:141], v[158:159], v[108:111]\n" \
  "v_cvt_pk_bf16_f32 v184, v92, v93\n" \
  "v_cvt_pk_bf16_f32 v185, v94, v95\n" \
  "v_mfma_f32_16x16x16_bf16 v[112:115], a[142:143], v[158:159], v[112:115]\n" \
  "v_cvt_pk_bf16_f32 v186, v96, v97\n" \
  "v_cvt_pk_bf16_f32 v187, v98, v99\n" \
  "buffer_store_dwordx4 v[184:187], v17, s[72:75], s86 offen offset:1024 sc1\n" \
  "v_cvt_pk_bf16_f32 v188, v100, v101\n" \
  "v_cvt_pk_bf16_f32 v189, v102, v103\n" \
  "v_cvt_pk_bf16_f32 v190, v104, v105\n" \
  "v_cvt_pk_bf16_f32 v191, v106, v107\n" \
  "buffer_store_dwordx4 v[188:191], v17, s[72:75], s86 offen offset:2048 sc1\n" \
  "s_nop 1\n" \
  "v_cvt_pk_bf16_f32 v192, v108, v109\n" \
  "v_cvt_pk_bf16_f32 v193, v110, v111\n" \
  "v_cvt_pk_bf16_f32 v194, v112, v113\n" \
  "v_cvt_pk_bf16_f32 v195, v114, v115\n" \
  "buffer_store_dwordx4 v[192:195], v17, s[72:75], s86 offen offset:3072 sc1\n" \
  "s_add_u32 s82, s82, 1\n" \
  "s_cmp_eq_u32 s82, 64\n" \
  "s_cbranch_scc1 LZ_%=\n" \
  "s_add_u32 s80, s82, 0\n" \
  "s_lshl_b32 s80, s80, 5\n" \
  "s_add_u32 s80, s80, %3\n" \
  "s_lshl_b32 s87, s80, 14\n" \
  "s_add_u32 s87, s87, " STR(EXP_B) "<<21\n" \
  "s_lshr_b32 s80, s87, 21\n" \
  "s_lshr_b32 s80, %4, s80\n" \
  "s_and_b32 s80, s80, 1\n" \
  "s_lshl_b32 s80, s80, 13\n" \
  "s_or_b32 s87, s87, s80\n" \
  "s_waitcnt vmcnt(8)\n" \
  "v_readlane_b32 s91, v14, 0\n" \
  "v_readlane_b32 s92, v14, 1\n" \
  "v_readlane_b32 s80, v14, 2\n" \
  "s_min_u32 s92, s92, s80\n" \
  "s_cmp_gt_u32 s91, s82\n" \
  "s_cselect_b32 s90, 1, 0\n" \
  "s_cselect_b32 s80, 0, 0x80000000\n" \
  "v_or_b32 v9, s80, v7\n" \
  "v_or_b32 v10, s80, v8\n" \
  "buffer_load_dwordx4 v[28:31], v9, s[68:71], s87 offen offset:0 sc1\n" \
  "buffer_load_dwordx4 v[32:35], v9, s[68:71], s87 offen offset:1024 sc1\n" \
  "buffer_load_dwordx4 v[36:39], v9, s[68:71], s87 offen offset:2048 sc1\n" \
  "buffer_load_dwordx4 v[40:43], v9, s[68:71], s87 offen offset:3072 sc1\n" \
  "buffer_load_dwordx4 v[44:47], v10, s[68:71], s87 offen offset:0 sc1\n" \
  "buffer_load_dwordx4 v[48:51], v10, s[68:71], s87 offen offset:1024 sc1\n" \
  "s_branch LA_%=\n" \
  "LG0_%=:\n" \
  "buffer_load_dword v14, v13, s[76:79], 0 offen sc1\n" \
  "s_waitcnt vmcnt(0)\n" \
  "v_readlane_b32 s91, v14, 0\n" \
  "v_readlane_b32 s92, v14, 1\n" \
  "v_readlane_b32 s80, v14, 2\n" \
  "s_min_u32 s92, s92, s80\n" \
  "s_cmp_ge_u32 s92, s81\n" \
  "s_cbranch_scc1 LH0_%=\n" \
  "s_sleep 4\n" \
  "s_add_u32 s88, s88, 1\n" \
  "s_cmp_lt_u32 s88, " STR(SPIN_CAP) "\n" \
  "s_cbranch_scc1 LG0_%=\n" \
  "v_mov_b32 v5, " STR(CW_DEAD) "*4\n" \
  "v_mov_b32 v6, 1\n" \
  "buffer_store_dword v6, v5, s[76:79], 0 offen sc1\n" \
  "s_branch LH0_%=\n" \
  "LW0_%=:\n" \
  "s_add_u32 s81, s82, 1\n" \
  "s_min_u32 s81, s81, 63\n" \
  "LQ0_%=:\n" \
  "buffer_load_dword v14, v13, s[76:79], 0 offen sc1\n" \
  "s_waitcnt vmcnt(0)\n" \
  "v_readlane_b32 s91, v14, 0\n" \
  "v_readlane_b32 s92, v14, 1\n" \
  "v_readlane_b32 s80, v14, 2\n" \
  "s_min_u32 s92, s92, s80\n" \
  "s_cmp_ge_u32 s91, s81\n" \
  "s_cbranch_scc1 LR0_%=\n" \
  "s_sleep 2\n" \
  "s_add_u32 s88, s88, 1\n" \
  "s_cmp_lt_u32 s88, " STR(SPIN_CAP) "\n" \
  "s_cbranch_scc1 LQ0_%=\n" \
  "v_mov_b32 v5, " STR(CW_DEAD) "*4\n" \
  "v_mov_b32 v6, 1\n" \
  "buffer_store_dword v6, v5, s[76:79], 0 offen sc1\n" \
  "s_branch LR0_%=\n" \
  "LR0_%=:\n" \
  "buffer_load_dwordx4 v[28:31], v7, s[68:71], s87 offen offset:0 sc1\n" \
  "buffer_load_dwordx4 v[32:35], v7, s[68:71], s87 offen offset:1024 sc1\n" \
  "buffer_load_dwordx4 v[36:39], v7, s[68:71], s87 offen offset:2048 sc1\n" \
  "buffer_load_dwordx4 v[40:43], v7, s[68:71], s87 offen offset:3072 sc1\n" \
  "buffer_load_dwordx4 v[44:47], v8, s[68:71], s87 offen offset:0 sc1\n" \
  "buffer_load_dwordx4 v[48:51], v8, s[68:71], s87 offen offset:1024 sc1\n" \
  "s_waitcnt vmcnt(0)\n" \
  "v_max3_u32 v5, v28, v29, v30\n" \
  "v_max3_u32 v5, v5, v31, v32\n" \
  "v_max3_u32 v5, v5, v33, v34\n" \
  "v_max3_u32 v5, v5, v35, v36\n" \
  "v_max3_u32 v5, v5, v37, v38\n" \
  "v_max3_u32 v5, v5, v39, v40\n" \
  "v_max3_u32 v5, v5, v41, v42\n" \
  "v_max3_u32 v5, v5, v43, v44\n" \
  "v_max3_u32 v5, v5, v45, v46\n" \
  "v_max3_u32 v5, v5, v47, v48\n" \
  "v_max3_u32 v5, v5, v49, v50\n" \
  "v_max_u32 v5, v5, v51\n" \
  "v_cmp_eq_u32 vcc, -1, v5\n" \
  "s_cbranch_vccz LU0_%=\n" \
  "s_sleep 1\n" \
  "s_add_u32 s88, s88, 1\n" \
  "s_cmp_lt_u32 s88, " STR(SPIN_CAP) "\n" \
  "s_cbranch_scc1 LR0_%=\n" \
  "v_mov_b32 v5, " STR(CW_DEAD) "*4\n" \
  "v_mov_b32 v6, 1\n" \
  "buffer_store_dword v6, v5, s[76:79], 0 offen sc1\n" \
  "s_branch LV0_%=\n" \
  "LV0_%=:\n" \
  "v_mov_b64 v[28:29], 0\n" \
  "v_mov_b64 v[30:31], 0\n" \
  "v_mov_b64 v[32:33], 0\n" \
  "v_mov_b64 v[34:35], 0\n" \
  "v_mov_b64 v[36:37], 0\n" \
  "v_mov_b64 v[38:39], 0\n" \
  "v_mov_b64 v[40:41], 0\n" \
  "v_mov_b64 v[42:43], 0\n" \
  "v_mov_b64 v[44:45], 0\n" \
  "v_mov_b64 v[46:47], 0\n" \
  "v_mov_b64 v[48:49], 0\n" \
  "v_mov_b64 v[50:51], 0\n" \
  "LU0_%=:\n" \
  "s_nop 1\n" \
  "s_branch LM0_%=\n" \
  "LG1_%=:\n" \
  "buffer_load_dword v14, v13, s[76:79], 0 offen sc1\n" \
  "s_waitcnt vmcnt(0)\n" \
  "v_readlane_b32 s91, v14, 0\n" \
  "v_readlane_b32 s92, v14, 1\n" \
  "v_readlane_b32 s80, v14, 2\n" \
  "s_min_u32 s92, s92, s80\n" \
  "s_cmp_ge_u32 s92, s81\n" \
  "s_cbranch_scc1 LH1_%=\n" \
  "s_sleep 4\n" \
  "s_add_u32 s88, s88, 1\n" \
  "s_cmp_lt_u32 s88, " STR(SPIN_CAP) "\n" \
  "s_cbranch_scc1 LG1_%=\n" \
  "v_mov_b32 v5, " STR(CW_DEAD) "*4\n" \
  "v_mov_b32 v6, 1\n" \
  "buffer_store_dword v6, v5, s[76:79], 0 offen sc1\n" \
  "s_branch LH1_%=\n" \
  "LW1_%=:\n" \
  "s_add_u32 s81, s82, 1\n" \
  "s_min_u32 s81, s81, 63\n" \
  "LQ1_%=:\n" \
  "buffer_load_dword v14, v13, s[76:79], 0 offen sc1\n" \
  "s_waitcnt vmcnt(0)\n" \
  "v_readlane_b32 s91, v14, 0\n" \
  "v_readlane_b32 s92, v14, 1\n" \
  "v_readlane_b32 s80, v14, 2\n" \
  "s_min_u32 s92, s92, s80\n" \
  "s_cmp_ge_u32 s91, s81\n" \
  "s_cbranch_scc1 LR1_%=\n" \
  "s_sleep 2\n" \
  "s_add_u32 s88, s88, 1\n" \
  "s_cmp_lt_u32 s88, " STR(SPIN_CAP) "\n" \
  "s_cbranch_scc1 LQ1_%=\n" \
  "v_mov_b32 v5, " STR(CW_DEAD) "*4\n" \
  "v_mov_b32 v6, 1\n" \
  "buffer_store_dword v6, v5, s[76:79], 0 offen sc1\n" \
  "s_branch LR1_%=\n" \
  "LR1_%=:\n" \
  "buffer_load_dwordx4 v[28:31], v7, s[68:71], s87 offen offset:0 sc1\n" \
  "buffer_load_dwordx4 v[32:35], v7, s[68:71], s87 offen offset:1024 sc1\n" \
  "buffer_load_dwordx4 v[36:39], v7, s[68:71], s87 offen offset:2048 sc1\n" \
  "buffer_load_dwordx4 v[40:43], v7, s[68:71], s87 offen offset:3072 sc1\n" \
  "buffer_load_dwordx4 v[44:47], v8, s[68:71], s87 offen offset:0 sc1\n" \
  "buffer_load_dwordx4 v[48:51], v8, s[68:71], s87 offen offset:1024 sc1\n" \
  "s_waitcnt vmcnt(0)\n" \
  "v_max3_u32 v5, v28, v29, v30\n" \
  "v_max3_u32 v5, v5, v31, v32\n" \
  "v_max3_u32 v5, v5, v33, v34\n" \
  "v_max3_u32 v5, v5, v35, v36\n" \
  "v_max3_u32 v5, v5, v37, v38\n" \
  "v_max3_u32 v5, v5, v39, v40\n" \
  "v_max3_u32 v5, v5, v41, v42\n" \
  "v_max3_u32 v5, v5, v43, v44\n" \
  "v_max3_u32 v5, v5, v45, v46\n" \
  "v_max3_u32 v5, v5, v47, v48\n" \
  "v_max3_u32 v5, v5, v49, v50\n" \
  "v_max_u32 v5, v5, v51\n" \
  "v_cmp_eq_u32 vcc, -1, v5\n" \
  "s_cbranch_vccz LU1_%=\n" \
  "s_sleep 1\n" \
  "s_add_u32 s88, s88, 1\n" \
  "s_cmp_lt_u32 s88, " STR(SPIN_CAP) "\n" \
  "s_cbranch_scc1 LR1_%=\n" \
  "v_mov_b32 v5, " STR(CW_DEAD) "*4\n" \
  "v_mov_b32 v6, 1\n" \
  "buffer_store_dword v6, v5, s[76:79], 0 offen sc1\n" \
  "s_branch LV1_%=\n" \
  "LV1_%=:\n" \
  "v_mov_b64 v[28:29], 0\n" \
  "v_mov_b64 v[30:31], 0\n" \
  "v_mov_b64 v[32:33], 0\n" \
  "v_mov_b64 v[34:35], 0\n" \
  "v_mov_b64 v[36:37], 0\n" \
  "v_mov_b64 v[38:39], 0\n" \
  "v_mov_b64 v[40:41], 0\n" \
  "v_mov_b64 v[42:43], 0\n" \
  "v_mov_b64 v[44:45], 0\n" \
  "v_mov_b64 v[46:47], 0\n" \
  "v_mov_b64 v[48:49], 0\n" \
  "v_mov_b64 v[50:51], 0\n" \
  "LU1_%=:\n" \
  "s_nop 1\n" \
  "s_branch LM1_%=\n" \
  "LZ_%=:\n" \
  "s_waitcnt vmcnt(0)\n" \
  "buffer_store_dword v16, v15, s[76:79], 0 offen sc1\n" \
  "s_waitcnt vmcnt(0)\n" \
  ""

#define CONV_ASM \
  "s_waitcnt lgkmcnt(0)\n" \
  "ds_read_b64 a[0:1], %2 offset:32\n" \
  "ds_read_b64 a[2:3], %2 offset:40\n" \
  "ds_read_b128 a[4:7], %3 offset:5040\n" \
  "ds_read_b128 a[8:11], %3 offset:2480\n" \
  "ds_read_b64 a[12:13], %2 offset:2080\n" \
  "ds_read_b64 a[14:15], %2 offset:2088\n" \
  "ds_read_b64 a[16:17], %2 offset:0\n" \
  "ds_read_b64 a[18:19], %2 offset:8\n" \
  "ds_read_b128 a[20:23], %3 offset:5072\n" \
  "ds_read_b128 a[24:27], %3 offset:2512\n" \
  "ds_read_b64 a[28:29], %2 offset:2048\n" \
  "ds_read_b64 a[30:31], %2 offset:2056\n" \
  "s_waitcnt lgkmcnt(6)\n" \
  "v_mfma_f32_32x32x16_bf16 %1, a[0:3], a[4:7], 0\n" \
  "ds_read_b64 a[32:33], %2 offset:96\n" \
  "ds_read_b64 a[34:35], %2 offset:104\n" \
  "v_mfma_f32_32x32x16_bf16 %0, a[0:3], a[8:11], 0\n" \
  "ds_read_b128 a[36:39], %3 offset:4960\n" \
  "ds_read_b128 a[40:43], %3 offset:2400\n" \
  "v_mfma_f32_32x32x16_bf16 %1, a[12:15], a[8:11], %1\n" \
  "ds_read_b64 a[44:45], %2 offset:2144\n" \
  "ds_read_b64 a[46:47], %2 offset:2152\n" \
  "s_waitcnt lgkmcnt(6)\n" \
  "v_mfma_f32_32x32x16_bf16 %1, a[16:19], a[20:23], %1\n" \
  "ds_read_b64 a[0:1], %2 offset:64\n" \
  "ds_read_b64 a[2:3], %2 offset:72\n" \
  "v_mfma_f32_32x32x16_bf16 %0, a[16:19], a[24:27], %0\n" \
  "ds_read_b128 a[4:7], %3 offset:4992\n" \
  "ds_read_b128 a[8:11], %3 offset:2432\n" \
  "v_mfma_f32_32x32x16_bf16 %1, a[28:31], a[24:27], %1\n" \
  "ds_read_b64 a[12:13], %2 offset:2112\n" \
  "ds_read_b64 a[14:15], %2 offset:2120\n" \
  "s_waitcnt lgkmcnt(6)\n" \
  "v_mfma_f32_32x32x16_bf16 %1, a[32:35], a[36:39], %1\n" \
  "ds_read_b64 a[16:17], %2 offset:160\n" \
  "ds_read_b64 a[18:19], %2 offset:168\n" \
  "v_mfma_f32_32x32x16_bf16 %0, a[32:35], a[40:43], %0\n" \
  "ds_read_b128 a[20:23], %3 offset:4880\n" \
  "ds_read_b128 a[24:27], %3 offset:2320\n" \
  "v_mfma_f32_32x32x16_bf16 %1, a[44:47], a[40:43], %1\n" \
  "ds_read_b64 a[28:29], %2 offset:2208\n" \
  "ds_read_b64 a[30:31], %2 offset:2216\n" \
  "s_waitcnt lgkmcnt(6)\n" \
  "v_mfma_f32_32x32x16_bf16 %1, a[0:3], a[4:7], %1\n" \
  "ds_read_b64 a[32:33], %2 offset:128\n" \
  "ds_read_b64 a[34:35], %2 offset:136\n" \
  "v_mfma_f32_32x32x16_bf16 %0, a[0:3], a[8:11], %0\n" \
  "ds_read_b128 a[36:39], %3 offset:4912\n" \
  "ds_read_b128 a[40:43], %3 offset:2352\n" \
  "v_mfma_f32_32x32x16_bf16 %1, a[12:15], a[8:11], %1\n" \
  "ds_read_b64 a[44:45], %2 offset:2176\n" \
  "ds_read_b64 a[46:47], %2 offset:2184\n" \
  "s_waitcnt lgkmcnt(6)\n" \
  "v_mfma_f32_32x32x16_bf16 %1, a[16:19], a[20:23], %1\n" \
  "ds_read_b64 a[0:1], %2 offset:224\n" \
  "ds_read_b64 a[2:3], %2 offset:232\n" \
  "v_mfma_f32_32x32x16_bf16 %0, a[16:19], a[24:27], %0\n" \
  "ds_read_b128 a[4:7], %3 offset:4800\n" \
  "ds_read_b128 a[8:11], %3 offset:2240\n" \
  "v_mfma_f32_32x32x16_bf16 %1, a[28:31], a[24:27], %1\n" \
  "ds_read_b64 a[12:13], %2 offset:2272\n" \
  "ds_read_b64 a[14:15], %2 offset:2280\n" \
  "s_waitcnt lgkmcnt(6)\n" \
  "v_mfma_f32_32x32x16_bf16 %1, a[32:35], a[36:39], %1\n" \
  "ds_read_b64 a[16:17], %2 offset:192\n" \
  "ds_read_b64 a[18:19], %2 offset:200\n" \
  "v_mfma_f32_32x32x16_bf16 %0, a[32:35], a[40:43], %0\n" \
  "ds_read_b128 a[20:23], %3 offset:4832\n" \
  "ds_read_b128 a[24:27], %3 offset:2272\n" \
  "v_mfma_f32_32x32x16_bf16 %1, a[44:47], a[40:43], %1\n" \
  "ds_read_b64 a[28:29], %2 offset:2240\n" \
  "ds_read_b64 a[30:31], %2 offset:2248\n" \
  "s_waitcnt lgkmcnt(6)\n" \
  "v_mfma_f32_32x32x16_bf16 %1, a[0:3], a[4:7], %1\n" \
  "ds_read_b64 a[32:33], %2 offset:288\n" \
  "ds_read_b64 a[34:35], %2 offset:296\n" \
  "v_mfma_f32_32x32x16_bf16 %0, a[0:3], a[8:11], %0\n" \
  "ds_read_b128 a[36:39], %3 offset:4720\n" \
  "ds_read_b128 a[40:43], %3 offset:2160\n" \
  "v_mfma_f32_32x32x16_bf16 %1, a[12:15], a[8:11], %1\n" \
  "ds_read_b64 a[44:45], %2 offset:2336\n" \
  "ds_read_b64 a[46:47], %2 offset:2344\n" \
  "s_waitcnt lgkmcnt(6)\n" \
  "v_mfma_f32_32x32x16_bf16 %1, a[16:19], a[20:23], %1\n" \
  "ds_read_b64 a[0:1], %2 offset:256\n" \
  "ds_read_b64 a[2:3], %2 offset:264\n" \
  "v_mfma_f32_32x32x16_bf16 %0, a[16:19], a[24:27], %0\n" \
  "ds_read_b128 a[4:7], %3 offset:4752\n" \
  "ds_read_b128 a[8:11], %3 offset:2192\n" \
  "v_mfma_f32_32x32x16_bf16 %1, a[28:31], a[24:27], %1\n" \
  "ds_read_b64 a[12:13], %2 offset:2304\n" \
  "ds_read_b64 a[14:15], %2 offset:2312\n" \
  "s_waitcnt lgkmcnt(6)\n" \
  "v_mfma_f32_32x32x16_bf16 %1, a[32:35], a[36:39], %1\n" \
  "ds_read_b64 a[16:17], %2 offset:352\n" \
  "ds_read_b64 a[18:19], %2 offset:360\n" \
  "v_mfma_f32_32x32x16_bf16 %0, a[32:35], a[40:43], %0\n" \
  "ds_read_b128 a[20:23], %3 offset:4640\n" \
  "ds_read_b128 a[24:27], %3 offset:2080\n" \
  "v_mfma_f32_32x32x16_bf16 %1, a[44:47], a[40:43], %1\n" \
  "ds_read_b64 a[28:29], %2 offset:2400\n" \
  "ds_read_b64 a[30:31], %2 offset:2408\n" \
  "s_waitcnt lgkmcnt(6)\n" \
  "v_mfma_f32_32x32x16_bf16 %1, a[0:3], a[4:7], %1\n" \
  "ds_read_b64 a[32:33], %2 offset:320\n" \
  "ds_read_b64 a[34:35], %2 offset:328\n" \
  "v_mfma_f32_32x32x16_bf16 %0, a[0:3], a[8:11], %0\n" \
  "ds_read_b128 a[36:39], %3 offset:4672\n" \
  "ds_read_b128 a[40:43], %3 offset:2112\n" \
  "v_mfma_f32_32x32x16_bf16 %1, a[12:15], a[8:11], %1\n" \
  "ds_read_b64 a[44:45], %2 offset:2368\n" \
  "ds_read_b64 a[46:47], %2 offset:2376\n" \
  "s_waitcnt lgkmcnt(6)\n" \
  "v_mfma_f32_32x32x16_bf16 %1, a[16:19], a[20:23], %1\n" \
  "ds_read_b64 a[0:1], %2 offset:416\n" \
  "ds_read_b64 a[2:3], %2 offset:424\n" \
  "v_mfma_f32_32x32x16_bf16 %0, a[16:19], a[24:27], %0\n" \
  "ds_read_b128 a[4:7], %3 offset:4560\n" \
  "ds_read_b128 a[8:11], %3 offset:2000\n" \
  "v_mfma_f32_32x32x16_bf16 %1, a[28:31], a[24:27], %1\n" \
  "ds_read_b64 a[12:13], %2 offset:2464\n" \
  "ds_read_b64 a[14:15], %2 offset:2472\n" \
  "s_waitcnt lgkmcnt(6)\n" \
  "v_mfma_f32_32x32x16_bf16 %1, a[32:35], a[36:39], %1\n" \
  "ds_read_b64 a[16:17], %2 offset:384\n" \
  "ds_read_b64 a[18:19], %2 offset:392\n" \
  "v_mfma_f32_32x32x16_bf16 %0, a[32:35], a[40:43], %0\n" \
  "ds_read_b128 a[20:23], %3 offset:4592\n" \
  "ds_read_b128 a[24:27], %3 offset:2032\n" \
  "v_mfma_f32_32x32x16_bf16 %1, a[44:47], a[40:43], %1\n" \
  "ds_read_b64 a[28:29], %2 offset:2432\n" \
  "ds_read_b64 a[30:31], %2 offset:2440\n" \
  "s_waitcnt lgkmcnt(6)\n" \
  "v_mfma_f32_32x32x16_bf16 %1, a[0:3], a[4:7], %1\n" \
  "ds_read_b64 a[32:33], %2 offset:480\n" \
  "ds_read_b64 a[34:35], %2 offset:488\n" \
  "v_mfma_f32_32x32x16_bf16 %0, a[0:3], a[8:11], %0\n" \
  "ds_read_b128 a[36:39], %3 offset:4480\n" \
  "ds_read_b128 a[40:43], %3 offset:1920\n" \
  "v_mfma_f32_32x32x16_bf16 %1, a[12:15], a[8:11], %1\n" \
  "ds_read_b64 a[44:45], %2 offset:2528\n" \
  "ds_read_b64 a[46:47], %2 offset:2536\n" \
  "s_waitcnt lgkmcnt(6)\n" \
  "v_mfma_f32_32x32x16_bf16 %1, a[16:19], a[20:23], %1\n" \
  "ds_read_b64 a[0:1], %2 offset:448\n" \
  "ds_read_b64 a[2:3], %2 offset:456\n" \
  "v_mfma_f32_32x32x16_bf16 %0, a[16:19], a[24:27], %0\n" \
  "ds_read_b128 a[4:7], %3 offset:4512\n" \
  "ds_read_b128 a[8:11], %3 offset:1952\n" \
  "v_mfma_f32_32x32x16_bf16 %1, a[28:31], a[24:27], %1\n" \
  "ds_read_b64 a[12:13], %2 offset:2496\n" \
  "ds_read_b64 a[14:15], %2 offset:2504\n" \
  "s_waitcnt lgkmcnt(6)\n" \
  "v_mfma_f32_32x32x16_bf16 %1, a[32:35], a[36:39], %1\n" \
  "ds_read_b64 a[16:17], %2 offset:544\n" \
  "ds_read_b64 a[18:19], %2 offset:552\n" \
  "v_mfma_f32_32x32x16_bf16 %0, a[32:35], a[40:43], %0\n" \
  "ds_read_b128 a[20:23], %3 offset:4400\n" \
  "ds_read_b128 a[24:27], %3 offset:1840\n" \
  "v_mfma_f32_32x32x16_bf16 %1, a[44:47], a[40:43], %1\n" \
  "ds_read_b64 a[28:29], %2 offset:2592\n" \
  "ds_read_b64 a[30:31], %2 offset:2600\n" \
  "s_waitcnt lgkmcnt(6)\n" \
  "v_mfma_f32_32x32x16_bf16 %1, a[0:3], a[4:7], %1\n" \
  "ds_read_b64 a[32:33], %2 offset:512\n" \
  "ds_read_b64 a[34:35], %2 offset:520\n" \
  "v_mfma_f32_32x32x16_bf16 %0, a[0:3], a[8:11], %0\n" \
  "ds_read_b128 a[36:39], %3 offset:4432\n" \
  "ds_read_b128 a[40:43], %3 offset:1872\n" \
  "v_mfma_f32_32x32x16_bf16 %1, a[12:15], a[8:11], %1\n" \
  "ds_read_b64 a[44:45], %2 offset:2560\n" \
  "ds_read_b64 a[46:47], %2 offset:2568\n" \
  "s_waitcnt lgkmcnt(6)\n" \
  "v_mfma_f32_32x32x16_bf16 %1, a[16:19], a[20:23], %1\n" \
  "ds_read_b64 a[0:1], %2 offset:608\n" \
  "ds_read_b64 a[2:3], %2 offset:616\n" \
  "v_mfma_f32_32x32x16_bf16 %0, a[16:19], a[24:27], %0\n" \
  "ds_read_b128 a[4:7], %3 offset:4320\n" \
  "ds_read_b128 a[8:11], %3 offset:1760\n" \
  "v_mfma_f32_32x32x16_bf16 %1, a[28:31], a[24:27], %1\n" \
  "ds_read_b64 a[12:13], %2 offset:2656\n" \
  "ds_read_b64 a[14:15], %2 offset:2664\n" \
  "s_waitcnt lgkmcnt(6)\n" \
  "v_mfma_f32_32x32x16_bf16 %1, a[32:35], a[36:39], %1\n" \
  "ds_read_b64 a[16:17], %2 offset:576\n" \
  "ds_read_b64 a[18:19], %2 offset:584\n" \
  "v_mfma_f32_32x32x16_bf16 %0, a[32:35], a[40:43], %0\n" \
  "ds_read_b128 a[20:23], %3 offset:4352\n" \
  "ds_read_b128 a[24:27], %3 offset:1792\n" \
  "v_mfma_f32_32x32x16_bf16 %1, a[44:47], a[40:43], %1\n" \
  "ds_read_b64 a[28:29], %2 offset:2624\n" \
  "ds_read_b64 a[30:31], %2 offset:2632\n" \
  "s_waitcnt lgkmcnt(6)\n" \
  "v_mfma_f32_32x32x16_bf16 %1, a[0:3], a[4:7], %1\n" \
  "ds_read_b64 a[32:33], %2 offset:672\n" \
  "ds_read_b64 a[34:35], %2 offset:680\n" \
  "v_mfma_f32_32x32x16_bf16 %0, a[0:3], a[8:11], %0\n" \
  "ds_read_b128 a[36:39], %3 offset:4240\n" \
  "ds_read_b128 a[40:43], %3 offset:1680\n" \
  "v_mfma_f32_32x32x16_bf16 %1, a[12:15], a[8:11], %1\n" \
  "ds_read_b64 a[44:45], %2 offset:2720\n" \
  "ds_read_b64 a[46:47], %2 offset:2728\n" \
  "s_waitcnt lgkmcnt(6)\n" \
  "v_mfma_f32_32x32x16_bf16 %1, a[16:19], a[20:23], %1\n" \
  "ds_read_b64 a[0:1], %2 offset:640\n" \
  "ds_read_b64 a[2:3], %2 offset:648\n" \
  "v_mfma_f32_32x32x16_bf16 %0, a[16:19], a[24:27], %0\n" \
  "ds_read_b128 a[4:7], %3 offset:4272\n" \
  "ds_read_b128 a[8:11], %3 offset:1712\n" \
  "v_mfma_f32_32x32x16_bf16 %1, a[28:31], a[24:27], %1\n" \
  "ds_read_b64 a[12:13], %2 offset:2688\n" \
  "ds_read_b64 a[14:15], %2 offset:2696\n" \
  "s_waitcnt lgkmcnt(6)\n" \
  "v_mfma_f32_32x32x16_bf16 %1, a[32:35], a[36:39], %1\n" \
  "ds_read_b64 a[16:17], %2 offset:736\n" \
  "ds_read_b64 a[18:19], %2 offset:744\n" \
  "v_mfma_f32_32x32x16_bf16 %0, a[32:35], a[40:43], %0\n" \
  "ds_read_b128 a[20:23], %3 offset:4160\n" \
  "ds_read_b128 a[24:27], %3 offset:1600\n" \
  "v_mfma_f32_32x32x16_bf16 %1, a[44:47], a[40:43], %1\n" \
  "ds_read_b64 a[28:29], %2 offset:2784\n" \
  "ds_read_b64 a[30:31], %2 offset:2792\n" \
  "s_waitcnt lgkmcnt(6)\n" \
  "v_mfma_f32_32x32x16_bf16 %1, a[0:3], a[4:7], %1\n" \
  "ds_read_b64 a[32:33], %2 offset:704\n" \
  "ds_read_b64 a[34:35], %2 offset:712\n" \
  "v_mfma_f32_32x32x16_bf16 %0, a[0:3], a[8:11], %0\n" \
  "ds_read_b128 a[36:39], %3 offset:4192\n" \
  "ds_read_b128 a[40:43], %3 offset:1632\n" \
  "v_mfma_f32_32x32x16_bf16 %1, a[12:15], a[8:11], %1\n" \
  "ds_read_b64 a[44:45], %2 offset:2752\n" \
  "ds_read_b64 a[46:47], %2 offset:2760\n" \
  "s_waitcnt lgkmcnt(6)\n" \
  "v_mfma_f32_32x32x16_bf16 %1, a[16:19], a[20:23], %1\n" \
  "ds_read_b64 a[0:1], %2 offset:800\n" \
  "ds_read_b64 a[2:3], %2 offset:808\n" \
  "v_mfma_f32_32x32x16_bf16 %0, a[16:19], a[24:27], %0\n" \
  "ds_read_b128 a[4:7], %3 offset:4080\n" \
  "ds_read_b128 a[8:11], %3 offset:1520\n" \
  "v_mfma_f32_32x32x16_bf16 %1, a[28:31], a[24:27], %1\n" \
  "ds_read_b64 a[12:13], %2 offset:2848\n" \
  "ds_read_b64 a[14:15], %2 offset:2856\n" \
  "s_waitcnt lgkmcnt(6)\n" \
  "v_mfma_f32_32x32x16_bf16 %1, a[32:35], a[36:39], %1\n" \
  "ds_read_b64 a[16:17], %2 offset:768\n" \
  "ds_read_b64 a[18:19], %2 offset:776\n" \
  "v_mfma_f32_32x32x16_bf16 %0, a[32:35], a[40:43], %0\n" \
  "ds_read_b128 a[20:23], %3 offset:4112\n" \
  "ds_read_b128 a[24:27], %3 offset:1552\n" \
  "v_mfma_f32_32x32x16_bf16 %1, a[44:47], a[40:43], %1\n" \
  "ds_read_b64 a[28:29], %2 offset:2816\n" \
  "ds_read_b64 a[30:31], %2 offset:2824\n" \
  "s_waitcnt lgkmcnt(6)\n" \
  "v_mfma_f32_32x32x16_bf16 %1, a[0:3], a[4:7], %1\n" \
  "ds_read_b64 a[32:33], %2 offset:864\n" \
  "ds_read_b64 a[34:35], %2 offset:872\n" \
  "v_mfma_f32_32x32x16_bf16 %0, a[0:3], a[8:11], %0\n" \
  "ds_read_b128 a[36:39], %3 offset:4000\n" \
  "ds_read_b128 a[40:43], %3 offset:1440\n" \
  "v_mfma_f32_32x32x16_bf16 %1, a[12:15], a[8:11], %1\n" \
  "ds_read_b64 a[44:45], %2 offset:2912\n" \
  "ds_read_b64 a[46:47], %2 offset:2920\n" \
  "s_waitcnt lgkmcnt(6)\n" \
  "v_mfma_f32_32x32x16_bf16 %1, a[16:19], a[20:23], %1\n" \
  "ds_read_b64 a[0:1], %2 offset:832\n" \
  "ds_read_b64 a[2:3], %2 offset:840\n" \
  "v_mfma_f32_32x32x16_bf16 %0, a[16:19], a[24:27], %0\n" \
  "ds_read_b128 a[4:7], %3 offset:4032\n" \
  "ds_read_b128 a[8:11], %3 offset:1472\n" \
  "v_mfma_f32_32x32x16_bf16 %1, a[28:31], a[24:27], %1\n" \
  "ds_read_b64 a[12:13], %2 offset:2880\n" \
  "ds_read_b64 a[14:15], %2 offset:2888\n" \
  "s_waitcnt lgkmcnt(6)\n" \
  "v_mfma_f32_32x32x16_bf16 %1, a[32:35], a[36:39], %1\n" \
  "ds_read_b64 a[16:17], %2 offset:928\n" \
  "ds_read_b64 a[18:19], %2 offset:936\n" \
  "v_mfma_f32_32x32x16_bf16 %0, a[32:35], a[40:43], %0\n" \
  "ds_read_b128 a[20:23], %3 offset:3920\n" \
  "ds_read_b128 a[24:27], %3 offset:1360\n" \
  "v_mfma_f32_32x32x16_bf16 %1, a[44:47], a[40:43], %1\n" \
  "ds_read_b64 a[28:29], %2 offset:2976\n" \
  "ds_read_b64 a[30:31], %2 offset:2984\n" \
  "s_waitcnt lgkmcnt(6)\n" \
  "v_mfma_f32_32x32x16_bf16 %1, a[0:3], a[4:7], %1\n" \
  "ds_read_b64 a[32:33], %2 offset:896\n" \
  "ds_read_b64 a[34:35], %2 offset:904\n" \
  "v_mfma_f32_32x32x16_bf16 %0, a[0:3], a[8:11], %0\n" \
  "ds_read_b128 a[36:39], %3 offset:3952\n" \
  "ds_read_b128 a[40:43], %3 offset:1392\n" \
  "v_mfma_f32_32x32x16_bf16 %1, a[12:15], a[8:11], %1\n" \
  "ds_read_b64 a[44:45], %2 offset:2944\n" \
  "ds_read_b64 a[46:47], %2 offset:2952\n" \
  "s_waitcnt lgkmcnt(6)\n" \
  "v_mfma_f32_32x32x16_bf16 %1, a[16:19], a[20:23], %1\n" \
  "ds_read_b64 a[0:1], %2 offset:992\n" \
  "ds_read_b64 a[2:3], %2 offset:1000\n" \
  "v_mfma_f32_32x32x16_bf16 %0, a[16:19], a[24:27], %0\n" \
  "ds_read_b128 a[4:7], %3 offset:3840\n" \
  "ds_read_b128 a[8:11], %3 offset:1280\n" \
  "v_mfma_f32_32x32x16_bf16 %1, a[28:31], a[24:27], %1\n" \
  "ds_read_b64 a[12:13], %2 offset:3040\n" \
  "ds_read_b64 a[14:15], %2 offset:3048\n" \
  "s_waitcnt lgkmcnt(6)\n" \
  "v_mfma_f32_32x32x16_bf16 %1, a[32:35], a[36:39], %1\n" \
  "ds_read_b64 a[16:17], %2 offset:960\n" \
  "ds_read_b64 a[18:19], %2 offset:968\n" \
  "v_mfma_f32_32x32x16_bf16 %0, a[32:35], a[40:43], %0\n" \
  "ds_read_b128 a[20:23], %3 offset:3872\n" \
  "ds_read_b128 a[24:27], %3 offset:1312\n" \
  "v_mfma_f32_32x32x16_bf16 %1, a[44:47], a[40:43], %1\n" \
  "ds_read_b64 a[28:29], %2 offset:3008\n" \
  "ds_read_b64 a[30:31], %2 offset:3016\n" \
  "s_waitcnt lgkmcnt(6)\n" \
  "v_mfma_f32_32x32x16_bf16 %1, a[0:3], a[4:7], %1\n" \
  "ds_read_b64 a[32:33], %2 offset:1056\n" \
  "ds_read_b64 a[34:35], %2 offset:1064\n" \
  "v_mfma_f32_32x32x16_bf16 %0, a[0:3], a[8:11], %0\n" \
  "ds_read_b128 a[36:39], %3 offset:3760\n" \
  "ds_read_b128 a[40:43], %3 offset:1200\n" \
  "v_mfma_f32_32x32x16_bf16 %1, a[12:15], a[8:11], %1\n" \
  "ds_read_b64 a[44:45], %2 offset:3104\n" \
  "ds_read_b64 a[46:47], %2 offset:3112\n" \
  "s_waitcnt lgkmcnt(6)\n" \
  "v_mfma_f32_32x32x16_bf16 %1, a[16:19], a[20:23], %1\n" \
  "ds_read_b64 a[0:1], %2 offset:1024\n" \
  "ds_read_b64 a[2:3], %2 offset:1032\n" \
  "v_mfma_f32_32x32x16_bf16 %0, a[16:19], a[24:27], %0\n" \
  "ds_read_b128 a[4:7], %3 offset:3792\n" \
  "ds_read_b128 a[8:11], %3 offset:1232\n" \
  "v_mfma_f32_32x32x16_bf16 %1, a[28:31], a[24:27], %1\n" \
  "ds_read_b64 a[12:13], %2 offset:3072\n" \
  "ds_read_b64 a[14:15], %2 offset:3080\n" \
  "s_waitcnt lgkmcnt(6)\n" \
  "v_mfma_f32_32x32x16_bf16 %1, a[32:35], a[36:39], %1\n" \
  "ds_read_b64 a[16:17], %2 offset:1120\n" \
  "ds_read_b64 a[18:19], %2 offset:1128\n" \
  "v_mfma_f32_32x32x16_bf16 %0, a[32:35], a[40:43], %0\n" \
  "ds_read_b128 a[20:23], %3 offset:3680\n" \
  "ds_read_b128 a[24:27], %3 offset:1120\n" \
  "v_mfma_f32_32x32x16_bf16 %1, a[44:47], a[40:43], %1\n" \
  "ds_read_b64 a[28:29], %2 offset:3168\n" \
  "ds_read_b64 a[30:31], %2 offset:3176\n" \
  "s_waitcnt lgkmcnt(6)\n" \
  "v_mfma_f32_32x32x16_bf16 %1, a[0:3], a[4:7], %1\n" \
  "ds_read_b64 a[32:33], %2 offset:1088\n" \
  "ds_read_b64 a[34:35], %2 offset:1096\n" \
  "v_mfma_f32_32x32x16_bf16 %0, a[0:3], a[8:11], %0\n" \
  "ds_read_b128 a[36:39], %3 offset:3712\n" \
  "ds_read_b128 a[40:43], %3 offset:1152\n" \
  "v_mfma_f32_32x32x16_bf16 %1, a[12:15], a[8:11], %1\n" \
  "ds_read_b64 a[44:45], %2 offset:3136\n" \
  "ds_read_b64 a[46:47], %2 offset:3144\n" \
  "s_waitcnt lgkmcnt(6)\n" \
  "v_mfma_f32_32x32x16_bf16 %1, a[16:19], a[20:23], %1\n" \
  "ds_read_b64 a[0:1], %2 offset:1184\n" \
  "ds_read_b64 a[2:3], %2 offset:1192\n" \
  "v_mfma_f32_32x32x16_bf16 %0, a[16:19], a[24:27], %0\n" \
  "ds_read_b128 a[4:7], %3 offset:3600\n" \
  "ds_read_b128 a[8:11], %3 offset:1040\n" \
  "v_mfma_f32_32x32x16_bf16 %1, a[28:31], a[24:27], %1\n" \
  "ds_read_b64 a[12:13], %2 offset:3232\n" \
  "ds_read_b64 a[14:15], %2 offset:3240\n" \
  "s_waitcnt lgkmcnt(6)\n" \
  "v_mfma_f32_32x32x16_bf16 %1, a[32:35], a[36:39], %1\n" \
  "ds_read_b64 a[16:17], %2 offset:1152\n" \
  "ds_read_b64 a[18:19], %2 offset:1160\n" \
  "v_mfma_f32_32x32x16_bf16 %0, a[32:35], a[40:43], %0\n" \
  "ds_read_b128 a[20:23], %3 offset:3632\n" \
  "ds_read_b128 a[24:27], %3 offset:1072\n" \
  "v_mfma_f32_32x32x16_bf16 %1, a[44:47], a[40:43], %1\n" \
  "ds_read_b64 a[28:29], %2 offset:3200\n" \
  "ds_read_b64 a[30:31], %2 offset:3208\n" \
  "s_waitcnt lgkmcnt(6)\n" \
  "v_mfma_f32_32x32x16_bf16 %1, a[0:3], a[4:7], %1\n" \
  "ds_read_b64 a[32:33], %2 offset:1248\n" \
  "ds_read_b64 a[34:35], %2 offset:1256\n" \
  "v_mfma_f32_32x32x16_bf16 %0, a[0:3], a[8:11], %0\n" \
  "ds_read_b128 a[36:39], %3 offset:3520\n" \
  "ds_read_b128 a[40:43], %3 offset:960\n" \
  "v_mfma_f32_32x32x16_bf16 %1, a[12:15], a[8:11], %1\n" \
  "ds_read_b64 a[44:45], %2 offset:3296\n" \
  "ds_read_b64 a[46:47], %2 offset:3304\n" \
  "s_waitcnt lgkmcnt(6)\n" \
  "v_mfma_f32_32x32x16_bf16 %1, a[16:19], a[20:23], %1\n" \
  "ds_read_b64 a[0:1], %2 offset:1216\n" \
  "ds_read_b64 a[2:3], %2 offset:1224\n" \
  "v_mfma_f32_32x32x16_bf16 %0, a[16:19], a[24:27], %0\n" \
  "ds_read_b128 a[4:7], %3 offset:3552\n" \
  "ds_read_b128 a[8:11], %3 offset:992\n" \
  "v_mfma_f32_32x32x16_bf16 %1, a[28:31], a[24:27], %1\n" \
  "ds_read_b64 a[12:13], %2 offset:3264\n" \
  "ds_read_b64 a[14:15], %2 offset:3272\n" \
  "s_waitcnt lgkmcnt(6)\n" \
  "v_mfma_f32_32x32x16_bf16 %1, a[32:35], a[36:39], %1\n" \
  "ds_read_b64 a[16:17], %2 offset:1312\n" \
  "ds_read_b64 a[18:19], %2 offset:1320\n" \
  "v_mfma_f32_32x32x16_bf16 %0, a[32:35], a[40:43], %0\n" \
  "ds_read_b128 a[20:23], %3 offset:3440\n" \
  "ds_read_b128 a[24:27], %3 offset:880\n" \
  "v_mfma_f32_32x32x16_bf16 %1, a[44:47], a[40:43], %1\n" \
  "ds_read_b64 a[28:29], %2 offset:3360\n" \
  "ds_read_b64 a[30:31], %2 offset:3368\n" \
  "s_waitcnt lgkmcnt(6)\n" \
  "v_mfma_f32_32x32x16_bf16 %1, a[0:3], a[4:7], %1\n" \
  "ds_read_b64 a[32:33], %2 offset:1280\n" \
  "ds_read_b64 a[34:35], %2 offset:1288\n" \
  "v_mfma_f32_32x32x16_bf16 %0, a[0:3], a[8:11], %0\n" \
  "ds_read_b128 a[36:39], %3 offset:3472\n" \
  "ds_read_b128 a[40:43], %3 offset:912\n" \
  "v_mfma_f32_32x32x16_bf16 %1, a[12:15], a[8:11], %1\n" \
  "ds_read_b64 a[44:45], %2 offset:3328\n" \
  "ds_read_b64 a[46:47], %2 offset:3336\n" \
  "s_waitcnt lgkmcnt(6)\n" \
  "v_mfma_f32_32x32x16_bf16 %1, a[16:19], a[20:23], %1\n" \
  "ds_read_b64 a[0:1], %2 offset:1376\n" \
  "ds_read_b64 a[2:3], %2 offset:1384\n" \
  "v_mfma_f32_32x32x16_bf16 %0, a[16:19], a[24:27], %0\n" \
  "ds_read_b128 a[4:7], %3 offset:3360\n" \
  "ds_read_b128 a[8:11], %3 offset:800\n" \
  "v_mfma_f32_32x32x16_bf16 %1, a[28:31], a[24:27], %1\n" \
  "ds_read_b64 a[12:13], %2 offset:3424\n" \
  "ds_read_b64 a[14:15], %2 offset:3432\n" \
  "s_waitcnt lgkmcnt(6)\n" \
  "v_mfma_f32_32x32x16_bf16 %1, a[32:35], a[36:39], %1\n" \
  "ds_read_b64 a[16:17], %2 offset:1344\n" \
  "ds_read_b64 a[18:19], %2 offset:1352\n" \
  "v_mfma_f32_32x32x16_bf16 %0, a[32:35], a[40:43], %0\n" \
  "ds_read_b128 a[20:23], %3 offset:3392\n" \
  "ds_read_b128 a[24:27], %3 offset:832\n" \
  "v_mfma_f32_32x32x16_bf16 %1, a[44:47], a[40:43], %1\n" \
  "ds_read_b64 a[28:29], %2 offset:3392\n" \
  "ds_read_b64 a[30:31], %2 offset:3400\n" \
  "s_waitcnt lgkmcnt(6)\n" \
  "v_mfma_f32_32x32x16_bf16 %1, a[0:3], a[4:7], %1\n" \
  "ds_read_b64 a[32:33], %2 offset:1440\n" \
  "ds_read_b64 a[34:35], %2 offset:1448\n" \
  "v_mfma_f32_32x32x16_bf16 %0, a[0:3], a[8:11], %0\n" \
  "ds_read_b128 a[36:39], %3 offset:3280\n" \
  "ds_read_b128 a[40:43], %3 offset:720\n" \
  "v_mfma_f32_32x32x16_bf16 %1, a[12:15], a[8:11], %1\n" \
  "ds_read_b64 a[44:45], %2 offset:3488\n" \
  "ds_read_b64 a[46:47], %2 offset:3496\n" \
  "s_waitcnt lgkmcnt(6)\n" \
  "v_mfma_f32_32x32x16_bf16 %1, a[16:19], a[20:23], %1\n" \
  "ds_read_b64 a[0:1], %2 offset:1408\n" \
  "ds_read_b64 a[2:3], %2 offset:1416\n" \
  "v_mfma_f32_32x32x16_bf16 %0, a[16:19], a[24:27], %0\n" \
  "ds_read_b128 a[4:7], %3 offset:3312\n" \
  "ds_read_b128 a[8:11], %3 offset:752\n" \
  "v_mfma_f32_32x32x16_bf16 %1, a[28:31], a[24:27], %1\n" \
  "ds_read_b64 a[12:13], %2 offset:3456\n" \
  "ds_read_b64 a[14:15], %2 offset:3464\n" \
  "s_waitcnt lgkmcnt(6)\n" \
  "v_mfma_f32_32x32x16_bf16 %1, a[32:35], a[36:39], %1\n" \
  "ds_read_b64 a[16:17], %2 offset:1504\n" \
  "ds_read_b64 a[18:19], %2 offset:1512\n" \
  "v_mfma_f32_32x32x16_bf16 %0, a[32:35], a[40:43], %0\n" \
  "ds_read_b128 a[20:23], %3 offset:3200\n" \
  "ds_read_b128 a[24:27], %3 offset:640\n" \
  "v_mfma_f32_32x32x16_bf16 %1, a[44:47], a[40:43], %1\n" \
  "ds_read_b64 a[28:29], %2 offset:3552\n" \
  "ds_read_b64 a[30:31], %2 offset:3560\n" \
  "s_waitcnt lgkmcnt(6)\n" \
  "v_mfma_f32_32x32x16_bf16 %1, a[0:3], a[4:7], %1\n" \
  "ds_read_b64 a[32:33], %2 offset:1472\n" \
  "ds_read_b64 a[34:35], %2 offset:1480\n" \
  "v_mfma_f32_32x32x16_bf16 %0, a[0:3], a[8:11], %0\n" \
  "ds_read_b128 a[36:39], %3 offset:3232\n" \
  "ds_read_b128 a[40:43], %3 offset:672\n" \
  "v_mfma_f32_32x32x16_bf16 %1, a[12:15], a[8:11], %1\n" \
  "ds_read_b64 a[44:45], %2 offset:3520\n" \
  "ds_read_b64 a[46:47], %2 offset:3528\n" \
  "s_waitcnt lgkmcnt(6)\n" \
  "v_mfma_f32_32x32x16_bf16 %1, a[16:19], a[20:23], %1\n" \
  "ds_read_b64 a[0:1], %2 offset:1568\n" \
  "ds_read_b64 a[2:3], %2 offset:1576\n" \
  "v_mfma_f32_32x32x16_bf16 %0, a[16:19], a[24:27], %0\n" \
  "ds_read_b128 a[4:7], %3 offset:3120\n" \
  "ds_read_b128 a[8:11], %3 offset:560\n" \
  "v_mfma_f32_32x32x16_bf16 %1, a[28:31], a[24:27], %1\n" \
  "ds_read_b64 a[12:13], %2 offset:3616\n" \
  "ds_read_b64 a[14:15], %2 offset:3624\n" \
  "s_waitcnt lgkmcnt(6)\n" \
  "v_mfma_f32_32x32x16_bf16 %1, a[32:35], a[36:39], %1\n" \
  "ds_read_b64 a[16:17], %2 offset:1536\n" \
  "ds_read_b64 a[18:19], %2 offset:1544\n" \
  "v_mfma_f32_32x32x16_bf16 %0, a[32:35], a[40:43], %0\n" \
  "ds_read_b128 a[20:23], %3 offset:3152\n" \
  "ds_read_b128 a[24:27], %3 offset:592\n" \
  "v_mfma_f32_32x32x16_bf16 %1, a[44:47], a[40:43], %1\n" \
  "ds_read_b64 a[28:29], %2 offset:3584\n" \
  "ds_read_b64 a[30:31], %2 offset:3592\n" \
  "s_waitcnt lgkmcnt(6)\n" \
  "v_mfma_f32_32x32x16_bf16 %1, a[0:3], a[4:7], %1\n" \
  "ds_read_b64 a[32:33], %2 offset:1632\n" \
  "ds_read_b64 a[34:35], %2 offset:1640\n" \
  "v_mfma_f32_32x32x16_bf16 %0, a[0:3], a[8:11], %0\n" \
  "ds_read_b128 a[36:39], %3 offset:3040\n" \
  "ds_read_b128 a[40:43], %3 offset:480\n" \
  "v_mfma_f32_32x32x16_bf16 %1, a[12:15], a[8:11], %1\n" \
  "ds_read_b64 a[44:45], %2 offset:3680\n" \
  "ds_read_b64 a[46:47], %2 offset:3688\n" \
  "s_waitcnt lgkmcnt(6)\n" \
  "v_mfma_f32_32x32x16_bf16 %1, a[16:19], a[20:23], %1\n" \
  "ds_read_b64 a[0:1], %2 offset:1600\n" \
  "ds_read_b64 a[2:3], %2 offset:1608\n" \
  "v_mfma_f32_32x32x16_bf16 %0, a[16:19], a[24:27], %0\n" \
  "ds_read_b128 a[4:7], %3 offset:3072\n" \
  "ds_read_b128 a[8:11], %3 offset:512\n" \
  "v_mfma_f32_32x32x16_bf16 %1, a[28:31], a[24:27], %1\n" \
  "ds_read_b64 a[12:13], %2 offset:3648\n" \
  "ds_read_b64 a[14:15], %2 offset:3656\n" \
  "s_waitcnt lgkmcnt(6)\n" \
  "v_mfma_f32_32x32x16_bf16 %1, a[32:35], a[36:39], %1\n" \
  "ds_read_b64 a[16:17], %2 offset:1696\n" \
  "ds_read_b64 a[18:19], %2 offset:1704\n" \
  "v_mfma_f32_32x32x16_bf16 %0, a[32:35], a[40:43], %0\n" \
  "ds_read_b128 a[20:23], %3 offset:2960\n" \
  "ds_read_b128 a[24:27], %3 offset:400\n" \
  "v_mfma_f32_32x32x16_bf16 %1, a[44:47], a[40:43], %1\n" \
  "ds_read_b64 a[28:29], %2 offset:3744\n" \
  "ds_read_b64 a[30:31], %2 offset:3752\n" \
  "s_waitcnt lgkmcnt(6)\n" \
  "v_mfma_f32_32x32x16_bf16 %1, a[0:3], a[4:7], %1\n" \
  "ds_read_b64 a[32:33], %2 offset:1664\n" \
  "ds_read_b64 a[34:35], %2 offset:1672\n" \
  "v_mfma_f32_32x32x16_bf16 %0, a[0:3], a[8:11], %0\n" \
  "ds_read_b128 a[36:39], %3 offset:2992\n" \
  "ds_read_b128 a[40:43], %3 offset:432\n" \
  "v_mfma_f32_32x32x16_bf16 %1, a[12:15], a[8:11], %1\n" \
  "ds_read_b64 a[44:45], %2 offset:3712\n" \
  "ds_read_b64 a[46:47], %2 offset:3720\n" \
  "s_waitcnt lgkmcnt(6)\n" \
  "v_mfma_f32_32x32x16_bf16 %1, a[16:19], a[20:23], %1\n" \
  "ds_read_b64 a[0:1], %2 offset:1760\n" \
  "ds_read_b64 a[2:3], %2 offset:1768\n" \
  "v_mfma_f32_32x32x16_bf16 %0, a[16:19], a[24:27], %0\n" \
  "ds_read_b128 a[4:7], %3 offset:2880\n" \
  "ds_read_b128 a[8:11], %3 offset:320\n" \
  "v_mfma_f32_32x32x16_bf16 %1, a[28:31], a[24:27], %1\n" \
  "ds_read_b64 a[12:13], %2 offset:3808\n" \
  "ds_read_b64 a[14:15], %2 offset:3816\n" \
  "s_waitcnt lgkmcnt(6)\n" \
  "v_mfma_f32_32x32x16_bf16 %1, a[32:35], a[36:39], %1\n" \
  "ds_read_b64 a[16:17], %2 offset:1728\n" \
  "ds_read_b64 a[18:19], %2 offset:1736\n" \
  "v_mfma_f32_32x32x16_bf16 %0, a[32:35], a[40:43], %0\n" \
  "ds_read_b128 a[20:23], %3 offset:2912\n" \
  "ds_read_b128 a[24:27], %3 offset:352\n" \
  "v_mfma_f32_32x32x16_bf16 %1, a[44:47], a[40:43], %1\n" \
  "ds_read_b64 a[28:29], %2 offset:3776\n" \
  "ds_read_b64 a[30:31], %2 offset:3784\n" \
  "s_waitcnt lgkmcnt(6)\n" \
  "v_mfma_f32_32x32x16_bf16 %1, a[0:3], a[4:7], %1\n" \
  "ds_read_b64 a[32:33], %2 offset:1824\n" \
  "ds_read_b64 a[34:35], %2 offset:1832\n" \
  "v_mfma_f32_32x32x16_bf16 %0, a[0:3], a[8:11], %0\n" \
  "ds_read_b128 a[36:39], %3 offset:2800\n" \
  "ds_read_b128 a[40:43], %3 offset:240\n" \
  "v_mfma_f32_32x32x16_bf16 %1, a[12:15], a[8:11], %1\n" \
  "ds_read_b64 a[44:45], %2 offset:3872\n" \
  "ds_read_b64 a[46:47], %2 offset:3880\n" \
  "s_waitcnt lgkmcnt(6)\n" \
  "v_mfma_f32_32x32x16_bf16 %1, a[16:19], a[20:23], %1\n" \
  "ds_read_b64 a[0:1], %2 offset:1792\n" \
  "ds_read_b64 a[2:3], %2 offset:1800\n" \
  "v_mfma_f32_32x32x16_bf16 %0, a[16:19], a[24:27], %0\n" \
  "ds_read_b128 a[4:7], %3 offset:2832\n" \
  "ds_read_b128 a[8:11], %3 offset:272\n" \
  "v_mfma_f32_32x32x16_bf16 %1, a[28:31], a[24:27], %1\n" \
  "ds_read_b64 a[12:13], %2 offset:3840\n" \
  "ds_read_b64 a[14:15], %2 offset:3848\n" \
  "s_waitcnt lgkmcnt(6)\n" \
  "v_mfma_f32_32x32x16_bf16 %1, a[32:35], a[36:39], %1\n" \
  "ds_read_b64 a[16:17], %2 offset:1888\n" \
  "ds_read_b64 a[18:19], %2 offset:1896\n" \
  "v_mfma_f32_32x32x16_bf16 %0, a[32:35], a[40:43], %0\n" \
  "ds_read_b128 a[20:23], %3 offset:2720\n" \
  "ds_read_b128 a[24:27], %3 offset:160\n" \
  "v_mfma_f32_32x32x16_bf16 %1, a[44:47], a[40:43], %1\n" \
  "ds_read_b64 a[28:29], %2 offset:3936\n" \
  "ds_read_b64 a[30:31], %2 offset:3944\n" \
  "s_waitcnt lgkmcnt(6)\n" \
  "v_mfma_f32_32x32x16_bf16 %1, a[0:3], a[4:7], %1\n" \
  "ds_read_b64 a[32:33], %2 offset:1856\n" \
  "ds_read_b64 a[34:35], %2 offset:1864\n" \
  "v_mfma_f32_32x32x16_bf16 %0, a[0:3], a[8:11], %0\n" \
  "ds_read_b128 a[36:39], %3 offset:2752\n" \
  "ds_read_b128 a[40:43], %3 offset:192\n" \
  "v_mfma_f32_32x32x16_bf16 %1, a[12:15], a[8:11], %1\n" \
  "ds_read_b64 a[44:45], %2 offset:3904\n" \
  "ds_read_b64 a[46:47], %2 offset:3912\n" \
  "s_waitcnt lgkmcnt(6)\n" \
  "v_mfma_f32_32x32x16_bf16 %1, a[16:19], a[20:23], %1\n" \
  "ds_read_b64 a[0:1], %2 offset:1952\n" \
  "ds_read_b64 a[2:3], %2 offset:1960\n" \
  "v_mfma_f32_32x32x16_bf16 %0, a[16:19], a[24:27], %0\n" \
  "ds_read_b128 a[4:7], %3 offset:2640\n" \
  "ds_read_b128 a[8:11], %3 offset:80\n" \
  "v_mfma_f32_32x32x16_bf16 %1, a[28:31], a[24:27], %1\n" \
  "ds_read_b64 a[12:13], %2 offset:4000\n" \
  "ds_read_b64 a[14:15], %2 offset:4008\n" \
  "s_waitcnt lgkmcnt(6)\n" \
  "v_mfma_f32_32x32x16_bf16 %1, a[32:35], a[36:39], %1\n" \
  "ds_read_b64 a[16:17], %2 offset:1920\n" \
  "ds_read_b64 a[18:19], %2 offset:1928\n" \
  "v_mfma_f32_32x32x16_bf16 %0, a[32:35], a[40:43], %0\n" \
  "ds_read_b128 a[20:23], %3 offset:2672\n" \
  "ds_read_b128 a[24:27], %3 offset:112\n" \
  "v_mfma_f32_32x32x16_bf16 %1, a[44:47], a[40:43], %1\n" \
  "ds_read_b64 a[28:29], %2 offset:3968\n" \
  "ds_read_b64 a[30:31], %2 offset:3976\n" \
  "s_waitcnt lgkmcnt(6)\n" \
  "v_mfma_f32_32x32x16_bf16 %1, a[0:3], a[4:7], %1\n" \
  "ds_read_b64 a[32:33], %2 offset:2016\n" \
  "ds_read_b64 a[34:35], %2 offset:2024\n" \
  "v_mfma_f32_32x32x16_bf16 %0, a[0:3], a[8:11], %0\n" \
  "ds_read_b128 a[36:39], %3 offset:2560\n" \
  "ds_read_b128 a[40:43], %3 offset:0\n" \
  "v_mfma_f32_32x32x16_bf16 %1, a[12:15], a[8:11], %1\n" \
  "ds_read_b64 a[44:45], %2 offset:4064\n" \
  "ds_read_b64 a[46:47], %2 offset:4072\n" \
  "s_waitcnt lgkmcnt(6)\n" \
  "v_mfma_f32_32x32x16_bf16 %1, a[16:19], a[20:23], %1\n" \
  "ds_read_b64 a[0:1], %2 offset:1984\n" \
  "ds_read_b64 a[2:3], %2 offset:1992\n" \
  "v_mfma_f32_32x32x16_bf16 %0, a[16:19], a[24:27], %0\n" \
  "ds_read_b128 a[4:7], %3 offset:2592\n" \
  "ds_read_b128 a[8:11], %3 offset:32\n" \
  "v_mfma_f32_32x32x16_bf16 %1, a[28:31], a[24:27], %1\n" \
  "ds_read_b64 a[12:13], %2 offset:4032\n" \
  "ds_read_b64 a[14:15], %2 offset:4040\n" \
  "s_waitcnt lgkmcnt(6)\n" \
  "v_mfma_f32_32x32x16_bf16 %1, a[32:35], a[36:39], %1\n" \
  "v_mfma_f32_32x32x16_bf16 %0, a[32:35], a[40:43], %0\n" \
  "v_mfma_f32_32x32x16_bf16 %1, a[44:47], a[40:43], %1\n" \
  "s_waitcnt lgkmcnt(0)\n" \
  "v_mfma_f32_32x32x16_bf16 %1, a[0:3], a[4:7], %1\n" \
  "v_mfma_f32_32x32x16_bf16 %0, a[0:3], a[8:11], %0\n" \
  "v_mfma_f32_32x32x16_bf16 %1, a[12:15], a[8:11], %1\n" \
  "s_nop 15\n" \
  ""

#define CONV_CLOBBERS "a0", "a1", "a2", "a3", "a4", "a5", "a6", "a7", "a8", "a9", "a10", "a11", "a12", "a13", "a14", "a15", "a16", "a17", "a18", "a19", "a20", "a21", "a22", "a23", "a24", "a25", "a26", "a27", "a28", "a29", "a30", "a31", "a32", "a33", "a34", "a35", "a36", "a37", "a38", "a39", "a40", "a41", "a42", "a43", "a44", "a45", "a46", "a47", "memory"
#define REC_CLOBBERS \
  "v0", "v1", "v2", "v3", "v4", "v5", "v6", "v7", "v8", "v9", "v10", "v11", "v12", "v13", "v14", "v15", "v16", "v17", "v18", "v19", "v20", "v21", "v22",\
  "v23", "v24", "v25", "v26", "v27", "v28", "v29", "v30", "v31", "v32", "v33", "v34", "v35", "v36", "v37", "v38", "v39", "v40", "v41", "v42", "v43",\
  "v44", "v45", "v46", "v47", "v48", "v49", "v50", "v51", "v52", "v53", "v54", "v55", "v56", "v57", "v58", "v59", "v60", "v61", "v62", "v63", "v64",\
  "v65", "v66", "v67", "v68", "v69", "v70", "v71", "v72", "v73", "v74", "v75", "v76", "v77", "v78", "v79", "v80", "v81", "v82", "v83", "v84", "v85",\
  "v86", "v87", "v88", "v89", "v90", "v91", "v92", "v93", "v94", "v95", "v96", "v97", "v98", "v99", "v100", "v101", "v102", "v103", "v104", "v105",\
  "v106", "v107", "v108", "v109", "v110", "v111", "v112", "v113", "v114", "v115", "v116", "v117", "v118", "v119", "v120", "v121", "v122", "v123",\
  "v124", "v125", "v126", "v127", "v128", "v129", "v130", "v131", "v132", "v133", "v134", "v135", "v136", "v137", "v138", "v139", "v140", "v141",\
  "v142", "v143", "v144", "v145", "v146", "v147", "v148", "v149", "v150", "v151", "v152", "v153", "v154", "v155", "v156", "v157", "v158", "v159",\
  "v160", "v161", "v162", "v163", "v164", "v165", "v166", "v167", "v168", "v169", "v170", "v171", "v172", "v173", "v174", "v175", "v176", "v177",\
  "v178", "v179", "v180", "v181", "v182", "v183", "v184", "v185", "v186", "v187", "v188", "v189", "v190", "v191", "v192", "v193", "v194", "v195",\
  "v196", "v197", "v198", "v199", "v200", "v201", "v202", "v203", "v204", "v205", "v206", "v207", "v208", "v209", "v210", "v211", "v212", "v213",\
  "v214", "v215", "v216", "v217", "v218", "v219", "v220", "v221", "v222", "v223", "v224", "v225", "v226", "v227", "v228", "v229", "v230", "v231",\
  "v232", "v233", "v234", "v235", "v236", "v237", "v238", "v239", "v240", "v241", "v242", "v243", "v244", "v245", "v246", "v247", "v248", "v249",\
  "v250", "v251", "v252", "v253", "v254", "v255", "a0", "a1", "a2", "a3", "a4", "a5", "a6", "a7", "a8", "a9", "a10", "a11", "a12", "a13", "a14", "a15",\
  "a16", "a17", "a18", "a19", "a20", "a21", "a22", "a23", "a24", "a25", "a26", "a27", "a28", "a29", "a30", "a31", "a32", "a33", "a34", "a35", "a36",\
  "a37", "a38", "a39", "a40", "a41", "a42", "a43", "a44", "a45", "a46", "a47", "a48", "a49", "a50", "a51", "a52", "a53", "a54", "a55", "a56", "a57",\
  "a58", "a59", "a60", "a61", "a62", "a63", "a64", "a65", "a66", "a67", "a68", "a69", "a70", "a71", "a72", "a73", "a74", "a75", "a76", "a77", "a78",\
  "a79", "a80", "a81", "a82", "a83", "a84", "a85", "a86", "a87", "a88", "a89", "a90", "a91", "a92", "a93", "a94", "a95", "a96", "a97", "a98", "a99",\
  "a100", "a101", "a102", "a103", "a104", "a105", "a106", "a107", "a108", "a109", "a110", "a111", "a112", "a113", "a114", "a115", "a116", "a117",\
  "a118", "a119", "a120", "a121", "a122", "a123", "a124", "a125", "a126", "a127", "a128", "a129", "a130", "a131", "a132", "a133", "a134", "a135",\
  "a136", "a137", "a138", "a139", "a140", "a141", "a142", "a143", "a144", "a145", "a146", "a147", "a148", "a149", "a150", "a151", "a152", "a153",\
  "a154", "a155", "a156", "a157", "a158", "a159", "a160", "a161", "a162", "a163", "a164", "a165", "a166", "a167", "a168", "a169", "a170", "a171",\
  "a172", "a173", "a174", "a175", "a176", "a177", "a178", "a179", "a180", "a181", "a182", "a183", "a184", "a185", "a186", "a187", "a188", "a189",\
  "a190", "a191", "a192", "a193", "a194", "a195", "a196", "a197", "a198", "a199", "a200", "a201", "a202", "a203", "a204", "a205", "a206", "a207",\
  "a208", "a209", "a210", "a211", "a212", "a213", "a214", "a215", "a216", "a217", "a218", "a219", "a220", "a221", "a222", "a223", "a224", "a225",\
  "a226", "a227", "a228", "a229", "a230", "a231", "a232", "a233", "a234", "a235", "a236", "a237", "a238", "a239", "a240", "a241", "a242", "a243",\
  "a244", "a245", "a246", "a247", "a248", "a249", "a250", "a251", "a252", "a253", "a254", "a255", "s34", "s35", "s36", "s37", "s38", "s39", "s40", "s41", "s42",\
  "s43", "s44", "s45", "s46", "s47", "s48", "s49", "s50", "s51", "s52", "s53", "s54", "s55", "s56", "s57", "s58", "s59", "s60", "s61", "s62", "s63",\
  "s64", "s65", "s66", "s67", "s68", "s69", "s70", "s71", "s72", "s73", "s74", "s75", "s76", "s77", "s78", "s79", "s80", "s81", "s82", "s83", "s84",\
  "s85", "s86", "s87", "s88", "s89", "s90", "s91", "s92", "s93", "s94", "s95", "s96", "s97", "s98", "s99", "vcc", "scc", "memory"
__device__ __forceinline__ void role_rec(const Args& a, char LAS* smem, const int cu) {
  const int tid = threadIdx.x, lane = tid & 63, w = __builtin_amdgcn_readfirstlane(tid >> 6);
  const int head = cu >> 2, j = cu & 3;
  const __amdgpu_buffer_rsrc_t ex = __builtin_amdgcn_make_buffer_rsrc(a.ex, 0, (int)EX_BYTES, 0x00020000);
  u32x4 v0 = *(const u32x4*)(a.mem0 + (size_t)(4 * (cu & 1) + 2 * ((tid >> 2) & 1) + (tid & 1)) * DM + 16 * (cu >> 1) + 8 * ((tid >> 1) & 1));
  const u32 pol = wait_pol(a.ctrl);
  {
#pragma unroll
    for (int e = 0; e < 4; ++e) v0[e] = nosent(v0[e]);
    const u32 oa = exoff(pol, EXP_A + w, cu >> 6) + (cu & 63) * 128;
    __builtin_amdgcn_raw_buffer_store_b128(v0, ex, (lane < 8 ? 0u : 0x80000000u) + lane * 16, oa, SC1);
    const u32x4 sv = (u32x4){SENT, SENT, SENT, SENT};
    const int ln = lane >> 3;
    u32 vb = ln < 2 ? (u32)((4 * j + w) * 256 + ln * 128) : (ln == 2 ? (u32)(4096 + (4 * j + w) * 128) : (u32)(6144 + (ln - 4) * 128));
    vb += (lane & 7) * 16;
    if (ln == 3 || (ln > 3 && ((cu & 31) != 0 || w != 1))) vb = 0x80000000u;
#pragma unroll
    for (int s = 0; s < 4; ++s) __builtin_amdgcn_raw_buffer_store_b128(sv, ex, vb, offB(pol, s, head), SC1);
#pragma unroll
    for (int i = 0; i < 2; ++i) __builtin_amdgcn_raw_buffer_store_b128(sv, ex, (cu & 1) * 4096 + tid * 16, exoff(pol, EXP_C, i * 64 + (cu >> 1)), SC1);
#pragma unroll
    for (int i = 0; i < 8; ++i) {
      const int s = i * 8 + ln;
      __builtin_amdgcn_raw_buffer_store_b128(sv, ex, (s == 0 ? 0x80000000u : 0u) + s * 32768 + (lane & 7) * 16, oa, SC1);
    }
  }
  const u16* pq = a.Wq + head * DH + 32 * j;
  const u16* pk = a.Wk + head * DH + 32 * j;
  const u16* pv = a.Wv + head * DH + 32 * j;
  const u16* po = a.Wo + (size_t)head * DH * DM + 256 * j;
  asm volatile(REC_ASM
               :: "s"(pq), "s"(pk), "s"(pv), "s"(po), "s"(a.ex), "s"(a.ctrl), "s"(cu), "s"(w), "s"(pol), "s"((u32)(uintptr_t)smem)
               : REC_CLOBBERS);
  __builtin_unreachable();
}
#define S_ROW 4160
#define S_WSZ (4 * S_ROW + 96 * 80 + 2048 + 64)
#define T_LD 132
__device__ __forceinline__ u32 offT(u32 pol, u32 ch) { return (ch >> 1) * 16384 + ((~(pol >> (ch >> 8)) & 1u) << 13) + (ch & 1) * 4096; }
__device__ __forceinline__ void wait_all(u32* ctrl, int word, int n, int slp) {
  const int lane = threadIdx.x & 63;
  for (int spins = 0;; ++spins) {
    asm volatile("" ::: "memory");
    const u32 v = __hip_atomic_load(ctrl + word + 32 * min(lane, n - 1), RLX_AGENT);
    if (__builtin_amdgcn_ballot_w64(v == 0) == 0) break;
    if (spins > SPIN_CAP) { set_dead(ctrl); break; }
    if ((spins & 63) == 63 && __builtin_amdgcn_readfirstlane((int)is_dead(ctrl))) break;
    sleepn(slp);
  }
  asm volatile("" ::: "memory");
}
__device__ __forceinline__ void ssm_in(const Args& a, char LAS* smem, const int rb, const u32 pol) {
  const int tid = threadIdx.x;
  const __amdgpu_buffer_rsrc_t rt = __builtin_amdgcn_make_buffer_rsrc(a.ex + WS_UT_OFF, 0, 8 << 20, 0x00020000);
#pragma unroll
  for (int q = 0; q < 4; ++q) {
    u32x4 x[8];
#pragma unroll
    for (int i = 0; i < 4; ++i) {
      const u16* p = a.u + (size_t)(64 * rb + 2 * (8 * q + 2 * i + (tid >> 7))) * SSM_D + (tid & 127) * 8;
      x[2 * i] = *(const u32x4*)p; x[2 * i + 1] = *(const u32x4*)(p + SSM_D);
    }
#pragma unroll
    for (int i = 0; i < 4; ++i)
#pragma unroll
      for (int e = 0; e < 4; ++e) {
        char LAS* o = smem + ((tid & 127) + 256 * e) * T_LD + (8 * q + 2 * i + (tid >> 7)) * 4;
        *(LAS u32*)o = __builtin_amdgcn_perm(x[2 * i + 1][e], x[2 * i][e], 0x05040100u);
        *(LAS u32*)(o + 128 * T_LD) = __builtin_amdgcn_perm(x[2 * i + 1][e], x[2 * i][e], 0x07060302u);
      }
  }
  __syncthreads();
#pragma unroll 4
  for (int i = 0; i < 32; ++i) {
    const int sl = 32 * i + (tid >> 3), ch = (sl & 127) * 8 + (sl >> 7);
    const LAS u32* p = (const LAS u32*)(smem + sl * T_LD + (tid & 7) * 16);
    __builtin_amdgcn_raw_buffer_store_b128((u32x4){p[0], p[1], p[2], p[3]}, rt, offT(pol, ch) + rb * 128 + (tid & 7) * 16, 0, SC1);
  }
  WAIT_VM0();
  __syncthreads();
  if (tid == 0) __hip_atomic_store(a.ctrl + CW_UIN + rb * 32, 1u, RLX_AGENT);
}
__device__ __forceinline__ void ssm_out(const Args& a, char LAS* smem, const int rb, const int hb, const u32 pol) {
  const int tid = threadIdx.x;
  const __amdgpu_buffer_rsrc_t rt = __builtin_amdgcn_make_buffer_rsrc(a.ex + WS_UT_OFF, 0, 8 << 20, 0x00020000);
  wait_all(a.ctrl, CW_YOUT + hb * 48 * 32, 48, 8);
  u32x4 x[16];
#pragma unroll
  for (int i = 0; i < 16; ++i) {
    const int sl = 32 * i + (tid >> 3), ch = (sl & 63) * 8 + (sl >> 6);
    x[i] = __builtin_amdgcn_raw_buffer_load_b128(rt, offT(pol, 512 * hb + ch) + rb * 128 + (tid & 7) * 16, 0, SC1);
  }
#pragma unroll
  for (int i = 0; i < 16; ++i) {
    LAS u32* p = (LAS u32*)(smem + (32 * i + (tid >> 3)) * T_LD + (tid & 7) * 16);
#pragma unroll
    for (int e = 0; e < 4; ++e) p[e] = x[i][e];
  }
  __syncthreads();
#pragma unroll 2
  for (int i = 0; i < 8; ++i) {
    const int rp = 4 * i + (tid >> 6);
    u32x4 v0, v1;
#pragma unroll
    for (int e = 0; e < 4; ++e) {
      const char LAS* o = smem + ((tid & 63) + 128 * e) * T_LD + rp * 4;
      const u32 x0 = *(const LAS u32*)o, x1 = *(const LAS u32*)(o + 64 * T_LD);
      v0[e] = __builtin_amdgcn_perm(x1, x0, 0x05040100u); v1[e] = __builtin_amdgcn_perm(x1, x0, 0x07060302u);
    }
    u16* p = a.out + (size_t)(64 * rb + 2 * rp) * OUTLD + DM + 512 * hb + (tid & 63) * 8;
    *(u32x4*)p = v0; *(u32x4*)(p + OUTLD) = v1;
  }
}
__device__ __forceinline__ void split2(float a, float b, u32& hi, u32& lo) {
  hi = pk2(a, b);
  lo = pk2(a - __uint_as_float(hi << 16), b - __uint_as_float(hi & 0xffff0000u));
}
__device__ __forceinline__ void cexp1(float ar, float ai, float l, float& xr, float& xi) {
  const float e = __builtin_amdgcn_exp2f(ar * l);
  const float rv = __builtin_amdgcn_fractf(ai * l);
  xr = e * __builtin_amdgcn_cosf(rv); xi = e * __builtin_amdgcn_sinf(rv);
}
__device__ __forceinline__ u32 lo_hi(u32 lo, u32 hi) { return __builtin_amdgcn_perm(hi, lo, 0x07060100u); }
__device__ __forceinline__ void role_ssm(const Args& a, char LAS* smem, const int cx, const int rank) {
  const int tid = threadIdx.x, lane = tid & 63, w = __builtin_amdgcn_readfirstlane(tid >> 6);
  const int c = lane & 15, g = lane >> 4, b = lane & 31, h = lane >> 5;
  const u32 pol = wait_pol(a.ctrl);
  const __amdgpu_buffer_rsrc_t rt = __builtin_amdgcn_make_buffer_rsrc(a.ex + WS_UT_OFF, 0, 8 << 20, 0x00020000);
  char LAS* lu = smem + w * S_WSZ;
  char LAS* lw = lu + S_ROW;
  char LAS* lk = lw + 3 * S_ROW + 31 * 80;
  char LAS* lp = lk + 65 * 80;
  if (lane < 4) *(LAS u32x4*)(lu + lane * 16) = (u32x4){0, 0, 0, 0};
  for (int i = lane; i < 160; i += 64) *(LAS u32x4*)(lw + 3 * S_ROW + i * 16) = (u32x4){0, 0, 0, 0};
  float la[3];
  la[0] = (float)c;
  la[1] = c < 8 ? (float)(256 + 2 * c) : (float)(512 + 256 * ((c - 8) >> 2) + 4 * ((c - 8) & 3));
  la[2] = c < 8 ? (float)(1024 + 256 * (c >> 1) + 8 * (c & 1)) : (c == 8 ? 2048.f : 0.f);
  const float ly = (float)(16 * c);
  u32x4 un[4];
  auto fetch_u = [&](int d) {
#pragma unroll
    for (int i = 0; i < 4; ++i) un[i] = __builtin_amdgcn_raw_buffer_load_b128(rt, lane * 16 + i * 1024, offT(pol, d), SC1);
  };
  u32 pr[6]; u16 pdt, pds;
  auto fetch_p = [&](int d) {
    const size_t o = (size_t)d * SSM_N + 2 * lane;
    pr[0] = *(const u32*)(a.A_re + o); pr[1] = *(const u32*)(a.A_im + o); pr[2] = *(const u32*)(a.B_re + o);
    pr[3] = *(const u32*)(a.B_im + o); pr[4] = *(const u32*)(a.C_re + o); pr[5] = *(const u32*)(a.C_im + o);
    pdt = a.log_dt[d]; pds = a.Dskip[d];
  };
#define SST(i)
  const int first = cx * 256 + rank * 4 + w, last = cx * 256 + 256;
  fetch_p(first);
  {
    for (int d = first; d < last; d += 24) {
      const int nxt = d + 24 < last ? d + 24 : d;
      SST(0);
      const float dt = rbf(expf(bf2f(pdt))), ds = bf2f(pds);
#pragma unroll
      for (int i = 0; i < 2; ++i) {
        auto el = [&](int k) { return __uint_as_float(i ? (pr[k] & 0xffff0000u) : (pr[k] << 16)); };
        const float br = dt * el(2), bi = dt * el(3), cr = el(4), ci = el(5);
        *(LAS f32x4*)(lp + (2 * lane + i) * 16) = (f32x4){dt * el(0) * 1.4426950408889634f, dt * el(1) * 0.15915494309189535f, cr * br - ci * bi, cr * bi + ci * br};
      }
      fetch_p(nxt);
      SST(1);
      f32x4 ka[3];
#pragma unroll
      for (int t = 0; t < 3; ++t) ka[t] = (f32x4){0, 0, 0, 0};
      f32x4 pn[4];
#pragma unroll
      for (int i = 0; i < 4; ++i) pn[i] = *(const LAS f32x4*)(lp + (4 * g + i) * 16);
      for (int k = 0; k < 8; ++k) {
        f32x4 p[4];
#pragma unroll
        for (int i = 0; i < 4; ++i) { p[i] = pn[i]; pn[i] = *(const LAS f32x4*)(lp + (16 * ((k + 1) & 7) + 4 * g + i) * 16); }
        u32x4 yh, yl, xh[3], xl[3];
#pragma unroll
        for (int i = 0; i < 4; ++i) {
          float er, ei; u32 hi, lo;
          cexp1(p[i][0], p[i][1], ly, er, ei);
          split2(p[i][2] * er - p[i][3] * ei, -(p[i][2] * ei + p[i][3] * er), hi, lo); yh[i] = hi; yl[i] = lo;
#pragma unroll
          for (int t = 0; t < 3; ++t) { cexp1(p[i][0], p[i][1], la[t], er, ei); split2(er, ei, hi, lo); xh[t][i] = hi; xl[t][i] = lo; }
        }
        __builtin_amdgcn_sched_barrier(0);
#pragma unroll
        for (int t = 0; t < 3; ++t) ka[t] = MFMA32(asb(xh[t]), asb(yh), ka[t]);
#pragma unroll
        for (int t = 0; t < 3; ++t) ka[t] = MFMA32(asb(xh[t]), asb(yl), ka[t]);
#pragma unroll
        for (int t = 0; t < 3; ++t) ka[t] = MFMA32(asb(xl[t]), asb(yh), ka[t]);
        __builtin_amdgcn_sched_barrier(0);
      }
      SST(2);
      {
        const int l = 16 * c + 4 * g;
        *(LAS u32x2*)(lk + (1 + (l >> 5)) * 80 + ((l & 31) ^ 4) * 2) = (u32x2){pk2(ka[0][3], ka[0][2]), pk2(ka[0][1], ka[0][0])};
        const int j1 = g < 2 ? 8 * c + 4 * g : 128 + 64 * (g - 2) + 4 * c;
        *(LAS u32x4*)(lp + j1 * 4) = (u32x4){pk2(ka[1][0], ka[1][0]), pk2(ka[1][1], ka[1][1]), pk2(ka[1][2], ka[1][2]), pk2(ka[1][3], ka[1][3])};
        if (g < 2) {
          *(LAS u32x2*)(lp + (256 + 64 * g + 2 * c) * 4) = (u32x2){pk2(ka[2][0], ka[2][0]), pk2(ka[2][1], ka[2][1])};
          *(LAS u32x2*)(lp + (256 + 64 * g + 32 + 2 * c) * 4) = (u32x2){pk2(ka[2][2], ka[2][2]), pk2(ka[2][3], ka[2][3])};
        } else if (lane == 32) *(LAS u32*)(lp + 384 * 4) = pk2(ka[2][0], ka[2][0]);
        if (lane < 32) {
          const u32x4 v = *(const LAS u32x4*)(lp + lane * 16);
          const u32 v4 = *(const LAS u32*)(lp + lane * 16 + 16);
          *(LAS u32x4*)(lk + (9 + (lane >> 2)) * 80 + (lane & 3) * 16) = (u32x4){lo_hi(v4, v[3]), v[2], lo_hi(v[2], v[1]), v[0]};
        }
        {
          const u32x2 v = *(const LAS u32x2*)(lp + 512 + lane * 8);
          const u32 v2 = *(const LAS u32*)(lp + 512 + lane * 8 + 8);
          *(LAS u32x4*)(lk + (17 + (lane >> 2)) * 80 + (lane & 3) * 16) = (u32x4){v2, v[1], lo_hi(v[1], v[0]), v[0]};
        }
        {
          const u32x2 v = *(const LAS u32x2*)(lp + 1024 + lane * 8);
          const u32 v2 = *(const LAS u32*)(lp + 1024 + lane * 8 + 8);
          char LAS* o = lk + (33 + (lane >> 1)) * 80 + (lane & 1) * 32;
          *(LAS u32x4*)o = (u32x4){v[1], lo_hi(v[1], v[0]), v[0], v[0]};
          *(LAS u32x4*)(o + 16) = (u32x4){v2, v2, v[1], v[1]};
        }
      }
      if (d == first) { wait_all(a.ctrl, CW_UIN, 32, 8); fetch_u(d); }
#pragma unroll
      for (int i = 0; i < 4; ++i) *(LAS u32x4*)(lu + 64 + lane * 16 + i * 1024) = un[i];
      if (nxt != d) fetch_u(nxt);
#pragma unroll
      for (int i = 0; i < 5; ++i) {
        const int q = lane + 64 * i;
        if (i < 4 || lane < 4) {
          const u32x4 x = *(const LAS u32x4*)(lu + q * 16);
          u32x2 y = *(const LAS u32x2*)(lu + q * 16 + 16);
          *(LAS u32x4*)(lw + q * 16) = (u32x4){__builtin_amdgcn_alignbit(x[1], x[0], 16), __builtin_amdgcn_alignbit(x[2], x[1], 16), __builtin_amdgcn_alignbit(x[3], x[2], 16), __builtin_amdgcn_alignbit(y[0], x[3], 16)};
          *(LAS u32x4*)(lw + S_ROW + q * 16) = (u32x4){x[1], x[2], x[3], y[0]};
          *(LAS u32x4*)(lw + 2 * S_ROW + q * 16) = (u32x4){__builtin_amdgcn_alignbit(x[2], x[1], 16), __builtin_amdgcn_alignbit(x[3], x[2], 16), __builtin_amdgcn_alignbit(y[0], x[3], 16), __builtin_amdgcn_alignbit(y[1], y[0], 16)};
        }
      }
      SST(3);
      const int p0 = 32 + b - 8 * h - 7, cp = p0 & 3;
      const char LAS* ra = lu + cp * S_ROW + (p0 - cp) * 2 - 32;
      const char LAS* rb = lk + 16 * h + 80 * (b - 30);
      f32x16 y0, y1;
      asm volatile(CONV_ASM : "=&v"(y0), "=&v"(y1) : "v"((u32)(uintptr_t)ra), "v"((u32)(uintptr_t)rb) : CONV_CLOBBERS);
      SST(4);
#pragma unroll
      for (int cg = 0; cg < 2; ++cg)
#pragma unroll
        for (int i4 = 0; i4 < 4; ++i4) {
          LAS u32x2* up = (LAS u32x2*)(lu + 64 + (32 * (32 * cg + b) + 8 * i4 + 4 * h) * 2);
          const u32x2 uv = *up;
          float v[4];
#pragma unroll
          for (int e = 0; e < 4; ++e) v[e] = cg ? y1[4 * i4 + e] : y0[4 * i4 + e];
          const u32 ya = pk2(v[0], v[1]), yb = pk2(v[2], v[3]);
          const u32 sa = pk2(__uint_as_float(uv[0] << 16) * ds, __uint_as_float(uv[0] & 0xffff0000u) * ds);
          const u32 sb = pk2(__uint_as_float(uv[1] << 16) * ds, __uint_as_float(uv[1] & 0xffff0000u) * ds);
          *up = (u32x2){pk2(__uint_as_float(ya << 16) + __uint_as_float(sa << 16), __uint_as_float(ya & 0xffff0000u) + __uint_as_float(sa & 0xffff0000u)),
                        pk2(__uint_as_float(yb << 16) + __uint_as_float(sb << 16), __uint_as_float(yb & 0xffff0000u) + __uint_as_float(sb & 0xffff0000u))};
        }
#pragma unroll
      for (int i = 0; i < 4; ++i) __builtin_amdgcn_raw_buffer_store_b128(*(const LAS u32x4*)(lu + 64 + lane * 16 + i * 1024), rt, lane * 16 + i * 1024, offT(pol, d), SC1);
      SST(5);
    }
    WAIT_VM0();
    if (lane == 0) __hip_atomic_store(a.ctrl + CW_YOUT + (cx * 24 + rank * 4 + w) * 32, 1u, RLX_AGENT);
  }
}
#define BULK_CLOBBERS \
  "v4", "v5", "v6", "v7", "v8", "v9", "v10", "v11", "v12", "v13", "v14", "v15", "v16", "v17", "v18", "v19", "v20", "v21", "v22", "v23", "v24", "v25",\
  "v26", "v27", "v28", "v29", "v30", "v31", "v32", "v33", "v34", "v35", "v36", "v37", "v38", "v39", "v40", "v41", "v42", "v43", "v44", "v45", "v46",\
  "v47", "v48", "v49", "v50", "v51", "v52", "v53", "v54", "v55", "v56", "v57", "v58", "v59", "v60", "v61", "v62", "v63", "v64", "v65", "v66", "v67",\
  "v68", "v69", "v70", "v71", "v72", "v73", "v74", "v75", "v76", "v77", "v78", "v79", "v80", "v81", "v82", "v83", "v84", "v85", "v86", "v87", "v88",\
  "v89", "v90", "v91", "v92", "v93", "v94", "v95", "v96", "v97", "v98", "v99", "v100", "v101", "v102", "v103", "v104", "v105", "v106", "v107", "v108",\
  "v109", "v110", "v111", "v112", "v113", "v114", "v115", "v116", "v117", "v118", "v119", "v120", "v121", "v122", "v123", "v124", "v125", "v126",\
  "v127", "v128", "v129", "v130", "v131", "v132", "v133", "v134", "v135", "v136", "v137", "v138", "v139", "v140", "v141", "v142", "v143", "v144",\
  "v145", "v146", "v147", "v148", "v149", "v150", "v151", "v152", "v153", "v154", "v155", "v156", "v157", "v158", "v159", "v160", "v161", "v162",\
  "v163", "v164", "v165", "v166", "v167", "v168", "v169", "v170", "v171", "v172", "v173", "v174", "v175", "v176", "v177", "v178", "v179", "v180",\
  "v181", "v182", "v183", "v184", "v185", "v186", "v187", "v188", "v189", "v190", "v191", "v192", "v193", "v194", "v195", "v196", "v197", "v198",\
  "v199", "v200", "v201", "v202", "v203", "v204", "v205", "v206", "v207", "v208", "v209", "v210", "v211", "v212", "v213", "v214", "v215", "v216",\
  "v217", "v218", "v219", "v220", "v221", "v222", "v223", "v224", "v225", "v226", "v227", "v228", "v229", "v230", "v231", "v232", "v233", "v234",\
  "v235", "v236", "v237", "v238", "v239", "v240", "v241", "v242", "v243", "v244", "v245", "v246", "v247", "v248", "v249", "v250", "v251", "v252",\
  "v253", "v254", "v255", "a0", "a1", "a2", "a3", "a4", "a5", "a6", "a7", "a8", "a9", "a10", "a11", "a12", "a13", "a14", "a15", "a16", "a17", "a18",\
  "a19", "a20", "a21", "a22", "a23", "a24", "a25", "a26", "a27", "a28", "a29", "a30", "a31", "a32", "a33", "a34", "a35", "a36", "a37", "a38", "a39",\
  "a40", "a41", "a42", "a43", "a44", "a45", "a46", "a47", "a48", "a49", "a50", "a51", "a52", "a53", "a54", "a55", "a56", "a57", "a58", "a59", "a60",\
  "a61", "a62", "a63", "a64", "a65", "a66", "a67", "a68", "a69", "a70", "a71", "a72", "a73", "a74", "a75", "a76", "a77", "a78", "a79", "a80", "a81",\
  "a82", "a83", "a84", "a85", "a86", "a87", "a88", "a89", "a90", "a91", "a92", "a93", "a94", "a95", "a96", "a97", "a98", "a99", "a100", "a101", "a102",\
  "a103", "a104", "a105", "a106", "a107", "a108", "a109", "a110", "a111", "a112", "a113", "a114", "a115", "a116", "a117", "a118", "a119", "a120",\
  "a121", "a122", "a123", "a124", "a125", "a126", "a127", "a128", "a129", "a130", "a131", "a132", "a133", "a134", "a135", "a136", "a137", "a138",\
  "a139", "a140", "a141", "a142", "a143", "a144", "a145", "a146", "a147", "a148", "a149", "a150", "a151", "a152", "a153", "a154", "a155", "a156",\
  "a157", "a158", "a159", "a160", "a161", "a162", "a163", "a164", "a165", "a166", "a167", "a168", "a169", "a170", "a171", "a172", "a173", "a174",\
  "a175", "a176", "a177", "a178", "a179", "a180", "a181", "a182", "a183", "a184", "a185", "a186", "a187", "a188", "a189", "a190", "a191", "a192",\
  "a193", "a194", "a195", "a196", "a197", "a198", "a199", "a200", "a201", "a202", "a203", "a204", "a205", "a206", "a207", "a208", "a209", "a210",\
  "a211", "a212", "a213", "a214", "a215", "a216", "a217", "a218", "a219", "a220", "a221", "a222", "a223", "a224", "a225", "a226", "a227", "a228",\
  "a229", "a230", "a231", "a232", "a233", "a234", "a235", "a236", "a237", "a238", "a239", "a240", "a241", "a242", "a243", "a244", "a245", "a246",\
  "a247", "a248", "a249", "a250", "a251", "a252", "a253", "a254", "a255", "s68", "s69", "s70", "s71", "s72", "s73", "s74", "s75", "s76", "s77", "s78",\
  "s79", "s80", "s81", "s82", "s83", "s84", "s85", "s86", "s87", "s88", "s89", "s90", "s91", "s92", "s93", "s94", "s95", "s96", "vcc", "scc", "memory"
#define GEMM_CLOBBERS \
  "v4", "v5", "v6", "v7", "v8", "v9", "v10", "v11", "v12", "v13", "v14", "v15", "v16", "v17", "v18", "v19", "v20", "v21", "v22", "v23", "v24", "v25",\
  "v26", "v27", "v28", "v29", "v30", "v31", "v32", "v33", "v34", "v35", "v36", "v37", "v38", "v39", "v40", "v41", "v42", "v43", "v44", "v45", "v46",\
  "v47", "v48", "v49", "v50", "v51", "v52", "v53", "v54", "v55", "v56", "v57", "v58", "v59", "v60", "v61", "v62", "v63", "v64", "v65", "v66", "v67",\
  "v68", "v69", "v70", "v71", "v72", "v73", "v74", "v75", "v76", "v77", "v78", "v79", "v80", "v81", "v82", "v83", "v84", "v85", "v86", "v87", "v88",\
  "v89", "v90", "v91", "v92", "v93", "v94", "v95", "v96", "v97", "v98", "v99", "v116", "v117", "v118", "v119", "v120", "v121", "v122", "v123", "v124",\
  "v125", "v126", "v127", "v128", "v129", "v130", "v131", "v132", "v133", "v134", "v135", "v136", "v137", "v138", "v139", "v140", "v141", "v142",\
  "v143", "v144", "v145", "v146", "v147", "v148", "v149", "v150", "v151", "v152", "v153", "v154", "v155", "v156", "v157", "v158", "v159", "v160",\
  "v161", "v162", "v163", "v164", "v165", "v166", "v167", "v168", "v169", "v170", "v171", "v172", "v173", "v174", "v175", "v176", "v177", "v178",\
  "v179", "v180", "v181", "v182", "v183", "v184", "v185", "v186", "v187", "v188", "v189", "v190", "v191", "v192", "v193", "v194", "v195", "v196",\
  "v197", "v198", "v199", "v200", "v201", "v202", "v203", "v204", "v205", "v206", "v207", "v208", "v209", "v210", "v211", "v212", "v213", "v214",\
  "v215", "v216", "v217", "v218", "v219", "v220", "v221", "v222", "v223", "v224", "v225", "v226", "v227", "v228", "v229", "v230", "v231", "v232",\
  "v233", "v234", "v235", "v236", "v237", "v238", "v239", "v240", "v241", "v242", "v243", "v244", "v245", "v246", "v247", "v248", "v249", "v250",\
  "v251", "v252", "v253", "v254", "v255", "a0", "a1", "a2", "a3", "a4", "a5", "a6", "a7", "a8", "a9", "a10", "a11", "a12", "a13", "a14", "a15", "a16",\
  "a17", "a18", "a19", "a20", "a21", "a22", "a23", "a24", "a25", "a26", "a27", "a28", "a29", "a30", "a31", "a32", "a33", "a34", "a35", "a36", "a37",\
  "a38", "a39", "a40", "a41", "a42", "a43", "a44", "a45", "a46", "a47", "a48", "a49", "a50", "a51", "a52", "a53", "a54", "a55", "a56", "a57", "a58",\
  "a59", "a60", "a61", "a62", "a63", "a64", "a65", "a66", "a67", "a68", "a69", "a70", "a71", "a72", "a73", "a74", "a75", "a76", "a77", "a78", "a79",\
  "a80", "a81", "a82", "a83", "a84", "a85", "a86", "a87", "a88", "a89", "a90", "a91", "a92", "a93", "a94", "a95", "a96", "a97", "a98", "a99", "a100",\
  "a101", "a102", "a103", "a104", "a105", "a106", "a107", "a108", "a109", "a110", "a111", "a112", "a113", "a114", "a115", "a116", "a117", "a118",\
  "a119", "a120", "a121", "a122", "a123", "a124", "a125", "a126", "a127", "a128", "a129", "a130", "a131", "a132", "a133", "a134", "a135", "a136",\
  "a137", "a138", "a139", "a140", "a141", "a142", "a143", "a144", "a145", "a146", "a147", "a148", "a149", "a150", "a151", "a152", "a153", "a154",\
  "a155", "a156", "a157", "a158", "a159", "a160", "a161", "a162", "a163", "a164", "a165", "a166", "a167", "a168", "a169", "a170", "a171", "a172",\
  "a173", "a174", "a175", "a176", "a177", "a178", "a179", "a180", "a181", "a182", "a183", "a184", "a185", "a186", "a187", "a188", "a189", "a190",\
  "a191", "a192", "a193", "a194", "a195", "a196", "a197", "a198", "a199", "a200", "a201", "a202", "a203", "a204", "a205", "a206", "a207", "a208",\
  "a209", "a210", "a211", "a212", "a213", "a214", "a215", "a216", "a217", "a218", "a219", "a220", "a221", "a222", "a223", "a224", "a225", "a226",\
  "a227", "a228", "a229", "a230", "a231", "a232", "a233", "a234", "a235", "a236", "a237", "a238", "a239", "a240", "a241", "a242", "a243", "a244",\
  "a245", "a246", "a247", "a248", "a249", "a250", "a251", "a252", "a253", "a254", "a255", "s68", "s69", "s70", "s71", "s72", "s73", "s74", "s75", "s76",\
  "s77", "s78", "s79", "s80", "s81", "s82", "s83", "s84", "s85", "s86", "s87", "s88", "s89", "s90", "s91", "s92", "s93", "s94", "s95", "s96", "vcc", "scc", "memory"
__device__ __forceinline__ void role_gemm(const Args& a, char LAS* smem, const int gi) {
  const int w = __builtin_amdgcn_readfirstlane(threadIdx.x >> 6);
  asm volatile(GEMM_ASM
               :: "s"(a.Wq + 64 * gi), "s"(a.Wk + 64 * gi), "s"(a.Wv + 64 * gi), "s"(a.x), "s"(a.ex + WS_Q_OFF), "s"(a.ctrl), "s"(gi), "s"(w), "s"((u32)(uintptr_t)smem), "s"(wait_pol(a.ctrl))
               : GEMM_CLOBBERS);
  ssm_out(a, smem, gi & 31, gi >> 5, wait_pol(a.ctrl));
}
__device__ __forceinline__ void role_xo(const Args& a, char LAS* smem, const int xi) {
  const int w = __builtin_amdgcn_readfirstlane(threadIdx.x >> 6);
  sleepn(XO_SLEEP);
  ssm_in(a, smem, xi, wait_pol(a.ctrl));
  asm volatile(XO_ASM
               :: "s"(a.Wo + 64 * (xi >> 1)), "s"(a.ex + WS_OX_OFF), "s"(a.out), "s"(a.ctrl), "s"(xi), "s"(w), "s"((u32)(uintptr_t)smem), "s"(wait_pol(a.ctrl))
               : BULK_CLOBBERS);
}
__device__ __forceinline__ void role_xa(const Args& a, char LAS* smem, const int idx) {
  const int w = __builtin_amdgcn_readfirstlane(threadIdx.x >> 6);
  const u32 pol = wait_pol(a.ctrl);
  sleepn(250);
  asm volatile(XA_ASM
               :: "s"(a.ex), "s"(a.ex + WS_Q_OFF), "s"(a.ctrl), "s"(idx * 4 + w), "s"(pol), "s"((u32)(uintptr_t)smem + w * 2048)
               : BULK_CLOBBERS);
}
__global__ void __launch_bounds__(128) prep_kernel(u32* ctrl, char* ex) {
  __shared__ u32 best[2];
  const int b = blockIdx.x, tid = threadIdx.x, w = tid >> 6;
  for (int i = b * 128 + tid; i < CTRL_BYTES / 4; i += EX_PAGES * 128)
    if ((i < CW_POL || i >= CW_POL + 32) && (i < 6000 || i >= 6064)) ctrl[i] = 0;
  if (w == 0) {
    u32* p0 = (u32*)(ex + ((size_t)b << 21));
    u32 b0 = 0xffffffffu, b1 = 0xffffffffu;
    for (int rep = 0; rep < NPROBE; ++rep) {
      u32* pp = p0 + ((rep * 5) & 31) * 64;
      u64 t0 = __builtin_amdgcn_s_memtime();
      u32 r0 = __hip_atomic_fetch_or(pp, 0u, RLX_AGENT);
      asm volatile("s_waitcnt vmcnt(0)" :: "v"(r0) : "memory");
      u64 t1 = __builtin_amdgcn_s_memtime();
      u32 r1 = __hip_atomic_fetch_or(pp + 2048, 0u, RLX_AGENT);
      asm volatile("s_waitcnt vmcnt(0)" :: "v"(r1) : "memory");
      u64 t2 = __builtin_amdgcn_s_memtime();
      b0 = min(b0, (u32)(t1 - t0)); b1 = min(b1, (u32)(t2 - t1));
    }
    if (tid == 0) { best[0] = b0; best[1] = b1; }
  }
  if (tid == 0) {
    const u32 xcc = (u32)__builtin_amdgcn_s_getreg(20 | (3 << 11)) & 7u;
    const u32 fast1 = best[1] < best[0] ? 1u : 0u;
    ctrl[CW_POL + b] = xcc < 4 ? fast1 : (fast1 ^ 1u);
  }
}

__global__ void __launch_bounds__(256, 1) mega_kernel(Args a) {
  __shared__ __attribute__((aligned(16))) char smem_raw[163840];
  char LAS* smem = (char LAS*)smem_raw;
  const int tid = threadIdx.x;
  if (tid == 0) {
    const u32 xcc = (u32)__builtin_amdgcn_s_getreg(20 | (3 << 11)) & 7u;
    int role = -1;
    for (int k = 0; k < 8 && role < 0; ++k) {
      const u32 cc = (xcc + k) & 7u;
      const u32 t = __hip_atomic_fetch_add(a.ctrl + CW_TICKET + cc * 32, 1u, RLX_AGENT);
      if (t < 32) role = (int)(cc * 32 + t);
    }
    *(LAS int*)smem = role;
  }
  __syncthreads();
  const int role = __builtin_amdgcn_readfirstlane(*(LAS int*)smem);
  __syncthreads();
  if (role < 0) return;
  if (role < 128) role_rec(a, smem, role);
  else {
    const int cx = (role - 128) >> 5, t = role & 31;
    if (t < 8) role_xo(a, smem, cx * 8 + t);
    else if (t < 10) role_xa(a, smem, cx * 2 + t - 8);
    else if (t < 16) role_ssm(a, smem, cx, t - 10);
    else role_gemm(a, smem, cx * 16 + t - 16);
  }
}

extern "C" void kernel_launch(void* const* d_in, const int* in_sizes, int n_in, void* d_out, int out_size,
                              void* d_ws, size_t ws_size, hipStream_t stream) {
  Args a;
  a.x = (const u16*)d_in[0]; a.mem0 = (const u16*)d_in[1];
  a.Wq = (const u16*)d_in[2]; a.Wk = (const u16*)d_in[3]; a.Wv = (const u16*)d_in[4]; a.Wo = (const u16*)d_in[5];
  a.u = (const u16*)d_in[7]; a.A_re = (const u16*)d_in[8]; a.A_im = (const u16*)d_in[9];
  a.B_re = (const u16*)d_in[10]; a.B_im = (const u16*)d_in[11]; a.C_re = (const u16*)d_in[12]; a.C_im = (const u16*)d_in[13];
  a.log_dt = (const u16*)d_in[14]; a.Dskip = (const u16*)d_in[15];
  a.out = (u16*)d_out;
  char* ws = (char*)d_ws;
  a.ctrl = (u32*)ws;
  a.ex = (char*)(((uintptr_t)ws + CTRL_BYTES + (2u << 20) - 1) & ~(uintptr_t)((2u << 20) - 1));
  prep_kernel<<<EX_PAGES, 128, 0, stream>>>(a.ctrl, a.ex);
  mega_kernel<<<256, 256, 0, stream>>>(a);
}
```

```cpp
#include <hip/hip_runtime.h>
#include <hip/hip_bf16.h>
#include <stdint.h>

#define S_LEN 2048
#define DM 1024
#define NH 32
#define DH 128
#define HD 4096
#define NSEG 64
#define SSM_L 2048
#define SSM_D 1024
#define SSM_N 128
#define OUTLD 2048

typedef unsigned short u16;
typedef unsigned int u32;
typedef unsigned long long u64;
typedef __attribute__((ext_vector_type(8))) short bf16x8;
typedef __attribute__((ext_vector_type(4))) short bf16x4;
typedef __attribute__((ext_vector_type(4))) float f32x4;
typedef __attribute__((ext_vector_type(16))) float f32x16;
typedef __attribute__((ext_vector_type(4))) unsigned int u32x4;
typedef __attribute__((ext_vector_type(2))) unsigned int u32x2;
typedef __attribute__((ext_vector_type(2))) float f32x2;
typedef __attribute__((ext_vector_type(2))) __bf16 bf2_t;
typedef short v4i16_t __attribute__((ext_vector_type(4)));
#define LAS __attribute__((address_space(3)))

#define SENT 0xFFFFFFFFu
#define RLX_AGENT __ATOMIC_RELAXED, __HIP_MEMORY_SCOPE_AGENT
#define SC1 16

__device__ __forceinline__ float bf2f(u16 v) { return __uint_as_float(((u32)v) << 16); }
__device__ __forceinline__ u32 pk2(float a, float b) {
  f32x2 f = {a, b};
  bf2_t h = __builtin_convertvector(f, bf2_t);
  return __builtin_bit_cast(u32, h);
}
__device__ __forceinline__ float rbf(float f) { return __uint_as_float(pk2(f, 0.f) << 16); }
__device__ __forceinline__ u32 nosent(u32 v) { return v < 0xFFFFFFFEu ? v : 0xFFFFFFFEu; }
__device__ __forceinline__ bool okv(u32x4 v) { return v[0] != SENT && v[1] != SENT && v[2] != SENT && v[3] != SENT; }
__device__ __forceinline__ bf16x8 asb(u32x4 v) { return __builtin_bit_cast(bf16x8, v); }
__device__ __forceinline__ void sleepn(int n) { for (int k = 0; k < n; ++k) __builtin_amdgcn_s_sleep(1); }
#define WAIT_VM0() asm volatile("s_waitcnt vmcnt(0)" ::: "memory")
#define BAR() do { asm volatile("s_waitcnt lgkmcnt(0)" ::: "memory"); __builtin_amdgcn_s_barrier(); asm volatile("" ::: "memory"); } while (0)

__device__ __forceinline__ float xmax16(float x) {
  auto r = __builtin_amdgcn_permlane16_swap(__float_as_uint(x), __float_as_uint(x), false, false);
  return fmaxf(__uint_as_float(r[0]), __uint_as_float(r[1]));
}
__device__ __forceinline__ float xmax32(float x) {
  auto r = __builtin_amdgcn_permlane32_swap(__float_as_uint(x), __float_as_uint(x), false, false);
  return fmaxf(__uint_as_float(r[0]), __uint_as_float(r[1]));
}
__device__ __forceinline__ float xadd16(float x) {
  auto r = __builtin_amdgcn_permlane16_swap(__float_as_uint(x), __float_as_uint(x), false, false);
  return __uint_as_float(r[0]) + __uint_as_float(r[1]);
}
__device__ __forceinline__ float xadd32(float x) {
  auto r = __builtin_amdgcn_permlane32_swap(__float_as_uint(x), __float_as_uint(x), false, false);
  return __uint_as_float(r[0]) + __uint_as_float(r[1]);
}
__device__ __forceinline__ u32 ror8(u32 x) { return (u32)__builtin_amdgcn_update_dpp(0, (int)x, 0x128, 0xf, 0xf, true); }
template <int N> __device__ __forceinline__ float rorf(float x) {
  return __uint_as_float((u32)__builtin_amdgcn_update_dpp(0, (int)__float_as_uint(x), 0x120 + N, 0xf, 0xf, true));
}
__device__ __forceinline__ float ror8f(float x) { return __uint_as_float(ror8(__float_as_uint(x))); }

#define MFMA32(a, b, c) __builtin_amdgcn_mfma_f32_16x16x32_bf16(a, b, c, 0, 0, 0)
#define MFMA16(a, b, c) __builtin_amdgcn_mfma_f32_16x16x16bf16_1k(a, b, c, 0, 0, 0)
#define MFMA_AV0(acc, wa, xb) asm volatile("v_mfma_f32_16x16x32_bf16 %0, %1, %2, 0" : "=&v"(acc) : "a"(wa), "v"(xb))
#define MFMA_AV(acc, wa, xb) asm volatile("v_mfma_f32_16x16x32_bf16 %0, %1, %2, %0" : "+v"(acc) : "a"(wa), "v"(xb))
#define MFMA_VA0(acc, xa, wb) asm volatile("v_mfma_f32_16x16x32_bf16 %0, %1, %2, 0" : "=&v"(acc) : "v"(xa), "a"(wb))
#define MFMA_VA(acc, xa, wb) asm volatile("v_mfma_f32_16x16x32_bf16 %0, %1, %2, %0" : "+v"(acc) : "v"(xa), "a"(wb))
#define MFMA_VV0(acc, xa, xb) asm volatile("v_mfma_f32_16x16x32_bf16 %0, %1, %2, 0" : "=&v"(acc) : "v"(xa), "v"(xb))
#define MFMA16_VV(acc, xa, xb) asm volatile("v_mfma_f32_16x16x16_bf16 %0, %1, %2, %0" : "+v"(acc) : "v"(xa), "v"(xb))
#define MFMA_ACC(acc, xa, xb) asm volatile("v_mfma_f32_16x16x32_bf16 %0, %1, %2, %0" : "+a"(acc) : "v"(xa), "v"(xb))
#define NOP_IN() asm volatile("s_nop 1" ::: "memory")
#define PIN(x) asm volatile("" : "+v"(x))
#define PINA(x) asm volatile("" : "+a"(x))

__device__ __forceinline__ bf16x4 trread(const char LAS* p) {
  return __builtin_amdgcn_ds_read_tr16_b64_v4i16((LAS v4i16_t*)p);
}
__device__ __forceinline__ bf16x8 cat8(bf16x4 a, bf16x4 b) {
  return __builtin_shufflevector(a, b, 0, 1, 2, 3, 4, 5, 6, 7);
}

#define CW_TICKET 0
#define CW_POL 256
#define CW_INIT 288
#define CW_SSMQ 352
#define CW_DEAD 320
#define CW_PROG 384
#define CW_XPROG 1408
#define CW_GPROG 3456
#define CW_UIN 2432
#define CW_YOUT 8192
#define CTRL_BYTES 49152
#define EX_PAGES 24
#define EX_BYTES ((u64)EX_PAGES << 21)
#define EXP_C 0
#define EXP_A 20
#define EXP_B 2
#define EXP_DBG 19
#define QKV_SZ ((u64)S_LEN * HD * 4)
#define WS_Q_OFF (EX_BYTES)
#define WS_K_OFF (WS_Q_OFF + QKV_SZ)
#define WS_VT_OFF (WS_K_OFF + QKV_SZ)
#define WS_OX_OFF (WS_VT_OFF + QKV_SZ)
#define WS_UT_OFF (WS_OX_OFF + QKV_SZ)
#define EX_BYTES32 50331648
#define QKV_SZ32 33554432
#define WS_K_OFF32 83886080
static_assert(EX_BYTES32 == EX_BYTES && QKV_SZ32 == QKV_SZ && WS_K_OFF32 == WS_K_OFF, "layout");

#define SPIN_CAP 300000
#define NPROBE 5
#define F_B 1520
#define F_C 3450
#define F_P 4480
#ifndef M_C
#define M_C 150
#endif
#ifndef M_A
#define M_A 150
#endif
#define T_Q 1300
#define T_TOL 100
#define D_K 0
#define DECAY 0.996
#define RI0 1.06
#define T_LATE 50
#define O_A 130
#define O_B 150
#define O_C 60
#define GAP_B 4
#define GAP_V 4
#define GAP_0 3
#define XO_SLEEP 250
#define D_PF (-200)
#define STR_(x) #x
#define STR(x) STR_(x)

struct Args {
  const u16 *x, *mem0, *Wq, *Wk, *Wv, *Wo;
  const u16 *u, *A_re, *A_im, *B_re, *B_im, *C_re, *C_im, *log_dt, *Dskip;
  u16* out;
  u32* ctrl;
  char* ex;
};

__device__ __forceinline__ bool is_dead(u32* ctrl) { return __hip_atomic_load(ctrl + CW_DEAD, RLX_AGENT) != 0; }
__device__ __forceinline__ void set_dead(u32* ctrl) { __hip_atomic_store(ctrl + CW_DEAD, 1u, RLX_AGENT); }
__device__ __forceinline__ void wait_ge(u32* ctrl, int word, u32 want, int slp) {
  for (int spins = 0;; ++spins) {
    asm volatile("" ::: "memory");
    u32 v = __builtin_amdgcn_readfirstlane(__hip_atomic_load(ctrl + word, RLX_AGENT));
    if (v >= want) break;
    if (spins > SPIN_CAP) { set_dead(ctrl); break; }
    if ((spins & 63) == 63 && __builtin_amdgcn_readfirstlane((int)is_dead(ctrl))) break;
    sleepn(slp);
  }
}
__device__ __forceinline__ u32 vote_pol(u32 v) {
  u32 m = (u32)__builtin_amdgcn_readfirstlane((u32)__ballot((v & 1u) != 0));
  const u32 full = (1u << EX_PAGES) - 1u;
  m = __builtin_popcount((m ^ 0xAAAAAAAAu) & full) <= EX_PAGES / 2 ? 0xAAAAAAAAu : 0x55555555u;
  return m;
}
__device__ __forceinline__ u32 exoff(u32 pol, u32 page, u32 pair) { return (page << 21) + (pair << 14) + (((pol >> page) & 1u) << 13); }
__device__ __forceinline__ u32 offB(u32 pol, int s, int h) { u32 pr = 32 * s + h; return exoff(pol, EXP_B + (pr >> 7), pr & 127); }
__device__ __forceinline__ u32 max4(u32x4 v) { return max(max(v[0], v[1]), max(v[2], v[3])); }
#define POLL_FAIL(spins) ((++(spins) > SPIN_CAP) ? (set_dead(ctrl), true) : (((spins) & 255) == 255 && __builtin_amdgcn_readfirstlane((int)is_dead(ctrl))))
#define REC_ASM \
  "s_mov_b64 exec, -1\n" \
  "v_mbcnt_lo_u32_b32 v0, -1, 0\n" \
  "v_mbcnt_hi_u32_b32 v0, -1, v0\n" \
  "s_lshr_b32 s70, %6, 2\n" \
  "s_and_b32 s71, %6, 3\n" \
  "v_lshlrev_b32 v1, 4, v0\n" \
  "v_and_b32 v27, 7, v0\n" \
  "v_lshrrev_b32 v28, 3, v0\n" \
  "v_lshlrev_b32 v34, 11, v28\n" \
  "v_lshl_add_u32 v34, v27, 4, v34\n" \
  "v_lshlrev_b32 v32, 13, v28\n" \
  "v_lshl_add_u32 v32, v27, 4, v32\n" \
  "v_and_b32 v27, 32, v0\n" \
  "v_xor_b32 v32, v32, v27\n" \
  "v_xor_b32 v33, 64, v32\n" \
  "s_mul_i32 s88, %7, 40960\n" \
  "s_add_u32 s88, s88, %9\n" \
  "s_lshl_b32 s60, %7, 21\n" \
  "s_mov_b64 s[72:73], %0\n" \
  "s_and_b32 s73, s73, 0xffff\n" \
  "s_mov_b32 s74, 8388608\n" \
  "s_mov_b32 s75, 0x00020000\n" \
  "s_mov_b64 s[76:77], %1\n" \
  "s_and_b32 s77, s77, 0xffff\n" \
  "s_mov_b32 s78, 8388608\n" \
  "s_mov_b32 s79, 0x00020000\n" \
  "s_mov_b64 s[80:81], %2\n" \
  "s_and_b32 s81, s81, 0xffff\n" \
  "s_mov_b32 s82, 8388608\n" \
  "s_mov_b32 s83, 0x00020000\n" \
  "s_mov_b64 s[84:85], %3\n" \
  "s_and_b32 s85, s85, 0xffff\n" \
  "s_mov_b32 s86, 262144\n" \
  "s_mov_b32 s87, 0x00020000\n" \
  "s_mov_b64 s[90:91], %11\n" \
  "v_and_b32 v27, 1, v0\n" \
  "v_lshlrev_b32 v27, 11, v27\n" \
  "v_and_b32 v28, 4, v0\n" \
  "v_lshl_add_u32 v27, v28, 10, v27\n" \
  "v_and_b32 v28, 2, v0\n" \
  "v_lshl_add_u32 v27, v28, 3, v27\n" \
  "global_load_dwordx4 v[64:67], v27, s[90:91]\n" \
  "s_lshr_b32 s59, %10, 1\n" \
  "s_add_u32 s59, s59, %7\n" \
  "s_add_u32 s59, s59, 0\n" \
  "s_and_b32 s59, s59, 3\n" \
  "s_lshl_b32 s59, s59, 19\n" \
  "s_lshl_b32 s60, %7, 21\n" \
  "s_add_u32 s59, s59, s60\n" \
  "s_add_u32 s59, s59, 0\n" \
  "s_add_u32 m0, s88, 0\n" \
  "s_nop 0\n" \
  "buffer_load_dwordx4 v32, s[72:75], s59 offen lds\n" \
  "s_add_u32 m0, s88, 4096\n" \
  "s_nop 0\n" \
  "buffer_load_dwordx4 v32, s[76:79], s59 offen lds\n" \
  "s_add_u32 s59, s59, 65536\n" \
  "s_add_u32 m0, s88, 1024\n" \
  "s_nop 0\n" \
  "buffer_load_dwordx4 v33, s[72:75], s59 offen lds\n" \
  "s_add_u32 m0, s88, 5120\n" \
  "s_nop 0\n" \
  "buffer_load_dwordx4 v33, s[76:79], s59 offen lds\n" \
  "s_add_u32 s59, s59, 65536\n" \
  "s_add_u32 m0, s88, 2048\n" \
  "s_nop 0\n" \
  "buffer_load_dwordx4 v32, s[72:75], s59 offen lds\n" \
  "s_add_u32 m0, s88, 6144\n" \
  "s_nop 0\n" \
  "buffer_load_dwordx4 v32, s[76:79], s59 offen lds\n" \
  "s_add_u32 s59, s59, 65536\n" \
  "s_add_u32 m0, s88, 3072\n" \
  "s_nop 0\n" \
  "buffer_load_dwordx4 v33, s[72:75], s59 offen lds\n" \
  "s_add_u32 m0, s88, 7168\n" \
  "s_nop 0\n" \
  "buffer_load_dwordx4 v33, s[76:79], s59 offen lds\n" \
  "s_lshr_b32 s59, %10, 1\n" \
  "s_add_u32 s59, s59, %7\n" \
  "s_add_u32 s59, s59, 0\n" \
  "s_and_b32 s59, s59, 3\n" \
  "s_lshl_b32 s59, s59, 19\n" \
  "s_lshl_b32 s60, %7, 21\n" \
  "s_add_u32 s59, s59, s60\n" \
  "s_add_u32 s59, s59, 262144\n" \
  "s_add_u32 m0, s88, 8192\n" \
  "s_nop 0\n" \
  "buffer_load_dwordx4 v32, s[72:75], s59 offen lds\n" \
  "s_add_u32 m0, s88, 12288\n" \
  "s_nop 0\n" \
  "buffer_load_dwordx4 v32, s[76:79], s59 offen lds\n" \
  "s_add_u32 s59, s59, 65536\n" \
  "s_add_u32 m0, s88, 9216\n" \
  "s_nop 0\n" \
  "buffer_load_dwordx4 v33, s[72:75], s59 offen lds\n" \
  "s_add_u32 m0, s88, 13312\n" \
  "s_nop 0\n" \
  "buffer_load_dwordx4 v33, s[76:79], s59 offen lds\n" \
  "s_add_u32 s59, s59, 65536\n" \
  "s_add_u32 m0, s88, 10240\n" \
  "s_nop 0\n" \
  "buffer_load_dwordx4 v32, s[72:75], s59 offen lds\n" \
  "s_add_u32 m0, s88, 14336\n" \
  "s_nop 0\n" \
  "buffer_load_dwordx4 v32, s[76:79], s59 offen lds\n" \
  "s_add_u32 s59, s59, 65536\n" \
  "s_add_u32 m0, s88, 11264\n" \
  "s_nop 0\n" \
  "buffer_load_dwordx4 v33, s[72:75], s59 offen lds\n" \
  "s_add_u32 m0, s88, 15360\n" \
  "s_nop 0\n" \
  "buffer_load_dwordx4 v33, s[76:79], s59 offen lds\n" \
  "s_lshr_b32 s59, %10, 1\n" \
  "s_add_u32 s59, s59, %7\n" \
  "s_add_u32 s59, s59, 1\n" \
  "s_and_b32 s59, s59, 3\n" \
  "s_lshl_b32 s59, s59, 19\n" \
  "s_lshl_b32 s60, %7, 21\n" \
  "s_add_u32 s59, s59, s60\n" \
  "s_add_u32 s59, s59, 0\n" \
  "s_add_u32 m0, s88, 16384\n" \
  "s_nop 0\n" \
  "buffer_load_dwordx4 v32, s[72:75], s59 offen lds\n" \
  "s_add_u32 m0, s88, 20480\n" \
  "s_nop 0\n" \
  "buffer_load_dwordx4 v32, s[76:79], s59 offen lds\n" \
  "s_add_u32 s59, s59, 65536\n" \
  "s_add_u32 m0, s88, 17408\n" \
  "s_nop 0\n" \
  "buffer_load_dwordx4 v33, s[72:75], s59 offen lds\n" \
  "s_add_u32 m0, s88, 21504\n" \
  "s_nop 0\n" \
  "buffer_load_dwordx4 v33, s[76:79], s59 offen lds\n" \
  "s_add_u32 s59, s59, 65536\n" \
  "s_add_u32 m0, s88, 18432\n" \
  "s_nop 0\n" \
  "buffer_load_dwordx4 v32, s[72:75], s59 offen lds\n" \
  "s_add_u32 m0, s88, 22528\n" \
  "s_nop 0\n" \
  "buffer_load_dwordx4 v32, s[76:79], s59 offen lds\n" \
  "s_add_u32 s59, s59, 65536\n" \
  "s_add_u32 m0, s88, 19456\n" \
  "s_nop 0\n" \
  "buffer_load_dwordx4 v33, s[72:75], s59 offen lds\n" \
  "s_add_u32 m0, s88, 23552\n" \
  "s_nop 0\n" \
  "buffer_load_dwordx4 v33, s[76:79], s59 offen lds\n" \
  "v_and_b32 v36, 15, v0\n" \
  "v_lshrrev_b32 v37, 4, v0\n" \
  "v_lshrrev_b32 v38, 2, v36\n" \
  "v_and_b32 v39, 3, v0\n" \
  "v_lshrrev_b32 v46, 1, v37\n" \
  "v_lshlrev_b32 v46, 4, v46\n" \
  "v_and_b32 v27, 1, v37\n" \
  "v_lshl_add_u32 v46, v27, 2, v46\n" \
  "v_and_b32 v28, 1, v38\n" \
  "v_add_u32 v46, v46, v28\n" \
  "v_lshrrev_b32 v28, 1, v38\n" \
  "v_lshl_add_u32 v46, v28, 3, v46\n" \
  "v_lshl_add_u32 v27, v28, 1, v27\n" \
  "v_lshlrev_b32 v27, 5, v27\n" \
  "s_and_b32 s59, s71, 1\n" \
  "s_lshl_b32 s59, s59, 6\n" \
  "v_xor_b32 v27, s59, v27\n" \
  "v_lshl_add_u32 v27, v46, 7, v27\n" \
  "v_lshl_add_u32 v28, v39, 3, v27\n" \
  "v_add_u32 v42, s88, v28\n" \
  "v_xor_b32 v28, 32, v28\n" \
  "v_add_u32 v43, s88, v28\n" \
  "v_and_b32 v28, 1, v39\n" \
  "v_lshl_add_u32 v27, v28, 3, v27\n" \
  "v_lshrrev_b32 v28, 1, v39\n" \
  "v_lshl_add_u32 v27, v28, 12, v27\n" \
  "s_add_u32 s59, %7, 0\n" \
  "s_and_b32 s59, s59, 3\n" \
  "s_lshl_b32 s59, s59, 4\n" \
  "v_xor_b32 v28, s59, v27\n" \
  "v_add_u32 v52, s88, v28\n" \
  "s_add_u32 s59, %7, 1\n" \
  "s_and_b32 s59, s59, 3\n" \
  "s_lshl_b32 s59, s59, 4\n" \
  "v_xor_b32 v28, s59, v27\n" \
  "v_add_u32 v53, s88, v28\n" \
  "s_add_u32 s59, %7, 2\n" \
  "s_and_b32 s59, s59, 3\n" \
  "s_lshl_b32 s59, s59, 4\n" \
  "v_xor_b32 v28, s59, v27\n" \
  "v_add_u32 v54, s88, v28\n" \
  "s_add_u32 s59, %7, 3\n" \
  "s_and_b32 s59, s59, 3\n" \
  "s_lshl_b32 s59, s59, 4\n" \
  "v_xor_b32 v28, s59, v27\n" \
  "v_add_u32 v55, s88, v28\n" \
  "v_lshlrev_b32 v49, 7, v46\n" \
  "v_lshl_add_u32 v49, v39, 5, v49\n" \
  "v_add_u32 v49, s88, v49\n" \
  "s_mov_b64 s[44:45], %5\n" \
  "s_and_b32 s45, s45, 0xffff\n" \
  "s_mov_b32 s46, " STR(CTRL_BYTES) "\n" \
  "s_mov_b32 s47, 0x00020000\n" \
  "s_mov_b64 s[36:37], %4\n" \
  "s_and_b32 s37, s37, 0xffff\n" \
  "s_mov_b32 s38, " STR(EX_BYTES32) "\n" \
  "s_mov_b32 s39, 0x00020000\n" \
  "s_mov_b64 s[40:41], %4\n" \
  "s_add_u32 s40, s40, " STR(WS_K_OFF32) "\n" \
  "s_addc_u32 s41, s41, 0\n" \
  "s_and_b32 s41, s41, 0xffff\n" \
  "s_mov_b32 s42, 3*" STR(QKV_SZ32) "\n" \
  "s_mov_b32 s43, 0x00020000\n" \
  "v_lshrrev_b32 v50, 5, v0\n" \
  "v_and_b32 v51, 31, v0\n" \
  "v_mov_b32 v2, 0x4444\n" \
  "v_mov_b32 v27, 0xeeee\n" \
  "v_cmp_lt_u32 vcc, 1, v39\n" \
  "s_mul_i32 s59, %7, 1280\n" \
  "s_add_u32 s59, s59, %9\n" \
  "v_cndmask_b32 v2, v2, v27, vcc\n" \
  "v_cndmask_b32 v29, 0, -1, vcc\n" \
  "v_cndmask_b32 v4, -1, 0, vcc\n" \
  "v_mul_u32_u24 v27, 80, v37\n" \
  "v_add_lshl_u32 v27, v27, v36, 2\n" \
  "v_add_u32 v5, s59, v27\n" \
  "v_lshlrev_b32 v27, 3, v0\n" \
  "s_add_u32 s60, %9, 8192\n" \
  "v_add_u32 v27, s60, v27\n" \
  "s_mul_i32 s59, %7, 1536\n" \
  "v_add_u32 v6, s59, v27\n" \
  "s_add_u32 s59, %7, 1\n" \
  "s_and_b32 s59, s59, 3\n" \
  "s_mul_i32 s59, s59, 1536\n" \
  "v_add_u32 v3, s59, v27\n" \
  "s_add_u32 s59, %7, 2\n" \
  "s_and_b32 s59, s59, 3\n" \
  "s_mul_i32 s59, s59, 1536\n" \
  "v_add_u32 v244, s59, v27\n" \
  "s_add_u32 s59, %7, 3\n" \
  "s_and_b32 s59, s59, 3\n" \
  "s_mul_i32 s59, s59, 1536\n" \
  "v_add_u32 v245, s59, v27\n" \
  "v_and_b32 v27, 1, v37\n" \
  "v_lshlrev_b32 v27, 1, v27\n" \
  "v_and_b32 v28, 1, v38\n" \
  "v_add_u32 v27, v27, v28\n" \
  "v_mul_u32_u24 v27, 160, v27\n" \
  "v_lshrrev_b32 v28, 1, v38\n" \
  "v_lshl_add_u32 v27, v28, 5, v27\n" \
  "v_lshl_add_u32 v27, v39, 3, v27\n" \
  "s_and_b32 s59, %7, 1\n" \
  "s_mul_i32 s59, s59, 640\n" \
  "s_lshr_b32 s60, %7, 1\n" \
  "s_lshl_b32 s60, s60, 6\n" \
  "s_add_u32 s59, s59, s60\n" \
  "s_add_u32 s59, s59, %9\n" \
  "v_add_u32 v7, s59, v27\n" \
  "v_mov_b32 v24, %9\n" \
  "s_lshl_b32 s94, s70, 14\n" \
  "v_add_u32 v17, s94, v1\n" \
  "v_add_u32 v17, 4096, v17\n" \
  "s_lshl_b32 s59, s71, 2\n" \
  "s_add_u32 s59, s59, %7\n" \
  "s_lshl_b32 s59, s59, 8\n" \
  "s_add_u32 s59, s59, s94\n" \
  "v_and_b32 v8, 0x38, v0\n" \
  "v_lshlrev_b32 v8, 2, v8\n" \
  "v_and_b32 v27, 1, v0\n" \
  "v_lshl_add_u32 v8, v27, 4, v8\n" \
  "v_and_b32 v27, 6, v0\n" \
  "v_lshl_add_u32 v8, v27, 1, v8\n" \
  "v_add_u32 v8, s59, v8\n" \
  "v_and_b32 v27, 7, v0\n" \
  "v_lshlrev_b32 v27, 4, v27\n" \
  "v_lshrrev_b32 v28, 3, v0\n" \
  "v_add_u32 v15, s59, v1\n" \
  "s_xor_b32 s59, s71, 2\n" \
  "s_lshl_b32 s59, s59, 2\n" \
  "s_add_u32 s59, s59, %7\n" \
  "s_lshl_b32 s59, s59, 7\n" \
  "s_add_u32 s59, s59, 4096\n" \
  "s_add_u32 s59, s59, s94\n" \
  "v_lshlrev_b32 v9, 2, v51\n" \
  "v_add_u32 v9, s59, v9\n" \
  "v_lshl_add_u32 v9, v50, 31, v9\n" \
  "v_add_u32 v25, s59, v27\n" \
  "v_cmp_eq_u32 vcc, 2, v28\n" \
  "s_lshr_b32 s59, %10, 5\n" \
  "s_lshl_b32 s59, s59, 17\n" \
  "s_add_u32 s59, s59, 6144\n" \
  "s_and_b32 s60, %10, 31\n" \
  "s_or_b32 s60, s60, %7\n" \
  "v_cndmask_b32 v15, v15, v25, vcc\n" \
  "s_cmp_eq_u32 s60, 1\n" \
  "s_cselect_b32 s60, 0, 0x80000000\n" \
  "s_and_b32 s61, %10, 1\n" \
  "s_lshl_b32 s61, s61, 31\n" \
  "s_or_b32 s60, s60, s61\n" \
  "s_or_b32 s59, s59, s60\n" \
  "v_mov_b32 v25, 0x80000000\n" \
  "v_cmp_eq_u32 vcc, 3, v28\n" \
  "v_lshl_add_u32 v14, v0, 3, s59\n" \
  "s_nop 0\n" \
  "v_cndmask_b32 v15, v15, v25, vcc\n" \
  "v_lshl_add_u32 v25, v51, 4, s59\n" \
  "v_cmp_eq_u32 vcc, 1, v50\n" \
  "s_lshr_b32 s59, %10, 5\n" \
  "s_lshl_b32 s59, s59, 7\n" \
  "s_add_u32 s59, s59, 6144\n" \
  "v_cndmask_b32 v15, v15, v25, vcc\n" \
  "v_lshl_add_u32 v13, v39, 17, s59\n" \
  "s_lshl_b32 s59, s71, 2\n" \
  "s_add_u32 s59, s59, %7\n" \
  "s_lshl_b32 s59, s59, 16\n" \
  "v_lshlrev_b32 v10, 14, v37\n" \
  "v_and_b32 v25, 1, v28\n" \
  "v_lshl_add_u32 v10, v25, 12, v10\n" \
  "v_add3_u32 v10, v10, v27, s59\n" \
  "s_lshl_b32 s60, %7, 4\n" \
  "s_add_u32 s60, s60, s70\n" \
  "v_lshl_add_u32 v25, v28, 1, s60\n" \
  "v_and_b32 v25, 31, v25\n" \
  "v_lshl_add_u32 v10, v25, 7, v10\n" \
  "s_lshr_b32 s59, %6, 1\n" \
  "s_lshl_b32 s59, s59, 14\n" \
  "s_and_b32 s60, %6, 1\n" \
  "s_lshl_b32 s60, s60, 12\n" \
  "s_add_u32 s59, s59, s60\n" \
  "s_lshl_b32 s60, %7, 10\n" \
  "s_add_u32 s59, s59, s60\n" \
  "v_add_u32 v11, s59, v1\n" \
  "s_lshr_b32 s59, %6, 6\n" \
  "s_lshl_b32 s59, s59, 14\n" \
  "s_and_b32 s60, %6, 63\n" \
  "s_lshl_b32 s60, s60, 7\n" \
  "s_add_u32 s59, s59, s60\n" \
  "v_add_u32 v12, s59, v27\n" \
  "v_lshrrev_b32 v25, 3, v36\n" \
  "v_lshl_add_u32 v25, v50, 1, v25\n" \
  "v_add_u32 v25, " STR(EXP_A) ", v25\n" \
  "v_lshl_add_u32 v12, v25, 21, v12\n" \
  "v_lshrrev_b32 v25, v25, %8\n" \
  "v_and_b32 v25, 1, v25\n" \
  "v_lshl_add_u32 v12, v25, 13, v12\n" \
  "v_lshl_add_u32 v12, v37, 31, v12\n" \
  "v_and_b32 v27, 1, v37\n" \
  "v_lshlrev_b32 v27, 5, v27\n" \
  "v_xor_b32 v27, v1, v27\n" \
  "s_add_u32 s59, %9, 32768\n" \
  "v_add_u32 v19, s59, v27\n" \
  "s_and_b32 s60, %7, 1\n" \
  "s_lshl_b32 s60, s60, 13\n" \
  "v_add_u32 v18, s60, v19\n" \
  "v_and_b32 v20, 15, v46\n" \
  "v_lshlrev_b32 v20, 4, v20\n" \
  "v_lshl_add_u32 v20, v50, 10, v20\n" \
  "v_lshl_add_u32 v20, v39, 8, v20\n" \
  "v_and_b32 v27, 1, v39\n" \
  "v_lshlrev_b32 v27, 5, v27\n" \
  "v_xor_b32 v20, v20, v27\n" \
  "v_xor_b32 v21, 32, v20\n" \
  "s_add_u32 s59, s59, 8192\n" \
  "v_add_u32 v20, s59, v20\n" \
  "v_add_u32 v21, s59, v21\n" \
  "v_and_b32 v22, 1, v0\n" \
  "v_lshlrev_b32 v22, 7, v22\n" \
  "s_and_b32 s59, %7, 1\n" \
  "s_xor_b32 s59, s59, s70\n" \
  "s_lshl_b32 s59, s59, 8\n" \
  "s_add_u32 s59, s59, " STR(CW_GPROG) "*4\n" \
  "v_add_u32 v22, s59, v22\n" \
  "s_lshr_b32 s59, %10, 1\n" \
  "s_add_u32 s59, s59, %7\n" \
  "s_add_u32 s59, s59, 0\n" \
  "s_and_b32 s59, s59, 3\n" \
  "s_lshl_b32 s59, s59, 10\n" \
  "v_add_u32 v80, s59, v1\n" \
  "v_add_u32 v84, s59, v19\n" \
  "v_mov_b32 v88, -1\n" \
  "s_lshr_b32 s59, %10, 1\n" \
  "s_add_u32 s59, s59, %7\n" \
  "s_add_u32 s59, s59, 1\n" \
  "s_and_b32 s59, s59, 3\n" \
  "s_lshl_b32 s59, s59, 10\n" \
  "v_add_u32 v81, s59, v1\n" \
  "v_add_u32 v85, s59, v19\n" \
  "v_mov_b32 v89, -1\n" \
  "s_lshr_b32 s59, %10, 1\n" \
  "s_add_u32 s59, s59, %7\n" \
  "s_add_u32 s59, s59, 2\n" \
  "s_and_b32 s59, s59, 3\n" \
  "s_lshl_b32 s59, s59, 10\n" \
  "v_add_u32 v82, s59, v1\n" \
  "v_add_u32 v86, s59, v19\n" \
  "v_mov_b32 v90, -1\n" \
  "s_lshr_b32 s59, %10, 1\n" \
  "s_add_u32 s59, s59, %7\n" \
  "s_add_u32 s59, s59, 3\n" \
  "s_and_b32 s59, s59, 3\n" \
  "s_lshl_b32 s59, s59, 10\n" \
  "v_add_u32 v83, s59, v1\n" \
  "v_add_u32 v87, s59, v19\n" \
  "v_mov_b32 v91, -1\n" \
  "v_mov_b32 v248, 0\n" \
  "v_mov_b32 v27, 0x3f80\n" \
  "v_and_b32 v25, 7, v36\n" \
  "v_cmp_eq_u32 vcc, 0, v25\n" \
  "v_mov_b32 v28, 1.0\n" \
  "s_nop 0\n" \
  "v_cndmask_b32 v248, v248, v27, vcc\n" \
  "v_cmp_eq_u32 vcc, 1, v25\n" \
  "s_nop 1\n" \
  "v_cndmask_b32 v248, v248, v28, vcc\n" \
  "v_mov_b32 v249, 0\n" \
  "v_mov_b32 v27, 0x3f80\n" \
  "v_and_b32 v25, 7, v36\n" \
  "v_cmp_eq_u32 vcc, 2, v25\n" \
  "v_mov_b32 v28, 1.0\n" \
  "s_nop 0\n" \
  "v_cndmask_b32 v249, v249, v27, vcc\n" \
  "v_cmp_eq_u32 vcc, 3, v25\n" \
  "s_nop 1\n" \
  "v_cndmask_b32 v249, v249, v28, vcc\n" \
  "v_mov_b32 v250, 0\n" \
  "v_mov_b32 v27, 0x3f80\n" \
  "v_and_b32 v25, 7, v36\n" \
  "v_cmp_eq_u32 vcc, 4, v25\n" \
  "v_mov_b32 v28, 1.0\n" \
  "s_nop 0\n" \
  "v_cndmask_b32 v250, v250, v27, vcc\n" \
  "v_cmp_eq_u32 vcc, 5, v25\n" \
  "s_nop 1\n" \
  "v_cndmask_b32 v250, v250, v28, vcc\n" \
  "v_mov_b32 v251, 0\n" \
  "v_mov_b32 v27, 0x3f80\n" \
  "v_and_b32 v25, 7, v36\n" \
  "v_cmp_eq_u32 vcc, 6, v25\n" \
  "v_mov_b32 v28, 1.0\n" \
  "s_nop 0\n" \
  "v_cndmask_b32 v251, v251, v27, vcc\n" \
  "v_cmp_eq_u32 vcc, 7, v25\n" \
  "s_nop 1\n" \
  "v_cndmask_b32 v251, v251, v28, vcc\n" \
  "v_and_b32 v27, 1, v0\n" \
  "v_cmp_eq_u32 vcc, 1, v27\n" \
  "v_mov_b32 v246, 0x05040100\n" \
  "v_mov_b32 v28, 0x03020706\n" \
  "v_cndmask_b32 v246, v246, v28, vcc\n" \
  "v_mov_b32 v252, 0x3f803f80\n" \
  "v_mov_b32 v253, 0x3f803f80\n" \
  "v_mov_b32 v254, 0x3f803f80\n" \
  "v_mov_b32 v255, 0\n" \
  "v_mov_b32 v30, 0\n" \
  "s_mov_b32 s92, 0x3e0293ee\n" \
  "s_mov_b32 s58, 0\n" \
  "s_mov_b32 s48, 0\n" \
  "s_lshr_b32 s59, %7, 1\n" \
  "s_lshl_b32 s59, s59, 14\n" \
  "s_and_b32 s60, %7, 1\n" \
  "s_lshl_b32 s60, s60, 12\n" \
  "s_add_u32 s93, s59, s60\n" \
  "s_lshr_b32 s60, %10, 5\n" \
  "s_add_u32 s60, s60, " STR(EXP_A) "\n" \
  "s_lshl_b32 s60, s60, 21\n" \
  "s_add_u32 s60, s60, s93\n" \
  "s_mov_b32 s53, 0\n" \
  "s_lshr_b32 s59, s60, 21\n" \
  "s_lshr_b32 s59, %8, s59\n" \
  "s_and_b32 s59, s59, 1\n" \
  "s_lshl_b32 s59, s59, 13\n" \
  "s_or_b32 s93, s60, s59\n" \
  "s_mov_b32 s60, " STR(EXP_C) "<<21\n" \
  "s_lshr_b32 s59, s60, 21\n" \
  "s_lshr_b32 s59, %8, s59\n" \
  "s_and_b32 s59, s59, 1\n" \
  "s_lshl_b32 s59, s59, 13\n" \
  "s_or_b32 s52, s60, s59\n" \
  "s_add_u32 s51, s52, 1<<20\n" \
  "s_and_b32 s59, %7, 1\n" \
  "s_mul_i32 s59, s59, " STR(QKV_SZ32) "\n" \
  "s_lshl_b32 s60, s70, 14\n" \
  "s_add_u32 s59, s59, s60\n" \
  "v_add_u32 v247, s59, v1\n" \
  "v_add_u32 v16, 4096, v247\n" \
  "s_lshr_b32 s60, %7, 1\n" \
  "s_lshr_b32 s59, s60, 2\n" \
  "s_xnor_b32 s59, s59, %8\n" \
  "s_and_b32 s59, s59, 1\n" \
  "s_lshl_b32 s59, s59, 13\n" \
  "s_lshl_b32 s55, s60, 19\n" \
  "s_or_b32 s55, s55, s59\n" \
  "s_waitcnt lgkmcnt(0)\n" \
  "s_waitcnt vmcnt(24)\n" \
  "s_mov_b32 s60, " STR(EXP_B) "<<21\n" \
  "s_lshr_b32 s59, s60, 21\n" \
  "s_lshr_b32 s59, %8, s59\n" \
  "s_and_b32 s59, s59, 1\n" \
  "s_lshl_b32 s59, s59, 13\n" \
  "s_or_b32 s60, s60, s59\n" \
  "buffer_store_dwordx4 v[88:91], v15, s[36:39], s60 offen sc1\n" \
  "s_add_u32 s60, s60, 1<<19\n" \
  "buffer_store_dwordx4 v[88:91], v15, s[36:39], s60 offen sc1\n" \
  "s_add_u32 s60, s60, 1<<19\n" \
  "buffer_store_dwordx4 v[88:91], v15, s[36:39], s60 offen sc1\n" \
  "s_add_u32 s60, s60, 1<<19\n" \
  "buffer_store_dwordx4 v[88:91], v15, s[36:39], s60 offen sc1\n" \
  "s_add_u32 s60, s60, 1<<19\n" \
  "buffer_store_dwordx4 v[88:91], v11, s[36:39], s51 offen sc1\n" \
  "buffer_store_dwordx4 v[88:91], v11, s[36:39], s52 offen sc1\n" \
  "s_add_u32 s59, %7, 1\n" \
  "s_lshl_b32 s59, s59, 15\n" \
  "buffer_store_dwordx4 v[88:91], v12, s[36:39], s59 offen sc1\n" \
  "v_min_u32 v64, -2, v64\n" \
  "v_min_u32 v65, -2, v65\n" \
  "v_min_u32 v66, -2, v66\n" \
  "v_min_u32 v67, -2, v67\n" \
  "s_lshl_b32 s59, %7, 29\n" \
  "v_or_b32 v27, s59, v12\n" \
  "buffer_store_dwordx4 v[64:67], v27, s[36:39], 0 offen sc1\n" \
  "s_lshr_b32 s59, %10, 1\n" \
  "s_add_u32 s59, s59, %7\n" \
  "s_add_u32 s59, s59, 1\n" \
  "s_and_b32 s59, s59, 3\n" \
  "s_lshl_b32 s59, s59, 19\n" \
  "s_lshl_b32 s60, %7, 21\n" \
  "s_add_u32 s59, s59, s60\n" \
  "s_add_u32 s59, s59, 262144\n" \
  "s_add_u32 m0, s88, 24576\n" \
  "s_nop 0\n" \
  "buffer_load_dwordx4 v32, s[72:75], s59 offen lds\n" \
  "s_add_u32 m0, s88, 28672\n" \
  "s_nop 0\n" \
  "buffer_load_dwordx4 v32, s[76:79], s59 offen lds\n" \
  "s_add_u32 s59, s59, 65536\n" \
  "s_add_u32 m0, s88, 25600\n" \
  "s_nop 0\n" \
  "buffer_load_dwordx4 v33, s[72:75], s59 offen lds\n" \
  "s_add_u32 m0, s88, 29696\n" \
  "s_nop 0\n" \
  "buffer_load_dwordx4 v33, s[76:79], s59 offen lds\n" \
  "s_add_u32 s59, s59, 65536\n" \
  "s_add_u32 m0, s88, 26624\n" \
  "s_nop 0\n" \
  "buffer_load_dwordx4 v32, s[72:75], s59 offen lds\n" \
  "s_add_u32 m0, s88, 30720\n" \
  "s_nop 0\n" \
  "buffer_load_dwordx4 v32, s[76:79], s59 offen lds\n" \
  "s_add_u32 s59, s59, 65536\n" \
  "s_add_u32 m0, s88, 27648\n" \
  "s_nop 0\n" \
  "buffer_load_dwordx4 v33, s[72:75], s59 offen lds\n" \
  "s_add_u32 m0, s88, 31744\n" \
  "s_nop 0\n" \
  "buffer_load_dwordx4 v33, s[76:79], s59 offen lds\n" \
  "s_lshr_b32 s59, %10, 1\n" \
  "s_add_u32 s59, s59, %7\n" \
  "s_add_u32 s59, s59, 2\n" \
  "s_and_b32 s59, s59, 3\n" \
  "s_lshl_b32 s59, s59, 19\n" \
  "s_lshl_b32 s60, %7, 21\n" \
  "s_add_u32 s59, s59, s60\n" \
  "s_add_u32 s59, s59, 0\n" \
  "s_add_u32 m0, s88, 32768\n" \
  "s_nop 0\n" \
  "buffer_load_dwordx4 v32, s[72:75], s59 offen lds\n" \
  "s_add_u32 m0, s88, 36864\n" \
  "s_nop 0\n" \
  "buffer_load_dwordx4 v32, s[76:79], s59 offen lds\n" \
  "s_add_u32 s59, s59, 65536\n" \
  "s_add_u32 m0, s88, 33792\n" \
  "s_nop 0\n" \
  "buffer_load_dwordx4 v33, s[72:75], s59 offen lds\n" \
  "s_add_u32 m0, s88, 37888\n" \
  "s_nop 0\n" \
  "buffer_load_dwordx4 v33, s[76:79], s59 offen lds\n" \
  "s_add_u32 s59, s59, 65536\n" \
  "s_add_u32 m0, s88, 34816\n" \
  "s_nop 0\n" \
  "buffer_load_dwordx4 v32, s[72:75], s59 offen lds\n" \
  "s_add_u32 m0, s88, 38912\n" \
  "s_nop 0\n" \
  "buffer_load_dwordx4 v32, s[76:79], s59 offen lds\n" \
  "s_add_u32 s59, s59, 65536\n" \
  "s_add_u32 m0, s88, 35840\n" \
  "s_nop 0\n" \
  "buffer_load_dwordx4 v33, s[72:75], s59 offen lds\n" \
  "s_add_u32 m0, s88, 39936\n" \
  "s_nop 0\n" \
  "buffer_load_dwordx4 v33, s[76:79], s59 offen lds\n" \
  "s_waitcnt vmcnt(40)\n" \
  "ds_read_b64_tr_b16 a[0:1], v52 offset:0\n" \
  "ds_read_b64_tr_b16 a[2:3], v52 offset:256\n" \
  "ds_read_b64_tr_b16 a[8:9], v53 offset:0\n" \
  "ds_read_b64_tr_b16 a[10:11], v53 offset:256\n" \
  "ds_read_b64_tr_b16 a[16:17], v54 offset:0\n" \
  "ds_read_b64_tr_b16 a[18:19], v54 offset:256\n" \
  "ds_read_b64_tr_b16 a[24:25], v55 offset:0\n" \
  "ds_read_b64_tr_b16 a[26:27], v55 offset:256\n" \
  "s_waitcnt lgkmcnt(0)\n" \
  "s_lshr_b32 s59, %10, 1\n" \
  "s_add_u32 s59, s59, %7\n" \
  "s_add_u32 s59, s59, 2\n" \
  "s_and_b32 s59, s59, 3\n" \
  "s_lshl_b32 s59, s59, 19\n" \
  "s_lshl_b32 s60, %7, 21\n" \
  "s_add_u32 s59, s59, s60\n" \
  "s_add_u32 s59, s59, 262144\n" \
  "s_add_u32 m0, s88, 0\n" \
  "s_nop 0\n" \
  "buffer_load_dwordx4 v32, s[72:75], s59 offen lds\n" \
  "s_add_u32 m0, s88, 4096\n" \
  "s_nop 0\n" \
  "buffer_load_dwordx4 v32, s[76:79], s59 offen lds\n" \
  "s_add_u32 s59, s59, 65536\n" \
  "s_add_u32 m0, s88, 1024\n" \
  "s_nop 0\n" \
  "buffer_load_dwordx4 v33, s[72:75], s59 offen lds\n" \
  "s_add_u32 m0, s88, 5120\n" \
  "s_nop 0\n" \
  "buffer_load_dwordx4 v33, s[76:79], s59 offen lds\n" \
  "s_add_u32 s59, s59, 65536\n" \
  "s_add_u32 m0, s88, 2048\n" \
  "s_nop 0\n" \
  "buffer_load_dwordx4 v32, s[72:75], s59 offen lds\n" \
  "s_add_u32 m0, s88, 6144\n" \
  "s_nop 0\n" \
  "buffer_load_dwordx4 v32, s[76:79], s59 offen lds\n" \
  "s_add_u32 s59, s59, 65536\n" \
  "s_add_u32 m0, s88, 3072\n" \
  "s_nop 0\n" \
  "buffer_load_dwordx4 v33, s[72:75], s59 offen lds\n" \
  "s_add_u32 m0, s88, 7168\n" \
  "s_nop 0\n" \
  "buffer_load_dwordx4 v33, s[76:79], s59 offen lds\n" \
  "s_waitcnt vmcnt(40)\n" \
  "ds_read_b64_tr_b16 a[4:5], v52 offset:8192\n" \
  "ds_read_b64_tr_b16 a[6:7], v52 offset:8448\n" \
  "ds_read_b64_tr_b16 a[12:13], v53 offset:8192\n" \
  "ds_read_b64_tr_b16 a[14:15], v53 offset:8448\n" \
  "ds_read_b64_tr_b16 a[20:21], v54 offset:8192\n" \
  "ds_read_b64_tr_b16 a[22:23], v54 offset:8448\n" \
  "ds_read_b64_tr_b16 a[28:29], v55 offset:8192\n" \
  "ds_read_b64_tr_b16 a[30:31], v55 offset:8448\n" \
  "s_waitcnt lgkmcnt(0)\n" \
  "s_lshr_b32 s59, %10, 1\n" \
  "s_add_u32 s59, s59, %7\n" \
  "s_add_u32 s59, s59, 3\n" \
  "s_and_b32 s59, s59, 3\n" \
  "s_lshl_b32 s59, s59, 19\n" \
  "s_lshl_b32 s60, %7, 21\n" \
  "s_add_u32 s59, s59, s60\n" \
  "s_add_u32 s59, s59, 0\n" \
  "s_add_u32 m0, s88, 8192\n" \
  "s_nop 0\n" \
  "buffer_load_dwordx4 v32, s[72:75], s59 offen lds\n" \
  "s_add_u32 m0, s88, 12288\n" \
  "s_nop 0\n" \
  "buffer_load_dwordx4 v32, s[76:79], s59 offen lds\n" \
  "s_add_u32 s59, s59, 65536\n" \
  "s_add_u32 m0, s88, 9216\n" \
  "s_nop 0\n" \
  "buffer_load_dwordx4 v33, s[72:75], s59 offen lds\n" \
  "s_add_u32 m0, s88, 13312\n" \
  "s_nop 0\n" \
  "buffer_load_dwordx4 v33, s[76:79], s59 offen lds\n" \
  "s_add_u32 s59, s59, 65536\n" \
  "s_add_u32 m0, s88, 10240\n" \
  "s_nop 0\n" \
  "buffer_load_dwordx4 v32, s[72:75], s59 offen lds\n" \
  "s_add_u32 m0, s88, 14336\n" \
  "s_nop 0\n" \
  "buffer_load_dwordx4 v32, s[76:79], s59 offen lds\n" \
  "s_add_u32 s59, s59, 65536\n" \
  "s_add_u32 m0, s88, 11264\n" \
  "s_nop 0\n" \
  "buffer_load_dwordx4 v33, s[72:75], s59 offen lds\n" \
  "s_add_u32 m0, s88, 15360\n" \
  "s_nop 0\n" \
  "buffer_load_dwordx4 v33, s[76:79], s59 offen lds\n" \
  "s_waitcnt vmcnt(40)\n" \
  "ds_read_b64_tr_b16 a[32:33], v52 offset:16384\n" \
  "ds_read_b64_tr_b16 a[34:35], v52 offset:16640\n" \
  "ds_read_b64_tr_b16 a[40:41], v53 offset:16384\n" \
  "ds_read_b64_tr_b16 a[42:43], v53 offset:16640\n" \
  "ds_read_b64_tr_b16 a[48:49], v54 offset:16384\n" \
  "ds_read_b64_tr_b16 a[50:51], v54 offset:16640\n" \
  "ds_read_b64_tr_b16 a[56:57], v55 offset:16384\n" \
  "ds_read_b64_tr_b16 a[58:59], v55 offset:16640\n" \
  "s_waitcnt lgkmcnt(0)\n" \
  "s_lshr_b32 s59, %10, 1\n" \
  "s_add_u32 s59, s59, %7\n" \
  "s_add_u32 s59, s59, 3\n" \
  "s_and_b32 s59, s59, 3\n" \
  "s_lshl_b32 s59, s59, 19\n" \
  "s_lshl_b32 s60, %7, 21\n" \
  "s_add_u32 s59, s59, s60\n" \
  "s_add_u32 s59, s59, 262144\n" \
  "s_add_u32 m0, s88, 16384\n" \
  "s_nop 0\n" \
  "buffer_load_dwordx4 v32, s[72:75], s59 offen lds\n" \
  "s_add_u32 m0, s88, 20480\n" \
  "s_nop 0\n" \
  "buffer_load_dwordx4 v32, s[76:79], s59 offen lds\n" \
  "s_add_u32 s59, s59, 65536\n" \
  "s_add_u32 m0, s88, 17408\n" \
  "s_nop 0\n" \
  "buffer_load_dwordx4 v33, s[72:75], s59 offen lds\n" \
  "s_add_u32 m0, s88, 21504\n" \
  "s_nop 0\n" \
  "buffer_load_dwordx4 v33, s[76:79], s59 offen lds\n" \
  "s_add_u32 s59, s59, 65536\n" \
  "s_add_u32 m0, s88, 18432\n" \
  "s_nop 0\n" \
  "buffer_load_dwordx4 v32, s[72:75], s59 offen lds\n" \
  "s_add_u32 m0, s88, 22528\n" \
  "s_nop 0\n" \
  "buffer_load_dwordx4 v32, s[76:79], s59 offen lds\n" \
  "s_add_u32 s59, s59, 65536\n" \
  "s_add_u32 m0, s88, 19456\n" \
  "s_nop 0\n" \
  "buffer_load_dwordx4 v33, s[72:75], s59 offen lds\n" \
  "s_add_u32 m0, s88, 23552\n" \
  "s_nop 0\n" \
  "buffer_load_dwordx4 v33, s[76:79], s59 offen lds\n" \
  "s_waitcnt vmcnt(32)\n" \
  "ds_read_b64_tr_b16 a[36:37], v52 offset:24576\n" \
  "ds_read_b64_tr_b16 a[38:39], v52 offset:24832\n" \
  "ds_read_b64_tr_b16 a[44:45], v53 offset:24576\n" \
  "ds_read_b64_tr_b16 a[46:47], v53 offset:24832\n" \
  "ds_read_b64_tr_b16 a[52:53], v54 offset:24576\n" \
  "ds_read_b64_tr_b16 a[54:55], v54 offset:24832\n" \
  "ds_read_b64_tr_b16 a[60:61], v55 offset:24576\n" \
  "ds_read_b64_tr_b16 a[62:63], v55 offset:24832\n" \
  "s_barrier\n" \
  "v_mov_b32 v27, " STR(CW_INIT) "*4\n" \
  "v_mov_b32 v28, 1\n" \
  "v_or_b32 v25, %7, v0\n" \
  "v_lshl_add_u32 v27, v25, 24, v27\n" \
  "buffer_atomic_add v28, v27, s[44:47], 0 offen\n" \
  "s_waitcnt lgkmcnt(0)\n" \
  "s_lshr_b32 s59, %10, 1\n" \
  "s_add_u32 s59, s59, %7\n" \
  "s_add_u32 s59, s59, 0\n" \
  "s_and_b32 s59, s59, 3\n" \
  "s_lshl_b32 s59, s59, 19\n" \
  "s_lshl_b32 s60, %7, 21\n" \
  "s_add_u32 s59, s59, s60\n" \
  "s_add_u32 s59, s59, 0\n" \
  "s_add_u32 m0, s88, 24576\n" \
  "s_nop 0\n" \
  "buffer_load_dwordx4 v32, s[80:83], s59 offen lds\n" \
  "s_add_u32 s59, s59, 65536\n" \
  "s_add_u32 m0, s88, 25600\n" \
  "s_nop 0\n" \
  "buffer_load_dwordx4 v33, s[80:83], s59 offen lds\n" \
  "s_add_u32 s59, s59, 65536\n" \
  "s_add_u32 m0, s88, 26624\n" \
  "s_nop 0\n" \
  "buffer_load_dwordx4 v32, s[80:83], s59 offen lds\n" \
  "s_add_u32 s59, s59, 65536\n" \
  "s_add_u32 m0, s88, 27648\n" \
  "s_nop 0\n" \
  "buffer_load_dwordx4 v33, s[80:83], s59 offen lds\n" \
  "s_add_u32 s59, s59, 65536\n" \
  "s_add_u32 m0, s88, 28672\n" \
  "s_nop 0\n" \
  "buffer_load_dwordx4 v32, s[80:83], s59 offen lds\n" \
  "s_add_u32 s59, s59, 65536\n" \
  "s_add_u32 m0, s88, 29696\n" \
  "s_nop 0\n" \
  "buffer_load_dwordx4 v33, s[80:83], s59 offen lds\n" \
  "s_add_u32 s59, s59, 65536\n" \
  "s_add_u32 m0, s88, 30720\n" \
  "s_nop 0\n" \
  "buffer_load_dwordx4 v32, s[80:83], s59 offen lds\n" \
  "s_add_u32 s59, s59, 65536\n" \
  "s_add_u32 m0, s88, 31744\n" \
  "s_nop 0\n" \
  "buffer_load_dwordx4 v33, s[80:83], s59 offen lds\n" \
  "s_waitcnt vmcnt(33)\n" \
  "ds_read_b64_tr_b16 a[64:65], v52 offset:32768\n" \
  "ds_read_b64_tr_b16 a[66:67], v52 offset:33024\n" \
  "ds_read_b64_tr_b16 a[72:73], v53 offset:32768\n" \
  "ds_read_b64_tr_b16 a[74:75], v53 offset:33024\n" \
  "ds_read_b64_tr_b16 a[80:81], v54 offset:32768\n" \
  "ds_read_b64_tr_b16 a[82:83], v54 offset:33024\n" \
  "ds_read_b64_tr_b16 a[88:89], v55 offset:32768\n" \
  "ds_read_b64_tr_b16 a[90:91], v55 offset:33024\n" \
  "s_waitcnt lgkmcnt(0)\n" \
  "s_lshr_b32 s59, %10, 1\n" \
  "s_add_u32 s59, s59, %7\n" \
  "s_add_u32 s59, s59, 1\n" \
  "s_and_b32 s59, s59, 3\n" \
  "s_lshl_b32 s59, s59, 19\n" \
  "s_lshl_b32 s60, %7, 21\n" \
  "s_add_u32 s59, s59, s60\n" \
  "s_add_u32 s59, s59, 0\n" \
  "s_add_u32 m0, s88, 32768\n" \
  "s_nop 0\n" \
  "buffer_load_dwordx4 v32, s[80:83], s59 offen lds\n" \
  "s_add_u32 s59, s59, 65536\n" \
  "s_add_u32 m0, s88, 33792\n" \
  "s_nop 0\n" \
  "buffer_load_dwordx4 v33, s[80:83], s59 offen lds\n" \
  "s_add_u32 s59, s59, 65536\n" \
  "s_add_u32 m0, s88, 34816\n" \
  "s_nop 0\n" \
  "buffer_load_dwordx4 v32, s[80:83], s59 offen lds\n" \
  "s_add_u32 s59, s59, 65536\n" \
  "s_add_u32 m0, s88, 35840\n" \
  "s_nop 0\n" \
  "buffer_load_dwordx4 v33, s[80:83], s59 offen lds\n" \
  "s_add_u32 s59, s59, 65536\n" \
  "s_add_u32 m0, s88, 36864\n" \
  "s_nop 0\n" \
  "buffer_load_dwordx4 v32, s[80:83], s59 offen lds\n" \
  "s_add_u32 s59, s59, 65536\n" \
  "s_add_u32 m0, s88, 37888\n" \
  "s_nop 0\n" \
  "buffer_load_dwordx4 v33, s[80:83], s59 offen lds\n" \
  "s_add_u32 s59, s59, 65536\n" \
  "s_add_u32 m0, s88, 38912\n" \
  "s_nop 0\n" \
  "buffer_load_dwordx4 v32, s[80:83], s59 offen lds\n" \
  "s_add_u32 s59, s59, 65536\n" \
  "s_add_u32 m0, s88, 39936\n" \
  "s_nop 0\n" \
  "buffer_load_dwordx4 v33, s[80:83], s59 offen lds\n" \
  "s_waitcnt vmcnt(33)\n" \
  "ds_read_b64_tr_b16 a[68:69], v52 offset:0\n" \
  "ds_read_b64_tr_b16 a[70:71], v52 offset:256\n" \
  "ds_read_b64_tr_b16 a[76:77], v53 offset:0\n" \
  "ds_read_b64_tr_b16 a[78:79], v53 offset:256\n" \
  "ds_read_b64_tr_b16 a[84:85], v54 offset:0\n" \
  "ds_read_b64_tr_b16 a[86:87], v54 offset:256\n" \
  "ds_read_b64_tr_b16 a[92:93], v55 offset:0\n" \
  "ds_read_b64_tr_b16 a[94:95], v55 offset:256\n" \
  "s_waitcnt lgkmcnt(0)\n" \
  "s_lshr_b32 s59, %10, 1\n" \
  "s_add_u32 s59, s59, %7\n" \
  "s_add_u32 s59, s59, 2\n" \
  "s_and_b32 s59, s59, 3\n" \
  "s_lshl_b32 s59, s59, 19\n" \
  "s_lshl_b32 s60, %7, 21\n" \
  "s_add_u32 s59, s59, s60\n" \
  "s_add_u32 s59, s59, 0\n" \
  "s_add_u32 m0, s88, 0\n" \
  "s_nop 0\n" \
  "buffer_load_dwordx4 v32, s[80:83], s59 offen lds\n" \
  "s_add_u32 s59, s59, 65536\n" \
  "s_add_u32 m0, s88, 1024\n" \
  "s_nop 0\n" \
  "buffer_load_dwordx4 v33, s[80:83], s59 offen lds\n" \
  "s_add_u32 s59, s59, 65536\n" \
  "s_add_u32 m0, s88, 2048\n" \
  "s_nop 0\n" \
  "buffer_load_dwordx4 v32, s[80:83], s59 offen lds\n" \
  "s_add_u32 s59, s59, 65536\n" \
  "s_add_u32 m0, s88, 3072\n" \
  "s_nop 0\n" \
  "buffer_load_dwordx4 v33, s[80:83], s59 offen lds\n" \
  "s_add_u32 s59, s59, 65536\n" \
  "s_add_u32 m0, s88, 4096\n" \
  "s_nop 0\n" \
  "buffer_load_dwordx4 v32, s[80:83], s59 offen lds\n" \
  "s_add_u32 s59, s59, 65536\n" \
  "s_add_u32 m0, s88, 5120\n" \
  "s_nop 0\n" \
  "buffer_load_dwordx4 v33, s[80:83], s59 offen lds\n" \
  "s_add_u32 s59, s59, 65536\n" \
  "s_add_u32 m0, s88, 6144\n" \
  "s_nop 0\n" \
  "buffer_load_dwordx4 v32, s[80:83], s59 offen lds\n" \
  "s_add_u32 s59, s59, 65536\n" \
  "s_add_u32 m0, s88, 7168\n" \
  "s_nop 0\n" \
  "buffer_load_dwordx4 v33, s[80:83], s59 offen lds\n" \
  "s_waitcnt vmcnt(33)\n" \
  "ds_read_b64_tr_b16 a[96:97], v52 offset:8192\n" \
  "ds_read_b64_tr_b16 a[98:99], v52 offset:8448\n" \
  "ds_read_b64_tr_b16 a[104:105], v53 offset:8192\n" \
  "ds_read_b64_tr_b16 a[106:107], v53 offset:8448\n" \
  "ds_read_b64_tr_b16 a[112:113], v54 offset:8192\n" \
  "ds_read_b64_tr_b16 a[114:115], v54 offset:8448\n" \
  "ds_read_b64_tr_b16 a[120:121], v55 offset:8192\n" \
  "ds_read_b64_tr_b16 a[122:123], v55 offset:8448\n" \
  "s_waitcnt lgkmcnt(0)\n" \
  "s_lshr_b32 s59, %10, 1\n" \
  "s_add_u32 s59, s59, %7\n" \
  "s_add_u32 s59, s59, 3\n" \
  "s_and_b32 s59, s59, 3\n" \
  "s_lshl_b32 s59, s59, 19\n" \
  "s_lshl_b32 s60, %7, 21\n" \
  "s_add_u32 s59, s59, s60\n" \
  "s_add_u32 s59, s59, 0\n" \
  "s_add_u32 m0, s88, 8192\n" \
  "s_nop 0\n" \
  "buffer_load_dwordx4 v32, s[80:83], s59 offen lds\n" \
  "s_add_u32 s59, s59, 65536\n" \
  "s_add_u32 m0, s88, 9216\n" \
  "s_nop 0\n" \
  "buffer_load_dwordx4 v33, s[80:83], s59 offen lds\n" \
  "s_add_u32 s59, s59, 65536\n" \
  "s_add_u32 m0, s88, 10240\n" \
  "s_nop 0\n" \
  "buffer_load_dwordx4 v32, s[80:83], s59 offen lds\n" \
  "s_add_u32 s59, s59, 65536\n" \
  "s_add_u32 m0, s88, 11264\n" \
  "s_nop 0\n" \
  "buffer_load_dwordx4 v33, s[80:83], s59 offen lds\n" \
  "s_add_u32 s59, s59, 65536\n" \
  "s_add_u32 m0, s88, 12288\n" \
  "s_nop 0\n" \
  "buffer_load_dwordx4 v32, s[80:83], s59 offen lds\n" \
  "s_add_u32 s59, s59, 65536\n" \
  "s_add_u32 m0, s88, 13312\n" \
  "s_nop 0\n" \
  "buffer_load_dwordx4 v33, s[80:83], s59 offen lds\n" \
  "s_add_u32 s59, s59, 65536\n" \
  "s_add_u32 m0, s88, 14336\n" \
  "s_nop 0\n" \
  "buffer_load_dwordx4 v32, s[80:83], s59 offen lds\n" \
  "s_add_u32 s59, s59, 65536\n" \
  "s_add_u32 m0, s88, 15360\n" \
  "s_nop 0\n" \
  "buffer_load_dwordx4 v33, s[80:83], s59 offen lds\n" \
  "s_waitcnt vmcnt(33)\n" \
  "ds_read_b64_tr_b16 a[100:101], v52 offset:16384\n" \
  "ds_read_b64_tr_b16 a[102:103], v52 offset:16640\n" \
  "ds_read_b64_tr_b16 a[108:109], v53 offset:16384\n" \
  "ds_read_b64_tr_b16 a[110:111], v53 offset:16640\n" \
  "ds_read_b64_tr_b16 a[116:117], v54 offset:16384\n" \
  "ds_read_b64_tr_b16 a[118:119], v54 offset:16640\n" \
  "ds_read_b64_tr_b16 a[124:125], v55 offset:16384\n" \
  "ds_read_b64_tr_b16 a[126:127], v55 offset:16640\n" \
  "s_waitcnt lgkmcnt(0)\n" \
  "s_lshl_b32 s59, %7, 7\n" \
  "s_add_u32 s59, s59, 0\n" \
  "s_add_u32 m0, s88, 16384\n" \
  "s_nop 0\n" \
  "buffer_load_dwordx4 v34, s[84:87], s59 offen lds\n" \
  "s_add_u32 s59, s59, 16384\n" \
  "s_add_u32 m0, s88, 17408\n" \
  "s_nop 0\n" \
  "buffer_load_dwordx4 v34, s[84:87], s59 offen lds\n" \
  "s_add_u32 s59, s59, 16384\n" \
  "s_add_u32 m0, s88, 18432\n" \
  "s_nop 0\n" \
  "buffer_load_dwordx4 v34, s[84:87], s59 offen lds\n" \
  "s_add_u32 s59, s59, 16384\n" \
  "s_add_u32 m0, s88, 19456\n" \
  "s_nop 0\n" \
  "buffer_load_dwordx4 v34, s[84:87], s59 offen lds\n" \
  "s_add_u32 s59, s59, 16384\n" \
  "s_add_u32 m0, s88, 20480\n" \
  "s_nop 0\n" \
  "buffer_load_dwordx4 v34, s[84:87], s59 offen lds\n" \
  "s_add_u32 s59, s59, 16384\n" \
  "s_add_u32 m0, s88, 21504\n" \
  "s_nop 0\n" \
  "buffer_load_dwordx4 v34, s[84:87], s59 offen lds\n" \
  "s_add_u32 s59, s59, 16384\n" \
  "s_add_u32 m0, s88, 22528\n" \
  "s_nop 0\n" \
  "buffer_load_dwordx4 v34, s[84:87], s59 offen lds\n" \
  "s_add_u32 s59, s59, 16384\n" \
  "s_add_u32 m0, s88, 23552\n" \
  "s_nop 0\n" \
  "buffer_load_dwordx4 v34, s[84:87], s59 offen lds\n" \
  "s_waitcnt vmcnt(32)\n" \
  "ds_read_b64_tr_b16 a[128:129], v42 offset:24576\n" \
  "ds_read_b64_tr_b16 a[130:131], v42 offset:24832\n" \
  "ds_read_b64_tr_b16 a[132:133], v42 offset:28672\n" \
  "ds_read_b64_tr_b16 a[134:135], v42 offset:28928\n" \
  "ds_read_b64_tr_b16 a[136:137], v43 offset:24576\n" \
  "ds_read_b64_tr_b16 a[138:139], v43 offset:24832\n" \
  "ds_read_b64_tr_b16 a[140:141], v43 offset:28672\n" \
  "ds_read_b64_tr_b16 a[142:143], v43 offset:28928\n" \
  "s_waitcnt lgkmcnt(0)\n" \
  "s_lshl_b32 s59, %7, 7\n" \
  "s_add_u32 s59, s59, 131072\n" \
  "s_add_u32 m0, s88, 24576\n" \
  "s_nop 0\n" \
  "buffer_load_dwordx4 v34, s[84:87], s59 offen lds\n" \
  "s_add_u32 s59, s59, 16384\n" \
  "s_add_u32 m0, s88, 25600\n" \
  "s_nop 0\n" \
  "buffer_load_dwordx4 v34, s[84:87], s59 offen lds\n" \
  "s_add_u32 s59, s59, 16384\n" \
  "s_add_u32 m0, s88, 26624\n" \
  "s_nop 0\n" \
  "buffer_load_dwordx4 v34, s[84:87], s59 offen lds\n" \
  "s_add_u32 s59, s59, 16384\n" \
  "s_add_u32 m0, s88, 27648\n" \
  "s_nop 0\n" \
  "buffer_load_dwordx4 v34, s[84:87], s59 offen lds\n" \
  "s_add_u32 s59, s59, 16384\n" \
  "s_add_u32 m0, s88, 28672\n" \
  "s_nop 0\n" \
  "buffer_load_dwordx4 v34, s[84:87], s59 offen lds\n" \
  "s_add_u32 s59, s59, 16384\n" \
  "s_add_u32 m0, s88, 29696\n" \
  "s_nop 0\n" \
  "buffer_load_dwordx4 v34, s[84:87], s59 offen lds\n" \
  "s_add_u32 s59, s59, 16384\n" \
  "s_add_u32 m0, s88, 30720\n" \
  "s_nop 0\n" \
  "buffer_load_dwordx4 v34, s[84:87], s59 offen lds\n" \
  "s_add_u32 s59, s59, 16384\n" \
  "s_add_u32 m0, s88, 31744\n" \
  "s_nop 0\n" \
  "buffer_load_dwordx4 v34, s[84:87], s59 offen lds\n" \
  "s_memtime s[72:73]\n" \
  "s_memrealtime s[74:75]\n" \
  "s_nop 1\n" \
  "s_memrealtime s[76:77]\n" \
  "s_nop 1\n" \
  "s_memrealtime s[78:79]\n" \
  "s_nop 1\n" \
  "s_memrealtime s[80:81]\n" \
  "s_nop 1\n" \
  "s_memrealtime s[82:83]\n" \
  "s_nop 1\n" \
  "s_memrealtime s[84:85]\n" \
  "s_nop 1\n" \
  "s_memrealtime s[86:87]\n" \
  "s_nop 1\n" \
  "s_memrealtime s[88:89]\n" \
  "s_nop 1\n" \
  "s_memrealtime s[90:91]\n" \
  "s_mov_b32 s59, 0\n" \
  "s_waitcnt lgkmcnt(0)\n" \
  "s_cmp_eq_u32 s76, s74\n" \
  "s_addc_u32 s59, s59, 0\n" \
  "s_cmp_eq_u32 s78, s74\n" \
  "s_addc_u32 s59, s59, 0\n" \
  "s_cmp_eq_u32 s80, s74\n" \
  "s_addc_u32 s59, s59, 0\n" \
  "s_cmp_eq_u32 s82, s74\n" \
  "s_addc_u32 s59, s59, 0\n" \
  "s_cmp_eq_u32 s84, s74\n" \
  "s_addc_u32 s59, s59, 0\n" \
  "s_cmp_eq_u32 s86, s74\n" \
  "s_addc_u32 s59, s59, 0\n" \
  "s_cmp_eq_u32 s88, s74\n" \
  "s_addc_u32 s59, s59, 0\n" \
  "s_cmp_eq_u32 s90, s74\n" \
  "s_addc_u32 s59, s59, 0\n" \
  "s_mul_i32 s59, s59, 12\n" \
  "s_add_u32 s68, s72, s59\n" \
  "s_add_u32 s59, s74, 4\n" \
  "s_mul_i32 s69, s59, 24\n" \
  "s_mov_b32 s62, s69\n" \
  "s_mov_b32 s56, s68\n" \
  "s_waitcnt vmcnt(32)\n" \
  "ds_read_b64_tr_b16 a[144:145], v42 offset:32768\n" \
  "ds_read_b64_tr_b16 a[146:147], v42 offset:33024\n" \
  "ds_read_b64_tr_b16 a[148:149], v42 offset:36864\n" \
  "ds_read_b64_tr_b16 a[150:151], v42 offset:37120\n" \
  "ds_read_b64_tr_b16 a[152:153], v43 offset:32768\n" \
  "ds_read_b64_tr_b16 a[154:155], v43 offset:33024\n" \
  "ds_read_b64_tr_b16 a[156:157], v43 offset:36864\n" \
  "ds_read_b64_tr_b16 a[158:159], v43 offset:37120\n" \
  "s_waitcnt vmcnt(24)\n" \
  "ds_read_b64_tr_b16 a[160:161], v42 offset:0\n" \
  "ds_read_b64_tr_b16 a[162:163], v42 offset:256\n" \
  "ds_read_b64_tr_b16 a[164:165], v42 offset:4096\n" \
  "ds_read_b64_tr_b16 a[166:167], v42 offset:4352\n" \
  "ds_read_b64_tr_b16 a[168:169], v43 offset:0\n" \
  "ds_read_b64_tr_b16 a[170:171], v43 offset:256\n" \
  "ds_read_b64_tr_b16 a[172:173], v43 offset:4096\n" \
  "ds_read_b64_tr_b16 a[174:175], v43 offset:4352\n" \
  "s_waitcnt vmcnt(16)\n" \
  "ds_read_b64_tr_b16 a[176:177], v42 offset:8192\n" \
  "ds_read_b64_tr_b16 a[178:179], v42 offset:8448\n" \
  "ds_read_b64_tr_b16 a[180:181], v42 offset:12288\n" \
  "ds_read_b64_tr_b16 a[182:183], v42 offset:12544\n" \
  "ds_read_b64_tr_b16 a[184:185], v43 offset:8192\n" \
  "ds_read_b64_tr_b16 a[186:187], v43 offset:8448\n" \
  "ds_read_b64_tr_b16 a[188:189], v43 offset:12288\n" \
  "ds_read_b64_tr_b16 a[190:191], v43 offset:12544\n" \
  "s_waitcnt vmcnt(8)\n" \
  "ds_read_b64_tr_b16 a[192:193], v49 offset:16384\n" \
  "ds_read_b64_tr_b16 a[194:195], v49 offset:16640\n" \
  "ds_read_b64_tr_b16 a[196:197], v49 offset:20480\n" \
  "ds_read_b64_tr_b16 a[198:199], v49 offset:20736\n" \
  "ds_read_b64_tr_b16 a[208:209], v49 offset:16400\n" \
  "ds_read_b64_tr_b16 a[210:211], v49 offset:16656\n" \
  "ds_read_b64_tr_b16 a[212:213], v49 offset:20496\n" \
  "ds_read_b64_tr_b16 a[214:215], v49 offset:20752\n" \
  "ds_read_b64_tr_b16 a[224:225], v49 offset:16392\n" \
  "ds_read_b64_tr_b16 a[226:227], v49 offset:16648\n" \
  "ds_read_b64_tr_b16 a[228:229], v49 offset:20488\n" \
  "ds_read_b64_tr_b16 a[230:231], v49 offset:20744\n" \
  "ds_read_b64_tr_b16 a[240:241], v49 offset:16408\n" \
  "ds_read_b64_tr_b16 a[242:243], v49 offset:16664\n" \
  "ds_read_b64_tr_b16 a[244:245], v49 offset:20504\n" \
  "ds_read_b64_tr_b16 a[246:247], v49 offset:20760\n" \
  "s_waitcnt vmcnt(0)\n" \
  "ds_read_b64_tr_b16 a[200:201], v49 offset:24576\n" \
  "ds_read_b64_tr_b16 a[202:203], v49 offset:24832\n" \
  "ds_read_b64_tr_b16 a[204:205], v49 offset:28672\n" \
  "ds_read_b64_tr_b16 a[206:207], v49 offset:28928\n" \
  "ds_read_b64_tr_b16 a[216:217], v49 offset:24592\n" \
  "ds_read_b64_tr_b16 a[218:219], v49 offset:24848\n" \
  "ds_read_b64_tr_b16 a[220:221], v49 offset:28688\n" \
  "ds_read_b64_tr_b16 a[222:223], v49 offset:28944\n" \
  "ds_read_b64_tr_b16 a[232:233], v49 offset:24584\n" \
  "ds_read_b64_tr_b16 a[234:235], v49 offset:24840\n" \
  "ds_read_b64_tr_b16 a[236:237], v49 offset:28680\n" \
  "ds_read_b64_tr_b16 a[238:239], v49 offset:28936\n" \
  "ds_read_b64_tr_b16 a[248:249], v49 offset:24600\n" \
  "ds_read_b64_tr_b16 a[250:251], v49 offset:24856\n" \
  "ds_read_b64_tr_b16 a[252:253], v49 offset:28696\n" \
  "ds_read_b64_tr_b16 a[254:255], v49 offset:28952\n" \
  "s_waitcnt lgkmcnt(0)\n" \
  "s_barrier\n" \
  "s_cmp_gt_u32 %7, 1\n" \
  "s_cbranch_scc1 LI6_%=\n" \
  "LI2a_%=:\n" \
  "buffer_load_dword v23, v22, s[44:47], 0 offen sc1\n" \
  "s_waitcnt vmcnt(0)\n" \
  "v_cmp_gt_u32 vcc, 1, v23\n" \
  "s_cbranch_vccz LI2b_%=\n" \
  "s_sleep 6\n" \
  "s_add_u32 s58, s58, 1\n" \
  "s_cmp_lt_u32 s58, " STR(SPIN_CAP) "\n" \
  "s_cbranch_scc1 LI2a_%=\n" \
  "v_mov_b32 v27, " STR(CW_DEAD) "*4\n" \
  "v_mov_b32 v28, 1\n" \
  "buffer_store_dword v28, v27, s[44:47], 0 offen sc1\n" \
  "s_branch LI2b_%=\n" \
  "LI2b_%=:\n" \
  "buffer_load_dwordx4 v[200:203], v247, s[40:43], s55 offen offset:0 sc1\n" \
  "buffer_load_dwordx4 v[204:207], v247, s[40:43], s55 offen offset:1024 sc1\n" \
  "buffer_load_dwordx4 v[208:211], v247, s[40:43], s55 offen offset:2048 sc1\n" \
  "buffer_load_dwordx4 v[212:215], v247, s[40:43], s55 offen offset:3072 sc1\n" \
  "buffer_load_dwordx4 v[216:219], v16, s[40:43], s55 offen offset:0 sc1\n" \
  "buffer_load_dwordx4 v[220:223], v16, s[40:43], s55 offen offset:1024 sc1\n" \
  "buffer_load_dwordx4 v[224:227], v16, s[40:43], s55 offen offset:2048 sc1\n" \
  "buffer_load_dwordx4 v[228:231], v16, s[40:43], s55 offen offset:3072 sc1\n" \
  "s_waitcnt vmcnt(0)\n" \
  "ds_write_b128 v18, v[200:203] offset:0\n" \
  "ds_write_b128 v18, v[204:207] offset:1024\n" \
  "ds_write_b128 v18, v[208:211] offset:2048\n" \
  "ds_write_b128 v18, v[212:215] offset:3072\n" \
  "ds_write_b128 v18, v[216:219] offset:4096\n" \
  "ds_write_b128 v18, v[220:223] offset:5120\n" \
  "ds_write_b128 v18, v[224:227] offset:6144\n" \
  "ds_write_b128 v18, v[228:231] offset:7168\n" \
  "s_branch LI4_%=\n" \
  "LI6_%=:\n" \
  "s_cmp_eq_u32 %7, 3\n" \
  "s_cbranch_scc1 LI4_%=\n" \
  "v_mov_b32 v25, " STR(CW_INIT) "*4\n" \
  "LI1a_%=:\n" \
  "buffer_load_dword v23, v25, s[44:47], 0 offen sc1\n" \
  "s_waitcnt vmcnt(0)\n" \
  "v_cmp_gt_u32 vcc, 128, v23\n" \
  "s_cbranch_vccz LI1b_%=\n" \
  "s_sleep 24\n" \
  "s_add_u32 s58, s58, 1\n" \
  "s_cmp_lt_u32 s58, " STR(SPIN_CAP) "\n" \
  "s_cbranch_scc1 LI1a_%=\n" \
  "v_mov_b32 v27, " STR(CW_DEAD) "*4\n" \
  "v_mov_b32 v28, 1\n" \
  "buffer_store_dword v28, v27, s[44:47], 0 offen sc1\n" \
  "s_branch LI1b_%=\n" \
  "LI1b_%=:\n" \
  "LI4_%=:\n" \
  "s_waitcnt lgkmcnt(0)\n" \
  "s_barrier\n" \
  "ds_read_b64_tr_b16 v[136:137], v20 offset:0\n" \
  "ds_read_b64_tr_b16 v[138:139], v21 offset:0\n" \
  "ds_read_b64_tr_b16 v[152:153], v20 offset:8\n" \
  "ds_read_b64_tr_b16 v[154:155], v21 offset:8\n" \
  "ds_read_b64_tr_b16 v[140:141], v20 offset:2048\n" \
  "ds_read_b64_tr_b16 v[142:143], v21 offset:2048\n" \
  "ds_read_b64_tr_b16 v[156:157], v20 offset:2056\n" \
  "ds_read_b64_tr_b16 v[158:159], v21 offset:2056\n" \
  "ds_read_b64_tr_b16 v[144:145], v20 offset:4096\n" \
  "ds_read_b64_tr_b16 v[146:147], v21 offset:4096\n" \
  "ds_read_b64_tr_b16 v[160:161], v20 offset:4104\n" \
  "ds_read_b64_tr_b16 v[162:163], v21 offset:4104\n" \
  "ds_read_b64_tr_b16 v[148:149], v20 offset:6144\n" \
  "ds_read_b64_tr_b16 v[150:151], v21 offset:6144\n" \
  "ds_read_b64_tr_b16 v[164:165], v20 offset:6152\n" \
  "ds_read_b64_tr_b16 v[166:167], v21 offset:6152\n" \
  "ds_read_b128 v[104:107], v84 offset:0\n" \
  "ds_read_b128 v[108:111], v85 offset:0\n" \
  "ds_read_b128 v[112:115], v86 offset:0\n" \
  "ds_read_b128 v[116:119], v87 offset:0\n" \
  "ds_read_b128 v[120:123], v84 offset:4096\n" \
  "ds_read_b128 v[124:127], v85 offset:4096\n" \
  "ds_read_b128 v[128:131], v86 offset:4096\n" \
  "ds_read_b128 v[132:135], v87 offset:4096\n" \
  "s_waitcnt lgkmcnt(0)\n" \
  "v_mfma_f32_16x16x32_bf16 v[184:187], v[136:139], a[224:227], 0\n" \
  "v_mfma_f32_16x16x32_bf16 v[188:191], v[152:155], a[224:227], 0\n" \
  "v_mfma_f32_16x16x32_bf16 v[192:195], v[136:139], a[240:243], 0\n" \
  "v_mfma_f32_16x16x32_bf16 v[196:199], v[152:155], a[240:243], 0\n" \
  "v_mfma_f32_16x16x32_bf16 v[184:187], v[140:143], a[228:231], v[184:187]\n" \
  "v_mfma_f32_16x16x32_bf16 v[188:191], v[156:159], a[228:231], v[188:191]\n" \
  "v_mfma_f32_16x16x32_bf16 v[192:195], v[140:143], a[244:247], v[192:195]\n" \
  "v_mfma_f32_16x16x32_bf16 v[196:199], v[156:159], a[244:247], v[196:199]\n" \
  "v_mfma_f32_16x16x32_bf16 v[184:187], v[144:147], a[232:235], v[184:187]\n" \
  "v_mfma_f32_16x16x32_bf16 v[188:191], v[160:163], a[232:235], v[188:191]\n" \
  "v_mfma_f32_16x16x32_bf16 v[192:195], v[144:147], a[248:251], v[192:195]\n" \
  "v_mfma_f32_16x16x32_bf16 v[196:199], v[160:163], a[248:251], v[196:199]\n" \
  "v_mfma_f32_16x16x32_bf16 v[184:187], v[148:151], a[236:239], v[184:187]\n" \
  "v_mfma_f32_16x16x32_bf16 v[188:191], v[164:167], a[236:239], v[188:191]\n" \
  "v_mfma_f32_16x16x32_bf16 v[192:195], v[148:151], a[252:255], v[192:195]\n" \
  "v_mfma_f32_16x16x32_bf16 v[196:199], v[164:167], a[252:255], v[196:199]\n" \
  "s_nop 7\n" \
  "v_cvt_pk_bf16_f32 v176, v184, v185\n" \
  "v_cvt_pk_bf16_f32 v177, v186, v187\n" \
  "v_cvt_pk_bf16_f32 v178, v188, v189\n" \
  "v_cvt_pk_bf16_f32 v179, v190, v191\n" \
  "v_cvt_pk_bf16_f32 v180, v192, v193\n" \
  "v_cvt_pk_bf16_f32 v181, v194, v195\n" \
  "v_cvt_pk_bf16_f32 v182, v196, v197\n" \
  "v_cvt_pk_bf16_f32 v183, v198, v199\n" \
  "v_mfma_f32_16x16x32_bf16 v[184:187], v[136:139], a[192:195], 0\n" \
  "v_mfma_f32_16x16x32_bf16 v[188:191], v[152:155], a[192:195], 0\n" \
  "v_mfma_f32_16x16x32_bf16 v[192:195], v[136:139], a[208:211], 0\n" \
  "v_mfma_f32_16x16x32_bf16 v[196:199], v[152:155], a[208:211], 0\n" \
  "v_mfma_f32_16x16x32_bf16 v[184:187], v[140:143], a[196:199], v[184:187]\n" \
  "v_mfma_f32_16x16x32_bf16 v[188:191], v[156:159], a[196:199], v[188:191]\n" \
  "v_mfma_f32_16x16x32_bf16 v[192:195], v[140:143], a[212:215], v[192:195]\n" \
  "v_mfma_f32_16x16x32_bf16 v[196:199], v[156:159], a[212:215], v[196:199]\n" \
  "v_mfma_f32_16x16x32_bf16 v[184:187], v[144:147], a[200:203], v[184:187]\n" \
  "v_mfma_f32_16x16x32_bf16 v[188:191], v[160:163], a[200:203], v[188:191]\n" \
  "v_mfma_f32_16x16x32_bf16 v[192:195], v[144:147], a[216:219], v[192:195]\n" \
  "v_mfma_f32_16x16x32_bf16 v[196:199], v[160:163], a[216:219], v[196:199]\n" \
  "v_mfma_f32_16x16x32_bf16 v[184:187], v[148:151], a[204:207], v[184:187]\n" \
  "v_mfma_f32_16x16x32_bf16 v[188:191], v[164:167], a[204:207], v[188:191]\n" \
  "v_mfma_f32_16x16x32_bf16 v[192:195], v[148:151], a[220:223], v[192:195]\n" \
  "v_mfma_f32_16x16x32_bf16 v[196:199], v[164:167], a[220:223], v[196:199]\n" \
  "s_memtime s[72:73]\n" \
  "s_memrealtime s[74:75]\n" \
  "s_nop 1\n" \
  "s_memrealtime s[76:77]\n" \
  "s_nop 1\n" \
  "s_memrealtime s[78:79]\n" \
  "s_nop 1\n" \
  "s_memrealtime s[80:81]\n" \
  "s_nop 1\n" \
  "s_memrealtime s[82:83]\n" \
  "s_nop 1\n" \
  "s_memrealtime s[84:85]\n" \
  "s_nop 1\n" \
  "s_memrealtime s[86:87]\n" \
  "s_nop 1\n" \
  "s_memrealtime s[88:89]\n" \
  "s_nop 1\n" \
  "s_memrealtime s[90:91]\n" \
  "s_mov_b32 s59, 0\n" \
  "s_waitcnt lgkmcnt(0)\n" \
  "s_cmp_eq_u32 s76, s74\n" \
  "s_addc_u32 s59, s59, 0\n" \
  "s_cmp_eq_u32 s78, s74\n" \
  "s_addc_u32 s59, s59, 0\n" \
  "s_cmp_eq_u32 s80, s74\n" \
  "s_addc_u32 s59, s59, 0\n" \
  "s_cmp_eq_u32 s82, s74\n" \
  "s_addc_u32 s59, s59, 0\n" \
  "s_cmp_eq_u32 s84, s74\n" \
  "s_addc_u32 s59, s59, 0\n" \
  "s_cmp_eq_u32 s86, s74\n" \
  "s_addc_u32 s59, s59, 0\n" \
  "s_cmp_eq_u32 s88, s74\n" \
  "s_addc_u32 s59, s59, 0\n" \
  "s_cmp_eq_u32 s90, s74\n" \
  "s_addc_u32 s59, s59, 0\n" \
  "s_mul_i32 s59, s59, 12\n" \
  "s_add_u32 s68, s72, s59\n" \
  "s_add_u32 s59, s74, 4\n" \
  "s_mul_i32 s69, s59, 24\n" \
  "v_cvt_pk_bf16_f32 v168, v184, v185\n" \
  "v_cvt_pk_bf16_f32 v169, v186, v187\n" \
  "v_cvt_pk_bf16_f32 v170, v188, v189\n" \
  "v_cvt_pk_bf16_f32 v171, v190, v191\n" \
  "v_cvt_pk_bf16_f32 v172, v192, v193\n" \
  "v_cvt_pk_bf16_f32 v173, v194, v195\n" \
  "v_cvt_pk_bf16_f32 v174, v196, v197\n" \
  "v_cvt_pk_bf16_f32 v175, v198, v199\n" \
  "v_mov_b32 v95, " STR(RI0) "\n" \
  "s_sub_u32 s60, s69, s62\n" \
  "s_sub_u32 s59, s68, s56\n" \
  "s_mov_b32 s62, s69\n" \
  "s_mov_b32 s56, s68\n" \
  "v_cvt_f32_u32 v27, s59\n" \
  "v_cvt_f32_u32 v28, s60\n" \
  "v_rcp_f32 v26, v27\n" \
  "v_rcp_f32 v19, v28\n" \
  "v_mul_f32 v26, v26, v28\n" \
  "v_mul_f32 v95, " STR(DECAY) ", v95\n" \
  "v_mul_f32 v19, v19, v27\n" \
  "v_max_f32 v95, v95, v26\n" \
  "v_cvt_f32_u32 v28, " STR(F_P) "\n" \
  "v_mul_f32 v27, v95, v28\n" \
  "v_cvt_u32_f32 v27, v27\n" \
  "v_min_u32 v27, 0x7fff, v27\n" \
  "s_nop 0\n" \
  "v_readfirstlane_b32 s63, v27\n" \
  "s_add_u32 s59, s74, 4+63\n" \
  "s_and_b32 s59, s59, -64\n" \
  "s_mul_i32 s59, s59, 24\n" \
  "s_sub_u32 s59, s59, s69\n" \
  "v_cvt_f32_u32 v27, s59\n" \
  "v_mul_f32 v28, 0x44c00000, v19\n" \
  "v_mul_f32 v27, v27, v19\n" \
  "v_cvt_u32_f32 v28, v28\n" \
  "v_cvt_u32_f32 v27, v27\n" \
  "v_max_u32 v28, " STR(T_Q) ", v28\n" \
  "v_min_u32 v28, 2047, v28\n" \
  "v_readfirstlane_b32 s59, v27\n" \
  "v_readfirstlane_b32 s95, v28\n" \
  "v_subrev_u32 v28, " STR(T_Q) ", v28\n" \
  "v_lshlrev_b32 v28, 2, v28\n" \
  "v_mov_b32 v27, 0\n" \
  "s_add_u32 s59, s59, s68\n" \
  "s_cmp_lg_u32 %7, 1\n" \
  "s_cbranch_scc1 LI8_%=\n" \
  "ds_write_b32 v24, v28 offset:14344\n" \
  "ds_write_b32 v24, v27 offset:14356\n" \
  "v_mov_b32 v92, s59\n" \
  "v_mov_b32 v93, 0\n" \
  "ds_write_b64 v24, v[92:93] offset:14336\n" \
  "LI8_%=:\n" \
  "s_waitcnt lgkmcnt(0)\n" \
  "s_add_u32 s49, s53, s93\n" \
  "s_lshl_b32 s59, s70, 7\n" \
  "s_add_u32 s59, s59, " STR(CW_PROG) "*4\n" \
  "s_lshl_b32 s60, s71, 2\n" \
  "s_or_b32 s60, s60, %7\n" \
  "s_cmp_eq_u32 s60, 3\n" \
  "s_cselect_b32 s59, s59, 0x80000000\n" \
  "v_lshl_add_u32 v0, v0, 24, s59\n" \
  "s_lshr_b32 s59, %7, 1\n" \
  "s_xor_b32 s59, s59, 1\n" \
  "s_lshl_b32 s59, s59, 31\n" \
  "v_or_b32 v22, s59, v22\n" \
  "v_mov_b64 v[48:49], 0\n" \
  "v_mov_b64 v[50:51], 0\n" \
  "v_mov_b64 v[52:53], 0\n" \
  "v_mov_b64 v[54:55], 0\n" \
  "v_mov_b64 v[56:57], 0\n" \
  "v_mov_b64 v[58:59], 0\n" \
  "v_mov_b64 v[60:61], 0\n" \
  "v_mov_b64 v[62:63], 0\n" \
  "v_mov_b64 v[64:65], 0\n" \
  "v_mov_b64 v[66:67], 0\n" \
  "v_mov_b64 v[68:69], 0\n" \
  "v_mov_b64 v[70:71], 0\n" \
  "s_cmp_lt_u32 %7, 2\n" \
  "s_cbranch_scc1 LL_%=\n" \
  "LI7a_%=:\n" \
  "buffer_load_dword v23, v22, s[44:47], 0 offen sc1\n" \
  "s_waitcnt vmcnt(0)\n" \
  "v_cmp_gt_u32 vcc, 2, v23\n" \
  "s_cbranch_vccz LI7b_%=\n" \
  "s_sleep 4\n" \
  "s_add_u32 s58, s58, 1\n" \
  "s_cmp_lt_u32 s58, " STR(SPIN_CAP) "\n" \
  "s_cbranch_scc1 LI7a_%=\n" \
  "v_mov_b32 v27, " STR(CW_DEAD) "*4\n" \
  "v_mov_b32 v28, 1\n" \
  "buffer_store_dword v28, v27, s[44:47], 0 offen sc1\n" \
  "s_branch LI7b_%=\n" \
  "LI7b_%=:\n" \
  "buffer_load_dwordx4 v[200:203], v247, s[40:43], s55 offen offset:0 sc1\n" \
  "buffer_load_dwordx4 v[204:207], v247, s[40:43], s55 offen offset:1024 sc1\n" \
  "buffer_load_dwordx4 v[208:211], v247, s[40:43], s55 offen offset:2048 sc1\n" \
  "buffer_load_dwordx4 v[212:215], v247, s[40:43], s55 offen offset:3072 sc1\n" \
  "buffer_load_dwordx4 v[216:219], v16, s[40:43], s55 offen offset:0 sc1\n" \
  "buffer_load_dwordx4 v[220:223], v16, s[40:43], s55 offen offset:1024 sc1\n" \
  "buffer_load_dwordx4 v[224:227], v16, s[40:43], s55 offen offset:2048 sc1\n" \
  "buffer_load_dwordx4 v[228:231], v16, s[40:43], s55 offen offset:3072 sc1\n" \
  "s_branch LM_%=\n" \
  ".p2align 6\n" \
  "LL_%=:\n" \
  "s_barrier\n" \
  "buffer_load_dwordx4 v[32:35], v80, s[36:39], s49 offen sc1\n" \
  "v_mfma_f32_16x16x32_bf16 v[184:187], v[136:139], a[224:227], 0\n" \
  "s_xor_b32 s51, s51, 1<<20\n" \
  "s_xor_b32 s52, s52, 1<<20\n" \
  "v_mfma_f32_16x16x32_bf16 v[188:191], v[152:155], a[224:227], 0\n" \
  "s_add_u32 s53, s53, 32768\n" \
  "s_lshl_b32 s60, s48, 19\n" \
  "v_mfma_f32_16x16x32_bf16 v[192:195], v[136:139], a[240:243], 0\n" \
  "s_add_u32 s60, s60, " STR(EXP_B) "<<21\n" \
  "s_lshr_b32 s59, s60, 21\n" \
  "buffer_load_dwordx4 v[36:39], v81, s[36:39], s49 offen sc1\n" \
  "v_mfma_f32_16x16x32_bf16 v[196:199], v[152:155], a[240:243], 0\n" \
  "s_lshr_b32 s59, %8, s59\n" \
  "s_and_b32 s94, s59, 1\n" \
  "v_mfma_f32_16x16x32_bf16 v[184:187], v[140:143], a[228:231], v[184:187]\n" \
  "s_lshl_b32 s94, s94, 13\n" \
  "s_or_b32 s50, s60, s94\n" \
  "v_mfma_f32_16x16x32_bf16 v[188:191], v[156:159], a[228:231], v[188:191]\n" \
  "s_lshl_b32 s94, s70, 14\n" \
  "s_add_u32 s91, s50, s94\n" \
  "buffer_load_dwordx4 v[40:43], v82, s[36:39], s49 offen sc1\n" \
  "v_mfma_f32_16x16x32_bf16 v[192:195], v[140:143], a[244:247], v[192:195]\n" \
  "s_and_b32 s94, s59, 2\n" \
  "s_lshl_b32 s94, s94, 12\n" \
  "v_mfma_f32_16x16x32_bf16 v[196:199], v[156:159], a[244:247], v[196:199]\n" \
  "s_add_u32 s60, s60, 1<<21\n" \
  "s_or_b32 s54, s60, s94\n" \
  "v_mfma_f32_16x16x32_bf16 v[184:187], v[144:147], a[232:235], v[184:187]\n" \
  "s_xor_b32 s59, %7, 3\n" \
  "s_lshl_b32 s59, s59, 21\n" \
  "buffer_load_dwordx4 v[44:47], v83, s[36:39], s49 offen sc1\n" \
  "v_mfma_f32_16x16x32_bf16 v[188:191], v[160:163], a[232:235], v[188:191]\n" \
  "s_add_u32 s59, s59, s53\n" \
  "s_add_u32 s59, s59, 4<<15\n" \
  "v_mfma_f32_16x16x32_bf16 v[192:195], v[144:147], a[248:251], v[192:195]\n" \
  "s_cmp_lt_u32 s59, 1<<21\n" \
  "s_cselect_b32 s59, s59, 1<<30\n" \
  "v_mfma_f32_16x16x32_bf16 v[196:199], v[160:163], a[248:251], v[196:199]\n" \
  "v_mfma_f32_16x16x32_bf16 v[184:187], v[148:151], a[236:239], v[184:187]\n" \
  "v_mfma_f32_16x16x32_bf16 v[188:191], v[164:167], a[236:239], v[188:191]\n" \
  "v_add_u32 v27, s59, v12\n" \
  "buffer_store_dwordx4 v[88:91], v11, s[36:39], s52 offen sc1\n" \
  "buffer_store_dwordx4 v[88:91], v15, s[36:39], s54 offen sc1\n" \
  "buffer_store_dwordx4 v[88:91], v27, s[36:39], 0 offen sc1\n" \
  "buffer_load_dword v23, v22, s[44:47], 0 offen sc1\n" \
  "v_mfma_f32_16x16x32_bf16 v[192:195], v[148:151], a[252:255], v[192:195]\n" \
  "v_mfma_f32_16x16x32_bf16 v[196:199], v[164:167], a[252:255], v[196:199]\n" \
  "s_nop 6\n" \
  "v_cvt_pk_bf16_f32 v176, v184, v185\n" \
  "v_cvt_pk_bf16_f32 v177, v186, v187\n" \
  "v_cvt_pk_bf16_f32 v178, v188, v189\n" \
  "v_cvt_pk_bf16_f32 v179, v190, v191\n" \
  "v_cvt_pk_bf16_f32 v180, v192, v193\n" \
  "v_cvt_pk_bf16_f32 v181, v194, v195\n" \
  "v_cvt_pk_bf16_f32 v182, v196, v197\n" \
  "v_cvt_pk_bf16_f32 v183, v198, v199\n" \
  "LS1_%=:\n" \
  "s_waitcnt vmcnt(7)\n" \
  "v_smfmac_f32_16x16x64_bf16 v[52:55], v[32:35], a[8:15], v2\n" \
  "v_smfmac_f32_16x16x64_bf16 v[56:59], v[32:35], a[16:23], v2\n" \
  "v_smfmac_f32_16x16x64_bf16 v[60:63], v[32:35], a[24:31], v2\n" \
  "v_smfmac_f32_16x16x64_bf16 v[48:51], v[32:35], a[0:7], v2\n" \
  "s_waitcnt vmcnt(6)\n" \
  "v_smfmac_f32_16x16x64_bf16 v[52:55], v[36:39], a[40:47], v2\n" \
  "v_smfmac_f32_16x16x64_bf16 v[56:59], v[36:39], a[48:55], v2\n" \
  "v_smfmac_f32_16x16x64_bf16 v[60:63], v[36:39], a[56:63], v2\n" \
  "v_smfmac_f32_16x16x64_bf16 v[48:51], v[36:39], a[32:39], v2\n" \
  "s_waitcnt vmcnt(5)\n" \
  "v_smfmac_f32_16x16x64_bf16 v[52:55], v[40:43], a[72:79], v2\n" \
  "v_smfmac_f32_16x16x64_bf16 v[56:59], v[40:43], a[80:87], v2\n" \
  "v_smfmac_f32_16x16x64_bf16 v[60:63], v[40:43], a[88:95], v2\n" \
  "v_smfmac_f32_16x16x64_bf16 v[48:51], v[40:43], a[64:71], v2\n" \
  "s_waitcnt vmcnt(4)\n" \
  "v_smfmac_f32_16x16x64_bf16 v[52:55], v[44:47], a[104:111], v2\n" \
  "v_smfmac_f32_16x16x64_bf16 v[56:59], v[44:47], a[112:119], v2\n" \
  "v_smfmac_f32_16x16x64_bf16 v[60:63], v[44:47], a[120:127], v2\n" \
  "v_add_f32 v52, v52, v54\n" \
  "v_add_f32 v53, v53, v55\n" \
  "v_smfmac_f32_16x16x64_bf16 v[48:51], v[44:47], a[96:103], v2\n" \
  "v_add_f32 v56, v56, v58\n" \
  "v_add_f32 v57, v57, v59\n" \
  "ds_write_b64 v3, v[52:53] offset:0\n" \
  "v_add_f32 v60, v60, v62\n" \
  "v_add_f32 v61, v61, v63\n" \
  "v_smfmac_f32_16x16x64_bf16 v[64:67], v[32:35], a[128:135], v2\n" \
  "ds_write_b64 v244, v[56:57] offset:512\n" \
  "v_smfmac_f32_16x16x64_bf16 v[68:71], v[32:35], a[136:143], v2\n" \
  "ds_write_b64 v245, v[60:61] offset:1024\n" \
  "v_cmp_u_f32 vcc, v52, v53\n" \
  "v_smfmac_f32_16x16x64_bf16 v[64:67], v[36:39], a[144:151], v2\n" \
  "v_add_f32 v48, v48, v50\n" \
  "v_add_f32 v49, v49, v51\n" \
  "v_smfmac_f32_16x16x64_bf16 v[68:71], v[36:39], a[152:159], v2\n" \
  "v_smfmac_f32_16x16x64_bf16 v[64:67], v[40:43], a[160:167], v2\n" \
  "s_cbranch_vccnz LA_%=\n" \
  "LA4_%=:\n" \
  "s_waitcnt lgkmcnt(0)\n" \
  "s_barrier\n" \
  "ds_read_b64 v[72:73], v6 offset:0\n" \
  "ds_read_b64 v[74:75], v6 offset:512\n" \
  "ds_read_b64 v[76:77], v6 offset:1024\n" \
  "ds_read_b96 v[92:94], v24 offset:14336\n" \
  "v_smfmac_f32_16x16x64_bf16 v[68:71], v[40:43], a[168:175], v2\n" \
  "v_smfmac_f32_16x16x64_bf16 v[64:67], v[44:47], a[176:183], v2\n" \
  "s_waitcnt lgkmcnt(3)\n" \
  "v_add_f32 v48, v48, v72\n" \
  "v_add_f32 v49, v49, v73\n" \
  "s_waitcnt lgkmcnt(1)\n" \
  "v_add_f32 v74, v74, v76\n" \
  "v_add_f32 v75, v75, v77\n" \
  "v_add_f32 v48, v48, v74\n" \
  "v_add_f32 v49, v49, v75\n" \
  "v_cvt_pk_bf16_f32 v27, v48, v49\n" \
  "v_smfmac_f32_16x16x64_bf16 v[68:71], v[44:47], a[184:191], v2\n" \
  "v_mov_b32_dpp v28, v27 quad_perm:[1,0,3,2] row_mask:0xf bank_mask:0xf\n" \
  "v_perm_b32 v27, v28, v27, v246\n" \
  "buffer_store_dword v27, v8, s[36:39], s50 offen sc1\n" \
  "v_add_f32 v64, v64, v66\n" \
  "v_add_f32 v65, v65, v67\n" \
  "s_nop 2\n" \
  "v_add_f32 v68, v68, v70\n" \
  "v_add_f32 v69, v69, v71\n" \
  "ds_write2_b32 v5, v64, v65 offset0:0 offset1:40\n" \
  "ds_write2_b32 v5, v68, v69 offset0:16 offset1:56\n" \
  "v_mov_b64 v[184:185], 0\n" \
  "s_add_u32 s49, s53, s93\n" \
  "s_nop 1\n" \
  "v_mov_b64 v[186:187], 0\n" \
  "s_nop 1\n" \
  "v_mov_b64 v[188:189], 0\n" \
  "s_waitcnt lgkmcnt(0)\n" \
  "s_barrier\n" \
  "s_memtime s[66:67]\n" \
  "ds_read_b64 v[72:73], v7 offset:0\n" \
  "ds_read_b64 v[74:75], v7 offset:1280\n" \
  "ds_read_b64 v[76:77], v7 offset:2560\n" \
  "ds_read_b64 v[78:79], v7 offset:3840\n" \
  "v_readfirstlane_b32 s64, v92\n" \
  "v_readfirstlane_b32 s59, v93\n" \
  "v_readfirstlane_b32 s60, v94\n" \
  "v_mov_b64 v[190:191], 0\n" \
  "s_add_u32 s64, s64, s59\n" \
  "s_add_u32 s61, s64, " STR(F_B) "\n" \
  "v_mov_b64 v[192:193], 0\n" \
  "s_cmp_eq_u32 s48, 63\n" \
  "s_waitcnt lgkmcnt(0)\n" \
  "v_pk_add_f32 v[72:73], v[72:73], v[74:75]\n" \
  "v_pk_add_f32 v[76:77], v[76:77], v[78:79]\n" \
  "v_pk_add_f32 v[72:73], v[72:73], v[76:77]\n" \
  "v_cvt_pk_bf16_f32 v27, v72, v73\n" \
  "buffer_store_dword v27, v9, s[36:39], s50 offen sc1\n" \
  "s_cbranch_scc1 LE_%=\n" \
  "s_sub_i32 s61, s61, s66\n" \
  "s_cmp_gt_i32 s61, 24\n" \
  "s_cbranch_scc1 LW_b_%=\n" \
  "LB_b_%=:\n" \
  "buffer_load_dwordx4 v[32:35], v80, s[36:39], s91 offen sc1\n" \
  "s_lshr_b32 s95, s60, 2\n" \
  "s_sub_u32 s96, s60, s95\n" \
  "s_add_u32 s95, s95, " STR(T_Q) "\n" \
  "s_add_u32 s96, s96, " STR(F_C) "\n" \
  "s_sub_u32 s59, s64, s56\n" \
  "s_add_u32 s59, s59, " STR(F_P) "\n" \
  "v_mov_b64 v[194:195], 0\n" \
  "v_mov_b64 v[196:197], 0\n" \
  "v_mov_b64 v[198:199], 0\n" \
  "s_nop " STR(GAP_0) "\n" \
  "buffer_load_dwordx4 v[36:39], v81, s[36:39], s91 offen sc1\n" \
  "ds_read_b64_tr_b16 v[136:137], v20 offset:0\n" \
  "ds_read_b64_tr_b16 v[138:139], v21 offset:0\n" \
  "ds_read_b64_tr_b16 v[152:153], v20 offset:8\n" \
  "ds_read_b64_tr_b16 v[154:155], v21 offset:8\n" \
  "s_nop " STR(GAP_B) "\n" \
  "buffer_load_dwordx4 v[40:43], v82, s[36:39], s91 offen sc1\n" \
  "ds_read_b64_tr_b16 v[140:141], v20 offset:2048\n" \
  "ds_read_b64_tr_b16 v[142:143], v21 offset:2048\n" \
  "ds_read_b64_tr_b16 v[156:157], v20 offset:2056\n" \
  "ds_read_b64_tr_b16 v[158:159], v21 offset:2056\n" \
  "s_nop " STR(GAP_B) "\n" \
  "buffer_load_dwordx4 v[44:47], v83, s[36:39], s91 offen sc1\n" \
  "ds_read_b64_tr_b16 v[144:145], v20 offset:4096\n" \
  "ds_read_b64_tr_b16 v[146:147], v21 offset:4096\n" \
  "ds_read_b64_tr_b16 v[160:161], v20 offset:4104\n" \
  "ds_read_b64_tr_b16 v[162:163], v21 offset:4104\n" \
  "s_nop " STR(GAP_V) "\n" \
  "buffer_load_dwordx4 v[96:99], v17, s[36:39], s50 offen offset:0 sc1\n" \
  "ds_read_b64_tr_b16 v[148:149], v20 offset:6144\n" \
  "ds_read_b64_tr_b16 v[150:151], v21 offset:6144\n" \
  "ds_read_b64_tr_b16 v[164:165], v20 offset:6152\n" \
  "ds_read_b64_tr_b16 v[166:167], v21 offset:6152\n" \
  "s_nop " STR(GAP_B) "\n" \
  "buffer_load_dwordx4 v[100:103], v17, s[36:39], s50 offen offset:1024 sc1\n" \
  "v_cvt_f32_i32 v92, s59\n" \
  "v_mov_b32 v93, s63\n" \
  "v_mul_f32 v92, v92, v95\n" \
  "v_cvt_i32_f32 v92, v92\n" \
  "v_add_u32 v92, s62, v92\n" \
  "v_min_u32 v92, -2, v92\n" \
  "buffer_store_dwordx2 v[92:93], v14, s[36:39], s50 offen sc1\n" \
  "s_mov_b32 vcc_lo, 0x33333333\n" \
  "s_mov_b32 vcc_hi, 0x33333333\n" \
  "LS2_%=:\n" \
  "s_waitcnt vmcnt(6)\n" \
  "v_mfma_f32_16x16x32_bf16 v[228:231], v[120:123], v[32:35], 0\n" \
  "v_mfma_f32_16x16x32_bf16 v[224:227], v[104:107], v[32:35], 0\n" \
  "v_mfma_f32_16x16x32_bf16 v[232:235], v[32:35], v[32:35], 0\n" \
  "s_waitcnt vmcnt(5)\n" \
  "v_mfma_f32_16x16x32_bf16 v[228:231], v[124:127], v[36:39], v[228:231]\n" \
  "v_mfma_f32_16x16x32_bf16 v[224:227], v[108:111], v[36:39], v[224:227]\n" \
  "v_mfma_f32_16x16x32_bf16 v[232:235], v[36:39], v[36:39], v[232:235]\n" \
  "s_waitcnt vmcnt(4)\n" \
  "v_mfma_f32_16x16x32_bf16 v[228:231], v[128:131], v[40:43], v[228:231]\n" \
  "v_mfma_f32_16x16x32_bf16 v[224:227], v[112:115], v[40:43], v[224:227]\n" \
  "v_mfma_f32_16x16x32_bf16 v[232:235], v[40:43], v[40:43], v[232:235]\n" \
  "s_waitcnt vmcnt(3)\n" \
  "v_mfma_f32_16x16x32_bf16 v[228:231], v[132:135], v[44:47], v[228:231]\n" \
  "v_mfma_f32_16x16x32_bf16 v[224:227], v[116:119], v[44:47], v[224:227]\n" \
  "v_mfma_f32_16x16x32_bf16 v[232:235], v[44:47], v[44:47], v[232:235]\n" \
  "s_nop 3\n" \
  "v_cndmask_b32_dpp v224, v228, v224, vcc quad_perm:[2,3,0,1] row_mask:0xf bank_mask:0xf\n" \
  "v_cndmask_b32_dpp v225, v229, v225, vcc quad_perm:[2,3,0,1] row_mask:0xf bank_mask:0xf\n" \
  "v_cndmask_b32_dpp v226, v230, v226, vcc quad_perm:[2,3,0,1] row_mask:0xf bank_mask:0xf\n" \
  "v_cndmask_b32_dpp v227, v231, v227, vcc quad_perm:[2,3,0,1] row_mask:0xf bank_mask:0xf\n" \
  "v_cndmask_b32_dpp v234, v235, v234, vcc quad_perm:[2,3,0,1] row_mask:0xf bank_mask:0xf\n" \
  "v_cmp_u_f32 vcc, v228, v228\n" \
  "v_max3_f32 v25, v224, v225, v226\n" \
  "v_max3_f32 v25, v25, v227, v234\n" \
  "s_waitcnt vmcnt(2)\n" \
  "v_smfmac_f32_16x16x64_bf16 v[184:187], v[96:99], a[192:199], v2\n" \
  "v_max_f32_dpp v26, v25, v25 quad_perm:[2,3,0,1] row_mask:0xf bank_mask:0xf\n" \
  "v_max_f32_dpp v25, v25, v25 quad_perm:[2,3,0,1] row_mask:0xf bank_mask:0xf\n" \
  "v_smfmac_f32_16x16x64_bf16 v[188:191], v[96:99], a[208:215], v2\n" \
  "v_permlane16_swap_b32 v25, v26\n" \
  "v_max_f32 v27, v25, v26\n" \
  "v_max_f32 v25, v25, v26\n" \
  "v_smfmac_f32_16x16x64_bf16 v[192:195], v[96:99], a[224:231], v2\n" \
  "s_cbranch_vccnz LBE_%=\n" \
  "LBH_%=:\n" \
  "v_permlane32_swap_b32 v25, v27\n" \
  "v_max_f32 v25, v25, v27\n" \
  "v_mul_f32 v25, s92, v25\n" \
  "v_fma_f32 v224, v224, s92, -v25\n" \
  "v_smfmac_f32_16x16x64_bf16 v[196:199], v[96:99], a[240:247], v2\n" \
  "v_fma_f32 v225, v225, s92, -v25\n" \
  "v_exp_f32 v224, v224\n" \
  "v_exp_f32 v225, v225\n" \
  "s_waitcnt vmcnt(1)\n" \
  "v_smfmac_f32_16x16x64_bf16 v[184:187], v[100:103], a[200:207], v2\n" \
  "v_fma_f32 v226, v226, s92, -v25\n" \
  "v_smfmac_f32_16x16x64_bf16 v[188:191], v[100:103], a[216:223], v2\n" \
  "v_fma_f32 v227, v227, s92, -v25\n" \
  "v_smfmac_f32_16x16x64_bf16 v[192:195], v[100:103], a[232:239], v2\n" \
  "v_exp_f32 v226, v226\n" \
  "v_smfmac_f32_16x16x64_bf16 v[196:199], v[100:103], a[248:255], v2\n" \
  "v_exp_f32 v227, v227\n" \
  "v_cvt_pk_bf16_f32 v228, v224, v225\n" \
  "v_cvt_pk_bf16_f32 v229, v226, v227\n" \
  "v_and_b32 v236, v228, v4\n" \
  "v_and_b32 v237, v229, v4\n" \
  "v_and_b32_dpp v238, v228, v4 quad_perm:[2,3,0,1] row_mask:0xf bank_mask:0xf\n" \
  "v_and_b32_dpp v239, v229, v4 quad_perm:[2,3,0,1] row_mask:0xf bank_mask:0xf\n" \
  "v_fma_f32 v234, v234, s92, -v25\n" \
  "v_and_b32_dpp v240, v228, v29 quad_perm:[2,3,0,1] row_mask:0xf bank_mask:0xf\n" \
  "v_mfma_f32_16x16x32_bf16 v[48:51], v[168:171], v[236:239], 0\n" \
  "v_and_b32_dpp v241, v229, v29 quad_perm:[2,3,0,1] row_mask:0xf bank_mask:0xf\n" \
  "v_and_b32 v242, v228, v29\n" \
  "v_mfma_f32_16x16x32_bf16 v[52:55], v[176:179], v[236:239], 0\n" \
  "v_and_b32 v243, v229, v29\n" \
  "v_exp_f32 v234, v234\n" \
  "v_cvt_pk_bf16_f32 v216, v184, v185\n" \
  "v_mfma_f32_16x16x32_bf16 v[48:51], v[172:175], v[240:243], v[48:51]\n" \
  "v_mov_b32_dpp v25, v234 quad_perm:[0,1,0,1] row_mask:0xf bank_mask:0xf\n" \
  "v_mov_b32_dpp v26, v234 quad_perm:[2,3,2,3] row_mask:0xf bank_mask:0xf\n" \
  "v_mfma_f32_16x16x32_bf16 v[52:55], v[180:183], v[240:243], v[52:55]\n" \
  "v_cvt_pk_bf16_f32 v25, v25, v26\n" \
  "v_and_b32 v230, v25, v4\n" \
  "v_and_b32 v231, v25, v4\n" \
  "v_and_b32 v232, v25, v29\n" \
  "v_and_b32 v233, v25, v29\n" \
  "v_mfma_f32_16x16x32_bf16 v[56:59], v[252:255], v[228:231], 0\n" \
  "v_cvt_pk_bf16_f32 v217, v186, v187\n" \
  "v_cvt_pk_bf16_f32 v218, v188, v189\n" \
  "v_cvt_pk_bf16_f32 v219, v190, v191\n" \
  "v_cvt_pk_bf16_f32 v220, v192, v193\n" \
  "v_cvt_pk_bf16_f32 v221, v194, v195\n" \
  "v_mfma_f32_16x16x32_bf16 v[48:51], v[216:219], v[230:233], v[48:51]\n" \
  "v_cvt_pk_bf16_f32 v222, v196, v197\n" \
  "v_cvt_pk_bf16_f32 v223, v198, v199\n" \
  "v_add_f32_dpp v56, v56, v56 quad_perm:[2,3,0,1] row_mask:0xf bank_mask:0xf\n" \
  "v_rcp_f32 v56, v56\n" \
  "v_mfma_f32_16x16x32_bf16 v[52:55], v[220:223], v[230:233], v[52:55]\n" \
  "s_nop 2\n" \
  "v_cmp_u_f32 vcc, v48, v48\n" \
  "v_mul_f32 v48, v48, v56\n" \
  "v_mul_f32 v49, v49, v56\n" \
  "v_mul_f32 v50, v50, v56\n" \
  "v_mul_f32 v51, v51, v56\n" \
  "v_pk_mul_f32 v[52:53], v[52:53], v[56:57] op_sel_hi:[1,0]\n" \
  "v_pk_mul_f32 v[54:55], v[54:55], v[56:57] op_sel_hi:[1,0]\n" \
  "v_cvt_pk_bf16_f32 v72, v48, v49\n" \
  "v_cvt_pk_bf16_f32 v73, v50, v51\n" \
  "v_cvt_pk_bf16_f32 v74, v52, v53\n" \
  "v_cvt_pk_bf16_f32 v75, v54, v55\n" \
  "s_cbranch_vccnz LBF_%=\n" \
  "LBG_%=:\n" \
  "buffer_store_dwordx4 v[72:75], v10, s[36:39], s51 offen sc1\n" \
  "s_memtime s[66:67]\n" \
  "s_add_u32 s61, s64, s96\n" \
  "s_cmp_lg_u32 %7, 0\n" \
  "s_cbranch_scc1 LO_%=\n" \
  "s_waitcnt lgkmcnt(0)\n" \
  "s_sub_i32 s61, s61, s66\n" \
  "s_max_i32 s61, s61, " STR(M_C) "\n" \
  "s_cmp_gt_i32 s61, 24\n" \
  "s_cbranch_scc1 LW_c_%=\n" \
  "LB_c_%=:\n" \
  "buffer_load_dwordx4 v[32:35], v11, s[36:39], s51 offen offset:0 sc1\n" \
  "buffer_load_dwordx4 v[36:39], v11, s[36:39], s51 offen offset:1024 sc1\n" \
  "buffer_load_dwordx4 v[40:43], v11, s[36:39], s51 offen offset:2048 sc1\n" \
  "buffer_load_dwordx4 v[44:47], v11, s[36:39], s51 offen offset:3072 sc1\n" \
  "v_mfma_f32_16x16x32_bf16 v[184:187], v[136:139], a[192:195], 0\n" \
  "v_mov_b64 v[48:49], 0\n" \
  "v_mfma_f32_16x16x32_bf16 v[188:191], v[152:155], a[192:195], 0\n" \
  "v_mov_b64 v[50:51], 0\n" \
  "v_mfma_f32_16x16x32_bf16 v[192:195], v[136:139], a[208:211], 0\n" \
  "v_mov_b64 v[52:53], 0\n" \
  "v_mfma_f32_16x16x32_bf16 v[196:199], v[152:155], a[208:211], 0\n" \
  "v_mov_b64 v[54:55], 0\n" \
  "v_mfma_f32_16x16x32_bf16 v[184:187], v[140:143], a[196:199], v[184:187]\n" \
  "v_mov_b64 v[56:57], 0\n" \
  "v_mfma_f32_16x16x32_bf16 v[188:191], v[156:159], a[196:199], v[188:191]\n" \
  "v_mov_b64 v[58:59], 0\n" \
  "v_mfma_f32_16x16x32_bf16 v[192:195], v[140:143], a[212:215], v[192:195]\n" \
  "v_mov_b64 v[60:61], 0\n" \
  "v_mfma_f32_16x16x32_bf16 v[196:199], v[156:159], a[212:215], v[196:199]\n" \
  "v_mov_b64 v[62:63], 0\n" \
  "v_mfma_f32_16x16x32_bf16 v[184:187], v[144:147], a[200:203], v[184:187]\n" \
  "v_mov_b64 v[64:65], 0\n" \
  "ds_read_b128 v[104:107], v84 offset:0\n" \
  "v_mfma_f32_16x16x32_bf16 v[188:191], v[160:163], a[200:203], v[188:191]\n" \
  "v_mov_b64 v[66:67], 0\n" \
  "ds_read_b128 v[108:111], v85 offset:0\n" \
  "v_mfma_f32_16x16x32_bf16 v[192:195], v[144:147], a[216:219], v[192:195]\n" \
  "v_mov_b64 v[68:69], 0\n" \
  "ds_read_b128 v[112:115], v86 offset:0\n" \
  "v_mfma_f32_16x16x32_bf16 v[196:199], v[160:163], a[216:219], v[196:199]\n" \
  "v_mov_b64 v[70:71], 0\n" \
  "ds_read_b128 v[116:119], v87 offset:0\n" \
  "v_mfma_f32_16x16x32_bf16 v[184:187], v[148:151], a[204:207], v[184:187]\n" \
  "ds_read_b128 v[120:123], v84 offset:4096\n" \
  "v_mfma_f32_16x16x32_bf16 v[188:191], v[164:167], a[204:207], v[188:191]\n" \
  "ds_read_b128 v[124:127], v85 offset:4096\n" \
  "v_mfma_f32_16x16x32_bf16 v[192:195], v[148:151], a[220:223], v[192:195]\n" \
  "ds_read_b128 v[128:131], v86 offset:4096\n" \
  "v_mfma_f32_16x16x32_bf16 v[196:199], v[164:167], a[220:223], v[196:199]\n" \
  "ds_read_b128 v[132:135], v87 offset:4096\n" \
  "s_add_u32 s48, s48, 1\n" \
  "s_nop 5\n" \
  "v_cvt_pk_bf16_f32 v168, v184, v185\n" \
  "v_cvt_pk_bf16_f32 v169, v186, v187\n" \
  "v_cvt_pk_bf16_f32 v170, v188, v189\n" \
  "v_cvt_pk_bf16_f32 v171, v190, v191\n" \
  "v_cvt_pk_bf16_f32 v172, v192, v193\n" \
  "v_cvt_pk_bf16_f32 v173, v194, v195\n" \
  "v_cvt_pk_bf16_f32 v174, v196, v197\n" \
  "v_cvt_pk_bf16_f32 v175, v198, v199\n" \
  "LS3_%=:\n" \
  "s_waitcnt vmcnt(3)\n" \
  "v_mfma_f32_16x16x32_bf16 v[224:227], v[248:251], v[32:35], 0\n" \
  "s_waitcnt vmcnt(2)\n" \
  "v_mfma_f32_16x16x32_bf16 v[224:227], v[248:251], v[36:39], v[224:227]\n" \
  "s_waitcnt vmcnt(1)\n" \
  "v_mfma_f32_16x16x32_bf16 v[224:227], v[248:251], v[40:43], v[224:227]\n" \
  "s_waitcnt vmcnt(0)\n" \
  "v_mfma_f32_16x16x32_bf16 v[224:227], v[248:251], v[44:47], v[224:227]\n" \
  "s_nop 7\n" \
  "v_cmp_u_f32 vcc, v224, v224\n" \
  "v_add_f32_dpp v224, v224, v224 row_ror:8 row_mask:0xf bank_mask:0xf\n" \
  "v_add_f32_dpp v225, v225, v225 row_ror:8 row_mask:0xf bank_mask:0xf\n" \
  "v_add_f32_dpp v226, v226, v226 row_ror:8 row_mask:0xf bank_mask:0xf\n" \
  "v_add_f32_dpp v227, v227, v227 row_ror:8 row_mask:0xf bank_mask:0xf\n" \
  "v_cvt_pk_bf16_f32 v228, v224, v225\n" \
  "v_cvt_pk_bf16_f32 v229, v226, v227\n" \
  "s_cbranch_vccnz LC_%=\n" \
  "LC2_%=:\n" \
  "v_permlane16_swap_b32 v228, v230\n" \
  "v_permlane16_swap_b32 v229, v231\n" \
  "buffer_store_dwordx4 v[228:231], v12, s[36:39], s53 offen sc1\n" \
  "s_memtime s[84:85]\n" \
  "LK4_%=:\n" \
  "ds_read_b64 v[92:93], v24 offset:14352\n" \
  "s_memtime s[66:67]\n" \
  "s_waitcnt lgkmcnt(0)\n" \
  "v_readfirstlane_b32 s59, v93\n" \
  "v_readfirstlane_b32 s63, v92\n" \
  "s_cmp_lg_u32 s59, s48\n" \
  "s_cbranch_scc1 LK4_%=\n" \
  "s_sub_i32 s59, s63, s64\n" \
  "s_sub_i32 s59, s59, " STR(F_P) "-300\n" \
  "s_sub_i32 s61, s63, s66\n" \
  "s_sub_i32 s60, s63, s84\n" \
  "s_add_i32 s60, s60, " STR(T_LATE) "\n" \
  "s_min_i32 s59, s59, s60\n" \
  "s_cmp_lt_i32 s59, 0\n" \
  "s_cbranch_scc1 LN_%=\n" \
  "LN2_%=:\n" \
  "v_mov_b32 v92, s63\n" \
  "v_mov_b32 v93, 0\n" \
  "ds_write_b64 v24, v[92:93] offset:14336\n" \
  "s_sub_i32 s59, s84, s66\n" \
  "s_add_i32 s59, s59, " STR(M_A) "-50\n" \
  "s_max_i32 s61, s61, s59\n" \
  "s_cmp_gt_i32 s61, 24\n" \
  "s_cbranch_scc1 LW_a_%=\n" \
  "LB_a_%=:\n" \
  "s_waitcnt lgkmcnt(0)\n" \
  "s_branch LL_%=\n" \
  "LO_%=:\n" \
  "s_cmp_eq_u32 %7, 1\n" \
  "s_waitcnt lgkmcnt(0)\n" \
  "s_cbranch_scc1 LK_%=\n" \
  "s_sub_i32 s61, s61, s66\n" \
  "s_max_i32 s61, s61, " STR(M_C) "\n" \
  "s_add_i32 s61, s61, " STR(D_PF) "\n" \
  "s_add_u32 s56, s48, 3\n" \
  "s_min_u32 s56, s56, 64\n" \
  "s_add_u32 s59, s48, 1\n" \
  "v_mov_b32 v28, s59\n" \
  "buffer_store_dword v28, v0, s[44:47], 0 offen sc1\n" \
  "v_cmp_gt_u32 vcc, s56, v23\n" \
  "v_mfma_f32_16x16x32_bf16 v[184:187], v[136:139], a[192:195], 0\n" \
  "v_mov_b64 v[48:49], 0\n" \
  "v_mfma_f32_16x16x32_bf16 v[188:191], v[152:155], a[192:195], 0\n" \
  "v_mov_b64 v[50:51], 0\n" \
  "v_mfma_f32_16x16x32_bf16 v[192:195], v[136:139], a[208:211], 0\n" \
  "v_mov_b64 v[52:53], 0\n" \
  "v_mfma_f32_16x16x32_bf16 v[196:199], v[152:155], a[208:211], 0\n" \
  "v_mov_b64 v[54:55], 0\n" \
  "v_mfma_f32_16x16x32_bf16 v[184:187], v[140:143], a[196:199], v[184:187]\n" \
  "v_mov_b64 v[56:57], 0\n" \
  "v_mfma_f32_16x16x32_bf16 v[188:191], v[156:159], a[196:199], v[188:191]\n" \
  "v_mov_b64 v[58:59], 0\n" \
  "v_mfma_f32_16x16x32_bf16 v[192:195], v[140:143], a[212:215], v[192:195]\n" \
  "v_mov_b64 v[60:61], 0\n" \
  "v_mfma_f32_16x16x32_bf16 v[196:199], v[156:159], a[212:215], v[196:199]\n" \
  "v_mov_b64 v[62:63], 0\n" \
  "v_mfma_f32_16x16x32_bf16 v[184:187], v[144:147], a[200:203], v[184:187]\n" \
  "v_mov_b64 v[64:65], 0\n" \
  "ds_read_b128 v[104:107], v84 offset:0\n" \
  "v_mfma_f32_16x16x32_bf16 v[188:191], v[160:163], a[200:203], v[188:191]\n" \
  "v_mov_b64 v[66:67], 0\n" \
  "ds_read_b128 v[108:111], v85 offset:0\n" \
  "v_mfma_f32_16x16x32_bf16 v[192:195], v[144:147], a[216:219], v[192:195]\n" \
  "v_mov_b64 v[68:69], 0\n" \
  "ds_read_b128 v[112:115], v86 offset:0\n" \
  "v_mfma_f32_16x16x32_bf16 v[196:199], v[160:163], a[216:219], v[196:199]\n" \
  "v_mov_b64 v[70:71], 0\n" \
  "ds_read_b128 v[116:119], v87 offset:0\n" \
  "v_mfma_f32_16x16x32_bf16 v[184:187], v[148:151], a[204:207], v[184:187]\n" \
  "ds_read_b128 v[120:123], v84 offset:4096\n" \
  "v_mfma_f32_16x16x32_bf16 v[188:191], v[164:167], a[204:207], v[188:191]\n" \
  "ds_read_b128 v[124:127], v85 offset:4096\n" \
  "v_mfma_f32_16x16x32_bf16 v[192:195], v[148:151], a[220:223], v[192:195]\n" \
  "ds_read_b128 v[128:131], v86 offset:4096\n" \
  "v_mfma_f32_16x16x32_bf16 v[196:199], v[164:167], a[220:223], v[196:199]\n" \
  "ds_read_b128 v[132:135], v87 offset:4096\n" \
  "s_add_u32 s48, s48, 1\n" \
  "s_sub_u32 s60, s56, 1\n" \
  "s_lshr_b32 s59, s60, 2\n" \
  "s_xnor_b32 s59, s59, %8\n" \
  "s_and_b32 s59, s59, 1\n" \
  "s_lshl_b32 s59, s59, 13\n" \
  "s_lshl_b32 s55, s60, 19\n" \
  "s_or_b32 s55, s55, s59\n" \
  "s_cbranch_vccnz LF_%=\n" \
  "LF2_%=:\n" \
  "s_cmp_gt_i32 s61, 24\n" \
  "s_cbranch_scc1 LW_p_%=\n" \
  "LB_p_%=:\n" \
  "buffer_load_dwordx4 v[200:203], v247, s[40:43], s55 offen offset:0 sc1\n" \
  "buffer_load_dwordx4 v[204:207], v247, s[40:43], s55 offen offset:1024 sc1\n" \
  "buffer_load_dwordx4 v[208:211], v247, s[40:43], s55 offen offset:2048 sc1\n" \
  "buffer_load_dwordx4 v[212:215], v247, s[40:43], s55 offen offset:3072 sc1\n" \
  "buffer_load_dwordx4 v[216:219], v16, s[40:43], s55 offen offset:0 sc1\n" \
  "buffer_load_dwordx4 v[220:223], v16, s[40:43], s55 offen offset:1024 sc1\n" \
  "buffer_load_dwordx4 v[224:227], v16, s[40:43], s55 offen offset:2048 sc1\n" \
  "buffer_load_dwordx4 v[228:231], v16, s[40:43], s55 offen offset:3072 sc1\n" \
  "v_cvt_pk_bf16_f32 v168, v184, v185\n" \
  "v_cvt_pk_bf16_f32 v169, v186, v187\n" \
  "v_cvt_pk_bf16_f32 v170, v188, v189\n" \
  "v_cvt_pk_bf16_f32 v171, v190, v191\n" \
  "v_cvt_pk_bf16_f32 v172, v192, v193\n" \
  "v_cvt_pk_bf16_f32 v173, v194, v195\n" \
  "v_cvt_pk_bf16_f32 v174, v196, v197\n" \
  "v_cvt_pk_bf16_f32 v175, v198, v199\n" \
  "v_mfma_f32_16x16x32_bf16 v[184:187], v[136:139], a[224:227], 0\n" \
  "v_mfma_f32_16x16x32_bf16 v[188:191], v[152:155], a[224:227], 0\n" \
  "v_mfma_f32_16x16x32_bf16 v[192:195], v[136:139], a[240:243], 0\n" \
  "v_mfma_f32_16x16x32_bf16 v[196:199], v[152:155], a[240:243], 0\n" \
  "v_mfma_f32_16x16x32_bf16 v[184:187], v[140:143], a[228:231], v[184:187]\n" \
  "v_mfma_f32_16x16x32_bf16 v[188:191], v[156:159], a[228:231], v[188:191]\n" \
  "v_mfma_f32_16x16x32_bf16 v[192:195], v[140:143], a[244:247], v[192:195]\n" \
  "v_mfma_f32_16x16x32_bf16 v[196:199], v[156:159], a[244:247], v[196:199]\n" \
  "v_mfma_f32_16x16x32_bf16 v[184:187], v[144:147], a[232:235], v[184:187]\n" \
  "v_mfma_f32_16x16x32_bf16 v[188:191], v[160:163], a[232:235], v[188:191]\n" \
  "v_mfma_f32_16x16x32_bf16 v[192:195], v[144:147], a[248:251], v[192:195]\n" \
  "v_mfma_f32_16x16x32_bf16 v[196:199], v[160:163], a[248:251], v[196:199]\n" \
  "v_mfma_f32_16x16x32_bf16 v[184:187], v[148:151], a[236:239], v[184:187]\n" \
  "v_mfma_f32_16x16x32_bf16 v[188:191], v[164:167], a[236:239], v[188:191]\n" \
  "v_mfma_f32_16x16x32_bf16 v[192:195], v[148:151], a[252:255], v[192:195]\n" \
  "v_mfma_f32_16x16x32_bf16 v[196:199], v[164:167], a[252:255], v[196:199]\n" \
  "s_nop 7\n" \
  "v_cvt_pk_bf16_f32 v176, v184, v185\n" \
  "v_cvt_pk_bf16_f32 v177, v186, v187\n" \
  "v_cvt_pk_bf16_f32 v178, v188, v189\n" \
  "v_cvt_pk_bf16_f32 v179, v190, v191\n" \
  "v_cvt_pk_bf16_f32 v180, v192, v193\n" \
  "v_cvt_pk_bf16_f32 v181, v194, v195\n" \
  "v_cvt_pk_bf16_f32 v182, v196, v197\n" \
  "v_cvt_pk_bf16_f32 v183, v198, v199\n" \
  "s_waitcnt lgkmcnt(0)\n" \
  "s_branch LM_%=\n" \
  "LK_%=:\n" \
  "buffer_load_dwordx2 v[92:93], v13, s[36:39], s50 offen sc1\n" \
  "s_mov_b32 s94, s63\n" \
  "v_mfma_f32_16x16x32_bf16 v[184:187], v[136:139], a[192:195], 0\n" \
  "v_mov_b64 v[48:49], 0\n" \
  "v_mfma_f32_16x16x32_bf16 v[188:191], v[152:155], a[192:195], 0\n" \
  "v_mov_b64 v[50:51], 0\n" \
  "v_mfma_f32_16x16x32_bf16 v[192:195], v[136:139], a[208:211], 0\n" \
  "v_mov_b64 v[52:53], 0\n" \
  "v_mfma_f32_16x16x32_bf16 v[196:199], v[152:155], a[208:211], 0\n" \
  "v_mov_b64 v[54:55], 0\n" \
  "v_mfma_f32_16x16x32_bf16 v[184:187], v[140:143], a[196:199], v[184:187]\n" \
  "v_mov_b64 v[56:57], 0\n" \
  "v_mfma_f32_16x16x32_bf16 v[188:191], v[156:159], a[196:199], v[188:191]\n" \
  "v_mov_b64 v[58:59], 0\n" \
  "v_mfma_f32_16x16x32_bf16 v[192:195], v[140:143], a[212:215], v[192:195]\n" \
  "v_mov_b64 v[60:61], 0\n" \
  "v_mfma_f32_16x16x32_bf16 v[196:199], v[156:159], a[212:215], v[196:199]\n" \
  "v_mov_b64 v[62:63], 0\n" \
  "v_mfma_f32_16x16x32_bf16 v[184:187], v[144:147], a[200:203], v[184:187]\n" \
  "v_mov_b64 v[64:65], 0\n" \
  "ds_read_b128 v[104:107], v84 offset:0\n" \
  "v_mfma_f32_16x16x32_bf16 v[188:191], v[160:163], a[200:203], v[188:191]\n" \
  "v_mov_b64 v[66:67], 0\n" \
  "ds_read_b128 v[108:111], v85 offset:0\n" \
  "v_mfma_f32_16x16x32_bf16 v[192:195], v[144:147], a[216:219], v[192:195]\n" \
  "v_mov_b64 v[68:69], 0\n" \
  "ds_read_b128 v[112:115], v86 offset:0\n" \
  "v_mfma_f32_16x16x32_bf16 v[196:199], v[160:163], a[216:219], v[196:199]\n" \
  "v_mov_b64 v[70:71], 0\n" \
  "ds_read_b128 v[116:119], v87 offset:0\n" \
  "v_mfma_f32_16x16x32_bf16 v[184:187], v[148:151], a[204:207], v[184:187]\n" \
  "ds_read_b128 v[120:123], v84 offset:4096\n" \
  "v_mfma_f32_16x16x32_bf16 v[188:191], v[164:167], a[204:207], v[188:191]\n" \
  "ds_read_b128 v[124:127], v85 offset:4096\n" \
  "v_mfma_f32_16x16x32_bf16 v[192:195], v[148:151], a[220:223], v[192:195]\n" \
  "ds_read_b128 v[128:131], v86 offset:4096\n" \
  "v_mfma_f32_16x16x32_bf16 v[196:199], v[164:167], a[220:223], v[196:199]\n" \
  "ds_read_b128 v[132:135], v87 offset:4096\n" \
  "s_add_u32 s48, s48, 1\n" \
  "s_nop 5\n" \
  "v_cvt_pk_bf16_f32 v168, v184, v185\n" \
  "v_cvt_pk_bf16_f32 v169, v186, v187\n" \
  "v_cvt_pk_bf16_f32 v170, v188, v189\n" \
  "v_cvt_pk_bf16_f32 v171, v190, v191\n" \
  "v_cvt_pk_bf16_f32 v172, v192, v193\n" \
  "v_cvt_pk_bf16_f32 v173, v194, v195\n" \
  "v_cvt_pk_bf16_f32 v174, v196, v197\n" \
  "v_cvt_pk_bf16_f32 v175, v198, v199\n" \
  "s_nop " STR(D_K) "\n" \
  "s_waitcnt lgkmcnt(0)\n" \
  "s_memtime s[72:73]\n" \
  "s_memrealtime s[74:75]\n" \
  "s_nop 1\n" \
  "s_memrealtime s[76:77]\n" \
  "s_nop 1\n" \
  "s_memrealtime s[78:79]\n" \
  "s_nop 1\n" \
  "s_memrealtime s[80:81]\n" \
  "s_nop 1\n" \
  "s_memrealtime s[82:83]\n" \
  "s_nop 1\n" \
  "s_memrealtime s[84:85]\n" \
  "s_nop 1\n" \
  "s_memrealtime s[86:87]\n" \
  "s_nop 1\n" \
  "s_memrealtime s[88:89]\n" \
  "s_nop 1\n" \
  "s_memrealtime s[90:91]\n" \
  "s_mov_b32 s59, 0\n" \
  "s_waitcnt lgkmcnt(0)\n" \
  "s_cmp_eq_u32 s76, s74\n" \
  "s_addc_u32 s59, s59, 0\n" \
  "s_cmp_eq_u32 s78, s74\n" \
  "s_addc_u32 s59, s59, 0\n" \
  "s_cmp_eq_u32 s80, s74\n" \
  "s_addc_u32 s59, s59, 0\n" \
  "s_cmp_eq_u32 s82, s74\n" \
  "s_addc_u32 s59, s59, 0\n" \
  "s_cmp_eq_u32 s84, s74\n" \
  "s_addc_u32 s59, s59, 0\n" \
  "s_cmp_eq_u32 s86, s74\n" \
  "s_addc_u32 s59, s59, 0\n" \
  "s_cmp_eq_u32 s88, s74\n" \
  "s_addc_u32 s59, s59, 0\n" \
  "s_cmp_eq_u32 s90, s74\n" \
  "s_addc_u32 s59, s59, 0\n" \
  "s_mul_i32 s59, s59, 12\n" \
  "s_add_u32 s68, s72, s59\n" \
  "s_add_u32 s59, s74, 4\n" \
  "s_mul_i32 s69, s59, 24\n" \
  "s_sub_u32 s60, s69, s62\n" \
  "s_sub_u32 s59, s68, s56\n" \
  "s_mov_b32 s62, s69\n" \
  "s_mov_b32 s56, s68\n" \
  "v_cvt_f32_u32 v27, s59\n" \
  "v_cvt_f32_u32 v28, s60\n" \
  "v_rcp_f32 v26, v27\n" \
  "v_rcp_f32 v19, v28\n" \
  "v_mul_f32 v26, v26, v28\n" \
  "v_mul_f32 v95, " STR(DECAY) ", v95\n" \
  "v_mul_f32 v19, v19, v27\n" \
  "v_max_f32 v95, v95, v26\n" \
  "v_cvt_f32_u32 v28, " STR(F_P) "\n" \
  "v_mul_f32 v27, v95, v28\n" \
  "v_cvt_u32_f32 v27, v27\n" \
  "v_min_u32 v27, 0x7fff, v27\n" \
  "s_nop 0\n" \
  "v_readfirstlane_b32 s63, v27\n" \
  "LK2_%=:\n" \
  "s_waitcnt vmcnt(0)\n" \
  "v_max_u32 v26, v92, v93\n" \
  "v_cmp_eq_u32 vcc, -1, v26\n" \
  "v_subrev_u32 v25, s62, v92\n" \
  "v_subrev_u32 v26, s94, v93\n" \
  "s_nop 0\n" \
  "v_max_i32_dpp v25, v25, v25 quad_perm:[1,0,3,2] row_mask:0xf bank_mask:0xf\n" \
  "v_max_i32_dpp v26, v26, v26 quad_perm:[1,0,3,2] row_mask:0xf bank_mask:0xf\n" \
  "s_cbranch_vccnz LK3_%=\n" \
  "v_max_i32_dpp v25, v25, v25 quad_perm:[2,3,0,1] row_mask:0xf bank_mask:0xf\n" \
  "v_max_i32_dpp v26, v26, v26 quad_perm:[2,3,0,1] row_mask:0xf bank_mask:0xf\n" \
  "LK5_%=:\n" \
  "v_cvt_f32_i32 v25, v25\n" \
  "v_mov_b32 v93, s48\n" \
  "v_mul_f32 v25, v25, v19\n" \
  "v_cvt_i32_f32 v25, v25\n" \
  "v_add_u32 v92, s68, v25\n" \
  "ds_write_b64 v24, v[92:93] offset:14352\n" \
  "v_max_i32 v26, 0, v26\n" \
  "v_min_i32 v26, 400, v26\n" \
  "ds_write_b32 v24, v26 offset:14344\n" \
  "s_waitcnt lgkmcnt(0)\n" \
  "s_branch LL_%=\n" \
  ".p2align 6\n" \
  "LM_%=:\n" \
  "s_barrier\n" \
  "buffer_load_dwordx4 v[32:35], v80, s[36:39], s49 offen sc1\n" \
  "s_xor_b32 s51, s51, 1<<20\n" \
  "s_xor_b32 s52, s52, 1<<20\n" \
  "s_add_u32 s53, s53, 32768\n" \
  "s_lshl_b32 s60, s48, 19\n" \
  "s_add_u32 s60, s60, " STR(EXP_B) "<<21\n" \
  "s_lshr_b32 s59, s60, 21\n" \
  "s_lshr_b32 s59, %8, s59\n" \
  "s_and_b32 s94, s59, 1\n" \
  "s_nop 3\n" \
  "buffer_load_dwordx4 v[36:39], v81, s[36:39], s49 offen sc1\n" \
  "s_lshl_b32 s94, s94, 13\n" \
  "s_or_b32 s50, s60, s94\n" \
  "s_lshl_b32 s94, s70, 14\n" \
  "s_add_u32 s91, s50, s94\n" \
  "s_and_b32 s94, s59, 2\n" \
  "s_lshl_b32 s94, s94, 12\n" \
  "s_add_u32 s60, s60, 1<<21\n" \
  "s_or_b32 s54, s60, s94\n" \
  "s_nop 3\n" \
  "buffer_load_dwordx4 v[40:43], v82, s[36:39], s49 offen sc1\n" \
  "s_xor_b32 s59, %7, 3\n" \
  "s_lshl_b32 s59, s59, 21\n" \
  "s_add_u32 s59, s59, s53\n" \
  "s_add_u32 s59, s59, 4<<15\n" \
  "s_cmp_lt_u32 s59, 1<<21\n" \
  "s_cselect_b32 s59, s59, 1<<30\n" \
  "s_nop 5\n" \
  "buffer_load_dwordx4 v[44:47], v83, s[36:39], s49 offen sc1\n" \
  "s_waitcnt vmcnt(4)\n" \
  "ds_write_b128 v18, v[200:203] offset:0\n" \
  "ds_write_b128 v18, v[204:207] offset:1024\n" \
  "ds_write_b128 v18, v[208:211] offset:2048\n" \
  "ds_write_b128 v18, v[212:215] offset:3072\n" \
  "v_add_u32 v27, s59, v12\n" \
  "buffer_store_dwordx4 v[88:91], v11, s[36:39], s52 offen sc1\n" \
  "buffer_store_dwordx4 v[88:91], v15, s[36:39], s54 offen sc1\n" \
  "buffer_store_dwordx4 v[88:91], v27, s[36:39], 0 offen sc1\n" \
  "buffer_load_dword v23, v22, s[44:47], 0 offen sc1\n" \
  "ds_write_b128 v18, v[216:219] offset:4096\n" \
  "ds_write_b128 v18, v[220:223] offset:5120\n" \
  "ds_write_b128 v18, v[224:227] offset:6144\n" \
  "ds_write_b128 v18, v[228:231] offset:7168\n" \
  "s_branch LS1_%=\n" \
  "LA_%=:\n" \
  "ds_read_b64 v[92:93], v24 offset:14336\n" \
  "v_max3_u32 v25, v32, v33, v34\n" \
  "v_max3_u32 v25, v25, v35, v36\n" \
  "v_max3_u32 v25, v25, v37, v38\n" \
  "v_max3_u32 v25, v25, v39, v40\n" \
  "v_max3_u32 v25, v25, v41, v42\n" \
  "v_max3_u32 v25, v25, v43, v44\n" \
  "v_max3_u32 v25, v25, v45, v46\n" \
  "v_max_u32 v25, v25, v47\n" \
  "v_cmp_eq_u32 vcc, -1, v25\n" \
  "s_cbranch_vccz LA4_%=\n" \
  "s_add_u32 s58, s58, 1\n" \
  "s_cmp_lt_u32 s58, " STR(SPIN_CAP) "\n" \
  "s_cbranch_scc1 LA1_%=\n" \
  "v_mov_b32 v27, " STR(CW_DEAD) "*4\n" \
  "v_mov_b32 v28, 1\n" \
  "buffer_store_dword v28, v27, s[44:47], 0 offen sc1\n" \
  "s_branch LA4_%=\n" \
  "LA1_%=:\n" \
  "s_waitcnt lgkmcnt(0)\n" \
  "v_readfirstlane_b32 s64, v92\n" \
  "s_memtime s[66:67]\n" \
  "s_waitcnt lgkmcnt(0)\n" \
  "s_sub_u32 s59, " STR(O_A) ", s66\n" \
  "LQ_ra_%=:\n" \
  "s_add_u32 s64, s64, s95\n" \
  "s_add_i32 s61, s64, s59\n" \
  "s_cmp_lt_i32 s61, -" STR(T_TOL) "\n" \
  "s_cbranch_scc1 LQ_ra_%=\n" \
  "s_max_i32 s61, s61, 0\n" \
  "s_min_i32 s61, s61, 4000\n" \
  "s_bitcmp0_b32 s61, 4\n" \
  "s_cbranch_scc1 LX_ra_%=\n" \
  "s_nop 8\n" \
  "LX_ra_%=:\n" \
  "s_lshr_b32 s61, s61, 5\n" \
  "s_cbranch_scc0 LB_ra_%=\n" \
  ".p2align 4\n" \
  "LY_ra_%=:\n" \
  "s_sub_u32 s61, s61, 1\n" \
  "s_cmp_lg_u32 s61, 0\n" \
  "s_cbranch_scc1 LY_ra_%=\n" \
  "LB_ra_%=:\n" \
  "buffer_load_dwordx4 v[32:35], v80, s[36:39], s49 offen sc1\n" \
  "s_nop 11\n" \
  "buffer_load_dwordx4 v[36:39], v81, s[36:39], s49 offen sc1\n" \
  "s_nop 11\n" \
  "buffer_load_dwordx4 v[40:43], v82, s[36:39], s49 offen sc1\n" \
  "s_nop 11\n" \
  "buffer_load_dwordx4 v[44:47], v83, s[36:39], s49 offen sc1\n" \
  "v_mov_b64 v[48:49], 0\n" \
  "v_mov_b64 v[50:51], 0\n" \
  "v_mov_b64 v[52:53], 0\n" \
  "v_mov_b64 v[54:55], 0\n" \
  "v_mov_b64 v[56:57], 0\n" \
  "v_mov_b64 v[58:59], 0\n" \
  "v_mov_b64 v[60:61], 0\n" \
  "v_mov_b64 v[62:63], 0\n" \
  "v_mov_b64 v[64:65], 0\n" \
  "v_mov_b64 v[66:67], 0\n" \
  "v_mov_b64 v[68:69], 0\n" \
  "v_mov_b64 v[70:71], 0\n" \
  "buffer_store_dword v27, v88, s[36:39], 0 offen sc1\n" \
  "buffer_store_dword v27, v88, s[36:39], 0 offen sc1\n" \
  "buffer_store_dword v27, v88, s[36:39], 0 offen sc1\n" \
  "buffer_store_dword v27, v88, s[36:39], 0 offen sc1\n" \
  "v_sub_u32 v27, s64, v92\n" \
  "ds_max_u32 v24, v27 offset:14340\n" \
  "s_branch LS1_%=\n" \
  "LW_b_%=:\n" \
  "s_min_i32 s61, s61, 4000\n" \
  "s_sub_i32 s61, s61, 72\n" \
  "s_max_i32 s61, s61, 0\n" \
  "s_bitcmp0_b32 s61, 4\n" \
  "s_cbranch_scc1 LX_b_%=\n" \
  "s_nop 8\n" \
  "LX_b_%=:\n" \
  "s_lshr_b32 s61, s61, 5\n" \
  "s_cbranch_scc0 LB_b_%=\n" \
  ".p2align 4\n" \
  "LY_b_%=:\n" \
  "s_sub_u32 s61, s61, 1\n" \
  "s_cmp_lg_u32 s61, 0\n" \
  "s_cbranch_scc1 LY_b_%=\n" \
  "s_branch LB_b_%=\n" \
  "LBE_%=:\n" \
  "v_max3_u32 v26, v32, v33, v34\n" \
  "v_max3_u32 v26, v26, v35, v36\n" \
  "v_max3_u32 v26, v26, v37, v38\n" \
  "v_max3_u32 v26, v26, v39, v40\n" \
  "v_max3_u32 v26, v26, v41, v42\n" \
  "v_max3_u32 v26, v26, v43, v44\n" \
  "v_max3_u32 v26, v26, v45, v46\n" \
  "v_max_u32 v26, v26, v47\n" \
  "v_cmp_eq_u32 vcc, -1, v26\n" \
  "s_cbranch_vccz LBH_%=\n" \
  "s_add_u32 s58, s58, 1\n" \
  "s_cmp_lt_u32 s58, " STR(SPIN_CAP) "\n" \
  "s_cbranch_scc1 LB1_%=\n" \
  "v_mov_b32 v27, " STR(CW_DEAD) "*4\n" \
  "v_mov_b32 v28, 1\n" \
  "buffer_store_dword v28, v27, s[44:47], 0 offen sc1\n" \
  "s_branch LBI_%=\n" \
  "LBI_%=:\n" \
  "v_mov_b32 v27, v25\n" \
  "s_nop 1\n" \
  "s_branch LBH_%=\n" \
  "LBF_%=:\n" \
  "v_max3_u32 v25, v32, v33, v34\n" \
  "v_max3_u32 v25, v25, v35, v36\n" \
  "v_max3_u32 v25, v25, v37, v38\n" \
  "v_max3_u32 v25, v25, v39, v40\n" \
  "v_max3_u32 v25, v25, v41, v42\n" \
  "v_max3_u32 v25, v25, v43, v44\n" \
  "v_max3_u32 v25, v25, v45, v46\n" \
  "v_max_u32 v25, v25, v47\n" \
  "v_max3_u32 v25, v25, v96, v97\n" \
  "v_max3_u32 v25, v25, v98, v99\n" \
  "v_max3_u32 v25, v25, v100, v101\n" \
  "v_max3_u32 v25, v25, v102, v103\n" \
  "v_cmp_eq_u32 vcc, -1, v25\n" \
  "s_cbranch_vccz LBG_%=\n" \
  "s_add_u32 s58, s58, 1\n" \
  "s_cmp_lt_u32 s58, " STR(SPIN_CAP) "\n" \
  "s_cbranch_scc1 LB1_%=\n" \
  "v_mov_b32 v27, " STR(CW_DEAD) "*4\n" \
  "v_mov_b32 v28, 1\n" \
  "buffer_store_dword v28, v27, s[44:47], 0 offen sc1\n" \
  "s_branch LBG_%=\n" \
  "LB1_%=:\n" \
  "s_waitcnt vmcnt(0)\n" \
  "v_mov_b64 v[184:185], 0\n" \
  "v_mov_b64 v[186:187], 0\n" \
  "v_mov_b64 v[188:189], 0\n" \
  "v_mov_b64 v[190:191], 0\n" \
  "v_mov_b64 v[192:193], 0\n" \
  "v_mov_b64 v[194:195], 0\n" \
  "v_mov_b64 v[196:197], 0\n" \
  "v_mov_b64 v[198:199], 0\n" \
  "s_memtime s[66:67]\n" \
  "s_waitcnt lgkmcnt(0)\n" \
  "s_sub_u32 s59, " STR(F_B) "+" STR(O_B) ", s66\n" \
  "LQ_rb_%=:\n" \
  "s_add_u32 s64, s64, s95\n" \
  "s_add_i32 s61, s64, s59\n" \
  "s_cmp_lt_i32 s61, -" STR(T_TOL) "\n" \
  "s_cbranch_scc1 LQ_rb_%=\n" \
  "s_max_i32 s61, s61, 0\n" \
  "s_min_i32 s61, s61, 4000\n" \
  "s_bitcmp0_b32 s61, 4\n" \
  "s_cbranch_scc1 LX_rb_%=\n" \
  "s_nop 8\n" \
  "LX_rb_%=:\n" \
  "s_lshr_b32 s61, s61, 5\n" \
  "s_cbranch_scc0 LB_rb_%=\n" \
  ".p2align 4\n" \
  "LY_rb_%=:\n" \
  "s_sub_u32 s61, s61, 1\n" \
  "s_cmp_lg_u32 s61, 0\n" \
  "s_cbranch_scc1 LY_rb_%=\n" \
  "LB_rb_%=:\n" \
  "buffer_load_dwordx4 v[32:35], v80, s[36:39], s91 offen sc1\n" \
  "s_nop 11\n" \
  "buffer_load_dwordx4 v[36:39], v81, s[36:39], s91 offen sc1\n" \
  "s_nop 11\n" \
  "buffer_load_dwordx4 v[40:43], v82, s[36:39], s91 offen sc1\n" \
  "s_nop 11\n" \
  "buffer_load_dwordx4 v[44:47], v83, s[36:39], s91 offen sc1\n" \
  "s_nop 11\n" \
  "buffer_load_dwordx4 v[96:99], v17, s[36:39], s50 offen offset:0 sc1\n" \
  "s_nop 11\n" \
  "buffer_load_dwordx4 v[100:103], v17, s[36:39], s50 offen offset:1024 sc1\n" \
  "buffer_store_dword v27, v88, s[36:39], 0 offen sc1\n" \
  "s_mov_b32 vcc_lo, 0x33333333\n" \
  "s_mov_b32 vcc_hi, 0x33333333\n" \
  "s_branch LS2_%=\n" \
  "LW_c_%=:\n" \
  "s_min_i32 s61, s61, 4000\n" \
  "s_sub_i32 s61, s61, 72\n" \
  "s_max_i32 s61, s61, 0\n" \
  "s_bitcmp0_b32 s61, 4\n" \
  "s_cbranch_scc1 LX_c_%=\n" \
  "s_nop 8\n" \
  "LX_c_%=:\n" \
  "s_lshr_b32 s61, s61, 5\n" \
  "s_cbranch_scc0 LB_c_%=\n" \
  ".p2align 4\n" \
  "LY_c_%=:\n" \
  "s_sub_u32 s61, s61, 1\n" \
  "s_cmp_lg_u32 s61, 0\n" \
  "s_cbranch_scc1 LY_c_%=\n" \
  "s_branch LB_c_%=\n" \
  "LW_a_%=:\n" \
  "s_min_i32 s61, s61, 4000\n" \
  "s_sub_i32 s61, s61, 72\n" \
  "s_max_i32 s61, s61, 0\n" \
  "s_bitcmp0_b32 s61, 4\n" \
  "s_cbranch_scc1 LX_a_%=\n" \
  "s_nop 8\n" \
  "LX_a_%=:\n" \
  "s_lshr_b32 s61, s61, 5\n" \
  "s_cbranch_scc0 LB_a_%=\n" \
  ".p2align 4\n" \
  "LY_a_%=:\n" \
  "s_sub_u32 s61, s61, 1\n" \
  "s_cmp_lg_u32 s61, 0\n" \
  "s_cbranch_scc1 LY_a_%=\n" \
  "s_branch LB_a_%=\n" \
  "LN_%=:\n" \
  "LN3_%=:\n" \
  "s_add_u32 s63, s63, s95\n" \
  "s_add_i32 s61, s61, s95\n" \
  "s_add_i32 s59, s59, s95\n" \
  "s_cmp_lt_i32 s59, 0\n" \
  "s_cbranch_scc1 LN3_%=\n" \
  "s_branch LN2_%=\n" \
  "LC_%=:\n" \
  "v_max3_u32 v25, v32, v33, v34\n" \
  "v_max3_u32 v25, v25, v35, v36\n" \
  "v_max3_u32 v25, v25, v37, v38\n" \
  "v_max3_u32 v25, v25, v39, v40\n" \
  "v_max3_u32 v25, v25, v41, v42\n" \
  "v_max3_u32 v25, v25, v43, v44\n" \
  "v_max3_u32 v25, v25, v45, v46\n" \
  "v_max_u32 v25, v25, v47\n" \
  "v_cmp_eq_u32 vcc, -1, v25\n" \
  "s_cbranch_vccz LC2_%=\n" \
  "s_add_u32 s58, s58, 1\n" \
  "s_cmp_lt_u32 s58, " STR(SPIN_CAP) "\n" \
  "s_cbranch_scc1 LC1_%=\n" \
  "v_mov_b32 v27, " STR(CW_DEAD) "*4\n" \
  "v_mov_b32 v28, 1\n" \
  "buffer_store_dword v28, v27, s[44:47], 0 offen sc1\n" \
  "s_branch LC2_%=\n" \
  "LC1_%=:\n" \
  "s_add_u32 s60, s96, " STR(O_C) "\n" \
  "s_memtime s[66:67]\n" \
  "s_waitcnt lgkmcnt(0)\n" \
  "s_sub_u32 s59, s60, s66\n" \
  "LQ_rc_%=:\n" \
  "s_add_u32 s64, s64, s95\n" \
  "s_add_i32 s61, s64, s59\n" \
  "s_cmp_lt_i32 s61, -" STR(T_TOL) "\n" \
  "s_cbranch_scc1 LQ_rc_%=\n" \
  "s_max_i32 s61, s61, 0\n" \
  "s_min_i32 s61, s61, 4000\n" \
  "s_bitcmp0_b32 s61, 4\n" \
  "s_cbranch_scc1 LX_rc_%=\n" \
  "s_nop 8\n" \
  "LX_rc_%=:\n" \
  "s_lshr_b32 s61, s61, 5\n" \
  "s_cbranch_scc0 LB_rc_%=\n" \
  ".p2align 4\n" \
  "LY_rc_%=:\n" \
  "s_sub_u32 s61, s61, 1\n" \
  "s_cmp_lg_u32 s61, 0\n" \
  "s_cbranch_scc1 LY_rc_%=\n" \
  "LB_rc_%=:\n" \
  "buffer_load_dwordx4 v[32:35], v11, s[36:39], s51 offen offset:0 sc1\n" \
  "buffer_load_dwordx4 v[36:39], v11, s[36:39], s51 offen offset:1024 sc1\n" \
  "buffer_load_dwordx4 v[40:43], v11, s[36:39], s51 offen offset:2048 sc1\n" \
  "buffer_load_dwordx4 v[44:47], v11, s[36:39], s51 offen offset:3072 sc1\n" \
  "s_branch LS3_%=\n" \
  "LW_p_%=:\n" \
  "s_min_i32 s61, s61, 4000\n" \
  "s_sub_i32 s61, s61, 72\n" \
  "s_max_i32 s61, s61, 0\n" \
  "s_bitcmp0_b32 s61, 4\n" \
  "s_cbranch_scc1 LX_p_%=\n" \
  "s_nop 8\n" \
  "LX_p_%=:\n" \
  "s_lshr_b32 s61, s61, 5\n" \
  "s_cbranch_scc0 LB_p_%=\n" \
  ".p2align 4\n" \
  "LY_p_%=:\n" \
  "s_sub_u32 s61, s61, 1\n" \
  "s_cmp_lg_u32 s61, 0\n" \
  "s_cbranch_scc1 LY_p_%=\n" \
  "s_branch LB_p_%=\n" \
  "LF_%=:\n" \
  "buffer_load_dword v23, v22, s[44:47], 0 offen sc1\n" \
  "s_mov_b32 s61, 0\n" \
  "s_waitcnt vmcnt(0)\n" \
  "v_cmp_gt_u32 vcc, s56, v23\n" \
  "s_cbranch_vccz LF2_%=\n" \
  "s_sleep 2\n" \
  "s_add_u32 s58, s58, 1\n" \
  "s_cmp_lt_u32 s58, " STR(SPIN_CAP) "\n" \
  "s_cbranch_scc1 LF_%=\n" \
  "v_mov_b32 v27, " STR(CW_DEAD) "*4\n" \
  "v_mov_b32 v28, 1\n" \
  "buffer_store_dword v28, v27, s[44:47], 0 offen sc1\n" \
  "s_branch LF2_%=\n" \
  "LK3_%=:\n" \
  "s_sleep 2\n" \
  "buffer_load_dwordx2 v[92:93], v13, s[36:39], s50 offen sc1\n" \
  "s_add_u32 s58, s58, 1\n" \
  "s_cmp_lt_u32 s58, " STR(SPIN_CAP) "\n" \
  "s_cbranch_scc1 LK2_%=\n" \
  "v_mov_b32 v27, " STR(CW_DEAD) "*4\n" \
  "v_mov_b32 v28, 1\n" \
  "buffer_store_dword v28, v27, s[44:47], 0 offen sc1\n" \
  "s_branch LK6_%=\n" \
  "LK6_%=:\n" \
  "v_mov_b32 v25, 0\n" \
  "s_branch LK5_%=\n" \
  "LE_%=:\n" \
  "s_add_u32 s59, s48, 1\n" \
  "v_mov_b32 v28, s59\n" \
  "buffer_store_dword v28, v0, s[44:47], 0 offen sc1\n" \
  "s_endpgm\n" \
  ""

#define GEMM_ASM \
  "v_mbcnt_lo_u32_b32 v4, -1, 0\n" \
  "v_mbcnt_hi_u32_b32 v4, -1, v4\n" \
  "v_and_b32 v116, 15, v4\n" \
  "v_lshrrev_b32 v117, 4, v4\n" \
  "s_lshl_b32 s86, %7, 13\n" \
  "s_add_u32 s86, s86, %8\n" \
  "v_lshrrev_b32 v5, 3, v4\n" \
  "v_and_b32 v6, 7, v4\n" \
  "v_lshlrev_b32 v8, 13, v5\n" \
  "v_lshl_add_u32 v8, v6, 4, v8\n" \
  "v_and_b32 v9, 1, v117\n" \
  "v_lshlrev_b32 v9, 5, v9\n" \
  "v_xor_b32 v8, v8, v9\n" \
  "v_xor_b32 v9, 64, v8\n" \
  "s_mov_b64 s[68:69], %1\n" \
  "s_and_b32 s69, s69, 0xffff\n" \
  "s_mov_b32 s70, 8388608\n" \
  "s_mov_b32 s71, 0x00020000\n" \
  "s_lshl_b32 s92, %7, 19\n" \
  "s_add_u32 s92, s92, 0\n" \
  "s_add_u32 m0, s86, 98304\n" \
  "s_nop 0\n" \
  "buffer_load_dwordx4 v8, s[68:71], s92 offen lds\n" \
  "s_add_u32 m0, s86, 99328\n" \
  "s_add_u32 s92, s92, 65536\n" \
  "buffer_load_dwordx4 v9, s[68:71], s92 offen lds\n" \
  "s_add_u32 m0, s86, 100352\n" \
  "s_add_u32 s92, s92, 65536\n" \
  "buffer_load_dwordx4 v8, s[68:71], s92 offen lds\n" \
  "s_add_u32 m0, s86, 101376\n" \
  "s_add_u32 s92, s92, 65536\n" \
  "buffer_load_dwordx4 v9, s[68:71], s92 offen lds\n" \
  "s_add_u32 m0, s86, 102400\n" \
  "s_add_u32 s92, s92, 65536\n" \
  "buffer_load_dwordx4 v8, s[68:71], s92 offen lds\n" \
  "s_add_u32 m0, s86, 103424\n" \
  "s_add_u32 s92, s92, 65536\n" \
  "buffer_load_dwordx4 v9, s[68:71], s92 offen lds\n" \
  "s_add_u32 m0, s86, 104448\n" \
  "s_add_u32 s92, s92, 65536\n" \
  "buffer_load_dwordx4 v8, s[68:71], s92 offen lds\n" \
  "s_add_u32 m0, s86, 105472\n" \
  "s_add_u32 s92, s92, 65536\n" \
  "buffer_load_dwordx4 v9, s[68:71], s92 offen lds\n" \
  "s_lshl_b32 s92, %7, 19\n" \
  "s_add_u32 s92, s92, 2097152\n" \
  "s_add_u32 m0, s86, 131072\n" \
  "s_nop 0\n" \
  "buffer_load_dwordx4 v8, s[68:71], s92 offen lds\n" \
  "s_add_u32 m0, s86, 132096\n" \
  "s_add_u32 s92, s92, 65536\n" \
  "buffer_load_dwordx4 v9, s[68:71], s92 offen lds\n" \
  "s_add_u32 m0, s86, 133120\n" \
  "s_add_u32 s92, s92, 65536\n" \
  "buffer_load_dwordx4 v8, s[68:71], s92 offen lds\n" \
  "s_add_u32 m0, s86, 134144\n" \
  "s_add_u32 s92, s92, 65536\n" \
  "buffer_load_dwordx4 v9, s[68:71], s92 offen lds\n" \
  "s_add_u32 m0, s86, 135168\n" \
  "s_add_u32 s92, s92, 65536\n" \
  "buffer_load_dwordx4 v8, s[68:71], s92 offen lds\n" \
  "s_add_u32 m0, s86, 136192\n" \
  "s_add_u32 s92, s92, 65536\n" \
  "buffer_load_dwordx4 v9, s[68:71], s92 offen lds\n" \
  "s_add_u32 m0, s86, 137216\n" \
  "s_add_u32 s92, s92, 65536\n" \
  "buffer_load_dwordx4 v8, s[68:71], s92 offen lds\n" \
  "s_add_u32 m0, s86, 138240\n" \
  "s_add_u32 s92, s92, 65536\n" \
  "buffer_load_dwordx4 v9, s[68:71], s92 offen lds\n" \
  "s_lshl_b32 s92, %7, 19\n" \
  "s_add_u32 s92, s92, 4194304\n" \
  "s_add_u32 m0, s86, 0\n" \
  "s_nop 0\n" \
  "buffer_load_dwordx4 v8, s[68:71], s92 offen lds\n" \
  "s_add_u32 m0, s86, 1024\n" \
  "s_add_u32 s92, s92, 65536\n" \
  "buffer_load_dwordx4 v9, s[68:71], s92 offen lds\n" \
  "s_add_u32 m0, s86, 2048\n" \
  "s_add_u32 s92, s92, 65536\n" \
  "buffer_load_dwordx4 v8, s[68:71], s92 offen lds\n" \
  "s_add_u32 m0, s86, 3072\n" \
  "s_add_u32 s92, s92, 65536\n" \
  "buffer_load_dwordx4 v9, s[68:71], s92 offen lds\n" \
  "s_add_u32 m0, s86, 4096\n" \
  "s_add_u32 s92, s92, 65536\n" \
  "buffer_load_dwordx4 v8, s[68:71], s92 offen lds\n" \
  "s_add_u32 m0, s86, 5120\n" \
  "s_add_u32 s92, s92, 65536\n" \
  "buffer_load_dwordx4 v9, s[68:71], s92 offen lds\n" \
  "s_add_u32 m0, s86, 6144\n" \
  "s_add_u32 s92, s92, 65536\n" \
  "buffer_load_dwordx4 v8, s[68:71], s92 offen lds\n" \
  "s_add_u32 m0, s86, 7168\n" \
  "s_add_u32 s92, s92, 65536\n" \
  "buffer_load_dwordx4 v9, s[68:71], s92 offen lds\n" \
  "s_lshl_b32 s92, %7, 19\n" \
  "s_add_u32 s92, s92, 6291456\n" \
  "s_add_u32 m0, s86, 32768\n" \
  "s_nop 0\n" \
  "buffer_load_dwordx4 v8, s[68:71], s92 offen lds\n" \
  "s_add_u32 m0, s86, 33792\n" \
  "s_add_u32 s92, s92, 65536\n" \
  "buffer_load_dwordx4 v9, s[68:71], s92 offen lds\n" \
  "s_add_u32 m0, s86, 34816\n" \
  "s_add_u32 s92, s92, 65536\n" \
  "buffer_load_dwordx4 v8, s[68:71], s92 offen lds\n" \
  "s_add_u32 m0, s86, 35840\n" \
  "s_add_u32 s92, s92, 65536\n" \
  "buffer_load_dwordx4 v9, s[68:71], s92 offen lds\n" \
  "s_add_u32 m0, s86, 36864\n" \
  "s_add_u32 s92, s92, 65536\n" \
  "buffer_load_dwordx4 v8, s[68:71], s92 offen lds\n" \
  "s_add_u32 m0, s86, 37888\n" \
  "s_add_u32 s92, s92, 65536\n" \
  "buffer_load_dwordx4 v9, s[68:71], s92 offen lds\n" \
  "s_add_u32 m0, s86, 38912\n" \
  "s_add_u32 s92, s92, 65536\n" \
  "buffer_load_dwordx4 v8, s[68:71], s92 offen lds\n" \
  "s_add_u32 m0, s86, 39936\n" \
  "s_add_u32 s92, s92, 65536\n" \
  "buffer_load_dwordx4 v9, s[68:71], s92 offen lds\n" \
  "s_mov_b64 s[72:73], %3\n" \
  "s_and_b32 s73, s73, 0xffff\n" \
  "s_mov_b32 s74, 4194304\n" \
  "s_mov_b32 s75, 0x00020000\n" \
  "s_mov_b64 s[76:77], %4\n" \
  "s_and_b32 s77, s77, 0xffff\n" \
  "s_mov_b32 s78, 3*" STR(QKV_SZ32) "\n" \
  "s_mov_b32 s79, 0x00020000\n" \
  "s_mov_b64 s[80:81], %5\n" \
  "s_and_b32 s81, s81, 0xffff\n" \
  "s_mov_b32 s82, " STR(CTRL_BYTES) "\n" \
  "s_mov_b32 s83, 0x00020000\n" \
  "v_xor_b32 v6, v6, v117\n" \
  "s_and_b32 s84, %7, 1\n" \
  "s_lshl_b32 s84, s84, 2\n" \
  "v_xor_b32 v6, s84, v6\n" \
  "v_lshlrev_b32 v7, 11, v5\n" \
  "v_lshl_add_u32 v7, v6, 4, v7\n" \
  "s_lshl_b32 s87, %7, 14\n" \
  "s_add_u32 s96, s87, 63*65536\n" \
  "v_lshrrev_b32 v5, 1, v116\n" \
  "v_xor_b32 v5, v5, v117\n" \
  "v_lshlrev_b32 v5, 4, v5\n" \
  "v_lshl_add_u32 v5, v116, 7, v5\n" \
  "v_add_u32 v10, %8, v5\n" \
  "v_xor_b32 v5, 64, v5\n" \
  "v_add_u32 v11, %8, v5\n" \
  "v_add_u32 v12, 0x10000, v10\n" \
  "v_add_u32 v13, 0x10000, v11\n" \
  "s_lshr_b32 s84, %6, 1\n" \
  "s_lshl_b32 s84, s84, 14\n" \
  "s_and_b32 s85, %6, 1\n" \
  "s_lshl_b32 s85, s85, 2\n" \
  "s_add_u32 s85, s85, %7\n" \
  "s_lshl_b32 s91, s85, 9\n" \
  "s_add_u32 s91, s91, s84\n" \
  "v_and_b32 v5, 1, v117\n" \
  "v_lshlrev_b32 v14, 12, v5\n" \
  "v_lshrrev_b32 v5, 1, v117\n" \
  "v_lshl_add_u32 v14, v5, 8, v14\n" \
  "v_lshl_add_u32 v14, v116, 4, v14\n" \
  "v_add_u32 v14, s91, v14\n" \
  "s_lshl_b32 s91, s85, 10\n" \
  "s_add_u32 s91, s91, s84\n" \
  "v_lshlrev_b32 v15, 4, v4\n" \
  "v_add_u32 v15, s91, v15\n" \
  "v_xor_b32 v18, 0x800, v14\n" \
  "v_xor_b32 v15, 0x4000, v15\n" \
  "s_lshl_b32 s84, %6, 7\n" \
  "s_add_u32 s84, s84, " STR(CW_GPROG) "*4\n" \
  "v_or_b32 v5, %7, v4\n" \
  "v_lshl_add_u32 v16, v5, 24, s84\n" \
  "v_mov_b32 v17, 0\n" \
  "v_or_b32 v14, 0x80000000, v14\n" \
  "v_or_b32 v15, 0x80000000, v15\n" \
  "v_or_b32 v18, 0x80000000, v18\n" \
  "v_lshrrev_b32 v5, 2, v116\n" \
  "v_lshl_add_u32 v6, v117, 3, v5\n" \
  "v_lshlrev_b32 v6, 7, v6\n" \
  "v_lshrrev_b32 v5, 1, v5\n" \
  "v_and_b32 v119, 1, v117\n" \
  "v_lshl_add_u32 v5, v119, 1, v5\n" \
  "v_xor_b32 v5, %7, v5\n" \
  "v_lshl_add_u32 v6, v5, 5, v6\n" \
  "v_and_b32 v5, 3, v4\n" \
  "v_lshl_add_u32 v6, v5, 3, v6\n" \
  "v_add_u32 v118, %8, v6\n" \
  "v_add_u32 v119, 0x10000, v118\n" \
  "v_add_u32 v120, 0x20000, v118\n" \
  "s_lshl_b32 s84, %7, 10\n" \
  "s_add_u32 s91, s84, %8\n" \
  "s_waitcnt vmcnt(24) lgkmcnt(0)\n" \
  "s_barrier\n" \
  "s_mov_b64 s[68:69], %2\n" \
  "s_and_b32 s69, s69, 0xffff\n" \
  "s_mov_b32 s70, 8388608\n" \
  "s_mov_b32 s71, 0x00020000\n" \
  "s_lshl_b32 s92, %7, 19\n" \
  "s_add_u32 s92, s92, 0\n" \
  "s_add_u32 m0, s86, 65536\n" \
  "s_nop 0\n" \
  "buffer_load_dwordx4 v8, s[68:71], s92 offen lds\n" \
  "s_add_u32 m0, s86, 66560\n" \
  "s_add_u32 s92, s92, 65536\n" \
  "buffer_load_dwordx4 v9, s[68:71], s92 offen lds\n" \
  "s_add_u32 m0, s86, 67584\n" \
  "s_add_u32 s92, s92, 65536\n" \
  "buffer_load_dwordx4 v8, s[68:71], s92 offen lds\n" \
  "s_add_u32 m0, s86, 68608\n" \
  "s_add_u32 s92, s92, 65536\n" \
  "buffer_load_dwordx4 v9, s[68:71], s92 offen lds\n" \
  "s_add_u32 m0, s86, 69632\n" \
  "s_add_u32 s92, s92, 65536\n" \
  "buffer_load_dwordx4 v8, s[68:71], s92 offen lds\n" \
  "s_add_u32 m0, s86, 70656\n" \
  "s_add_u32 s92, s92, 65536\n" \
  "buffer_load_dwordx4 v9, s[68:71], s92 offen lds\n" \
  "s_add_u32 m0, s86, 71680\n" \
  "s_add_u32 s92, s92, 65536\n" \
  "buffer_load_dwordx4 v8, s[68:71], s92 offen lds\n" \
  "s_add_u32 m0, s86, 72704\n" \
  "s_add_u32 s92, s92, 65536\n" \
  "buffer_load_dwordx4 v9, s[68:71], s92 offen lds\n" \
  "ds_read_b64_tr_b16 a[0:1], v119 offset:32768\n" \
  "ds_read_b64_tr_b16 a[2:3], v119 offset:33280\n" \
  "ds_read_b64_tr_b16 a[4:5], v119 offset:36864\n" \
  "ds_read_b64_tr_b16 a[6:7], v119 offset:37376\n" \
  "ds_read_b64_tr_b16 a[8:9], v119 offset:40960\n" \
  "ds_read_b64_tr_b16 a[10:11], v119 offset:41472\n" \
  "ds_read_b64_tr_b16 a[12:13], v119 offset:45056\n" \
  "ds_read_b64_tr_b16 a[14:15], v119 offset:45568\n" \
  "ds_read_b64_tr_b16 a[16:17], v119 offset:49152\n" \
  "ds_read_b64_tr_b16 a[18:19], v119 offset:49664\n" \
  "ds_read_b64_tr_b16 a[20:21], v119 offset:53248\n" \
  "ds_read_b64_tr_b16 a[22:23], v119 offset:53760\n" \
  "ds_read_b64_tr_b16 a[24:25], v119 offset:57344\n" \
  "ds_read_b64_tr_b16 a[26:27], v119 offset:57856\n" \
  "ds_read_b64_tr_b16 a[28:29], v119 offset:61440\n" \
  "ds_read_b64_tr_b16 a[30:31], v119 offset:61952\n" \
  "s_waitcnt vmcnt(24) lgkmcnt(0)\n" \
  "s_barrier\n" \
  "s_lshl_b32 s92, %7, 19\n" \
  "s_add_u32 s92, s92, 2097152\n" \
  "s_add_u32 m0, s86, 98304\n" \
  "s_nop 0\n" \
  "buffer_load_dwordx4 v8, s[68:71], s92 offen lds\n" \
  "s_add_u32 m0, s86, 99328\n" \
  "s_add_u32 s92, s92, 65536\n" \
  "buffer_load_dwordx4 v9, s[68:71], s92 offen lds\n" \
  "s_add_u32 m0, s86, 100352\n" \
  "s_add_u32 s92, s92, 65536\n" \
  "buffer_load_dwordx4 v8, s[68:71], s92 offen lds\n" \
  "s_add_u32 m0, s86, 101376\n" \
  "s_add_u32 s92, s92, 65536\n" \
  "buffer_load_dwordx4 v9, s[68:71], s92 offen lds\n" \
  "s_add_u32 m0, s86, 102400\n" \
  "s_add_u32 s92, s92, 65536\n" \
  "buffer_load_dwordx4 v8, s[68:71], s92 offen lds\n" \
  "s_add_u32 m0, s86, 103424\n" \
  "s_add_u32 s92, s92, 65536\n" \
  "buffer_load_dwordx4 v9, s[68:71], s92 offen lds\n" \
  "s_add_u32 m0, s86, 104448\n" \
  "s_add_u32 s92, s92, 65536\n" \
  "buffer_load_dwordx4 v8, s[68:71], s92 offen lds\n" \
  "s_add_u32 m0, s86, 105472\n" \
  "s_add_u32 s92, s92, 65536\n" \
  "buffer_load_dwordx4 v9, s[68:71], s92 offen lds\n" \
  "ds_read_b64_tr_b16 a[32:33], v120 offset:0\n" \
  "ds_read_b64_tr_b16 a[34:35], v120 offset:512\n" \
  "ds_read_b64_tr_b16 a[36:37], v120 offset:4096\n" \
  "ds_read_b64_tr_b16 a[38:39], v120 offset:4608\n" \
  "ds_read_b64_tr_b16 a[40:41], v120 offset:8192\n" \
  "ds_read_b64_tr_b16 a[42:43], v120 offset:8704\n" \
  "ds_read_b64_tr_b16 a[44:45], v120 offset:12288\n" \
  "ds_read_b64_tr_b16 a[46:47], v120 offset:12800\n" \
  "ds_read_b64_tr_b16 a[48:49], v120 offset:16384\n" \
  "ds_read_b64_tr_b16 a[50:51], v120 offset:16896\n" \
  "ds_read_b64_tr_b16 a[52:53], v120 offset:20480\n" \
  "ds_read_b64_tr_b16 a[54:55], v120 offset:20992\n" \
  "ds_read_b64_tr_b16 a[56:57], v120 offset:24576\n" \
  "ds_read_b64_tr_b16 a[58:59], v120 offset:25088\n" \
  "ds_read_b64_tr_b16 a[60:61], v120 offset:28672\n" \
  "ds_read_b64_tr_b16 a[62:63], v120 offset:29184\n" \
  "s_waitcnt vmcnt(24) lgkmcnt(0)\n" \
  "s_barrier\n" \
  "s_lshl_b32 s92, %7, 19\n" \
  "s_add_u32 s92, s92, 4194304\n" \
  "s_add_u32 m0, s86, 131072\n" \
  "s_nop 0\n" \
  "buffer_load_dwordx4 v8, s[68:71], s92 offen lds\n" \
  "s_add_u32 m0, s86, 132096\n" \
  "s_add_u32 s92, s92, 65536\n" \
  "buffer_load_dwordx4 v9, s[68:71], s92 offen lds\n" \
  "s_add_u32 m0, s86, 133120\n" \
  "s_add_u32 s92, s92, 65536\n" \
  "buffer_load_dwordx4 v8, s[68:71], s92 offen lds\n" \
  "s_add_u32 m0, s86, 134144\n" \
  "s_add_u32 s92, s92, 65536\n" \
  "buffer_load_dwordx4 v9, s[68:71], s92 offen lds\n" \
  "s_add_u32 m0, s86, 135168\n" \
  "s_add_u32 s92, s92, 65536\n" \
  "buffer_load_dwordx4 v8, s[68:71], s92 offen lds\n" \
  "s_add_u32 m0, s86, 136192\n" \
  "s_add_u32 s92, s92, 65536\n" \
  "buffer_load_dwordx4 v9, s[68:71], s92 offen lds\n" \
  "s_add_u32 m0, s86, 137216\n" \
  "s_add_u32 s92, s92, 65536\n" \
  "buffer_load_dwordx4 v8, s[68:71], s92 offen lds\n" \
  "s_add_u32 m0, s86, 138240\n" \
  "s_add_u32 s92, s92, 65536\n" \
  "buffer_load_dwordx4 v9, s[68:71], s92 offen lds\n" \
  "ds_read_b64_tr_b16 a[64:65], v118 offset:0\n" \
  "ds_read_b64_tr_b16 a[66:67], v118 offset:512\n" \
  "ds_read_b64_tr_b16 a[68:69], v118 offset:4096\n" \
  "ds_read_b64_tr_b16 a[70:71], v118 offset:4608\n" \
  "ds_read_b64_tr_b16 a[72:73], v118 offset:8192\n" \
  "ds_read_b64_tr_b16 a[74:75], v118 offset:8704\n" \
  "ds_read_b64_tr_b16 a[76:77], v118 offset:12288\n" \
  "ds_read_b64_tr_b16 a[78:79], v118 offset:12800\n" \
  "ds_read_b64_tr_b16 a[80:81], v118 offset:16384\n" \
  "ds_read_b64_tr_b16 a[82:83], v118 offset:16896\n" \
  "ds_read_b64_tr_b16 a[84:85], v118 offset:20480\n" \
  "ds_read_b64_tr_b16 a[86:87], v118 offset:20992\n" \
  "ds_read_b64_tr_b16 a[88:89], v118 offset:24576\n" \
  "ds_read_b64_tr_b16 a[90:91], v118 offset:25088\n" \
  "ds_read_b64_tr_b16 a[92:93], v118 offset:28672\n" \
  "ds_read_b64_tr_b16 a[94:95], v118 offset:29184\n" \
  "s_waitcnt vmcnt(24) lgkmcnt(0)\n" \
  "s_barrier\n" \
  "s_lshl_b32 s92, %7, 19\n" \
  "s_add_u32 s92, s92, 6291456\n" \
  "s_add_u32 m0, s86, 0\n" \
  "s_nop 0\n" \
  "buffer_load_dwordx4 v8, s[68:71], s92 offen lds\n" \
  "s_add_u32 m0, s86, 1024\n" \
  "s_add_u32 s92, s92, 65536\n" \
  "buffer_load_dwordx4 v9, s[68:71], s92 offen lds\n" \
  "s_add_u32 m0, s86, 2048\n" \
  "s_add_u32 s92, s92, 65536\n" \
  "buffer_load_dwordx4 v8, s[68:71], s92 offen lds\n" \
  "s_add_u32 m0, s86, 3072\n" \
  "s_add_u32 s92, s92, 65536\n" \
  "buffer_load_dwordx4 v9, s[68:71], s92 offen lds\n" \
  "s_add_u32 m0, s86, 4096\n" \
  "s_add_u32 s92, s92, 65536\n" \
  "buffer_load_dwordx4 v8, s[68:71], s92 offen lds\n" \
  "s_add_u32 m0, s86, 5120\n" \
  "s_add_u32 s92, s92, 65536\n" \
  "buffer_load_dwordx4 v9, s[68:71], s92 offen lds\n" \
  "s_add_u32 m0, s86, 6144\n" \
  "s_add_u32 s92, s92, 65536\n" \
  "buffer_load_dwordx4 v8, s[68:71], s92 offen lds\n" \
  "s_add_u32 m0, s86, 7168\n" \
  "s_add_u32 s92, s92, 65536\n" \
  "buffer_load_dwordx4 v9, s[68:71], s92 offen lds\n" \
  "ds_read_b64_tr_b16 a[96:97], v118 offset:32768\n" \
  "ds_read_b64_tr_b16 a[98:99], v118 offset:33280\n" \
  "ds_read_b64_tr_b16 a[100:101], v118 offset:36864\n" \
  "ds_read_b64_tr_b16 a[102:103], v118 offset:37376\n" \
  "ds_read_b64_tr_b16 a[104:105], v118 offset:40960\n" \
  "ds_read_b64_tr_b16 a[106:107], v118 offset:41472\n" \
  "ds_read_b64_tr_b16 a[108:109], v118 offset:45056\n" \
  "ds_read_b64_tr_b16 a[110:111], v118 offset:45568\n" \
  "ds_read_b64_tr_b16 a[112:113], v118 offset:49152\n" \
  "ds_read_b64_tr_b16 a[114:115], v118 offset:49664\n" \
  "ds_read_b64_tr_b16 a[116:117], v118 offset:53248\n" \
  "ds_read_b64_tr_b16 a[118:119], v118 offset:53760\n" \
  "ds_read_b64_tr_b16 a[120:121], v118 offset:57344\n" \
  "ds_read_b64_tr_b16 a[122:123], v118 offset:57856\n" \
  "ds_read_b64_tr_b16 a[124:125], v118 offset:61440\n" \
  "ds_read_b64_tr_b16 a[126:127], v118 offset:61952\n" \
  "s_waitcnt vmcnt(24) lgkmcnt(0)\n" \
  "s_barrier\n" \
  "s_mov_b64 s[68:69], %0\n" \
  "s_and_b32 s69, s69, 0xffff\n" \
  "s_mov_b32 s70, 8388608\n" \
  "s_mov_b32 s71, 0x00020000\n" \
  "s_lshl_b32 s92, %7, 19\n" \
  "s_add_u32 s92, s92, 0\n" \
  "s_add_u32 m0, s86, 32768\n" \
  "s_nop 0\n" \
  "buffer_load_dwordx4 v8, s[68:71], s92 offen lds\n" \
  "s_add_u32 m0, s86, 33792\n" \
  "s_add_u32 s92, s92, 65536\n" \
  "buffer_load_dwordx4 v9, s[68:71], s92 offen lds\n" \
  "s_add_u32 m0, s86, 34816\n" \
  "s_add_u32 s92, s92, 65536\n" \
  "buffer_load_dwordx4 v8, s[68:71], s92 offen lds\n" \
  "s_add_u32 m0, s86, 35840\n" \
  "s_add_u32 s92, s92, 65536\n" \
  "buffer_load_dwordx4 v9, s[68:71], s92 offen lds\n" \
  "s_add_u32 m0, s86, 36864\n" \
  "s_add_u32 s92, s92, 65536\n" \
  "buffer_load_dwordx4 v8, s[68:71], s92 offen lds\n" \
  "s_add_u32 m0, s86, 37888\n" \
  "s_add_u32 s92, s92, 65536\n" \
  "buffer_load_dwordx4 v9, s[68:71], s92 offen lds\n" \
  "s_add_u32 m0, s86, 38912\n" \
  "s_add_u32 s92, s92, 65536\n" \
  "buffer_load_dwordx4 v8, s[68:71], s92 offen lds\n" \
  "s_add_u32 m0, s86, 39936\n" \
  "s_add_u32 s92, s92, 65536\n" \
  "buffer_load_dwordx4 v9, s[68:71], s92 offen lds\n" \
  "ds_read_b64_tr_b16 a[128:129], v119 offset:0\n" \
  "ds_read_b64_tr_b16 a[130:131], v119 offset:512\n" \
  "ds_read_b64_tr_b16 a[132:133], v119 offset:4096\n" \
  "ds_read_b64_tr_b16 a[134:135], v119 offset:4608\n" \
  "ds_read_b64_tr_b16 a[136:137], v119 offset:8192\n" \
  "ds_read_b64_tr_b16 a[138:139], v119 offset:8704\n" \
  "ds_read_b64_tr_b16 a[140:141], v119 offset:12288\n" \
  "ds_read_b64_tr_b16 a[142:143], v119 offset:12800\n" \
  "ds_read_b64_tr_b16 a[144:145], v119 offset:16384\n" \
  "ds_read_b64_tr_b16 a[146:147], v119 offset:16896\n" \
  "ds_read_b64_tr_b16 a[148:149], v119 offset:20480\n" \
  "ds_read_b64_tr_b16 a[150:151], v119 offset:20992\n" \
  "ds_read_b64_tr_b16 a[152:153], v119 offset:24576\n" \
  "ds_read_b64_tr_b16 a[154:155], v119 offset:25088\n" \
  "ds_read_b64_tr_b16 a[156:157], v119 offset:28672\n" \
  "ds_read_b64_tr_b16 a[158:159], v119 offset:29184\n" \
  "s_waitcnt vmcnt(24) lgkmcnt(0)\n" \
  "s_barrier\n" \
  "s_lshl_b32 s92, %7, 19\n" \
  "s_add_u32 s92, s92, 2097152\n" \
  "s_add_u32 m0, s86, 65536\n" \
  "s_nop 0\n" \
  "buffer_load_dwordx4 v8, s[68:71], s92 offen lds\n" \
  "s_add_u32 m0, s86, 66560\n" \
  "s_add_u32 s92, s92, 65536\n" \
  "buffer_load_dwordx4 v9, s[68:71], s92 offen lds\n" \
  "s_add_u32 m0, s86, 67584\n" \
  "s_add_u32 s92, s92, 65536\n" \
  "buffer_load_dwordx4 v8, s[68:71], s92 offen lds\n" \
  "s_add_u32 m0, s86, 68608\n" \
  "s_add_u32 s92, s92, 65536\n" \
  "buffer_load_dwordx4 v9, s[68:71], s92 offen lds\n" \
  "s_add_u32 m0, s86, 69632\n" \
  "s_add_u32 s92, s92, 65536\n" \
  "buffer_load_dwordx4 v8, s[68:71], s92 offen lds\n" \
  "s_add_u32 m0, s86, 70656\n" \
  "s_add_u32 s92, s92, 65536\n" \
  "buffer_load_dwordx4 v9, s[68:71], s92 offen lds\n" \
  "s_add_u32 m0, s86, 71680\n" \
  "s_add_u32 s92, s92, 65536\n" \
  "buffer_load_dwordx4 v8, s[68:71], s92 offen lds\n" \
  "s_add_u32 m0, s86, 72704\n" \
  "s_add_u32 s92, s92, 65536\n" \
  "buffer_load_dwordx4 v9, s[68:71], s92 offen lds\n" \
  "ds_read_b64_tr_b16 a[160:161], v119 offset:32768\n" \
  "ds_read_b64_tr_b16 a[162:163], v119 offset:33280\n" \
  "ds_read_b64_tr_b16 a[164:165], v119 offset:36864\n" \
  "ds_read_b64_tr_b16 a[166:167], v119 offset:37376\n" \
  "ds_read_b64_tr_b16 a[168:169], v119 offset:40960\n" \
  "ds_read_b64_tr_b16 a[170:171], v119 offset:41472\n" \
  "ds_read_b64_tr_b16 a[172:173], v119 offset:45056\n" \
  "ds_read_b64_tr_b16 a[174:175], v119 offset:45568\n" \
  "ds_read_b64_tr_b16 a[176:177], v119 offset:49152\n" \
  "ds_read_b64_tr_b16 a[178:179], v119 offset:49664\n" \
  "ds_read_b64_tr_b16 a[180:181], v119 offset:53248\n" \
  "ds_read_b64_tr_b16 a[182:183], v119 offset:53760\n" \
  "ds_read_b64_tr_b16 a[184:185], v119 offset:57344\n" \
  "ds_read_b64_tr_b16 a[186:187], v119 offset:57856\n" \
  "ds_read_b64_tr_b16 a[188:189], v119 offset:61440\n" \
  "ds_read_b64_tr_b16 a[190:191], v119 offset:61952\n" \
  "s_waitcnt vmcnt(24) lgkmcnt(0)\n" \
  "s_barrier\n" \
  "s_lshl_b32 s92, %7, 19\n" \
  "s_add_u32 s92, s92, 4194304\n" \
  "s_add_u32 m0, s86, 98304\n" \
  "s_nop 0\n" \
  "buffer_load_dwordx4 v8, s[68:71], s92 offen lds\n" \
  "s_add_u32 m0, s86, 99328\n" \
  "s_add_u32 s92, s92, 65536\n" \
  "buffer_load_dwordx4 v9, s[68:71], s92 offen lds\n" \
  "s_add_u32 m0, s86, 100352\n" \
  "s_add_u32 s92, s92, 65536\n" \
  "buffer_load_dwordx4 v8, s[68:71], s92 offen lds\n" \
  "s_add_u32 m0, s86, 101376\n" \
  "s_add_u32 s92, s92, 65536\n" \
  "buffer_load_dwordx4 v9, s[68:71], s92 offen lds\n" \
  "s_add_u32 m0, s86, 102400\n" \
  "s_add_u32 s92, s92, 65536\n" \
  "buffer_load_dwordx4 v8, s[68:71], s92 offen lds\n" \
  "s_add_u32 m0, s86, 103424\n" \
  "s_add_u32 s92, s92, 65536\n" \
  "buffer_load_dwordx4 v9, s[68:71], s92 offen lds\n" \
  "s_add_u32 m0, s86, 104448\n" \
  "s_add_u32 s92, s92, 65536\n" \
  "buffer_load_dwordx4 v8, s[68:71], s92 offen lds\n" \
  "s_add_u32 m0, s86, 105472\n" \
  "s_add_u32 s92, s92, 65536\n" \
  "buffer_load_dwordx4 v9, s[68:71], s92 offen lds\n" \
  "ds_read_b64_tr_b16 a[192:193], v120 offset:0\n" \
  "ds_read_b64_tr_b16 a[194:195], v120 offset:512\n" \
  "ds_read_b64_tr_b16 a[196:197], v120 offset:4096\n" \
  "ds_read_b64_tr_b16 a[198:199], v120 offset:4608\n" \
  "ds_read_b64_tr_b16 a[200:201], v120 offset:8192\n" \
  "ds_read_b64_tr_b16 a[202:203], v120 offset:8704\n" \
  "ds_read_b64_tr_b16 a[204:205], v120 offset:12288\n" \
  "ds_read_b64_tr_b16 a[206:207], v120 offset:12800\n" \
  "ds_read_b64_tr_b16 a[208:209], v120 offset:16384\n" \
  "ds_read_b64_tr_b16 a[210:211], v120 offset:16896\n" \
  "ds_read_b64_tr_b16 a[212:213], v120 offset:20480\n" \
  "ds_read_b64_tr_b16 a[214:215], v120 offset:20992\n" \
  "ds_read_b64_tr_b16 a[216:217], v120 offset:24576\n" \
  "ds_read_b64_tr_b16 a[218:219], v120 offset:25088\n" \
  "ds_read_b64_tr_b16 a[220:221], v120 offset:28672\n" \
  "ds_read_b64_tr_b16 a[222:223], v120 offset:29184\n" \
  "s_waitcnt vmcnt(24) lgkmcnt(0)\n" \
  "s_barrier\n" \
  "s_lshl_b32 s92, %7, 19\n" \
  "s_add_u32 s92, s92, 6291456\n" \
  "s_add_u32 m0, s86, 131072\n" \
  "s_nop 0\n" \
  "buffer_load_dwordx4 v8, s[68:71], s92 offen lds\n" \
  "s_add_u32 m0, s86, 132096\n" \
  "s_add_u32 s92, s92, 65536\n" \
  "buffer_load_dwordx4 v9, s[68:71], s92 offen lds\n" \
  "s_add_u32 m0, s86, 133120\n" \
  "s_add_u32 s92, s92, 65536\n" \
  "buffer_load_dwordx4 v8, s[68:71], s92 offen lds\n" \
  "s_add_u32 m0, s86, 134144\n" \
  "s_add_u32 s92, s92, 65536\n" \
  "buffer_load_dwordx4 v9, s[68:71], s92 offen lds\n" \
  "s_add_u32 m0, s86, 135168\n" \
  "s_add_u32 s92, s92, 65536\n" \
  "buffer_load_dwordx4 v8, s[68:71], s92 offen lds\n" \
  "s_add_u32 m0, s86, 136192\n" \
  "s_add_u32 s92, s92, 65536\n" \
  "buffer_load_dwordx4 v9, s[68:71], s92 offen lds\n" \
  "s_add_u32 m0, s86, 137216\n" \
  "s_add_u32 s92, s92, 65536\n" \
  "buffer_load_dwordx4 v8, s[68:71], s92 offen lds\n" \
  "s_add_u32 m0, s86, 138240\n" \
  "s_add_u32 s92, s92, 65536\n" \
  "buffer_load_dwordx4 v9, s[68:71], s92 offen lds\n" \
  "ds_read_b64_tr_b16 a[224:225], v118 offset:0\n" \
  "ds_read_b64_tr_b16 a[226:227], v118 offset:512\n" \
  "ds_read_b64_tr_b16 a[228:229], v118 offset:4096\n" \
  "ds_read_b64_tr_b16 a[230:231], v118 offset:4608\n" \
  "ds_read_b64_tr_b16 a[232:233], v118 offset:8192\n" \
  "ds_read_b64_tr_b16 a[234:235], v118 offset:8704\n" \
  "ds_read_b64_tr_b16 a[236:237], v118 offset:12288\n" \
  "ds_read_b64_tr_b16 a[238:239], v118 offset:12800\n" \
  "ds_read_b64_tr_b16 a[240:241], v118 offset:16384\n" \
  "ds_read_b64_tr_b16 a[242:243], v118 offset:16896\n" \
  "ds_read_b64_tr_b16 a[244:245], v118 offset:20480\n" \
  "ds_read_b64_tr_b16 a[246:247], v118 offset:20992\n" \
  "ds_read_b64_tr_b16 a[248:249], v118 offset:24576\n" \
  "ds_read_b64_tr_b16 a[250:251], v118 offset:25088\n" \
  "ds_read_b64_tr_b16 a[252:253], v118 offset:28672\n" \
  "ds_read_b64_tr_b16 a[254:255], v118 offset:29184\n" \
  "s_waitcnt vmcnt(24) lgkmcnt(0)\n" \
  "s_barrier\n" \
  "s_add_u32 m0, s91, 0\n" \
  "s_nop 0\n" \
  "buffer_load_dwordx4 v7, s[72:75], s87 offen lds\n" \
  "s_add_u32 s87, s87, 128\n" \
  "s_add_u32 m0, s91, 4096\n" \
  "s_nop 0\n" \
  "buffer_load_dwordx4 v7, s[72:75], s87 offen lds\n" \
  "s_add_u32 s87, s87, 128\n" \
  "s_add_u32 m0, s91, 8192\n" \
  "s_nop 0\n" \
  "buffer_load_dwordx4 v7, s[72:75], s87 offen lds\n" \
  "s_add_u32 s87, s87, 128\n" \
  "s_add_u32 m0, s91, 12288\n" \
  "s_nop 0\n" \
  "buffer_load_dwordx4 v7, s[72:75], s87 offen lds\n" \
  "s_add_u32 s87, s87, 128\n" \
  "s_add_u32 m0, s91, 16384\n" \
  "s_nop 0\n" \
  "buffer_load_dwordx4 v7, s[72:75], s87 offen lds\n" \
  "s_add_u32 s87, s87, 128\n" \
  "s_add_u32 m0, s91, 20480\n" \
  "s_nop 0\n" \
  "buffer_load_dwordx4 v7, s[72:75], s87 offen lds\n" \
  "s_add_u32 s87, s87, 128\n" \
  "s_add_u32 m0, s91, 24576\n" \
  "s_nop 0\n" \
  "buffer_load_dwordx4 v7, s[72:75], s87 offen lds\n" \
  "s_add_u32 s87, s87, 128\n" \
  "s_add_u32 m0, s91, 28672\n" \
  "s_nop 0\n" \
  "buffer_load_dwordx4 v7, s[72:75], s87 offen lds\n" \
  "s_add_u32 s87, s87, 128\n" \
  "ds_read_b64_tr_b16 v[128:129], v118 offset:32768\n" \
  "ds_read_b64_tr_b16 v[130:131], v118 offset:33280\n" \
  "ds_read_b64_tr_b16 v[132:133], v118 offset:36864\n" \
  "ds_read_b64_tr_b16 v[134:135], v118 offset:37376\n" \
  "ds_read_b64_tr_b16 v[136:137], v118 offset:40960\n" \
  "ds_read_b64_tr_b16 v[138:139], v118 offset:41472\n" \
  "ds_read_b64_tr_b16 v[140:141], v118 offset:45056\n" \
  "ds_read_b64_tr_b16 v[142:143], v118 offset:45568\n" \
  "ds_read_b64_tr_b16 v[144:145], v118 offset:49152\n" \
  "ds_read_b64_tr_b16 v[146:147], v118 offset:49664\n" \
  "ds_read_b64_tr_b16 v[148:149], v118 offset:53248\n" \
  "ds_read_b64_tr_b16 v[150:151], v118 offset:53760\n" \
  "ds_read_b64_tr_b16 v[152:153], v118 offset:57344\n" \
  "ds_read_b64_tr_b16 v[154:155], v118 offset:57856\n" \
  "ds_read_b64_tr_b16 v[156:157], v118 offset:61440\n" \
  "ds_read_b64_tr_b16 v[158:159], v118 offset:61952\n" \
  "s_waitcnt vmcnt(24) lgkmcnt(0)\n" \
  "s_barrier\n" \
  "s_add_u32 m0, s91, 32768\n" \
  "s_nop 0\n" \
  "buffer_load_dwordx4 v7, s[72:75], s87 offen lds\n" \
  "s_add_u32 s87, s87, 128\n" \
  "s_add_u32 m0, s91, 36864\n" \
  "s_nop 0\n" \
  "buffer_load_dwordx4 v7, s[72:75], s87 offen lds\n" \
  "s_add_u32 s87, s87, 128\n" \
  "s_add_u32 m0, s91, 40960\n" \
  "s_nop 0\n" \
  "buffer_load_dwordx4 v7, s[72:75], s87 offen lds\n" \
  "s_add_u32 s87, s87, 128\n" \
  "s_add_u32 m0, s91, 45056\n" \
  "s_nop 0\n" \
  "buffer_load_dwordx4 v7, s[72:75], s87 offen lds\n" \
  "s_add_u32 s87, s87, 128\n" \
  "s_add_u32 m0, s91, 49152\n" \
  "s_nop 0\n" \
  "buffer_load_dwordx4 v7, s[72:75], s87 offen lds\n" \
  "s_add_u32 s87, s87, 128\n" \
  "s_add_u32 m0, s91, 53248\n" \
  "s_nop 0\n" \
  "buffer_load_dwordx4 v7, s[72:75], s87 offen lds\n" \
  "s_add_u32 s87, s87, 128\n" \
  "s_add_u32 m0, s91, 57344\n" \
  "s_nop 0\n" \
  "buffer_load_dwordx4 v7, s[72:75], s87 offen lds\n" \
  "s_add_u32 s87, s87, 128\n" \
  "s_add_u32 m0, s91, 61440\n" \
  "s_nop 0\n" \
  "buffer_load_dwordx4 v7, s[72:75], s87 offen lds\n" \
  "s_add_u32 s87, s87, 63616\n" \
  "s_min_u32 s87, s87, s96\n" \
  "ds_read_b64_tr_b16 v[160:161], v119 offset:0\n" \
  "ds_read_b64_tr_b16 v[162:163], v119 offset:512\n" \
  "ds_read_b64_tr_b16 v[164:165], v119 offset:4096\n" \
  "ds_read_b64_tr_b16 v[166:167], v119 offset:4608\n" \
  "ds_read_b64_tr_b16 v[168:169], v119 offset:8192\n" \
  "ds_read_b64_tr_b16 v[170:171], v119 offset:8704\n" \
  "ds_read_b64_tr_b16 v[172:173], v119 offset:12288\n" \
  "ds_read_b64_tr_b16 v[174:175], v119 offset:12800\n" \
  "ds_read_b64_tr_b16 v[176:177], v119 offset:16384\n" \
  "ds_read_b64_tr_b16 v[178:179], v119 offset:16896\n" \
  "ds_read_b64_tr_b16 v[180:181], v119 offset:20480\n" \
  "ds_read_b64_tr_b16 v[182:183], v119 offset:20992\n" \
  "ds_read_b64_tr_b16 v[184:185], v119 offset:24576\n" \
  "ds_read_b64_tr_b16 v[186:187], v119 offset:25088\n" \
  "ds_read_b64_tr_b16 v[188:189], v119 offset:28672\n" \
  "ds_read_b64_tr_b16 v[190:191], v119 offset:29184\n" \
  "s_waitcnt vmcnt(24) lgkmcnt(0)\n" \
  "s_barrier\n" \
  "s_add_u32 m0, s91, 65536\n" \
  "s_nop 0\n" \
  "buffer_load_dwordx4 v7, s[72:75], s87 offen lds\n" \
  "s_add_u32 s87, s87, 128\n" \
  "s_add_u32 m0, s91, 69632\n" \
  "s_nop 0\n" \
  "buffer_load_dwordx4 v7, s[72:75], s87 offen lds\n" \
  "s_add_u32 s87, s87, 128\n" \
  "s_add_u32 m0, s91, 73728\n" \
  "s_nop 0\n" \
  "buffer_load_dwordx4 v7, s[72:75], s87 offen lds\n" \
  "s_add_u32 s87, s87, 128\n" \
  "s_add_u32 m0, s91, 77824\n" \
  "s_nop 0\n" \
  "buffer_load_dwordx4 v7, s[72:75], s87 offen lds\n" \
  "s_add_u32 s87, s87, 128\n" \
  "s_add_u32 m0, s91, 81920\n" \
  "s_nop 0\n" \
  "buffer_load_dwordx4 v7, s[72:75], s87 offen lds\n" \
  "s_add_u32 s87, s87, 128\n" \
  "s_add_u32 m0, s91, 86016\n" \
  "s_nop 0\n" \
  "buffer_load_dwordx4 v7, s[72:75], s87 offen lds\n" \
  "s_add_u32 s87, s87, 128\n" \
  "s_add_u32 m0, s91, 90112\n" \
  "s_nop 0\n" \
  "buffer_load_dwordx4 v7, s[72:75], s87 offen lds\n" \
  "s_add_u32 s87, s87, 128\n" \
  "s_add_u32 m0, s91, 94208\n" \
  "s_nop 0\n" \
  "buffer_load_dwordx4 v7, s[72:75], s87 offen lds\n" \
  "s_add_u32 s87, s87, 128\n" \
  "ds_read_b64_tr_b16 v[192:193], v119 offset:32768\n" \
  "ds_read_b64_tr_b16 v[194:195], v119 offset:33280\n" \
  "ds_read_b64_tr_b16 v[196:197], v119 offset:36864\n" \
  "ds_read_b64_tr_b16 v[198:199], v119 offset:37376\n" \
  "ds_read_b64_tr_b16 v[200:201], v119 offset:40960\n" \
  "ds_read_b64_tr_b16 v[202:203], v119 offset:41472\n" \
  "ds_read_b64_tr_b16 v[204:205], v119 offset:45056\n" \
  "ds_read_b64_tr_b16 v[206:207], v119 offset:45568\n" \
  "ds_read_b64_tr_b16 v[208:209], v119 offset:49152\n" \
  "ds_read_b64_tr_b16 v[210:211], v119 offset:49664\n" \
  "ds_read_b64_tr_b16 v[212:213], v119 offset:53248\n" \
  "ds_read_b64_tr_b16 v[214:215], v119 offset:53760\n" \
  "ds_read_b64_tr_b16 v[216:217], v119 offset:57344\n" \
  "ds_read_b64_tr_b16 v[218:219], v119 offset:57856\n" \
  "ds_read_b64_tr_b16 v[220:221], v119 offset:61440\n" \
  "ds_read_b64_tr_b16 v[222:223], v119 offset:61952\n" \
  "s_waitcnt vmcnt(24) lgkmcnt(0)\n" \
  "s_barrier\n" \
  "ds_read_b64_tr_b16 v[224:225], v120 offset:0\n" \
  "ds_read_b64_tr_b16 v[226:227], v120 offset:512\n" \
  "ds_read_b64_tr_b16 v[228:229], v120 offset:4096\n" \
  "ds_read_b64_tr_b16 v[230:231], v120 offset:4608\n" \
  "ds_read_b64_tr_b16 v[232:233], v120 offset:8192\n" \
  "ds_read_b64_tr_b16 v[234:235], v120 offset:8704\n" \
  "ds_read_b64_tr_b16 v[236:237], v120 offset:12288\n" \
  "ds_read_b64_tr_b16 v[238:239], v120 offset:12800\n" \
  "ds_read_b64_tr_b16 v[240:241], v120 offset:16384\n" \
  "ds_read_b64_tr_b16 v[242:243], v120 offset:16896\n" \
  "ds_read_b64_tr_b16 v[244:245], v120 offset:20480\n" \
  "ds_read_b64_tr_b16 v[246:247], v120 offset:20992\n" \
  "ds_read_b64_tr_b16 v[248:249], v120 offset:24576\n" \
  "ds_read_b64_tr_b16 v[250:251], v120 offset:25088\n" \
  "ds_read_b64_tr_b16 v[252:253], v120 offset:28672\n" \
  "ds_read_b64_tr_b16 v[254:255], v120 offset:29184\n" \
  "s_waitcnt vmcnt(8) lgkmcnt(0)\n" \
  "s_barrier\n" \
  "ds_read_b128 v[68:71], v10 offset:0\n" \
  "ds_read_b128 v[72:75], v10 offset:2048\n" \
  "ds_read_b128 v[76:79], v11 offset:0\n" \
  "ds_read_b128 v[80:83], v11 offset:2048\n" \
  "ds_read_b128 v[84:87], v10 offset:4096\n" \
  "ds_read_b128 v[88:91], v10 offset:6144\n" \
  "s_mov_b32 s85, 0\n" \
  ".p2align 6\n" \
  "LG_%=:\n" \
  "s_cmp_eq_u32 s85, 64\n" \
  "s_cbranch_scc1 LX0_%=\n" \
  "s_waitcnt vmcnt(8)\n" \
  "s_barrier\n" \
  "s_waitcnt lgkmcnt(4)\n" \
  "v_mfma_f32_16x16x32_bf16 v[20:23], v[128:131], v[68:71], 0\n" \
  "ds_read_b128 v[92:95], v11 offset:4096\n" \
  "s_sub_u32 s89, s85, 1\n" \
  "s_and_b32 s89, s89, 63\n" \
  "v_mfma_f32_16x16x32_bf16 v[28:31], a[0:3], v[68:71], 0\n" \
  "ds_read_b128 v[96:99], v11 offset:6144\n" \
  "s_lshr_b32 s84, s89, 2\n" \
  "s_xnor_b32 s84, s84, %9\n" \
  "v_mfma_f32_16x16x32_bf16 v[36:39], v[68:71], a[128:131], 0\n" \
  "s_and_b32 s84, s84, 1\n" \
  "s_lshl_b32 s84, s84, 13\n" \
  "v_mfma_f32_16x16x32_bf16 v[24:27], v[128:131], v[72:75], 0\n" \
  "s_lshl_b32 s88, s89, 19\n" \
  "s_or_b32 s88, s88, s84\n" \
  "v_mfma_f32_16x16x32_bf16 v[32:35], a[0:3], v[72:75], 0\n" \
  "s_add_u32 s89, s88, " STR(QKV_SZ32) "\n" \
  "s_add_u32 s90, s89, " STR(QKV_SZ32) "\n" \
  "v_mfma_f32_16x16x32_bf16 v[40:43], v[72:75], a[128:131], 0\n" \
  "s_add_u32 s85, s85, 1\n" \
  "s_waitcnt lgkmcnt(4)\n" \
  "v_mfma_f32_16x16x32_bf16 v[20:23], v[132:135], v[76:79], v[20:23]\n" \
  "ds_read_b128 v[68:71], v10 offset:8192\n" \
  "v_cvt_pk_bf16_f32 v116, v44, v45\n" \
  "v_mfma_f32_16x16x32_bf16 v[28:31], a[4:7], v[76:79], v[28:31]\n" \
  "ds_read_b128 v[72:75], v10 offset:10240\n" \
  "v_cvt_pk_bf16_f32 v117, v46, v47\n" \
  "v_mfma_f32_16x16x32_bf16 v[36:39], v[76:79], a[132:135], v[36:39]\n" \
  "v_cvt_pk_bf16_f32 v118, v48, v49\n" \
  "v_mfma_f32_16x16x32_bf16 v[24:27], v[132:135], v[80:83], v[24:27]\n" \
  "v_cvt_pk_bf16_f32 v119, v50, v51\n" \
  "v_mfma_f32_16x16x32_bf16 v[32:35], a[4:7], v[80:83], v[32:35]\n" \
  "v_cvt_pk_bf16_f32 v120, v52, v53\n" \
  "v_mfma_f32_16x16x32_bf16 v[40:43], v[80:83], a[132:135], v[40:43]\n" \
  "v_cvt_pk_bf16_f32 v121, v54, v55\n" \
  "s_waitcnt lgkmcnt(4)\n" \
  "v_mfma_f32_16x16x32_bf16 v[20:23], v[136:139], v[84:87], v[20:23]\n" \
  "ds_read_b128 v[76:79], v11 offset:8192\n" \
  "v_cvt_pk_bf16_f32 v122, v56, v57\n" \
  "v_mfma_f32_16x16x32_bf16 v[28:31], a[8:11], v[84:87], v[28:31]\n" \
  "ds_read_b128 v[80:83], v11 offset:10240\n" \
  "v_cvt_pk_bf16_f32 v123, v58, v59\n" \
  "v_mfma_f32_16x16x32_bf16 v[36:39], v[84:87], a[136:139], v[36:39]\n" \
  "v_cvt_pk_bf16_f32 v124, v60, v61\n" \
  "v_mfma_f32_16x16x32_bf16 v[24:27], v[136:139], v[88:91], v[24:27]\n" \
  "v_cvt_pk_bf16_f32 v125, v62, v63\n" \
  "v_mfma_f32_16x16x32_bf16 v[32:35], a[8:11], v[88:91], v[32:35]\n" \
  "v_cvt_pk_bf16_f32 v126, v64, v65\n" \
  "v_mfma_f32_16x16x32_bf16 v[40:43], v[88:91], a[136:139], v[40:43]\n" \
  "v_cvt_pk_bf16_f32 v127, v66, v67\n" \
  "s_waitcnt lgkmcnt(4)\n" \
  "v_mfma_f32_16x16x32_bf16 v[20:23], v[140:143], v[92:95], v[20:23]\n" \
  "ds_read_b128 v[84:87], v10 offset:12288\n" \
  "v_permlane16_swap_b32 v116, v118\n" \
  "v_mfma_f32_16x16x32_bf16 v[28:31], a[12:15], v[92:95], v[28:31]\n" \
  "ds_read_b128 v[88:91], v10 offset:14336\n" \
  "v_permlane16_swap_b32 v117, v119\n" \
  "v_mfma_f32_16x16x32_bf16 v[36:39], v[92:95], a[140:143], v[36:39]\n" \
  "v_permlane16_swap_b32 v120, v122\n" \
  "v_mfma_f32_16x16x32_bf16 v[24:27], v[140:143], v[96:99], v[24:27]\n" \
  "v_permlane16_swap_b32 v121, v123\n" \
  "v_mfma_f32_16x16x32_bf16 v[32:35], a[12:15], v[96:99], v[32:35]\n" \
  "v_mfma_f32_16x16x32_bf16 v[40:43], v[96:99], a[140:143], v[40:43]\n" \
  "s_waitcnt lgkmcnt(4)\n" \
  "v_mfma_f32_16x16x32_bf16 v[20:23], v[144:147], v[68:71], v[20:23]\n" \
  "ds_read_b128 v[92:95], v11 offset:12288\n" \
  "buffer_store_dwordx4 v[116:119], v14, s[76:79], s88 offen sc1\n" \
  "v_mfma_f32_16x16x32_bf16 v[28:31], a[16:19], v[68:71], v[28:31]\n" \
  "ds_read_b128 v[96:99], v11 offset:14336\n" \
  "buffer_store_dwordx4 v[120:123], v18, s[76:79], s89 offen sc1\n" \
  "v_mfma_f32_16x16x32_bf16 v[36:39], v[68:71], a[144:147], v[36:39]\n" \
  "buffer_store_dwordx4 v[124:127], v15, s[76:79], s90 offen sc1\n" \
  "v_mfma_f32_16x16x32_bf16 v[24:27], v[144:147], v[72:75], v[24:27]\n" \
  "v_mfma_f32_16x16x32_bf16 v[32:35], a[16:19], v[72:75], v[32:35]\n" \
  "v_mfma_f32_16x16x32_bf16 v[40:43], v[72:75], a[144:147], v[40:43]\n" \
  "s_waitcnt lgkmcnt(4)\n" \
  "v_mfma_f32_16x16x32_bf16 v[20:23], v[148:151], v[76:79], v[20:23]\n" \
  "ds_read_b128 v[68:71], v10 offset:16384\n" \
  "v_mfma_f32_16x16x32_bf16 v[28:31], a[20:23], v[76:79], v[28:31]\n" \
  "ds_read_b128 v[72:75], v10 offset:18432\n" \
  "v_mfma_f32_16x16x32_bf16 v[36:39], v[76:79], a[148:151], v[36:39]\n" \
  "v_mfma_f32_16x16x32_bf16 v[24:27], v[148:151], v[80:83], v[24:27]\n" \
  "s_add_u32 m0, s91, 98304\n" \
  "s_nop 0\n" \
  "buffer_load_dwordx4 v7, s[72:75], s87 offen lds\n" \
  "s_add_u32 s87, s87, 128\n" \
  "v_mfma_f32_16x16x32_bf16 v[32:35], a[20:23], v[80:83], v[32:35]\n" \
  "v_mfma_f32_16x16x32_bf16 v[40:43], v[80:83], a[148:151], v[40:43]\n" \
  "s_waitcnt lgkmcnt(4)\n" \
  "v_mfma_f32_16x16x32_bf16 v[20:23], v[152:155], v[84:87], v[20:23]\n" \
  "ds_read_b128 v[76:79], v11 offset:16384\n" \
  "v_mfma_f32_16x16x32_bf16 v[28:31], a[24:27], v[84:87], v[28:31]\n" \
  "ds_read_b128 v[80:83], v11 offset:18432\n" \
  "v_mfma_f32_16x16x32_bf16 v[36:39], v[84:87], a[152:155], v[36:39]\n" \
  "v_mfma_f32_16x16x32_bf16 v[24:27], v[152:155], v[88:91], v[24:27]\n" \
  "s_add_u32 m0, s91, 102400\n" \
  "s_nop 0\n" \
  "buffer_load_dwordx4 v7, s[72:75], s87 offen lds\n" \
  "s_add_u32 s87, s87, 128\n" \
  "v_mfma_f32_16x16x32_bf16 v[32:35], a[24:27], v[88:91], v[32:35]\n" \
  "v_mfma_f32_16x16x32_bf16 v[40:43], v[88:91], a[152:155], v[40:43]\n" \
  "s_waitcnt lgkmcnt(4)\n" \
  "v_mfma_f32_16x16x32_bf16 v[20:23], v[156:159], v[92:95], v[20:23]\n" \
  "ds_read_b128 v[84:87], v10 offset:20480\n" \
  "v_mfma_f32_16x16x32_bf16 v[28:31], a[28:31], v[92:95], v[28:31]\n" \
  "ds_read_b128 v[88:91], v10 offset:22528\n" \
  "v_mfma_f32_16x16x32_bf16 v[36:39], v[92:95], a[156:159], v[36:39]\n" \
  "v_mfma_f32_16x16x32_bf16 v[24:27], v[156:159], v[96:99], v[24:27]\n" \
  "s_add_u32 m0, s91, 106496\n" \
  "s_nop 0\n" \
  "buffer_load_dwordx4 v7, s[72:75], s87 offen lds\n" \
  "s_add_u32 s87, s87, 128\n" \
  "v_mfma_f32_16x16x32_bf16 v[32:35], a[28:31], v[96:99], v[32:35]\n" \
  "v_mfma_f32_16x16x32_bf16 v[40:43], v[96:99], a[156:159], v[40:43]\n" \
  "s_waitcnt lgkmcnt(4)\n" \
  "v_mfma_f32_16x16x32_bf16 v[20:23], v[160:163], v[68:71], v[20:23]\n" \
  "ds_read_b128 v[92:95], v11 offset:20480\n" \
  "v_mfma_f32_16x16x32_bf16 v[28:31], a[32:35], v[68:71], v[28:31]\n" \
  "ds_read_b128 v[96:99], v11 offset:22528\n" \
  "v_mfma_f32_16x16x32_bf16 v[36:39], v[68:71], a[160:163], v[36:39]\n" \
  "v_mfma_f32_16x16x32_bf16 v[24:27], v[160:163], v[72:75], v[24:27]\n" \
  "s_add_u32 m0, s91, 110592\n" \
  "s_nop 0\n" \
  "buffer_load_dwordx4 v7, s[72:75], s87 offen lds\n" \
  "s_add_u32 s87, s87, 128\n" \
  "v_mfma_f32_16x16x32_bf16 v[32:35], a[32:35], v[72:75], v[32:35]\n" \
  "v_mfma_f32_16x16x32_bf16 v[40:43], v[72:75], a[160:163], v[40:43]\n" \
  "s_waitcnt lgkmcnt(4)\n" \
  "v_mfma_f32_16x16x32_bf16 v[20:23], v[164:167], v[76:79], v[20:23]\n" \
  "ds_read_b128 v[68:71], v10 offset:24576\n" \
  "v_mfma_f32_16x16x32_bf16 v[28:31], a[36:39], v[76:79], v[28:31]\n" \
  "ds_read_b128 v[72:75], v10 offset:26624\n" \
  "v_mfma_f32_16x16x32_bf16 v[36:39], v[76:79], a[164:167], v[36:39]\n" \
  "v_mfma_f32_16x16x32_bf16 v[24:27], v[164:167], v[80:83], v[24:27]\n" \
  "s_add_u32 m0, s91, 114688\n" \
  "s_nop 0\n" \
  "buffer_load_dwordx4 v7, s[72:75], s87 offen lds\n" \
  "s_add_u32 s87, s87, 128\n" \
  "v_mfma_f32_16x16x32_bf16 v[32:35], a[36:39], v[80:83], v[32:35]\n" \
  "v_mfma_f32_16x16x32_bf16 v[40:43], v[80:83], a[164:167], v[40:43]\n" \
  "s_waitcnt lgkmcnt(4)\n" \
  "v_mfma_f32_16x16x32_bf16 v[20:23], v[168:171], v[84:87], v[20:23]\n" \
  "ds_read_b128 v[76:79], v11 offset:24576\n" \
  "v_mfma_f32_16x16x32_bf16 v[28:31], a[40:43], v[84:87], v[28:31]\n" \
  "ds_read_b128 v[80:83], v11 offset:26624\n" \
  "v_mfma_f32_16x16x32_bf16 v[36:39], v[84:87], a[168:171], v[36:39]\n" \
  "v_mfma_f32_16x16x32_bf16 v[24:27], v[168:171], v[88:91], v[24:27]\n" \
  "s_add_u32 m0, s91, 118784\n" \
  "s_nop 0\n" \
  "buffer_load_dwordx4 v7, s[72:75], s87 offen lds\n" \
  "s_add_u32 s87, s87, 128\n" \
  "v_mfma_f32_16x16x32_bf16 v[32:35], a[40:43], v[88:91], v[32:35]\n" \
  "v_mfma_f32_16x16x32_bf16 v[40:43], v[88:91], a[168:171], v[40:43]\n" \
  "s_waitcnt lgkmcnt(4)\n" \
  "v_mfma_f32_16x16x32_bf16 v[20:23], v[172:175], v[92:95], v[20:23]\n" \
  "ds_read_b128 v[84:87], v10 offset:28672\n" \
  "v_mfma_f32_16x16x32_bf16 v[28:31], a[44:47], v[92:95], v[28:31]\n" \
  "ds_read_b128 v[88:91], v10 offset:30720\n" \
  "v_mfma_f32_16x16x32_bf16 v[36:39], v[92:95], a[172:175], v[36:39]\n" \
  "v_mfma_f32_16x16x32_bf16 v[24:27], v[172:175], v[96:99], v[24:27]\n" \
  "s_add_u32 m0, s91, 122880\n" \
  "s_nop 0\n" \
  "buffer_load_dwordx4 v7, s[72:75], s87 offen lds\n" \
  "s_add_u32 s87, s87, 128\n" \
  "v_mfma_f32_16x16x32_bf16 v[32:35], a[44:47], v[96:99], v[32:35]\n" \
  "v_mfma_f32_16x16x32_bf16 v[40:43], v[96:99], a[172:175], v[40:43]\n" \
  "s_waitcnt lgkmcnt(4)\n" \
  "v_mfma_f32_16x16x32_bf16 v[20:23], v[176:179], v[68:71], v[20:23]\n" \
  "ds_read_b128 v[92:95], v11 offset:28672\n" \
  "v_mfma_f32_16x16x32_bf16 v[28:31], a[48:51], v[68:71], v[28:31]\n" \
  "ds_read_b128 v[96:99], v11 offset:30720\n" \
  "v_mfma_f32_16x16x32_bf16 v[36:39], v[68:71], a[176:179], v[36:39]\n" \
  "v_mfma_f32_16x16x32_bf16 v[24:27], v[176:179], v[72:75], v[24:27]\n" \
  "s_add_u32 m0, s91, 126976\n" \
  "s_nop 0\n" \
  "buffer_load_dwordx4 v7, s[72:75], s87 offen lds\n" \
  "s_add_u32 s87, s87, 63616\n" \
  "s_min_u32 s87, s87, s96\n" \
  "v_mfma_f32_16x16x32_bf16 v[32:35], a[48:51], v[72:75], v[32:35]\n" \
  "v_mfma_f32_16x16x32_bf16 v[40:43], v[72:75], a[176:179], v[40:43]\n" \
  "s_waitcnt lgkmcnt(4)\n" \
  "v_mfma_f32_16x16x32_bf16 v[20:23], v[180:183], v[76:79], v[20:23]\n" \
  "ds_read_b128 v[68:71], v10 offset:32768\n" \
  "v_mfma_f32_16x16x32_bf16 v[28:31], a[52:55], v[76:79], v[28:31]\n" \
  "ds_read_b128 v[72:75], v10 offset:34816\n" \
  "v_mfma_f32_16x16x32_bf16 v[36:39], v[76:79], a[180:183], v[36:39]\n" \
  "v_mfma_f32_16x16x32_bf16 v[24:27], v[180:183], v[80:83], v[24:27]\n" \
  "v_mfma_f32_16x16x32_bf16 v[32:35], a[52:55], v[80:83], v[32:35]\n" \
  "v_mfma_f32_16x16x32_bf16 v[40:43], v[80:83], a[180:183], v[40:43]\n" \
  "s_waitcnt lgkmcnt(4)\n" \
  "v_mfma_f32_16x16x32_bf16 v[20:23], v[184:187], v[84:87], v[20:23]\n" \
  "ds_read_b128 v[76:79], v11 offset:32768\n" \
  "v_mfma_f32_16x16x32_bf16 v[28:31], a[56:59], v[84:87], v[28:31]\n" \
  "ds_read_b128 v[80:83], v11 offset:34816\n" \
  "v_mfma_f32_16x16x32_bf16 v[36:39], v[84:87], a[184:187], v[36:39]\n" \
  "v_mfma_f32_16x16x32_bf16 v[24:27], v[184:187], v[88:91], v[24:27]\n" \
  "v_mfma_f32_16x16x32_bf16 v[32:35], a[56:59], v[88:91], v[32:35]\n" \
  "v_mfma_f32_16x16x32_bf16 v[40:43], v[88:91], a[184:187], v[40:43]\n" \
  "s_waitcnt lgkmcnt(4)\n" \
  "v_mfma_f32_16x16x32_bf16 v[20:23], v[188:191], v[92:95], v[20:23]\n" \
  "ds_read_b128 v[84:87], v10 offset:36864\n" \
  "v_mfma_f32_16x16x32_bf16 v[28:31], a[60:63], v[92:95], v[28:31]\n" \
  "ds_read_b128 v[88:91], v10 offset:38912\n" \
  "v_mfma_f32_16x16x32_bf16 v[36:39], v[92:95], a[188:191], v[36:39]\n" \
  "v_mfma_f32_16x16x32_bf16 v[24:27], v[188:191], v[96:99], v[24:27]\n" \
  "v_mfma_f32_16x16x32_bf16 v[32:35], a[60:63], v[96:99], v[32:35]\n" \
  "v_mfma_f32_16x16x32_bf16 v[40:43], v[96:99], a[188:191], v[40:43]\n" \
  "s_waitcnt vmcnt(8)\n" \
  "s_barrier\n" \
  "s_waitcnt lgkmcnt(4)\n" \
  "v_mfma_f32_16x16x32_bf16 v[20:23], v[192:195], v[68:71], v[20:23]\n" \
  "ds_read_b128 v[92:95], v11 offset:36864\n" \
  "buffer_store_dword v17, v16, s[80:83], 0 offen sc1\n" \
  "v_mfma_f32_16x16x32_bf16 v[28:31], a[64:67], v[68:71], v[28:31]\n" \
  "ds_read_b128 v[96:99], v11 offset:38912\n" \
  "v_add_u32 v17, 1, v17\n" \
  "v_mfma_f32_16x16x32_bf16 v[36:39], v[68:71], a[192:195], v[36:39]\n" \
  "v_and_b32 v14, 0x7fffffff, v14\n" \
  "v_mfma_f32_16x16x32_bf16 v[24:27], v[192:195], v[72:75], v[24:27]\n" \
  "v_and_b32 v15, 0x7fffffff, v15\n" \
  "v_mfma_f32_16x16x32_bf16 v[32:35], a[64:67], v[72:75], v[32:35]\n" \
  "v_and_b32 v18, 0x7fffffff, v18\n" \
  "v_mfma_f32_16x16x32_bf16 v[40:43], v[72:75], a[192:195], v[40:43]\n" \
  "s_waitcnt lgkmcnt(4)\n" \
  "v_mfma_f32_16x16x32_bf16 v[20:23], v[196:199], v[76:79], v[20:23]\n" \
  "ds_read_b128 v[68:71], v10 offset:40960\n" \
  "v_mfma_f32_16x16x32_bf16 v[28:31], a[68:71], v[76:79], v[28:31]\n" \
  "ds_read_b128 v[72:75], v10 offset:43008\n" \
  "v_mfma_f32_16x16x32_bf16 v[36:39], v[76:79], a[196:199], v[36:39]\n" \
  "v_mfma_f32_16x16x32_bf16 v[24:27], v[196:199], v[80:83], v[24:27]\n" \
  "v_mfma_f32_16x16x32_bf16 v[32:35], a[68:71], v[80:83], v[32:35]\n" \
  "v_mfma_f32_16x16x32_bf16 v[40:43], v[80:83], a[196:199], v[40:43]\n" \
  "s_waitcnt lgkmcnt(4)\n" \
  "v_mfma_f32_16x16x32_bf16 v[20:23], v[200:203], v[84:87], v[20:23]\n" \
  "ds_read_b128 v[76:79], v11 offset:40960\n" \
  "v_mfma_f32_16x16x32_bf16 v[28:31], a[72:75], v[84:87], v[28:31]\n" \
  "ds_read_b128 v[80:83], v11 offset:43008\n" \
  "v_mfma_f32_16x16x32_bf16 v[36:39], v[84:87], a[200:203], v[36:39]\n" \
  "v_mfma_f32_16x16x32_bf16 v[24:27], v[200:203], v[88:91], v[24:27]\n" \
  "v_mfma_f32_16x16x32_bf16 v[32:35], a[72:75], v[88:91], v[32:35]\n" \
  "v_mfma_f32_16x16x32_bf16 v[40:43], v[88:91], a[200:203], v[40:43]\n" \
  "s_waitcnt lgkmcnt(4)\n" \
  "v_mfma_f32_16x16x32_bf16 v[20:23], v[204:207], v[92:95], v[20:23]\n" \
  "ds_read_b128 v[84:87], v10 offset:45056\n" \
  "v_mfma_f32_16x16x32_bf16 v[28:31], a[76:79], v[92:95], v[28:31]\n" \
  "ds_read_b128 v[88:91], v10 offset:47104\n" \
  "v_mfma_f32_16x16x32_bf16 v[36:39], v[92:95], a[204:207], v[36:39]\n" \
  "v_mfma_f32_16x16x32_bf16 v[24:27], v[204:207], v[96:99], v[24:27]\n" \
  "v_mfma_f32_16x16x32_bf16 v[32:35], a[76:79], v[96:99], v[32:35]\n" \
  "v_mfma_f32_16x16x32_bf16 v[40:43], v[96:99], a[204:207], v[40:43]\n" \
  "s_waitcnt lgkmcnt(4)\n" \
  "v_mfma_f32_16x16x32_bf16 v[20:23], v[208:211], v[68:71], v[20:23]\n" \
  "ds_read_b128 v[92:95], v11 offset:45056\n" \
  "v_mfma_f32_16x16x32_bf16 v[28:31], a[80:83], v[68:71], v[28:31]\n" \
  "ds_read_b128 v[96:99], v11 offset:47104\n" \
  "v_mfma_f32_16x16x32_bf16 v[36:39], v[68:71], a[208:211], v[36:39]\n" \
  "v_mfma_f32_16x16x32_bf16 v[24:27], v[208:211], v[72:75], v[24:27]\n" \
  "v_mfma_f32_16x16x32_bf16 v[32:35], a[80:83], v[72:75], v[32:35]\n" \
  "v_mfma_f32_16x16x32_bf16 v[40:43], v[72:75], a[208:211], v[40:43]\n" \
  "s_waitcnt lgkmcnt(4)\n" \
  "v_mfma_f32_16x16x32_bf16 v[20:23], v[212:215], v[76:79], v[20:23]\n" \
  "ds_read_b128 v[68:71], v10 offset:49152\n" \
  "v_mfma_f32_16x16x32_bf16 v[28:31], a[84:87], v[76:79], v[28:31]\n" \
  "ds_read_b128 v[72:75], v10 offset:51200\n" \
  "v_mfma_f32_16x16x32_bf16 v[36:39], v[76:79], a[212:215], v[36:39]\n" \
  "v_mfma_f32_16x16x32_bf16 v[24:27], v[212:215], v[80:83], v[24:27]\n" \
  "s_add_u32 m0, s91, 0\n" \
  "s_nop 0\n" \
  "buffer_load_dwordx4 v7, s[72:75], s87 offen lds\n" \
  "s_add_u32 s87, s87, 128\n" \
  "v_mfma_f32_16x16x32_bf16 v[32:35], a[84:87], v[80:83], v[32:35]\n" \
  "v_mfma_f32_16x16x32_bf16 v[40:43], v[80:83], a[212:215], v[40:43]\n" \
  "s_waitcnt lgkmcnt(4)\n" \
  "v_mfma_f32_16x16x32_bf16 v[20:23], v[216:219], v[84:87], v[20:23]\n" \
  "ds_read_b128 v[76:79], v11 offset:49152\n" \
  "v_mfma_f32_16x16x32_bf16 v[28:31], a[88:91], v[84:87], v[28:31]\n" \
  "ds_read_b128 v[80:83], v11 offset:51200\n" \
  "v_mfma_f32_16x16x32_bf16 v[36:39], v[84:87], a[216:219], v[36:39]\n" \
  "v_mfma_f32_16x16x32_bf16 v[24:27], v[216:219], v[88:91], v[24:27]\n" \
  "s_add_u32 m0, s91, 4096\n" \
  "s_nop 0\n" \
  "buffer_load_dwordx4 v7, s[72:75], s87 offen lds\n" \
  "s_add_u32 s87, s87, 128\n" \
  "v_mfma_f32_16x16x32_bf16 v[32:35], a[88:91], v[88:91], v[32:35]\n" \
  "v_mfma_f32_16x16x32_bf16 v[40:43], v[88:91], a[216:219], v[40:43]\n" \
  "s_waitcnt lgkmcnt(4)\n" \
  "v_mfma_f32_16x16x32_bf16 v[20:23], v[220:223], v[92:95], v[20:23]\n" \
  "ds_read_b128 v[84:87], v10 offset:53248\n" \
  "v_mfma_f32_16x16x32_bf16 v[28:31], a[92:95], v[92:95], v[28:31]\n" \
  "ds_read_b128 v[88:91], v10 offset:55296\n" \
  "v_mfma_f32_16x16x32_bf16 v[36:39], v[92:95], a[220:223], v[36:39]\n" \
  "v_mfma_f32_16x16x32_bf16 v[24:27], v[220:223], v[96:99], v[24:27]\n" \
  "s_add_u32 m0, s91, 8192\n" \
  "s_nop 0\n" \
  "buffer_load_dwordx4 v7, s[72:75], s87 offen lds\n" \
  "s_add_u32 s87, s87, 128\n" \
  "v_mfma_f32_16x16x32_bf16 v[32:35], a[92:95], v[96:99], v[32:35]\n" \
  "v_mfma_f32_16x16x32_bf16 v[40:43], v[96:99], a[220:223], v[40:43]\n" \
  "s_waitcnt lgkmcnt(4)\n" \
  "v_mfma_f32_16x16x32_bf16 v[20:23], v[224:227], v[68:71], v[20:23]\n" \
  "ds_read_b128 v[92:95], v11 offset:53248\n" \
  "v_mfma_f32_16x16x32_bf16 v[28:31], a[96:99], v[68:71], v[28:31]\n" \
  "ds_read_b128 v[96:99], v11 offset:55296\n" \
  "v_mfma_f32_16x16x32_bf16 v[36:39], v[68:71], a[224:227], v[36:39]\n" \
  "v_mfma_f32_16x16x32_bf16 v[24:27], v[224:227], v[72:75], v[24:27]\n" \
  "s_add_u32 m0, s91, 12288\n" \
  "s_nop 0\n" \
  "buffer_load_dwordx4 v7, s[72:75], s87 offen lds\n" \
  "s_add_u32 s87, s87, 128\n" \
  "v_mfma_f32_16x16x32_bf16 v[32:35], a[96:99], v[72:75], v[32:35]\n" \
  "v_mfma_f32_16x16x32_bf16 v[40:43], v[72:75], a[224:227], v[40:43]\n" \
  "s_waitcnt lgkmcnt(4)\n" \
  "v_mfma_f32_16x16x32_bf16 v[20:23], v[228:231], v[76:79], v[20:23]\n" \
  "ds_read_b128 v[68:71], v10 offset:57344\n" \
  "v_mfma_f32_16x16x32_bf16 v[28:31], a[100:103], v[76:79], v[28:31]\n" \
  "ds_read_b128 v[72:75], v10 offset:59392\n" \
  "v_mfma_f32_16x16x32_bf16 v[36:39], v[76:79], a[228:231], v[36:39]\n" \
  "v_mfma_f32_16x16x32_bf16 v[24:27], v[228:231], v[80:83], v[24:27]\n" \
  "s_add_u32 m0, s91, 16384\n" \
  "s_nop 0\n" \
  "buffer_load_dwordx4 v7, s[72:75], s87 offen lds\n" \
  "s_add_u32 s87, s87, 128\n" \
  "v_mfma_f32_16x16x32_bf16 v[32:35], a[100:103], v[80:83], v[32:35]\n" \
  "v_mfma_f32_16x16x32_bf16 v[40:43], v[80:83], a[228:231], v[40:43]\n" \
  "s_waitcnt lgkmcnt(4)\n" \
  "v_mfma_f32_16x16x32_bf16 v[20:23], v[232:235], v[84:87], v[20:23]\n" \
  "ds_read_b128 v[76:79], v11 offset:57344\n" \
  "v_mfma_f32_16x16x32_bf16 v[28:31], a[104:107], v[84:87], v[28:31]\n" \
  "ds_read_b128 v[80:83], v11 offset:59392\n" \
  "v_mfma_f32_16x16x32_bf16 v[36:39], v[84:87], a[232:235], v[36:39]\n" \
  "v_mfma_f32_16x16x32_bf16 v[24:27], v[232:235], v[88:91], v[24:27]\n" \
  "s_add_u32 m0, s91, 20480\n" \
  "s_nop 0\n" \
  "buffer_load_dwordx4 v7, s[72:75], s87 offen lds\n" \
  "s_add_u32 s87, s87, 128\n" \
  "v_mfma_f32_16x16x32_bf16 v[32:35], a[104:107], v[88:91], v[32:35]\n" \
  "v_mfma_f32_16x16x32_bf16 v[40:43], v[88:91], a[232:235], v[40:43]\n" \
  "s_waitcnt lgkmcnt(4)\n" \
  "v_mfma_f32_16x16x32_bf16 v[20:23], v[236:239], v[92:95], v[20:23]\n" \
  "ds_read_b128 v[84:87], v10 offset:61440\n" \
  "v_mfma_f32_16x16x32_bf16 v[28:31], a[108:111], v[92:95], v[28:31]\n" \
  "ds_read_b128 v[88:91], v10 offset:63488\n" \
  "v_mfma_f32_16x16x32_bf16 v[36:39], v[92:95], a[236:239], v[36:39]\n" \
  "v_mfma_f32_16x16x32_bf16 v[24:27], v[236:239], v[96:99], v[24:27]\n" \
  "s_add_u32 m0, s91, 24576\n" \
  "s_nop 0\n" \
  "buffer_load_dwordx4 v7, s[72:75], s87 offen lds\n" \
  "s_add_u32 s87, s87, 128\n" \
  "v_mfma_f32_16x16x32_bf16 v[32:35], a[108:111], v[96:99], v[32:35]\n" \
  "v_mfma_f32_16x16x32_bf16 v[40:43], v[96:99], a[236:239], v[40:43]\n" \
  "s_waitcnt lgkmcnt(4)\n" \
  "v_mfma_f32_16x16x32_bf16 v[20:23], v[240:243], v[68:71], v[20:23]\n" \
  "ds_read_b128 v[92:95], v11 offset:61440\n" \
  "v_mfma_f32_16x16x32_bf16 v[28:31], a[112:115], v[68:71], v[28:31]\n" \
  "ds_read_b128 v[96:99], v11 offset:63488\n" \
  "v_mfma_f32_16x16x32_bf16 v[36:39], v[68:71], a[240:243], v[36:39]\n" \
  "v_mfma_f32_16x16x32_bf16 v[24:27], v[240:243], v[72:75], v[24:27]\n" \
  "s_add_u32 m0, s91, 28672\n" \
  "s_nop 0\n" \
  "buffer_load_dwordx4 v7, s[72:75], s87 offen lds\n" \
  "s_add_u32 s87, s87, 128\n" \
  "v_mfma_f32_16x16x32_bf16 v[32:35], a[112:115], v[72:75], v[32:35]\n" \
  "v_mfma_f32_16x16x32_bf16 v[40:43], v[72:75], a[240:243], v[40:43]\n" \
  "s_waitcnt lgkmcnt(4)\n" \
  "v_mfma_f32_16x16x32_bf16 v[20:23], v[244:247], v[76:79], v[20:23]\n" \
  "ds_read_b128 v[68:71], v12 offset:0\n" \
  "v_mfma_f32_16x16x32_bf16 v[28:31], a[116:119], v[76:79], v[28:31]\n" \
  "ds_read_b128 v[72:75], v12 offset:2048\n" \
  "v_mfma_f32_16x16x32_bf16 v[36:39], v[76:79], a[244:247], v[36:39]\n" \
  "v_mfma_f32_16x16x32_bf16 v[24:27], v[244:247], v[80:83], v[24:27]\n" \
  "v_mfma_f32_16x16x32_bf16 v[32:35], a[116:119], v[80:83], v[32:35]\n" \
  "v_mfma_f32_16x16x32_bf16 v[40:43], v[80:83], a[244:247], v[40:43]\n" \
  "s_waitcnt lgkmcnt(4)\n" \
  "v_mfma_f32_16x16x32_bf16 v[20:23], v[248:251], v[84:87], v[20:23]\n" \
  "ds_read_b128 v[76:79], v13 offset:0\n" \
  "v_mfma_f32_16x16x32_bf16 v[28:31], a[120:123], v[84:87], v[28:31]\n" \
  "ds_read_b128 v[80:83], v13 offset:2048\n" \
  "v_mfma_f32_16x16x32_bf16 v[36:39], v[84:87], a[248:251], v[36:39]\n" \
  "v_mfma_f32_16x16x32_bf16 v[24:27], v[248:251], v[88:91], v[24:27]\n" \
  "v_mfma_f32_16x16x32_bf16 v[32:35], a[120:123], v[88:91], v[32:35]\n" \
  "v_mfma_f32_16x16x32_bf16 v[40:43], v[88:91], a[248:251], v[40:43]\n" \
  "s_waitcnt lgkmcnt(4)\n" \
  "v_mfma_f32_16x16x32_bf16 v[20:23], v[252:255], v[92:95], v[20:23]\n" \
  "ds_read_b128 v[84:87], v12 offset:4096\n" \
  "v_mfma_f32_16x16x32_bf16 v[28:31], a[124:127], v[92:95], v[28:31]\n" \
  "ds_read_b128 v[88:91], v12 offset:6144\n" \
  "v_mfma_f32_16x16x32_bf16 v[36:39], v[92:95], a[252:255], v[36:39]\n" \
  "v_mfma_f32_16x16x32_bf16 v[24:27], v[252:255], v[96:99], v[24:27]\n" \
  "v_mfma_f32_16x16x32_bf16 v[32:35], a[124:127], v[96:99], v[32:35]\n" \
  "v_mfma_f32_16x16x32_bf16 v[40:43], v[96:99], a[252:255], v[40:43]\n" \
  "s_cmp_eq_u32 s85, 64\n" \
  "s_cbranch_scc1 LX1_%=\n" \
  "s_waitcnt vmcnt(8)\n" \
  "s_barrier\n" \
  "s_waitcnt lgkmcnt(4)\n" \
  "v_mfma_f32_16x16x32_bf16 v[44:47], v[128:131], v[68:71], 0\n" \
  "ds_read_b128 v[92:95], v13 offset:4096\n" \
  "s_sub_u32 s89, s85, 1\n" \
  "s_and_b32 s89, s89, 63\n" \
  "v_mfma_f32_16x16x32_bf16 v[52:55], a[0:3], v[68:71], 0\n" \
  "ds_read_b128 v[96:99], v13 offset:6144\n" \
  "s_lshr_b32 s84, s89, 2\n" \
  "s_xnor_b32 s84, s84, %9\n" \
  "v_mfma_f32_16x16x32_bf16 v[60:63], v[68:71], a[128:131], 0\n" \
  "s_and_b32 s84, s84, 1\n" \
  "s_lshl_b32 s84, s84, 13\n" \
  "v_mfma_f32_16x16x32_bf16 v[48:51], v[128:131], v[72:75], 0\n" \
  "s_lshl_b32 s88, s89, 19\n" \
  "s_or_b32 s88, s88, s84\n" \
  "v_mfma_f32_16x16x32_bf16 v[56:59], a[0:3], v[72:75], 0\n" \
  "s_add_u32 s89, s88, " STR(QKV_SZ32) "\n" \
  "s_add_u32 s90, s89, " STR(QKV_SZ32) "\n" \
  "v_mfma_f32_16x16x32_bf16 v[64:67], v[72:75], a[128:131], 0\n" \
  "s_add_u32 s85, s85, 1\n" \
  "s_waitcnt lgkmcnt(4)\n" \
  "v_mfma_f32_16x16x32_bf16 v[44:47], v[132:135], v[76:79], v[44:47]\n" \
  "ds_read_b128 v[68:71], v12 offset:8192\n" \
  "v_cvt_pk_bf16_f32 v116, v20, v21\n" \
  "v_mfma_f32_16x16x32_bf16 v[52:55], a[4:7], v[76:79], v[52:55]\n" \
  "ds_read_b128 v[72:75], v12 offset:10240\n" \
  "v_cvt_pk_bf16_f32 v117, v22, v23\n" \
  "v_mfma_f32_16x16x32_bf16 v[60:63], v[76:79], a[132:135], v[60:63]\n" \
  "v_cvt_pk_bf16_f32 v118, v24, v25\n" \
  "v_mfma_f32_16x16x32_bf16 v[48:51], v[132:135], v[80:83], v[48:51]\n" \
  "v_cvt_pk_bf16_f32 v119, v26, v27\n" \
  "v_mfma_f32_16x16x32_bf16 v[56:59], a[4:7], v[80:83], v[56:59]\n" \
  "v_cvt_pk_bf16_f32 v120, v28, v29\n" \
  "v_mfma_f32_16x16x32_bf16 v[64:67], v[80:83], a[132:135], v[64:67]\n" \
  "v_cvt_pk_bf16_f32 v121, v30, v31\n" \
  "s_waitcnt lgkmcnt(4)\n" \
  "v_mfma_f32_16x16x32_bf16 v[44:47], v[136:139], v[84:87], v[44:47]\n" \
  "ds_read_b128 v[76:79], v13 offset:8192\n" \
  "v_cvt_pk_bf16_f32 v122, v32, v33\n" \
  "v_mfma_f32_16x16x32_bf16 v[52:55], a[8:11], v[84:87], v[52:55]\n" \
  "ds_read_b128 v[80:83], v13 offset:10240\n" \
  "v_cvt_pk_bf16_f32 v123, v34, v35\n" \
  "v_mfma_f32_16x16x32_bf16 v[60:63], v[84:87], a[136:139], v[60:63]\n" \
  "v_cvt_pk_bf16_f32 v124, v36, v37\n" \
  "v_mfma_f32_16x16x32_bf16 v[48:51], v[136:139], v[88:91], v[48:51]\n" \
  "v_cvt_pk_bf16_f32 v125, v38, v39\n" \
  "v_mfma_f32_16x16x32_bf16 v[56:59], a[8:11], v[88:91], v[56:59]\n" \
  "v_cvt_pk_bf16_f32 v126, v40, v41\n" \
  "v_mfma_f32_16x16x32_bf16 v[64:67], v[88:91], a[136:139], v[64:67]\n" \
  "v_cvt_pk_bf16_f32 v127, v42, v43\n" \
  "s_waitcnt lgkmcnt(4)\n" \
  "v_mfma_f32_16x16x32_bf16 v[44:47], v[140:143], v[92:95], v[44:47]\n" \
  "ds_read_b128 v[84:87], v12 offset:12288\n" \
  "v_permlane16_swap_b32 v116, v118\n" \
  "v_mfma_f32_16x16x32_bf16 v[52:55], a[12:15], v[92:95], v[52:55]\n" \
  "ds_read_b128 v[88:91], v12 offset:14336\n" \
  "v_permlane16_swap_b32 v117, v119\n" \
  "v_mfma_f32_16x16x32_bf16 v[60:63], v[92:95], a[140:143], v[60:63]\n" \
  "v_permlane16_swap_b32 v120, v122\n" \
  "v_mfma_f32_16x16x32_bf16 v[48:51], v[140:143], v[96:99], v[48:51]\n" \
  "v_permlane16_swap_b32 v121, v123\n" \
  "v_mfma_f32_16x16x32_bf16 v[56:59], a[12:15], v[96:99], v[56:59]\n" \
  "v_mfma_f32_16x16x32_bf16 v[64:67], v[96:99], a[140:143], v[64:67]\n" \
  "s_waitcnt lgkmcnt(4)\n" \
  "v_mfma_f32_16x16x32_bf16 v[44:47], v[144:147], v[68:71], v[44:47]\n" \
  "ds_read_b128 v[92:95], v13 offset:12288\n" \
  "buffer_store_dwordx4 v[116:119], v14, s[76:79], s88 offen sc1\n" \
  "v_mfma_f32_16x16x32_bf16 v[52:55], a[16:19], v[68:71], v[52:55]\n" \
  "ds_read_b128 v[96:99], v13 offset:14336\n" \
  "buffer_store_dwordx4 v[120:123], v18, s[76:79], s89 offen sc1\n" \
  "v_mfma_f32_16x16x32_bf16 v[60:63], v[68:71], a[144:147], v[60:63]\n" \
  "buffer_store_dwordx4 v[124:127], v15, s[76:79], s90 offen sc1\n" \
  "v_mfma_f32_16x16x32_bf16 v[48:51], v[144:147], v[72:75], v[48:51]\n" \
  "v_mfma_f32_16x16x32_bf16 v[56:59], a[16:19], v[72:75], v[56:59]\n" \
  "v_mfma_f32_16x16x32_bf16 v[64:67], v[72:75], a[144:147], v[64:67]\n" \
  "s_waitcnt lgkmcnt(4)\n" \
  "v_mfma_f32_16x16x32_bf16 v[44:47], v[148:151], v[76:79], v[44:47]\n" \
  "ds_read_b128 v[68:71], v12 offset:16384\n" \
  "v_mfma_f32_16x16x32_bf16 v[52:55], a[20:23], v[76:79], v[52:55]\n" \
  "ds_read_b128 v[72:75], v12 offset:18432\n" \
  "v_mfma_f32_16x16x32_bf16 v[60:63], v[76:79], a[148:151], v[60:63]\n" \
  "v_mfma_f32_16x16x32_bf16 v[48:51], v[148:151], v[80:83], v[48:51]\n" \
  "s_add_u32 m0, s91, 32768\n" \
  "s_nop 0\n" \
  "buffer_load_dwordx4 v7, s[72:75], s87 offen lds\n" \
  "s_add_u32 s87, s87, 128\n" \
  "v_mfma_f32_16x16x32_bf16 v[56:59], a[20:23], v[80:83], v[56:59]\n" \
  "v_mfma_f32_16x16x32_bf16 v[64:67], v[80:83], a[148:151], v[64:67]\n" \
  "s_waitcnt lgkmcnt(4)\n" \
  "v_mfma_f32_16x16x32_bf16 v[44:47], v[152:155], v[84:87], v[44:47]\n" \
  "ds_read_b128 v[76:79], v13 offset:16384\n" \
  "v_mfma_f32_16x16x32_bf16 v[52:55], a[24:27], v[84:87], v[52:55]\n" \
  "ds_read_b128 v[80:83], v13 offset:18432\n" \
  "v_mfma_f32_16x16x32_bf16 v[60:63], v[84:87], a[152:155], v[60:63]\n" \
  "v_mfma_f32_16x16x32_bf16 v[48:51], v[152:155], v[88:91], v[48:51]\n" \
  "s_add_u32 m0, s91, 36864\n" \
  "s_nop 0\n" \
  "buffer_load_dwordx4 v7, s[72:75], s87 offen lds\n" \
  "s_add_u32 s87, s87, 128\n" \
  "v_mfma_f32_16x16x32_bf16 v[56:59], a[24:27], v[88:91], v[56:59]\n" \
  "v_mfma_f32_16x16x32_bf16 v[64:67], v[88:91], a[152:155], v[64:67]\n" \
  "s_waitcnt lgkmcnt(4)\n" \
  "v_mfma_f32_16x16x32_bf16 v[44:47], v[156:159], v[92:95], v[44:47]\n" \
  "ds_read_b128 v[84:87], v12 offset:20480\n" \
  "v_mfma_f32_16x16x32_bf16 v[52:55], a[28:31], v[92:95], v[52:55]\n" \
  "ds_read_b128 v[88:91], v12 offset:22528\n" \
  "v_mfma_f32_16x16x32_bf16 v[60:63], v[92:95], a[156:159], v[60:63]\n" \
  "v_mfma_f32_16x16x32_bf16 v[48:51], v[156:159], v[96:99], v[48:51]\n" \
  "s_add_u32 m0, s91, 40960\n" \
  "s_nop 0\n" \
  "buffer_load_dwordx4 v7, s[72:75], s87 offen lds\n" \
  "s_add_u32 s87, s87, 128\n" \
  "v_mfma_f32_16x16x32_bf16 v[56:59], a[28:31], v[96:99], v[56:59]\n" \
  "v_mfma_f32_16x16x32_bf16 v[64:67], v[96:99], a[156:159], v[64:67]\n" \
  "s_waitcnt lgkmcnt(4)\n" \
  "v_mfma_f32_16x16x32_bf16 v[44:47], v[160:163], v[68:71], v[44:47]\n" \
  "ds_read_b128 v[92:95], v13 offset:20480\n" \
  "v_mfma_f32_16x16x32_bf16 v[52:55], a[32:35], v[68:71], v[52:55]\n" \
  "ds_read_b128 v[96:99], v13 offset:22528\n" \
  "v_mfma_f32_16x16x32_bf16 v[60:63], v[68:71], a[160:163], v[60:63]\n" \
  "v_mfma_f32_16x16x32_bf16 v[48:51], v[160:163], v[72:75], v[48:51]\n" \
  "s_add_u32 m0, s91, 45056\n" \
  "s_nop 0\n" \
  "buffer_load_dwordx4 v7, s[72:75], s87 offen lds\n" \
  "s_add_u32 s87, s87, 128\n" \
  "v_mfma_f32_16x16x32_bf16 v[56:59], a[32:35], v[72:75], v[56:59]\n" \
  "v_mfma_f32_16x16x32_bf16 v[64:67], v[72:75], a[160:163], v[64:67]\n" \
  "s_waitcnt lgkmcnt(4)\n" \
  "v_mfma_f32_16x16x32_bf16 v[44:47], v[164:167], v[76:79], v[44:47]\n" \
  "ds_read_b128 v[68:71], v12 offset:24576\n" \
  "v_mfma_f32_16x16x32_bf16 v[52:55], a[36:39], v[76:79], v[52:55]\n" \
  "ds_read_b128 v[72:75], v12 offset:26624\n" \
  "v_mfma_f32_16x16x32_bf16 v[60:63], v[76:79], a[164:167], v[60:63]\n" \
  "v_mfma_f32_16x16x32_bf16 v[48:51], v[164:167], v[80:83], v[48:51]\n" \
  "s_add_u32 m0, s91, 49152\n" \
  "s_nop 0\n" \
  "buffer_load_dwordx4 v7, s[72:75], s87 offen lds\n" \
  "s_add_u32 s87, s87, 128\n" \
  "v_mfma_f32_16x16x32_bf16 v[56:59], a[36:39], v[80:83], v[56:59]\n" \
  "v_mfma_f32_16x16x32_bf16 v[64:67], v[80:83], a[164:167], v[64:67]\n" \
  "s_waitcnt lgkmcnt(4)\n" \
  "v_mfma_f32_16x16x32_bf16 v[44:47], v[168:171], v[84:87], v[44:47]\n" \
  "ds_read_b128 v[76:79], v13 offset:24576\n" \
  "v_mfma_f32_16x16x32_bf16 v[52:55], a[40:43], v[84:87], v[52:55]\n" \
  "ds_read_b128 v[80:83], v13 offset:26624\n" \
  "v_mfma_f32_16x16x32_bf16 v[60:63], v[84:87], a[168:171], v[60:63]\n" \
  "v_mfma_f32_16x16x32_bf16 v[48:51], v[168:171], v[88:91], v[48:51]\n" \
  "s_add_u32 m0, s91, 53248\n" \
  "s_nop 0\n" \
  "buffer_load_dwordx4 v7, s[72:75], s87 offen lds\n" \
  "s_add_u32 s87, s87, 128\n" \
  "v_mfma_f32_16x16x32_bf16 v[56:59], a[40:43], v[88:91], v[56:59]\n" \
  "v_mfma_f32_16x16x32_bf16 v[64:67], v[88:91], a[168:171], v[64:67]\n" \
  "s_waitcnt lgkmcnt(4)\n" \
  "v_mfma_f32_16x16x32_bf16 v[44:47], v[172:175], v[92:95], v[44:47]\n" \
  "ds_read_b128 v[84:87], v12 offset:28672\n" \
  "v_mfma_f32_16x16x32_bf16 v[52:55], a[44:47], v[92:95], v[52:55]\n" \
  "ds_read_b128 v[88:91], v12 offset:30720\n" \
  "v_mfma_f32_16x16x32_bf16 v[60:63], v[92:95], a[172:175], v[60:63]\n" \
  "v_mfma_f32_16x16x32_bf16 v[48:51], v[172:175], v[96:99], v[48:51]\n" \
  "s_add_u32 m0, s91, 57344\n" \
  "s_nop 0\n" \
  "buffer_load_dwordx4 v7, s[72:75], s87 offen lds\n" \
  "s_add_u32 s87, s87, 128\n" \
  "v_mfma_f32_16x16x32_bf16 v[56:59], a[44:47], v[96:99], v[56:59]\n" \
  "v_mfma_f32_16x16x32_bf16 v[64:67], v[96:99], a[172:175], v[64:67]\n" \
  "s_waitcnt lgkmcnt(4)\n" \
  "v_mfma_f32_16x16x32_bf16 v[44:47], v[176:179], v[68:71], v[44:47]\n" \
  "ds_read_b128 v[92:95], v13 offset:28672\n" \
  "v_mfma_f32_16x16x32_bf16 v[52:55], a[48:51], v[68:71], v[52:55]\n" \
  "ds_read_b128 v[96:99], v13 offset:30720\n" \
  "v_mfma_f32_16x16x32_bf16 v[60:63], v[68:71], a[176:179], v[60:63]\n" \
  "v_mfma_f32_16x16x32_bf16 v[48:51], v[176:179], v[72:75], v[48:51]\n" \
  "s_add_u32 m0, s91, 61440\n" \
  "s_nop 0\n" \
  "buffer_load_dwordx4 v7, s[72:75], s87 offen lds\n" \
  "s_add_u32 s87, s87, 63616\n" \
  "s_min_u32 s87, s87, s96\n" \
  "v_mfma_f32_16x16x32_bf16 v[56:59], a[48:51], v[72:75], v[56:59]\n" \
  "v_mfma_f32_16x16x32_bf16 v[64:67], v[72:75], a[176:179], v[64:67]\n" \
  "s_waitcnt lgkmcnt(4)\n" \
  "v_mfma_f32_16x16x32_bf16 v[44:47], v[180:183], v[76:79], v[44:47]\n" \
  "ds_read_b128 v[68:71], v12 offset:32768\n" \
  "v_mfma_f32_16x16x32_bf16 v[52:55], a[52:55], v[76:79], v[52:55]\n" \
  "ds_read_b128 v[72:75], v12 offset:34816\n" \
  "v_mfma_f32_16x16x32_bf16 v[60:63], v[76:79], a[180:183], v[60:63]\n" \
  "v_mfma_f32_16x16x32_bf16 v[48:51], v[180:183], v[80:83], v[48:51]\n" \
  "v_mfma_f32_16x16x32_bf16 v[56:59], a[52:55], v[80:83], v[56:59]\n" \
  "v_mfma_f32_16x16x32_bf16 v[64:67], v[80:83], a[180:183], v[64:67]\n" \
  "s_waitcnt lgkmcnt(4)\n" \
  "v_mfma_f32_16x16x32_bf16 v[44:47], v[184:187], v[84:87], v[44:47]\n" \
  "ds_read_b128 v[76:79], v13 offset:32768\n" \
  "v_mfma_f32_16x16x32_bf16 v[52:55], a[56:59], v[84:87], v[52:55]\n" \
  "ds_read_b128 v[80:83], v13 offset:34816\n" \
  "v_mfma_f32_16x16x32_bf16 v[60:63], v[84:87], a[184:187], v[60:63]\n" \
  "v_mfma_f32_16x16x32_bf16 v[48:51], v[184:187], v[88:91], v[48:51]\n" \
  "v_mfma_f32_16x16x32_bf16 v[56:59], a[56:59], v[88:91], v[56:59]\n" \
  "v_mfma_f32_16x16x32_bf16 v[64:67], v[88:91], a[184:187], v[64:67]\n" \
  "s_waitcnt lgkmcnt(4)\n" \
  "v_mfma_f32_16x16x32_bf16 v[44:47], v[188:191], v[92:95], v[44:47]\n" \
  "ds_read_b128 v[84:87], v12 offset:36864\n" \
  "v_mfma_f32_16x16x32_bf16 v[52:55], a[60:63], v[92:95], v[52:55]\n" \
  "ds_read_b128 v[88:91], v12 offset:38912\n" \
  "v_mfma_f32_16x16x32_bf16 v[60:63], v[92:95], a[188:191], v[60:63]\n" \
  "v_mfma_f32_16x16x32_bf16 v[48:51], v[188:191], v[96:99], v[48:51]\n" \
  "v_mfma_f32_16x16x32_bf16 v[56:59], a[60:63], v[96:99], v[56:59]\n" \
  "v_mfma_f32_16x16x32_bf16 v[64:67], v[96:99], a[188:191], v[64:67]\n" \
  "s_waitcnt vmcnt(8)\n" \
  "s_barrier\n" \
  "s_waitcnt lgkmcnt(4)\n" \
  "v_mfma_f32_16x16x32_bf16 v[44:47], v[192:195], v[68:71], v[44:47]\n" \
  "ds_read_b128 v[92:95], v13 offset:36864\n" \
  "buffer_store_dword v17, v16, s[80:83], 0 offen sc1\n" \
  "v_mfma_f32_16x16x32_bf16 v[52:55], a[64:67], v[68:71], v[52:55]\n" \
  "ds_read_b128 v[96:99], v13 offset:38912\n" \
  "v_add_u32 v17, 1, v17\n" \
  "v_mfma_f32_16x16x32_bf16 v[60:63], v[68:71], a[192:195], v[60:63]\n" \
  "v_and_b32 v14, 0x7fffffff, v14\n" \
  "v_mfma_f32_16x16x32_bf16 v[48:51], v[192:195], v[72:75], v[48:51]\n" \
  "v_and_b32 v15, 0x7fffffff, v15\n" \
  "v_mfma_f32_16x16x32_bf16 v[56:59], a[64:67], v[72:75], v[56:59]\n" \
  "v_and_b32 v18, 0x7fffffff, v18\n" \
  "v_mfma_f32_16x16x32_bf16 v[64:67], v[72:75], a[192:195], v[64:67]\n" \
  "s_waitcnt lgkmcnt(4)\n" \
  "v_mfma_f32_16x16x32_bf16 v[44:47], v[196:199], v[76:79], v[44:47]\n" \
  "ds_read_b128 v[68:71], v12 offset:40960\n" \
  "v_mfma_f32_16x16x32_bf16 v[52:55], a[68:71], v[76:79], v[52:55]\n" \
  "ds_read_b128 v[72:75], v12 offset:43008\n" \
  "v_mfma_f32_16x16x32_bf16 v[60:63], v[76:79], a[196:199], v[60:63]\n" \
  "v_mfma_f32_16x16x32_bf16 v[48:51], v[196:199], v[80:83], v[48:51]\n" \
  "v_mfma_f32_16x16x32_bf16 v[56:59], a[68:71], v[80:83], v[56:59]\n" \
  "v_mfma_f32_16x16x32_bf16 v[64:67], v[80:83], a[196:199], v[64:67]\n" \
  "s_waitcnt lgkmcnt(4)\n" \
  "v_mfma_f32_16x16x32_bf16 v[44:47], v[200:203], v[84:87], v[44:47]\n" \
  "ds_read_b128 v[76:79], v13 offset:40960\n" \
  "v_mfma_f32_16x16x32_bf16 v[52:55], a[72:75], v[84:87], v[52:55]\n" \
  "ds_read_b128 v[80:83], v13 offset:43008\n" \
  "v_mfma_f32_16x16x32_bf16 v[60:63], v[84:87], a[200:203], v[60:63]\n" \
  "v_mfma_f32_16x16x32_bf16 v[48:51], v[200:203], v[88:91], v[48:51]\n" \
  "v_mfma_f32_16x16x32_bf16 v[56:59], a[72:75], v[88:91], v[56:59]\n" \
  "v_mfma_f32_16x16x32_bf16 v[64:67], v[88:91], a[200:203], v[64:67]\n" \
  "s_waitcnt lgkmcnt(4)\n" \
  "v_mfma_f32_16x16x32_bf16 v[44:47], v[204:207], v[92:95], v[44:47]\n" \
  "ds_read_b128 v[84:87], v12 offset:45056\n" \
  "v_mfma_f32_16x16x32_bf16 v[52:55], a[76:79], v[92:95], v[52:55]\n" \
  "ds_read_b128 v[88:91], v12 offset:47104\n" \
  "v_mfma_f32_16x16x32_bf16 v[60:63], v[92:95], a[204:207], v[60:63]\n" \
  "v_mfma_f32_16x16x32_bf16 v[48:51], v[204:207], v[96:99], v[48:51]\n" \
  "v_mfma_f32_16x16x32_bf16 v[56:59], a[76:79], v[96:99], v[56:59]\n" \
  "v_mfma_f32_16x16x32_bf16 v[64:67], v[96:99], a[204:207], v[64:67]\n" \
  "s_waitcnt lgkmcnt(4)\n" \
  "v_mfma_f32_16x16x32_bf16 v[44:47], v[208:211], v[68:71], v[44:47]\n" \
  "ds_read_b128 v[92:95], v13 offset:45056\n" \
  "v_mfma_f32_16x16x32_bf16 v[52:55], a[80:83], v[68:71], v[52:55]\n" \
  "ds_read_b128 v[96:99], v13 offset:47104\n" \
  "v_mfma_f32_16x16x32_bf16 v[60:63], v[68:71], a[208:211], v[60:63]\n" \
  "v_mfma_f32_16x16x32_bf16 v[48:51], v[208:211], v[72:75], v[48:51]\n" \
  "v_mfma_f32_16x16x32_bf16 v[56:59], a[80:83], v[72:75], v[56:59]\n" \
  "v_mfma_f32_16x16x32_bf16 v[64:67], v[72:75], a[208:211], v[64:67]\n" \
  "s_waitcnt lgkmcnt(4)\n" \
  "v_mfma_f32_16x16x32_bf16 v[44:47], v[212:215], v[76:79], v[44:47]\n" \
  "ds_read_b128 v[68:71], v12 offset:49152\n" \
  "v_mfma_f32_16x16x32_bf16 v[52:55], a[84:87], v[76:79], v[52:55]\n" \
  "ds_read_b128 v[72:75], v12 offset:51200\n" \
  "v_mfma_f32_16x16x32_bf16 v[60:63], v[76:79], a[212:215], v[60:63]\n" \
  "v_mfma_f32_16x16x32_bf16 v[48:51], v[212:215], v[80:83], v[48:51]\n" \
  "s_add_u32 m0, s91, 65536\n" \
  "s_nop 0\n" \
  "buffer_load_dwordx4 v7, s[72:75], s87 offen lds\n" \
  "s_add_u32 s87, s87, 128\n" \
  "v_mfma_f32_16x16x32_bf16 v[56:59], a[84:87], v[80:83], v[56:59]\n" \
  "v_mfma_f32_16x16x32_bf16 v[64:67], v[80:83], a[212:215], v[64:67]\n" \
  "s_waitcnt lgkmcnt(4)\n" \
  "v_mfma_f32_16x16x32_bf16 v[44:47], v[216:219], v[84:87], v[44:47]\n" \
  "ds_read_b128 v[76:79], v13 offset:49152\n" \
  "v_mfma_f32_16x16x32_bf16 v[52:55], a[88:91], v[84:87], v[52:55]\n" \
  "ds_read_b128 v[80:83], v13 offset:51200\n" \
  "v_mfma_f32_16x16x32_bf16 v[60:63], v[84:87], a[216:219], v[60:63]\n" \
  "v_mfma_f32_16x16x32_bf16 v[48:51], v[216:219], v[88:91], v[48:51]\n" \
  "s_add_u32 m0, s91, 69632\n" \
  "s_nop 0\n" \
  "buffer_load_dwordx4 v7, s[72:75], s87 offen lds\n" \
  "s_add_u32 s87, s87, 128\n" \
  "v_mfma_f32_16x16x32_bf16 v[56:59], a[88:91], v[88:91], v[56:59]\n" \
  "v_mfma_f32_16x16x32_bf16 v[64:67], v[88:91], a[216:219], v[64:67]\n" \
  "s_waitcnt lgkmcnt(4)\n" \
  "v_mfma_f32_16x16x32_bf16 v[44:47], v[220:223], v[92:95], v[44:47]\n" \
  "ds_read_b128 v[84:87], v12 offset:53248\n" \
  "v_mfma_f32_16x16x32_bf16 v[52:55], a[92:95], v[92:95], v[52:55]\n" \
  "ds_read_b128 v[88:91], v12 offset:55296\n" \
  "v_mfma_f32_16x16x32_bf16 v[60:63], v[92:95], a[220:223], v[60:63]\n" \
  "v_mfma_f32_16x16x32_bf16 v[48:51], v[220:223], v[96:99], v[48:51]\n" \
  "s_add_u32 m0, s91, 73728\n" \
  "s_nop 0\n" \
  "buffer_load_dwordx4 v7, s[72:75], s87 offen lds\n" \
  "s_add_u32 s87, s87, 128\n" \
  "v_mfma_f32_16x16x32_bf16 v[56:59], a[92:95], v[96:99], v[56:59]\n" \
  "v_mfma_f32_16x16x32_bf16 v[64:67], v[96:99], a[220:223], v[64:67]\n" \
  "s_waitcnt lgkmcnt(4)\n" \
  "v_mfma_f32_16x16x32_bf16 v[44:47], v[224:227], v[68:71], v[44:47]\n" \
  "ds_read_b128 v[92:95], v13 offset:53248\n" \
  "v_mfma_f32_16x16x32_bf16 v[52:55], a[96:99], v[68:71], v[52:55]\n" \
  "ds_read_b128 v[96:99], v13 offset:55296\n" \
  "v_mfma_f32_16x16x32_bf16 v[60:63], v[68:71], a[224:227], v[60:63]\n" \
  "v_mfma_f32_16x16x32_bf16 v[48:51], v[224:227], v[72:75], v[48:51]\n" \
  "s_add_u32 m0, s91, 77824\n" \
  "s_nop 0\n" \
  "buffer_load_dwordx4 v7, s[72:75], s87 offen lds\n" \
  "s_add_u32 s87, s87, 128\n" \
  "v_mfma_f32_16x16x32_bf16 v[56:59], a[96:99], v[72:75], v[56:59]\n" \
  "v_mfma_f32_16x16x32_bf16 v[64:67], v[72:75], a[224:227], v[64:67]\n" \
  "s_waitcnt lgkmcnt(4)\n" \
  "v_mfma_f32_16x16x32_bf16 v[44:47], v[228:231], v[76:79], v[44:47]\n" \
  "ds_read_b128 v[68:71], v12 offset:57344\n" \
  "v_mfma_f32_16x16x32_bf16 v[52:55], a[100:103], v[76:79], v[52:55]\n" \
  "ds_read_b128 v[72:75], v12 offset:59392\n" \
  "v_mfma_f32_16x16x32_bf16 v[60:63], v[76:79], a[228:231], v[60:63]\n" \
  "v_mfma_f32_16x16x32_bf16 v[48:51], v[228:231], v[80:83], v[48:51]\n" \
  "s_add_u32 m0, s91, 81920\n" \
  "s_nop 0\n" \
  "buffer_load_dwordx4 v7, s[72:75], s87 offen lds\n" \
  "s_add_u32 s87, s87, 128\n" \
  "v_mfma_f32_16x16x32_bf16 v[56:59], a[100:103], v[80:83], v[56:59]\n" \
  "v_mfma_f32_16x16x32_bf16 v[64:67], v[80:83], a[228:231], v[64:67]\n" \
  "s_waitcnt lgkmcnt(4)\n" \
  "v_mfma_f32_16x16x32_bf16 v[44:47], v[232:235], v[84:87], v[44:47]\n" \
  "ds_read_b128 v[76:79], v13 offset:57344\n" \
  "v_mfma_f32_16x16x32_bf16 v[52:55], a[104:107], v[84:87], v[52:55]\n" \
  "ds_read_b128 v[80:83], v13 offset:59392\n" \
  "v_mfma_f32_16x16x32_bf16 v[60:63], v[84:87], a[232:235], v[60:63]\n" \
  "v_mfma_f32_16x16x32_bf16 v[48:51], v[232:235], v[88:91], v[48:51]\n" \
  "s_add_u32 m0, s91, 86016\n" \
  "s_nop 0\n" \
  "buffer_load_dwordx4 v7, s[72:75], s87 offen lds\n" \
  "s_add_u32 s87, s87, 128\n" \
  "v_mfma_f32_16x16x32_bf16 v[56:59], a[104:107], v[88:91], v[56:59]\n" \
  "v_mfma_f32_16x16x32_bf16 v[64:67], v[88:91], a[232:235], v[64:67]\n" \
  "s_waitcnt lgkmcnt(4)\n" \
  "v_mfma_f32_16x16x32_bf16 v[44:47], v[236:239], v[92:95], v[44:47]\n" \
  "ds_read_b128 v[84:87], v12 offset:61440\n" \
  "v_mfma_f32_16x16x32_bf16 v[52:55], a[108:111], v[92:95], v[52:55]\n" \
  "ds_read_b128 v[88:91], v12 offset:63488\n" \
  "v_mfma_f32_16x16x32_bf16 v[60:63], v[92:95], a[236:239], v[60:63]\n" \
  "v_mfma_f32_16x16x32_bf16 v[48:51], v[236:239], v[96:99], v[48:51]\n" \
  "s_add_u32 m0, s91, 90112\n" \
  "s_nop 0\n" \
  "buffer_load_dwordx4 v7, s[72:75], s87 offen lds\n" \
  "s_add_u32 s87, s87, 128\n" \
  "v_mfma_f32_16x16x32_bf16 v[56:59], a[108:111], v[96:99], v[56:59]\n" \
  "v_mfma_f32_16x16x32_bf16 v[64:67], v[96:99], a[236:239], v[64:67]\n" \
  "s_waitcnt lgkmcnt(4)\n" \
  "v_mfma_f32_16x16x32_bf16 v[44:47], v[240:243], v[68:71], v[44:47]\n" \
  "ds_read_b128 v[92:95], v13 offset:61440\n" \
  "v_mfma_f32_16x16x32_bf16 v[52:55], a[112:115], v[68:71], v[52:55]\n" \
  "ds_read_b128 v[96:99], v13 offset:63488\n" \
  "v_mfma_f32_16x16x32_bf16 v[60:63], v[68:71], a[240:243], v[60:63]\n" \
  "v_mfma_f32_16x16x32_bf16 v[48:51], v[240:243], v[72:75], v[48:51]\n" \
  "s_add_u32 m0, s91, 94208\n" \
  "s_nop 0\n" \
  "buffer_load_dwordx4 v7, s[72:75], s87 offen lds\n" \
  "s_add_u32 s87, s87, 128\n" \
  "v_mfma_f32_16x16x32_bf16 v[56:59], a[112:115], v[72:75], v[56:59]\n" \
  "v_mfma_f32_16x16x32_bf16 v[64:67], v[72:75], a[240:243], v[64:67]\n" \
  "s_waitcnt lgkmcnt(4)\n" \
  "v_mfma_f32_16x16x32_bf16 v[44:47], v[244:247], v[76:79], v[44:47]\n" \
  "ds_read_b128 v[68:71], v10 offset:0\n" \
  "v_mfma_f32_16x16x32_bf16 v[52:55], a[116:119], v[76:79], v[52:55]\n" \
  "ds_read_b128 v[72:75], v10 offset:2048\n" \
  "v_mfma_f32_16x16x32_bf16 v[60:63], v[76:79], a[244:247], v[60:63]\n" \
  "v_mfma_f32_16x16x32_bf16 v[48:51], v[244:247], v[80:83], v[48:51]\n" \
  "v_mfma_f32_16x16x32_bf16 v[56:59], a[116:119], v[80:83], v[56:59]\n" \
  "v_mfma_f32_16x16x32_bf16 v[64:67], v[80:83], a[244:247], v[64:67]\n" \
  "s_waitcnt lgkmcnt(4)\n" \
  "v_mfma_f32_16x16x32_bf16 v[44:47], v[248:251], v[84:87], v[44:47]\n" \
  "ds_read_b128 v[76:79], v11 offset:0\n" \
  "v_mfma_f32_16x16x32_bf16 v[52:55], a[120:123], v[84:87], v[52:55]\n" \
  "ds_read_b128 v[80:83], v11 offset:2048\n" \
  "v_mfma_f32_16x16x32_bf16 v[60:63], v[84:87], a[248:251], v[60:63]\n" \
  "v_mfma_f32_16x16x32_bf16 v[48:51], v[248:251], v[88:91], v[48:51]\n" \
  "v_mfma_f32_16x16x32_bf16 v[56:59], a[120:123], v[88:91], v[56:59]\n" \
  "v_mfma_f32_16x16x32_bf16 v[64:67], v[88:91], a[248:251], v[64:67]\n" \
  "s_waitcnt lgkmcnt(4)\n" \
  "v_mfma_f32_16x16x32_bf16 v[44:47], v[252:255], v[92:95], v[44:47]\n" \
  "ds_read_b128 v[84:87], v10 offset:4096\n" \
  "v_mfma_f32_16x16x32_bf16 v[52:55], a[124:127], v[92:95], v[52:55]\n" \
  "ds_read_b128 v[88:91], v10 offset:6144\n" \
  "v_mfma_f32_16x16x32_bf16 v[60:63], v[92:95], a[252:255], v[60:63]\n" \
  "v_mfma_f32_16x16x32_bf16 v[48:51], v[252:255], v[96:99], v[48:51]\n" \
  "v_mfma_f32_16x16x32_bf16 v[56:59], a[124:127], v[96:99], v[56:59]\n" \
  "v_mfma_f32_16x16x32_bf16 v[64:67], v[96:99], a[252:255], v[64:67]\n" \
  "s_branch LG_%=\n" \
  "LX0_%=:\n" \
  "s_sub_u32 s89, s85, 1\n" \
  "s_and_b32 s89, s89, 63\n" \
  "s_lshr_b32 s84, s89, 2\n" \
  "s_xnor_b32 s84, s84, %9\n" \
  "s_and_b32 s84, s84, 1\n" \
  "s_lshl_b32 s84, s84, 13\n" \
  "s_lshl_b32 s88, s89, 19\n" \
  "s_or_b32 s88, s88, s84\n" \
  "s_add_u32 s89, s88, " STR(QKV_SZ32) "\n" \
  "s_add_u32 s90, s89, " STR(QKV_SZ32) "\n" \
  "s_add_u32 s85, s85, 1\n" \
  "v_cvt_pk_bf16_f32 v116, v44, v45\n" \
  "v_cvt_pk_bf16_f32 v117, v46, v47\n" \
  "v_cvt_pk_bf16_f32 v118, v48, v49\n" \
  "v_cvt_pk_bf16_f32 v119, v50, v51\n" \
  "v_cvt_pk_bf16_f32 v120, v52, v53\n" \
  "v_cvt_pk_bf16_f32 v121, v54, v55\n" \
  "v_cvt_pk_bf16_f32 v122, v56, v57\n" \
  "v_cvt_pk_bf16_f32 v123, v58, v59\n" \
  "v_cvt_pk_bf16_f32 v124, v60, v61\n" \
  "v_cvt_pk_bf16_f32 v125, v62, v63\n" \
  "v_cvt_pk_bf16_f32 v126, v64, v65\n" \
  "v_cvt_pk_bf16_f32 v127, v66, v67\n" \
  "s_nop 1\n" \
  "v_permlane16_swap_b32 v116, v118\n" \
  "v_permlane16_swap_b32 v117, v119\n" \
  "v_permlane16_swap_b32 v120, v122\n" \
  "v_permlane16_swap_b32 v121, v123\n" \
  "buffer_store_dwordx4 v[116:119], v14, s[76:79], s88 offen sc1\n" \
  "buffer_store_dwordx4 v[120:123], v18, s[76:79], s89 offen sc1\n" \
  "buffer_store_dwordx4 v[124:127], v15, s[76:79], s90 offen sc1\n" \
  "s_branch LZ_%=\n" \
  "LX1_%=:\n" \
  "s_sub_u32 s89, s85, 1\n" \
  "s_and_b32 s89, s89, 63\n" \
  "s_lshr_b32 s84, s89, 2\n" \
  "s_xnor_b32 s84, s84, %9\n" \
  "s_and_b32 s84, s84, 1\n" \
  "s_lshl_b32 s84, s84, 13\n" \
  "s_lshl_b32 s88, s89, 19\n" \
  "s_or_b32 s88, s88, s84\n" \
  "s_add_u32 s89, s88, " STR(QKV_SZ32) "\n" \
  "s_add_u32 s90, s89, " STR(QKV_SZ32) "\n" \
  "s_add_u32 s85, s85, 1\n" \
  "v_cvt_pk_bf16_f32 v116, v20, v21\n" \
  "v_cvt_pk_bf16_f32 v117, v22, v23\n" \
  "v_cvt_pk_bf16_f32 v118, v24, v25\n" \
  "v_cvt_pk_bf16_f32 v119, v26, v27\n" \
  "v_cvt_pk_bf16_f32 v120, v28, v29\n" \
  "v_cvt_pk_bf16_f32 v121, v30, v31\n" \
  "v_cvt_pk_bf16_f32 v122, v32, v33\n" \
  "v_cvt_pk_bf16_f32 v123, v34, v35\n" \
  "v_cvt_pk_bf16_f32 v124, v36, v37\n" \
  "v_cvt_pk_bf16_f32 v125, v38, v39\n" \
  "v_cvt_pk_bf16_f32 v126, v40, v41\n" \
  "v_cvt_pk_bf16_f32 v127, v42, v43\n" \
  "s_nop 1\n" \
  "v_permlane16_swap_b32 v116, v118\n" \
  "v_permlane16_swap_b32 v117, v119\n" \
  "v_permlane16_swap_b32 v120, v122\n" \
  "v_permlane16_swap_b32 v121, v123\n" \
  "buffer_store_dwordx4 v[116:119], v14, s[76:79], s88 offen sc1\n" \
  "buffer_store_dwordx4 v[120:123], v18, s[76:79], s89 offen sc1\n" \
  "buffer_store_dwordx4 v[124:127], v15, s[76:79], s90 offen sc1\n" \
  "s_branch LZ_%=\n" \
  "LZ_%=:\n" \
  "s_waitcnt vmcnt(0) lgkmcnt(0)\n" \
  "s_barrier\n" \
  "buffer_store_dword v17, v16, s[80:83], 0 offen sc1\n" \
  "s_waitcnt vmcnt(0)\n" \
  ""

#define XO_ASM \
  "s_mov_b64 s[68:69], %0\n" \
  "s_and_b32 s69, s69, 0xffff\n" \
  "s_mov_b32 s70, 8388608\n" \
  "s_mov_b32 s71, 0x00020000\n" \
  "s_mov_b64 s[72:73], %1\n" \
  "s_and_b32 s73, s73, 0xffff\n" \
  "s_mov_b32 s74, " STR(QKV_SZ32) "\n" \
  "s_mov_b32 s75, 0x00020000\n" \
  "s_mov_b64 s[76:77], %2\n" \
  "s_and_b32 s77, s77, 0xffff\n" \
  "s_mov_b32 s78, 8388608\n" \
  "s_mov_b32 s79, 0x00020000\n" \
  "s_mov_b64 s[80:81], %3\n" \
  "s_and_b32 s81, s81, 0xffff\n" \
  "s_mov_b32 s82, " STR(CTRL_BYTES) "\n" \
  "s_mov_b32 s83, 0x00020000\n" \
  "v_mbcnt_lo_u32_b32 v4, -1, 0\n" \
  "v_mbcnt_hi_u32_b32 v4, -1, v4\n" \
  "v_and_b32 v20, 15, v4\n" \
  "v_lshrrev_b32 v21, 4, v4\n" \
  "v_lshlrev_b32 v6, 4, v4\n" \
  "s_lshl_b32 s86, %5, 15\n" \
  "s_add_u32 s86, s86, %6\n" \
  "s_lshl_b32 s90, %5, 21\n" \
  "v_lshrrev_b32 v5, 3, v4\n" \
  "v_and_b32 v15, 7, v4\n" \
  "v_lshlrev_b32 v7, 11, v5\n" \
  "v_lshl_add_u32 v7, v15, 4, v7\n" \
  "v_lshlrev_b32 v5, 5, v21\n" \
  "v_xor_b32 v7, v7, v5\n" \
  "s_add_u32 m0, s86, 0\n" \
  "s_nop 0\n" \
  "buffer_load_dwordx4 v7, s[68:71], s90 offen lds\n" \
  "s_add_u32 s90, s90, 16384\n" \
  "s_add_u32 m0, s86, 1024\n" \
  "s_nop 0\n" \
  "buffer_load_dwordx4 v7, s[68:71], s90 offen lds\n" \
  "s_add_u32 s90, s90, 16384\n" \
  "s_add_u32 m0, s86, 2048\n" \
  "s_nop 0\n" \
  "buffer_load_dwordx4 v7, s[68:71], s90 offen lds\n" \
  "s_add_u32 s90, s90, 16384\n" \
  "s_add_u32 m0, s86, 3072\n" \
  "s_nop 0\n" \
  "buffer_load_dwordx4 v7, s[68:71], s90 offen lds\n" \
  "s_add_u32 s90, s90, 16384\n" \
  "s_add_u32 m0, s86, 4096\n" \
  "s_nop 0\n" \
  "buffer_load_dwordx4 v7, s[68:71], s90 offen lds\n" \
  "s_add_u32 s90, s90, 16384\n" \
  "s_add_u32 m0, s86, 5120\n" \
  "s_nop 0\n" \
  "buffer_load_dwordx4 v7, s[68:71], s90 offen lds\n" \
  "s_add_u32 s90, s90, 16384\n" \
  "s_add_u32 m0, s86, 6144\n" \
  "s_nop 0\n" \
  "buffer_load_dwordx4 v7, s[68:71], s90 offen lds\n" \
  "s_add_u32 s90, s90, 16384\n" \
  "s_add_u32 m0, s86, 7168\n" \
  "s_nop 0\n" \
  "buffer_load_dwordx4 v7, s[68:71], s90 offen lds\n" \
  "s_add_u32 s90, s90, 16384\n" \
  "s_add_u32 m0, s86, 8192\n" \
  "s_nop 0\n" \
  "buffer_load_dwordx4 v7, s[68:71], s90 offen lds\n" \
  "s_add_u32 s90, s90, 16384\n" \
  "s_add_u32 m0, s86, 9216\n" \
  "s_nop 0\n" \
  "buffer_load_dwordx4 v7, s[68:71], s90 offen lds\n" \
  "s_add_u32 s90, s90, 16384\n" \
  "s_add_u32 m0, s86, 10240\n" \
  "s_nop 0\n" \
  "buffer_load_dwordx4 v7, s[68:71], s90 offen lds\n" \
  "s_add_u32 s90, s90, 16384\n" \
  "s_add_u32 m0, s86, 11264\n" \
  "s_nop 0\n" \
  "buffer_load_dwordx4 v7, s[68:71], s90 offen lds\n" \
  "s_add_u32 s90, s90, 16384\n" \
  "s_add_u32 m0, s86, 12288\n" \
  "s_nop 0\n" \
  "buffer_load_dwordx4 v7, s[68:71], s90 offen lds\n" \
  "s_add_u32 s90, s90, 16384\n" \
  "s_add_u32 m0, s86, 13312\n" \
  "s_nop 0\n" \
  "buffer_load_dwordx4 v7, s[68:71], s90 offen lds\n" \
  "s_add_u32 s90, s90, 16384\n" \
  "s_add_u32 m0, s86, 14336\n" \
  "s_nop 0\n" \
  "buffer_load_dwordx4 v7, s[68:71], s90 offen lds\n" \
  "s_add_u32 s90, s90, 16384\n" \
  "s_add_u32 m0, s86, 15360\n" \
  "s_nop 0\n" \
  "buffer_load_dwordx4 v7, s[68:71], s90 offen lds\n" \
  "s_add_u32 s90, s90, 16384\n" \
  "s_add_u32 m0, s86, 16384\n" \
  "s_nop 0\n" \
  "buffer_load_dwordx4 v7, s[68:71], s90 offen lds\n" \
  "s_add_u32 s90, s90, 16384\n" \
  "s_add_u32 m0, s86, 17408\n" \
  "s_nop 0\n" \
  "buffer_load_dwordx4 v7, s[68:71], s90 offen lds\n" \
  "s_add_u32 s90, s90, 16384\n" \
  "s_add_u32 m0, s86, 18432\n" \
  "s_nop 0\n" \
  "buffer_load_dwordx4 v7, s[68:71], s90 offen lds\n" \
  "s_add_u32 s90, s90, 16384\n" \
  "s_add_u32 m0, s86, 19456\n" \
  "s_nop 0\n" \
  "buffer_load_dwordx4 v7, s[68:71], s90 offen lds\n" \
  "s_add_u32 s90, s90, 16384\n" \
  "s_add_u32 m0, s86, 20480\n" \
  "s_nop 0\n" \
  "buffer_load_dwordx4 v7, s[68:71], s90 offen lds\n" \
  "s_add_u32 s90, s90, 16384\n" \
  "s_add_u32 m0, s86, 21504\n" \
  "s_nop 0\n" \
  "buffer_load_dwordx4 v7, s[68:71], s90 offen lds\n" \
  "s_add_u32 s90, s90, 16384\n" \
  "s_add_u32 m0, s86, 22528\n" \
  "s_nop 0\n" \
  "buffer_load_dwordx4 v7, s[68:71], s90 offen lds\n" \
  "s_add_u32 s90, s90, 16384\n" \
  "s_add_u32 m0, s86, 23552\n" \
  "s_nop 0\n" \
  "buffer_load_dwordx4 v7, s[68:71], s90 offen lds\n" \
  "s_add_u32 s90, s90, 16384\n" \
  "s_add_u32 m0, s86, 24576\n" \
  "s_nop 0\n" \
  "buffer_load_dwordx4 v7, s[68:71], s90 offen lds\n" \
  "s_add_u32 s90, s90, 16384\n" \
  "s_add_u32 m0, s86, 25600\n" \
  "s_nop 0\n" \
  "buffer_load_dwordx4 v7, s[68:71], s90 offen lds\n" \
  "s_add_u32 s90, s90, 16384\n" \
  "s_add_u32 m0, s86, 26624\n" \
  "s_nop 0\n" \
  "buffer_load_dwordx4 v7, s[68:71], s90 offen lds\n" \
  "s_add_u32 s90, s90, 16384\n" \
  "s_add_u32 m0, s86, 27648\n" \
  "s_nop 0\n" \
  "buffer_load_dwordx4 v7, s[68:71], s90 offen lds\n" \
  "s_add_u32 s90, s90, 16384\n" \
  "s_add_u32 m0, s86, 28672\n" \
  "s_nop 0\n" \
  "buffer_load_dwordx4 v7, s[68:71], s90 offen lds\n" \
  "s_add_u32 s90, s90, 16384\n" \
  "s_add_u32 m0, s86, 29696\n" \
  "s_nop 0\n" \
  "buffer_load_dwordx4 v7, s[68:71], s90 offen lds\n" \
  "s_add_u32 s90, s90, 16384\n" \
  "s_add_u32 m0, s86, 30720\n" \
  "s_nop 0\n" \
  "buffer_load_dwordx4 v7, s[68:71], s90 offen lds\n" \
  "s_add_u32 s90, s90, 16384\n" \
  "s_add_u32 m0, s86, 31744\n" \
  "s_nop 0\n" \
  "buffer_load_dwordx4 v7, s[68:71], s90 offen lds\n" \
  "s_add_u32 s90, s90, 16384\n" \
  "v_lshrrev_b32 v5, 2, v20\n" \
  "v_lshl_add_u32 v5, v21, 2, v5\n" \
  "v_lshlrev_b32 v15, 7, v5\n" \
  "v_lshrrev_b32 v5, 1, v5\n" \
  "v_and_b32 v5, 3, v5\n" \
  "v_lshl_add_u32 v15, v5, 5, v15\n" \
  "v_and_b32 v5, 3, v4\n" \
  "v_lshl_add_u32 v15, v5, 3, v15\n" \
  "v_xor_b32 v5, 0, v15\n" \
  "v_add_u32 v16, s86, v5\n" \
  "v_xor_b32 v5, 32, v15\n" \
  "v_add_u32 v17, s86, v5\n" \
  "v_xor_b32 v5, 64, v15\n" \
  "v_add_u32 v18, s86, v5\n" \
  "v_xor_b32 v5, 96, v15\n" \
  "v_add_u32 v19, s86, v5\n" \
  "s_lshl_b32 s84, %5, 12\n" \
  "s_add_u32 s84, s84, 131072\n" \
  "s_add_u32 s84, s84, %6\n" \
  "v_xor_b32 v5, v21, v20\n" \
  "v_lshlrev_b32 v5, 4, v5\n" \
  "v_lshl_add_u32 v5, v20, 8, v5\n" \
  "v_add_u32 v5, s84, v5\n" \
  "v_xor_b32 v12, 64, v5\n" \
  "v_xor_b32 v13, 128, v5\n" \
  "v_xor_b32 v14, 192, v5\n" \
  "s_lshl_b32 s84, %5, 2\n" \
  "v_add_u32 v5, s84, v21\n" \
  "v_lshlrev_b32 v10, 12, v5\n" \
  "v_lshl_add_u32 v10, v20, 3, v10\n" \
  "v_xor_b32 v15, v5, v20\n" \
  "v_lshlrev_b32 v15, 4, v15\n" \
  "v_lshl_add_u32 v15, v5, 8, v15\n" \
  "s_add_u32 s84, %6, 131072\n" \
  "v_add_u32 v11, s84, v15\n" \
  "s_and_b32 s84, %4, 1\n" \
  "s_lshl_b32 s96, s84, 18\n" \
  "s_lshl_b32 s85, %5, 16\n" \
  "s_add_u32 s96, s96, s85\n" \
  "s_lshl_b32 s88, s84, 16\n" \
  "s_lshr_b32 s84, %4, 1\n" \
  "s_lshl_b32 s84, s84, 7\n" \
  "s_add_u32 s88, s88, s84\n" \
  "v_and_b32 v8, 7, v4\n" \
  "v_lshlrev_b32 v8, 7, v8\n" \
  "s_lshl_b32 s84, %5, 10\n" \
  "s_add_u32 s84, s84, " STR(CW_XPROG) "*4\n" \
  "v_add_u32 v8, s84, v8\n" \
  "s_mov_b32 s91, 0\n" \
  "s_mov_b32 s89, 0\n" \
  "s_lshr_b32 s84, s89, 2\n" \
  "s_xnor_b32 s84, s84, %7\n" \
  "s_and_b32 s84, s84, 1\n" \
  "s_lshl_b32 s84, s84, 13\n" \
  "s_lshl_b32 s87, s89, 19\n" \
  "s_or_b32 s87, s87, s84\n" \
  "s_add_u32 s87, s87, s96\n" \
  "s_waitcnt vmcnt(16)\n" \
  "ds_read_b64_tr_b16 v[128:129], v16 offset:0\n" \
  "ds_read_b64_tr_b16 v[130:131], v16 offset:2048\n" \
  "ds_read_b64_tr_b16 v[132:133], v17 offset:0\n" \
  "ds_read_b64_tr_b16 v[134:135], v17 offset:2048\n" \
  "ds_read_b64_tr_b16 v[136:137], v18 offset:0\n" \
  "ds_read_b64_tr_b16 v[138:139], v18 offset:2048\n" \
  "ds_read_b64_tr_b16 v[140:141], v19 offset:0\n" \
  "ds_read_b64_tr_b16 v[142:143], v19 offset:2048\n" \
  "ds_read_b64_tr_b16 v[144:145], v16 offset:4096\n" \
  "ds_read_b64_tr_b16 v[146:147], v16 offset:6144\n" \
  "ds_read_b64_tr_b16 v[148:149], v17 offset:4096\n" \
  "ds_read_b64_tr_b16 v[150:151], v17 offset:6144\n" \
  "ds_read_b64_tr_b16 v[152:153], v18 offset:4096\n" \
  "ds_read_b64_tr_b16 v[154:155], v18 offset:6144\n" \
  "ds_read_b64_tr_b16 v[156:157], v19 offset:4096\n" \
  "ds_read_b64_tr_b16 v[158:159], v19 offset:6144\n" \
  "ds_read_b64_tr_b16 v[160:161], v16 offset:8192\n" \
  "ds_read_b64_tr_b16 v[162:163], v16 offset:10240\n" \
  "ds_read_b64_tr_b16 v[164:165], v17 offset:8192\n" \
  "ds_read_b64_tr_b16 v[166:167], v17 offset:10240\n" \
  "ds_read_b64_tr_b16 v[168:169], v18 offset:8192\n" \
  "ds_read_b64_tr_b16 v[170:171], v18 offset:10240\n" \
  "ds_read_b64_tr_b16 v[172:173], v19 offset:8192\n" \
  "ds_read_b64_tr_b16 v[174:175], v19 offset:10240\n" \
  "ds_read_b64_tr_b16 v[176:177], v16 offset:12288\n" \
  "ds_read_b64_tr_b16 v[178:179], v16 offset:14336\n" \
  "ds_read_b64_tr_b16 v[180:181], v17 offset:12288\n" \
  "ds_read_b64_tr_b16 v[182:183], v17 offset:14336\n" \
  "ds_read_b64_tr_b16 v[184:185], v18 offset:12288\n" \
  "ds_read_b64_tr_b16 v[186:187], v18 offset:14336\n" \
  "ds_read_b64_tr_b16 v[188:189], v19 offset:12288\n" \
  "ds_read_b64_tr_b16 v[190:191], v19 offset:14336\n" \
  "s_waitcnt lgkmcnt(0)\n" \
  "s_add_u32 m0, s86, 0\n" \
  "s_nop 0\n" \
  "buffer_load_dwordx4 v7, s[68:71], s90 offen lds\n" \
  "s_add_u32 s90, s90, 16384\n" \
  "s_add_u32 m0, s86, 1024\n" \
  "s_nop 0\n" \
  "buffer_load_dwordx4 v7, s[68:71], s90 offen lds\n" \
  "s_add_u32 s90, s90, 16384\n" \
  "s_add_u32 m0, s86, 2048\n" \
  "s_nop 0\n" \
  "buffer_load_dwordx4 v7, s[68:71], s90 offen lds\n" \
  "s_add_u32 s90, s90, 16384\n" \
  "s_add_u32 m0, s86, 3072\n" \
  "s_nop 0\n" \
  "buffer_load_dwordx4 v7, s[68:71], s90 offen lds\n" \
  "s_add_u32 s90, s90, 16384\n" \
  "s_add_u32 m0, s86, 4096\n" \
  "s_nop 0\n" \
  "buffer_load_dwordx4 v7, s[68:71], s90 offen lds\n" \
  "s_add_u32 s90, s90, 16384\n" \
  "s_add_u32 m0, s86, 5120\n" \
  "s_nop 0\n" \
  "buffer_load_dwordx4 v7, s[68:71], s90 offen lds\n" \
  "s_add_u32 s90, s90, 16384\n" \
  "s_add_u32 m0, s86, 6144\n" \
  "s_nop 0\n" \
  "buffer_load_dwordx4 v7, s[68:71], s90 offen lds\n" \
  "s_add_u32 s90, s90, 16384\n" \
  "s_add_u32 m0, s86, 7168\n" \
  "s_nop 0\n" \
  "buffer_load_dwordx4 v7, s[68:71], s90 offen lds\n" \
  "s_add_u32 s90, s90, 16384\n" \
  "s_add_u32 m0, s86, 8192\n" \
  "s_nop 0\n" \
  "buffer_load_dwordx4 v7, s[68:71], s90 offen lds\n" \
  "s_add_u32 s90, s90, 16384\n" \
  "s_add_u32 m0, s86, 9216\n" \
  "s_nop 0\n" \
  "buffer_load_dwordx4 v7, s[68:71], s90 offen lds\n" \
  "s_add_u32 s90, s90, 16384\n" \
  "s_add_u32 m0, s86, 10240\n" \
  "s_nop 0\n" \
  "buffer_load_dwordx4 v7, s[68:71], s90 offen lds\n" \
  "s_add_u32 s90, s90, 16384\n" \
  "s_add_u32 m0, s86, 11264\n" \
  "s_nop 0\n" \
  "buffer_load_dwordx4 v7, s[68:71], s90 offen lds\n" \
  "s_add_u32 s90, s90, 16384\n" \
  "s_add_u32 m0, s86, 12288\n" \
  "s_nop 0\n" \
  "buffer_load_dwordx4 v7, s[68:71], s90 offen lds\n" \
  "s_add_u32 s90, s90, 16384\n" \
  "s_add_u32 m0, s86, 13312\n" \
  "s_nop 0\n" \
  "buffer_load_dwordx4 v7, s[68:71], s90 offen lds\n" \
  "s_add_u32 s90, s90, 16384\n" \
  "s_add_u32 m0, s86, 14336\n" \
  "s_nop 0\n" \
  "buffer_load_dwordx4 v7, s[68:71], s90 offen lds\n" \
  "s_add_u32 s90, s90, 16384\n" \
  "s_add_u32 m0, s86, 15360\n" \
  "s_nop 0\n" \
  "buffer_load_dwordx4 v7, s[68:71], s90 offen lds\n" \
  "s_add_u32 s90, s90, 16384\n" \
  "s_waitcnt vmcnt(16)\n" \
  "ds_read_b64_tr_b16 v[192:193], v16 offset:16384\n" \
  "ds_read_b64_tr_b16 v[194:195], v16 offset:18432\n" \
  "ds_read_b64_tr_b16 v[196:197], v17 offset:16384\n" \
  "ds_read_b64_tr_b16 v[198:199], v17 offset:18432\n" \
  "ds_read_b64_tr_b16 v[200:201], v18 offset:16384\n" \
  "ds_read_b64_tr_b16 v[202:203], v18 offset:18432\n" \
  "ds_read_b64_tr_b16 v[204:205], v19 offset:16384\n" \
  "ds_read_b64_tr_b16 v[206:207], v19 offset:18432\n" \
  "ds_read_b64_tr_b16 v[208:209], v16 offset:20480\n" \
  "ds_read_b64_tr_b16 v[210:211], v16 offset:22528\n" \
  "ds_read_b64_tr_b16 v[212:213], v17 offset:20480\n" \
  "ds_read_b64_tr_b16 v[214:215], v17 offset:22528\n" \
  "ds_read_b64_tr_b16 v[216:217], v18 offset:20480\n" \
  "ds_read_b64_tr_b16 v[218:219], v18 offset:22528\n" \
  "ds_read_b64_tr_b16 v[220:221], v19 offset:20480\n" \
  "ds_read_b64_tr_b16 v[222:223], v19 offset:22528\n" \
  "ds_read_b64_tr_b16 v[224:225], v16 offset:24576\n" \
  "ds_read_b64_tr_b16 v[226:227], v16 offset:26624\n" \
  "ds_read_b64_tr_b16 v[228:229], v17 offset:24576\n" \
  "ds_read_b64_tr_b16 v[230:231], v17 offset:26624\n" \
  "ds_read_b64_tr_b16 v[232:233], v18 offset:24576\n" \
  "ds_read_b64_tr_b16 v[234:235], v18 offset:26624\n" \
  "ds_read_b64_tr_b16 v[236:237], v19 offset:24576\n" \
  "ds_read_b64_tr_b16 v[238:239], v19 offset:26624\n" \
  "ds_read_b64_tr_b16 v[240:241], v16 offset:28672\n" \
  "ds_read_b64_tr_b16 v[242:243], v16 offset:30720\n" \
  "ds_read_b64_tr_b16 v[244:245], v17 offset:28672\n" \
  "ds_read_b64_tr_b16 v[246:247], v17 offset:30720\n" \
  "ds_read_b64_tr_b16 v[248:249], v18 offset:28672\n" \
  "ds_read_b64_tr_b16 v[250:251], v18 offset:30720\n" \
  "ds_read_b64_tr_b16 v[252:253], v19 offset:28672\n" \
  "ds_read_b64_tr_b16 v[254:255], v19 offset:30720\n" \
  "s_waitcnt lgkmcnt(0)\n" \
  "s_add_u32 m0, s86, 16384\n" \
  "s_nop 0\n" \
  "buffer_load_dwordx4 v7, s[68:71], s90 offen lds\n" \
  "s_add_u32 s90, s90, 16384\n" \
  "s_add_u32 m0, s86, 17408\n" \
  "s_nop 0\n" \
  "buffer_load_dwordx4 v7, s[68:71], s90 offen lds\n" \
  "s_add_u32 s90, s90, 16384\n" \
  "s_add_u32 m0, s86, 18432\n" \
  "s_nop 0\n" \
  "buffer_load_dwordx4 v7, s[68:71], s90 offen lds\n" \
  "s_add_u32 s90, s90, 16384\n" \
  "s_add_u32 m0, s86, 19456\n" \
  "s_nop 0\n" \
  "buffer_load_dwordx4 v7, s[68:71], s90 offen lds\n" \
  "s_add_u32 s90, s90, 16384\n" \
  "s_add_u32 m0, s86, 20480\n" \
  "s_nop 0\n" \
  "buffer_load_dwordx4 v7, s[68:71], s90 offen lds\n" \
  "s_add_u32 s90, s90, 16384\n" \
  "s_add_u32 m0, s86, 21504\n" \
  "s_nop 0\n" \
  "buffer_load_dwordx4 v7, s[68:71], s90 offen lds\n" \
  "s_add_u32 s90, s90, 16384\n" \
  "s_add_u32 m0, s86, 22528\n" \
  "s_nop 0\n" \
  "buffer_load_dwordx4 v7, s[68:71], s90 offen lds\n" \
  "s_add_u32 s90, s90, 16384\n" \
  "s_add_u32 m0, s86, 23552\n" \
  "s_nop 0\n" \
  "buffer_load_dwordx4 v7, s[68:71], s90 offen lds\n" \
  "s_add_u32 s90, s90, 16384\n" \
  "s_add_u32 m0, s86, 24576\n" \
  "s_nop 0\n" \
  "buffer_load_dwordx4 v7, s[68:71], s90 offen lds\n" \
  "s_add_u32 s90, s90, 16384\n" \
  "s_add_u32 m0, s86, 25600\n" \
  "s_nop 0\n" \
  "buffer_load_dwordx4 v7, s[68:71], s90 offen lds\n" \
  "s_add_u32 s90, s90, 16384\n" \
  "s_add_u32 m0, s86, 26624\n" \
  "s_nop 0\n" \
  "buffer_load_dwordx4 v7, s[68:71], s90 offen lds\n" \
  "s_add_u32 s90, s90, 16384\n" \
  "s_add_u32 m0, s86, 27648\n" \
  "s_nop 0\n" \
  "buffer_load_dwordx4 v7, s[68:71], s90 offen lds\n" \
  "s_add_u32 s90, s90, 16384\n" \
  "s_add_u32 m0, s86, 28672\n" \
  "s_nop 0\n" \
  "buffer_load_dwordx4 v7, s[68:71], s90 offen lds\n" \
  "s_add_u32 s90, s90, 16384\n" \
  "s_add_u32 m0, s86, 29696\n" \
  "s_nop 0\n" \
  "buffer_load_dwordx4 v7, s[68:71], s90 offen lds\n" \
  "s_add_u32 s90, s90, 16384\n" \
  "s_add_u32 m0, s86, 30720\n" \
  "s_nop 0\n" \
  "buffer_load_dwordx4 v7, s[68:71], s90 offen lds\n" \
  "s_add_u32 s90, s90, 16384\n" \
  "s_add_u32 m0, s86, 31744\n" \
  "s_nop 0\n" \
  "buffer_load_dwordx4 v7, s[68:71], s90 offen lds\n" \
  "s_add_u32 s90, s90, 16384\n" \
  "s_waitcnt vmcnt(16)\n" \
  "ds_read_b64_tr_b16 a[0:1], v16 offset:0\n" \
  "ds_read_b64_tr_b16 a[2:3], v16 offset:2048\n" \
  "ds_read_b64_tr_b16 a[4:5], v17 offset:0\n" \
  "ds_read_b64_tr_b16 a[6:7], v17 offset:2048\n" \
  "ds_read_b64_tr_b16 a[8:9], v18 offset:0\n" \
  "ds_read_b64_tr_b16 a[10:11], v18 offset:2048\n" \
  "ds_read_b64_tr_b16 a[12:13], v19 offset:0\n" \
  "ds_read_b64_tr_b16 a[14:15], v19 offset:2048\n" \
  "ds_read_b64_tr_b16 a[16:17], v16 offset:4096\n" \
  "ds_read_b64_tr_b16 a[18:19], v16 offset:6144\n" \
  "ds_read_b64_tr_b16 a[20:21], v17 offset:4096\n" \
  "ds_read_b64_tr_b16 a[22:23], v17 offset:6144\n" \
  "ds_read_b64_tr_b16 a[24:25], v18 offset:4096\n" \
  "ds_read_b64_tr_b16 a[26:27], v18 offset:6144\n" \
  "ds_read_b64_tr_b16 a[28:29], v19 offset:4096\n" \
  "ds_read_b64_tr_b16 a[30:31], v19 offset:6144\n" \
  "ds_read_b64_tr_b16 a[32:33], v16 offset:8192\n" \
  "ds_read_b64_tr_b16 a[34:35], v16 offset:10240\n" \
  "ds_read_b64_tr_b16 a[36:37], v17 offset:8192\n" \
  "ds_read_b64_tr_b16 a[38:39], v17 offset:10240\n" \
  "ds_read_b64_tr_b16 a[40:41], v18 offset:8192\n" \
  "ds_read_b64_tr_b16 a[42:43], v18 offset:10240\n" \
  "ds_read_b64_tr_b16 a[44:45], v19 offset:8192\n" \
  "ds_read_b64_tr_b16 a[46:47], v19 offset:10240\n" \
  "ds_read_b64_tr_b16 a[48:49], v16 offset:12288\n" \
  "ds_read_b64_tr_b16 a[50:51], v16 offset:14336\n" \
  "ds_read_b64_tr_b16 a[52:53], v17 offset:12288\n" \
  "ds_read_b64_tr_b16 a[54:55], v17 offset:14336\n" \
  "ds_read_b64_tr_b16 a[56:57], v18 offset:12288\n" \
  "ds_read_b64_tr_b16 a[58:59], v18 offset:14336\n" \
  "ds_read_b64_tr_b16 a[60:61], v19 offset:12288\n" \
  "ds_read_b64_tr_b16 a[62:63], v19 offset:14336\n" \
  "s_waitcnt lgkmcnt(0)\n" \
  "s_add_u32 m0, s86, 0\n" \
  "s_nop 0\n" \
  "buffer_load_dwordx4 v7, s[68:71], s90 offen lds\n" \
  "s_add_u32 s90, s90, 16384\n" \
  "s_add_u32 m0, s86, 1024\n" \
  "s_nop 0\n" \
  "buffer_load_dwordx4 v7, s[68:71], s90 offen lds\n" \
  "s_add_u32 s90, s90, 16384\n" \
  "s_add_u32 m0, s86, 2048\n" \
  "s_nop 0\n" \
  "buffer_load_dwordx4 v7, s[68:71], s90 offen lds\n" \
  "s_add_u32 s90, s90, 16384\n" \
  "s_add_u32 m0, s86, 3072\n" \
  "s_nop 0\n" \
  "buffer_load_dwordx4 v7, s[68:71], s90 offen lds\n" \
  "s_add_u32 s90, s90, 16384\n" \
  "s_add_u32 m0, s86, 4096\n" \
  "s_nop 0\n" \
  "buffer_load_dwordx4 v7, s[68:71], s90 offen lds\n" \
  "s_add_u32 s90, s90, 16384\n" \
  "s_add_u32 m0, s86, 5120\n" \
  "s_nop 0\n" \
  "buffer_load_dwordx4 v7, s[68:71], s90 offen lds\n" \
  "s_add_u32 s90, s90, 16384\n" \
  "s_add_u32 m0, s86, 6144\n" \
  "s_nop 0\n" \
  "buffer_load_dwordx4 v7, s[68:71], s90 offen lds\n" \
  "s_add_u32 s90, s90, 16384\n" \
  "s_add_u32 m0, s86, 7168\n" \
  "s_nop 0\n" \
  "buffer_load_dwordx4 v7, s[68:71], s90 offen lds\n" \
  "s_add_u32 s90, s90, 16384\n" \
  "s_add_u32 m0, s86, 8192\n" \
  "s_nop 0\n" \
  "buffer_load_dwordx4 v7, s[68:71], s90 offen lds\n" \
  "s_add_u32 s90, s90, 16384\n" \
  "s_add_u32 m0, s86, 9216\n" \
  "s_nop 0\n" \
  "buffer_load_dwordx4 v7, s[68:71], s90 offen lds\n" \
  "s_add_u32 s90, s90, 16384\n" \
  "s_add_u32 m0, s86, 10240\n" \
  "s_nop 0\n" \
  "buffer_load_dwordx4 v7, s[68:71], s90 offen lds\n" \
  "s_add_u32 s90, s90, 16384\n" \
  "s_add_u32 m0, s86, 11264\n" \
  "s_nop 0\n" \
  "buffer_load_dwordx4 v7, s[68:71], s90 offen lds\n" \
  "s_add_u32 s90, s90, 16384\n" \
  "s_add_u32 m0, s86, 12288\n" \
  "s_nop 0\n" \
  "buffer_load_dwordx4 v7, s[68:71], s90 offen lds\n" \
  "s_add_u32 s90, s90, 16384\n" \
  "s_add_u32 m0, s86, 13312\n" \
  "s_nop 0\n" \
  "buffer_load_dwordx4 v7, s[68:71], s90 offen lds\n" \
  "s_add_u32 s90, s90, 16384\n" \
  "s_add_u32 m0, s86, 14336\n" \
  "s_nop 0\n" \
  "buffer_load_dwordx4 v7, s[68:71], s90 offen lds\n" \
  "s_add_u32 s90, s90, 16384\n" \
  "s_add_u32 m0, s86, 15360\n" \
  "s_nop 0\n" \
  "buffer_load_dwordx4 v7, s[68:71], s90 offen lds\n" \
  "s_add_u32 s90, s90, 16384\n" \
  "s_waitcnt vmcnt(16)\n" \
  "ds_read_b64_tr_b16 a[64:65], v16 offset:16384\n" \
  "ds_read_b64_tr_b16 a[66:67], v16 offset:18432\n" \
  "ds_read_b64_tr_b16 a[68:69], v17 offset:16384\n" \
  "ds_read_b64_tr_b16 a[70:71], v17 offset:18432\n" \
  "ds_read_b64_tr_b16 a[72:73], v18 offset:16384\n" \
  "ds_read_b64_tr_b16 a[74:75], v18 offset:18432\n" \
  "ds_read_b64_tr_b16 a[76:77], v19 offset:16384\n" \
  "ds_read_b64_tr_b16 a[78:79], v19 offset:18432\n" \
  "ds_read_b64_tr_b16 a[80:81], v16 offset:20480\n" \
  "ds_read_b64_tr_b16 a[82:83], v16 offset:22528\n" \
  "ds_read_b64_tr_b16 a[84:85], v17 offset:20480\n" \
  "ds_read_b64_tr_b16 a[86:87], v17 offset:22528\n" \
  "ds_read_b64_tr_b16 a[88:89], v18 offset:20480\n" \
  "ds_read_b64_tr_b16 a[90:91], v18 offset:22528\n" \
  "ds_read_b64_tr_b16 a[92:93], v19 offset:20480\n" \
  "ds_read_b64_tr_b16 a[94:95], v19 offset:22528\n" \
  "ds_read_b64_tr_b16 a[96:97], v16 offset:24576\n" \
  "ds_read_b64_tr_b16 a[98:99], v16 offset:26624\n" \
  "ds_read_b64_tr_b16 a[100:101], v17 offset:24576\n" \
  "ds_read_b64_tr_b16 a[102:103], v17 offset:26624\n" \
  "ds_read_b64_tr_b16 a[104:105], v18 offset:24576\n" \
  "ds_read_b64_tr_b16 a[106:107], v18 offset:26624\n" \
  "ds_read_b64_tr_b16 a[108:109], v19 offset:24576\n" \
  "ds_read_b64_tr_b16 a[110:111], v19 offset:26624\n" \
  "ds_read_b64_tr_b16 a[112:113], v16 offset:28672\n" \
  "ds_read_b64_tr_b16 a[114:115], v16 offset:30720\n" \
  "ds_read_b64_tr_b16 a[116:117], v17 offset:28672\n" \
  "ds_read_b64_tr_b16 a[118:119], v17 offset:30720\n" \
  "ds_read_b64_tr_b16 a[120:121], v18 offset:28672\n" \
  "ds_read_b64_tr_b16 a[122:123], v18 offset:30720\n" \
  "ds_read_b64_tr_b16 a[124:125], v19 offset:28672\n" \
  "ds_read_b64_tr_b16 a[126:127], v19 offset:30720\n" \
  "s_waitcnt lgkmcnt(0)\n" \
  "s_add_u32 m0, s86, 16384\n" \
  "s_nop 0\n" \
  "buffer_load_dwordx4 v7, s[68:71], s90 offen lds\n" \
  "s_add_u32 s90, s90, 16384\n" \
  "s_add_u32 m0, s86, 17408\n" \
  "s_nop 0\n" \
  "buffer_load_dwordx4 v7, s[68:71], s90 offen lds\n" \
  "s_add_u32 s90, s90, 16384\n" \
  "s_add_u32 m0, s86, 18432\n" \
  "s_nop 0\n" \
  "buffer_load_dwordx4 v7, s[68:71], s90 offen lds\n" \
  "s_add_u32 s90, s90, 16384\n" \
  "s_add_u32 m0, s86, 19456\n" \
  "s_nop 0\n" \
  "buffer_load_dwordx4 v7, s[68:71], s90 offen lds\n" \
  "s_add_u32 s90, s90, 16384\n" \
  "s_add_u32 m0, s86, 20480\n" \
  "s_nop 0\n" \
  "buffer_load_dwordx4 v7, s[68:71], s90 offen lds\n" \
  "s_add_u32 s90, s90, 16384\n" \
  "s_add_u32 m0, s86, 21504\n" \
  "s_nop 0\n" \
  "buffer_load_dwordx4 v7, s[68:71], s90 offen lds\n" \
  "s_add_u32 s90, s90, 16384\n" \
  "s_add_u32 m0, s86, 22528\n" \
  "s_nop 0\n" \
  "buffer_load_dwordx4 v7, s[68:71], s90 offen lds\n" \
  "s_add_u32 s90, s90, 16384\n" \
  "s_add_u32 m0, s86, 23552\n" \
  "s_nop 0\n" \
  "buffer_load_dwordx4 v7, s[68:71], s90 offen lds\n" \
  "s_add_u32 s90, s90, 16384\n" \
  "s_add_u32 m0, s86, 24576\n" \
  "s_nop 0\n" \
  "buffer_load_dwordx4 v7, s[68:71], s90 offen lds\n" \
  "s_add_u32 s90, s90, 16384\n" \
  "s_add_u32 m0, s86, 25600\n" \
  "s_nop 0\n" \
  "buffer_load_dwordx4 v7, s[68:71], s90 offen lds\n" \
  "s_add_u32 s90, s90, 16384\n" \
  "s_add_u32 m0, s86, 26624\n" \
  "s_nop 0\n" \
  "buffer_load_dwordx4 v7, s[68:71], s90 offen lds\n" \
  "s_add_u32 s90, s90, 16384\n" \
  "s_add_u32 m0, s86, 27648\n" \
  "s_nop 0\n" \
  "buffer_load_dwordx4 v7, s[68:71], s90 offen lds\n" \
  "s_add_u32 s90, s90, 16384\n" \
  "s_add_u32 m0, s86, 28672\n" \
  "s_nop 0\n" \
  "buffer_load_dwordx4 v7, s[68:71], s90 offen lds\n" \
  "s_add_u32 s90, s90, 16384\n" \
  "s_add_u32 m0, s86, 29696\n" \
  "s_nop 0\n" \
  "buffer_load_dwordx4 v7, s[68:71], s90 offen lds\n" \
  "s_add_u32 s90, s90, 16384\n" \
  "s_add_u32 m0, s86, 30720\n" \
  "s_nop 0\n" \
  "buffer_load_dwordx4 v7, s[68:71], s90 offen lds\n" \
  "s_add_u32 s90, s90, 16384\n" \
  "s_add_u32 m0, s86, 31744\n" \
  "s_nop 0\n" \
  "buffer_load_dwordx4 v7, s[68:71], s90 offen lds\n" \
  "s_add_u32 s90, s90, 16384\n" \
  "s_waitcnt vmcnt(16)\n" \
  "ds_read_b64_tr_b16 a[128:129], v16 offset:0\n" \
  "ds_read_b64_tr_b16 a[130:131], v16 offset:2048\n" \
  "ds_read_b64_tr_b16 a[132:133], v17 offset:0\n" \
  "ds_read_b64_tr_b16 a[134:135], v17 offset:2048\n" \
  "ds_read_b64_tr_b16 a[136:137], v18 offset:0\n" \
  "ds_read_b64_tr_b16 a[138:139], v18 offset:2048\n" \
  "ds_read_b64_tr_b16 a[140:141], v19 offset:0\n" \
  "ds_read_b64_tr_b16 a[142:143], v19 offset:2048\n" \
  "ds_read_b64_tr_b16 a[144:145], v16 offset:4096\n" \
  "ds_read_b64_tr_b16 a[146:147], v16 offset:6144\n" \
  "ds_read_b64_tr_b16 a[148:149], v17 offset:4096\n" \
  "ds_read_b64_tr_b16 a[150:151], v17 offset:6144\n" \
  "ds_read_b64_tr_b16 a[152:153], v18 offset:4096\n" \
  "ds_read_b64_tr_b16 a[154:155], v18 offset:6144\n" \
  "ds_read_b64_tr_b16 a[156:157], v19 offset:4096\n" \
  "ds_read_b64_tr_b16 a[158:159], v19 offset:6144\n" \
  "ds_read_b64_tr_b16 a[160:161], v16 offset:8192\n" \
  "ds_read_b64_tr_b16 a[162:163], v16 offset:10240\n" \
  "ds_read_b64_tr_b16 a[164:165], v17 offset:8192\n" \
  "ds_read_b64_tr_b16 a[166:167], v17 offset:10240\n" \
  "ds_read_b64_tr_b16 a[168:169], v18 offset:8192\n" \
  "ds_read_b64_tr_b16 a[170:171], v18 offset:10240\n" \
  "ds_read_b64_tr_b16 a[172:173], v19 offset:8192\n" \
  "ds_read_b64_tr_b16 a[174:175], v19 offset:10240\n" \
  "ds_read_b64_tr_b16 a[176:177], v16 offset:12288\n" \
  "ds_read_b64_tr_b16 a[178:179], v16 offset:14336\n" \
  "ds_read_b64_tr_b16 a[180:181], v17 offset:12288\n" \
  "ds_read_b64_tr_b16 a[182:183], v17 offset:14336\n" \
  "ds_read_b64_tr_b16 a[184:185], v18 offset:12288\n" \
  "ds_read_b64_tr_b16 a[186:187], v18 offset:14336\n" \
  "ds_read_b64_tr_b16 a[188:189], v19 offset:12288\n" \
  "ds_read_b64_tr_b16 a[190:191], v19 offset:14336\n" \
  "s_waitcnt lgkmcnt(0)\n" \
  "s_add_u32 m0, s86, 0\n" \
  "s_nop 0\n" \
  "buffer_load_dwordx4 v7, s[68:71], s90 offen lds\n" \
  "s_add_u32 s90, s90, 16384\n" \
  "s_add_u32 m0, s86, 1024\n" \
  "s_nop 0\n" \
  "buffer_load_dwordx4 v7, s[68:71], s90 offen lds\n" \
  "s_add_u32 s90, s90, 16384\n" \
  "s_add_u32 m0, s86, 2048\n" \
  "s_nop 0\n" \
  "buffer_load_dwordx4 v7, s[68:71], s90 offen lds\n" \
  "s_add_u32 s90, s90, 16384\n" \
  "s_add_u32 m0, s86, 3072\n" \
  "s_nop 0\n" \
  "buffer_load_dwordx4 v7, s[68:71], s90 offen lds\n" \
  "s_add_u32 s90, s90, 16384\n" \
  "s_add_u32 m0, s86, 4096\n" \
  "s_nop 0\n" \
  "buffer_load_dwordx4 v7, s[68:71], s90 offen lds\n" \
  "s_add_u32 s90, s90, 16384\n" \
  "s_add_u32 m0, s86, 5120\n" \
  "s_nop 0\n" \
  "buffer_load_dwordx4 v7, s[68:71], s90 offen lds\n" \
  "s_add_u32 s90, s90, 16384\n" \
  "s_add_u32 m0, s86, 6144\n" \
  "s_nop 0\n" \
  "buffer_load_dwordx4 v7, s[68:71], s90 offen lds\n" \
  "s_add_u32 s90, s90, 16384\n" \
  "s_add_u32 m0, s86, 7168\n" \
  "s_nop 0\n" \
  "buffer_load_dwordx4 v7, s[68:71], s90 offen lds\n" \
  "s_add_u32 s90, s90, 16384\n" \
  "s_add_u32 m0, s86, 8192\n" \
  "s_nop 0\n" \
  "buffer_load_dwordx4 v7, s[68:71], s90 offen lds\n" \
  "s_add_u32 s90, s90, 16384\n" \
  "s_add_u32 m0, s86, 9216\n" \
  "s_nop 0\n" \
  "buffer_load_dwordx4 v7, s[68:71], s90 offen lds\n" \
  "s_add_u32 s90, s90, 16384\n" \
  "s_add_u32 m0, s86, 10240\n" \
  "s_nop 0\n" \
  "buffer_load_dwordx4 v7, s[68:71], s90 offen lds\n" \
  "s_add_u32 s90, s90, 16384\n" \
  "s_add_u32 m0, s86, 11264\n" \
  "s_nop 0\n" \
  "buffer_load_dwordx4 v7, s[68:71], s90 offen lds\n" \
  "s_add_u32 s90, s90, 16384\n" \
  "s_add_u32 m0, s86, 12288\n" \
  "s_nop 0\n" \
  "buffer_load_dwordx4 v7, s[68:71], s90 offen lds\n" \
  "s_add_u32 s90, s90, 16384\n" \
  "s_add_u32 m0, s86, 13312\n" \
  "s_nop 0\n" \
  "buffer_load_dwordx4 v7, s[68:71], s90 offen lds\n" \
  "s_add_u32 s90, s90, 16384\n" \
  "s_add_u32 m0, s86, 14336\n" \
  "s_nop 0\n" \
  "buffer_load_dwordx4 v7, s[68:71], s90 offen lds\n" \
  "s_add_u32 s90, s90, 16384\n" \
  "s_add_u32 m0, s86, 15360\n" \
  "s_nop 0\n" \
  "buffer_load_dwordx4 v7, s[68:71], s90 offen lds\n" \
  "s_add_u32 s90, s90, 16384\n" \
  "s_waitcnt vmcnt(16)\n" \
  "ds_read_b64_tr_b16 a[192:193], v16 offset:16384\n" \
  "ds_read_b64_tr_b16 a[194:195], v16 offset:18432\n" \
  "ds_read_b64_tr_b16 a[196:197], v17 offset:16384\n" \
  "ds_read_b64_tr_b16 a[198:199], v17 offset:18432\n" \
  "ds_read_b64_tr_b16 a[200:201], v18 offset:16384\n" \
  "ds_read_b64_tr_b16 a[202:203], v18 offset:18432\n" \
  "ds_read_b64_tr_b16 a[204:205], v19 offset:16384\n" \
  "ds_read_b64_tr_b16 a[206:207], v19 offset:18432\n" \
  "ds_read_b64_tr_b16 a[208:209], v16 offset:20480\n" \
  "ds_read_b64_tr_b16 a[210:211], v16 offset:22528\n" \
  "ds_read_b64_tr_b16 a[212:213], v17 offset:20480\n" \
  "ds_read_b64_tr_b16 a[214:215], v17 offset:22528\n" \
  "ds_read_b64_tr_b16 a[216:217], v18 offset:20480\n" \
  "ds_read_b64_tr_b16 a[218:219], v18 offset:22528\n" \
  "ds_read_b64_tr_b16 a[220:221], v19 offset:20480\n" \
  "ds_read_b64_tr_b16 a[222:223], v19 offset:22528\n" \
  "ds_read_b64_tr_b16 a[224:225], v16 offset:24576\n" \
  "ds_read_b64_tr_b16 a[226:227], v16 offset:26624\n" \
  "ds_read_b64_tr_b16 a[228:229], v17 offset:24576\n" \
  "ds_read_b64_tr_b16 a[230:231], v17 offset:26624\n" \
  "ds_read_b64_tr_b16 a[232:233], v18 offset:24576\n" \
  "ds_read_b64_tr_b16 a[234:235], v18 offset:26624\n" \
  "ds_read_b64_tr_b16 a[236:237], v19 offset:24576\n" \
  "ds_read_b64_tr_b16 a[238:239], v19 offset:26624\n" \
  "ds_read_b64_tr_b16 a[240:241], v16 offset:28672\n" \
  "ds_read_b64_tr_b16 a[242:243], v16 offset:30720\n" \
  "ds_read_b64_tr_b16 a[244:245], v17 offset:28672\n" \
  "ds_read_b64_tr_b16 a[246:247], v17 offset:30720\n" \
  "ds_read_b64_tr_b16 a[248:249], v18 offset:28672\n" \
  "ds_read_b64_tr_b16 a[250:251], v18 offset:30720\n" \
  "ds_read_b64_tr_b16 a[252:253], v19 offset:28672\n" \
  "ds_read_b64_tr_b16 a[254:255], v19 offset:30720\n" \
  "s_waitcnt lgkmcnt(0)\n" \
  "s_add_u32 m0, s86, 16384\n" \
  "s_nop 0\n" \
  "buffer_load_dwordx4 v7, s[68:71], s90 offen lds\n" \
  "s_add_u32 s90, s90, 16384\n" \
  "s_add_u32 m0, s86, 17408\n" \
  "s_nop 0\n" \
  "buffer_load_dwordx4 v7, s[68:71], s90 offen lds\n" \
  "s_add_u32 s90, s90, 16384\n" \
  "s_add_u32 m0, s86, 18432\n" \
  "s_nop 0\n" \
  "buffer_load_dwordx4 v7, s[68:71], s90 offen lds\n" \
  "s_add_u32 s90, s90, 16384\n" \
  "s_add_u32 m0, s86, 19456\n" \
  "s_nop 0\n" \
  "buffer_load_dwordx4 v7, s[68:71], s90 offen lds\n" \
  "s_add_u32 s90, s90, 16384\n" \
  "s_add_u32 m0, s86, 20480\n" \
  "s_nop 0\n" \
  "buffer_load_dwordx4 v7, s[68:71], s90 offen lds\n" \
  "s_add_u32 s90, s90, 16384\n" \
  "s_add_u32 m0, s86, 21504\n" \
  "s_nop 0\n" \
  "buffer_load_dwordx4 v7, s[68:71], s90 offen lds\n" \
  "s_add_u32 s90, s90, 16384\n" \
  "s_add_u32 m0, s86, 22528\n" \
  "s_nop 0\n" \
  "buffer_load_dwordx4 v7, s[68:71], s90 offen lds\n" \
  "s_add_u32 s90, s90, 16384\n" \
  "s_add_u32 m0, s86, 23552\n" \
  "s_nop 0\n" \
  "buffer_load_dwordx4 v7, s[68:71], s90 offen lds\n" \
  "s_add_u32 s90, s90, 16384\n" \
  "s_add_u32 m0, s86, 24576\n" \
  "s_nop 0\n" \
  "buffer_load_dwordx4 v7, s[68:71], s90 offen lds\n" \
  "s_add_u32 s90, s90, 16384\n" \
  "s_add_u32 m0, s86, 25600\n" \
  "s_nop 0\n" \
  "buffer_load_dwordx4 v7, s[68:71], s90 offen lds\n" \
  "s_add_u32 s90, s90, 16384\n" \
  "s_add_u32 m0, s86, 26624\n" \
  "s_nop 0\n" \
  "buffer_load_dwordx4 v7, s[68:71], s90 offen lds\n" \
  "s_add_u32 s90, s90, 16384\n" \
  "s_add_u32 m0, s86, 27648\n" \
  "s_nop 0\n" \
  "buffer_load_dwordx4 v7, s[68:71], s90 offen lds\n" \
  "s_add_u32 s90, s90, 16384\n" \
  "s_add_u32 m0, s86, 28672\n" \
  "s_nop 0\n" \
  "buffer_load_dwordx4 v7, s[68:71], s90 offen lds\n" \
  "s_add_u32 s90, s90, 16384\n" \
  "s_add_u32 m0, s86, 29696\n" \
  "s_nop 0\n" \
  "buffer_load_dwordx4 v7, s[68:71], s90 offen lds\n" \
  "s_add_u32 s90, s90, 16384\n" \
  "s_add_u32 m0, s86, 30720\n" \
  "s_nop 0\n" \
  "buffer_load_dwordx4 v7, s[68:71], s90 offen lds\n" \
  "s_add_u32 s90, s90, 16384\n" \
  "s_add_u32 m0, s86, 31744\n" \
  "s_nop 0\n" \
  "buffer_load_dwordx4 v7, s[68:71], s90 offen lds\n" \
  "s_add_u32 s90, s90, 16384\n" \
  "s_mov_b32 s92, 1\n" \
  "v_xor_b32 v7, 64, v12\n" \
  "LP0_%=:\n" \
  "buffer_load_dword v9, v8, s[80:83], 0 offen sc1\n" \
  "s_waitcnt vmcnt(0)\n" \
  "v_cmp_gt_u32 vcc, s92, v9\n" \
  "s_cbranch_vccz LP1_%=\n" \
  "s_sleep 8\n" \
  "s_add_u32 s91, s91, 1\n" \
  "s_cmp_lt_u32 s91, " STR(SPIN_CAP) "\n" \
  "s_cbranch_scc1 LP0_%=\n" \
  "v_mov_b32 v5, " STR(CW_DEAD) "*4\n" \
  "v_mov_b32 v15, 1\n" \
  "buffer_store_dword v15, v5, s[80:83], 0 offen sc1\n" \
  "s_branch LP1_%=\n" \
  "LP1_%=:\n" \
  "s_add_u32 s84, s87, 0\n" \
  "buffer_load_dwordx4 v[64:67], v6, s[72:75], s84 offen sc1\n" \
  "s_add_u32 s84, s87, 1024\n" \
  "buffer_load_dwordx4 v[68:71], v6, s[72:75], s84 offen sc1\n" \
  "s_add_u32 s84, s87, 2048\n" \
  "buffer_load_dwordx4 v[72:75], v6, s[72:75], s84 offen sc1\n" \
  "s_add_u32 s84, s87, 49152\n" \
  "buffer_load_dwordx4 v[76:79], v6, s[72:75], s84 offen sc1\n" \
  "s_add_u32 s84, s87, 3072\n" \
  "buffer_load_dwordx4 v[80:83], v6, s[72:75], s84 offen sc1\n" \
  "s_add_u32 s84, s87, 4096\n" \
  "buffer_load_dwordx4 v[84:87], v6, s[72:75], s84 offen sc1\n" \
  "s_add_u32 s84, s87, 5120\n" \
  "buffer_load_dwordx4 v[88:91], v6, s[72:75], s84 offen sc1\n" \
  "s_add_u32 s84, s87, 50176\n" \
  "buffer_load_dwordx4 v[92:95], v6, s[72:75], s84 offen sc1\n" \
  "s_add_u32 s84, s87, 6144\n" \
  "buffer_load_dwordx4 v[96:99], v6, s[72:75], s84 offen sc1\n" \
  "s_add_u32 s84, s87, 7168\n" \
  "buffer_load_dwordx4 v[100:103], v6, s[72:75], s84 offen sc1\n" \
  "s_add_u32 s84, s87, 16384\n" \
  "buffer_load_dwordx4 v[104:107], v6, s[72:75], s84 offen sc1\n" \
  "s_add_u32 s84, s87, 51200\n" \
  "buffer_load_dwordx4 v[108:111], v6, s[72:75], s84 offen sc1\n" \
  "s_add_u32 s84, s87, 17408\n" \
  "buffer_load_dwordx4 v[112:115], v6, s[72:75], s84 offen sc1\n" \
  "s_add_u32 s84, s87, 18432\n" \
  "buffer_load_dwordx4 v[116:119], v6, s[72:75], s84 offen sc1\n" \
  "s_add_u32 s84, s87, 19456\n" \
  "buffer_load_dwordx4 v[120:123], v6, s[72:75], s84 offen sc1\n" \
  "s_add_u32 s84, s87, 52224\n" \
  "buffer_load_dwordx4 v[124:127], v6, s[72:75], s84 offen sc1\n" \
  "v_or_b32 v10, 0x80000000, v10\n" \
  "s_sub_u32 s88, s88, 131072\n" \
  ".p2align 6\n" \
  "LX_%=:\n" \
  "s_waitcnt vmcnt(15)\n" \
  "v_mfma_f32_16x16x32_bf16 v[20:23], v[128:131], v[64:67], 0\n" \
  "v_mfma_f32_16x16x32_bf16 v[24:27], v[132:135], v[64:67], 0\n" \
  "v_mfma_f32_16x16x32_bf16 v[28:31], v[136:139], v[64:67], 0\n" \
  "v_mfma_f32_16x16x32_bf16 v[32:35], v[140:143], v[64:67], 0\n" \
  "s_add_u32 s84, s87, 20480\n" \
  "buffer_load_dwordx4 v[64:67], v6, s[72:75], s84 offen sc1\n" \
  "s_waitcnt vmcnt(15)\n" \
  "v_mfma_f32_16x16x32_bf16 v[20:23], v[144:147], v[68:71], v[20:23]\n" \
  "v_mfma_f32_16x16x32_bf16 v[24:27], v[148:151], v[68:71], v[24:27]\n" \
  "v_mfma_f32_16x16x32_bf16 v[28:31], v[152:155], v[68:71], v[28:31]\n" \
  "v_mfma_f32_16x16x32_bf16 v[32:35], v[156:159], v[68:71], v[32:35]\n" \
  "s_add_u32 s84, s87, 21504\n" \
  "buffer_load_dwordx4 v[68:71], v6, s[72:75], s84 offen sc1\n" \
  "s_waitcnt vmcnt(15)\n" \
  "v_mfma_f32_16x16x32_bf16 v[20:23], v[160:163], v[72:75], v[20:23]\n" \
  "ds_read_b64_tr_b16 v[40:41], v16 offset:0\n" \
  "ds_read_b64_tr_b16 v[42:43], v16 offset:2048\n" \
  "v_mfma_f32_16x16x32_bf16 v[24:27], v[164:167], v[72:75], v[24:27]\n" \
  "ds_read_b64_tr_b16 v[44:45], v17 offset:0\n" \
  "ds_read_b64_tr_b16 v[46:47], v17 offset:2048\n" \
  "v_mfma_f32_16x16x32_bf16 v[28:31], v[168:171], v[72:75], v[28:31]\n" \
  "ds_read_b64_tr_b16 v[48:49], v18 offset:0\n" \
  "ds_read_b64_tr_b16 v[50:51], v18 offset:2048\n" \
  "v_mfma_f32_16x16x32_bf16 v[32:35], v[172:175], v[72:75], v[32:35]\n" \
  "ds_read_b64_tr_b16 v[52:53], v19 offset:0\n" \
  "ds_read_b64_tr_b16 v[54:55], v19 offset:2048\n" \
  "s_add_u32 s84, s87, 22528\n" \
  "buffer_load_dwordx4 v[72:75], v6, s[72:75], s84 offen sc1\n" \
  "s_waitcnt vmcnt(15)\n" \
  "s_waitcnt lgkmcnt(6)\n" \
  "v_mfma_f32_16x16x32_bf16 v[20:23], v[40:43], v[76:79], v[20:23]\n" \
  "s_waitcnt lgkmcnt(4)\n" \
  "v_mfma_f32_16x16x32_bf16 v[24:27], v[44:47], v[76:79], v[24:27]\n" \
  "s_waitcnt lgkmcnt(2)\n" \
  "v_mfma_f32_16x16x32_bf16 v[28:31], v[48:51], v[76:79], v[28:31]\n" \
  "s_waitcnt lgkmcnt(0)\n" \
  "v_mfma_f32_16x16x32_bf16 v[32:35], v[52:55], v[76:79], v[32:35]\n" \
  "s_add_u32 s84, s87, 53248\n" \
  "buffer_load_dwordx4 v[76:79], v6, s[72:75], s84 offen sc1\n" \
  "s_waitcnt lgkmcnt(0)\n" \
  "s_barrier\n" \
  "ds_read_b128 v[52:55], v11 offset:0\n" \
  "ds_read_b128 v[56:59], v11 offset:4096\n" \
  "ds_read_b128 v[60:63], v11 offset:8192\n" \
  "ds_read_b128 v[36:39], v11 offset:12288\n" \
  "s_waitcnt vmcnt(15)\n" \
  "v_mfma_f32_16x16x32_bf16 v[20:23], v[176:179], v[80:83], v[20:23]\n" \
  "v_mfma_f32_16x16x32_bf16 v[24:27], v[180:183], v[80:83], v[24:27]\n" \
  "v_mfma_f32_16x16x32_bf16 v[28:31], v[184:187], v[80:83], v[28:31]\n" \
  "v_mfma_f32_16x16x32_bf16 v[32:35], v[188:191], v[80:83], v[32:35]\n" \
  "s_add_u32 s84, s87, 23552\n" \
  "buffer_load_dwordx4 v[80:83], v6, s[72:75], s84 offen sc1\n" \
  "s_waitcnt lgkmcnt(0)\n" \
  "s_waitcnt vmcnt(15)\n" \
  "v_mfma_f32_16x16x32_bf16 v[20:23], v[192:195], v[84:87], v[20:23]\n" \
  "v_add_f32 v36, v36, v52\n" \
  "v_add_f32 v37, v37, v53\n" \
  "v_add_f32 v38, v38, v54\n" \
  "v_add_f32 v39, v39, v55\n" \
  "v_mfma_f32_16x16x32_bf16 v[24:27], v[196:199], v[84:87], v[24:27]\n" \
  "v_add_f32 v56, v56, v60\n" \
  "v_add_f32 v57, v57, v61\n" \
  "v_add_f32 v58, v58, v62\n" \
  "v_add_f32 v59, v59, v63\n" \
  "v_mfma_f32_16x16x32_bf16 v[28:31], v[200:203], v[84:87], v[28:31]\n" \
  "v_add_f32 v36, v36, v56\n" \
  "v_add_f32 v37, v37, v57\n" \
  "v_add_f32 v38, v38, v58\n" \
  "v_add_f32 v39, v39, v59\n" \
  "v_mfma_f32_16x16x32_bf16 v[32:35], v[204:207], v[84:87], v[32:35]\n" \
  "v_cvt_pk_bf16_f32 v36, v36, v37\n" \
  "v_cvt_pk_bf16_f32 v37, v38, v39\n" \
  "buffer_store_dwordx2 v[36:37], v10, s[76:79], s88 offen\n" \
  "s_add_u32 s88, s88, 131072\n" \
  "v_and_b32 v10, 0x7fffffff, v10\n" \
  "s_add_u32 s84, s87, 32768\n" \
  "buffer_load_dwordx4 v[84:87], v6, s[72:75], s84 offen sc1\n" \
  "s_waitcnt vmcnt(16)\n" \
  "v_mfma_f32_16x16x32_bf16 v[20:23], v[208:211], v[88:91], v[20:23]\n" \
  "ds_read_b64_tr_b16 v[40:41], v16 offset:4096\n" \
  "ds_read_b64_tr_b16 v[42:43], v16 offset:6144\n" \
  "v_mfma_f32_16x16x32_bf16 v[24:27], v[212:215], v[88:91], v[24:27]\n" \
  "ds_read_b64_tr_b16 v[44:45], v17 offset:4096\n" \
  "ds_read_b64_tr_b16 v[46:47], v17 offset:6144\n" \
  "v_mfma_f32_16x16x32_bf16 v[28:31], v[216:219], v[88:91], v[28:31]\n" \
  "ds_read_b64_tr_b16 v[48:49], v18 offset:4096\n" \
  "ds_read_b64_tr_b16 v[50:51], v18 offset:6144\n" \
  "v_mfma_f32_16x16x32_bf16 v[32:35], v[220:223], v[88:91], v[32:35]\n" \
  "ds_read_b64_tr_b16 v[52:53], v19 offset:4096\n" \
  "ds_read_b64_tr_b16 v[54:55], v19 offset:6144\n" \
  "s_add_u32 s84, s87, 33792\n" \
  "buffer_load_dwordx4 v[88:91], v6, s[72:75], s84 offen sc1\n" \
  "s_waitcnt vmcnt(16)\n" \
  "s_waitcnt lgkmcnt(6)\n" \
  "v_mfma_f32_16x16x32_bf16 v[20:23], v[40:43], v[92:95], v[20:23]\n" \
  "s_waitcnt lgkmcnt(4)\n" \
  "v_mfma_f32_16x16x32_bf16 v[24:27], v[44:47], v[92:95], v[24:27]\n" \
  "s_waitcnt lgkmcnt(2)\n" \
  "v_mfma_f32_16x16x32_bf16 v[28:31], v[48:51], v[92:95], v[28:31]\n" \
  "s_waitcnt lgkmcnt(0)\n" \
  "v_mfma_f32_16x16x32_bf16 v[32:35], v[52:55], v[92:95], v[32:35]\n" \
  "s_add_u32 s84, s87, 54272\n" \
  "buffer_load_dwordx4 v[92:95], v6, s[72:75], s84 offen sc1\n" \
  "s_waitcnt vmcnt(16)\n" \
  "v_mfma_f32_16x16x32_bf16 v[20:23], v[224:227], v[96:99], v[20:23]\n" \
  "buffer_load_dword v9, v8, s[80:83], 0 offen sc1\n" \
  "v_mfma_f32_16x16x32_bf16 v[24:27], v[228:231], v[96:99], v[24:27]\n" \
  "v_xor_b32 v11, 0x4000, v11\n" \
  "v_xor_b32 v7, 0x4000, v7\n" \
  "v_mfma_f32_16x16x32_bf16 v[28:31], v[232:235], v[96:99], v[28:31]\n" \
  "v_xor_b32 v12, 0x4000, v12\n" \
  "v_xor_b32 v13, 0x4000, v13\n" \
  "v_xor_b32 v14, 0x4000, v14\n" \
  "v_mfma_f32_16x16x32_bf16 v[32:35], v[236:239], v[96:99], v[32:35]\n" \
  "s_add_u32 s84, s87, 34816\n" \
  "buffer_load_dwordx4 v[96:99], v6, s[72:75], s84 offen sc1\n" \
  "s_waitcnt vmcnt(17)\n" \
  "v_mfma_f32_16x16x32_bf16 v[20:23], v[240:243], v[100:103], v[20:23]\n" \
  "v_mfma_f32_16x16x32_bf16 v[24:27], v[244:247], v[100:103], v[24:27]\n" \
  "v_mfma_f32_16x16x32_bf16 v[28:31], v[248:251], v[100:103], v[28:31]\n" \
  "v_mfma_f32_16x16x32_bf16 v[32:35], v[252:255], v[100:103], v[32:35]\n" \
  "s_add_u32 s84, s87, 35840\n" \
  "buffer_load_dwordx4 v[100:103], v6, s[72:75], s84 offen sc1\n" \
  "s_waitcnt vmcnt(17)\n" \
  "v_mfma_f32_16x16x32_bf16 v[20:23], a[0:3], v[104:107], v[20:23]\n" \
  "ds_read_b64_tr_b16 v[40:41], v16 offset:8192\n" \
  "ds_read_b64_tr_b16 v[42:43], v16 offset:10240\n" \
  "v_mfma_f32_16x16x32_bf16 v[24:27], a[4:7], v[104:107], v[24:27]\n" \
  "ds_read_b64_tr_b16 v[44:45], v17 offset:8192\n" \
  "ds_read_b64_tr_b16 v[46:47], v17 offset:10240\n" \
  "v_mfma_f32_16x16x32_bf16 v[28:31], a[8:11], v[104:107], v[28:31]\n" \
  "ds_read_b64_tr_b16 v[48:49], v18 offset:8192\n" \
  "ds_read_b64_tr_b16 v[50:51], v18 offset:10240\n" \
  "v_mfma_f32_16x16x32_bf16 v[32:35], a[12:15], v[104:107], v[32:35]\n" \
  "ds_read_b64_tr_b16 v[52:53], v19 offset:8192\n" \
  "ds_read_b64_tr_b16 v[54:55], v19 offset:10240\n" \
  "s_add_u32 s84, s87, 36864\n" \
  "buffer_load_dwordx4 v[104:107], v6, s[72:75], s84 offen sc1\n" \
  "s_waitcnt vmcnt(17)\n" \
  "s_waitcnt lgkmcnt(6)\n" \
  "v_mfma_f32_16x16x32_bf16 v[20:23], v[40:43], v[108:111], v[20:23]\n" \
  "s_waitcnt lgkmcnt(4)\n" \
  "v_mfma_f32_16x16x32_bf16 v[24:27], v[44:47], v[108:111], v[24:27]\n" \
  "s_waitcnt lgkmcnt(2)\n" \
  "v_mfma_f32_16x16x32_bf16 v[28:31], v[48:51], v[108:111], v[28:31]\n" \
  "s_waitcnt lgkmcnt(0)\n" \
  "v_mfma_f32_16x16x32_bf16 v[32:35], v[52:55], v[108:111], v[32:35]\n" \
  "s_add_u32 s84, s87, 55296\n" \
  "buffer_load_dwordx4 v[108:111], v6, s[72:75], s84 offen sc1\n" \
  "s_waitcnt vmcnt(17)\n" \
  "v_mfma_f32_16x16x32_bf16 v[20:23], a[16:19], v[112:115], v[20:23]\n" \
  "v_mfma_f32_16x16x32_bf16 v[24:27], a[20:23], v[112:115], v[24:27]\n" \
  "v_mfma_f32_16x16x32_bf16 v[28:31], a[24:27], v[112:115], v[28:31]\n" \
  "v_mfma_f32_16x16x32_bf16 v[32:35], a[28:31], v[112:115], v[32:35]\n" \
  "s_add_u32 s84, s87, 37888\n" \
  "buffer_load_dwordx4 v[112:115], v6, s[72:75], s84 offen sc1\n" \
  "s_waitcnt vmcnt(17)\n" \
  "v_mfma_f32_16x16x32_bf16 v[20:23], a[32:35], v[116:119], v[20:23]\n" \
  "v_mfma_f32_16x16x32_bf16 v[24:27], a[36:39], v[116:119], v[24:27]\n" \
  "v_mfma_f32_16x16x32_bf16 v[28:31], a[40:43], v[116:119], v[28:31]\n" \
  "v_mfma_f32_16x16x32_bf16 v[32:35], a[44:47], v[116:119], v[32:35]\n" \
  "s_add_u32 s84, s87, 38912\n" \
  "buffer_load_dwordx4 v[116:119], v6, s[72:75], s84 offen sc1\n" \
  "s_waitcnt vmcnt(17)\n" \
  "v_mfma_f32_16x16x32_bf16 v[20:23], a[48:51], v[120:123], v[20:23]\n" \
  "ds_read_b64_tr_b16 v[40:41], v16 offset:12288\n" \
  "ds_read_b64_tr_b16 v[42:43], v16 offset:14336\n" \
  "v_mfma_f32_16x16x32_bf16 v[24:27], a[52:55], v[120:123], v[24:27]\n" \
  "ds_read_b64_tr_b16 v[44:45], v17 offset:12288\n" \
  "ds_read_b64_tr_b16 v[46:47], v17 offset:14336\n" \
  "v_mfma_f32_16x16x32_bf16 v[28:31], a[56:59], v[120:123], v[28:31]\n" \
  "ds_read_b64_tr_b16 v[48:49], v18 offset:12288\n" \
  "ds_read_b64_tr_b16 v[50:51], v18 offset:14336\n" \
  "v_mfma_f32_16x16x32_bf16 v[32:35], a[60:63], v[120:123], v[32:35]\n" \
  "ds_read_b64_tr_b16 v[52:53], v19 offset:12288\n" \
  "ds_read_b64_tr_b16 v[54:55], v19 offset:14336\n" \
  "s_add_u32 s84, s87, 39936\n" \
  "buffer_load_dwordx4 v[120:123], v6, s[72:75], s84 offen sc1\n" \
  "s_waitcnt vmcnt(17)\n" \
  "s_waitcnt lgkmcnt(6)\n" \
  "v_mfma_f32_16x16x32_bf16 v[20:23], v[40:43], v[124:127], v[20:23]\n" \
  "s_waitcnt lgkmcnt(4)\n" \
  "v_mfma_f32_16x16x32_bf16 v[24:27], v[44:47], v[124:127], v[24:27]\n" \
  "s_waitcnt lgkmcnt(2)\n" \
  "v_mfma_f32_16x16x32_bf16 v[28:31], v[48:51], v[124:127], v[28:31]\n" \
  "s_waitcnt lgkmcnt(0)\n" \
  "v_mfma_f32_16x16x32_bf16 v[32:35], v[52:55], v[124:127], v[32:35]\n" \
  "s_add_u32 s84, s87, 56320\n" \
  "buffer_load_dwordx4 v[124:127], v6, s[72:75], s84 offen sc1\n" \
  "s_waitcnt vmcnt(8)\n" \
  "s_add_u32 s92, s89, 2\n" \
  "s_min_u32 s92, s92, 64\n" \
  "v_cmp_gt_u32 vcc, s92, v9\n" \
  "s_sub_u32 s85, s92, 1\n" \
  "s_lshr_b32 s84, s85, 2\n" \
  "s_xnor_b32 s84, s84, %7\n" \
  "s_and_b32 s84, s84, 1\n" \
  "s_lshl_b32 s84, s84, 13\n" \
  "s_lshl_b32 s87, s85, 19\n" \
  "s_or_b32 s87, s87, s84\n" \
  "s_add_u32 s87, s87, s96\n" \
  "s_cbranch_vccnz LP2_%=\n" \
  "LP3_%=:\n" \
  "s_waitcnt vmcnt(17)\n" \
  "v_mfma_f32_16x16x32_bf16 v[20:23], a[64:67], v[64:67], v[20:23]\n" \
  "v_mfma_f32_16x16x32_bf16 v[24:27], a[68:71], v[64:67], v[24:27]\n" \
  "v_mfma_f32_16x16x32_bf16 v[28:31], a[72:75], v[64:67], v[28:31]\n" \
  "v_mfma_f32_16x16x32_bf16 v[32:35], a[76:79], v[64:67], v[32:35]\n" \
  "s_add_u32 s84, s87, 0\n" \
  "buffer_load_dwordx4 v[64:67], v6, s[72:75], s84 offen sc1\n" \
  "s_waitcnt vmcnt(17)\n" \
  "v_mfma_f32_16x16x32_bf16 v[20:23], a[80:83], v[68:71], v[20:23]\n" \
  "v_mfma_f32_16x16x32_bf16 v[24:27], a[84:87], v[68:71], v[24:27]\n" \
  "v_mfma_f32_16x16x32_bf16 v[28:31], a[88:91], v[68:71], v[28:31]\n" \
  "v_mfma_f32_16x16x32_bf16 v[32:35], a[92:95], v[68:71], v[32:35]\n" \
  "s_add_u32 s84, s87, 1024\n" \
  "buffer_load_dwordx4 v[68:71], v6, s[72:75], s84 offen sc1\n" \
  "s_waitcnt vmcnt(17)\n" \
  "v_mfma_f32_16x16x32_bf16 v[20:23], a[96:99], v[72:75], v[20:23]\n" \
  "ds_read_b64_tr_b16 v[40:41], v16 offset:16384\n" \
  "ds_read_b64_tr_b16 v[42:43], v16 offset:18432\n" \
  "v_mfma_f32_16x16x32_bf16 v[24:27], a[100:103], v[72:75], v[24:27]\n" \
  "ds_read_b64_tr_b16 v[44:45], v17 offset:16384\n" \
  "ds_read_b64_tr_b16 v[46:47], v17 offset:18432\n" \
  "v_mfma_f32_16x16x32_bf16 v[28:31], a[104:107], v[72:75], v[28:31]\n" \
  "ds_read_b64_tr_b16 v[48:49], v18 offset:16384\n" \
  "ds_read_b64_tr_b16 v[50:51], v18 offset:18432\n" \
  "v_mfma_f32_16x16x32_bf16 v[32:35], a[108:111], v[72:75], v[32:35]\n" \
  "ds_read_b64_tr_b16 v[52:53], v19 offset:16384\n" \
  "ds_read_b64_tr_b16 v[54:55], v19 offset:18432\n" \
  "s_add_u32 s84, s87, 2048\n" \
  "buffer_load_dwordx4 v[72:75], v6, s[72:75], s84 offen sc1\n" \
  "s_waitcnt vmcnt(17)\n" \
  "s_waitcnt lgkmcnt(6)\n" \
  "v_mfma_f32_16x16x32_bf16 v[20:23], v[40:43], v[76:79], v[20:23]\n" \
  "s_waitcnt lgkmcnt(4)\n" \
  "v_mfma_f32_16x16x32_bf16 v[24:27], v[44:47], v[76:79], v[24:27]\n" \
  "s_waitcnt lgkmcnt(2)\n" \
  "v_mfma_f32_16x16x32_bf16 v[28:31], v[48:51], v[76:79], v[28:31]\n" \
  "s_waitcnt lgkmcnt(0)\n" \
  "v_mfma_f32_16x16x32_bf16 v[32:35], v[52:55], v[76:79], v[32:35]\n" \
  "s_add_u32 s84, s87, 49152\n" \
  "buffer_load_dwordx4 v[76:79], v6, s[72:75], s84 offen sc1\n" \
  "s_waitcnt vmcnt(17)\n" \
  "v_mfma_f32_16x16x32_bf16 v[20:23], a[112:115], v[80:83], v[20:23]\n" \
  "v_mfma_f32_16x16x32_bf16 v[24:27], a[116:119], v[80:83], v[24:27]\n" \
  "v_mfma_f32_16x16x32_bf16 v[28:31], a[120:123], v[80:83], v[28:31]\n" \
  "v_mfma_f32_16x16x32_bf16 v[32:35], a[124:127], v[80:83], v[32:35]\n" \
  "s_add_u32 s84, s87, 3072\n" \
  "buffer_load_dwordx4 v[80:83], v6, s[72:75], s84 offen sc1\n" \
  "s_waitcnt vmcnt(16)\n" \
  "v_mfma_f32_16x16x32_bf16 v[20:23], a[128:131], v[84:87], v[20:23]\n" \
  "v_mfma_f32_16x16x32_bf16 v[24:27], a[132:135], v[84:87], v[24:27]\n" \
  "v_mfma_f32_16x16x32_bf16 v[28:31], a[136:139], v[84:87], v[28:31]\n" \
  "v_mfma_f32_16x16x32_bf16 v[32:35], a[140:143], v[84:87], v[32:35]\n" \
  "s_add_u32 s84, s87, 4096\n" \
  "buffer_load_dwordx4 v[84:87], v6, s[72:75], s84 offen sc1\n" \
  "s_waitcnt vmcnt(16)\n" \
  "v_mfma_f32_16x16x32_bf16 v[20:23], a[144:147], v[88:91], v[20:23]\n" \
  "ds_read_b64_tr_b16 v[40:41], v16 offset:20480\n" \
  "ds_read_b64_tr_b16 v[42:43], v16 offset:22528\n" \
  "v_mfma_f32_16x16x32_bf16 v[24:27], a[148:151], v[88:91], v[24:27]\n" \
  "ds_read_b64_tr_b16 v[44:45], v17 offset:20480\n" \
  "ds_read_b64_tr_b16 v[46:47], v17 offset:22528\n" \
  "v_mfma_f32_16x16x32_bf16 v[28:31], a[152:155], v[88:91], v[28:31]\n" \
  "ds_read_b64_tr_b16 v[48:49], v18 offset:20480\n" \
  "ds_read_b64_tr_b16 v[50:51], v18 offset:22528\n" \
  "v_mfma_f32_16x16x32_bf16 v[32:35], a[156:159], v[88:91], v[32:35]\n" \
  "ds_read_b64_tr_b16 v[52:53], v19 offset:20480\n" \
  "ds_read_b64_tr_b16 v[54:55], v19 offset:22528\n" \
  "s_add_u32 s84, s87, 5120\n" \
  "buffer_load_dwordx4 v[88:91], v6, s[72:75], s84 offen sc1\n" \
  "s_waitcnt vmcnt(16)\n" \
  "s_waitcnt lgkmcnt(6)\n" \
  "v_mfma_f32_16x16x32_bf16 v[20:23], v[40:43], v[92:95], v[20:23]\n" \
  "s_waitcnt lgkmcnt(4)\n" \
  "v_mfma_f32_16x16x32_bf16 v[24:27], v[44:47], v[92:95], v[24:27]\n" \
  "s_waitcnt lgkmcnt(2)\n" \
  "v_mfma_f32_16x16x32_bf16 v[28:31], v[48:51], v[92:95], v[28:31]\n" \
  "s_waitcnt lgkmcnt(0)\n" \
  "v_mfma_f32_16x16x32_bf16 v[32:35], v[52:55], v[92:95], v[32:35]\n" \
  "s_add_u32 s84, s87, 50176\n" \
  "buffer_load_dwordx4 v[92:95], v6, s[72:75], s84 offen sc1\n" \
  "s_waitcnt vmcnt(15)\n" \
  "v_mfma_f32_16x16x32_bf16 v[20:23], a[160:163], v[96:99], v[20:23]\n" \
  "v_mfma_f32_16x16x32_bf16 v[24:27], a[164:167], v[96:99], v[24:27]\n" \
  "v_mfma_f32_16x16x32_bf16 v[28:31], a[168:171], v[96:99], v[28:31]\n" \
  "v_mfma_f32_16x16x32_bf16 v[32:35], a[172:175], v[96:99], v[32:35]\n" \
  "s_add_u32 s84, s87, 6144\n" \
  "buffer_load_dwordx4 v[96:99], v6, s[72:75], s84 offen sc1\n" \
  "s_waitcnt vmcnt(15)\n" \
  "v_mfma_f32_16x16x32_bf16 v[20:23], a[176:179], v[100:103], v[20:23]\n" \
  "v_mfma_f32_16x16x32_bf16 v[24:27], a[180:183], v[100:103], v[24:27]\n" \
  "v_mfma_f32_16x16x32_bf16 v[28:31], a[184:187], v[100:103], v[28:31]\n" \
  "v_mfma_f32_16x16x32_bf16 v[32:35], a[188:191], v[100:103], v[32:35]\n" \
  "s_add_u32 s84, s87, 7168\n" \
  "buffer_load_dwordx4 v[100:103], v6, s[72:75], s84 offen sc1\n" \
  "s_waitcnt vmcnt(15)\n" \
  "v_mfma_f32_16x16x32_bf16 v[20:23], a[192:195], v[104:107], v[20:23]\n" \
  "ds_read_b64_tr_b16 v[40:41], v16 offset:24576\n" \
  "ds_read_b64_tr_b16 v[42:43], v16 offset:26624\n" \
  "v_mfma_f32_16x16x32_bf16 v[24:27], a[196:199], v[104:107], v[24:27]\n" \
  "ds_read_b64_tr_b16 v[44:45], v17 offset:24576\n" \
  "ds_read_b64_tr_b16 v[46:47], v17 offset:26624\n" \
  "v_mfma_f32_16x16x32_bf16 v[28:31], a[200:203], v[104:107], v[28:31]\n" \
  "ds_read_b64_tr_b16 v[48:49], v18 offset:24576\n" \
  "ds_read_b64_tr_b16 v[50:51], v18 offset:26624\n" \
  "v_mfma_f32_16x16x32_bf16 v[32:35], a[204:207], v[104:107], v[32:35]\n" \
  "ds_read_b64_tr_b16 v[52:53], v19 offset:24576\n" \
  "ds_read_b64_tr_b16 v[54:55], v19 offset:26624\n" \
  "s_add_u32 s84, s87, 16384\n" \
  "buffer_load_dwordx4 v[104:107], v6, s[72:75], s84 offen sc1\n" \
  "s_waitcnt vmcnt(15)\n" \
  "s_waitcnt lgkmcnt(6)\n" \
  "v_mfma_f32_16x16x32_bf16 v[20:23], v[40:43], v[108:111], v[20:23]\n" \
  "s_waitcnt lgkmcnt(4)\n" \
  "v_mfma_f32_16x16x32_bf16 v[24:27], v[44:47], v[108:111], v[24:27]\n" \
  "s_waitcnt lgkmcnt(2)\n" \
  "v_mfma_f32_16x16x32_bf16 v[28:31], v[48:51], v[108:111], v[28:31]\n" \
  "s_waitcnt lgkmcnt(0)\n" \
  "v_mfma_f32_16x16x32_bf16 v[32:35], v[52:55], v[108:111], v[32:35]\n" \
  "s_add_u32 s84, s87, 51200\n" \
  "buffer_load_dwordx4 v[108:111], v6, s[72:75], s84 offen sc1\n" \
  "s_waitcnt vmcnt(15)\n" \
  "v_mfma_f32_16x16x32_bf16 v[20:23], a[208:211], v[112:115], v[20:23]\n" \
  "v_mfma_f32_16x16x32_bf16 v[24:27], a[212:215], v[112:115], v[24:27]\n" \
  "v_mfma_f32_16x16x32_bf16 v[28:31], a[216:219], v[112:115], v[28:31]\n" \
  "v_mfma_f32_16x16x32_bf16 v[32:35], a[220:223], v[112:115], v[32:35]\n" \
  "s_add_u32 s84, s87, 17408\n" \
  "buffer_load_dwordx4 v[112:115], v6, s[72:75], s84 offen sc1\n" \
  "s_waitcnt vmcnt(15)\n" \
  "v_mfma_f32_16x16x32_bf16 v[20:23], a[224:227], v[116:119], v[20:23]\n" \
  "v_mfma_f32_16x16x32_bf16 v[24:27], a[228:231], v[116:119], v[24:27]\n" \
  "v_mfma_f32_16x16x32_bf16 v[28:31], a[232:235], v[116:119], v[28:31]\n" \
  "v_mfma_f32_16x16x32_bf16 v[32:35], a[236:239], v[116:119], v[32:35]\n" \
  "s_add_u32 s84, s87, 18432\n" \
  "buffer_load_dwordx4 v[116:119], v6, s[72:75], s84 offen sc1\n" \
  "s_waitcnt vmcnt(15)\n" \
  "v_mfma_f32_16x16x32_bf16 v[20:23], a[240:243], v[120:123], v[20:23]\n" \
  "ds_read_b64_tr_b16 v[40:41], v16 offset:28672\n" \
  "ds_read_b64_tr_b16 v[42:43], v16 offset:30720\n" \
  "v_mfma_f32_16x16x32_bf16 v[24:27], a[244:247], v[120:123], v[24:27]\n" \
  "ds_read_b64_tr_b16 v[44:45], v17 offset:28672\n" \
  "ds_read_b64_tr_b16 v[46:47], v17 offset:30720\n" \
  "v_mfma_f32_16x16x32_bf16 v[28:31], a[248:251], v[120:123], v[28:31]\n" \
  "ds_read_b64_tr_b16 v[48:49], v18 offset:28672\n" \
  "ds_read_b64_tr_b16 v[50:51], v18 offset:30720\n" \
  "v_mfma_f32_16x16x32_bf16 v[32:35], a[252:255], v[120:123], v[32:35]\n" \
  "ds_read_b64_tr_b16 v[52:53], v19 offset:28672\n" \
  "ds_read_b64_tr_b16 v[54:55], v19 offset:30720\n" \
  "s_add_u32 s84, s87, 19456\n" \
  "buffer_load_dwordx4 v[120:123], v6, s[72:75], s84 offen sc1\n" \
  "s_waitcnt vmcnt(15)\n" \
  "s_waitcnt lgkmcnt(6)\n" \
  "v_mfma_f32_16x16x32_bf16 v[20:23], v[40:43], v[124:127], v[20:23]\n" \
  "s_waitcnt lgkmcnt(4)\n" \
  "v_mfma_f32_16x16x32_bf16 v[24:27], v[44:47], v[124:127], v[24:27]\n" \
  "s_waitcnt lgkmcnt(2)\n" \
  "v_mfma_f32_16x16x32_bf16 v[28:31], v[48:51], v[124:127], v[28:31]\n" \
  "s_waitcnt lgkmcnt(0)\n" \
  "v_mfma_f32_16x16x32_bf16 v[32:35], v[52:55], v[124:127], v[32:35]\n" \
  "s_add_u32 s84, s87, 52224\n" \
  "buffer_load_dwordx4 v[124:127], v6, s[72:75], s84 offen sc1\n" \
  "s_add_u32 s89, s89, 1\n" \
  "s_cmp_eq_u32 s89, 64\n" \
  "ds_write_b128 v7, v[20:23]\n" \
  "ds_write_b128 v12, v[24:27]\n" \
  "ds_write_b128 v13, v[28:31]\n" \
  "s_nop 0\n" \
  "ds_write_b128 v14, v[32:35]\n" \
  "s_cbranch_scc0 LX_%=\n" \
  "s_waitcnt lgkmcnt(0)\n" \
  "s_barrier\n" \
  "ds_read_b128 v[52:55], v11 offset:0\n" \
  "ds_read_b128 v[56:59], v11 offset:4096\n" \
  "ds_read_b128 v[60:63], v11 offset:8192\n" \
  "ds_read_b128 v[36:39], v11 offset:12288\n" \
  "s_waitcnt lgkmcnt(0)\n" \
  "v_add_f32 v36, v36, v52\n" \
  "v_add_f32 v37, v37, v53\n" \
  "v_add_f32 v38, v38, v54\n" \
  "v_add_f32 v39, v39, v55\n" \
  "v_add_f32 v56, v56, v60\n" \
  "v_add_f32 v57, v57, v61\n" \
  "v_add_f32 v58, v58, v62\n" \
  "v_add_f32 v59, v59, v63\n" \
  "v_add_f32 v36, v36, v56\n" \
  "v_add_f32 v37, v37, v57\n" \
  "v_add_f32 v38, v38, v58\n" \
  "v_add_f32 v39, v39, v59\n" \
  "v_cvt_pk_bf16_f32 v36, v36, v37\n" \
  "v_cvt_pk_bf16_f32 v37, v38, v39\n" \
  "buffer_store_dwordx2 v[36:37], v10, s[76:79], s88 offen\n" \
  "s_add_u32 s88, s88, 131072\n" \
  "v_and_b32 v10, 0x7fffffff, v10\n" \
  "s_waitcnt vmcnt(0)\n" \
  "s_branch LE_%=\n" \
  "LP2_%=:\n" \
  "buffer_load_dword v9, v8, s[80:83], 0 offen sc1\n" \
  "s_waitcnt vmcnt(0)\n" \
  "v_cmp_gt_u32 vcc, s92, v9\n" \
  "s_cbranch_vccz LP3_%=\n" \
  "s_sleep 2\n" \
  "s_add_u32 s91, s91, 1\n" \
  "s_cmp_lt_u32 s91, " STR(SPIN_CAP) "\n" \
  "s_cbranch_scc1 LP2_%=\n" \
  "v_mov_b32 v5, " STR(CW_DEAD) "*4\n" \
  "v_mov_b32 v15, 1\n" \
  "buffer_store_dword v15, v5, s[80:83], 0 offen sc1\n" \
  "s_branch LP3_%=\n" \
  "LE_%=:\n" \
  ""

#define XA_ASM \
  "s_mov_b64 s[68:69], %0\n" \
  "s_and_b32 s69, s69, 0xffff\n" \
  "s_mov_b32 s70, " STR(EX_BYTES32) "\n" \
  "s_mov_b32 s71, 0x00020000\n" \
  "s_mov_b64 s[72:73], %1\n" \
  "s_and_b32 s73, s73, 0xffff\n" \
  "s_mov_b32 s74, 4*" STR(QKV_SZ32) "\n" \
  "s_mov_b32 s75, 0x00020000\n" \
  "s_mov_b64 s[76:77], %2\n" \
  "s_and_b32 s77, s77, 0xffff\n" \
  "s_mov_b32 s78, " STR(CTRL_BYTES) "\n" \
  "s_mov_b32 s79, 0x00020000\n" \
  "v_mbcnt_lo_u32_b32 v4, -1, 0\n" \
  "v_mbcnt_hi_u32_b32 v4, -1, v4\n" \
  "v_and_b32 v116, 15, v4\n" \
  "v_lshrrev_b32 v117, 4, v4\n" \
  "v_lshlrev_b32 v7, 4, v4\n" \
  "v_add_u32 v8, 4096, v7\n" \
  "v_add_u32 v17, 0x40000, v7\n" \
  "v_add_u32 v11, %5, v7\n" \
  "v_lshrrev_b32 v5, 2, v116\n" \
  "v_and_b32 v5, 1, v5\n" \
  "v_lshl_add_u32 v5, v117, 2, v5\n" \
  "v_and_b32 v6, 2, v4\n" \
  "v_add_lshl_u32 v5, v5, v6, 4\n" \
  "v_and_b32 v6, 1, v4\n" \
  "v_lshl_add_u32 v5, v6, 3, v5\n" \
  "v_add_u32 v12, %5, v5\n" \
  "s_lshl_b32 s80, %3, 7\n" \
  "s_add_u32 s81, s80, " STR(CW_XPROG) "*4\n" \
  "v_lshl_add_u32 v15, v4, 24, s81\n" \
  "s_add_u32 s81, s80, " STR(CW_PROG) "*4\n" \
  "v_mov_b32 v13, s81\n" \
  "s_lshr_b32 s80, %3, 1\n" \
  "s_lshl_b32 s80, s80, 9\n" \
  "s_add_u32 s80, s80, " STR(CW_GPROG) "*4\n" \
  "v_writelane_b32 v13, s80, 1\n" \
  "s_add_u32 s80, s80, 128\n" \
  "v_writelane_b32 v13, s80, 2\n" \
  "s_add_u32 s80, s80, 128\n" \
  "v_writelane_b32 v13, s80, 3\n" \
  "s_add_u32 s80, s80, 128\n" \
  "v_writelane_b32 v13, s80, 4\n" \
  "v_mov_b32 v16, 0\n" \
  "v_mov_b32 v6, 0xff800000\n" \
  "v_lshlrev_b32 v5, 2, v117\n" \
  "v_cmp_gt_u32 vcc, v5, v116\n" \
  "v_mov_b32 v24, 0x3f803f80\n" \
  "v_add_u32 v5, 1, v5\n" \
  "v_cndmask_b32 v20, 0, v6, vcc\n" \
  "v_cmp_gt_u32 vcc, v5, v116\n" \
  "v_mov_b32 v25, 0x3f803f80\n" \
  "v_add_u32 v5, 1, v5\n" \
  "v_cndmask_b32 v21, 0, v6, vcc\n" \
  "v_cmp_gt_u32 vcc, v5, v116\n" \
  "v_mov_b32 v26, 0x3f803f80\n" \
  "v_add_u32 v5, 1, v5\n" \
  "v_cndmask_b32 v22, 0, v6, vcc\n" \
  "v_cmp_gt_u32 vcc, v5, v116\n" \
  "v_mov_b32 v27, 0x3f803f80\n" \
  "v_add_u32 v5, 1, v5\n" \
  "v_cndmask_b32 v23, 0, v6, vcc\n" \
  "v_mov_b64 v[130:131], 0\n" \
  "v_mov_b32 v156, 0\n" \
  "v_mov_b32 v158, 0\n" \
  "s_mov_b32 s89, 0x3e0293ee\n" \
  "s_mov_b32 s88, 0\n" \
  "s_mov_b32 s82, 0\n" \
  "s_mov_b32 s90, 0\n" \
  "s_lshr_b32 s80, s82, 2\n" \
  "s_xnor_b32 s80, s80, %4\n" \
  "s_and_b32 s80, s80, 1\n" \
  "s_lshl_b32 s80, s80, 13\n" \
  "s_lshl_b32 s83, s82, 19\n" \
  "s_or_b32 s83, s83, s80\n" \
  "s_lshl_b32 s80, %3, 14\n" \
  "s_add_u32 s83, s83, s80\n" \
  "s_add_u32 s84, s83, " STR(QKV_SZ32) "\n" \
  "s_add_u32 s85, s84, " STR(QKV_SZ32) "\n" \
  "s_mov_b32 s81, 1\n" \
  "LP0_%=:\n" \
  "buffer_load_dword v14, v13, s[76:79], 0 offen sc1\n" \
  "s_waitcnt vmcnt(0)\n" \
  "v_readlane_b32 s91, v14, 0\n" \
  "v_readlane_b32 s92, v14, 1\n" \
  "v_readlane_b32 s80, v14, 2\n" \
  "s_min_u32 s92, s92, s80\n" \
  "v_readlane_b32 s80, v14, 3\n" \
  "s_min_u32 s92, s92, s80\n" \
  "v_readlane_b32 s80, v14, 4\n" \
  "s_min_u32 s92, s92, s80\n" \
  "s_cmp_ge_u32 s92, s81\n" \
  "s_cbranch_scc1 LP1_%=\n" \
  "s_sleep 16\n" \
  "s_add_u32 s88, s88, 1\n" \
  "s_cmp_lt_u32 s88, " STR(SPIN_CAP) "\n" \
  "s_cbranch_scc1 LP0_%=\n" \
  "v_mov_b32 v5, " STR(CW_DEAD) "*4\n" \
  "v_mov_b32 v6, 1\n" \
  "buffer_store_dword v6, v5, s[76:79], 0 offen sc1\n" \
  "s_branch LP1_%=\n" \
  "LP1_%=:\n" \
  "buffer_load_dwordx4 a[64:67], v7, s[72:75], s84 offen offset:0 sc1\n" \
  "buffer_load_dwordx4 a[68:71], v7, s[72:75], s84 offen offset:1024 sc1\n" \
  "buffer_load_dwordx4 a[72:75], v7, s[72:75], s84 offen offset:2048 sc1\n" \
  "buffer_load_dwordx4 a[76:79], v7, s[72:75], s84 offen offset:3072 sc1\n" \
  "buffer_load_dwordx4 a[80:83], v8, s[72:75], s84 offen offset:0 sc1\n" \
  "buffer_load_dwordx4 a[84:87], v8, s[72:75], s84 offen offset:1024 sc1\n" \
  "buffer_load_dwordx4 a[88:91], v8, s[72:75], s84 offen offset:2048 sc1\n" \
  "buffer_load_dwordx4 a[92:95], v8, s[72:75], s84 offen offset:3072 sc1\n" \
  "buffer_load_dwordx4 a[0:3], v7, s[72:75], s83 offen offset:0 sc1\n" \
  "buffer_load_dwordx4 a[4:7], v7, s[72:75], s83 offen offset:1024 sc1\n" \
  "buffer_load_dwordx4 a[8:11], v7, s[72:75], s83 offen offset:2048 sc1\n" \
  "buffer_load_dwordx4 a[12:15], v7, s[72:75], s83 offen offset:3072 sc1\n" \
  "buffer_load_dwordx4 a[16:19], v8, s[72:75], s83 offen offset:0 sc1\n" \
  "buffer_load_dwordx4 a[20:23], v8, s[72:75], s83 offen offset:1024 sc1\n" \
  "buffer_load_dwordx4 a[24:27], v8, s[72:75], s83 offen offset:2048 sc1\n" \
  "buffer_load_dwordx4 a[28:31], v8, s[72:75], s83 offen offset:3072 sc1\n" \
  "buffer_load_dwordx4 a[96:99], v7, s[72:75], s85 offen offset:0 sc1\n" \
  "buffer_load_dwordx4 a[100:103], v7, s[72:75], s85 offen offset:1024 sc1\n" \
  "buffer_load_dwordx4 a[104:107], v7, s[72:75], s85 offen offset:2048 sc1\n" \
  "buffer_load_dwordx4 a[108:111], v7, s[72:75], s85 offen offset:3072 sc1\n" \
  "buffer_load_dwordx4 a[112:115], v8, s[72:75], s85 offen offset:0 sc1\n" \
  "buffer_load_dwordx4 a[116:119], v8, s[72:75], s85 offen offset:1024 sc1\n" \
  "buffer_load_dwordx4 a[120:123], v8, s[72:75], s85 offen offset:2048 sc1\n" \
  "buffer_load_dwordx4 a[124:127], v8, s[72:75], s85 offen offset:3072 sc1\n" \
  "s_add_u32 s80, s82, 0\n" \
  "s_lshl_b32 s80, s80, 5\n" \
  "s_add_u32 s80, s80, %3\n" \
  "s_lshl_b32 s87, s80, 14\n" \
  "s_add_u32 s87, s87, " STR(EXP_B) "<<21\n" \
  "s_lshr_b32 s80, s87, 21\n" \
  "s_lshr_b32 s80, %4, s80\n" \
  "s_and_b32 s80, s80, 1\n" \
  "s_lshl_b32 s80, s80, 13\n" \
  "s_or_b32 s87, s87, s80\n" \
  "s_waitcnt vmcnt(0)\n" \
  ".p2align 6\n" \
  "LA_%=:\n" \
  "s_waitcnt vmcnt(24)\n" \
  "s_add_u32 s81, s82, 2\n" \
  "s_min_u32 s81, s81, 64\n" \
  "s_sub_u32 s84, s81, 1\n" \
  "v_mfma_f32_16x16x32_bf16 v[116:119], a[64:67], a[0:3], v[20:23]\n" \
  "s_lshr_b32 s80, s84, 2\n" \
  "s_xnor_b32 s80, s80, %4\n" \
  "v_mfma_f32_16x16x32_bf16 v[120:123], a[64:67], a[16:19], 0\n" \
  "s_and_b32 s80, s80, 1\n" \
  "s_lshl_b32 s80, s80, 13\n" \
  "v_mfma_f32_16x16x32_bf16 v[124:127], a[80:83], a[16:19], v[20:23]\n" \
  "s_lshl_b32 s83, s84, 19\n" \
  "s_or_b32 s83, s83, s80\n" \
  "v_mfma_f32_16x16x32_bf16 v[116:119], a[68:71], a[4:7], v[116:119]\n" \
  "s_lshl_b32 s80, %3, 14\n" \
  "s_add_u32 s83, s83, s80\n" \
  "v_mfma_f32_16x16x32_bf16 v[120:123], a[68:71], a[20:23], v[120:123]\n" \
  "s_add_u32 s84, s83, " STR(QKV_SZ32) "\n" \
  "s_add_u32 s85, s84, " STR(QKV_SZ32) "\n" \
  "v_mfma_f32_16x16x32_bf16 v[124:127], a[84:87], a[20:23], v[124:127]\n" \
  "s_lshr_b32 s80, s82, 2\n" \
  "s_xnor_b32 s80, s80, %4\n" \
  "v_mfma_f32_16x16x32_bf16 v[116:119], a[72:75], a[8:11], v[116:119]\n" \
  "s_and_b32 s80, s80, 1\n" \
  "s_lshl_b32 s80, s80, 13\n" \
  "v_mfma_f32_16x16x32_bf16 v[120:123], a[72:75], a[24:27], v[120:123]\n" \
  "s_lshl_b32 s86, s82, 19\n" \
  "s_or_b32 s86, s86, s80\n" \
  "v_mfma_f32_16x16x32_bf16 v[124:127], a[88:91], a[24:27], v[124:127]\n" \
  "s_lshr_b32 s80, %3, 1\n" \
  "s_lshl_b32 s80, s80, 14\n" \
  "v_mfma_f32_16x16x32_bf16 v[116:119], a[76:79], a[12:15], v[116:119]\n" \
  "s_add_u32 s86, s86, s80\n" \
  "s_and_b32 s80, %3, 1\n" \
  "v_mfma_f32_16x16x32_bf16 v[120:123], a[76:79], a[28:31], v[120:123]\n" \
  "s_lshl_b32 s80, s80, 12\n" \
  "s_add_u32 s86, s86, s80\n" \
  "v_mfma_f32_16x16x32_bf16 v[124:127], a[92:95], a[28:31], v[124:127]\n" \
  "s_add_u32 s86, s86, 3*" STR(QKV_SZ32) "\n" \
  "s_cmp_lt_u32 s92, s81\n" \
  "s_cbranch_scc1 LG0_%=\n" \
  "LH0_%=:\n" \
  "buffer_load_dwordx4 a[64:67], v7, s[72:75], s84 offen offset:0 sc1\n" \
  "buffer_load_dwordx4 a[68:71], v7, s[72:75], s84 offen offset:1024 sc1\n" \
  "buffer_load_dwordx4 a[72:75], v7, s[72:75], s84 offen offset:2048 sc1\n" \
  "buffer_load_dwordx4 a[76:79], v7, s[72:75], s84 offen offset:3072 sc1\n" \
  "buffer_load_dwordx4 a[80:83], v8, s[72:75], s84 offen offset:0 sc1\n" \
  "buffer_load_dwordx4 a[84:87], v8, s[72:75], s84 offen offset:1024 sc1\n" \
  "v_max3_f32 v144, v116, v117, v118\n" \
  "v_max3_f32 v145, v120, v121, v122\n" \
  "v_max_f32 v144, v144, v119\n" \
  "v_max3_f32 v145, v145, v123, v124\n" \
  "v_max3_f32 v145, v145, v125, v126\n" \
  "v_max_f32 v145, v145, v127\n" \
  "v_mov_b32 v5, v144\n" \
  "v_mov_b32 v6, v145\n" \
  "s_nop 0\n" \
  "v_permlane16_swap_b32 v144, v5\n" \
  "v_permlane16_swap_b32 v145, v6\n" \
  "v_max_f32 v144, v144, v5\n" \
  "v_max_f32 v145, v145, v6\n" \
  "v_mov_b32 v5, v144\n" \
  "v_mov_b32 v6, v145\n" \
  "s_nop 0\n" \
  "v_permlane32_swap_b32 v144, v5\n" \
  "v_permlane32_swap_b32 v145, v6\n" \
  "v_max_f32 v144, v144, v5\n" \
  "v_max_f32 v145, v145, v6\n" \
  "v_mul_f32 v144, s89, v144\n" \
  "v_mul_f32 v145, s89, v145\n" \
  "v_fma_f32 v116, v116, s89, -v144\n" \
  "v_exp_f32 v116, v116\n" \
  "buffer_load_dwordx4 a[88:91], v8, s[72:75], s84 offen offset:2048 sc1\n" \
  "v_fma_f32 v117, v117, s89, -v144\n" \
  "v_exp_f32 v117, v117\n" \
  "buffer_load_dwordx4 a[92:95], v8, s[72:75], s84 offen offset:3072 sc1\n" \
  "v_fma_f32 v118, v118, s89, -v144\n" \
  "v_exp_f32 v118, v118\n" \
  "buffer_load_dwordx4 a[32:35], v7, s[72:75], s83 offen offset:0 sc1\n" \
  "v_fma_f32 v119, v119, s89, -v144\n" \
  "v_exp_f32 v119, v119\n" \
  "buffer_load_dwordx4 a[36:39], v7, s[72:75], s83 offen offset:1024 sc1\n" \
  "v_fma_f32 v120, v120, s89, -v145\n" \
  "v_exp_f32 v120, v120\n" \
  "buffer_load_dwordx4 a[40:43], v7, s[72:75], s83 offen offset:2048 sc1\n" \
  "v_fma_f32 v121, v121, s89, -v145\n" \
  "v_exp_f32 v121, v121\n" \
  "buffer_load_dwordx4 a[44:47], v7, s[72:75], s83 offen offset:3072 sc1\n" \
  "v_fma_f32 v122, v122, s89, -v145\n" \
  "v_exp_f32 v122, v122\n" \
  "buffer_load_dwordx4 a[48:51], v8, s[72:75], s83 offen offset:0 sc1\n" \
  "v_fma_f32 v123, v123, s89, -v145\n" \
  "v_exp_f32 v123, v123\n" \
  "buffer_load_dwordx4 a[52:55], v8, s[72:75], s83 offen offset:1024 sc1\n" \
  "v_fma_f32 v124, v124, s89, -v145\n" \
  "v_exp_f32 v124, v124\n" \
  "buffer_load_dwordx4 a[56:59], v8, s[72:75], s83 offen offset:2048 sc1\n" \
  "v_fma_f32 v125, v125, s89, -v145\n" \
  "v_exp_f32 v125, v125\n" \
  "buffer_load_dwordx4 a[60:63], v8, s[72:75], s83 offen offset:3072 sc1\n" \
  "v_fma_f32 v126, v126, s89, -v145\n" \
  "v_exp_f32 v126, v126\n" \
  "v_fma_f32 v127, v127, s89, -v145\n" \
  "v_exp_f32 v127, v127\n" \
  "s_nop 0\n" \
  "v_cvt_pk_bf16_f32 v128, v116, v117\n" \
  "v_cvt_pk_bf16_f32 v129, v118, v119\n" \
  "v_cvt_pk_bf16_f32 v132, v120, v121\n" \
  "v_cvt_pk_bf16_f32 v133, v122, v123\n" \
  "v_cvt_pk_bf16_f32 v134, v124, v125\n" \
  "v_cvt_pk_bf16_f32 v135, v126, v127\n" \
  "s_waitcnt vmcnt(22)\n" \
  "buffer_store_dword v16, v15, s[76:79], 0 offen sc1\n" \
  "v_mfma_f32_16x16x32_bf16 v[136:139], v[24:27], v[128:131], 0\n" \
  "v_mfma_f32_16x16x32_bf16 v[140:143], v[24:27], v[132:135], 0\n" \
  "v_add_u32 v16, 1, v16\n" \
  "v_mfma_f32_16x16x32_bf16 v[52:55], a[96:99], v[128:131], 0\n" \
  "v_mfma_f32_16x16x32_bf16 v[84:87], a[96:99], v[132:135], 0\n" \
  "buffer_load_dwordx4 a[96:99], v7, s[72:75], s85 offen offset:0 sc1\n" \
  "v_mfma_f32_16x16x32_bf16 v[56:59], a[100:103], v[128:131], 0\n" \
  "v_mfma_f32_16x16x32_bf16 v[88:91], a[100:103], v[132:135], 0\n" \
  "buffer_load_dwordx4 a[100:103], v7, s[72:75], s85 offen offset:1024 sc1\n" \
  "v_mfma_f32_16x16x32_bf16 v[60:63], a[104:107], v[128:131], 0\n" \
  "v_mfma_f32_16x16x32_bf16 v[92:95], a[104:107], v[132:135], 0\n" \
  "buffer_load_dwordx4 a[104:107], v7, s[72:75], s85 offen offset:2048 sc1\n" \
  "v_mfma_f32_16x16x32_bf16 v[64:67], a[108:111], v[128:131], 0\n" \
  "v_mfma_f32_16x16x32_bf16 v[96:99], a[108:111], v[132:135], 0\n" \
  "buffer_load_dwordx4 a[108:111], v7, s[72:75], s85 offen offset:3072 sc1\n" \
  "v_mfma_f32_16x16x32_bf16 v[68:71], a[112:115], v[128:131], 0\n" \
  "v_mfma_f32_16x16x32_bf16 v[100:103], a[112:115], v[132:135], 0\n" \
  "buffer_load_dwordx4 a[112:115], v8, s[72:75], s85 offen offset:0 sc1\n" \
  "v_mfma_f32_16x16x32_bf16 v[72:75], a[116:119], v[128:131], 0\n" \
  "v_mfma_f32_16x16x32_bf16 v[104:107], a[116:119], v[132:135], 0\n" \
  "buffer_load_dwordx4 a[116:119], v8, s[72:75], s85 offen offset:1024 sc1\n" \
  "v_mfma_f32_16x16x32_bf16 v[76:79], a[120:123], v[128:131], 0\n" \
  "v_mfma_f32_16x16x32_bf16 v[108:111], a[120:123], v[132:135], 0\n" \
  "buffer_load_dwordx4 a[120:123], v8, s[72:75], s85 offen offset:2048 sc1\n" \
  "v_mfma_f32_16x16x32_bf16 v[80:83], a[124:127], v[128:131], 0\n" \
  "v_mfma_f32_16x16x32_bf16 v[112:115], a[124:127], v[132:135], 0\n" \
  "buffer_load_dwordx4 a[124:127], v8, s[72:75], s85 offen offset:3072 sc1\n" \
  "buffer_load_dword v14, v13, s[76:79], 0 offen sc1\n" \
  "s_cmp_eq_u32 s90, 0\n" \
  "s_cbranch_scc1 LW0_%=\n" \
  "s_waitcnt vmcnt(26)\n" \
  "LM0_%=:\n" \
  "ds_write_b128 v11, v[44:47]\n" \
  "ds_write_b128 v11, v[48:51] offset:1024\n" \
  "v_mfma_f32_16x16x32_bf16 v[148:151], v[28:31], a[0:3], 0\n" \
  "v_max3_u32 v5, v28, v29, v30\n" \
  "v_max3_u32 v5, v5, v31, v32\n" \
  "v_mfma_f32_16x16x32_bf16 v[152:155], v[28:31], a[16:19], 0\n" \
  "v_max3_u32 v5, v5, v33, v34\n" \
  "v_max3_u32 v5, v5, v35, v36\n" \
  "v_mfma_f32_16x16x32_bf16 v[148:151], v[32:35], a[4:7], v[148:151]\n" \
  "v_max3_u32 v5, v5, v37, v38\n" \
  "v_max3_u32 v5, v5, v39, v40\n" \
  "v_mfma_f32_16x16x32_bf16 v[152:155], v[32:35], a[20:23], v[152:155]\n" \
  "v_max3_u32 v5, v5, v41, v42\n" \
  "v_max3_u32 v5, v5, v43, v44\n" \
  "v_mfma_f32_16x16x32_bf16 v[148:151], v[36:39], a[8:11], v[148:151]\n" \
  "v_max3_u32 v5, v5, v45, v46\n" \
  "v_max3_u32 v5, v5, v47, v48\n" \
  "v_mfma_f32_16x16x32_bf16 v[152:155], v[36:39], a[24:27], v[152:155]\n" \
  "v_max3_u32 v5, v5, v49, v50\n" \
  "v_max_u32 v5, v5, v51\n" \
  "v_mfma_f32_16x16x32_bf16 v[148:151], v[40:43], a[12:15], v[148:151]\n" \
  "v_cmp_eq_u32 vcc, -1, v5\n" \
  "v_mfma_f32_16x16x32_bf16 v[152:155], v[40:43], a[28:31], v[152:155]\n" \
  "ds_read_b64_tr_b16 a[128:129], v12 offset:0\n" \
  "ds_read_b64_tr_b16 a[130:131], v12 offset:256\n" \
  "ds_read_b64_tr_b16 a[132:133], v12 offset:512\n" \
  "ds_read_b64_tr_b16 a[134:135], v12 offset:768\n" \
  "ds_read_b64_tr_b16 a[136:137], v12 offset:1024\n" \
  "ds_read_b64_tr_b16 a[138:139], v12 offset:1280\n" \
  "ds_read_b64_tr_b16 a[140:141], v12 offset:1536\n" \
  "ds_read_b64_tr_b16 a[142:143], v12 offset:1792\n" \
  "s_cbranch_vccnz LR0_%=\n" \
  "v_max_f32 v146, v150, v151\n" \
  "v_max_f32 v147, v154, v155\n" \
  "v_mov_b32 v5, v146\n" \
  "v_mov_b32 v6, v147\n" \
  "s_nop 0\n" \
  "v_permlane16_swap_b32 v146, v5\n" \
  "v_permlane16_swap_b32 v147, v6\n" \
  "v_max_f32 v146, v146, v5\n" \
  "v_max_f32 v147, v147, v6\n" \
  "v_mov_b32 v5, v146\n" \
  "v_mov_b32 v6, v147\n" \
  "s_nop 0\n" \
  "v_permlane32_swap_b32 v146, v5\n" \
  "v_permlane32_swap_b32 v147, v6\n" \
  "v_max_f32 v146, v146, v5\n" \
  "v_max_f32 v147, v147, v6\n" \
  "v_mul_f32 v146, s89, v146\n" \
  "v_mul_f32 v147, s89, v147\n" \
  "v_max_f32 v146, v146, v144\n" \
  "v_max_f32 v147, v147, v145\n" \
  "v_sub_f32 v160, v144, v146\n" \
  "v_sub_f32 v162, v145, v147\n" \
  "v_fma_f32 v150, v150, s89, -v146\n" \
  "v_fma_f32 v151, v151, s89, -v146\n" \
  "v_fma_f32 v154, v154, s89, -v147\n" \
  "v_fma_f32 v155, v155, s89, -v147\n" \
  "v_exp_f32 v150, v150\n" \
  "v_exp_f32 v151, v151\n" \
  "v_exp_f32 v154, v154\n" \
  "v_exp_f32 v155, v155\n" \
  "v_exp_f32 v160, v160\n" \
  "v_exp_f32 v162, v162\n" \
  "v_add_f32 v146, v150, v151\n" \
  "v_add_f32 v147, v154, v155\n" \
  "v_mov_b32 v5, v146\n" \
  "v_mov_b32 v6, v147\n" \
  "s_nop 0\n" \
  "v_permlane16_swap_b32 v146, v5\n" \
  "v_permlane16_swap_b32 v147, v6\n" \
  "v_add_f32 v146, v146, v5\n" \
  "v_add_f32 v147, v147, v6\n" \
  "v_mov_b32 v5, v146\n" \
  "v_mov_b32 v6, v147\n" \
  "s_nop 0\n" \
  "v_permlane32_swap_b32 v146, v5\n" \
  "v_permlane32_swap_b32 v147, v6\n" \
  "v_add_f32 v146, v146, v5\n" \
  "v_add_f32 v147, v147, v6\n" \
  "v_fma_f32 v146, v160, v136, v146\n" \
  "v_fma_f32 v147, v162, v140, v147\n" \
  "v_rcp_f32 v146, v146\n" \
  "v_rcp_f32 v147, v147\n" \
  "v_mul_f32 v160, v160, v146\n" \
  "v_mul_f32 v162, v162, v147\n" \
  "v_mul_f32 v150, v150, v146\n" \
  "v_mul_f32 v151, v151, v146\n" \
  "v_mul_f32 v154, v154, v147\n" \
  "v_mul_f32 v155, v155, v147\n" \
  "v_cvt_pk_bf16_f32 v157, v150, v151\n" \
  "v_cvt_pk_bf16_f32 v159, v154, v155\n" \
  "v_pk_mul_f32 v[52:53], v[52:53], v[160:161] op_sel_hi:[1,0]\n" \
  "v_pk_mul_f32 v[54:55], v[54:55], v[160:161] op_sel_hi:[1,0]\n" \
  "v_pk_mul_f32 v[56:57], v[56:57], v[160:161] op_sel_hi:[1,0]\n" \
  "v_pk_mul_f32 v[58:59], v[58:59], v[160:161] op_sel_hi:[1,0]\n" \
  "v_pk_mul_f32 v[60:61], v[60:61], v[160:161] op_sel_hi:[1,0]\n" \
  "v_pk_mul_f32 v[62:63], v[62:63], v[160:161] op_sel_hi:[1,0]\n" \
  "v_pk_mul_f32 v[64:65], v[64:65], v[160:161] op_sel_hi:[1,0]\n" \
  "v_pk_mul_f32 v[66:67], v[66:67], v[160:161] op_sel_hi:[1,0]\n" \
  "v_pk_mul_f32 v[68:69], v[68:69], v[160:161] op_sel_hi:[1,0]\n" \
  "v_pk_mul_f32 v[70:71], v[70:71], v[160:161] op_sel_hi:[1,0]\n" \
  "v_pk_mul_f32 v[72:73], v[72:73], v[160:161] op_sel_hi:[1,0]\n" \
  "v_pk_mul_f32 v[74:75], v[74:75], v[160:161] op_sel_hi:[1,0]\n" \
  "v_pk_mul_f32 v[76:77], v[76:77], v[160:161] op_sel_hi:[1,0]\n" \
  "v_pk_mul_f32 v[78:79], v[78:79], v[160:161] op_sel_hi:[1,0]\n" \
  "v_pk_mul_f32 v[80:81], v[80:81], v[160:161] op_sel_hi:[1,0]\n" \
  "v_pk_mul_f32 v[82:83], v[82:83], v[160:161] op_sel_hi:[1,0]\n" \
  "v_pk_mul_f32 v[84:85], v[84:85], v[162:163] op_sel_hi:[1,0]\n" \
  "v_pk_mul_f32 v[86:87], v[86:87], v[162:163] op_sel_hi:[1,0]\n" \
  "v_pk_mul_f32 v[88:89], v[88:89], v[162:163] op_sel_hi:[1,0]\n" \
  "v_pk_mul_f32 v[90:91], v[90:91], v[162:163] op_sel_hi:[1,0]\n" \
  "v_pk_mul_f32 v[92:93], v[92:93], v[162:163] op_sel_hi:[1,0]\n" \
  "v_pk_mul_f32 v[94:95], v[94:95], v[162:163] op_sel_hi:[1,0]\n" \
  "v_pk_mul_f32 v[96:97], v[96:97], v[162:163] op_sel_hi:[1,0]\n" \
  "v_pk_mul_f32 v[98:99], v[98:99], v[162:163] op_sel_hi:[1,0]\n" \
  "v_pk_mul_f32 v[100:101], v[100:101], v[162:163] op_sel_hi:[1,0]\n" \
  "v_pk_mul_f32 v[102:103], v[102:103], v[162:163] op_sel_hi:[1,0]\n" \
  "v_pk_mul_f32 v[104:105], v[104:105], v[162:163] op_sel_hi:[1,0]\n" \
  "v_pk_mul_f32 v[106:107], v[106:107], v[162:163] op_sel_hi:[1,0]\n" \
  "v_pk_mul_f32 v[108:109], v[108:109], v[162:163] op_sel_hi:[1,0]\n" \
  "v_pk_mul_f32 v[110:111], v[110:111], v[162:163] op_sel_hi:[1,0]\n" \
  "v_pk_mul_f32 v[112:113], v[112:113], v[162:163] op_sel_hi:[1,0]\n" \
  "v_pk_mul_f32 v[114:115], v[114:115], v[162:163] op_sel_hi:[1,0]\n" \
  "s_waitcnt lgkmcnt(0)\n" \
  "v_mfma_f32_16x16x16_bf16 v[52:55], a[128:129], v[156:157], v[52:55]\n" \
  "v_mfma_f32_16x16x16_bf16 v[56:59], a[130:131], v[156:157], v[56:59]\n" \
  "v_mfma_f32_16x16x16_bf16 v[60:63], a[132:133], v[156:157], v[60:63]\n" \
  "v_mfma_f32_16x16x16_bf16 v[64:67], a[134:135], v[156:157], v[64:67]\n" \
  "v_mfma_f32_16x16x16_bf16 v[68:71], a[136:137], v[156:157], v[68:71]\n" \
  "v_cvt_pk_bf16_f32 v164, v52, v53\n" \
  "v_cvt_pk_bf16_f32 v165, v54, v55\n" \
  "v_mfma_f32_16x16x16_bf16 v[72:75], a[138:139], v[156:157], v[72:75]\n" \
  "v_cvt_pk_bf16_f32 v166, v56, v57\n" \
  "v_cvt_pk_bf16_f32 v167, v58, v59\n" \
  "buffer_store_dwordx4 v[164:167], v7, s[72:75], s86 offen offset:0 sc1\n" \
  "v_mfma_f32_16x16x16_bf16 v[76:79], a[140:141], v[156:157], v[76:79]\n" \
  "v_cvt_pk_bf16_f32 v168, v60, v61\n" \
  "v_cvt_pk_bf16_f32 v169, v62, v63\n" \
  "v_mfma_f32_16x16x16_bf16 v[80:83], a[142:143], v[156:157], v[80:83]\n" \
  "v_cvt_pk_bf16_f32 v170, v64, v65\n" \
  "v_cvt_pk_bf16_f32 v171, v66, v67\n" \
  "buffer_store_dwordx4 v[168:171], v7, s[72:75], s86 offen offset:1024 sc1\n" \
  "v_mfma_f32_16x16x16_bf16 v[84:87], a[128:129], v[158:159], v[84:87]\n" \
  "v_cvt_pk_bf16_f32 v172, v68, v69\n" \
  "v_cvt_pk_bf16_f32 v173, v70, v71\n" \
  "v_mfma_f32_16x16x16_bf16 v[88:91], a[130:131], v[158:159], v[88:91]\n" \
  "v_cvt_pk_bf16_f32 v174, v72, v73\n" \
  "v_cvt_pk_bf16_f32 v175, v74, v75\n" \
  "buffer_store_dwordx4 v[172:175], v7, s[72:75], s86 offen offset:2048 sc1\n" \
  "v_mfma_f32_16x16x16_bf16 v[92:95], a[132:133], v[158:159], v[92:95]\n" \
  "v_cvt_pk_bf16_f32 v176, v76, v77\n" \
  "v_cvt_pk_bf16_f32 v177, v78, v79\n" \
  "v_mfma_f32_16x16x16_bf16 v[96:99], a[134:135], v[158:159], v[96:99]\n" \
  "v_cvt_pk_bf16_f32 v178, v80, v81\n" \
  "v_cvt_pk_bf16_f32 v179, v82, v83\n" \
  "buffer_store_dwordx4 v[176:179], v7, s[72:75], s86 offen offset:3072 sc1\n" \
  "v_mfma_f32_16x16x16_bf16 v[100:103], a[136:137], v[158:159], v[100:103]\n" \
  "v_cvt_pk_bf16_f32 v180, v84, v85\n" \
  "v_cvt_pk_bf16_f32 v181, v86, v87\n" \
  "v_mfma_f32_16x16x16_bf16 v[104:107], a[138:139], v[158:159], v[104:107]\n" \
  "v_cvt_pk_bf16_f32 v182, v88, v89\n" \
  "v_cvt_pk_bf16_f32 v183, v90, v91\n" \
  "buffer_store_dwordx4 v[180:183], v17, s[72:75], s86 offen offset:0 sc1\n" \
  "v_mfma_f32_16x16x16_bf16 v[108:111], a[140:141], v[158:159], v[108:111]\n" \
  "v_cvt_pk_bf16_f32 v184, v92, v93\n" \
  "v_cvt_pk_bf16_f32 v185, v94, v95\n" \
  "v_mfma_f32_16x16x16_bf16 v[112:115], a[142:143], v[158:159], v[112:115]\n" \
  "v_cvt_pk_bf16_f32 v186, v96, v97\n" \
  "v_cvt_pk_bf16_f32 v187, v98, v99\n" \
  "buffer_store_dwordx4 v[184:187], v17, s[72:75], s86 offen offset:1024 sc1\n" \
  "v_cvt_pk_bf16_f32 v188, v100, v101\n" \
  "v_cvt_pk_bf16_f32 v189, v102, v103\n" \
  "v_cvt_pk_bf16_f32 v190, v104, v105\n" \
  "v_cvt_pk_bf16_f32 v191, v106, v107\n" \
  "buffer_store_dwordx4 v[188:191], v17, s[72:75], s86 offen offset:2048 sc1\n" \
  "s_nop 1\n" \
  "v_cvt_pk_bf16_f32 v192, v108, v109\n" \
  "v_cvt_pk_bf16_f32 v193, v110, v111\n" \
  "v_cvt_pk_bf16_f32 v194, v112, v113\n" \
  "v_cvt_pk_bf16_f32 v195, v114, v115\n" \
  "buffer_store_dwordx4 v[192:195], v17, s[72:75], s86 offen offset:3072 sc1\n" \
  "s_add_u32 s82, s82, 1\n" \
  "s_cmp_eq_u32 s82, 64\n" \
  "s_cbranch_scc1 LZ_%=\n" \
  "s_add_u32 s80, s82, 0\n" \
  "s_lshl_b32 s80, s80, 5\n" \
  "s_add_u32 s80, s80, %3\n" \
  "s_lshl_b32 s87, s80, 14\n" \
  "s_add_u32 s87, s87, " STR(EXP_B) "<<21\n" \
  "s_lshr_b32 s80, s87, 21\n" \
  "s_lshr_b32 s80, %4, s80\n" \
  "s_and_b32 s80, s80, 1\n" \
  "s_lshl_b32 s80, s80, 13\n" \
  "s_or_b32 s87, s87, s80\n" \
  "s_waitcnt vmcnt(8)\n" \
  "v_readlane_b32 s91, v14, 0\n" \
  "v_readlane_b32 s92, v14, 1\n" \
  "v_readlane_b32 s80, v14, 2\n" \
  "s_min_u32 s92, s92, s80\n" \
  "v_readlane_b32 s80, v14, 3\n" \
  "s_min_u32 s92, s92, s80\n" \
  "v_readlane_b32 s80, v14, 4\n" \
  "s_min_u32 s92, s92, s80\n" \
  "s_cmp_gt_u32 s91, s82\n" \
  "s_cselect_b32 s90, 1, 0\n" \
  "s_cselect_b32 s80, 0, 0x80000000\n" \
  "v_or_b32 v9, s80, v7\n" \
  "v_or_b32 v10, s80, v8\n" \
  "buffer_load_dwordx4 v[28:31], v9, s[68:71], s87 offen offset:0 sc1\n" \
  "buffer_load_dwordx4 v[32:35], v9, s[68:71], s87 offen offset:1024 sc1\n" \
  "buffer_load_dwordx4 v[36:39], v9, s[68:71], s87 offen offset:2048 sc1\n" \
  "buffer_load_dwordx4 v[40:43], v9, s[68:71], s87 offen offset:3072 sc1\n" \
  "buffer_load_dwordx4 v[44:47], v10, s[68:71], s87 offen offset:0 sc1\n" \
  "buffer_load_dwordx4 v[48:51], v10, s[68:71], s87 offen offset:1024 sc1\n" \
  "s_waitcnt vmcnt(24)\n" \
  "s_add_u32 s81, s82, 2\n" \
  "s_min_u32 s81, s81, 64\n" \
  "s_sub_u32 s84, s81, 1\n" \
  "v_mfma_f32_16x16x32_bf16 v[116:119], a[64:67], a[32:35], v[20:23]\n" \
  "s_lshr_b32 s80, s84, 2\n" \
  "s_xnor_b32 s80, s80, %4\n" \
  "v_mfma_f32_16x16x32_bf16 v[120:123], a[64:67], a[48:51], 0\n" \
  "s_and_b32 s80, s80, 1\n" \
  "s_lshl_b32 s80, s80, 13\n" \
  "v_mfma_f32_16x16x32_bf16 v[124:127], a[80:83], a[48:51], v[20:23]\n" \
  "s_lshl_b32 s83, s84, 19\n" \
  "s_or_b32 s83, s83, s80\n" \
  "v_mfma_f32_16x16x32_bf16 v[116:119], a[68:71], a[36:39], v[116:119]\n" \
  "s_lshl_b32 s80, %3, 14\n" \
  "s_add_u32 s83, s83, s80\n" \
  "v_mfma_f32_16x16x32_bf16 v[120:123], a[68:71], a[52:55], v[120:123]\n" \
  "s_add_u32 s84, s83, " STR(QKV_SZ32) "\n" \
  "s_add_u32 s85, s84, " STR(QKV_SZ32) "\n" \
  "v_mfma_f32_16x16x32_bf16 v[124:127], a[84:87], a[52:55], v[124:127]\n" \
  "s_lshr_b32 s80, s82, 2\n" \
  "s_xnor_b32 s80, s80, %4\n" \
  "v_mfma_f32_16x16x32_bf16 v[116:119], a[72:75], a[40:43], v[116:119]\n" \
  "s_and_b32 s80, s80, 1\n" \
  "s_lshl_b32 s80, s80, 13\n" \
  "v_mfma_f32_16x16x32_bf16 v[120:123], a[72:75], a[56:59], v[120:123]\n" \
  "s_lshl_b32 s86, s82, 19\n" \
  "s_or_b32 s86, s86, s80\n" \
  "v_mfma_f32_16x16x32_bf16 v[124:127], a[88:91], a[56:59], v[124:127]\n" \
  "s_lshr_b32 s80, %3, 1\n" \
  "s_lshl_b32 s80, s80, 14\n" \
  "v_mfma_f32_16x16x32_bf16 v[116:119], a[76:79], a[44:47], v[116:119]\n" \
  "s_add_u32 s86, s86, s80\n" \
  "s_and_b32 s80, %3, 1\n" \
  "v_mfma_f32_16x16x32_bf16 v[120:123], a[76:79], a[60:63], v[120:123]\n" \
  "s_lshl_b32 s80, s80, 12\n" \
  "s_add_u32 s86, s86, s80\n" \
  "v_mfma_f32_16x16x32_bf16 v[124:127], a[92:95], a[60:63], v[124:127]\n" \
  "s_add_u32 s86, s86, 3*" STR(QKV_SZ32) "\n" \
  "s_cmp_lt_u32 s92, s81\n" \
  "s_cbranch_scc1 LG1_%=\n" \
  "LH1_%=:\n" \
  "buffer_load_dwordx4 a[64:67], v7, s[72:75], s84 offen offset:0 sc1\n" \
  "buffer_load_dwordx4 a[68:71], v7, s[72:75], s84 offen offset:1024 sc1\n" \
  "buffer_load_dwordx4 a[72:75], v7, s[72:75], s84 offen offset:2048 sc1\n" \
  "buffer_load_dwordx4 a[76:79], v7, s[72:75], s84 offen offset:3072 sc1\n" \
  "buffer_load_dwordx4 a[80:83], v8, s[72:75], s84 offen offset:0 sc1\n" \
  "buffer_load_dwordx4 a[84:87], v8, s[72:75], s84 offen offset:1024 sc1\n" \
  "v_max3_f32 v144, v116, v117, v118\n" \
  "v_max3_f32 v145, v120, v121, v122\n" \
  "v_max_f32 v144, v144, v119\n" \
  "v_max3_f32 v145, v145, v123, v124\n" \
  "v_max3_f32 v145, v145, v125, v126\n" \
  "v_max_f32 v145, v145, v127\n" \
  "v_mov_b32 v5, v144\n" \
  "v_mov_b32 v6, v145\n" \
  "s_nop 0\n" \
  "v_permlane16_swap_b32 v144, v5\n" \
  "v_permlane16_swap_b32 v145, v6\n" \
  "v_max_f32 v144, v144, v5\n" \
  "v_max_f32 v145, v145, v6\n" \
  "v_mov_b32 v5, v144\n" \
  "v_mov_b32 v6, v145\n" \
  "s_nop 0\n" \
  "v_permlane32_swap_b32 v144, v5\n" \
  "v_permlane32_swap_b32 v145, v6\n" \
  "v_max_f32 v144, v144, v5\n" \
  "v_max_f32 v145, v145, v6\n" \
  "v_mul_f32 v144, s89, v144\n" \
  "v_mul_f32 v145, s89, v145\n" \
  "v_fma_f32 v116, v116, s89, -v144\n" \
  "v_exp_f32 v116, v116\n" \
  "buffer_load_dwordx4 a[88:91], v8, s[72:75], s84 offen offset:2048 sc1\n" \
  "v_fma_f32 v117, v117, s89, -v144\n" \
  "v_exp_f32 v117, v117\n" \
  "buffer_load_dwordx4 a[92:95], v8, s[72:75], s84 offen offset:3072 sc1\n" \
  "v_fma_f32 v118, v118, s89, -v144\n" \
  "v_exp_f32 v118, v118\n" \
  "buffer_load_dwordx4 a[0:3], v7, s[72:75], s83 offen offset:0 sc1\n" \
  "v_fma_f32 v119, v119, s89, -v144\n" \
  "v_exp_f32 v119, v119\n" \
  "buffer_load_dwordx4 a[4:7], v7, s[72:75], s83 offen offset:1024 sc1\n" \
  "v_fma_f32 v120, v120, s89, -v145\n" \
  "v_exp_f32 v120, v120\n" \
  "buffer_load_dwordx4 a[8:11], v7, s[72:75], s83 offen offset:2048 sc1\n" \
  "v_fma_f32 v121, v121, s89, -v145\n" \
  "v_exp_f32 v121, v121\n" \
  "buffer_load_dwordx4 a[12:15], v7, s[72:75], s83 offen offset:3072 sc1\n" \
  "v_fma_f32 v122, v122, s89, -v145\n" \
  "v_exp_f32 v122, v122\n" \
  "buffer_load_dwordx4 a[16:19], v8, s[72:75], s83 offen offset:0 sc1\n" \
  "v_fma_f32 v123, v123, s89, -v145\n" \
  "v_exp_f32 v123, v123\n" \
  "buffer_load_dwordx4 a[20:23], v8, s[72:75], s83 offen offset:1024 sc1\n" \
  "v_fma_f32 v124, v124, s89, -v145\n" \
  "v_exp_f32 v124, v124\n" \
  "buffer_load_dwordx4 a[24:27], v8, s[72:75], s83 offen offset:2048 sc1\n" \
  "v_fma_f32 v125, v125, s89, -v145\n" \
  "v_exp_f32 v125, v125\n" \
  "buffer_load_dwordx4 a[28:31], v8, s[72:75], s83 offen offset:3072 sc1\n" \
  "v_fma_f32 v126, v126, s89, -v145\n" \
  "v_exp_f32 v126, v126\n" \
  "v_fma_f32 v127, v127, s89, -v145\n" \
  "v_exp_f32 v127, v127\n" \
  "s_nop 0\n" \
  "v_cvt_pk_bf16_f32 v128, v116, v117\n" \
  "v_cvt_pk_bf16_f32 v129, v118, v119\n" \
  "v_cvt_pk_bf16_f32 v132, v120, v121\n" \
  "v_cvt_pk_bf16_f32 v133, v122, v123\n" \
  "v_cvt_pk_bf16_f32 v134, v124, v125\n" \
  "v_cvt_pk_bf16_f32 v135, v126, v127\n" \
  "s_waitcnt vmcnt(22)\n" \
  "buffer_store_dword v16, v15, s[76:79], 0 offen sc1\n" \
  "v_mfma_f32_16x16x32_bf16 v[136:139], v[24:27], v[128:131], 0\n" \
  "v_mfma_f32_16x16x32_bf16 v[140:143], v[24:27], v[132:135], 0\n" \
  "v_add_u32 v16, 1, v16\n" \
  "v_mfma_f32_16x16x32_bf16 v[52:55], a[96:99], v[128:131], 0\n" \
  "v_mfma_f32_16x16x32_bf16 v[84:87], a[96:99], v[132:135], 0\n" \
  "buffer_load_dwordx4 a[96:99], v7, s[72:75], s85 offen offset:0 sc1\n" \
  "v_mfma_f32_16x16x32_bf16 v[56:59], a[100:103], v[128:131], 0\n" \
  "v_mfma_f32_16x16x32_bf16 v[88:91], a[100:103], v[132:135], 0\n" \
  "buffer_load_dwordx4 a[100:103], v7, s[72:75], s85 offen offset:1024 sc1\n" \
  "v_mfma_f32_16x16x32_bf16 v[60:63], a[104:107], v[128:131], 0\n" \
  "v_mfma_f32_16x16x32_bf16 v[92:95], a[104:107], v[132:135], 0\n" \
  "buffer_load_dwordx4 a[104:107], v7, s[72:75], s85 offen offset:2048 sc1\n" \
  "v_mfma_f32_16x16x32_bf16 v[64:67], a[108:111], v[128:131], 0\n" \
  "v_mfma_f32_16x16x32_bf16 v[96:99], a[108:111], v[132:135], 0\n" \
  "buffer_load_dwordx4 a[108:111], v7, s[72:75], s85 offen offset:3072 sc1\n" \
  "v_mfma_f32_16x16x32_bf16 v[68:71], a[112:115], v[128:131], 0\n" \
  "v_mfma_f32_16x16x32_bf16 v[100:103], a[112:115], v[132:135], 0\n" \
  "buffer_load_dwordx4 a[112:115], v8, s[72:75], s85 offen offset:0 sc1\n" \
  "v_mfma_f32_16x16x32_bf16 v[72:75], a[116:119], v[128:131], 0\n" \
  "v_mfma_f32_16x16x32_bf16 v[104:107], a[116:119], v[132:135], 0\n" \
  "buffer_load_dwordx4 a[116:119], v8, s[72:75], s85 offen offset:1024 sc1\n" \
  "v_mfma_f32_16x16x32_bf16 v[76:79], a[120:123], v[128:131], 0\n" \
  "v_mfma_f32_16x16x32_bf16 v[108:111], a[120:123], v[132:135], 0\n" \
  "buffer_load_dwordx4 a[120:123], v8, s[72:75], s85 offen offset:2048 sc1\n" \
  "v_mfma_f32_16x16x32_bf16 v[80:83], a[124:127], v[128:131], 0\n" \
  "v_mfma_f32_16x16x32_bf16 v[112:115], a[124:127], v[132:135], 0\n" \
  "buffer_load_dwordx4 a[124:127], v8, s[72:75], s85 offen offset:3072 sc1\n" \
  "buffer_load_dword v14, v13, s[76:79], 0 offen sc1\n" \
  "s_cmp_eq_u32 s90, 0\n" \
  "s_cbranch_scc1 LW1_%=\n" \
  "s_waitcnt vmcnt(26)\n" \
  "LM1_%=:\n" \
  "ds_write_b128 v11, v[44:47]\n" \
  "ds_write_b128 v11, v[48:51] offset:1024\n" \
  "v_mfma_f32_16x16x32_bf16 v[148:151], v[28:31], a[32:35], 0\n" \
  "v_max3_u32 v5, v28, v29, v30\n" \
  "v_max3_u32 v5, v5, v31, v32\n" \
  "v_mfma_f32_16x16x32_bf16 v[152:155], v[28:31], a[48:51], 0\n" \
  "v_max3_u32 v5, v5, v33, v34\n" \
  "v_max3_u32 v5, v5, v35, v36\n" \
  "v_mfma_f32_16x16x32_bf16 v[148:151], v[32:35], a[36:39], v[148:151]\n" \
  "v_max3_u32 v5, v5, v37, v38\n" \
  "v_max3_u32 v5, v5, v39, v40\n" \
  "v_mfma_f32_16x16x32_bf16 v[152:155], v[32:35], a[52:55], v[152:155]\n" \
  "v_max3_u32 v5, v5, v41, v42\n" \
  "v_max3_u32 v5, v5, v43, v44\n" \
  "v_mfma_f32_16x16x32_bf16 v[148:151], v[36:39], a[40:43], v[148:151]\n" \
  "v_max3_u32 v5, v5, v45, v46\n" \
  "v_max3_u32 v5, v5, v47, v48\n" \
  "v_mfma_f32_16x16x32_bf16 v[152:155], v[36:39], a[56:59], v[152:155]\n" \
  "v_max3_u32 v5, v5, v49, v50\n" \
  "v_max_u32 v5, v5, v51\n" \
  "v_mfma_f32_16x16x32_bf16 v[148:151], v[40:43], a[44:47], v[148:151]\n" \
  "v_cmp_eq_u32 vcc, -1, v5\n" \
  "v_mfma_f32_16x16x32_bf16 v[152:155], v[40:43], a[60:63], v[152:155]\n" \
  "ds_read_b64_tr_b16 a[128:129], v12 offset:0\n" \
  "ds_read_b64_tr_b16 a[130:131], v12 offset:256\n" \
  "ds_read_b64_tr_b16 a[132:133], v12 offset:512\n" \
  "ds_read_b64_tr_b16 a[134:135], v12 offset:768\n" \
  "ds_read_b64_tr_b16 a[136:137], v12 offset:1024\n" \
  "ds_read_b64_tr_b16 a[138:139], v12 offset:1280\n" \
  "ds_read_b64_tr_b16 a[140:141], v12 offset:1536\n" \
  "ds_read_b64_tr_b16 a[142:143], v12 offset:1792\n" \
  "s_cbranch_vccnz LR1_%=\n" \
  "v_max_f32 v146, v150, v151\n" \
  "v_max_f32 v147, v154, v155\n" \
  "v_mov_b32 v5, v146\n" \
  "v_mov_b32 v6, v147\n" \
  "s_nop 0\n" \
  "v_permlane16_swap_b32 v146, v5\n" \
  "v_permlane16_swap_b32 v147, v6\n" \
  "v_max_f32 v146, v146, v5\n" \
  "v_max_f32 v147, v147, v6\n" \
  "v_mov_b32 v5, v146\n" \
  "v_mov_b32 v6, v147\n" \
  "s_nop 0\n" \
  "v_permlane32_swap_b32 v146, v5\n" \
  "v_permlane32_swap_b32 v147, v6\n" \
  "v_max_f32 v146, v146, v5\n" \
  "v_max_f32 v147, v147, v6\n" \
  "v_mul_f32 v146, s89, v146\n" \
  "v_mul_f32 v147, s89, v147\n" \
  "v_max_f32 v146, v146, v144\n" \
  "v_max_f32 v147, v147, v145\n" \
  "v_sub_f32 v160, v144, v146\n" \
  "v_sub_f32 v162, v145, v147\n" \
  "v_fma_f32 v150, v150, s89, -v146\n" \
  "v_fma_f32 v151, v151, s89, -v146\n" \
  "v_fma_f32 v154, v154, s89, -v147\n" \
  "v_fma_f32 v155, v155, s89, -v147\n" \
  "v_exp_f32 v150, v150\n" \
  "v_exp_f32 v151, v151\n" \
  "v_exp_f32 v154, v154\n" \
  "v_exp_f32 v155, v155\n" \
  "v_exp_f32 v160, v160\n" \
  "v_exp_f32 v162, v162\n" \
  "v_add_f32 v146, v150, v151\n" \
  "v_add_f32 v147, v154, v155\n" \
  "v_mov_b32 v5, v146\n" \
  "v_mov_b32 v6, v147\n" \
  "s_nop 0\n" \
  "v_permlane16_swap_b32 v146, v5\n" \
  "v_permlane16_swap_b32 v147, v6\n" \
  "v_add_f32 v146, v146, v5\n" \
  "v_add_f32 v147, v147, v6\n" \
  "v_mov_b32 v5, v146\n" \
  "v_mov_b32 v6, v147\n" \
  "s_nop 0\n" \
  "v_permlane32_swap_b32 v146, v5\n" \
  "v_permlane32_swap_b32 v147, v6\n" \
  "v_add_f32 v146, v146, v5\n" \
  "v_add_f32 v147, v147, v6\n" \
  "v_fma_f32 v146, v160, v136, v146\n" \
  "v_fma_f32 v147, v162, v140, v147\n" \
  "v_rcp_f32 v146, v146\n" \
  "v_rcp_f32 v147, v147\n" \
  "v_mul_f32 v160, v160, v146\n" \
  "v_mul_f32 v162, v162, v147\n" \
  "v_mul_f32 v150, v150, v146\n" \
  "v_mul_f32 v151, v151, v146\n" \
  "v_mul_f32 v154, v154, v147\n" \
  "v_mul_f32 v155, v155, v147\n" \
  "v_cvt_pk_bf16_f32 v157, v150, v151\n" \
  "v_cvt_pk_bf16_f32 v159, v154, v155\n" \
  "v_pk_mul_f32 v[52:53], v[52:53], v[160:161] op_sel_hi:[1,0]\n" \
  "v_pk_mul_f32 v[54:55], v[54:55], v[160:161] op_sel_hi:[1,0]\n" \
  "v_pk_mul_f32 v[56:57], v[56:57], v[160:161] op_sel_hi:[1,0]\n" \
  "v_pk_mul_f32 v[58:59], v[58:59], v[160:161] op_sel_hi:[1,0]\n" \
  "v_pk_mul_f32 v[60:61], v[60:61], v[160:161] op_sel_hi:[1,0]\n" \
  "v_pk_mul_f32 v[62:63], v[62:63], v[160:161] op_sel_hi:[1,0]\n" \
  "v_pk_mul_f32 v[64:65], v[64:65], v[160:161] op_sel_hi:[1,0]\n" \
  "v_pk_mul_f32 v[66:67], v[66:67], v[160:161] op_sel_hi:[1,0]\n" \
  "v_pk_mul_f32 v[68:69], v[68:69], v[160:161] op_sel_hi:[1,0]\n" \
  "v_pk_mul_f32 v[70:71], v[70:71], v[160:161] op_sel_hi:[1,0]\n" \
  "v_pk_mul_f32 v[72:73], v[72:73], v[160:161] op_sel_hi:[1,0]\n" \
  "v_pk_mul_f32 v[74:75], v[74:75], v[160:161] op_sel_hi:[1,0]\n" \
  "v_pk_mul_f32 v[76:77], v[76:77], v[160:161] op_sel_hi:[1,0]\n" \
  "v_pk_mul_f32 v[78:79], v[78:79], v[160:161] op_sel_hi:[1,0]\n" \
  "v_pk_mul_f32 v[80:81], v[80:81], v[160:161] op_sel_hi:[1,0]\n" \
  "v_pk_mul_f32 v[82:83], v[82:83], v[160:161] op_sel_hi:[1,0]\n" \
  "v_pk_mul_f32 v[84:85], v[84:85], v[162:163] op_sel_hi:[1,0]\n" \
  "v_pk_mul_f32 v[86:87], v[86:87], v[162:163] op_sel_hi:[1,0]\n" \
  "v_pk_mul_f32 v[88:89], v[88:89], v[162:163] op_sel_hi:[1,0]\n" \
  "v_pk_mul_f32 v[90:91], v[90:91], v[162:163] op_sel_hi:[1,0]\n" \
  "v_pk_mul_f32 v[92:93], v[92:93], v[162:163] op_sel_hi:[1,0]\n" \
  "v_pk_mul_f32 v[94:95], v[94:95], v[162:163] op_sel_hi:[1,0]\n" \
  "v_pk_mul_f32 v[96:97], v[96:97], v[162:163] op_sel_hi:[1,0]\n" \
  "v_pk_mul_f32 v[98:99], v[98:99], v[162:163] op_sel_hi:[1,0]\n" \
  "v_pk_mul_f32 v[100:101], v[100:101], v[162:163] op_sel_hi:[1,0]\n" \
  "v_pk_mul_f32 v[102:103], v[102:103], v[162:163] op_sel_hi:[1,0]\n" \
  "v_pk_mul_f32 v[104:105], v[104:105], v[162:163] op_sel_hi:[1,0]\n" \
  "v_pk_mul_f32 v[106:107], v[106:107], v[162:163] op_sel_hi:[1,0]\n" \
  "v_pk_mul_f32 v[108:109], v[108:109], v[162:163] op_sel_hi:[1,0]\n" \
  "v_pk_mul_f32 v[110:111], v[110:111], v[162:163] op_sel_hi:[1,0]\n" \
  "v_pk_mul_f32 v[112:113], v[112:113], v[162:163] op_sel_hi:[1,0]\n" \
  "v_pk_mul_f32 v[114:115], v[114:115], v[162:163] op_sel_hi:[1,0]\n" \
  "s_waitcnt lgkmcnt(0)\n" \
  "v_mfma_f32_16x16x16_bf16 v[52:55], a[128:129], v[156:157], v[52:55]\n" \
  "v_mfma_f32_16x16x16_bf16 v[56:59], a[130:131], v[156:157], v[56:59]\n" \
  "v_mfma_f32_16x16x16_bf16 v[60:63], a[132:133], v[156:157], v[60:63]\n" \
  "v_mfma_f32_16x16x16_bf16 v[64:67], a[134:135], v[156:157], v[64:67]\n" \
  "v_mfma_f32_16x16x16_bf16 v[68:71], a[136:137], v[156:157], v[68:71]\n" \
  "v_cvt_pk_bf16_f32 v164, v52, v53\n" \
  "v_cvt_pk_bf16_f32 v165, v54, v55\n" \
  "v_mfma_f32_16x16x16_bf16 v[72:75], a[138:139], v[156:157], v[72:75]\n" \
  "v_cvt_pk_bf16_f32 v166, v56, v57\n" \
  "v_cvt_pk_bf16_f32 v167, v58, v59\n" \
  "buffer_store_dwordx4 v[164:167], v7, s[72:75], s86 offen offset:0 sc1\n" \
  "v_mfma_f32_16x16x16_bf16 v[76:79], a[140:141], v[156:157], v[76:79]\n" \
  "v_cvt_pk_bf16_f32 v168, v60, v61\n" \
  "v_cvt_pk_bf16_f32 v169, v62, v63\n" \
  "v_mfma_f32_16x16x16_bf16 v[80:83], a[142:143], v[156:157], v[80:83]\n" \
  "v_cvt_pk_bf16_f32 v170, v64, v65\n" \
  "v_cvt_pk_bf16_f32 v171, v66, v67\n" \
  "buffer_store_dwordx4 v[168:171], v7, s[72:75], s86 offen offset:1024 sc1\n" \
  "v_mfma_f32_16x16x16_bf16 v[84:87], a[128:129], v[158:159], v[84:87]\n" \
  "v_cvt_pk_bf16_f32 v172, v68, v69\n" \
  "v_cvt_pk_bf16_f32 v173, v70, v71\n" \
  "v_mfma_f32_16x16x16_bf16 v[88:91], a[130:131], v[158:159], v[88:91]\n" \
  "v_cvt_pk_bf16_f32 v174, v72, v73\n" \
  "v_cvt_pk_bf16_f32 v175, v74, v75\n" \
  "buffer_store_dwordx4 v[172:175], v7, s[72:75], s86 offen offset:2048 sc1\n" \
  "v_mfma_f32_16x16x16_bf16 v[92:95], a[132:133], v[158:159], v[92:95]\n" \
  "v_cvt_pk_bf16_f32 v176, v76, v77\n" \
  "v_cvt_pk_bf16_f32 v177, v78, v79\n" \
  "v_mfma_f32_16x16x16_bf16 v[96:99], a[134:135], v[158:159], v[96:99]\n" \
  "v_cvt_pk_bf16_f32 v178, v80, v81\n" \
  "v_cvt_pk_bf16_f32 v179, v82, v83\n" \
  "buffer_store_dwordx4 v[176:179], v7, s[72:75], s86 offen offset:3072 sc1\n" \
  "v_mfma_f32_16x16x16_bf16 v[100:103], a[136:137], v[158:159], v[100:103]\n" \
  "v_cvt_pk_bf16_f32 v180, v84, v85\n" \
  "v_cvt_pk_bf16_f32 v181, v86, v87\n" \
  "v_mfma_f32_16x16x16_bf16 v[104:107], a[138:139], v[158:159], v[104:107]\n" \
  "v_cvt_pk_bf16_f32 v182, v88, v89\n" \
  "v_cvt_pk_bf16_f32 v183, v90, v91\n" \
  "buffer_store_dwordx4 v[180:183], v17, s[72:75], s86 offen offset:0 sc1\n" \
  "v_mfma_f32_16x16x16_bf16 v[108:111], a[140:141], v[158:159], v[108:111]\n" \
  "v_cvt_pk_bf16_f32 v184, v92, v93\n" \
  "v_cvt_pk_bf16_f32 v185, v94, v95\n" \
  "v_mfma_f32_16x16x16_bf16 v[112:115], a[142:143], v[158:159], v[112:115]\n" \
  "v_cvt_pk_bf16_f32 v186, v96, v97\n" \
  "v_cvt_pk_bf16_f32 v187, v98, v99\n" \
  "buffer_store_dwordx4 v[184:187], v17, s[72:75], s86 offen offset:1024 sc1\n" \
  "v_cvt_pk_bf16_f32 v188, v100, v101\n" \
  "v_cvt_pk_bf16_f32 v189, v102, v103\n" \
  "v_cvt_pk_bf16_f32 v190, v104, v105\n" \
  "v_cvt_pk_bf16_f32 v191, v106, v107\n" \
  "buffer_store_dwordx4 v[188:191], v17, s[72:75], s86 offen offset:2048 sc1\n" \
  "s_nop 1\n" \
  "v_cvt_pk_bf16_f32 v192, v108, v109\n" \
  "v_cvt_pk_bf16_f32 v193, v110, v111\n" \
  "v_cvt_pk_bf16_f32 v194, v112, v113\n" \
  "v_cvt_pk_bf16_f32 v195, v114, v115\n" \
  "buffer_store_dwordx4 v[192:195], v17, s[72:75], s86 offen offset:3072 sc1\n" \
  "s_add_u32 s82, s82, 1\n" \
  "s_cmp_eq_u32 s82, 64\n" \
  "s_cbranch_scc1 LZ_%=\n" \
  "s_add_u32 s80, s82, 0\n" \
  "s_lshl_b32 s80, s80, 5\n" \
  "s_add_u32 s80, s80, %3\n" \
  "s_lshl_b32 s87, s80, 14\n" \
  "s_add_u32 s87, s87, " STR(EXP_B) "<<21\n" \
  "s_lshr_b32 s80, s87, 21\n" \
  "s_lshr_b32 s80, %4, s80\n" \
  "s_and_b32 s80, s80, 1\n" \
  "s_lshl_b32 s80, s80, 13\n" \
  "s_or_b32 s87, s87, s80\n" \
  "s_waitcnt vmcnt(8)\n" \
  "v_readlane_b32 s91, v14, 0\n" \
  "v_readlane_b32 s92, v14, 1\n" \
  "v_readlane_b32 s80, v14, 2\n" \
  "s_min_u32 s92, s92, s80\n" \
  "v_readlane_b32 s80, v14, 3\n" \
  "s_min_u32 s92, s92, s80\n" \
  "v_readlane_b32 s80, v14, 4\n" \
  "s_min_u32 s92, s92, s80\n" \
  "s_cmp_gt_u32 s91, s82\n" \
  "s_cselect_b32 s90, 1, 0\n" \
  "s_cselect_b32 s80, 0, 0x80000000\n" \
  "v_or_b32 v9, s80, v7\n" \
  "v_or_b32 v10, s80, v8\n" \
  "buffer_load_dwordx4 v[28:31], v9, s[68:71], s87 offen offset:0 sc1\n" \
  "buffer_load_dwordx4 v[32:35], v9, s[68:71], s87 offen offset:1024 sc1\n" \
  "buffer_load_dwordx4 v[36:39], v9, s[68:71], s87 offen offset:2048 sc1\n" \
  "buffer_load_dwordx4 v[40:43], v9, s[68:71], s87 offen offset:3072 sc1\n" \
  "buffer_load_dwordx4 v[44:47], v10, s[68:71], s87 offen offset:0 sc1\n" \
  "buffer_load_dwordx4 v[48:51], v10, s[68:71], s87 offen offset:1024 sc1\n" \
  "s_branch LA_%=\n" \
  "LG0_%=:\n" \
  "buffer_load_dword v14, v13, s[76:79], 0 offen sc1\n" \
  "s_waitcnt vmcnt(0)\n" \
  "v_readlane_b32 s91, v14, 0\n" \
  "v_readlane_b32 s92, v14, 1\n" \
  "v_readlane_b32 s80, v14, 2\n" \
  "s_min_u32 s92, s92, s80\n" \
  "v_readlane_b32 s80, v14, 3\n" \
  "s_min_u32 s92, s92, s80\n" \
  "v_readlane_b32 s80, v14, 4\n" \
  "s_min_u32 s92, s92, s80\n" \
  "s_cmp_ge_u32 s92, s81\n" \
  "s_cbranch_scc1 LH0_%=\n" \
  "s_sleep 4\n" \
  "s_add_u32 s88, s88, 1\n" \
  "s_cmp_lt_u32 s88, " STR(SPIN_CAP) "\n" \
  "s_cbranch_scc1 LG0_%=\n" \
  "v_mov_b32 v5, " STR(CW_DEAD) "*4\n" \
  "v_mov_b32 v6, 1\n" \
  "buffer_store_dword v6, v5, s[76:79], 0 offen sc1\n" \
  "s_branch LH0_%=\n" \
  "LW0_%=:\n" \
  "s_add_u32 s81, s82, 1\n" \
  "LQ0_%=:\n" \
  "buffer_load_dword v14, v13, s[76:79], 0 offen sc1\n" \
  "s_waitcnt vmcnt(0)\n" \
  "v_readlane_b32 s91, v14, 0\n" \
  "v_readlane_b32 s92, v14, 1\n" \
  "v_readlane_b32 s80, v14, 2\n" \
  "s_min_u32 s92, s92, s80\n" \
  "v_readlane_b32 s80, v14, 3\n" \
  "s_min_u32 s92, s92, s80\n" \
  "v_readlane_b32 s80, v14, 4\n" \
  "s_min_u32 s92, s92, s80\n" \
  "s_cmp_ge_u32 s91, s81\n" \
  "s_cbranch_scc1 LR0_%=\n" \
  "s_sleep 2\n" \
  "s_add_u32 s88, s88, 1\n" \
  "s_cmp_lt_u32 s88, " STR(SPIN_CAP) "\n" \
  "s_cbranch_scc1 LQ0_%=\n" \
  "v_mov_b32 v5, " STR(CW_DEAD) "*4\n" \
  "v_mov_b32 v6, 1\n" \
  "buffer_store_dword v6, v5, s[76:79], 0 offen sc1\n" \
  "s_branch LR0_%=\n" \
  "LR0_%=:\n" \
  "buffer_load_dwordx4 v[28:31], v7, s[68:71], s87 offen offset:0 sc1\n" \
  "buffer_load_dwordx4 v[32:35], v7, s[68:71], s87 offen offset:1024 sc1\n" \
  "buffer_load_dwordx4 v[36:39], v7, s[68:71], s87 offen offset:2048 sc1\n" \
  "buffer_load_dwordx4 v[40:43], v7, s[68:71], s87 offen offset:3072 sc1\n" \
  "buffer_load_dwordx4 v[44:47], v8, s[68:71], s87 offen offset:0 sc1\n" \
  "buffer_load_dwordx4 v[48:51], v8, s[68:71], s87 offen offset:1024 sc1\n" \
  "s_waitcnt vmcnt(0)\n" \
  "v_max3_u32 v5, v28, v29, v30\n" \
  "v_max3_u32 v5, v5, v31, v32\n" \
  "v_max3_u32 v5, v5, v33, v34\n" \
  "v_max3_u32 v5, v5, v35, v36\n" \
  "v_max3_u32 v5, v5, v37, v38\n" \
  "v_max3_u32 v5, v5, v39, v40\n" \
  "v_max3_u32 v5, v5, v41, v42\n" \
  "v_max3_u32 v5, v5, v43, v44\n" \
  "v_max3_u32 v5, v5, v45, v46\n" \
  "v_max3_u32 v5, v5, v47, v48\n" \
  "v_max3_u32 v5, v5, v49, v50\n" \
  "v_max_u32 v5, v5, v51\n" \
  "v_cmp_eq_u32 vcc, -1, v5\n" \
  "s_cbranch_vccz LU0_%=\n" \
  "s_sleep 1\n" \
  "s_add_u32 s88, s88, 1\n" \
  "s_cmp_lt_u32 s88, " STR(SPIN_CAP) "\n" \
  "s_cbranch_scc1 LR0_%=\n" \
  "v_mov_b32 v5, " STR(CW_DEAD) "*4\n" \
  "v_mov_b32 v6, 1\n" \
  "buffer_store_dword v6, v5, s[76:79], 0 offen sc1\n" \
  "s_branch LV0_%=\n" \
  "LV0_%=:\n" \
  "v_mov_b64 v[28:29], 0\n" \
  "v_mov_b64 v[30:31], 0\n" \
  "v_mov_b64 v[32:33], 0\n" \
  "v_mov_b64 v[34:35], 0\n" \
  "v_mov_b64 v[36:37], 0\n" \
  "v_mov_b64 v[38:39], 0\n" \
  "v_mov_b64 v[40:41], 0\n" \
  "v_mov_b64 v[42:43], 0\n" \
  "v_mov_b64 v[44:45], 0\n" \
  "v_mov_b64 v[46:47], 0\n" \
  "v_mov_b64 v[48:49], 0\n" \
  "v_mov_b64 v[50:51], 0\n" \
  "LU0_%=:\n" \
  "s_nop 1\n" \
  "s_branch LM0_%=\n" \
  "LG1_%=:\n" \
  "buffer_load_dword v14, v13, s[76:79], 0 offen sc1\n" \
  "s_waitcnt vmcnt(0)\n" \
  "v_readlane_b32 s91, v14, 0\n" \
  "v_readlane_b32 s92, v14, 1\n" \
  "v_readlane_b32 s80, v14, 2\n" \
  "s_min_u32 s92, s92, s80\n" \
  "v_readlane_b32 s80, v14, 3\n" \
  "s_min_u32 s92, s92, s80\n" \
  "v_readlane_b32 s80, v14, 4\n" \
  "s_min_u32 s92, s92, s80\n" \
  "s_cmp_ge_u32 s92, s81\n" \
  "s_cbranch_scc1 LH1_%=\n" \
  "s_sleep 4\n" \
  "s_add_u32 s88, s88, 1\n" \
  "s_cmp_lt_u32 s88, " STR(SPIN_CAP) "\n" \
  "s_cbranch_scc1 LG1_%=\n" \
  "v_mov_b32 v5, " STR(CW_DEAD) "*4\n" \
  "v_mov_b32 v6, 1\n" \
  "buffer_store_dword v6, v5, s[76:79], 0 offen sc1\n" \
  "s_branch LH1_%=\n" \
  "LW1_%=:\n" \
  "s_add_u32 s81, s82, 1\n" \
  "LQ1_%=:\n" \
  "buffer_load_dword v14, v13, s[76:79], 0 offen sc1\n" \
  "s_waitcnt vmcnt(0)\n" \
  "v_readlane_b32 s91, v14, 0\n" \
  "v_readlane_b32 s92, v14, 1\n" \
  "v_readlane_b32 s80, v14, 2\n" \
  "s_min_u32 s92, s92, s80\n" \
  "v_readlane_b32 s80, v14, 3\n" \
  "s_min_u32 s92, s92, s80\n" \
  "v_readlane_b32 s80, v14, 4\n" \
  "s_min_u32 s92, s92, s80\n" \
  "s_cmp_ge_u32 s91, s81\n" \
  "s_cbranch_scc1 LR1_%=\n" \
  "s_sleep 2\n" \
  "s_add_u32 s88, s88, 1\n" \
  "s_cmp_lt_u32 s88, " STR(SPIN_CAP) "\n" \
  "s_cbranch_scc1 LQ1_%=\n" \
  "v_mov_b32 v5, " STR(CW_DEAD) "*4\n" \
  "v_mov_b32 v6, 1\n" \
  "buffer_store_dword v6, v5, s[76:79], 0 offen sc1\n" \
  "s_branch LR1_%=\n" \
  "LR1_%=:\n" \
  "buffer_load_dwordx4 v[28:31], v7, s[68:71], s87 offen offset:0 sc1\n" \
  "buffer_load_dwordx4 v[32:35], v7, s[68:71], s87 offen offset:1024 sc1\n" \
  "buffer_load_dwordx4 v[36:39], v7, s[68:71], s87 offen offset:2048 sc1\n" \
  "buffer_load_dwordx4 v[40:43], v7, s[68:71], s87 offen offset:3072 sc1\n" \
  "buffer_load_dwordx4 v[44:47], v8, s[68:71], s87 offen offset:0 sc1\n" \
  "buffer_load_dwordx4 v[48:51], v8, s[68:71], s87 offen offset:1024 sc1\n" \
  "s_waitcnt vmcnt(0)\n" \
  "v_max3_u32 v5, v28, v29, v30\n" \
  "v_max3_u32 v5, v5, v31, v32\n" \
  "v_max3_u32 v5, v5, v33, v34\n" \
  "v_max3_u32 v5, v5, v35, v36\n" \
  "v_max3_u32 v5, v5, v37, v38\n" \
  "v_max3_u32 v5, v5, v39, v40\n" \
  "v_max3_u32 v5, v5, v41, v42\n" \
  "v_max3_u32 v5, v5, v43, v44\n" \
  "v_max3_u32 v5, v5, v45, v46\n" \
  "v_max3_u32 v5, v5, v47, v48\n" \
  "v_max3_u32 v5, v5, v49, v50\n" \
  "v_max_u32 v5, v5, v51\n" \
  "v_cmp_eq_u32 vcc, -1, v5\n" \
  "s_cbranch_vccz LU1_%=\n" \
  "s_sleep 1\n" \
  "s_add_u32 s88, s88, 1\n" \
  "s_cmp_lt_u32 s88, " STR(SPIN_CAP) "\n" \
  "s_cbranch_scc1 LR1_%=\n" \
  "v_mov_b32 v5, " STR(CW_DEAD) "*4\n" \
  "v_mov_b32 v6, 1\n" \
  "buffer_store_dword v6, v5, s[76:79], 0 offen sc1\n" \
  "s_branch LV1_%=\n" \
  "LV1_%=:\n" \
  "v_mov_b64 v[28:29], 0\n" \
  "v_mov_b64 v[30:31], 0\n" \
  "v_mov_b64 v[32:33], 0\n" \
  "v_mov_b64 v[34:35], 0\n" \
  "v_mov_b64 v[36:37], 0\n" \
  "v_mov_b64 v[38:39], 0\n" \
  "v_mov_b64 v[40:41], 0\n" \
  "v_mov_b64 v[42:43], 0\n" \
  "v_mov_b64 v[44:45], 0\n" \
  "v_mov_b64 v[46:47], 0\n" \
  "v_mov_b64 v[48:49], 0\n" \
  "v_mov_b64 v[50:51], 0\n" \
  "LU1_%=:\n" \
  "s_nop 1\n" \
  "s_branch LM1_%=\n" \
  "LZ_%=:\n" \
  "s_waitcnt vmcnt(0)\n" \
  "buffer_store_dword v16, v15, s[76:79], 0 offen sc1\n" \
  "s_waitcnt vmcnt(0)\n" \
  ""

#define CONV_ASM \
  "s_waitcnt lgkmcnt(0)\n" \
  "ds_read_b64 a[0:1], %2 offset:32\n" \
  "ds_read_b64 a[2:3], %2 offset:40\n" \
  "ds_read_b128 a[4:7], %3 offset:5040\n" \
  "ds_read_b128 a[8:11], %3 offset:2480\n" \
  "ds_read_b64 a[12:13], %2 offset:2080\n" \
  "ds_read_b64 a[14:15], %2 offset:2088\n" \
  "ds_read_b64 a[16:17], %2 offset:0\n" \
  "ds_read_b64 a[18:19], %2 offset:8\n" \
  "ds_read_b128 a[20:23], %3 offset:5072\n" \
  "ds_read_b128 a[24:27], %3 offset:2512\n" \
  "ds_read_b64 a[28:29], %2 offset:2048\n" \
  "ds_read_b64 a[30:31], %2 offset:2056\n" \
  "s_waitcnt lgkmcnt(6)\n" \
  "v_mfma_f32_32x32x16_bf16 %1, a[0:3], a[4:7], 0\n" \
  "ds_read_b64 a[32:33], %2 offset:96\n" \
  "ds_read_b64 a[34:35], %2 offset:104\n" \
  "v_mfma_f32_32x32x16_bf16 %0, a[0:3], a[8:11], 0\n" \
  "ds_read_b128 a[36:39], %3 offset:4960\n" \
  "ds_read_b128 a[40:43], %3 offset:2400\n" \
  "v_mfma_f32_32x32x16_bf16 %1, a[12:15], a[8:11], %1\n" \
  "ds_read_b64 a[44:45], %2 offset:2144\n" \
  "ds_read_b64 a[46:47], %2 offset:2152\n" \
  "s_waitcnt lgkmcnt(6)\n" \
  "v_mfma_f32_32x32x16_bf16 %1, a[16:19], a[20:23], %1\n" \
  "ds_read_b64 a[0:1], %2 offset:64\n" \
  "ds_read_b64 a[2:3], %2 offset:72\n" \
  "v_mfma_f32_32x32x16_bf16 %0, a[16:19], a[24:27], %0\n" \
  "ds_read_b128 a[4:7], %3 offset:4992\n" \
  "ds_read_b128 a[8:11], %3 offset:2432\n" \
  "v_mfma_f32_32x32x16_bf16 %1, a[28:31], a[24:27], %1\n" \
  "ds_read_b64 a[12:13], %2 offset:2112\n" \
  "ds_read_b64 a[14:15], %2 offset:2120\n" \
  "s_waitcnt lgkmcnt(6)\n" \
  "v_mfma_f32_32x32x16_bf16 %1, a[32:35], a[36:39], %1\n" \
  "ds_read_b64 a[16:17], %2 offset:160\n" \
  "ds_read_b64 a[18:19], %2 offset:168\n" \
  "v_mfma_f32_32x32x16_bf16 %0, a[32:35], a[40:43], %0\n" \
  "ds_read_b128 a[20:23], %3 offset:4880\n" \
  "ds_read_b128 a[24:27], %3 offset:2320\n" \
  "v_mfma_f32_32x32x16_bf16 %1, a[44:47], a[40:43], %1\n" \
  "ds_read_b64 a[28:29], %2 offset:2208\n" \
  "ds_read_b64 a[30:31], %2 offset:2216\n" \
  "s_waitcnt lgkmcnt(6)\n" \
  "v_mfma_f32_32x32x16_bf16 %1, a[0:3], a[4:7], %1\n" \
  "ds_read_b64 a[32:33], %2 offset:128\n" \
  "ds_read_b64 a[34:35], %2 offset:136\n" \
  "v_mfma_f32_32x32x16_bf16 %0, a[0:3], a[8:11], %0\n" \
  "ds_read_b128 a[36:39], %3 offset:4912\n" \
  "ds_read_b128 a[40:43], %3 offset:2352\n" \
  "v_mfma_f32_32x32x16_bf16 %1, a[12:15], a[8:11], %1\n" \
  "ds_read_b64 a[44:45], %2 offset:2176\n" \
  "ds_read_b64 a[46:47], %2 offset:2184\n" \
  "s_waitcnt lgkmcnt(6)\n" \
  "v_mfma_f32_32x32x16_bf16 %1, a[16:19], a[20:23], %1\n" \
  "ds_read_b64 a[0:1], %2 offset:224\n" \
  "ds_read_b64 a[2:3], %2 offset:232\n" \
  "v_mfma_f32_32x32x16_bf16 %0, a[16:19], a[24:27], %0\n" \
  "ds_read_b128 a[4:7], %3 offset:4800\n" \
  "ds_read_b128 a[8:11], %3 offset:2240\n" \
  "v_mfma_f32_32x32x16_bf16 %1, a[28:31], a[24:27], %1\n" \
  "ds_read_b64 a[12:13], %2 offset:2272\n" \
  "ds_read_b64 a[14:15], %2 offset:2280\n" \
  "s_waitcnt lgkmcnt(6)\n" \
  "v_mfma_f32_32x32x16_bf16 %1, a[32:35], a[36:39], %1\n" \
  "ds_read_b64 a[16:17], %2 offset:192\n" \
  "ds_read_b64 a[18:19], %2 offset:200\n" \
  "v_mfma_f32_32x32x16_bf16 %0, a[32:35], a[40:43], %0\n" \
  "ds_read_b128 a[20:23], %3 offset:4832\n" \
  "ds_read_b128 a[24:27], %3 offset:2272\n" \
  "v_mfma_f32_32x32x16_bf16 %1, a[44:47], a[40:43], %1\n" \
  "ds_read_b64 a[28:29], %2 offset:2240\n" \
  "ds_read_b64 a[30:31], %2 offset:2248\n" \
  "s_waitcnt lgkmcnt(6)\n" \
  "v_mfma_f32_32x32x16_bf16 %1, a[0:3], a[4:7], %1\n" \
  "ds_read_b64 a[32:33], %2 offset:288\n" \
  "ds_read_b64 a[34:35], %2 offset:296\n" \
  "v_mfma_f32_32x32x16_bf16 %0, a[0:3], a[8:11], %0\n" \
  "ds_read_b128 a[36:39], %3 offset:4720\n" \
  "ds_read_b128 a[40:43], %3 offset:2160\n" \
  "v_mfma_f32_32x32x16_bf16 %1, a[12:15], a[8:11], %1\n" \
  "ds_read_b64 a[44:45], %2 offset:2336\n" \
  "ds_read_b64 a[46:47], %2 offset:2344\n" \
  "s_waitcnt lgkmcnt(6)\n" \
  "v_mfma_f32_32x32x16_bf16 %1, a[16:19], a[20:23], %1\n" \
  "ds_read_b64 a[0:1], %2 offset:256\n" \
  "ds_read_b64 a[2:3], %2 offset:264\n" \
  "v_mfma_f32_32x32x16_bf16 %0, a[16:19], a[24:27], %0\n" \
  "ds_read_b128 a[4:7], %3 offset:4752\n" \
  "ds_read_b128 a[8:11], %3 offset:2192\n" \
  "v_mfma_f32_32x32x16_bf16 %1, a[28:31], a[24:27], %1\n" \
  "ds_read_b64 a[12:13], %2 offset:2304\n" \
  "ds_read_b64 a[14:15], %2 offset:2312\n" \
  "s_waitcnt lgkmcnt(6)\n" \
  "v_mfma_f32_32x32x16_bf16 %1, a[32:35], a[36:39], %1\n" \
  "ds_read_b64 a[16:17], %2 offset:352\n" \
  "ds_read_b64 a[18:19], %2 offset:360\n" \
  "v_mfma_f32_32x32x16_bf16 %0, a[32:35], a[40:43], %0\n" \
  "ds_read_b128 a[20:23], %3 offset:4640\n" \
  "ds_read_b128 a[24:27], %3 offset:2080\n" \
  "v_mfma_f32_32x32x16_bf16 %1, a[44:47], a[40:43], %1\n" \
  "ds_read_b64 a[28:29], %2 offset:2400\n" \
  "ds_read_b64 a[30:31], %2 offset:2408\n" \
  "s_waitcnt lgkmcnt(6)\n" \
  "v_mfma_f32_32x32x16_bf16 %1, a[0:3], a[4:7], %1\n" \
  "ds_read_b64 a[32:33], %2 offset:320\n" \
  "ds_read_b64 a[34:35], %2 offset:328\n" \
  "v_mfma_f32_32x32x16_bf16 %0, a[0:3], a[8:11], %0\n" \
  "ds_read_b128 a[36:39], %3 offset:4672\n" \
  "ds_read_b128 a[40:43], %3 offset:2112\n" \
  "v_mfma_f32_32x32x16_bf16 %1, a[12:15], a[8:11], %1\n" \
  "ds_read_b64 a[44:45], %2 offset:2368\n" \
  "ds_read_b64 a[46:47], %2 offset:2376\n" \
  "s_waitcnt lgkmcnt(6)\n" \
  "v_mfma_f32_32x32x16_bf16 %1, a[16:19], a[20:23], %1\n" \
  "ds_read_b64 a[0:1], %2 offset:416\n" \
  "ds_read_b64 a[2:3], %2 offset:424\n" \
  "v_mfma_f32_32x32x16_bf16 %0, a[16:19], a[24:27], %0\n" \
  "ds_read_b128 a[4:7], %3 offset:4560\n" \
  "ds_read_b128 a[8:11], %3 offset:2000\n" \
  "v_mfma_f32_32x32x16_bf16 %1, a[28:31], a[24:27], %1\n" \
  "ds_read_b64 a[12:13], %2 offset:2464\n" \
  "ds_read_b64 a[14:15], %2 offset:2472\n" \
  "s_waitcnt lgkmcnt(6)\n" \
  "v_mfma_f32_32x32x16_bf16 %1, a[32:35], a[36:39], %1\n" \
  "ds_read_b64 a[16:17], %2 offset:384\n" \
  "ds_read_b64 a[18:19], %2 offset:392\n" \
  "v_mfma_f32_32x32x16_bf16 %0, a[32:35], a[40:43], %0\n" \
  "ds_read_b128 a[20:23], %3 offset:4592\n" \
  "ds_read_b128 a[24:27], %3 offset:2032\n" \
  "v_mfma_f32_32x32x16_bf16 %1, a[44:47], a[40:43], %1\n" \
  "ds_read_b64 a[28:29], %2 offset:2432\n" \
  "ds_read_b64 a[30:31], %2 offset:2440\n" \
  "s_waitcnt lgkmcnt(6)\n" \
  "v_mfma_f32_32x32x16_bf16 %1, a[0:3], a[4:7], %1\n" \
  "ds_read_b64 a[32:33], %2 offset:480\n" \
  "ds_read_b64 a[34:35], %2 offset:488\n" \
  "v_mfma_f32_32x32x16_bf16 %0, a[0:3], a[8:11], %0\n" \
  "ds_read_b128 a[36:39], %3 offset:4480\n" \
  "ds_read_b128 a[40:43], %3 offset:1920\n" \
  "v_mfma_f32_32x32x16_bf16 %1, a[12:15], a[8:11], %1\n" \
  "ds_read_b64 a[44:45], %2 offset:2528\n" \
  "ds_read_b64 a[46:47], %2 offset:2536\n" \
  "s_waitcnt lgkmcnt(6)\n" \
  "v_mfma_f32_32x32x16_bf16 %1, a[16:19], a[20:23], %1\n" \
  "ds_read_b64 a[0:1], %2 offset:448\n" \
  "ds_read_b64 a[2:3], %2 offset:456\n" \
  "v_mfma_f32_32x32x16_bf16 %0, a[16:19], a[24:27], %0\n" \
  "ds_read_b128 a[4:7], %3 offset:4512\n" \
  "ds_read_b128 a[8:11], %3 offset:1952\n" \
  "v_mfma_f32_32x32x16_bf16 %1, a[28:31], a[24:27], %1\n" \
  "ds_read_b64 a[12:13], %2 offset:2496\n" \
  "ds_read_b64 a[14:15], %2 offset:2504\n" \
  "s_waitcnt lgkmcnt(6)\n" \
  "v_mfma_f32_32x32x16_bf16 %1, a[32:35], a[36:39], %1\n" \
  "ds_read_b64 a[16:17], %2 offset:544\n" \
  "ds_read_b64 a[18:19], %2 offset:552\n" \
  "v_mfma_f32_32x32x16_bf16 %0, a[32:35], a[40:43], %0\n" \
  "ds_read_b128 a[20:23], %3 offset:4400\n" \
  "ds_read_b128 a[24:27], %3 offset:1840\n" \
  "v_mfma_f32_32x32x16_bf16 %1, a[44:47], a[40:43], %1\n" \
  "ds_read_b64 a[28:29], %2 offset:2592\n" \
  "ds_read_b64 a[30:31], %2 offset:2600\n" \
  "s_waitcnt lgkmcnt(6)\n" \
  "v_mfma_f32_32x32x16_bf16 %1, a[0:3], a[4:7], %1\n" \
  "ds_read_b64 a[32:33], %2 offset:512\n" \
  "ds_read_b64 a[34:35], %2 offset:520\n" \
  "v_mfma_f32_32x32x16_bf16 %0, a[0:3], a[8:11], %0\n" \
  "ds_read_b128 a[36:39], %3 offset:4432\n" \
  "ds_read_b128 a[40:43], %3 offset:1872\n" \
  "v_mfma_f32_32x32x16_bf16 %1, a[12:15], a[8:11], %1\n" \
  "ds_read_b64 a[44:45], %2 offset:2560\n" \
  "ds_read_b64 a[46:47], %2 offset:2568\n" \
  "s_waitcnt lgkmcnt(6)\n" \
  "v_mfma_f32_32x32x16_bf16 %1, a[16:19], a[20:23], %1\n" \
  "ds_read_b64 a[0:1], %2 offset:608\n" \
  "ds_read_b64 a[2:3], %2 offset:616\n" \
  "v_mfma_f32_32x32x16_bf16 %0, a[16:19], a[24:27], %0\n" \
  "ds_read_b128 a[4:7], %3 offset:4320\n" \
  "ds_read_b128 a[8:11], %3 offset:1760\n" \
  "v_mfma_f32_32x32x16_bf16 %1, a[28:31], a[24:27], %1\n" \
  "ds_read_b64 a[12:13], %2 offset:2656\n" \
  "ds_read_b64 a[14:15], %2 offset:2664\n" \
  "s_waitcnt lgkmcnt(6)\n" \
  "v_mfma_f32_32x32x16_bf16 %1, a[32:35], a[36:39], %1\n" \
  "ds_read_b64 a[16:17], %2 offset:576\n" \
  "ds_read_b64 a[18:19], %2 offset:584\n" \
  "v_mfma_f32_32x32x16_bf16 %0, a[32:35], a[40:43], %0\n" \
  "ds_read_b128 a[20:23], %3 offset:4352\n" \
  "ds_read_b128 a[24:27], %3 offset:1792\n" \
  "v_mfma_f32_32x32x16_bf16 %1, a[44:47], a[40:43], %1\n" \
  "ds_read_b64 a[28:29], %2 offset:2624\n" \
  "ds_read_b64 a[30:31], %2 offset:2632\n" \
  "s_waitcnt lgkmcnt(6)\n" \
  "v_mfma_f32_32x32x16_bf16 %1, a[0:3], a[4:7], %1\n" \
  "ds_read_b64 a[32:33], %2 offset:672\n" \
  "ds_read_b64 a[34:35], %2 offset:680\n" \
  "v_mfma_f32_32x32x16_bf16 %0, a[0:3], a[8:11], %0\n" \
  "ds_read_b128 a[36:39], %3 offset:4240\n" \
  "ds_read_b128 a[40:43], %3 offset:1680\n" \
  "v_mfma_f32_32x32x16_bf16 %1, a[12:15], a[8:11], %1\n" \
  "ds_read_b64 a[44:45], %2 offset:2720\n" \
  "ds_read_b64 a[46:47], %2 offset:2728\n" \
  "s_waitcnt lgkmcnt(6)\n" \
  "v_mfma_f32_32x32x16_bf16 %1, a[16:19], a[20:23], %1\n" \
  "ds_read_b64 a[0:1], %2 offset:640\n" \
  "ds_read_b64 a[2:3], %2 offset:648\n" \
  "v_mfma_f32_32x32x16_bf16 %0, a[16:19], a[24:27], %0\n" \
  "ds_read_b128 a[4:7], %3 offset:4272\n" \
  "ds_read_b128 a[8:11], %3 offset:1712\n" \
  "v_mfma_f32_32x32x16_bf16 %1, a[28:31], a[24:27], %1\n" \
  "ds_read_b64 a[12:13], %2 offset:2688\n" \
  "ds_read_b64 a[14:15], %2 offset:2696\n" \
  "s_waitcnt lgkmcnt(6)\n" \
  "v_mfma_f32_32x32x16_bf16 %1, a[32:35], a[36:39], %1\n" \
  "ds_read_b64 a[16:17], %2 offset:736\n" \
  "ds_read_b64 a[18:19], %2 offset:744\n" \
  "v_mfma_f32_32x32x16_bf16 %0, a[32:35], a[40:43], %0\n" \
  "ds_read_b128 a[20:23], %3 offset:4160\n" \
  "ds_read_b128 a[24:27], %3 offset:1600\n" \
  "v_mfma_f32_32x32x16_bf16 %1, a[44:47], a[40:43], %1\n" \
  "ds_read_b64 a[28:29], %2 offset:2784\n" \
  "ds_read_b64 a[30:31], %2 offset:2792\n" \
  "s_waitcnt lgkmcnt(6)\n" \
  "v_mfma_f32_32x32x16_bf16 %1, a[0:3], a[4:7], %1\n" \
  "ds_read_b64 a[32:33], %2 offset:704\n" \
  "ds_read_b64 a[34:35], %2 offset:712\n" \
  "v_mfma_f32_32x32x16_bf16 %0, a[0:3], a[8:11], %0\n" \
  "ds_read_b128 a[36:39], %3 offset:4192\n" \
  "ds_read_b128 a[40:43], %3 offset:1632\n" \
  "v_mfma_f32_32x32x16_bf16 %1, a[12:15], a[8:11], %1\n" \
  "ds_read_b64 a[44:45], %2 offset:2752\n" \
  "ds_read_b64 a[46:47], %2 offset:2760\n" \
  "s_waitcnt lgkmcnt(6)\n" \
  "v_mfma_f32_32x32x16_bf16 %1, a[16:19], a[20:23], %1\n" \
  "ds_read_b64 a[0:1], %2 offset:800\n" \
  "ds_read_b64 a[2:3], %2 offset:808\n" \
  "v_mfma_f32_32x32x16_bf16 %0, a[16:19], a[24:27], %0\n" \
  "ds_read_b128 a[4:7], %3 offset:4080\n" \
  "ds_read_b128 a[8:11], %3 offset:1520\n" \
  "v_mfma_f32_32x32x16_bf16 %1, a[28:31], a[24:27], %1\n" \
  "ds_read_b64 a[12:13], %2 offset:2848\n" \
  "ds_read_b64 a[14:15], %2 offset:2856\n" \
  "s_waitcnt lgkmcnt(6)\n" \
  "v_mfma_f32_32x32x16_bf16 %1, a[32:35], a[36:39], %1\n" \
  "ds_read_b64 a[16:17], %2 offset:768\n" \
  "ds_read_b64 a[18:19], %2 offset:776\n" \
  "v_mfma_f32_32x32x16_bf16 %0, a[32:35], a[40:43], %0\n" \
  "ds_read_b128 a[20:23], %3 offset:4112\n" \
  "ds_read_b128 a[24:27], %3 offset:1552\n" \
  "v_mfma_f32_32x32x16_bf16 %1, a[44:47], a[40:43], %1\n" \
  "ds_read_b64 a[28:29], %2 offset:2816\n" \
  "ds_read_b64 a[30:31], %2 offset:2824\n" \
  "s_waitcnt lgkmcnt(6)\n" \
  "v_mfma_f32_32x32x16_bf16 %1, a[0:3], a[4:7], %1\n" \
  "ds_read_b64 a[32:33], %2 offset:864\n" \
  "ds_read_b64 a[34:35], %2 offset:872\n" \
  "v_mfma_f32_32x32x16_bf16 %0, a[0:3], a[8:11], %0\n" \
  "ds_read_b128 a[36:39], %3 offset:4000\n" \
  "ds_read_b128 a[40:43], %3 offset:1440\n" \
  "v_mfma_f32_32x32x16_bf16 %1, a[12:15], a[8:11], %1\n" \
  "ds_read_b64 a[44:45], %2 offset:2912\n" \
  "ds_read_b64 a[46:47], %2 offset:2920\n" \
  "s_waitcnt lgkmcnt(6)\n" \
  "v_mfma_f32_32x32x16_bf16 %1, a[16:19], a[20:23], %1\n" \
  "ds_read_b64 a[0:1], %2 offset:832\n" \
  "ds_read_b64 a[2:3], %2 offset:840\n" \
  "v_mfma_f32_32x32x16_bf16 %0, a[16:19], a[24:27], %0\n" \
  "ds_read_b128 a[4:7], %3 offset:4032\n" \
  "ds_read_b128 a[8:11], %3 offset:1472\n" \
  "v_mfma_f32_32x32x16_bf16 %1, a[28:31], a[24:27], %1\n" \
  "ds_read_b64 a[12:13], %2 offset:2880\n" \
  "ds_read_b64 a[14:15], %2 offset:2888\n" \
  "s_waitcnt lgkmcnt(6)\n" \
  "v_mfma_f32_32x32x16_bf16 %1, a[32:35], a[36:39], %1\n" \
  "ds_read_b64 a[16:17], %2 offset:928\n" \
  "ds_read_b64 a[18:19], %2 offset:936\n" \
  "v_mfma_f32_32x32x16_bf16 %0, a[32:35], a[40:43], %0\n" \
  "ds_read_b128 a[20:23], %3 offset:3920\n" \
  "ds_read_b128 a[24:27], %3 offset:1360\n" \
  "v_mfma_f32_32x32x16_bf16 %1, a[44:47], a[40:43], %1\n" \
  "ds_read_b64 a[28:29], %2 offset:2976\n" \
  "ds_read_b64 a[30:31], %2 offset:2984\n" \
  "s_waitcnt lgkmcnt(6)\n" \
  "v_mfma_f32_32x32x16_bf16 %1, a[0:3], a[4:7], %1\n" \
  "ds_read_b64 a[32:33], %2 offset:896\n" \
  "ds_read_b64 a[34:35], %2 offset:904\n" \
  "v_mfma_f32_32x32x16_bf16 %0, a[0:3], a[8:11], %0\n" \
  "ds_read_b128 a[36:39], %3 offset:3952\n" \
  "ds_read_b128 a[40:43], %3 offset:1392\n" \
  "v_mfma_f32_32x32x16_bf16 %1, a[12:15], a[8:11], %1\n" \
  "ds_read_b64 a[44:45], %2 offset:2944\n" \
  "ds_read_b64 a[46:47], %2 offset:2952\n" \
  "s_waitcnt lgkmcnt(6)\n" \
  "v_mfma_f32_32x32x16_bf16 %1, a[16:19], a[20:23], %1\n" \
  "ds_read_b64 a[0:1], %2 offset:992\n" \
  "ds_read_b64 a[2:3], %2 offset:1000\n" \
  "v_mfma_f32_32x32x16_bf16 %0, a[16:19], a[24:27], %0\n" \
  "ds_read_b128 a[4:7], %3 offset:3840\n" \
  "ds_read_b128 a[8:11], %3 offset:1280\n" \
  "v_mfma_f32_32x32x16_bf16 %1, a[28:31], a[24:27], %1\n" \
  "ds_read_b64 a[12:13], %2 offset:3040\n" \
  "ds_read_b64 a[14:15], %2 offset:3048\n" \
  "s_waitcnt lgkmcnt(6)\n" \
  "v_mfma_f32_32x32x16_bf16 %1, a[32:35], a[36:39], %1\n" \
  "ds_read_b64 a[16:17], %2 offset:960\n" \
  "ds_read_b64 a[18:19], %2 offset:968\n" \
  "v_mfma_f32_32x32x16_bf16 %0, a[32:35], a[40:43], %0\n" \
  "ds_read_b128 a[20:23], %3 offset:3872\n" \
  "ds_read_b128 a[24:27], %3 offset:1312\n" \
  "v_mfma_f32_32x32x16_bf16 %1, a[44:47], a[40:43], %1\n" \
  "ds_read_b64 a[28:29], %2 offset:3008\n" \
  "ds_read_b64 a[30:31], %2 offset:3016\n" \
  "s_waitcnt lgkmcnt(6)\n" \
  "v_mfma_f32_32x32x16_bf16 %1, a[0:3], a[4:7], %1\n" \
  "ds_read_b64 a[32:33], %2 offset:1056\n" \
  "ds_read_b64 a[34:35], %2 offset:1064\n" \
  "v_mfma_f32_32x32x16_bf16 %0, a[0:3], a[8:11], %0\n" \
  "ds_read_b128 a[36:39], %3 offset:3760\n" \
  "ds_read_b128 a[40:43], %3 offset:1200\n" \
  "v_mfma_f32_32x32x16_bf16 %1, a[12:15], a[8:11], %1\n" \
  "ds_read_b64 a[44:45], %2 offset:3104\n" \
  "ds_read_b64 a[46:47], %2 offset:3112\n" \
  "s_waitcnt lgkmcnt(6)\n" \
  "v_mfma_f32_32x32x16_bf16 %1, a[16:19], a[20:23], %1\n" \
  "ds_read_b64 a[0:1], %2 offset:1024\n" \
  "ds_read_b64 a[2:3], %2 offset:1032\n" \
  "v_mfma_f32_32x32x16_bf16 %0, a[16:19], a[24:27], %0\n" \
  "ds_read_b128 a[4:7], %3 offset:3792\n" \
  "ds_read_b128 a[8:11], %3 offset:1232\n" \
  "v_mfma_f32_32x32x16_bf16 %1, a[28:31], a[24:27], %1\n" \
  "ds_read_b64 a[12:13], %2 offset:3072\n" \
  "ds_read_b64 a[14:15], %2 offset:3080\n" \
  "s_waitcnt lgkmcnt(6)\n" \
  "v_mfma_f32_32x32x16_bf16 %1, a[32:35], a[36:39], %1\n" \
  "ds_read_b64 a[16:17], %2 offset:1120\n" \
  "ds_read_b64 a[18:19], %2 offset:1128\n" \
  "v_mfma_f32_32x32x16_bf16 %0, a[32:35], a[40:43], %0\n" \
  "ds_read_b128 a[20:23], %3 offset:3680\n" \
  "ds_read_b128 a[24:27], %3 offset:1120\n" \
  "v_mfma_f32_32x32x16_bf16 %1, a[44:47], a[40:43], %1\n" \
  "ds_read_b64 a[28:29], %2 offset:3168\n" \
  "ds_read_b64 a[30:31], %2 offset:3176\n" \
  "s_waitcnt lgkmcnt(6)\n" \
  "v_mfma_f32_32x32x16_bf16 %1, a[0:3], a[4:7], %1\n" \
  "ds_read_b64 a[32:33], %2 offset:1088\n" \
  "ds_read_b64 a[34:35], %2 offset:1096\n" \
  "v_mfma_f32_32x32x16_bf16 %0, a[0:3], a[8:11], %0\n" \
  "ds_read_b128 a[36:39], %3 offset:3712\n" \
  "ds_read_b128 a[40:43], %3 offset:1152\n" \
  "v_mfma_f32_32x32x16_bf16 %1, a[12:15], a[8:11], %1\n" \
  "ds_read_b64 a[44:45], %2 offset:3136\n" \
  "ds_read_b64 a[46:47], %2 offset:3144\n" \
  "s_waitcnt lgkmcnt(6)\n" \
  "v_mfma_f32_32x32x16_bf16 %1, a[16:19], a[20:23], %1\n" \
  "ds_read_b64 a[0:1], %2 offset:1184\n" \
  "ds_read_b64 a[2:3], %2 offset:1192\n" \
  "v_mfma_f32_32x32x16_bf16 %0, a[16:19], a[24:27], %0\n" \
  "ds_read_b128 a[4:7], %3 offset:3600\n" \
  "ds_read_b128 a[8:11], %3 offset:1040\n" \
  "v_mfma_f32_32x32x16_bf16 %1, a[28:31], a[24:27], %1\n" \
  "ds_read_b64 a[12:13], %2 offset:3232\n" \
  "ds_read_b64 a[14:15], %2 offset:3240\n" \
  "s_waitcnt lgkmcnt(6)\n" \
  "v_mfma_f32_32x32x16_bf16 %1, a[32:35], a[36:39], %1\n" \
  "ds_read_b64 a[16:17], %2 offset:1152\n" \
  "ds_read_b64 a[18:19], %2 offset:1160\n" \
  "v_mfma_f32_32x32x16_bf16 %0, a[32:35], a[40:43], %0\n" \
  "ds_read_b128 a[20:23], %3 offset:3632\n" \
  "ds_read_b128 a[24:27], %3 offset:1072\n" \
  "v_mfma_f32_32x32x16_bf16 %1, a[44:47], a[40:43], %1\n" \
  "ds_read_b64 a[28:29], %2 offset:3200\n" \
  "ds_read_b64 a[30:31], %2 offset:3208\n" \
  "s_waitcnt lgkmcnt(6)\n" \
  "v_mfma_f32_32x32x16_bf16 %1, a[0:3], a[4:7], %1\n" \
  "ds_read_b64 a[32:33], %2 offset:1248\n" \
  "ds_read_b64 a[34:35], %2 offset:1256\n" \
  "v_mfma_f32_32x32x16_bf16 %0, a[0:3], a[8:11], %0\n" \
  "ds_read_b128 a[36:39], %3 offset:3520\n" \
  "ds_read_b128 a[40:43], %3 offset:960\n" \
  "v_mfma_f32_32x32x16_bf16 %1, a[12:15], a[8:11], %1\n" \
  "ds_read_b64 a[44:45], %2 offset:3296\n" \
  "ds_read_b64 a[46:47], %2 offset:3304\n" \
  "s_waitcnt lgkmcnt(6)\n" \
  "v_mfma_f32_32x32x16_bf16 %1, a[16:19], a[20:23], %1\n" \
  "ds_read_b64 a[0:1], %2 offset:1216\n" \
  "ds_read_b64 a[2:3], %2 offset:1224\n" \
  "v_mfma_f32_32x32x16_bf16 %0, a[16:19], a[24:27], %0\n" \
  "ds_read_b128 a[4:7], %3 offset:3552\n" \
  "ds_read_b128 a[8:11], %3 offset:992\n" \
  "v_mfma_f32_32x32x16_bf16 %1, a[28:31], a[24:27], %1\n" \
  "ds_read_b64 a[12:13], %2 offset:3264\n" \
  "ds_read_b64 a[14:15], %2 offset:3272\n" \
  "s_waitcnt lgkmcnt(6)\n" \
  "v_mfma_f32_32x32x16_bf16 %1, a[32:35], a[36:39], %1\n" \
  "ds_read_b64 a[16:17], %2 offset:1312\n" \
  "ds_read_b64 a[18:19], %2 offset:1320\n" \
  "v_mfma_f32_32x32x16_bf16 %0, a[32:35], a[40:43], %0\n" \
  "ds_read_b128 a[20:23], %3 offset:3440\n" \
  "ds_read_b128 a[24:27], %3 offset:880\n" \
  "v_mfma_f32_32x32x16_bf16 %1, a[44:47], a[40:43], %1\n" \
  "ds_read_b64 a[28:29], %2 offset:3360\n" \
  "ds_read_b64 a[30:31], %2 offset:3368\n" \
  "s_waitcnt lgkmcnt(6)\n" \
  "v_mfma_f32_32x32x16_bf16 %1, a[0:3], a[4:7], %1\n" \
  "ds_read_b64 a[32:33], %2 offset:1280\n" \
  "ds_read_b64 a[34:35], %2 offset:1288\n" \
  "v_mfma_f32_32x32x16_bf16 %0, a[0:3], a[8:11], %0\n" \
  "ds_read_b128 a[36:39], %3 offset:3472\n" \
  "ds_read_b128 a[40:43], %3 offset:912\n" \
  "v_mfma_f32_32x32x16_bf16 %1, a[12:15], a[8:11], %1\n" \
  "ds_read_b64 a[44:45], %2 offset:3328\n" \
  "ds_read_b64 a[46:47], %2 offset:3336\n" \
  "s_waitcnt lgkmcnt(6)\n" \
  "v_mfma_f32_32x32x16_bf16 %1, a[16:19], a[20:23], %1\n" \
  "ds_read_b64 a[0:1], %2 offset:1376\n" \
  "ds_read_b64 a[2:3], %2 offset:1384\n" \
  "v_mfma_f32_32x32x16_bf16 %0, a[16:19], a[24:27], %0\n" \
  "ds_read_b128 a[4:7], %3 offset:3360\n" \
  "ds_read_b128 a[8:11], %3 offset:800\n" \
  "v_mfma_f32_32x32x16_bf16 %1, a[28:31], a[24:27], %1\n" \
  "ds_read_b64 a[12:13], %2 offset:3424\n" \
  "ds_read_b64 a[14:15], %2 offset:3432\n" \
  "s_waitcnt lgkmcnt(6)\n" \
  "v_mfma_f32_32x32x16_bf16 %1, a[32:35], a[36:39], %1\n" \
  "ds_read_b64 a[16:17], %2 offset:1344\n" \
  "ds_read_b64 a[18:19], %2 offset:1352\n" \
  "v_mfma_f32_32x32x16_bf16 %0, a[32:35], a[40:43], %0\n" \
  "ds_read_b128 a[20:23], %3 offset:3392\n" \
  "ds_read_b128 a[24:27], %3 offset:832\n" \
  "v_mfma_f32_32x32x16_bf16 %1, a[44:47], a[40:43], %1\n" \
  "ds_read_b64 a[28:29], %2 offset:3392\n" \
  "ds_read_b64 a[30:31], %2 offset:3400\n" \
  "s_waitcnt lgkmcnt(6)\n" \
  "v_mfma_f32_32x32x16_bf16 %1, a[0:3], a[4:7], %1\n" \
  "ds_read_b64 a[32:33], %2 offset:1440\n" \
  "ds_read_b64 a[34:35], %2 offset:1448\n" \
  "v_mfma_f32_32x32x16_bf16 %0, a[0:3], a[8:11], %0\n" \
  "ds_read_b128 a[36:39], %3 offset:3280\n" \
  "ds_read_b128 a[40:43], %3 offset:720\n" \
  "v_mfma_f32_32x32x16_bf16 %1, a[12:15], a[8:11], %1\n" \
  "ds_read_b64 a[44:45], %2 offset:3488\n" \
  "ds_read_b64 a[46:47], %2 offset:3496\n" \
  "s_waitcnt lgkmcnt(6)\n" \
  "v_mfma_f32_32x32x16_bf16 %1, a[16:19], a[20:23], %1\n" \
  "ds_read_b64 a[0:1], %2 offset:1408\n" \
  "ds_read_b64 a[2:3], %2 offset:1416\n" \
  "v_mfma_f32_32x32x16_bf16 %0, a[16:19], a[24:27], %0\n" \
  "ds_read_b128 a[4:7], %3 offset:3312\n" \
  "ds_read_b128 a[8:11], %3 offset:752\n" \
  "v_mfma_f32_32x32x16_bf16 %1, a[28:31], a[24:27], %1\n" \
  "ds_read_b64 a[12:13], %2 offset:3456\n" \
  "ds_read_b64 a[14:15], %2 offset:3464\n" \
  "s_waitcnt lgkmcnt(6)\n" \
  "v_mfma_f32_32x32x16_bf16 %1, a[32:35], a[36:39], %1\n" \
  "ds_read_b64 a[16:17], %2 offset:1504\n" \
  "ds_read_b64 a[18:19], %2 offset:1512\n" \
  "v_mfma_f32_32x32x16_bf16 %0, a[32:35], a[40:43], %0\n" \
  "ds_read_b128 a[20:23], %3 offset:3200\n" \
  "ds_read_b128 a[24:27], %3 offset:640\n" \
  "v_mfma_f32_32x32x16_bf16 %1, a[44:47], a[40:43], %1\n" \
  "ds_read_b64 a[28:29], %2 offset:3552\n" \
  "ds_read_b64 a[30:31], %2 offset:3560\n" \
  "s_waitcnt lgkmcnt(6)\n" \
  "v_mfma_f32_32x32x16_bf16 %1, a[0:3], a[4:7], %1\n" \
  "ds_read_b64 a[32:33], %2 offset:1472\n" \
  "ds_read_b64 a[34:35], %2 offset:1480\n" \
  "v_mfma_f32_32x32x16_bf16 %0, a[0:3], a[8:11], %0\n" \
  "ds_read_b128 a[36:39], %3 offset:3232\n" \
  "ds_read_b128 a[40:43], %3 offset:672\n" \
  "v_mfma_f32_32x32x16_bf16 %1, a[12:15], a[8:11], %1\n" \
  "ds_read_b64 a[44:45], %2 offset:3520\n" \
  "ds_read_b64 a[46:47], %2 offset:3528\n" \
  "s_waitcnt lgkmcnt(6)\n" \
  "v_mfma_f32_32x32x16_bf16 %1, a[16:19], a[20:23], %1\n" \
  "ds_read_b64 a[0:1], %2 offset:1568\n" \
  "ds_read_b64 a[2:3], %2 offset:1576\n" \
  "v_mfma_f32_32x32x16_bf16 %0, a[16:19], a[24:27], %0\n" \
  "ds_read_b128 a[4:7], %3 offset:3120\n" \
  "ds_read_b128 a[8:11], %3 offset:560\n" \
  "v_mfma_f32_32x32x16_bf16 %1, a[28:31], a[24:27], %1\n" \
  "ds_read_b64 a[12:13], %2 offset:3616\n" \
  "ds_read_b64 a[14:15], %2 offset:3624\n" \
  "s_waitcnt lgkmcnt(6)\n" \
  "v_mfma_f32_32x32x16_bf16 %1, a[32:35], a[36:39], %1\n" \
  "ds_read_b64 a[16:17], %2 offset:1536\n" \
  "ds_read_b64 a[18:19], %2 offset:1544\n" \
  "v_mfma_f32_32x32x16_bf16 %0, a[32:35], a[40:43], %0\n" \
  "ds_read_b128 a[20:23], %3 offset:3152\n" \
  "ds_read_b128 a[24:27], %3 offset:592\n" \
  "v_mfma_f32_32x32x16_bf16 %1, a[44:47], a[40:43], %1\n" \
  "ds_read_b64 a[28:29], %2 offset:3584\n" \
  "ds_read_b64 a[30:31], %2 offset:3592\n" \
  "s_waitcnt lgkmcnt(6)\n" \
  "v_mfma_f32_32x32x16_bf16 %1, a[0:3], a[4:7], %1\n" \
  "ds_read_b64 a[32:33], %2 offset:1632\n" \
  "ds_read_b64 a[34:35], %2 offset:1640\n" \
  "v_mfma_f32_32x32x16_bf16 %0, a[0:3], a[8:11], %0\n" \
  "ds_read_b128 a[36:39], %3 offset:3040\n" \
  "ds_read_b128 a[40:43], %3 offset:480\n" \
  "v_mfma_f32_32x32x16_bf16 %1, a[12:15], a[8:11], %1\n" \
  "ds_read_b64 a[44:45], %2 offset:3680\n" \
  "ds_read_b64 a[46:47], %2 offset:3688\n" \
  "s_waitcnt lgkmcnt(6)\n" \
  "v_mfma_f32_32x32x16_bf16 %1, a[16:19], a[20:23], %1\n" \
  "ds_read_b64 a[0:1], %2 offset:1600\n" \
  "ds_read_b64 a[2:3], %2 offset:1608\n" \
  "v_mfma_f32_32x32x16_bf16 %0, a[16:19], a[24:27], %0\n" \
  "ds_read_b128 a[4:7], %3 offset:3072\n" \
  "ds_read_b128 a[8:11], %3 offset:512\n" \
  "v_mfma_f32_32x32x16_bf16 %1, a[28:31], a[24:27], %1\n" \
  "ds_read_b64 a[12:13], %2 offset:3648\n" \
  "ds_read_b64 a[14:15], %2 offset:3656\n" \
  "s_waitcnt lgkmcnt(6)\n" \
  "v_mfma_f32_32x32x16_bf16 %1, a[32:35], a[36:39], %1\n" \
  "ds_read_b64 a[16:17], %2 offset:1696\n" \
  "ds_read_b64 a[18:19], %2 offset:1704\n" \
  "v_mfma_f32_32x32x16_bf16 %0, a[32:35], a[40:43], %0\n" \
  "ds_read_b128 a[20:23], %3 offset:2960\n" \
  "ds_read_b128 a[24:27], %3 offset:400\n" \
  "v_mfma_f32_32x32x16_bf16 %1, a[44:47], a[40:43], %1\n" \
  "ds_read_b64 a[28:29], %2 offset:3744\n" \
  "ds_read_b64 a[30:31], %2 offset:3752\n" \
  "s_waitcnt lgkmcnt(6)\n" \
  "v_mfma_f32_32x32x16_bf16 %1, a[0:3], a[4:7], %1\n" \
  "ds_read_b64 a[32:33], %2 offset:1664\n" \
  "ds_read_b64 a[34:35], %2 offset:1672\n" \
  "v_mfma_f32_32x32x16_bf16 %0, a[0:3], a[8:11], %0\n" \
  "ds_read_b128 a[36:39], %3 offset:2992\n" \
  "ds_read_b128 a[40:43], %3 offset:432\n" \
  "v_mfma_f32_32x32x16_bf16 %1, a[12:15], a[8:11], %1\n" \
  "ds_read_b64 a[44:45], %2 offset:3712\n" \
  "ds_read_b64 a[46:47], %2 offset:3720\n" \
  "s_waitcnt lgkmcnt(6)\n" \
  "v_mfma_f32_32x32x16_bf16 %1, a[16:19], a[20:23], %1\n" \
  "ds_read_b64 a[0:1], %2 offset:1760\n" \
  "ds_read_b64 a[2:3], %2 offset:1768\n" \
  "v_mfma_f32_32x32x16_bf16 %0, a[16:19], a[24:27], %0\n" \
  "ds_read_b128 a[4:7], %3 offset:2880\n" \
  "ds_read_b128 a[8:11], %3 offset:320\n" \
  "v_mfma_f32_32x32x16_bf16 %1, a[28:31], a[24:27], %1\n" \
  "ds_read_b64 a[12:13], %2 offset:3808\n" \
  "ds_read_b64 a[14:15], %2 offset:3816\n" \
  "s_waitcnt lgkmcnt(6)\n" \
  "v_mfma_f32_32x32x16_bf16 %1, a[32:35], a[36:39], %1\n" \
  "ds_read_b64 a[16:17], %2 offset:1728\n" \
  "ds_read_b64 a[18:19], %2 offset:1736\n" \
  "v_mfma_f32_32x32x16_bf16 %0, a[32:35], a[40:43], %0\n" \
  "ds_read_b128 a[20:23], %3 offset:2912\n" \
  "ds_read_b128 a[24:27], %3 offset:352\n" \
  "v_mfma_f32_32x32x16_bf16 %1, a[44:47], a[40:43], %1\n" \
  "ds_read_b64 a[28:29], %2 offset:3776\n" \
  "ds_read_b64 a[30:31], %2 offset:3784\n" \
  "s_waitcnt lgkmcnt(6)\n" \
  "v_mfma_f32_32x32x16_bf16 %1, a[0:3], a[4:7], %1\n" \
  "ds_read_b64 a[32:33], %2 offset:1824\n" \
  "ds_read_b64 a[34:35], %2 offset:1832\n" \
  "v_mfma_f32_32x32x16_bf16 %0, a[0:3], a[8:11], %0\n" \
  "ds_read_b128 a[36:39], %3 offset:2800\n" \
  "ds_read_b128 a[40:43], %3 offset:240\n" \
  "v_mfma_f32_32x32x16_bf16 %1, a[12:15], a[8:11], %1\n" \
  "ds_read_b64 a[44:45], %2 offset:3872\n" \
  "ds_read_b64 a[46:47], %2 offset:3880\n" \
  "s_waitcnt lgkmcnt(6)\n" \
  "v_mfma_f32_32x32x16_bf16 %1, a[16:19], a[20:23], %1\n" \
  "ds_read_b64 a[0:1], %2 offset:1792\n" \
  "ds_read_b64 a[2:3], %2 offset:1800\n" \
  "v_mfma_f32_32x32x16_bf16 %0, a[16:19], a[24:27], %0\n" \
  "ds_read_b128 a[4:7], %3 offset:2832\n" \
  "ds_read_b128 a[8:11], %3 offset:272\n" \
  "v_mfma_f32_32x32x16_bf16 %1, a[28:31], a[24:27], %1\n" \
  "ds_read_b64 a[12:13], %2 offset:3840\n" \
  "ds_read_b64 a[14:15], %2 offset:3848\n" \
  "s_waitcnt lgkmcnt(6)\n" \
  "v_mfma_f32_32x32x16_bf16 %1, a[32:35], a[36:39], %1\n" \
  "ds_read_b64 a[16:17], %2 offset:1888\n" \
  "ds_read_b64 a[18:19], %2 offset:1896\n" \
  "v_mfma_f32_32x32x16_bf16 %0, a[32:35], a[40:43], %0\n" \
  "ds_read_b128 a[20:23], %3 offset:2720\n" \
  "ds_read_b128 a[24:27], %3 offset:160\n" \
  "v_mfma_f32_32x32x16_bf16 %1, a[44:47], a[40:43], %1\n" \
  "ds_read_b64 a[28:29], %2 offset:3936\n" \
  "ds_read_b64 a[30:31], %2 offset:3944\n" \
  "s_waitcnt lgkmcnt(6)\n" \
  "v_mfma_f32_32x32x16_bf16 %1, a[0:3], a[4:7], %1\n" \
  "ds_read_b64 a[32:33], %2 offset:1856\n" \
  "ds_read_b64 a[34:35], %2 offset:1864\n" \
  "v_mfma_f32_32x32x16_bf16 %0, a[0:3], a[8:11], %0\n" \
  "ds_read_b128 a[36:39], %3 offset:2752\n" \
  "ds_read_b128 a[40:43], %3 offset:192\n" \
  "v_mfma_f32_32x32x16_bf16 %1, a[12:15], a[8:11], %1\n" \
  "ds_read_b64 a[44:45], %2 offset:3904\n" \
  "ds_read_b64 a[46:47], %2 offset:3912\n" \
  "s_waitcnt lgkmcnt(6)\n" \
  "v_mfma_f32_32x32x16_bf16 %1, a[16:19], a[20:23], %1\n" \
  "ds_read_b64 a[0:1], %2 offset:1952\n" \
  "ds_read_b64 a[2:3], %2 offset:1960\n" \
  "v_mfma_f32_32x32x16_bf16 %0, a[16:19], a[24:27], %0\n" \
  "ds_read_b128 a[4:7], %3 offset:2640\n" \
  "ds_read_b128 a[8:11], %3 offset:80\n" \
  "v_mfma_f32_32x32x16_bf16 %1, a[28:31], a[24:27], %1\n" \
  "ds_read_b64 a[12:13], %2 offset:4000\n" \
  "ds_read_b64 a[14:15], %2 offset:4008\n" \
  "s_waitcnt lgkmcnt(6)\n" \
  "v_mfma_f32_32x32x16_bf16 %1, a[32:35], a[36:39], %1\n" \
  "ds_read_b64 a[16:17], %2 offset:1920\n" \
  "ds_read_b64 a[18:19], %2 offset:1928\n" \
  "v_mfma_f32_32x32x16_bf16 %0, a[32:35], a[40:43], %0\n" \
  "ds_read_b128 a[20:23], %3 offset:2672\n" \
  "ds_read_b128 a[24:27], %3 offset:112\n" \
  "v_mfma_f32_32x32x16_bf16 %1, a[44:47], a[40:43], %1\n" \
  "ds_read_b64 a[28:29], %2 offset:3968\n" \
  "ds_read_b64 a[30:31], %2 offset:3976\n" \
  "s_waitcnt lgkmcnt(6)\n" \
  "v_mfma_f32_32x32x16_bf16 %1, a[0:3], a[4:7], %1\n" \
  "ds_read_b64 a[32:33], %2 offset:2016\n" \
  "ds_read_b64 a[34:35], %2 offset:2024\n" \
  "v_mfma_f32_32x32x16_bf16 %0, a[0:3], a[8:11], %0\n" \
  "ds_read_b128 a[36:39], %3 offset:2560\n" \
  "ds_read_b128 a[40:43], %3 offset:0\n" \
  "v_mfma_f32_32x32x16_bf16 %1, a[12:15], a[8:11], %1\n" \
  "ds_read_b64 a[44:45], %2 offset:4064\n" \
  "ds_read_b64 a[46:47], %2 offset:4072\n" \
  "s_waitcnt lgkmcnt(6)\n" \
  "v_mfma_f32_32x32x16_bf16 %1, a[16:19], a[20:23], %1\n" \
  "ds_read_b64 a[0:1], %2 offset:1984\n" \
  "ds_read_b64 a[2:3], %2 offset:1992\n" \
  "v_mfma_f32_32x32x16_bf16 %0, a[16:19], a[24:27], %0\n" \
  "ds_read_b128 a[4:7], %3 offset:2592\n" \
  "ds_read_b128 a[8:11], %3 offset:32\n" \
  "v_mfma_f32_32x32x16_bf16 %1, a[28:31], a[24:27], %1\n" \
  "ds_read_b64 a[12:13], %2 offset:4032\n" \
  "ds_read_b64 a[14:15], %2 offset:4040\n" \
  "s_waitcnt lgkmcnt(6)\n" \
  "v_mfma_f32_32x32x16_bf16 %1, a[32:35], a[36:39], %1\n" \
  "v_mfma_f32_32x32x16_bf16 %0, a[32:35], a[40:43], %0\n" \
  "v_mfma_f32_32x32x16_bf16 %1, a[44:47], a[40:43], %1\n" \
  "s_waitcnt lgkmcnt(0)\n" \
  "v_mfma_f32_32x32x16_bf16 %1, a[0:3], a[4:7], %1\n" \
  "v_mfma_f32_32x32x16_bf16 %0, a[0:3], a[8:11], %0\n" \
  "v_mfma_f32_32x32x16_bf16 %1, a[12:15], a[8:11], %1\n" \
  "s_nop 15\n" \
  ""

#define CONV_CLOBBERS "a0", "a1", "a2", "a3", "a4", "a5", "a6", "a7", "a8", "a9", "a10", "a11", "a12", "a13", "a14", "a15", "a16", "a17", "a18", "a19", "a20", "a21", "a22", "a23", "a24", "a25", "a26", "a27", "a28", "a29", "a30", "a31", "a32", "a33", "a34", "a35", "a36", "a37", "a38", "a39", "a40", "a41", "a42", "a43", "a44", "a45", "a46", "a47", "memory"
#define REC_CLOBBERS \
  "v0", "v1", "v2", "v3", "v4", "v5", "v6", "v7", "v8", "v9", "v10", "v11", "v12", "v13", "v14", "v15", "v16", "v17", "v18", "v19", "v20", "v21", "v22",\
  "v23", "v24", "v25", "v26", "v27", "v28", "v29", "v30", "v31", "v32", "v33", "v34", "v35", "v36", "v37", "v38", "v39", "v40", "v41", "v42", "v43",\
  "v44", "v45", "v46", "v47", "v48", "v49", "v50", "v51", "v52", "v53", "v54", "v55", "v56", "v57", "v58", "v59", "v60", "v61", "v62", "v63", "v64",\
  "v65", "v66", "v67", "v68", "v69", "v70", "v71", "v72", "v73", "v74", "v75", "v76", "v77", "v78", "v79", "v80", "v81", "v82", "v83", "v84", "v85",\
  "v86", "v87", "v88", "v89", "v90", "v91", "v92", "v93", "v94", "v95", "v96", "v97", "v98", "v99", "v100", "v101", "v102", "v103", "v104", "v105",\
  "v106", "v107", "v108", "v109", "v110", "v111", "v112", "v113", "v114", "v115", "v116", "v117", "v118", "v119", "v120", "v121", "v122", "v123",\
  "v124", "v125", "v126", "v127", "v128", "v129", "v130", "v131", "v132", "v133", "v134", "v135", "v136", "v137", "v138", "v139", "v140", "v141",\
  "v142", "v143", "v144", "v145", "v146", "v147", "v148", "v149", "v150", "v151", "v152", "v153", "v154", "v155", "v156", "v157", "v158", "v159",\
  "v160", "v161", "v162", "v163", "v164", "v165", "v166", "v167", "v168", "v169", "v170", "v171", "v172", "v173", "v174", "v175", "v176", "v177",\
  "v178", "v179", "v180", "v181", "v182", "v183", "v184", "v185", "v186", "v187", "v188", "v189", "v190", "v191", "v192", "v193", "v194", "v195",\
  "v196", "v197", "v198", "v199", "v200", "v201", "v202", "v203", "v204", "v205", "v206", "v207", "v208", "v209", "v210", "v211", "v212", "v213",\
  "v214", "v215", "v216", "v217", "v218", "v219", "v220", "v221", "v222", "v223", "v224", "v225", "v226", "v227", "v228", "v229", "v230", "v231",\
  "v232", "v233", "v234", "v235", "v236", "v237", "v238", "v239", "v240", "v241", "v242", "v243", "v244", "v245", "v246", "v247", "v248", "v249",\
  "v250", "v251", "v252", "v253", "v254", "v255", "a0", "a1", "a2", "a3", "a4", "a5", "a6", "a7", "a8", "a9", "a10", "a11", "a12", "a13", "a14", "a15",\
  "a16", "a17", "a18", "a19", "a20", "a21", "a22", "a23", "a24", "a25", "a26", "a27", "a28", "a29", "a30", "a31", "a32", "a33", "a34", "a35", "a36",\
  "a37", "a38", "a39", "a40", "a41", "a42", "a43", "a44", "a45", "a46", "a47", "a48", "a49", "a50", "a51", "a52", "a53", "a54", "a55", "a56", "a57",\
  "a58", "a59", "a60", "a61", "a62", "a63", "a64", "a65", "a66", "a67", "a68", "a69", "a70", "a71", "a72", "a73", "a74", "a75", "a76", "a77", "a78",\
  "a79", "a80", "a81", "a82", "a83", "a84", "a85", "a86", "a87", "a88", "a89", "a90", "a91", "a92", "a93", "a94", "a95", "a96", "a97", "a98", "a99",\
  "a100", "a101", "a102", "a103", "a104", "a105", "a106", "a107", "a108", "a109", "a110", "a111", "a112", "a113", "a114", "a115", "a116", "a117",\
  "a118", "a119", "a120", "a121", "a122", "a123", "a124", "a125", "a126", "a127", "a128", "a129", "a130", "a131", "a132", "a133", "a134", "a135",\
  "a136", "a137", "a138", "a139", "a140", "a141", "a142", "a143", "a144", "a145", "a146", "a147", "a148", "a149", "a150", "a151", "a152", "a153",\
  "a154", "a155", "a156", "a157", "a158", "a159", "a160", "a161", "a162", "a163", "a164", "a165", "a166", "a167", "a168", "a169", "a170", "a171",\
  "a172", "a173", "a174", "a175", "a176", "a177", "a178", "a179", "a180", "a181", "a182", "a183", "a184", "a185", "a186", "a187", "a188", "a189",\
  "a190", "a191", "a192", "a193", "a194", "a195", "a196", "a197", "a198", "a199", "a200", "a201", "a202", "a203", "a204", "a205", "a206", "a207",\
  "a208", "a209", "a210", "a211", "a212", "a213", "a214", "a215", "a216", "a217", "a218", "a219", "a220", "a221", "a222", "a223", "a224", "a225",\
  "a226", "a227", "a228", "a229", "a230", "a231", "a232", "a233", "a234", "a235", "a236", "a237", "a238", "a239", "a240", "a241", "a242", "a243",\
  "a244", "a245", "a246", "a247", "a248", "a249", "a250", "a251", "a252", "a253", "a254", "a255", "s34", "s35", "s36", "s37", "s38", "s39", "s40", "s41", "s42",\
  "s43", "s44", "s45", "s46", "s47", "s48", "s49", "s50", "s51", "s52", "s53", "s54", "s55", "s56", "s57", "s58", "s59", "s60", "s61", "s62", "s63",\
  "s64", "s65", "s66", "s67", "s68", "s69", "s70", "s71", "s72", "s73", "s74", "s75", "s76", "s77", "s78", "s79", "s80", "s81", "s82", "s83", "s84",\
  "s85", "s86", "s87", "s88", "s89", "s90", "s91", "s92", "s93", "s94", "s95", "s96", "s97", "s98", "s99", "vcc", "scc", "memory"
__device__ __forceinline__ void role_rec(const Args& a, char LAS* smem, const int rk, const u32 pol) {
  const int cu = rk;
  const int tid = threadIdx.x, w = __builtin_amdgcn_readfirstlane(tid >> 6);
  const int head = cu >> 2, j = cu & 3;
  const u16* pq = a.Wq + head * DH + 64 * (j >> 1);
  const u16* pk = a.Wk + head * DH + 64 * (j >> 1);
  const u16* pv = a.Wv + head * DH + 64 * ((j >> 1) ^ 1);
  const u16* po = a.Wo + (size_t)head * DH * DM + 256 * j;
  asm volatile(REC_ASM
               :: "s"(pq), "s"(pk), "s"(pv), "s"(po), "s"(a.ex), "s"(a.ctrl), "s"(cu), "s"(w), "s"(pol), "s"((u32)(uintptr_t)smem), "s"(rk), "s"(a.mem0 + (size_t)(4 * (cu & 1)) * DM + 16 * (cu >> 1))
               : REC_CLOBBERS);
  __builtin_unreachable();
}
#define S_ROW 4160
#define S_WSZ (4 * S_ROW + 96 * 80 + 2048 + 64)
#define T_LD 132
__device__ __forceinline__ u32 offT(u32 pol, u32 ch) { return (ch >> 1) * 16384 + ((~(pol >> (ch >> 8)) & 1u) << 13) + (ch & 1) * 4096; }
__device__ __forceinline__ void wait_all(u32* ctrl, int word, int n, int slp) {
  const int lane = threadIdx.x & 63;
  for (int spins = 0;; ++spins) {
    asm volatile("" ::: "memory");
    const u32 v = __hip_atomic_load(ctrl + word + 32 * min(lane, n - 1), RLX_AGENT);
    if (__builtin_amdgcn_ballot_w64(v == 0) == 0) break;
    if (spins > SPIN_CAP) { set_dead(ctrl); break; }
    if ((spins & 63) == 63 && __builtin_amdgcn_readfirstlane((int)is_dead(ctrl))) break;
    sleepn(slp);
  }
  asm volatile("" ::: "memory");
}
__device__ __forceinline__ void ssm_in(const Args& a, char LAS* smem, const int rb, const u32 pol) {
  const int tid = threadIdx.x;
  const __amdgpu_buffer_rsrc_t rt = __builtin_amdgcn_make_buffer_rsrc(a.ex + WS_UT_OFF, 0, 8 << 20, 0x00020000);
#pragma unroll
  for (int q = 0; q < 4; ++q) {
    u32x4 x[8];
#pragma unroll
    for (int i = 0; i < 4; ++i) {
      const u16* p = a.u + (size_t)(64 * rb + 2 * (8 * q + 2 * i + (tid >> 7))) * SSM_D + (tid & 127) * 8;
      x[2 * i] = *(const u32x4*)p; x[2 * i + 1] = *(const u32x4*)(p + SSM_D);
    }
#pragma unroll
    for (int i = 0; i < 4; ++i)
#pragma unroll
      for (int e = 0; e < 4; ++e) {
        char LAS* o = smem + ((tid & 127) + 256 * e) * T_LD + (8 * q + 2 * i + (tid >> 7)) * 4;
        *(LAS u32*)o = __builtin_amdgcn_perm(x[2 * i + 1][e], x[2 * i][e], 0x05040100u);
        *(LAS u32*)(o + 128 * T_LD) = __builtin_amdgcn_perm(x[2 * i + 1][e], x[2 * i][e], 0x07060302u);
      }
  }
  __syncthreads();
#pragma unroll 4
  for (int i = 0; i < 32; ++i) {
    const int sl = 32 * i + (tid >> 3), ch = (sl & 127) * 8 + (sl >> 7);
    const LAS u32* p = (const LAS u32*)(smem + sl * T_LD + (tid & 7) * 16);
    __builtin_amdgcn_raw_buffer_store_b128((u32x4){p[0], p[1], p[2], p[3]}, rt, offT(pol, ch) + rb * 128 + (tid & 7) * 16, 0, SC1);
  }
  WAIT_VM0();
  __syncthreads();
  if (tid == 0) __hip_atomic_store(a.ctrl + CW_UIN + rb * 32, 1u, RLX_AGENT);
}
__device__ __forceinline__ void ssm_out(const Args& a, char LAS* smem, const int rb, const int hb, const u32 pol) {
  const int tid = threadIdx.x;
  const __amdgpu_buffer_rsrc_t rt = __builtin_amdgcn_make_buffer_rsrc(a.ex + WS_UT_OFF, 0, 8 << 20, 0x00020000);
  wait_all(a.ctrl, CW_YOUT + hb * 48 * 32, 48, 8);
  u32x4 x[16];
#pragma unroll
  for (int i = 0; i < 16; ++i) {
    const int sl = 32 * i + (tid >> 3), ch = (sl & 63) * 8 + (sl >> 6);
    x[i] = __builtin_amdgcn_raw_buffer_load_b128(rt, offT(pol, 512 * hb + ch) + rb * 128 + (tid & 7) * 16, 0, SC1);
  }
#pragma unroll
  for (int i = 0; i < 16; ++i) {
    LAS u32* p = (LAS u32*)(smem + (32 * i + (tid >> 3)) * T_LD + (tid & 7) * 16);
#pragma unroll
    for (int e = 0; e < 4; ++e) p[e] = x[i][e];
  }
  __syncthreads();
#pragma unroll 2
  for (int i = 0; i < 8; ++i) {
    const int rp = 4 * i + (tid >> 6);
    u32x4 v0, v1;
#pragma unroll
    for (int e = 0; e < 4; ++e) {
      const char LAS* o = smem + ((tid & 63) + 128 * e) * T_LD + rp * 4;
      const u32 x0 = *(const LAS u32*)o, x1 = *(const LAS u32*)(o + 64 * T_LD);
      v0[e] = __builtin_amdgcn_perm(x1, x0, 0x05040100u); v1[e] = __builtin_amdgcn_perm(x1, x0, 0x07060302u);
    }
    u16* p = a.out + (size_t)(64 * rb + 2 * rp) * OUTLD + DM + 512 * hb + (tid & 63) * 8;
    *(u32x4*)p = v0; *(u32x4*)(p + OUTLD) = v1;
  }
}
__device__ __forceinline__ void split2(float a, float b, u32& hi, u32& lo) {
  hi = pk2(a, b);
  lo = pk2(a - __uint_as_float(hi << 16), b - __uint_as_float(hi & 0xffff0000u));
}
__device__ __forceinline__ void cexp1(float ar, float ai, float l, float& xr, float& xi) {
  const float e = __builtin_amdgcn_exp2f(ar * l);
  const float rv = __builtin_amdgcn_fractf(ai * l);
  xr = e * __builtin_amdgcn_cosf(rv); xi = e * __builtin_amdgcn_sinf(rv);
}
__device__ __forceinline__ u32 lo_hi(u32 lo, u32 hi) { return __builtin_amdgcn_perm(hi, lo, 0x07060100u); }
__device__ __forceinline__ void role_ssm(const Args& a, char LAS* smem, const int cx, const int rank, const u32 pol) {
  const int tid = threadIdx.x, lane = tid & 63, w = __builtin_amdgcn_readfirstlane(tid >> 6);
  const int c = lane & 15, g = lane >> 4, b = lane & 31, h = lane >> 5;
  const __amdgpu_buffer_rsrc_t rt = __builtin_amdgcn_make_buffer_rsrc(a.ex + WS_UT_OFF, 0, 8 << 20, 0x00020000);
  char LAS* lu = smem + w * S_WSZ;
  char LAS* lw = lu + S_ROW;
  char LAS* lk = lw + 3 * S_ROW + 31 * 80;
  char LAS* lp = lk + 65 * 80;
  if (lane < 4) *(LAS u32x4*)(lu + lane * 16) = (u32x4){0, 0, 0, 0};
  for (int i = lane; i < 160; i += 64) *(LAS u32x4*)(lw + 3 * S_ROW + i * 16) = (u32x4){0, 0, 0, 0};
  float la[3];
  la[0] = (float)c;
  la[1] = c < 8 ? (float)(256 + 2 * c) : (float)(512 + 256 * ((c - 8) >> 2) + 4 * ((c - 8) & 3));
  la[2] = c < 8 ? (float)(1024 + 256 * (c >> 1) + 8 * (c & 1)) : (c == 8 ? 2048.f : 0.f);
  const float ly = (float)(16 * c);
  u32x4 un[4];
  auto fetch_u = [&](int d) {
#pragma unroll
    for (int i = 0; i < 4; ++i) un[i] = __builtin_amdgcn_raw_buffer_load_b128(rt, lane * 16 + i * 1024, offT(pol, d), SC1);
  };
  u32 pr[6]; u16 pdt, pds;
  auto fetch_p = [&](int d) {
    const size_t o = (size_t)d * SSM_N + 2 * lane;
    pr[0] = *(const u32*)(a.A_re + o); pr[1] = *(const u32*)(a.A_im + o); pr[2] = *(const u32*)(a.B_re + o);
    pr[3] = *(const u32*)(a.B_im + o); pr[4] = *(const u32*)(a.C_re + o); pr[5] = *(const u32*)(a.C_im + o);
    pdt = a.log_dt[d]; pds = a.Dskip[d];
  };
#define SST(i)
  const int first = cx * 256 + rank * 4 + w, last = cx * 256 + 256;
  fetch_p(first);
  {
    for (int d = first; d < last; d += 24) {
      const int nxt = d + 24 < last ? d + 24 : d;
      SST(0);
      const float dt = rbf(expf(bf2f(pdt))), ds = bf2f(pds);
#pragma unroll
      for (int i = 0; i < 2; ++i) {
        auto el = [&](int k) { return __uint_as_float(i ? (pr[k] & 0xffff0000u) : (pr[k] << 16)); };
        const float br = dt * el(2), bi = dt * el(3), cr = el(4), ci = el(5);
        *(LAS f32x4*)(lp + (2 * lane + i) * 16) = (f32x4){dt * el(0) * 1.4426950408889634f, dt * el(1) * 0.15915494309189535f, cr * br - ci * bi, cr * bi + ci * br};
      }
      fetch_p(nxt);
      SST(1);
      f32x4 ka[3];
#pragma unroll
      for (int t = 0; t < 3; ++t) ka[t] = (f32x4){0, 0, 0, 0};
      f32x4 pn[4];
#pragma unroll
      for (int i = 0; i < 4; ++i) pn[i] = *(const LAS f32x4*)(lp + (4 * g + i) * 16);
      for (int k = 0; k < 8; ++k) {
        f32x4 p[4];
#pragma unroll
        for (int i = 0; i < 4; ++i) { p[i] = pn[i]; pn[i] = *(const LAS f32x4*)(lp + (16 * ((k + 1) & 7) + 4 * g + i) * 16); }
        u32x4 yh, yl, xh[3], xl[3];
#pragma unroll
        for (int i = 0; i < 4; ++i) {
          float er, ei; u32 hi, lo;
          cexp1(p[i][0], p[i][1], ly, er, ei);
          split2(p[i][2] * er - p[i][3] * ei, -(p[i][2] * ei + p[i][3] * er), hi, lo); yh[i] = hi; yl[i] = lo;
#pragma unroll
          for (int t = 0; t < 3; ++t) { cexp1(p[i][0], p[i][1], la[t], er, ei); split2(er, ei, hi, lo); xh[t][i] = hi; xl[t][i] = lo; }
        }
        __builtin_amdgcn_sched_barrier(0);
#pragma unroll
        for (int t = 0; t < 3; ++t) ka[t] = MFMA32(asb(xh[t]), asb(yh), ka[t]);
#pragma unroll
        for (int t = 0; t < 3; ++t) ka[t] = MFMA32(asb(xh[t]), asb(yl), ka[t]);
#pragma unroll
        for (int t = 0; t < 3; ++t) ka[t] = MFMA32(asb(xl[t]), asb(yh), ka[t]);
        __builtin_amdgcn_sched_barrier(0);
      }
      SST(2);
      {
        const int l = 16 * c + 4 * g;
        *(LAS u32x2*)(lk + (1 + (l >> 5)) * 80 + ((l & 31) ^ 4) * 2) = (u32x2){pk2(ka[0][3], ka[0][2]), pk2(ka[0][1], ka[0][0])};
        const int j1 = g < 2 ? 8 * c + 4 * g : 128 + 64 * (g - 2) + 4 * c;
        *(LAS u32x4*)(lp + j1 * 4) = (u32x4){pk2(ka[1][0], ka[1][0]), pk2(ka[1][1], ka[1][1]), pk2(ka[1][2], ka[1][2]), pk2(ka[1][3], ka[1][3])};
        if (g < 2) {
          *(LAS u32x2*)(lp + (256 + 64 * g + 2 * c) * 4) = (u32x2){pk2(ka[2][0], ka[2][0]), pk2(ka[2][1], ka[2][1])};
          *(LAS u32x2*)(lp + (256 + 64 * g + 32 + 2 * c) * 4) = (u32x2){pk2(ka[2][2], ka[2][2]), pk2(ka[2][3], ka[2][3])};
        } else if (lane == 32) *(LAS u32*)(lp + 384 * 4) = pk2(ka[2][0], ka[2][0]);
        if (lane < 32) {
          const u32x4 v = *(const LAS u32x4*)(lp + lane * 16);
          const u32 v4 = *(const LAS u32*)(lp + lane * 16 + 16);
          *(LAS u32x4*)(lk + (9 + (lane >> 2)) * 80 + (lane & 3) * 16) = (u32x4){lo_hi(v4, v[3]), v[2], lo_hi(v[2], v[1]), v[0]};
        }
        {
          const u32x2 v = *(const LAS u32x2*)(lp + 512 + lane * 8);
          const u32 v2 = *(const LAS u32*)(lp + 512 + lane * 8 + 8);
          *(LAS u32x4*)(lk + (17 + (lane >> 2)) * 80 + (lane & 3) * 16) = (u32x4){v2, v[1], lo_hi(v[1], v[0]), v[0]};
        }
        {
          const u32x2 v = *(const LAS u32x2*)(lp + 1024 + lane * 8);
          const u32 v2 = *(const LAS u32*)(lp + 1024 + lane * 8 + 8);
          char LAS* o = lk + (33 + (lane >> 1)) * 80 + (lane & 1) * 32;
          *(LAS u32x4*)o = (u32x4){v[1], lo_hi(v[1], v[0]), v[0], v[0]};
          *(LAS u32x4*)(o + 16) = (u32x4){v2, v2, v[1], v[1]};
        }
      }
      if (d == first) { wait_all(a.ctrl, CW_UIN, 32, 8); fetch_u(d); }
#pragma unroll
      for (int i = 0; i < 4; ++i) *(LAS u32x4*)(lu + 64 + lane * 16 + i * 1024) = un[i];
      if (nxt != d) fetch_u(nxt);
#pragma unroll
      for (int i = 0; i < 5; ++i) {
        const int q = lane + 64 * i;
        if (i < 4 || lane < 4) {
          const u32x4 x = *(const LAS u32x4*)(lu + q * 16);
          u32x2 y = *(const LAS u32x2*)(lu + q * 16 + 16);
          *(LAS u32x4*)(lw + q * 16) = (u32x4){__builtin_amdgcn_alignbit(x[1], x[0], 16), __builtin_amdgcn_alignbit(x[2], x[1], 16), __builtin_amdgcn_alignbit(x[3], x[2], 16), __builtin_amdgcn_alignbit(y[0], x[3], 16)};
          *(LAS u32x4*)(lw + S_ROW + q * 16) = (u32x4){x[1], x[2], x[3], y[0]};
          *(LAS u32x4*)(lw + 2 * S_ROW + q * 16) = (u32x4){__builtin_amdgcn_alignbit(x[2], x[1], 16), __builtin_amdgcn_alignbit(x[3], x[2], 16), __builtin_amdgcn_alignbit(y[0], x[3], 16), __builtin_amdgcn_alignbit(y[1], y[0], 16)};
        }
      }
      SST(3);
      const int p0 = 32 + b - 8 * h - 7, cp = p0 & 3;
      const char LAS* ra = lu + cp * S_ROW + (p0 - cp) * 2 - 32;
      const char LAS* rb = lk + 16 * h + 80 * (b - 30);
      f32x16 y0, y1;
      asm volatile(CONV_ASM : "=&v"(y0), "=&v"(y1) : "v"((u32)(uintptr_t)ra), "v"((u32)(uintptr_t)rb) : CONV_CLOBBERS);
      SST(4);
#pragma unroll
      for (int cg = 0; cg < 2; ++cg)
#pragma unroll
        for (int i4 = 0; i4 < 4; ++i4) {
          LAS u32x2* up = (LAS u32x2*)(lu + 64 + (32 * (32 * cg + b) + 8 * i4 + 4 * h) * 2);
          const u32x2 uv = *up;
          float v[4];
#pragma unroll
          for (int e = 0; e < 4; ++e) v[e] = cg ? y1[4 * i4 + e] : y0[4 * i4 + e];
          const u32 ya = pk2(v[0], v[1]), yb = pk2(v[2], v[3]);
          const u32 sa = pk2(__uint_as_float(uv[0] << 16) * ds, __uint_as_float(uv[0] & 0xffff0000u) * ds);
          const u32 sb = pk2(__uint_as_float(uv[1] << 16) * ds, __uint_as_float(uv[1] & 0xffff0000u) * ds);
          *up = (u32x2){pk2(__uint_as_float(ya << 16) + __uint_as_float(sa << 16), __uint_as_float(ya & 0xffff0000u) + __uint_as_float(sa & 0xffff0000u)),
                        pk2(__uint_as_float(yb << 16) + __uint_as_float(sb << 16), __uint_as_float(yb & 0xffff0000u) + __uint_as_float(sb & 0xffff0000u))};
        }
#pragma unroll
      for (int i = 0; i < 4; ++i) __builtin_amdgcn_raw_buffer_store_b128(*(const LAS u32x4*)(lu + 64 + lane * 16 + i * 1024), rt, lane * 16 + i * 1024, offT(pol, d), SC1);
      SST(5);
    }
    WAIT_VM0();
    if (lane == 0) __hip_atomic_store(a.ctrl + CW_YOUT + (cx * 24 + rank * 4 + w) * 32, 1u, RLX_AGENT);
  }
}
#define BULK_CLOBBERS \
  "v4", "v5", "v6", "v7", "v8", "v9", "v10", "v11", "v12", "v13", "v14", "v15", "v16", "v17", "v18", "v19", "v20", "v21", "v22", "v23", "v24", "v25",\
  "v26", "v27", "v28", "v29", "v30", "v31", "v32", "v33", "v34", "v35", "v36", "v37", "v38", "v39", "v40", "v41", "v42", "v43", "v44", "v45", "v46",\
  "v47", "v48", "v49", "v50", "v51", "v52", "v53", "v54", "v55", "v56", "v57", "v58", "v59", "v60", "v61", "v62", "v63", "v64", "v65", "v66", "v67",\
  "v68", "v69", "v70", "v71", "v72", "v73", "v74", "v75", "v76", "v77", "v78", "v79", "v80", "v81", "v82", "v83", "v84", "v85", "v86", "v87", "v88",\
  "v89", "v90", "v91", "v92", "v93", "v94", "v95", "v96", "v97", "v98", "v99", "v100", "v101", "v102", "v103", "v104", "v105", "v106", "v107", "v108",\
  "v109", "v110", "v111", "v112", "v113", "v114", "v115", "v116", "v117", "v118", "v119", "v120", "v121", "v122", "v123", "v124", "v125", "v126",\
  "v127", "v128", "v129", "v130", "v131", "v132", "v133", "v134", "v135", "v136", "v137", "v138", "v139", "v140", "v141", "v142", "v143", "v144",\
  "v145", "v146", "v147", "v148", "v149", "v150", "v151", "v152", "v153", "v154", "v155", "v156", "v157", "v158", "v159", "v160", "v161", "v162",\
  "v163", "v164", "v165", "v166", "v167", "v168", "v169", "v170", "v171", "v172", "v173", "v174", "v175", "v176", "v177", "v178", "v179", "v180",\
  "v181", "v182", "v183", "v184", "v185", "v186", "v187", "v188", "v189", "v190", "v191", "v192", "v193", "v194", "v195", "v196", "v197", "v198",\
  "v199", "v200", "v201", "v202", "v203", "v204", "v205", "v206", "v207", "v208", "v209", "v210", "v211", "v212", "v213", "v214", "v215", "v216",\
  "v217", "v218", "v219", "v220", "v221", "v222", "v223", "v224", "v225", "v226", "v227", "v228", "v229", "v230", "v231", "v232", "v233", "v234",\
  "v235", "v236", "v237", "v238", "v239", "v240", "v241", "v242", "v243", "v244", "v245", "v246", "v247", "v248", "v249", "v250", "v251", "v252",\
  "v253", "v254", "v255", "a0", "a1", "a2", "a3", "a4", "a5", "a6", "a7", "a8", "a9", "a10", "a11", "a12", "a13", "a14", "a15", "a16", "a17", "a18",\
  "a19", "a20", "a21", "a22", "a23", "a24", "a25", "a26", "a27", "a28", "a29", "a30", "a31", "a32", "a33", "a34", "a35", "a36", "a37", "a38", "a39",\
  "a40", "a41", "a42", "a43", "a44", "a45", "a46", "a47", "a48", "a49", "a50", "a51", "a52", "a53", "a54", "a55", "a56", "a57", "a58", "a59", "a60",\
  "a61", "a62", "a63", "a64", "a65", "a66", "a67", "a68", "a69", "a70", "a71", "a72", "a73", "a74", "a75", "a76", "a77", "a78", "a79", "a80", "a81",\
  "a82", "a83", "a84", "a85", "a86", "a87", "a88", "a89", "a90", "a91", "a92", "a93", "a94", "a95", "a96", "a97", "a98", "a99", "a100", "a101", "a102",\
  "a103", "a104", "a105", "a106", "a107", "a108", "a109", "a110", "a111", "a112", "a113", "a114", "a115", "a116", "a117", "a118", "a119", "a120",\
  "a121", "a122", "a123", "a124", "a125", "a126", "a127", "a128", "a129", "a130", "a131", "a132", "a133", "a134", "a135", "a136", "a137", "a138",\
  "a139", "a140", "a141", "a142", "a143", "a144", "a145", "a146", "a147", "a148", "a149", "a150", "a151", "a152", "a153", "a154", "a155", "a156",\
  "a157", "a158", "a159", "a160", "a161", "a162", "a163", "a164", "a165", "a166", "a167", "a168", "a169", "a170", "a171", "a172", "a173", "a174",\
  "a175", "a176", "a177", "a178", "a179", "a180", "a181", "a182", "a183", "a184", "a185", "a186", "a187", "a188", "a189", "a190", "a191", "a192",\
  "a193", "a194", "a195", "a196", "a197", "a198", "a199", "a200", "a201", "a202", "a203", "a204", "a205", "a206", "a207", "a208", "a209", "a210",\
  "a211", "a212", "a213", "a214", "a215", "a216", "a217", "a218", "a219", "a220", "a221", "a222", "a223", "a224", "a225", "a226", "a227", "a228",\
  "a229", "a230", "a231", "a232", "a233", "a234", "a235", "a236", "a237", "a238", "a239", "a240", "a241", "a242", "a243", "a244", "a245", "a246",\
  "a247", "a248", "a249", "a250", "a251", "a252", "a253", "a254", "a255", "s68", "s69", "s70", "s71", "s72", "s73", "s74", "s75", "s76", "s77", "s78",\
  "s79", "s80", "s81", "s82", "s83", "s84", "s85", "s86", "s87", "s88", "s89", "s90", "s91", "s92", "s93", "s94", "s95", "s96", "vcc", "scc", "memory"
#define GEMM_CLOBBERS \
  "v4", "v5", "v6", "v7", "v8", "v9", "v10", "v11", "v12", "v13", "v14", "v15", "v16", "v17", "v18", "v19", "v20", "v21", "v22", "v23", "v24", "v25",\
  "v26", "v27", "v28", "v29", "v30", "v31", "v32", "v33", "v34", "v35", "v36", "v37", "v38", "v39", "v40", "v41", "v42", "v43", "v44", "v45", "v46",\
  "v47", "v48", "v49", "v50", "v51", "v52", "v53", "v54", "v55", "v56", "v57", "v58", "v59", "v60", "v61", "v62", "v63", "v64", "v65", "v66", "v67",\
  "v68", "v69", "v70", "v71", "v72", "v73", "v74", "v75", "v76", "v77", "v78", "v79", "v80", "v81", "v82", "v83", "v84", "v85", "v86", "v87", "v88",\
  "v89", "v90", "v91", "v92", "v93", "v94", "v95", "v96", "v97", "v98", "v99", "v116", "v117", "v118", "v119", "v120", "v121", "v122", "v123", "v124",\
  "v125", "v126", "v127", "v128", "v129", "v130", "v131", "v132", "v133", "v134", "v135", "v136", "v137", "v138", "v139", "v140", "v141", "v142",\
  "v143", "v144", "v145", "v146", "v147", "v148", "v149", "v150", "v151", "v152", "v153", "v154", "v155", "v156", "v157", "v158", "v159", "v160",\
  "v161", "v162", "v163", "v164", "v165", "v166", "v167", "v168", "v169", "v170", "v171", "v172", "v173", "v174", "v175", "v176", "v177", "v178",\
  "v179", "v180", "v181", "v182", "v183", "v184", "v185", "v186", "v187", "v188", "v189", "v190", "v191", "v192", "v193", "v194", "v195", "v196",\
  "v197", "v198", "v199", "v200", "v201", "v202", "v203", "v204", "v205", "v206", "v207", "v208", "v209", "v210", "v211", "v212", "v213", "v214",\
  "v215", "v216", "v217", "v218", "v219", "v220", "v221", "v222", "v223", "v224", "v225", "v226", "v227", "v228", "v229", "v230", "v231", "v232",\
  "v233", "v234", "v235", "v236", "v237", "v238", "v239", "v240", "v241", "v242", "v243", "v244", "v245", "v246", "v247", "v248", "v249", "v250",\
  "v251", "v252", "v253", "v254", "v255", "a0", "a1", "a2", "a3", "a4", "a5", "a6", "a7", "a8", "a9", "a10", "a11", "a12", "a13", "a14", "a15", "a16",\
  "a17", "a18", "a19", "a20", "a21", "a22", "a23", "a24", "a25", "a26", "a27", "a28", "a29", "a30", "a31", "a32", "a33", "a34", "a35", "a36", "a37",\
  "a38", "a39", "a40", "a41", "a42", "a43", "a44", "a45", "a46", "a47", "a48", "a49", "a50", "a51", "a52", "a53", "a54", "a55", "a56", "a57", "a58",\
  "a59", "a60", "a61", "a62", "a63", "a64", "a65", "a66", "a67", "a68", "a69", "a70", "a71", "a72", "a73", "a74", "a75", "a76", "a77", "a78", "a79",\
  "a80", "a81", "a82", "a83", "a84", "a85", "a86", "a87", "a88", "a89", "a90", "a91", "a92", "a93", "a94", "a95", "a96", "a97", "a98", "a99", "a100",\
  "a101", "a102", "a103", "a104", "a105", "a106", "a107", "a108", "a109", "a110", "a111", "a112", "a113", "a114", "a115", "a116", "a117", "a118",\
  "a119", "a120", "a121", "a122", "a123", "a124", "a125", "a126", "a127", "a128", "a129", "a130", "a131", "a132", "a133", "a134", "a135", "a136",\
  "a137", "a138", "a139", "a140", "a141", "a142", "a143", "a144", "a145", "a146", "a147", "a148", "a149", "a150", "a151", "a152", "a153", "a154",\
  "a155", "a156", "a157", "a158", "a159", "a160", "a161", "a162", "a163", "a164", "a165", "a166", "a167", "a168", "a169", "a170", "a171", "a172",\
  "a173", "a174", "a175", "a176", "a177", "a178", "a179", "a180", "a181", "a182", "a183", "a184", "a185", "a186", "a187", "a188", "a189", "a190",\
  "a191", "a192", "a193", "a194", "a195", "a196", "a197", "a198", "a199", "a200", "a201", "a202", "a203", "a204", "a205", "a206", "a207", "a208",\
  "a209", "a210", "a211", "a212", "a213", "a214", "a215", "a216", "a217", "a218", "a219", "a220", "a221", "a222", "a223", "a224", "a225", "a226",\
  "a227", "a228", "a229", "a230", "a231", "a232", "a233", "a234", "a235", "a236", "a237", "a238", "a239", "a240", "a241", "a242", "a243", "a244",\
  "a245", "a246", "a247", "a248", "a249", "a250", "a251", "a252", "a253", "a254", "a255", "s68", "s69", "s70", "s71", "s72", "s73", "s74", "s75", "s76",\
  "s77", "s78", "s79", "s80", "s81", "s82", "s83", "s84", "s85", "s86", "s87", "s88", "s89", "s90", "s91", "s92", "s93", "s94", "s95", "s96", "vcc", "scc", "memory"
__device__ __forceinline__ void role_gemm(const Args& a, char LAS* smem, const int gi, const u32 pol) {
  const int w = __builtin_amdgcn_readfirstlane(threadIdx.x >> 6);
  asm volatile(GEMM_ASM
               :: "s"(a.Wq + 64 * gi), "s"(a.Wk + 64 * (gi ^ 1)), "s"(a.Wv + 64 * (gi ^ 2)), "s"(a.x), "s"(a.ex + WS_Q_OFF), "s"(a.ctrl), "s"(gi), "s"(w), "s"((u32)(uintptr_t)smem), "s"(pol)
               : GEMM_CLOBBERS);
  ssm_out(a, smem, gi & 31, gi >> 5, pol);
}
__device__ __forceinline__ void role_xo(const Args& a, char LAS* smem, const int xi, const u32 pol) {
  const int w = __builtin_amdgcn_readfirstlane(threadIdx.x >> 6);
  sleepn(XO_SLEEP);
  ssm_in(a, smem, xi, pol);
  asm volatile(XO_ASM
               :: "s"(a.Wo + 64 * (xi >> 1)), "s"(a.ex + WS_OX_OFF), "s"(a.out), "s"(a.ctrl), "s"(xi), "s"(w), "s"((u32)(uintptr_t)smem), "s"(pol)
               : BULK_CLOBBERS);
}
__device__ __forceinline__ void role_xa(const Args& a, char LAS* smem, const int idx, const u32 pol) {
  const int w = __builtin_amdgcn_readfirstlane(threadIdx.x >> 6);
  sleepn(250);
  asm volatile(XA_ASM
               :: "s"(a.ex), "s"(a.ex + WS_Q_OFF), "s"(a.ctrl), "s"(idx * 4 + w), "s"(pol), "s"((u32)(uintptr_t)smem + w * 2048)
               : BULK_CLOBBERS);
}
__global__ void __launch_bounds__(128) prep_kernel(u32* ctrl, char* ex) {
  __shared__ u32 best[2];
  const int b = blockIdx.x, tid = threadIdx.x, w = tid >> 6;
  for (int i = b * 128 + tid; i < CTRL_BYTES / 4; i += EX_PAGES * 128)
    if ((i < CW_POL || i >= CW_POL + 32) && (i < 6000 || i >= 6064)) ctrl[i] = 0;
  if (w == 0) {
    u32* p0 = (u32*)(ex + ((size_t)b << 21));
    u32 b0 = 0xffffffffu, b1 = 0xffffffffu;
    for (int rep = 0; rep < NPROBE; ++rep) {
      u32* pp = p0 + ((rep * 5) & 31) * 64;
      u64 t0 = __builtin_amdgcn_s_memtime();
      u32 r0 = __hip_atomic_fetch_or(pp, 0u, RLX_AGENT);
      asm volatile("s_waitcnt vmcnt(0)" :: "v"(r0) : "memory");
      u64 t1 = __builtin_amdgcn_s_memtime();
      u32 r1 = __hip_atomic_fetch_or(pp + 2048, 0u, RLX_AGENT);
      asm volatile("s_waitcnt vmcnt(0)" :: "v"(r1) : "memory");
      u64 t2 = __builtin_amdgcn_s_memtime();
      b0 = min(b0, (u32)(t1 - t0)); b1 = min(b1, (u32)(t2 - t1));
    }
    if (tid == 0) { best[0] = b0; best[1] = b1; }
  }
  if (tid == 0) {
    const u32 xcc = (u32)__builtin_amdgcn_s_getreg(20 | (3 << 11)) & 7u;
    const u32 fast1 = best[1] < best[0] ? 1u : 0u;
    ctrl[CW_POL + b] = xcc < 4 ? fast1 : (fast1 ^ 1u);
  }
}

__global__ void __launch_bounds__(256, 1) mega_kernel(Args a) {
  __shared__ __attribute__((aligned(16))) char smem_raw[163840];
  char LAS* smem = (char LAS*)smem_raw;
  const int tid = threadIdx.x;
  const u32 pw = (tid & 63) < EX_PAGES ? a.ctrl[CW_POL + (tid & 63)] : 0u;
  if (tid == 0) {
    const u32 xcc = (u32)__builtin_amdgcn_s_getreg(20 | (3 << 11)) & 7u;
    int role = -1;
    for (int k = 0; k < 8 && role < 0; ++k) {
      const u32 cc = (xcc + k) & 7u;
      const u32 t = __hip_atomic_fetch_add(a.ctrl + CW_TICKET + cc * 32, 1u, RLX_AGENT);
      if (t < 32) role = (int)(cc * 32 + t);
    }
    *(LAS int*)smem = role;
  }
  __syncthreads();
  const int role = __builtin_amdgcn_readfirstlane(*(LAS int*)smem);
  __syncthreads();
  if (role < 0) return;
  const u32 pol = vote_pol(pw);
  if (role < 128) role_rec(a, smem, role, pol);
  else {
    const int cx = (role - 128) >> 5, t = role & 31;
    if (t < 8) role_xo(a, smem, cx * 8 + t, pol);
    else if (t < 10) role_xa(a, smem, cx * 2 + t - 8, pol);
    else if (t < 16) role_ssm(a, smem, cx, t - 10, pol);
    else role_gemm(a, smem, (cx >> 1) * 32 + (t - 16) * 2 + (cx & 1), pol);
  }
}

extern "C" void kernel_launch(void* const* d_in, const int* in_sizes, int n_in, void* d_out, int out_size,
                              void* d_ws, size_t ws_size, hipStream_t stream) {
  Args a;
  a.x = (const u16*)d_in[0]; a.mem0 = (const u16*)d_in[1];
  a.Wq = (const u16*)d_in[2]; a.Wk = (const u16*)d_in[3]; a.Wv = (const u16*)d_in[4]; a.Wo = (const u16*)d_in[5];
  a.u = (const u16*)d_in[7]; a.A_re = (const u16*)d_in[8]; a.A_im = (const u16*)d_in[9];
  a.B_re = (const u16*)d_in[10]; a.B_im = (const u16*)d_in[11]; a.C_re = (const u16*)d_in[12]; a.C_im = (const u16*)d_in[13];
  a.log_dt = (const u16*)d_in[14]; a.Dskip = (const u16*)d_in[15];
  a.out = (u16*)d_out;
  char* ws = (char*)d_ws;
  a.ctrl = (u32*)ws;
  a.ex = (char*)(((uintptr_t)ws + CTRL_BYTES + (2u << 20) - 1) & ~(uintptr_t)((2u << 20) - 1));
  prep_kernel<<<EX_PAGES, 128, 0, stream>>>(a.ctrl, a.ex);
  mega_kernel<<<256, 256, 0, stream>>>(a);
}
```

```cpp
#include <hip/hip_runtime.h>
#include <hip/hip_bf16.h>
#include <stdint.h>

#define S_LEN 2048
#define DM 1024
#define NH 32
#define DH 128
#define HD 4096
#define NSEG 64
#define SSM_L 2048
#define SSM_D 1024
#define SSM_N 128
#define OUTLD 2048

typedef unsigned short u16;
typedef unsigned int u32;
typedef unsigned long long u64;
typedef __attribute__((ext_vector_type(8))) short bf16x8;
typedef __attribute__((ext_vector_type(4))) short bf16x4;
typedef __attribute__((ext_vector_type(4))) float f32x4;
typedef __attribute__((ext_vector_type(16))) float f32x16;
typedef __attribute__((ext_vector_type(4))) unsigned int u32x4;
typedef __attribute__((ext_vector_type(2))) unsigned int u32x2;
typedef __attribute__((ext_vector_type(2))) float f32x2;
typedef __attribute__((ext_vector_type(2))) __bf16 bf2_t;
typedef short v4i16_t __attribute__((ext_vector_type(4)));
#define LAS __attribute__((address_space(3)))

#define SENT 0xFFFFFFFFu
#define RLX_AGENT __ATOMIC_RELAXED, __HIP_MEMORY_SCOPE_AGENT
#define SC1 16

__device__ __forceinline__ float bf2f(u16 v) { return __uint_as_float(((u32)v) << 16); }
__device__ __forceinline__ u32 pk2(float a, float b) {
  f32x2 f = {a, b};
  bf2_t h = __builtin_convertvector(f, bf2_t);
  return __builtin_bit_cast(u32, h);
}
__device__ __forceinline__ float rbf(float f) { return __uint_as_float(pk2(f, 0.f) << 16); }
__device__ __forceinline__ u32 nosent(u32 v) { return v < 0xFFFFFFFEu ? v : 0xFFFFFFFEu; }
__device__ __forceinline__ bool okv(u32x4 v) { return v[0] != SENT && v[1] != SENT && v[2] != SENT && v[3] != SENT; }
__device__ __forceinline__ bf16x8 asb(u32x4 v) { return __builtin_bit_cast(bf16x8, v); }
__device__ __forceinline__ void sleepn(int n) { for (int k = 0; k < n; ++k) __builtin_amdgcn_s_sleep(1); }
#define WAIT_VM0() asm volatile("s_waitcnt vmcnt(0)" ::: "memory")
#define BAR() do { asm volatile("s_waitcnt lgkmcnt(0)" ::: "memory"); __builtin_amdgcn_s_barrier(); asm volatile("" ::: "memory"); } while (0)

__device__ __forceinline__ float xmax16(float x) {
  auto r = __builtin_amdgcn_permlane16_swap(__float_as_uint(x), __float_as_uint(x), false, false);
  return fmaxf(__uint_as_float(r[0]), __uint_as_float(r[1]));
}
__device__ __forceinline__ float xmax32(float x) {
  auto r = __builtin_amdgcn_permlane32_swap(__float_as_uint(x), __float_as_uint(x), false, false);
  return fmaxf(__uint_as_float(r[0]), __uint_as_float(r[1]));
}
__device__ __forceinline__ float xadd16(float x) {
  auto r = __builtin_amdgcn_permlane16_swap(__float_as_uint(x), __float_as_uint(x), false, false);
  return __uint_as_float(r[0]) + __uint_as_float(r[1]);
}
__device__ __forceinline__ float xadd32(float x) {
  auto r = __builtin_amdgcn_permlane32_swap(__float_as_uint(x), __float_as_uint(x), false, false);
  return __uint_as_float(r[0]) + __uint_as_float(r[1]);
}
__device__ __forceinline__ u32 ror8(u32 x) { return (u32)__builtin_amdgcn_update_dpp(0, (int)x, 0x128, 0xf, 0xf, true); }
template <int N> __device__ __forceinline__ float rorf(float x) {
  return __uint_as_float((u32)__builtin_amdgcn_update_dpp(0, (int)__float_as_uint(x), 0x120 + N, 0xf, 0xf, true));
}
__device__ __forceinline__ float ror8f(float x) { return __uint_as_float(ror8(__float_as_uint(x))); }

#define MFMA32(a, b, c) __builtin_amdgcn_mfma_f32_16x16x32_bf16(a, b, c, 0, 0, 0)
#define MFMA16(a, b, c) __builtin_amdgcn_mfma_f32_16x16x16bf16_1k(a, b, c, 0, 0, 0)
#define MFMA_AV0(acc, wa, xb) asm volatile("v_mfma_f32_16x16x32_bf16 %0, %1, %2, 0" : "=&v"(acc) : "a"(wa), "v"(xb))
#define MFMA_AV(acc, wa, xb) asm volatile("v_mfma_f32_16x16x32_bf16 %0, %1, %2, %0" : "+v"(acc) : "a"(wa), "v"(xb))
#define MFMA_VA0(acc, xa, wb) asm volatile("v_mfma_f32_16x16x32_bf16 %0, %1, %2, 0" : "=&v"(acc) : "v"(xa), "a"(wb))
#define MFMA_VA(acc, xa, wb) asm volatile("v_mfma_f32_16x16x32_bf16 %0, %1, %2, %0" : "+v"(acc) : "v"(xa), "a"(wb))
#define MFMA_VV0(acc, xa, xb) asm volatile("v_mfma_f32_16x16x32_bf16 %0, %1, %2, 0" : "=&v"(acc) : "v"(xa), "v"(xb))
#define MFMA16_VV(acc, xa, xb) asm volatile("v_mfma_f32_16x16x16_bf16 %0, %1, %2, %0" : "+v"(acc) : "v"(xa), "v"(xb))
#define MFMA_ACC(acc, xa, xb) asm volatile("v_mfma_f32_16x16x32_bf16 %0, %1, %2, %0" : "+a"(acc) : "v"(xa), "v"(xb))
#define NOP_IN() asm volatile("s_nop 1" ::: "memory")
#define PIN(x) asm volatile("" : "+v"(x))
#define PINA(x) asm volatile("" : "+a"(x))

__device__ __forceinline__ bf16x4 trread(const char LAS* p) {
  return __builtin_amdgcn_ds_read_tr16_b64_v4i16((LAS v4i16_t*)p);
}
__device__ __forceinline__ bf16x8 cat8(bf16x4 a, bf16x4 b) {
  return __builtin_shufflevector(a, b, 0, 1, 2, 3, 4, 5, 6, 7);
}

#define CW_TICKET 0
#define CW_POL 256
#define CW_INIT 288
#define CW_SSMQ 352
#define CW_DEAD 320
#define CW_PROG 384
#define CW_XPROG 1408
#define CW_GPROG 3456
#define CW_UIN 2432
#define CW_YOUT 8192
#define CTRL_BYTES 49152
#define EX_PAGES 24
#define EX_BYTES ((u64)EX_PAGES << 21)
#define EXP_C 0
#define EXP_A 20
#define EXP_B 2
#define EXP_DBG 19
#define QKV_SZ ((u64)S_LEN * HD * 4)
#define WS_Q_OFF (EX_BYTES)
#define WS_K_OFF (WS_Q_OFF + QKV_SZ)
#define WS_VT_OFF (WS_K_OFF + QKV_SZ)
#define WS_OX_OFF (WS_VT_OFF + QKV_SZ)
#define WS_UT_OFF (WS_OX_OFF + QKV_SZ)
#define EX_BYTES32 50331648
#define QKV_SZ32 33554432
#define WS_K_OFF32 83886080
static_assert(EX_BYTES32 == EX_BYTES && QKV_SZ32 == QKV_SZ && WS_K_OFF32 == WS_K_OFF, "layout");

#define SPIN_CAP 300000
#define NPROBE 5
#define F_B 1520
#define F_C 3450
#define F_P 4480
#ifndef M_C
#define M_C 150
#endif
#ifndef M_A
#define M_A 150
#endif
#define T_Q 1300
#define T_TOL 100
#define D_K 0
#define DECAY 0.996
#define RI0 1.06
#define XPRE 300
#define T_LATE 50
#define O_A 130
#define O_B 150
#define O_C 60
#define GAP_B 4
#define GAP_V 4
#define GAP_0 3
#define XO_SLEEP 250
#define D_PF (-200)
#define STR_(x) #x
#define STR(x) STR_(x)

struct Args {
  const u16 *x, *mem0, *Wq, *Wk, *Wv, *Wo;
  const u16 *u, *A_re, *A_im, *B_re, *B_im, *C_re, *C_im, *log_dt, *Dskip;
  u16* out;
  u32* ctrl;
  char* ex;
};

__device__ __forceinline__ bool is_dead(u32* ctrl) { return __hip_atomic_load(ctrl + CW_DEAD, RLX_AGENT) != 0; }
__device__ __forceinline__ void set_dead(u32* ctrl) { __hip_atomic_store(ctrl + CW_DEAD, 1u, RLX_AGENT); }
__device__ __forceinline__ void wait_ge(u32* ctrl, int word, u32 want, int slp) {
  for (int spins = 0;; ++spins) {
    asm volatile("" ::: "memory");
    u32 v = __builtin_amdgcn_readfirstlane(__hip_atomic_load(ctrl + word, RLX_AGENT));
    if (v >= want) break;
    if (spins > SPIN_CAP) { set_dead(ctrl); break; }
    if ((spins & 63) == 63 && __builtin_amdgcn_readfirstlane((int)is_dead(ctrl))) break;
    sleepn(slp);
  }
}
__device__ __forceinline__ u32 vote_pol(u32 v) {
  u32 m = (u32)__builtin_amdgcn_readfirstlane((u32)__ballot((v & 1u) != 0));
  const u32 full = (1u << EX_PAGES) - 1u;
  m = __builtin_popcount((m ^ 0xAAAAAAAAu) & full) <= EX_PAGES / 2 ? 0xAAAAAAAAu : 0x55555555u;
  return m;
}
__device__ __forceinline__ u32 exoff(u32 pol, u32 page, u32 pair) { return (page << 21) + (pair << 14) + (((pol >> page) & 1u) << 13); }
__device__ __forceinline__ u32 offB(u32 pol, int s, int h) { u32 pr = 32 * s + h; return exoff(pol, EXP_B + (pr >> 7), pr & 127); }
__device__ __forceinline__ u32 max4(u32x4 v) { return max(max(v[0], v[1]), max(v[2], v[3])); }
#define POLL_FAIL(spins) ((++(spins) > SPIN_CAP) ? (set_dead(ctrl), true) : (((spins) & 255) == 255 && __builtin_amdgcn_readfirstlane((int)is_dead(ctrl))))
#define REC_ASM \
  "s_mov_b64 exec, -1\n" \
  "v_mbcnt_lo_u32_b32 v0, -1, 0\n" \
  "v_mbcnt_hi_u32_b32 v0, -1, v0\n" \
  "s_lshr_b32 s70, %6, 2\n" \
  "s_and_b32 s71, %6, 3\n" \
  "v_lshlrev_b32 v1, 4, v0\n" \
  "v_and_b32 v27, 7, v0\n" \
  "v_lshrrev_b32 v28, 3, v0\n" \
  "v_lshlrev_b32 v34, 11, v28\n" \
  "v_lshl_add_u32 v34, v27, 4, v34\n" \
  "v_lshlrev_b32 v32, 13, v28\n" \
  "v_lshl_add_u32 v32, v27, 4, v32\n" \
  "v_and_b32 v27, 32, v0\n" \
  "v_xor_b32 v32, v32, v27\n" \
  "v_xor_b32 v33, 64, v32\n" \
  "s_mul_i32 s88, %7, 40960\n" \
  "s_add_u32 s88, s88, %9\n" \
  "s_lshl_b32 s60, %7, 21\n" \
  "s_mov_b64 s[72:73], %0\n" \
  "s_and_b32 s73, s73, 0xffff\n" \
  "s_mov_b32 s74, 8388608\n" \
  "s_mov_b32 s75, 0x00020000\n" \
  "s_mov_b64 s[76:77], %1\n" \
  "s_and_b32 s77, s77, 0xffff\n" \
  "s_mov_b32 s78, 8388608\n" \
  "s_mov_b32 s79, 0x00020000\n" \
  "s_mov_b64 s[80:81], %2\n" \
  "s_and_b32 s81, s81, 0xffff\n" \
  "s_mov_b32 s82, 8388608\n" \
  "s_mov_b32 s83, 0x00020000\n" \
  "s_mov_b64 s[84:85], %3\n" \
  "s_and_b32 s85, s85, 0xffff\n" \
  "s_mov_b32 s86, 262144\n" \
  "s_mov_b32 s87, 0x00020000\n" \
  "s_mov_b64 s[90:91], %11\n" \
  "v_and_b32 v27, 1, v0\n" \
  "v_lshlrev_b32 v27, 11, v27\n" \
  "v_and_b32 v28, 4, v0\n" \
  "v_lshl_add_u32 v27, v28, 10, v27\n" \
  "v_and_b32 v28, 2, v0\n" \
  "v_lshl_add_u32 v27, v28, 3, v27\n" \
  "global_load_dwordx4 v[64:67], v27, s[90:91]\n" \
  "s_lshr_b32 s59, %10, 1\n" \
  "s_add_u32 s59, s59, %7\n" \
  "s_add_u32 s59, s59, 0\n" \
  "s_and_b32 s59, s59, 3\n" \
  "s_lshl_b32 s59, s59, 19\n" \
  "s_lshl_b32 s60, %7, 21\n" \
  "s_add_u32 s59, s59, s60\n" \
  "s_add_u32 s59, s59, 0\n" \
  "s_add_u32 m0, s88, 0\n" \
  "s_nop 0\n" \
  "buffer_load_dwordx4 v32, s[72:75], s59 offen lds\n" \
  "s_add_u32 m0, s88, 4096\n" \
  "s_nop 0\n" \
  "buffer_load_dwordx4 v32, s[76:79], s59 offen lds\n" \
  "s_add_u32 s59, s59, 65536\n" \
  "s_add_u32 m0, s88, 1024\n" \
  "s_nop 0\n" \
  "buffer_load_dwordx4 v33, s[72:75], s59 offen lds\n" \
  "s_add_u32 m0, s88, 5120\n" \
  "s_nop 0\n" \
  "buffer_load_dwordx4 v33, s[76:79], s59 offen lds\n" \
  "s_add_u32 s59, s59, 65536\n" \
  "s_add_u32 m0, s88, 2048\n" \
  "s_nop 0\n" \
  "buffer_load_dwordx4 v32, s[72:75], s59 offen lds\n" \
  "s_add_u32 m0, s88, 6144\n" \
  "s_nop 0\n" \
  "buffer_load_dwordx4 v32, s[76:79], s59 offen lds\n" \
  "s_add_u32 s59, s59, 65536\n" \
  "s_add_u32 m0, s88, 3072\n" \
  "s_nop 0\n" \
  "buffer_load_dwordx4 v33, s[72:75], s59 offen lds\n" \
  "s_add_u32 m0, s88, 7168\n" \
  "s_nop 0\n" \
  "buffer_load_dwordx4 v33, s[76:79], s59 offen lds\n" \
  "s_lshr_b32 s59, %10, 1\n" \
  "s_add_u32 s59, s59, %7\n" \
  "s_add_u32 s59, s59, 0\n" \
  "s_and_b32 s59, s59, 3\n" \
  "s_lshl_b32 s59, s59, 19\n" \
  "s_lshl_b32 s60, %7, 21\n" \
  "s_add_u32 s59, s59, s60\n" \
  "s_add_u32 s59, s59, 262144\n" \
  "s_add_u32 m0, s88, 8192\n" \
  "s_nop 0\n" \
  "buffer_load_dwordx4 v32, s[72:75], s59 offen lds\n" \
  "s_add_u32 m0, s88, 12288\n" \
  "s_nop 0\n" \
  "buffer_load_dwordx4 v32, s[76:79], s59 offen lds\n" \
  "s_add_u32 s59, s59, 65536\n" \
  "s_add_u32 m0, s88, 9216\n" \
  "s_nop 0\n" \
  "buffer_load_dwordx4 v33, s[72:75], s59 offen lds\n" \
  "s_add_u32 m0, s88, 13312\n" \
  "s_nop 0\n" \
  "buffer_load_dwordx4 v33, s[76:79], s59 offen lds\n" \
  "s_add_u32 s59, s59, 65536\n" \
  "s_add_u32 m0, s88, 10240\n" \
  "s_nop 0\n" \
  "buffer_load_dwordx4 v32, s[72:75], s59 offen lds\n" \
  "s_add_u32 m0, s88, 14336\n" \
  "s_nop 0\n" \
  "buffer_load_dwordx4 v32, s[76:79], s59 offen lds\n" \
  "s_add_u32 s59, s59, 65536\n" \
  "s_add_u32 m0, s88, 11264\n" \
  "s_nop 0\n" \
  "buffer_load_dwordx4 v33, s[72:75], s59 offen lds\n" \
  "s_add_u32 m0, s88, 15360\n" \
  "s_nop 0\n" \
  "buffer_load_dwordx4 v33, s[76:79], s59 offen lds\n" \
  "s_lshr_b32 s59, %10, 1\n" \
  "s_add_u32 s59, s59, %7\n" \
  "s_add_u32 s59, s59, 1\n" \
  "s_and_b32 s59, s59, 3\n" \
  "s_lshl_b32 s59, s59, 19\n" \
  "s_lshl_b32 s60, %7, 21\n" \
  "s_add_u32 s59, s59, s60\n" \
  "s_add_u32 s59, s59, 0\n" \
  "s_add_u32 m0, s88, 16384\n" \
  "s_nop 0\n" \
  "buffer_load_dwordx4 v32, s[72:75], s59 offen lds\n" \
  "s_add_u32 m0, s88, 20480\n" \
  "s_nop 0\n" \
  "buffer_load_dwordx4 v32, s[76:79], s59 offen lds\n" \
  "s_add_u32 s59, s59, 65536\n" \
  "s_add_u32 m0, s88, 17408\n" \
  "s_nop 0\n" \
  "buffer_load_dwordx4 v33, s[72:75], s59 offen lds\n" \
  "s_add_u32 m0, s88, 21504\n" \
  "s_nop 0\n" \
  "buffer_load_dwordx4 v33, s[76:79], s59 offen lds\n" \
  "s_add_u32 s59, s59, 65536\n" \
  "s_add_u32 m0, s88, 18432\n" \
  "s_nop 0\n" \
  "buffer_load_dwordx4 v32, s[72:75], s59 offen lds\n" \
  "s_add_u32 m0, s88, 22528\n" \
  "s_nop 0\n" \
  "buffer_load_dwordx4 v32, s[76:79], s59 offen lds\n" \
  "s_add_u32 s59, s59, 65536\n" \
  "s_add_u32 m0, s88, 19456\n" \
  "s_nop 0\n" \
  "buffer_load_dwordx4 v33, s[72:75], s59 offen lds\n" \
  "s_add_u32 m0, s88, 23552\n" \
  "s_nop 0\n" \
  "buffer_load_dwordx4 v33, s[76:79], s59 offen lds\n" \
  "v_and_b32 v36, 15, v0\n" \
  "v_lshrrev_b32 v37, 4, v0\n" \
  "v_lshrrev_b32 v38, 2, v36\n" \
  "v_and_b32 v39, 3, v0\n" \
  "v_lshrrev_b32 v46, 1, v37\n" \
  "v_lshlrev_b32 v46, 4, v46\n" \
  "v_and_b32 v27, 1, v37\n" \
  "v_lshl_add_u32 v46, v27, 2, v46\n" \
  "v_and_b32 v28, 1, v38\n" \
  "v_add_u32 v46, v46, v28\n" \
  "v_lshrrev_b32 v28, 1, v38\n" \
  "v_lshl_add_u32 v46, v28, 3, v46\n" \
  "v_lshl_add_u32 v27, v28, 1, v27\n" \
  "v_lshlrev_b32 v27, 5, v27\n" \
  "s_and_b32 s59, s71, 1\n" \
  "s_lshl_b32 s59, s59, 6\n" \
  "v_xor_b32 v27, s59, v27\n" \
  "v_lshl_add_u32 v27, v46, 7, v27\n" \
  "v_lshl_add_u32 v28, v39, 3, v27\n" \
  "v_add_u32 v42, s88, v28\n" \
  "v_xor_b32 v28, 32, v28\n" \
  "v_add_u32 v43, s88, v28\n" \
  "v_and_b32 v28, 1, v39\n" \
  "v_lshl_add_u32 v27, v28, 3, v27\n" \
  "v_lshrrev_b32 v28, 1, v39\n" \
  "v_lshl_add_u32 v27, v28, 12, v27\n" \
  "s_add_u32 s59, %7, 0\n" \
  "s_and_b32 s59, s59, 3\n" \
  "s_lshl_b32 s59, s59, 4\n" \
  "v_xor_b32 v28, s59, v27\n" \
  "v_add_u32 v52, s88, v28\n" \
  "s_add_u32 s59, %7, 1\n" \
  "s_and_b32 s59, s59, 3\n" \
  "s_lshl_b32 s59, s59, 4\n" \
  "v_xor_b32 v28, s59, v27\n" \
  "v_add_u32 v53, s88, v28\n" \
  "s_add_u32 s59, %7, 2\n" \
  "s_and_b32 s59, s59, 3\n" \
  "s_lshl_b32 s59, s59, 4\n" \
  "v_xor_b32 v28, s59, v27\n" \
  "v_add_u32 v54, s88, v28\n" \
  "s_add_u32 s59, %7, 3\n" \
  "s_and_b32 s59, s59, 3\n" \
  "s_lshl_b32 s59, s59, 4\n" \
  "v_xor_b32 v28, s59, v27\n" \
  "v_add_u32 v55, s88, v28\n" \
  "v_lshlrev_b32 v49, 7, v46\n" \
  "v_lshl_add_u32 v49, v39, 5, v49\n" \
  "v_add_u32 v49, s88, v49\n" \
  "s_mov_b64 s[44:45], %5\n" \
  "s_and_b32 s45, s45, 0xffff\n" \
  "s_mov_b32 s46, " STR(CTRL_BYTES) "\n" \
  "s_mov_b32 s47, 0x00020000\n" \
  "s_mov_b64 s[36:37], %4\n" \
  "s_and_b32 s37, s37, 0xffff\n" \
  "s_mov_b32 s38, " STR(EX_BYTES32) "\n" \
  "s_mov_b32 s39, 0x00020000\n" \
  "s_mov_b64 s[40:41], %4\n" \
  "s_add_u32 s40, s40, " STR(WS_K_OFF32) "\n" \
  "s_addc_u32 s41, s41, 0\n" \
  "s_and_b32 s41, s41, 0xffff\n" \
  "s_mov_b32 s42, 3*" STR(QKV_SZ32) "\n" \
  "s_mov_b32 s43, 0x00020000\n" \
  "v_lshrrev_b32 v50, 5, v0\n" \
  "v_and_b32 v51, 31, v0\n" \
  "v_mov_b32 v2, 0x4444\n" \
  "v_mov_b32 v27, 0xeeee\n" \
  "v_cmp_lt_u32 vcc, 1, v39\n" \
  "s_mul_i32 s59, %7, 1280\n" \
  "s_add_u32 s59, s59, %9\n" \
  "v_cndmask_b32 v2, v2, v27, vcc\n" \
  "v_cndmask_b32 v29, 0, -1, vcc\n" \
  "v_cndmask_b32 v4, -1, 0, vcc\n" \
  "v_mul_u32_u24 v27, 80, v37\n" \
  "v_add_lshl_u32 v27, v27, v36, 2\n" \
  "v_add_u32 v5, s59, v27\n" \
  "v_lshlrev_b32 v27, 3, v0\n" \
  "s_add_u32 s60, %9, 8192\n" \
  "v_add_u32 v27, s60, v27\n" \
  "s_mul_i32 s59, %7, 1536\n" \
  "v_add_u32 v6, s59, v27\n" \
  "s_add_u32 s59, %7, 1\n" \
  "s_and_b32 s59, s59, 3\n" \
  "s_mul_i32 s59, s59, 1536\n" \
  "v_add_u32 v3, s59, v27\n" \
  "s_add_u32 s59, %7, 2\n" \
  "s_and_b32 s59, s59, 3\n" \
  "s_mul_i32 s59, s59, 1536\n" \
  "v_add_u32 v244, s59, v27\n" \
  "s_add_u32 s59, %7, 3\n" \
  "s_and_b32 s59, s59, 3\n" \
  "s_mul_i32 s59, s59, 1536\n" \
  "v_add_u32 v245, s59, v27\n" \
  "v_and_b32 v27, 1, v37\n" \
  "v_lshlrev_b32 v27, 1, v27\n" \
  "v_and_b32 v28, 1, v38\n" \
  "v_add_u32 v27, v27, v28\n" \
  "v_mul_u32_u24 v27, 160, v27\n" \
  "v_lshrrev_b32 v28, 1, v38\n" \
  "v_lshl_add_u32 v27, v28, 5, v27\n" \
  "v_lshl_add_u32 v27, v39, 3, v27\n" \
  "s_and_b32 s59, %7, 1\n" \
  "s_mul_i32 s59, s59, 640\n" \
  "s_lshr_b32 s60, %7, 1\n" \
  "s_lshl_b32 s60, s60, 6\n" \
  "s_add_u32 s59, s59, s60\n" \
  "s_add_u32 s59, s59, %9\n" \
  "v_add_u32 v7, s59, v27\n" \
  "v_mov_b32 v24, %9\n" \
  "s_lshl_b32 s94, s70, 14\n" \
  "v_add_u32 v17, s94, v1\n" \
  "v_add_u32 v17, 4096, v17\n" \
  "s_lshl_b32 s59, s71, 2\n" \
  "s_add_u32 s59, s59, %7\n" \
  "s_lshl_b32 s59, s59, 8\n" \
  "s_add_u32 s59, s59, s94\n" \
  "v_and_b32 v8, 0x38, v0\n" \
  "v_lshlrev_b32 v8, 2, v8\n" \
  "v_and_b32 v27, 1, v0\n" \
  "v_lshl_add_u32 v8, v27, 4, v8\n" \
  "v_and_b32 v27, 6, v0\n" \
  "v_lshl_add_u32 v8, v27, 1, v8\n" \
  "v_add_u32 v8, s59, v8\n" \
  "v_and_b32 v27, 7, v0\n" \
  "v_lshlrev_b32 v27, 4, v27\n" \
  "v_lshrrev_b32 v28, 3, v0\n" \
  "v_add_u32 v15, s59, v1\n" \
  "s_xor_b32 s59, s71, 2\n" \
  "s_lshl_b32 s59, s59, 2\n" \
  "s_add_u32 s59, s59, %7\n" \
  "s_lshl_b32 s59, s59, 7\n" \
  "s_add_u32 s59, s59, 4096\n" \
  "s_add_u32 s59, s59, s94\n" \
  "v_lshlrev_b32 v9, 2, v51\n" \
  "v_add_u32 v9, s59, v9\n" \
  "v_lshl_add_u32 v9, v50, 31, v9\n" \
  "v_add_u32 v25, s59, v27\n" \
  "v_cmp_eq_u32 vcc, 2, v28\n" \
  "s_lshr_b32 s59, %10, 5\n" \
  "s_lshl_b32 s59, s59, 17\n" \
  "s_add_u32 s59, s59, 6144\n" \
  "s_and_b32 s60, %10, 31\n" \
  "s_or_b32 s60, s60, %7\n" \
  "v_cndmask_b32 v15, v15, v25, vcc\n" \
  "s_cmp_eq_u32 s60, 1\n" \
  "s_cselect_b32 s60, 0, 0x80000000\n" \
  "s_and_b32 s61, %10, 1\n" \
  "s_lshl_b32 s61, s61, 31\n" \
  "s_or_b32 s60, s60, s61\n" \
  "s_or_b32 s59, s59, s60\n" \
  "v_mov_b32 v25, 0x80000000\n" \
  "v_cmp_eq_u32 vcc, 3, v28\n" \
  "v_lshl_add_u32 v14, v0, 3, s59\n" \
  "s_nop 0\n" \
  "v_cndmask_b32 v15, v15, v25, vcc\n" \
  "v_lshl_add_u32 v25, v51, 4, s59\n" \
  "v_cmp_eq_u32 vcc, 1, v50\n" \
  "s_lshr_b32 s59, %10, 5\n" \
  "s_lshl_b32 s59, s59, 7\n" \
  "s_add_u32 s59, s59, 6144\n" \
  "v_cndmask_b32 v15, v15, v25, vcc\n" \
  "v_lshl_add_u32 v13, v39, 17, s59\n" \
  "s_lshl_b32 s59, s71, 2\n" \
  "s_add_u32 s59, s59, %7\n" \
  "s_lshl_b32 s59, s59, 16\n" \
  "v_lshlrev_b32 v10, 14, v37\n" \
  "v_and_b32 v25, 1, v28\n" \
  "v_lshl_add_u32 v10, v25, 12, v10\n" \
  "v_add3_u32 v10, v10, v27, s59\n" \
  "s_lshl_b32 s60, %7, 4\n" \
  "s_add_u32 s60, s60, s70\n" \
  "v_lshl_add_u32 v25, v28, 1, s60\n" \
  "v_and_b32 v25, 31, v25\n" \
  "v_lshl_add_u32 v10, v25, 7, v10\n" \
  "s_lshr_b32 s59, %6, 1\n" \
  "s_lshl_b32 s59, s59, 14\n" \
  "s_and_b32 s60, %6, 1\n" \
  "s_lshl_b32 s60, s60, 12\n" \
  "s_add_u32 s59, s59, s60\n" \
  "s_lshl_b32 s60, %7, 10\n" \
  "s_add_u32 s59, s59, s60\n" \
  "v_add_u32 v11, s59, v1\n" \
  "s_lshr_b32 s59, %6, 6\n" \
  "s_lshl_b32 s59, s59, 14\n" \
  "s_and_b32 s60, %6, 63\n" \
  "s_lshl_b32 s60, s60, 7\n" \
  "s_add_u32 s59, s59, s60\n" \
  "v_add_u32 v12, s59, v27\n" \
  "v_lshrrev_b32 v25, 3, v36\n" \
  "v_lshl_add_u32 v25, v50, 1, v25\n" \
  "v_add_u32 v25, " STR(EXP_A) ", v25\n" \
  "v_lshl_add_u32 v12, v25, 21, v12\n" \
  "v_lshrrev_b32 v25, v25, %8\n" \
  "v_and_b32 v25, 1, v25\n" \
  "v_lshl_add_u32 v12, v25, 13, v12\n" \
  "v_lshl_add_u32 v12, v37, 31, v12\n" \
  "v_and_b32 v27, 1, v37\n" \
  "v_lshlrev_b32 v27, 5, v27\n" \
  "v_xor_b32 v27, v1, v27\n" \
  "s_add_u32 s59, %9, 32768\n" \
  "v_add_u32 v19, s59, v27\n" \
  "s_and_b32 s60, %7, 1\n" \
  "s_lshl_b32 s60, s60, 13\n" \
  "v_add_u32 v18, s60, v19\n" \
  "v_and_b32 v20, 15, v46\n" \
  "v_lshlrev_b32 v20, 4, v20\n" \
  "v_lshl_add_u32 v20, v50, 10, v20\n" \
  "v_lshl_add_u32 v20, v39, 8, v20\n" \
  "v_and_b32 v27, 1, v39\n" \
  "v_lshlrev_b32 v27, 5, v27\n" \
  "v_xor_b32 v20, v20, v27\n" \
  "v_xor_b32 v21, 32, v20\n" \
  "s_add_u32 s59, s59, 8192\n" \
  "v_add_u32 v20, s59, v20\n" \
  "v_add_u32 v21, s59, v21\n" \
  "v_and_b32 v22, 1, v0\n" \
  "v_lshlrev_b32 v22, 7, v22\n" \
  "s_and_b32 s59, %7, 1\n" \
  "s_xor_b32 s59, s59, s70\n" \
  "s_lshl_b32 s59, s59, 8\n" \
  "s_add_u32 s59, s59, " STR(CW_GPROG) "*4\n" \
  "v_add_u32 v22, s59, v22\n" \
  "s_lshr_b32 s59, %10, 1\n" \
  "s_add_u32 s59, s59, %7\n" \
  "s_add_u32 s59, s59, 0\n" \
  "s_and_b32 s59, s59, 3\n" \
  "s_lshl_b32 s59, s59, 10\n" \
  "v_add_u32 v80, s59, v1\n" \
  "v_add_u32 v84, s59, v19\n" \
  "v_mov_b32 v88, -1\n" \
  "s_lshr_b32 s59, %10, 1\n" \
  "s_add_u32 s59, s59, %7\n" \
  "s_add_u32 s59, s59, 1\n" \
  "s_and_b32 s59, s59, 3\n" \
  "s_lshl_b32 s59, s59, 10\n" \
  "v_add_u32 v81, s59, v1\n" \
  "v_add_u32 v85, s59, v19\n" \
  "v_mov_b32 v89, -1\n" \
  "s_lshr_b32 s59, %10, 1\n" \
  "s_add_u32 s59, s59, %7\n" \
  "s_add_u32 s59, s59, 2\n" \
  "s_and_b32 s59, s59, 3\n" \
  "s_lshl_b32 s59, s59, 10\n" \
  "v_add_u32 v82, s59, v1\n" \
  "v_add_u32 v86, s59, v19\n" \
  "v_mov_b32 v90, -1\n" \
  "s_lshr_b32 s59, %10, 1\n" \
  "s_add_u32 s59, s59, %7\n" \
  "s_add_u32 s59, s59, 3\n" \
  "s_and_b32 s59, s59, 3\n" \
  "s_lshl_b32 s59, s59, 10\n" \
  "v_add_u32 v83, s59, v1\n" \
  "v_add_u32 v87, s59, v19\n" \
  "v_mov_b32 v91, -1\n" \
  "v_mov_b32 v248, 0\n" \
  "v_mov_b32 v27, 0x3f80\n" \
  "v_and_b32 v25, 7, v36\n" \
  "v_cmp_eq_u32 vcc, 0, v25\n" \
  "v_mov_b32 v28, 1.0\n" \
  "s_nop 0\n" \
  "v_cndmask_b32 v248, v248, v27, vcc\n" \
  "v_cmp_eq_u32 vcc, 1, v25\n" \
  "s_nop 1\n" \
  "v_cndmask_b32 v248, v248, v28, vcc\n" \
  "v_mov_b32 v249, 0\n" \
  "v_mov_b32 v27, 0x3f80\n" \
  "v_and_b32 v25, 7, v36\n" \
  "v_cmp_eq_u32 vcc, 2, v25\n" \
  "v_mov_b32 v28, 1.0\n" \
  "s_nop 0\n" \
  "v_cndmask_b32 v249, v249, v27, vcc\n" \
  "v_cmp_eq_u32 vcc, 3, v25\n" \
  "s_nop 1\n" \
  "v_cndmask_b32 v249, v249, v28, vcc\n" \
  "v_mov_b32 v250, 0\n" \
  "v_mov_b32 v27, 0x3f80\n" \
  "v_and_b32 v25, 7, v36\n" \
  "v_cmp_eq_u32 vcc, 4, v25\n" \
  "v_mov_b32 v28, 1.0\n" \
  "s_nop 0\n" \
  "v_cndmask_b32 v250, v250, v27, vcc\n" \
  "v_cmp_eq_u32 vcc, 5, v25\n" \
  "s_nop 1\n" \
  "v_cndmask_b32 v250, v250, v28, vcc\n" \
  "v_mov_b32 v251, 0\n" \
  "v_mov_b32 v27, 0x3f80\n" \
  "v_and_b32 v25, 7, v36\n" \
  "v_cmp_eq_u32 vcc, 6, v25\n" \
  "v_mov_b32 v28, 1.0\n" \
  "s_nop 0\n" \
  "v_cndmask_b32 v251, v251, v27, vcc\n" \
  "v_cmp_eq_u32 vcc, 7, v25\n" \
  "s_nop 1\n" \
  "v_cndmask_b32 v251, v251, v28, vcc\n" \
  "v_and_b32 v27, 1, v0\n" \
  "v_cmp_eq_u32 vcc, 1, v27\n" \
  "v_mov_b32 v246, 0x05040100\n" \
  "v_mov_b32 v28, 0x03020706\n" \
  "v_cndmask_b32 v246, v246, v28, vcc\n" \
  "v_mov_b32 v252, 0x3f803f80\n" \
  "v_mov_b32 v253, 0x3f803f80\n" \
  "v_mov_b32 v254, 0x3f803f80\n" \
  "v_mov_b32 v255, 0\n" \
  "v_mov_b32 v30, 0\n" \
  "s_mov_b32 s92, 0x3e0293ee\n" \
  "s_mov_b32 s58, 0\n" \
  "s_mov_b32 s48, 0\n" \
  "s_lshr_b32 s59, %7, 1\n" \
  "s_lshl_b32 s59, s59, 14\n" \
  "s_and_b32 s60, %7, 1\n" \
  "s_lshl_b32 s60, s60, 12\n" \
  "s_add_u32 s93, s59, s60\n" \
  "s_lshr_b32 s60, %10, 5\n" \
  "s_add_u32 s60, s60, " STR(EXP_A) "\n" \
  "s_lshl_b32 s60, s60, 21\n" \
  "s_add_u32 s60, s60, s93\n" \
  "s_mov_b32 s53, 0\n" \
  "s_lshr_b32 s59, s60, 21\n" \
  "s_lshr_b32 s59, %8, s59\n" \
  "s_and_b32 s59, s59, 1\n" \
  "s_lshl_b32 s59, s59, 13\n" \
  "s_or_b32 s93, s60, s59\n" \
  "s_mov_b32 s60, " STR(EXP_C) "<<21\n" \
  "s_lshr_b32 s59, s60, 21\n" \
  "s_lshr_b32 s59, %8, s59\n" \
  "s_and_b32 s59, s59, 1\n" \
  "s_lshl_b32 s59, s59, 13\n" \
  "s_or_b32 s52, s60, s59\n" \
  "s_add_u32 s51, s52, 1<<20\n" \
  "s_and_b32 s59, %7, 1\n" \
  "s_mul_i32 s59, s59, " STR(QKV_SZ32) "\n" \
  "s_lshl_b32 s60, s70, 14\n" \
  "s_add_u32 s59, s59, s60\n" \
  "v_add_u32 v247, s59, v1\n" \
  "v_add_u32 v16, 4096, v247\n" \
  "s_lshr_b32 s60, %7, 1\n" \
  "s_lshr_b32 s59, s60, 2\n" \
  "s_xnor_b32 s59, s59, %8\n" \
  "s_and_b32 s59, s59, 1\n" \
  "s_lshl_b32 s59, s59, 13\n" \
  "s_lshl_b32 s55, s60, 19\n" \
  "s_or_b32 s55, s55, s59\n" \
  "s_waitcnt lgkmcnt(0)\n" \
  "s_waitcnt vmcnt(24)\n" \
  "s_mov_b32 s60, " STR(EXP_B) "<<21\n" \
  "s_lshr_b32 s59, s60, 21\n" \
  "s_lshr_b32 s59, %8, s59\n" \
  "s_and_b32 s59, s59, 1\n" \
  "s_lshl_b32 s59, s59, 13\n" \
  "s_or_b32 s60, s60, s59\n" \
  "buffer_store_dwordx4 v[88:91], v15, s[36:39], s60 offen sc1\n" \
  "s_add_u32 s60, s60, 1<<19\n" \
  "buffer_store_dwordx4 v[88:91], v15, s[36:39], s60 offen sc1\n" \
  "s_add_u32 s60, s60, 1<<19\n" \
  "buffer_store_dwordx4 v[88:91], v15, s[36:39], s60 offen sc1\n" \
  "s_add_u32 s60, s60, 1<<19\n" \
  "buffer_store_dwordx4 v[88:91], v15, s[36:39], s60 offen sc1\n" \
  "s_add_u32 s60, s60, 1<<19\n" \
  "buffer_store_dwordx4 v[88:91], v11, s[36:39], s51 offen sc1\n" \
  "buffer_store_dwordx4 v[88:91], v11, s[36:39], s52 offen sc1\n" \
  "s_add_u32 s59, %7, 1\n" \
  "s_lshl_b32 s59, s59, 15\n" \
  "buffer_store_dwordx4 v[88:91], v12, s[36:39], s59 offen sc1\n" \
  "v_min_u32 v64, -2, v64\n" \
  "v_min_u32 v65, -2, v65\n" \
  "v_min_u32 v66, -2, v66\n" \
  "v_min_u32 v67, -2, v67\n" \
  "s_lshl_b32 s59, %7, 29\n" \
  "v_or_b32 v27, s59, v12\n" \
  "buffer_store_dwordx4 v[64:67], v27, s[36:39], 0 offen sc1\n" \
  "s_lshr_b32 s59, %10, 1\n" \
  "s_add_u32 s59, s59, %7\n" \
  "s_add_u32 s59, s59, 1\n" \
  "s_and_b32 s59, s59, 3\n" \
  "s_lshl_b32 s59, s59, 19\n" \
  "s_lshl_b32 s60, %7, 21\n" \
  "s_add_u32 s59, s59, s60\n" \
  "s_add_u32 s59, s59, 262144\n" \
  "s_add_u32 m0, s88, 24576\n" \
  "s_nop 0\n" \
  "buffer_load_dwordx4 v32, s[72:75], s59 offen lds\n" \
  "s_add_u32 m0, s88, 28672\n" \
  "s_nop 0\n" \
  "buffer_load_dwordx4 v32, s[76:79], s59 offen lds\n" \
  "s_add_u32 s59, s59, 65536\n" \
  "s_add_u32 m0, s88, 25600\n" \
  "s_nop 0\n" \
  "buffer_load_dwordx4 v33, s[72:75], s59 offen lds\n" \
  "s_add_u32 m0, s88, 29696\n" \
  "s_nop 0\n" \
  "buffer_load_dwordx4 v33, s[76:79], s59 offen lds\n" \
  "s_add_u32 s59, s59, 65536\n" \
  "s_add_u32 m0, s88, 26624\n" \
  "s_nop 0\n" \
  "buffer_load_dwordx4 v32, s[72:75], s59 offen lds\n" \
  "s_add_u32 m0, s88, 30720\n" \
  "s_nop 0\n" \
  "buffer_load_dwordx4 v32, s[76:79], s59 offen lds\n" \
  "s_add_u32 s59, s59, 65536\n" \
  "s_add_u32 m0, s88, 27648\n" \
  "s_nop 0\n" \
  "buffer_load_dwordx4 v33, s[72:75], s59 offen lds\n" \
  "s_add_u32 m0, s88, 31744\n" \
  "s_nop 0\n" \
  "buffer_load_dwordx4 v33, s[76:79], s59 offen lds\n" \
  "s_lshr_b32 s59, %10, 1\n" \
  "s_add_u32 s59, s59, %7\n" \
  "s_add_u32 s59, s59, 2\n" \
  "s_and_b32 s59, s59, 3\n" \
  "s_lshl_b32 s59, s59, 19\n" \
  "s_lshl_b32 s60, %7, 21\n" \
  "s_add_u32 s59, s59, s60\n" \
  "s_add_u32 s59, s59, 0\n" \
  "s_add_u32 m0, s88, 32768\n" \
  "s_nop 0\n" \
  "buffer_load_dwordx4 v32, s[72:75], s59 offen lds\n" \
  "s_add_u32 m0, s88, 36864\n" \
  "s_nop 0\n" \
  "buffer_load_dwordx4 v32, s[76:79], s59 offen lds\n" \
  "s_add_u32 s59, s59, 65536\n" \
  "s_add_u32 m0, s88, 33792\n" \
  "s_nop 0\n" \
  "buffer_load_dwordx4 v33, s[72:75], s59 offen lds\n" \
  "s_add_u32 m0, s88, 37888\n" \
  "s_nop 0\n" \
  "buffer_load_dwordx4 v33, s[76:79], s59 offen lds\n" \
  "s_add_u32 s59, s59, 65536\n" \
  "s_add_u32 m0, s88, 34816\n" \
  "s_nop 0\n" \
  "buffer_load_dwordx4 v32, s[72:75], s59 offen lds\n" \
  "s_add_u32 m0, s88, 38912\n" \
  "s_nop 0\n" \
  "buffer_load_dwordx4 v32, s[76:79], s59 offen lds\n" \
  "s_add_u32 s59, s59, 65536\n" \
  "s_add_u32 m0, s88, 35840\n" \
  "s_nop 0\n" \
  "buffer_load_dwordx4 v33, s[72:75], s59 offen lds\n" \
  "s_add_u32 m0, s88, 39936\n" \
  "s_nop 0\n" \
  "buffer_load_dwordx4 v33, s[76:79], s59 offen lds\n" \
  "s_waitcnt vmcnt(40)\n" \
  "ds_read_b64_tr_b16 a[0:1], v52 offset:0\n" \
  "ds_read_b64_tr_b16 a[2:3], v52 offset:256\n" \
  "ds_read_b64_tr_b16 a[8:9], v53 offset:0\n" \
  "ds_read_b64_tr_b16 a[10:11], v53 offset:256\n" \
  "ds_read_b64_tr_b16 a[16:17], v54 offset:0\n" \
  "ds_read_b64_tr_b16 a[18:19], v54 offset:256\n" \
  "ds_read_b64_tr_b16 a[24:25], v55 offset:0\n" \
  "ds_read_b64_tr_b16 a[26:27], v55 offset:256\n" \
  "s_waitcnt lgkmcnt(0)\n" \
  "s_lshr_b32 s59, %10, 1\n" \
  "s_add_u32 s59, s59, %7\n" \
  "s_add_u32 s59, s59, 2\n" \
  "s_and_b32 s59, s59, 3\n" \
  "s_lshl_b32 s59, s59, 19\n" \
  "s_lshl_b32 s60, %7, 21\n" \
  "s_add_u32 s59, s59, s60\n" \
  "s_add_u32 s59, s59, 262144\n" \
  "s_add_u32 m0, s88, 0\n" \
  "s_nop 0\n" \
  "buffer_load_dwordx4 v32, s[72:75], s59 offen lds\n" \
  "s_add_u32 m0, s88, 4096\n" \
  "s_nop 0\n" \
  "buffer_load_dwordx4 v32, s[76:79], s59 offen lds\n" \
  "s_add_u32 s59, s59, 65536\n" \
  "s_add_u32 m0, s88, 1024\n" \
  "s_nop 0\n" \
  "buffer_load_dwordx4 v33, s[72:75], s59 offen lds\n" \
  "s_add_u32 m0, s88, 5120\n" \
  "s_nop 0\n" \
  "buffer_load_dwordx4 v33, s[76:79], s59 offen lds\n" \
  "s_add_u32 s59, s59, 65536\n" \
  "s_add_u32 m0, s88, 2048\n" \
  "s_nop 0\n" \
  "buffer_load_dwordx4 v32, s[72:75], s59 offen lds\n" \
  "s_add_u32 m0, s88, 6144\n" \
  "s_nop 0\n" \
  "buffer_load_dwordx4 v32, s[76:79], s59 offen lds\n" \
  "s_add_u32 s59, s59, 65536\n" \
  "s_add_u32 m0, s88, 3072\n" \
  "s_nop 0\n" \
  "buffer_load_dwordx4 v33, s[72:75], s59 offen lds\n" \
  "s_add_u32 m0, s88, 7168\n" \
  "s_nop 0\n" \
  "buffer_load_dwordx4 v33, s[76:79], s59 offen lds\n" \
  "s_waitcnt vmcnt(40)\n" \
  "ds_read_b64_tr_b16 a[4:5], v52 offset:8192\n" \
  "ds_read_b64_tr_b16 a[6:7], v52 offset:8448\n" \
  "ds_read_b64_tr_b16 a[12:13], v53 offset:8192\n" \
  "ds_read_b64_tr_b16 a[14:15], v53 offset:8448\n" \
  "ds_read_b64_tr_b16 a[20:21], v54 offset:8192\n" \
  "ds_read_b64_tr_b16 a[22:23], v54 offset:8448\n" \
  "ds_read_b64_tr_b16 a[28:29], v55 offset:8192\n" \
  "ds_read_b64_tr_b16 a[30:31], v55 offset:8448\n" \
  "s_waitcnt lgkmcnt(0)\n" \
  "s_lshr_b32 s59, %10, 1\n" \
  "s_add_u32 s59, s59, %7\n" \
  "s_add_u32 s59, s59, 3\n" \
  "s_and_b32 s59, s59, 3\n" \
  "s_lshl_b32 s59, s59, 19\n" \
  "s_lshl_b32 s60, %7, 21\n" \
  "s_add_u32 s59, s59, s60\n" \
  "s_add_u32 s59, s59, 0\n" \
  "s_add_u32 m0, s88, 8192\n" \
  "s_nop 0\n" \
  "buffer_load_dwordx4 v32, s[72:75], s59 offen lds\n" \
  "s_add_u32 m0, s88, 12288\n" \
  "s_nop 0\n" \
  "buffer_load_dwordx4 v32, s[76:79], s59 offen lds\n" \
  "s_add_u32 s59, s59, 65536\n" \
  "s_add_u32 m0, s88, 9216\n" \
  "s_nop 0\n" \
  "buffer_load_dwordx4 v33, s[72:75], s59 offen lds\n" \
  "s_add_u32 m0, s88, 13312\n" \
  "s_nop 0\n" \
  "buffer_load_dwordx4 v33, s[76:79], s59 offen lds\n" \
  "s_add_u32 s59, s59, 65536\n" \
  "s_add_u32 m0, s88, 10240\n" \
  "s_nop 0\n" \
  "buffer_load_dwordx4 v32, s[72:75], s59 offen lds\n" \
  "s_add_u32 m0, s88, 14336\n" \
  "s_nop 0\n" \
  "buffer_load_dwordx4 v32, s[76:79], s59 offen lds\n" \
  "s_add_u32 s59, s59, 65536\n" \
  "s_add_u32 m0, s88, 11264\n" \
  "s_nop 0\n" \
  "buffer_load_dwordx4 v33, s[72:75], s59 offen lds\n" \
  "s_add_u32 m0, s88, 15360\n" \
  "s_nop 0\n" \
  "buffer_load_dwordx4 v33, s[76:79], s59 offen lds\n" \
  "s_waitcnt vmcnt(40)\n" \
  "ds_read_b64_tr_b16 a[32:33], v52 offset:16384\n" \
  "ds_read_b64_tr_b16 a[34:35], v52 offset:16640\n" \
  "ds_read_b64_tr_b16 a[40:41], v53 offset:16384\n" \
  "ds_read_b64_tr_b16 a[42:43], v53 offset:16640\n" \
  "ds_read_b64_tr_b16 a[48:49], v54 offset:16384\n" \
  "ds_read_b64_tr_b16 a[50:51], v54 offset:16640\n" \
  "ds_read_b64_tr_b16 a[56:57], v55 offset:16384\n" \
  "ds_read_b64_tr_b16 a[58:59], v55 offset:16640\n" \
  "s_waitcnt lgkmcnt(0)\n" \
  "s_lshr_b32 s59, %10, 1\n" \
  "s_add_u32 s59, s59, %7\n" \
  "s_add_u32 s59, s59, 3\n" \
  "s_and_b32 s59, s59, 3\n" \
  "s_lshl_b32 s59, s59, 19\n" \
  "s_lshl_b32 s60, %7, 21\n" \
  "s_add_u32 s59, s59, s60\n" \
  "s_add_u32 s59, s59, 262144\n" \
  "s_add_u32 m0, s88, 16384\n" \
  "s_nop 0\n" \
  "buffer_load_dwordx4 v32, s[72:75], s59 offen lds\n" \
  "s_add_u32 m0, s88, 20480\n" \
  "s_nop 0\n" \
  "buffer_load_dwordx4 v32, s[76:79], s59 offen lds\n" \
  "s_add_u32 s59, s59, 65536\n" \
  "s_add_u32 m0, s88, 17408\n" \
  "s_nop 0\n" \
  "buffer_load_dwordx4 v33, s[72:75], s59 offen lds\n" \
  "s_add_u32 m0, s88, 21504\n" \
  "s_nop 0\n" \
  "buffer_load_dwordx4 v33, s[76:79], s59 offen lds\n" \
  "s_add_u32 s59, s59, 65536\n" \
  "s_add_u32 m0, s88, 18432\n" \
  "s_nop 0\n" \
  "buffer_load_dwordx4 v32, s[72:75], s59 offen lds\n" \
  "s_add_u32 m0, s88, 22528\n" \
  "s_nop 0\n" \
  "buffer_load_dwordx4 v32, s[76:79], s59 offen lds\n" \
  "s_add_u32 s59, s59, 65536\n" \
  "s_add_u32 m0, s88, 19456\n" \
  "s_nop 0\n" \
  "buffer_load_dwordx4 v33, s[72:75], s59 offen lds\n" \
  "s_add_u32 m0, s88, 23552\n" \
  "s_nop 0\n" \
  "buffer_load_dwordx4 v33, s[76:79], s59 offen lds\n" \
  "s_waitcnt vmcnt(32)\n" \
  "ds_read_b64_tr_b16 a[36:37], v52 offset:24576\n" \
  "ds_read_b64_tr_b16 a[38:39], v52 offset:24832\n" \
  "ds_read_b64_tr_b16 a[44:45], v53 offset:24576\n" \
  "ds_read_b64_tr_b16 a[46:47], v53 offset:24832\n" \
  "ds_read_b64_tr_b16 a[52:53], v54 offset:24576\n" \
  "ds_read_b64_tr_b16 a[54:55], v54 offset:24832\n" \
  "ds_read_b64_tr_b16 a[60:61], v55 offset:24576\n" \
  "ds_read_b64_tr_b16 a[62:63], v55 offset:24832\n" \
  "s_barrier\n" \
  "v_mov_b32 v27, " STR(CW_INIT) "*4\n" \
  "v_mov_b32 v28, 1\n" \
  "v_or_b32 v25, %7, v0\n" \
  "v_lshl_add_u32 v27, v25, 24, v27\n" \
  "buffer_atomic_add v28, v27, s[44:47], 0 offen\n" \
  "s_waitcnt lgkmcnt(0)\n" \
  "s_lshr_b32 s59, %10, 1\n" \
  "s_add_u32 s59, s59, %7\n" \
  "s_add_u32 s59, s59, 0\n" \
  "s_and_b32 s59, s59, 3\n" \
  "s_lshl_b32 s59, s59, 19\n" \
  "s_lshl_b32 s60, %7, 21\n" \
  "s_add_u32 s59, s59, s60\n" \
  "s_add_u32 s59, s59, 0\n" \
  "s_add_u32 m0, s88, 24576\n" \
  "s_nop 0\n" \
  "buffer_load_dwordx4 v32, s[80:83], s59 offen lds\n" \
  "s_add_u32 s59, s59, 65536\n" \
  "s_add_u32 m0, s88, 25600\n" \
  "s_nop 0\n" \
  "buffer_load_dwordx4 v33, s[80:83], s59 offen lds\n" \
  "s_add_u32 s59, s59, 65536\n" \
  "s_add_u32 m0, s88, 26624\n" \
  "s_nop 0\n" \
  "buffer_load_dwordx4 v32, s[80:83], s59 offen lds\n" \
  "s_add_u32 s59, s59, 65536\n" \
  "s_add_u32 m0, s88, 27648\n" \
  "s_nop 0\n" \
  "buffer_load_dwordx4 v33, s[80:83], s59 offen lds\n" \
  "s_add_u32 s59, s59, 65536\n" \
  "s_add_u32 m0, s88, 28672\n" \
  "s_nop 0\n" \
  "buffer_load_dwordx4 v32, s[80:83], s59 offen lds\n" \
  "s_add_u32 s59, s59, 65536\n" \
  "s_add_u32 m0, s88, 29696\n" \
  "s_nop 0\n" \
  "buffer_load_dwordx4 v33, s[80:83], s59 offen lds\n" \
  "s_add_u32 s59, s59, 65536\n" \
  "s_add_u32 m0, s88, 30720\n" \
  "s_nop 0\n" \
  "buffer_load_dwordx4 v32, s[80:83], s59 offen lds\n" \
  "s_add_u32 s59, s59, 65536\n" \
  "s_add_u32 m0, s88, 31744\n" \
  "s_nop 0\n" \
  "buffer_load_dwordx4 v33, s[80:83], s59 offen lds\n" \
  "s_waitcnt vmcnt(33)\n" \
  "ds_read_b64_tr_b16 a[64:65], v52 offset:32768\n" \
  "ds_read_b64_tr_b16 a[66:67], v52 offset:33024\n" \
  "ds_read_b64_tr_b16 a[72:73], v53 offset:32768\n" \
  "ds_read_b64_tr_b16 a[74:75], v53 offset:33024\n" \
  "ds_read_b64_tr_b16 a[80:81], v54 offset:32768\n" \
  "ds_read_b64_tr_b16 a[82:83], v54 offset:33024\n" \
  "ds_read_b64_tr_b16 a[88:89], v55 offset:32768\n" \
  "ds_read_b64_tr_b16 a[90:91], v55 offset:33024\n" \
  "s_waitcnt lgkmcnt(0)\n" \
  "s_lshr_b32 s59, %10, 1\n" \
  "s_add_u32 s59, s59, %7\n" \
  "s_add_u32 s59, s59, 1\n" \
  "s_and_b32 s59, s59, 3\n" \
  "s_lshl_b32 s59, s59, 19\n" \
  "s_lshl_b32 s60, %7, 21\n" \
  "s_add_u32 s59, s59, s60\n" \
  "s_add_u32 s59, s59, 0\n" \
  "s_add_u32 m0, s88, 32768\n" \
  "s_nop 0\n" \
  "buffer_load_dwordx4 v32, s[80:83], s59 offen lds\n" \
  "s_add_u32 s59, s59, 65536\n" \
  "s_add_u32 m0, s88, 33792\n" \
  "s_nop 0\n" \
  "buffer_load_dwordx4 v33, s[80:83], s59 offen lds\n" \
  "s_add_u32 s59, s59, 65536\n" \
  "s_add_u32 m0, s88, 34816\n" \
  "s_nop 0\n" \
  "buffer_load_dwordx4 v32, s[80:83], s59 offen lds\n" \
  "s_add_u32 s59, s59, 65536\n" \
  "s_add_u32 m0, s88, 35840\n" \
  "s_nop 0\n" \
  "buffer_load_dwordx4 v33, s[80:83], s59 offen lds\n" \
  "s_add_u32 s59, s59, 65536\n" \
  "s_add_u32 m0, s88, 36864\n" \
  "s_nop 0\n" \
  "buffer_load_dwordx4 v32, s[80:83], s59 offen lds\n" \
  "s_add_u32 s59, s59, 65536\n" \
  "s_add_u32 m0, s88, 37888\n" \
  "s_nop 0\n" \
  "buffer_load_dwordx4 v33, s[80:83], s59 offen lds\n" \
  "s_add_u32 s59, s59, 65536\n" \
  "s_add_u32 m0, s88, 38912\n" \
  "s_nop 0\n" \
  "buffer_load_dwordx4 v32, s[80:83], s59 offen lds\n" \
  "s_add_u32 s59, s59, 65536\n" \
  "s_add_u32 m0, s88, 39936\n" \
  "s_nop 0\n" \
  "buffer_load_dwordx4 v33, s[80:83], s59 offen lds\n" \
  "s_waitcnt vmcnt(33)\n" \
  "ds_read_b64_tr_b16 a[68:69], v52 offset:0\n" \
  "ds_read_b64_tr_b16 a[70:71], v52 offset:256\n" \
  "ds_read_b64_tr_b16 a[76:77], v53 offset:0\n" \
  "ds_read_b64_tr_b16 a[78:79], v53 offset:256\n" \
  "ds_read_b64_tr_b16 a[84:85], v54 offset:0\n" \
  "ds_read_b64_tr_b16 a[86:87], v54 offset:256\n" \
  "ds_read_b64_tr_b16 a[92:93], v55 offset:0\n" \
  "ds_read_b64_tr_b16 a[94:95], v55 offset:256\n" \
  "s_waitcnt lgkmcnt(0)\n" \
  "s_lshr_b32 s59, %10, 1\n" \
  "s_add_u32 s59, s59, %7\n" \
  "s_add_u32 s59, s59, 2\n" \
  "s_and_b32 s59, s59, 3\n" \
  "s_lshl_b32 s59, s59, 19\n" \
  "s_lshl_b32 s60, %7, 21\n" \
  "s_add_u32 s59, s59, s60\n" \
  "s_add_u32 s59, s59, 0\n" \
  "s_add_u32 m0, s88, 0\n" \
  "s_nop 0\n" \
  "buffer_load_dwordx4 v32, s[80:83], s59 offen lds\n" \
  "s_add_u32 s59, s59, 65536\n" \
  "s_add_u32 m0, s88, 1024\n" \
  "s_nop 0\n" \
  "buffer_load_dwordx4 v33, s[80:83], s59 offen lds\n" \
  "s_add_u32 s59, s59, 65536\n" \
  "s_add_u32 m0, s88, 2048\n" \
  "s_nop 0\n" \
  "buffer_load_dwordx4 v32, s[80:83], s59 offen lds\n" \
  "s_add_u32 s59, s59, 65536\n" \
  "s_add_u32 m0, s88, 3072\n" \
  "s_nop 0\n" \
  "buffer_load_dwordx4 v33, s[80:83], s59 offen lds\n" \
  "s_add_u32 s59, s59, 65536\n" \
  "s_add_u32 m0, s88, 4096\n" \
  "s_nop 0\n" \
  "buffer_load_dwordx4 v32, s[80:83], s59 offen lds\n" \
  "s_add_u32 s59, s59, 65536\n" \
  "s_add_u32 m0, s88, 5120\n" \
  "s_nop 0\n" \
  "buffer_load_dwordx4 v33, s[80:83], s59 offen lds\n" \
  "s_add_u32 s59, s59, 65536\n" \
  "s_add_u32 m0, s88, 6144\n" \
  "s_nop 0\n" \
  "buffer_load_dwordx4 v32, s[80:83], s59 offen lds\n" \
  "s_add_u32 s59, s59, 65536\n" \
  "s_add_u32 m0, s88, 7168\n" \
  "s_nop 0\n" \
  "buffer_load_dwordx4 v33, s[80:83], s59 offen lds\n" \
  "s_waitcnt vmcnt(33)\n" \
  "ds_read_b64_tr_b16 a[96:97], v52 offset:8192\n" \
  "ds_read_b64_tr_b16 a[98:99], v52 offset:8448\n" \
  "ds_read_b64_tr_b16 a[104:105], v53 offset:8192\n" \
  "ds_read_b64_tr_b16 a[106:107], v53 offset:8448\n" \
  "ds_read_b64_tr_b16 a[112:113], v54 offset:8192\n" \
  "ds_read_b64_tr_b16 a[114:115], v54 offset:8448\n" \
  "ds_read_b64_tr_b16 a[120:121], v55 offset:8192\n" \
  "ds_read_b64_tr_b16 a[122:123], v55 offset:8448\n" \
  "s_waitcnt lgkmcnt(0)\n" \
  "s_lshr_b32 s59, %10, 1\n" \
  "s_add_u32 s59, s59, %7\n" \
  "s_add_u32 s59, s59, 3\n" \
  "s_and_b32 s59, s59, 3\n" \
  "s_lshl_b32 s59, s59, 19\n" \
  "s_lshl_b32 s60, %7, 21\n" \
  "s_add_u32 s59, s59, s60\n" \
  "s_add_u32 s59, s59, 0\n" \
  "s_add_u32 m0, s88, 8192\n" \
  "s_nop 0\n" \
  "buffer_load_dwordx4 v32, s[80:83], s59 offen lds\n" \
  "s_add_u32 s59, s59, 65536\n" \
  "s_add_u32 m0, s88, 9216\n" \
  "s_nop 0\n" \
  "buffer_load_dwordx4 v33, s[80:83], s59 offen lds\n" \
  "s_add_u32 s59, s59, 65536\n" \
  "s_add_u32 m0, s88, 10240\n" \
  "s_nop 0\n" \
  "buffer_load_dwordx4 v32, s[80:83], s59 offen lds\n" \
  "s_add_u32 s59, s59, 65536\n" \
  "s_add_u32 m0, s88, 11264\n" \
  "s_nop 0\n" \
  "buffer_load_dwordx4 v33, s[80:83], s59 offen lds\n" \
  "s_add_u32 s59, s59, 65536\n" \
  "s_add_u32 m0, s88, 12288\n" \
  "s_nop 0\n" \
  "buffer_load_dwordx4 v32, s[80:83], s59 offen lds\n" \
  "s_add_u32 s59, s59, 65536\n" \
  "s_add_u32 m0, s88, 13312\n" \
  "s_nop 0\n" \
  "buffer_load_dwordx4 v33, s[80:83], s59 offen lds\n" \
  "s_add_u32 s59, s59, 65536\n" \
  "s_add_u32 m0, s88, 14336\n" \
  "s_nop 0\n" \
  "buffer_load_dwordx4 v32, s[80:83], s59 offen lds\n" \
  "s_add_u32 s59, s59, 65536\n" \
  "s_add_u32 m0, s88, 15360\n" \
  "s_nop 0\n" \
  "buffer_load_dwordx4 v33, s[80:83], s59 offen lds\n" \
  "s_waitcnt vmcnt(33)\n" \
  "ds_read_b64_tr_b16 a[100:101], v52 offset:16384\n" \
  "ds_read_b64_tr_b16 a[102:103], v52 offset:16640\n" \
  "ds_read_b64_tr_b16 a[108:109], v53 offset:16384\n" \
  "ds_read_b64_tr_b16 a[110:111], v53 offset:16640\n" \
  "ds_read_b64_tr_b16 a[116:117], v54 offset:16384\n" \
  "ds_read_b64_tr_b16 a[118:119], v54 offset:16640\n" \
  "ds_read_b64_tr_b16 a[124:125], v55 offset:16384\n" \
  "ds_read_b64_tr_b16 a[126:127], v55 offset:16640\n" \
  "s_waitcnt lgkmcnt(0)\n" \
  "s_lshl_b32 s59, %7, 7\n" \
  "s_add_u32 s59, s59, 0\n" \
  "s_add_u32 m0, s88, 16384\n" \
  "s_nop 0\n" \
  "buffer_load_dwordx4 v34, s[84:87], s59 offen lds\n" \
  "s_add_u32 s59, s59, 16384\n" \
  "s_add_u32 m0, s88, 17408\n" \
  "s_nop 0\n" \
  "buffer_load_dwordx4 v34, s[84:87], s59 offen lds\n" \
  "s_add_u32 s59, s59, 16384\n" \
  "s_add_u32 m0, s88, 18432\n" \
  "s_nop 0\n" \
  "buffer_load_dwordx4 v34, s[84:87], s59 offen lds\n" \
  "s_add_u32 s59, s59, 16384\n" \
  "s_add_u32 m0, s88, 19456\n" \
  "s_nop 0\n" \
  "buffer_load_dwordx4 v34, s[84:87], s59 offen lds\n" \
  "s_add_u32 s59, s59, 16384\n" \
  "s_add_u32 m0, s88, 20480\n" \
  "s_nop 0\n" \
  "buffer_load_dwordx4 v34, s[84:87], s59 offen lds\n" \
  "s_add_u32 s59, s59, 16384\n" \
  "s_add_u32 m0, s88, 21504\n" \
  "s_nop 0\n" \
  "buffer_load_dwordx4 v34, s[84:87], s59 offen lds\n" \
  "s_add_u32 s59, s59, 16384\n" \
  "s_add_u32 m0, s88, 22528\n" \
  "s_nop 0\n" \
  "buffer_load_dwordx4 v34, s[84:87], s59 offen lds\n" \
  "s_add_u32 s59, s59, 16384\n" \
  "s_add_u32 m0, s88, 23552\n" \
  "s_nop 0\n" \
  "buffer_load_dwordx4 v34, s[84:87], s59 offen lds\n" \
  "s_waitcnt vmcnt(32)\n" \
  "ds_read_b64_tr_b16 a[128:129], v42 offset:24576\n" \
  "ds_read_b64_tr_b16 a[130:131], v42 offset:24832\n" \
  "ds_read_b64_tr_b16 a[132:133], v42 offset:28672\n" \
  "ds_read_b64_tr_b16 a[134:135], v42 offset:28928\n" \
  "ds_read_b64_tr_b16 a[136:137], v43 offset:24576\n" \
  "ds_read_b64_tr_b16 a[138:139], v43 offset:24832\n" \
  "ds_read_b64_tr_b16 a[140:141], v43 offset:28672\n" \
  "ds_read_b64_tr_b16 a[142:143], v43 offset:28928\n" \
  "s_waitcnt lgkmcnt(0)\n" \
  "s_lshl_b32 s59, %7, 7\n" \
  "s_add_u32 s59, s59, 131072\n" \
  "s_add_u32 m0, s88, 24576\n" \
  "s_nop 0\n" \
  "buffer_load_dwordx4 v34, s[84:87], s59 offen lds\n" \
  "s_add_u32 s59, s59, 16384\n" \
  "s_add_u32 m0, s88, 25600\n" \
  "s_nop 0\n" \
  "buffer_load_dwordx4 v34, s[84:87], s59 offen lds\n" \
  "s_add_u32 s59, s59, 16384\n" \
  "s_add_u32 m0, s88, 26624\n" \
  "s_nop 0\n" \
  "buffer_load_dwordx4 v34, s[84:87], s59 offen lds\n" \
  "s_add_u32 s59, s59, 16384\n" \
  "s_add_u32 m0, s88, 27648\n" \
  "s_nop 0\n" \
  "buffer_load_dwordx4 v34, s[84:87], s59 offen lds\n" \
  "s_add_u32 s59, s59, 16384\n" \
  "s_add_u32 m0, s88, 28672\n" \
  "s_nop 0\n" \
  "buffer_load_dwordx4 v34, s[84:87], s59 offen lds\n" \
  "s_add_u32 s59, s59, 16384\n" \
  "s_add_u32 m0, s88, 29696\n" \
  "s_nop 0\n" \
  "buffer_load_dwordx4 v34, s[84:87], s59 offen lds\n" \
  "s_add_u32 s59, s59, 16384\n" \
  "s_add_u32 m0, s88, 30720\n" \
  "s_nop 0\n" \
  "buffer_load_dwordx4 v34, s[84:87], s59 offen lds\n" \
  "s_add_u32 s59, s59, 16384\n" \
  "s_add_u32 m0, s88, 31744\n" \
  "s_nop 0\n" \
  "buffer_load_dwordx4 v34, s[84:87], s59 offen lds\n" \
  "s_memtime s[72:73]\n" \
  "s_memrealtime s[74:75]\n" \
  "s_nop 1\n" \
  "s_memrealtime s[76:77]\n" \
  "s_nop 1\n" \
  "s_memrealtime s[78:79]\n" \
  "s_nop 1\n" \
  "s_memrealtime s[80:81]\n" \
  "s_nop 1\n" \
  "s_memrealtime s[82:83]\n" \
  "s_nop 1\n" \
  "s_memrealtime s[84:85]\n" \
  "s_nop 1\n" \
  "s_memrealtime s[86:87]\n" \
  "s_nop 1\n" \
  "s_memrealtime s[88:89]\n" \
  "s_nop 1\n" \
  "s_memrealtime s[90:91]\n" \
  "s_mov_b32 s59, 0\n" \
  "s_waitcnt lgkmcnt(0)\n" \
  "s_cmp_eq_u32 s76, s74\n" \
  "s_addc_u32 s59, s59, 0\n" \
  "s_cmp_eq_u32 s78, s74\n" \
  "s_addc_u32 s59, s59, 0\n" \
  "s_cmp_eq_u32 s80, s74\n" \
  "s_addc_u32 s59, s59, 0\n" \
  "s_cmp_eq_u32 s82, s74\n" \
  "s_addc_u32 s59, s59, 0\n" \
  "s_cmp_eq_u32 s84, s74\n" \
  "s_addc_u32 s59, s59, 0\n" \
  "s_cmp_eq_u32 s86, s74\n" \
  "s_addc_u32 s59, s59, 0\n" \
  "s_cmp_eq_u32 s88, s74\n" \
  "s_addc_u32 s59, s59, 0\n" \
  "s_cmp_eq_u32 s90, s74\n" \
  "s_addc_u32 s59, s59, 0\n" \
  "s_mul_i32 s59, s59, 12\n" \
  "s_add_u32 s68, s72, s59\n" \
  "s_add_u32 s59, s74, 4\n" \
  "s_mul_i32 s69, s59, 24\n" \
  "s_mov_b32 s62, s69\n" \
  "s_mov_b32 s56, s68\n" \
  "s_waitcnt vmcnt(32)\n" \
  "ds_read_b64_tr_b16 a[144:145], v42 offset:32768\n" \
  "ds_read_b64_tr_b16 a[146:147], v42 offset:33024\n" \
  "ds_read_b64_tr_b16 a[148:149], v42 offset:36864\n" \
  "ds_read_b64_tr_b16 a[150:151], v42 offset:37120\n" \
  "ds_read_b64_tr_b16 a[152:153], v43 offset:32768\n" \
  "ds_read_b64_tr_b16 a[154:155], v43 offset:33024\n" \
  "ds_read_b64_tr_b16 a[156:157], v43 offset:36864\n" \
  "ds_read_b64_tr_b16 a[158:159], v43 offset:37120\n" \
  "s_waitcnt vmcnt(24)\n" \
  "ds_read_b64_tr_b16 a[160:161], v42 offset:0\n" \
  "ds_read_b64_tr_b16 a[162:163], v42 offset:256\n" \
  "ds_read_b64_tr_b16 a[164:165], v42 offset:4096\n" \
  "ds_read_b64_tr_b16 a[166:167], v42 offset:4352\n" \
  "ds_read_b64_tr_b16 a[168:169], v43 offset:0\n" \
  "ds_read_b64_tr_b16 a[170:171], v43 offset:256\n" \
  "ds_read_b64_tr_b16 a[172:173], v43 offset:4096\n" \
  "ds_read_b64_tr_b16 a[174:175], v43 offset:4352\n" \
  "s_waitcnt vmcnt(16)\n" \
  "ds_read_b64_tr_b16 a[176:177], v42 offset:8192\n" \
  "ds_read_b64_tr_b16 a[178:179], v42 offset:8448\n" \
  "ds_read_b64_tr_b16 a[180:181], v42 offset:12288\n" \
  "ds_read_b64_tr_b16 a[182:183], v42 offset:12544\n" \
  "ds_read_b64_tr_b16 a[184:185], v43 offset:8192\n" \
  "ds_read_b64_tr_b16 a[186:187], v43 offset:8448\n" \
  "ds_read_b64_tr_b16 a[188:189], v43 offset:12288\n" \
  "ds_read_b64_tr_b16 a[190:191], v43 offset:12544\n" \
  "s_waitcnt vmcnt(8)\n" \
  "ds_read_b64_tr_b16 a[192:193], v49 offset:16384\n" \
  "ds_read_b64_tr_b16 a[194:195], v49 offset:16640\n" \
  "ds_read_b64_tr_b16 a[196:197], v49 offset:20480\n" \
  "ds_read_b64_tr_b16 a[198:199], v49 offset:20736\n" \
  "ds_read_b64_tr_b16 a[208:209], v49 offset:16400\n" \
  "ds_read_b64_tr_b16 a[210:211], v49 offset:16656\n" \
  "ds_read_b64_tr_b16 a[212:213], v49 offset:20496\n" \
  "ds_read_b64_tr_b16 a[214:215], v49 offset:20752\n" \
  "ds_read_b64_tr_b16 a[224:225], v49 offset:16392\n" \
  "ds_read_b64_tr_b16 a[226:227], v49 offset:16648\n" \
  "ds_read_b64_tr_b16 a[228:229], v49 offset:20488\n" \
  "ds_read_b64_tr_b16 a[230:231], v49 offset:20744\n" \
  "ds_read_b64_tr_b16 a[240:241], v49 offset:16408\n" \
  "ds_read_b64_tr_b16 a[242:243], v49 offset:16664\n" \
  "ds_read_b64_tr_b16 a[244:245], v49 offset:20504\n" \
  "ds_read_b64_tr_b16 a[246:247], v49 offset:20760\n" \
  "s_waitcnt vmcnt(0)\n" \
  "ds_read_b64_tr_b16 a[200:201], v49 offset:24576\n" \
  "ds_read_b64_tr_b16 a[202:203], v49 offset:24832\n" \
  "ds_read_b64_tr_b16 a[204:205], v49 offset:28672\n" \
  "ds_read_b64_tr_b16 a[206:207], v49 offset:28928\n" \
  "ds_read_b64_tr_b16 a[216:217], v49 offset:24592\n" \
  "ds_read_b64_tr_b16 a[218:219], v49 offset:24848\n" \
  "ds_read_b64_tr_b16 a[220:221], v49 offset:28688\n" \
  "ds_read_b64_tr_b16 a[222:223], v49 offset:28944\n" \
  "ds_read_b64_tr_b16 a[232:233], v49 offset:24584\n" \
  "ds_read_b64_tr_b16 a[234:235], v49 offset:24840\n" \
  "ds_read_b64_tr_b16 a[236:237], v49 offset:28680\n" \
  "ds_read_b64_tr_b16 a[238:239], v49 offset:28936\n" \
  "ds_read_b64_tr_b16 a[248:249], v49 offset:24600\n" \
  "ds_read_b64_tr_b16 a[250:251], v49 offset:24856\n" \
  "ds_read_b64_tr_b16 a[252:253], v49 offset:28696\n" \
  "ds_read_b64_tr_b16 a[254:255], v49 offset:28952\n" \
  "s_waitcnt lgkmcnt(0)\n" \
  "s_barrier\n" \
  "s_cmp_gt_u32 %7, 1\n" \
  "s_cbranch_scc1 LI6_%=\n" \
  "LI2a_%=:\n" \
  "buffer_load_dword v23, v22, s[44:47], 0 offen sc1\n" \
  "s_waitcnt vmcnt(0)\n" \
  "v_cmp_gt_u32 vcc, 1, v23\n" \
  "s_cbranch_vccz LI2b_%=\n" \
  "s_sleep 6\n" \
  "s_add_u32 s58, s58, 1\n" \
  "s_cmp_lt_u32 s58, " STR(SPIN_CAP) "\n" \
  "s_cbranch_scc1 LI2a_%=\n" \
  "v_mov_b32 v27, " STR(CW_DEAD) "*4\n" \
  "v_mov_b32 v28, 1\n" \
  "buffer_store_dword v28, v27, s[44:47], 0 offen sc1\n" \
  "s_branch LI2b_%=\n" \
  "LI2b_%=:\n" \
  "buffer_load_dwordx4 v[200:203], v247, s[40:43], s55 offen offset:0 sc1\n" \
  "buffer_load_dwordx4 v[204:207], v247, s[40:43], s55 offen offset:1024 sc1\n" \
  "buffer_load_dwordx4 v[208:211], v247, s[40:43], s55 offen offset:2048 sc1\n" \
  "buffer_load_dwordx4 v[212:215], v247, s[40:43], s55 offen offset:3072 sc1\n" \
  "buffer_load_dwordx4 v[216:219], v16, s[40:43], s55 offen offset:0 sc1\n" \
  "buffer_load_dwordx4 v[220:223], v16, s[40:43], s55 offen offset:1024 sc1\n" \
  "buffer_load_dwordx4 v[224:227], v16, s[40:43], s55 offen offset:2048 sc1\n" \
  "buffer_load_dwordx4 v[228:231], v16, s[40:43], s55 offen offset:3072 sc1\n" \
  "s_waitcnt vmcnt(0)\n" \
  "ds_write_b128 v18, v[200:203] offset:0\n" \
  "ds_write_b128 v18, v[204:207] offset:1024\n" \
  "ds_write_b128 v18, v[208:211] offset:2048\n" \
  "ds_write_b128 v18, v[212:215] offset:3072\n" \
  "ds_write_b128 v18, v[216:219] offset:4096\n" \
  "ds_write_b128 v18, v[220:223] offset:5120\n" \
  "ds_write_b128 v18, v[224:227] offset:6144\n" \
  "ds_write_b128 v18, v[228:231] offset:7168\n" \
  "s_branch LI4_%=\n" \
  "LI6_%=:\n" \
  "s_cmp_eq_u32 %7, 3\n" \
  "s_cbranch_scc1 LI4_%=\n" \
  "v_mov_b32 v25, " STR(CW_INIT) "*4\n" \
  "LI1a_%=:\n" \
  "buffer_load_dword v23, v25, s[44:47], 0 offen sc1\n" \
  "s_waitcnt vmcnt(0)\n" \
  "v_cmp_gt_u32 vcc, 128, v23\n" \
  "s_cbranch_vccz LI1b_%=\n" \
  "s_sleep 24\n" \
  "s_add_u32 s58, s58, 1\n" \
  "s_cmp_lt_u32 s58, " STR(SPIN_CAP) "\n" \
  "s_cbranch_scc1 LI1a_%=\n" \
  "v_mov_b32 v27, " STR(CW_DEAD) "*4\n" \
  "v_mov_b32 v28, 1\n" \
  "buffer_store_dword v28, v27, s[44:47], 0 offen sc1\n" \
  "s_branch LI1b_%=\n" \
  "LI1b_%=:\n" \
  "LI4_%=:\n" \
  "s_waitcnt lgkmcnt(0)\n" \
  "s_barrier\n" \
  "ds_read_b64_tr_b16 v[136:137], v20 offset:0\n" \
  "ds_read_b64_tr_b16 v[138:139], v21 offset:0\n" \
  "ds_read_b64_tr_b16 v[152:153], v20 offset:8\n" \
  "ds_read_b64_tr_b16 v[154:155], v21 offset:8\n" \
  "ds_read_b64_tr_b16 v[140:141], v20 offset:2048\n" \
  "ds_read_b64_tr_b16 v[142:143], v21 offset:2048\n" \
  "ds_read_b64_tr_b16 v[156:157], v20 offset:2056\n" \
  "ds_read_b64_tr_b16 v[158:159], v21 offset:2056\n" \
  "ds_read_b64_tr_b16 v[144:145], v20 offset:4096\n" \
  "ds_read_b64_tr_b16 v[146:147], v21 offset:4096\n" \
  "ds_read_b64_tr_b16 v[160:161], v20 offset:4104\n" \
  "ds_read_b64_tr_b16 v[162:163], v21 offset:4104\n" \
  "ds_read_b64_tr_b16 v[148:149], v20 offset:6144\n" \
  "ds_read_b64_tr_b16 v[150:151], v21 offset:6144\n" \
  "ds_read_b64_tr_b16 v[164:165], v20 offset:6152\n" \
  "ds_read_b64_tr_b16 v[166:167], v21 offset:6152\n" \
  "ds_read_b128 v[104:107], v84 offset:0\n" \
  "ds_read_b128 v[108:111], v85 offset:0\n" \
  "ds_read_b128 v[112:115], v86 offset:0\n" \
  "ds_read_b128 v[116:119], v87 offset:0\n" \
  "ds_read_b128 v[120:123], v84 offset:4096\n" \
  "ds_read_b128 v[124:127], v85 offset:4096\n" \
  "ds_read_b128 v[128:131], v86 offset:4096\n" \
  "ds_read_b128 v[132:135], v87 offset:4096\n" \
  "s_waitcnt lgkmcnt(0)\n" \
  "v_mfma_f32_16x16x32_bf16 v[184:187], v[136:139], a[224:227], 0\n" \
  "v_mfma_f32_16x16x32_bf16 v[188:191], v[152:155], a[224:227], 0\n" \
  "v_mfma_f32_16x16x32_bf16 v[192:195], v[136:139], a[240:243], 0\n" \
  "v_mfma_f32_16x16x32_bf16 v[196:199], v[152:155], a[240:243], 0\n" \
  "v_mfma_f32_16x16x32_bf16 v[184:187], v[140:143], a[228:231], v[184:187]\n" \
  "v_mfma_f32_16x16x32_bf16 v[188:191], v[156:159], a[228:231], v[188:191]\n" \
  "v_mfma_f32_16x16x32_bf16 v[192:195], v[140:143], a[244:247], v[192:195]\n" \
  "v_mfma_f32_16x16x32_bf16 v[196:199], v[156:159], a[244:247], v[196:199]\n" \
  "v_mfma_f32_16x16x32_bf16 v[184:187], v[144:147], a[232:235], v[184:187]\n" \
  "v_mfma_f32_16x16x32_bf16 v[188:191], v[160:163], a[232:235], v[188:191]\n" \
  "v_mfma_f32_16x16x32_bf16 v[192:195], v[144:147], a[248:251], v[192:195]\n" \
  "v_mfma_f32_16x16x32_bf16 v[196:199], v[160:163], a[248:251], v[196:199]\n" \
  "v_mfma_f32_16x16x32_bf16 v[184:187], v[148:151], a[236:239], v[184:187]\n" \
  "v_mfma_f32_16x16x32_bf16 v[188:191], v[164:167], a[236:239], v[188:191]\n" \
  "v_mfma_f32_16x16x32_bf16 v[192:195], v[148:151], a[252:255], v[192:195]\n" \
  "v_mfma_f32_16x16x32_bf16 v[196:199], v[164:167], a[252:255], v[196:199]\n" \
  "s_nop 7\n" \
  "v_cvt_pk_bf16_f32 v176, v184, v185\n" \
  "v_cvt_pk_bf16_f32 v177, v186, v187\n" \
  "v_cvt_pk_bf16_f32 v178, v188, v189\n" \
  "v_cvt_pk_bf16_f32 v179, v190, v191\n" \
  "v_cvt_pk_bf16_f32 v180, v192, v193\n" \
  "v_cvt_pk_bf16_f32 v181, v194, v195\n" \
  "v_cvt_pk_bf16_f32 v182, v196, v197\n" \
  "v_cvt_pk_bf16_f32 v183, v198, v199\n" \
  "v_mfma_f32_16x16x32_bf16 v[184:187], v[136:139], a[192:195], 0\n" \
  "v_mfma_f32_16x16x32_bf16 v[188:191], v[152:155], a[192:195], 0\n" \
  "v_mfma_f32_16x16x32_bf16 v[192:195], v[136:139], a[208:211], 0\n" \
  "v_mfma_f32_16x16x32_bf16 v[196:199], v[152:155], a[208:211], 0\n" \
  "v_mfma_f32_16x16x32_bf16 v[184:187], v[140:143], a[196:199], v[184:187]\n" \
  "v_mfma_f32_16x16x32_bf16 v[188:191], v[156:159], a[196:199], v[188:191]\n" \
  "v_mfma_f32_16x16x32_bf16 v[192:195], v[140:143], a[212:215], v[192:195]\n" \
  "v_mfma_f32_16x16x32_bf16 v[196:199], v[156:159], a[212:215], v[196:199]\n" \
  "v_mfma_f32_16x16x32_bf16 v[184:187], v[144:147], a[200:203], v[184:187]\n" \
  "v_mfma_f32_16x16x32_bf16 v[188:191], v[160:163], a[200:203], v[188:191]\n" \
  "v_mfma_f32_16x16x32_bf16 v[192:195], v[144:147], a[216:219], v[192:195]\n" \
  "v_mfma_f32_16x16x32_bf16 v[196:199], v[160:163], a[216:219], v[196:199]\n" \
  "v_mfma_f32_16x16x32_bf16 v[184:187], v[148:151], a[204:207], v[184:187]\n" \
  "v_mfma_f32_16x16x32_bf16 v[188:191], v[164:167], a[204:207], v[188:191]\n" \
  "v_mfma_f32_16x16x32_bf16 v[192:195], v[148:151], a[220:223], v[192:195]\n" \
  "v_mfma_f32_16x16x32_bf16 v[196:199], v[164:167], a[220:223], v[196:199]\n" \
  "s_memtime s[72:73]\n" \
  "s_memrealtime s[74:75]\n" \
  "s_nop 1\n" \
  "s_memrealtime s[76:77]\n" \
  "s_nop 1\n" \
  "s_memrealtime s[78:79]\n" \
  "s_nop 1\n" \
  "s_memrealtime s[80:81]\n" \
  "s_nop 1\n" \
  "s_memrealtime s[82:83]\n" \
  "s_nop 1\n" \
  "s_memrealtime s[84:85]\n" \
  "s_nop 1\n" \
  "s_memrealtime s[86:87]\n" \
  "s_nop 1\n" \
  "s_memrealtime s[88:89]\n" \
  "s_nop 1\n" \
  "s_memrealtime s[90:91]\n" \
  "s_mov_b32 s59, 0\n" \
  "s_waitcnt lgkmcnt(0)\n" \
  "s_cmp_eq_u32 s76, s74\n" \
  "s_addc_u32 s59, s59, 0\n" \
  "s_cmp_eq_u32 s78, s74\n" \
  "s_addc_u32 s59, s59, 0\n" \
  "s_cmp_eq_u32 s80, s74\n" \
  "s_addc_u32 s59, s59, 0\n" \
  "s_cmp_eq_u32 s82, s74\n" \
  "s_addc_u32 s59, s59, 0\n" \
  "s_cmp_eq_u32 s84, s74\n" \
  "s_addc_u32 s59, s59, 0\n" \
  "s_cmp_eq_u32 s86, s74\n" \
  "s_addc_u32 s59, s59, 0\n" \
  "s_cmp_eq_u32 s88, s74\n" \
  "s_addc_u32 s59, s59, 0\n" \
  "s_cmp_eq_u32 s90, s74\n" \
  "s_addc_u32 s59, s59, 0\n" \
  "s_mul_i32 s59, s59, 12\n" \
  "s_add_u32 s68, s72, s59\n" \
  "s_add_u32 s59, s74, 4\n" \
  "s_mul_i32 s69, s59, 24\n" \
  "v_cvt_pk_bf16_f32 v168, v184, v185\n" \
  "v_cvt_pk_bf16_f32 v169, v186, v187\n" \
  "v_cvt_pk_bf16_f32 v170, v188, v189\n" \
  "v_cvt_pk_bf16_f32 v171, v190, v191\n" \
  "v_cvt_pk_bf16_f32 v172, v192, v193\n" \
  "v_cvt_pk_bf16_f32 v173, v194, v195\n" \
  "v_cvt_pk_bf16_f32 v174, v196, v197\n" \
  "v_cvt_pk_bf16_f32 v175, v198, v199\n" \
  "v_mov_b32 v95, " STR(RI0) "\n" \
  "s_sub_u32 s60, s69, s62\n" \
  "s_sub_u32 s59, s68, s56\n" \
  "s_mov_b32 s62, s69\n" \
  "s_mov_b32 s56, s68\n" \
  "v_cvt_f32_u32 v27, s59\n" \
  "v_cvt_f32_u32 v28, s60\n" \
  "v_rcp_f32 v26, v27\n" \
  "v_rcp_f32 v19, v28\n" \
  "v_mul_f32 v26, v26, v28\n" \
  "v_mul_f32 v95, " STR(DECAY) ", v95\n" \
  "v_mul_f32 v19, v19, v27\n" \
  "v_max_f32 v95, v95, v26\n" \
  "v_cvt_f32_u32 v28, " STR(F_P) "\n" \
  "v_mul_f32 v27, v95, v28\n" \
  "v_cvt_u32_f32 v27, v27\n" \
  "v_min_u32 v27, 0x7fff, v27\n" \
  "s_nop 0\n" \
  "v_readfirstlane_b32 s63, v27\n" \
  "s_add_u32 s59, s74, 4+63\n" \
  "s_and_b32 s59, s59, -64\n" \
  "s_mul_i32 s59, s59, 24\n" \
  "s_sub_u32 s59, s59, s69\n" \
  "v_cvt_f32_u32 v27, s59\n" \
  "v_mul_f32 v28, 0x44c00000, v19\n" \
  "v_mul_f32 v27, v27, v19\n" \
  "v_cvt_u32_f32 v28, v28\n" \
  "v_cvt_u32_f32 v27, v27\n" \
  "v_max_u32 v28, " STR(T_Q) ", v28\n" \
  "v_min_u32 v28, 2047, v28\n" \
  "v_readfirstlane_b32 s59, v27\n" \
  "v_readfirstlane_b32 s95, v28\n" \
  "v_subrev_u32 v28, " STR(T_Q) ", v28\n" \
  "v_lshlrev_b32 v28, 2, v28\n" \
  "v_mov_b32 v27, 0\n" \
  "s_add_u32 s59, s59, s68\n" \
  "s_cmp_lg_u32 %7, 1\n" \
  "s_cbranch_scc1 LI8_%=\n" \
  "ds_write_b32 v24, v28 offset:14344\n" \
  "ds_write_b32 v24, v27 offset:14356\n" \
  "v_mov_b32 v92, s59\n" \
  "v_mov_b32 v93, 0\n" \
  "ds_write_b64 v24, v[92:93] offset:14336\n" \
  "LI8_%=:\n" \
  "s_waitcnt lgkmcnt(0)\n" \
  "s_add_u32 s49, s53, s93\n" \
  "s_lshl_b32 s59, s70, 7\n" \
  "s_add_u32 s59, s59, " STR(CW_PROG) "*4\n" \
  "s_lshl_b32 s60, s71, 2\n" \
  "s_or_b32 s60, s60, %7\n" \
  "s_cmp_eq_u32 s60, 3\n" \
  "s_cselect_b32 s59, s59, 0x80000000\n" \
  "v_lshl_add_u32 v0, v0, 24, s59\n" \
  "s_lshr_b32 s59, %7, 1\n" \
  "s_xor_b32 s59, s59, 1\n" \
  "s_lshl_b32 s59, s59, 31\n" \
  "v_or_b32 v22, s59, v22\n" \
  "v_mov_b64 v[48:49], 0\n" \
  "v_mov_b64 v[50:51], 0\n" \
  "v_mov_b64 v[52:53], 0\n" \
  "v_mov_b64 v[54:55], 0\n" \
  "v_mov_b64 v[56:57], 0\n" \
  "v_mov_b64 v[58:59], 0\n" \
  "v_mov_b64 v[60:61], 0\n" \
  "v_mov_b64 v[62:63], 0\n" \
  "v_mov_b64 v[64:65], 0\n" \
  "v_mov_b64 v[66:67], 0\n" \
  "v_mov_b64 v[68:69], 0\n" \
  "v_mov_b64 v[70:71], 0\n" \
  "s_cmp_lt_u32 %7, 2\n" \
  "s_cbranch_scc1 LL_%=\n" \
  "LI7a_%=:\n" \
  "buffer_load_dword v23, v22, s[44:47], 0 offen sc1\n" \
  "s_waitcnt vmcnt(0)\n" \
  "v_cmp_gt_u32 vcc, 2, v23\n" \
  "s_cbranch_vccz LI7b_%=\n" \
  "s_sleep 4\n" \
  "s_add_u32 s58, s58, 1\n" \
  "s_cmp_lt_u32 s58, " STR(SPIN_CAP) "\n" \
  "s_cbranch_scc1 LI7a_%=\n" \
  "v_mov_b32 v27, " STR(CW_DEAD) "*4\n" \
  "v_mov_b32 v28, 1\n" \
  "buffer_store_dword v28, v27, s[44:47], 0 offen sc1\n" \
  "s_branch LI7b_%=\n" \
  "LI7b_%=:\n" \
  "buffer_load_dwordx4 v[200:203], v247, s[40:43], s55 offen offset:0 sc1\n" \
  "buffer_load_dwordx4 v[204:207], v247, s[40:43], s55 offen offset:1024 sc1\n" \
  "buffer_load_dwordx4 v[208:211], v247, s[40:43], s55 offen offset:2048 sc1\n" \
  "buffer_load_dwordx4 v[212:215], v247, s[40:43], s55 offen offset:3072 sc1\n" \
  "buffer_load_dwordx4 v[216:219], v16, s[40:43], s55 offen offset:0 sc1\n" \
  "buffer_load_dwordx4 v[220:223], v16, s[40:43], s55 offen offset:1024 sc1\n" \
  "buffer_load_dwordx4 v[224:227], v16, s[40:43], s55 offen offset:2048 sc1\n" \
  "buffer_load_dwordx4 v[228:231], v16, s[40:43], s55 offen offset:3072 sc1\n" \
  "s_branch LM_%=\n" \
  ".p2align 6\n" \
  "LL_%=:\n" \
  "s_barrier\n" \
  "buffer_load_dwordx4 v[32:35], v80, s[36:39], s49 offen sc1\n" \
  "v_mfma_f32_16x16x32_bf16 v[184:187], v[136:139], a[224:227], 0\n" \
  "s_xor_b32 s51, s51, 1<<20\n" \
  "s_xor_b32 s52, s52, 1<<20\n" \
  "v_mfma_f32_16x16x32_bf16 v[188:191], v[152:155], a[224:227], 0\n" \
  "s_add_u32 s53, s53, 32768\n" \
  "s_lshl_b32 s60, s48, 19\n" \
  "v_mfma_f32_16x16x32_bf16 v[192:195], v[136:139], a[240:243], 0\n" \
  "s_add_u32 s60, s60, " STR(EXP_B) "<<21\n" \
  "s_lshr_b32 s59, s60, 21\n" \
  "buffer_load_dwordx4 v[36:39], v81, s[36:39], s49 offen sc1\n" \
  "v_mfma_f32_16x16x32_bf16 v[196:199], v[152:155], a[240:243], 0\n" \
  "s_lshr_b32 s59, %8, s59\n" \
  "s_and_b32 s94, s59, 1\n" \
  "v_mfma_f32_16x16x32_bf16 v[184:187], v[140:143], a[228:231], v[184:187]\n" \
  "s_lshl_b32 s94, s94, 13\n" \
  "s_or_b32 s50, s60, s94\n" \
  "v_mfma_f32_16x16x32_bf16 v[188:191], v[156:159], a[228:231], v[188:191]\n" \
  "s_lshl_b32 s94, s70, 14\n" \
  "s_add_u32 s91, s50, s94\n" \
  "buffer_load_dwordx4 v[40:43], v82, s[36:39], s49 offen sc1\n" \
  "v_mfma_f32_16x16x32_bf16 v[192:195], v[140:143], a[244:247], v[192:195]\n" \
  "s_and_b32 s94, s59, 2\n" \
  "s_lshl_b32 s94, s94, 12\n" \
  "v_mfma_f32_16x16x32_bf16 v[196:199], v[156:159], a[244:247], v[196:199]\n" \
  "s_add_u32 s60, s60, 1<<21\n" \
  "s_or_b32 s54, s60, s94\n" \
  "v_mfma_f32_16x16x32_bf16 v[184:187], v[144:147], a[232:235], v[184:187]\n" \
  "s_xor_b32 s59, %7, 3\n" \
  "s_lshl_b32 s59, s59, 21\n" \
  "buffer_load_dwordx4 v[44:47], v83, s[36:39], s49 offen sc1\n" \
  "v_mfma_f32_16x16x32_bf16 v[188:191], v[160:163], a[232:235], v[188:191]\n" \
  "s_add_u32 s59, s59, s53\n" \
  "s_add_u32 s59, s59, 4<<15\n" \
  "v_mfma_f32_16x16x32_bf16 v[192:195], v[144:147], a[248:251], v[192:195]\n" \
  "s_cmp_lt_u32 s59, 1<<21\n" \
  "s_cselect_b32 s59, s59, 1<<30\n" \
  "v_mfma_f32_16x16x32_bf16 v[196:199], v[160:163], a[248:251], v[196:199]\n" \
  "v_mfma_f32_16x16x32_bf16 v[184:187], v[148:151], a[236:239], v[184:187]\n" \
  "v_mfma_f32_16x16x32_bf16 v[188:191], v[164:167], a[236:239], v[188:191]\n" \
  "v_add_u32 v27, s59, v12\n" \
  "buffer_store_dwordx4 v[88:91], v11, s[36:39], s52 offen sc1\n" \
  "buffer_store_dwordx4 v[88:91], v15, s[36:39], s54 offen sc1\n" \
  "buffer_store_dwordx4 v[88:91], v27, s[36:39], 0 offen sc1\n" \
  "buffer_load_dword v23, v22, s[44:47], 0 offen sc1\n" \
  "v_mfma_f32_16x16x32_bf16 v[192:195], v[148:151], a[252:255], v[192:195]\n" \
  "v_mfma_f32_16x16x32_bf16 v[196:199], v[164:167], a[252:255], v[196:199]\n" \
  "s_nop 6\n" \
  "v_cvt_pk_bf16_f32 v176, v184, v185\n" \
  "v_cvt_pk_bf16_f32 v177, v186, v187\n" \
  "v_cvt_pk_bf16_f32 v178, v188, v189\n" \
  "v_cvt_pk_bf16_f32 v179, v190, v191\n" \
  "v_cvt_pk_bf16_f32 v180, v192, v193\n" \
  "v_cvt_pk_bf16_f32 v181, v194, v195\n" \
  "v_cvt_pk_bf16_f32 v182, v196, v197\n" \
  "v_cvt_pk_bf16_f32 v183, v198, v199\n" \
  "LS1_%=:\n" \
  "s_waitcnt vmcnt(7)\n" \
  "v_smfmac_f32_16x16x64_bf16 v[52:55], v[32:35], a[8:15], v2\n" \
  "v_smfmac_f32_16x16x64_bf16 v[56:59], v[32:35], a[16:23], v2\n" \
  "v_smfmac_f32_16x16x64_bf16 v[60:63], v[32:35], a[24:31], v2\n" \
  "v_smfmac_f32_16x16x64_bf16 v[48:51], v[32:35], a[0:7], v2\n" \
  "s_waitcnt vmcnt(6)\n" \
  "v_smfmac_f32_16x16x64_bf16 v[52:55], v[36:39], a[40:47], v2\n" \
  "v_smfmac_f32_16x16x64_bf16 v[56:59], v[36:39], a[48:55], v2\n" \
  "v_smfmac_f32_16x16x64_bf16 v[60:63], v[36:39], a[56:63], v2\n" \
  "v_smfmac_f32_16x16x64_bf16 v[48:51], v[36:39], a[32:39], v2\n" \
  "s_waitcnt vmcnt(5)\n" \
  "v_smfmac_f32_16x16x64_bf16 v[52:55], v[40:43], a[72:79], v2\n" \
  "v_smfmac_f32_16x16x64_bf16 v[56:59], v[40:43], a[80:87], v2\n" \
  "v_smfmac_f32_16x16x64_bf16 v[60:63], v[40:43], a[88:95], v2\n" \
  "v_smfmac_f32_16x16x64_bf16 v[48:51], v[40:43], a[64:71], v2\n" \
  "s_waitcnt vmcnt(4)\n" \
  "v_smfmac_f32_16x16x64_bf16 v[52:55], v[44:47], a[104:111], v2\n" \
  "v_smfmac_f32_16x16x64_bf16 v[56:59], v[44:47], a[112:119], v2\n" \
  "v_smfmac_f32_16x16x64_bf16 v[60:63], v[44:47], a[120:127], v2\n" \
  "v_add_f32 v52, v52, v54\n" \
  "v_add_f32 v53, v53, v55\n" \
  "v_smfmac_f32_16x16x64_bf16 v[48:51], v[44:47], a[96:103], v2\n" \
  "v_add_f32 v56, v56, v58\n" \
  "v_add_f32 v57, v57, v59\n" \
  "ds_write_b64 v3, v[52:53] offset:0\n" \
  "v_add_f32 v60, v60, v62\n" \
  "v_add_f32 v61, v61, v63\n" \
  "v_smfmac_f32_16x16x64_bf16 v[64:67], v[32:35], a[128:135], v2\n" \
  "ds_write_b64 v244, v[56:57] offset:512\n" \
  "v_smfmac_f32_16x16x64_bf16 v[68:71], v[32:35], a[136:143], v2\n" \
  "ds_write_b64 v245, v[60:61] offset:1024\n" \
  "v_cmp_u_f32 vcc, v52, v53\n" \
  "v_smfmac_f32_16x16x64_bf16 v[64:67], v[36:39], a[144:151], v2\n" \
  "v_add_f32 v48, v48, v50\n" \
  "v_add_f32 v49, v49, v51\n" \
  "v_smfmac_f32_16x16x64_bf16 v[68:71], v[36:39], a[152:159], v2\n" \
  "v_smfmac_f32_16x16x64_bf16 v[64:67], v[40:43], a[160:167], v2\n" \
  "s_cbranch_vccnz LA_%=\n" \
  "LA4_%=:\n" \
  "s_waitcnt lgkmcnt(0)\n" \
  "s_barrier\n" \
  "ds_read_b64 v[72:73], v6 offset:0\n" \
  "ds_read_b64 v[74:75], v6 offset:512\n" \
  "ds_read_b64 v[76:77], v6 offset:1024\n" \
  "ds_read_b96 v[92:94], v24 offset:14336\n" \
  "v_smfmac_f32_16x16x64_bf16 v[68:71], v[40:43], a[168:175], v2\n" \
  "v_smfmac_f32_16x16x64_bf16 v[64:67], v[44:47], a[176:183], v2\n" \
  "s_waitcnt lgkmcnt(3)\n" \
  "v_add_f32 v48, v48, v72\n" \
  "v_add_f32 v49, v49, v73\n" \
  "s_waitcnt lgkmcnt(1)\n" \
  "v_add_f32 v74, v74, v76\n" \
  "v_add_f32 v75, v75, v77\n" \
  "v_add_f32 v48, v48, v74\n" \
  "v_add_f32 v49, v49, v75\n" \
  "v_cvt_pk_bf16_f32 v27, v48, v49\n" \
  "v_smfmac_f32_16x16x64_bf16 v[68:71], v[44:47], a[184:191], v2\n" \
  "v_mov_b32_dpp v28, v27 quad_perm:[1,0,3,2] row_mask:0xf bank_mask:0xf\n" \
  "v_perm_b32 v27, v28, v27, v246\n" \
  "buffer_store_dword v27, v8, s[36:39], s50 offen sc1\n" \
  "v_add_f32 v64, v64, v66\n" \
  "v_add_f32 v65, v65, v67\n" \
  "s_nop 2\n" \
  "v_add_f32 v68, v68, v70\n" \
  "v_add_f32 v69, v69, v71\n" \
  "ds_write2_b32 v5, v64, v65 offset0:0 offset1:40\n" \
  "ds_write2_b32 v5, v68, v69 offset0:16 offset1:56\n" \
  "v_mov_b64 v[184:185], 0\n" \
  "s_add_u32 s49, s53, s93\n" \
  "s_nop 1\n" \
  "v_mov_b64 v[186:187], 0\n" \
  "s_nop 1\n" \
  "v_mov_b64 v[188:189], 0\n" \
  "s_waitcnt lgkmcnt(0)\n" \
  "s_barrier\n" \
  "s_memtime s[66:67]\n" \
  "ds_read_b64 v[72:73], v7 offset:0\n" \
  "ds_read_b64 v[74:75], v7 offset:1280\n" \
  "ds_read_b64 v[76:77], v7 offset:2560\n" \
  "ds_read_b64 v[78:79], v7 offset:3840\n" \
  "v_readfirstlane_b32 s64, v92\n" \
  "v_readfirstlane_b32 s59, v93\n" \
  "v_readfirstlane_b32 s60, v94\n" \
  "v_mov_b64 v[190:191], 0\n" \
  "s_add_u32 s64, s64, s59\n" \
  "s_add_u32 s61, s64, " STR(F_B) "\n" \
  "v_mov_b64 v[192:193], 0\n" \
  "s_cmp_eq_u32 s48, 63\n" \
  "s_waitcnt lgkmcnt(0)\n" \
  "v_pk_add_f32 v[72:73], v[72:73], v[74:75]\n" \
  "v_pk_add_f32 v[76:77], v[76:77], v[78:79]\n" \
  "v_pk_add_f32 v[72:73], v[72:73], v[76:77]\n" \
  "v_cvt_pk_bf16_f32 v27, v72, v73\n" \
  "buffer_store_dword v27, v9, s[36:39], s50 offen sc1\n" \
  "s_cbranch_scc1 LE_%=\n" \
  "s_sub_i32 s61, s61, s66\n" \
  "s_cmp_gt_i32 s61, 24\n" \
  "s_cbranch_scc1 LW_b_%=\n" \
  "LB_b_%=:\n" \
  "buffer_load_dwordx4 v[32:35], v80, s[36:39], s91 offen sc1\n" \
  "s_lshr_b32 s95, s60, 2\n" \
  "s_sub_u32 s96, s60, s95\n" \
  "s_add_u32 s95, s95, " STR(T_Q) "\n" \
  "s_add_u32 s96, s96, " STR(F_C) "\n" \
  "s_sub_u32 s59, s64, s56\n" \
  "s_add_u32 s59, s59, " STR(F_P) "\n" \
  "v_mov_b64 v[194:195], 0\n" \
  "v_mov_b64 v[196:197], 0\n" \
  "v_mov_b64 v[198:199], 0\n" \
  "s_nop " STR(GAP_0) "\n" \
  "buffer_load_dwordx4 v[36:39], v81, s[36:39], s91 offen sc1\n" \
  "ds_read_b64_tr_b16 v[136:137], v20 offset:0\n" \
  "ds_read_b64_tr_b16 v[138:139], v21 offset:0\n" \
  "ds_read_b64_tr_b16 v[152:153], v20 offset:8\n" \
  "ds_read_b64_tr_b16 v[154:155], v21 offset:8\n" \
  "s_nop " STR(GAP_B) "\n" \
  "buffer_load_dwordx4 v[40:43], v82, s[36:39], s91 offen sc1\n" \
  "ds_read_b64_tr_b16 v[140:141], v20 offset:2048\n" \
  "ds_read_b64_tr_b16 v[142:143], v21 offset:2048\n" \
  "ds_read_b64_tr_b16 v[156:157], v20 offset:2056\n" \
  "ds_read_b64_tr_b16 v[158:159], v21 offset:2056\n" \
  "s_nop " STR(GAP_B) "\n" \
  "buffer_load_dwordx4 v[44:47], v83, s[36:39], s91 offen sc1\n" \
  "ds_read_b64_tr_b16 v[144:145], v20 offset:4096\n" \
  "ds_read_b64_tr_b16 v[146:147], v21 offset:4096\n" \
  "ds_read_b64_tr_b16 v[160:161], v20 offset:4104\n" \
  "ds_read_b64_tr_b16 v[162:163], v21 offset:4104\n" \
  "s_nop " STR(GAP_V) "\n" \
  "buffer_load_dwordx4 v[96:99], v17, s[36:39], s50 offen offset:0 sc1\n" \
  "ds_read_b64_tr_b16 v[148:149], v20 offset:6144\n" \
  "ds_read_b64_tr_b16 v[150:151], v21 offset:6144\n" \
  "ds_read_b64_tr_b16 v[164:165], v20 offset:6152\n" \
  "ds_read_b64_tr_b16 v[166:167], v21 offset:6152\n" \
  "s_nop " STR(GAP_B) "\n" \
  "buffer_load_dwordx4 v[100:103], v17, s[36:39], s50 offen offset:1024 sc1\n" \
  "v_cvt_f32_i32 v92, s59\n" \
  "v_mov_b32 v93, s63\n" \
  "v_mul_f32 v92, v92, v95\n" \
  "v_cvt_i32_f32 v92, v92\n" \
  "v_add_u32 v92, s62, v92\n" \
  "v_min_u32 v92, -2, v92\n" \
  "buffer_store_dwordx2 v[92:93], v14, s[36:39], s50 offen sc1\n" \
  "s_mov_b32 vcc_lo, 0x33333333\n" \
  "s_mov_b32 vcc_hi, 0x33333333\n" \
  "LS2_%=:\n" \
  "s_waitcnt vmcnt(6)\n" \
  "v_mfma_f32_16x16x32_bf16 v[228:231], v[120:123], v[32:35], 0\n" \
  "v_mfma_f32_16x16x32_bf16 v[224:227], v[104:107], v[32:35], 0\n" \
  "v_mfma_f32_16x16x32_bf16 v[232:235], v[32:35], v[32:35], 0\n" \
  "s_waitcnt vmcnt(5)\n" \
  "v_mfma_f32_16x16x32_bf16 v[228:231], v[124:127], v[36:39], v[228:231]\n" \
  "v_mfma_f32_16x16x32_bf16 v[224:227], v[108:111], v[36:39], v[224:227]\n" \
  "v_mfma_f32_16x16x32_bf16 v[232:235], v[36:39], v[36:39], v[232:235]\n" \
  "s_waitcnt vmcnt(4)\n" \
  "v_mfma_f32_16x16x32_bf16 v[228:231], v[128:131], v[40:43], v[228:231]\n" \
  "v_mfma_f32_16x16x32_bf16 v[224:227], v[112:115], v[40:43], v[224:227]\n" \
  "v_mfma_f32_16x16x32_bf16 v[232:235], v[40:43], v[40:43], v[232:235]\n" \
  "s_waitcnt vmcnt(3)\n" \
  "v_mfma_f32_16x16x32_bf16 v[228:231], v[132:135], v[44:47], v[228:231]\n" \
  "v_mfma_f32_16x16x32_bf16 v[224:227], v[116:119], v[44:47], v[224:227]\n" \
  "v_mfma_f32_16x16x32_bf16 v[232:235], v[44:47], v[44:47], v[232:235]\n" \
  "s_nop 3\n" \
  "v_cndmask_b32_dpp v224, v228, v224, vcc quad_perm:[2,3,0,1] row_mask:0xf bank_mask:0xf\n" \
  "v_cndmask_b32_dpp v225, v229, v225, vcc quad_perm:[2,3,0,1] row_mask:0xf bank_mask:0xf\n" \
  "v_cndmask_b32_dpp v226, v230, v226, vcc quad_perm:[2,3,0,1] row_mask:0xf bank_mask:0xf\n" \
  "v_cndmask_b32_dpp v227, v231, v227, vcc quad_perm:[2,3,0,1] row_mask:0xf bank_mask:0xf\n" \
  "v_cndmask_b32_dpp v234, v235, v234, vcc quad_perm:[2,3,0,1] row_mask:0xf bank_mask:0xf\n" \
  "v_cmp_u_f32 vcc, v228, v228\n" \
  "v_max3_f32 v25, v224, v225, v226\n" \
  "v_max3_f32 v25, v25, v227, v234\n" \
  "s_waitcnt vmcnt(2)\n" \
  "v_smfmac_f32_16x16x64_bf16 v[184:187], v[96:99], a[192:199], v2\n" \
  "v_max_f32_dpp v26, v25, v25 quad_perm:[2,3,0,1] row_mask:0xf bank_mask:0xf\n" \
  "v_max_f32_dpp v25, v25, v25 quad_perm:[2,3,0,1] row_mask:0xf bank_mask:0xf\n" \
  "v_smfmac_f32_16x16x64_bf16 v[188:191], v[96:99], a[208:215], v2\n" \
  "v_permlane16_swap_b32 v25, v26\n" \
  "v_max_f32 v27, v25, v26\n" \
  "v_max_f32 v25, v25, v26\n" \
  "v_smfmac_f32_16x16x64_bf16 v[192:195], v[96:99], a[224:231], v2\n" \
  "s_cbranch_vccnz LBE_%=\n" \
  "LBH_%=:\n" \
  "v_permlane32_swap_b32 v25, v27\n" \
  "v_max_f32 v25, v25, v27\n" \
  "v_mul_f32 v25, s92, v25\n" \
  "v_fma_f32 v224, v224, s92, -v25\n" \
  "v_smfmac_f32_16x16x64_bf16 v[196:199], v[96:99], a[240:247], v2\n" \
  "v_fma_f32 v225, v225, s92, -v25\n" \
  "v_exp_f32 v224, v224\n" \
  "v_exp_f32 v225, v225\n" \
  "s_waitcnt vmcnt(1)\n" \
  "v_smfmac_f32_16x16x64_bf16 v[184:187], v[100:103], a[200:207], v2\n" \
  "v_fma_f32 v226, v226, s92, -v25\n" \
  "v_smfmac_f32_16x16x64_bf16 v[188:191], v[100:103], a[216:223], v2\n" \
  "v_fma_f32 v227, v227, s92, -v25\n" \
  "v_smfmac_f32_16x16x64_bf16 v[192:195], v[100:103], a[232:239], v2\n" \
  "v_exp_f32 v226, v226\n" \
  "v_smfmac_f32_16x16x64_bf16 v[196:199], v[100:103], a[248:255], v2\n" \
  "v_exp_f32 v227, v227\n" \
  "v_cvt_pk_bf16_f32 v228, v224, v225\n" \
  "v_cvt_pk_bf16_f32 v229, v226, v227\n" \
  "v_and_b32 v236, v228, v4\n" \
  "v_and_b32 v237, v229, v4\n" \
  "v_and_b32_dpp v238, v228, v4 quad_perm:[2,3,0,1] row_mask:0xf bank_mask:0xf\n" \
  "v_and_b32_dpp v239, v229, v4 quad_perm:[2,3,0,1] row_mask:0xf bank_mask:0xf\n" \
  "v_fma_f32 v234, v234, s92, -v25\n" \
  "v_and_b32_dpp v240, v228, v29 quad_perm:[2,3,0,1] row_mask:0xf bank_mask:0xf\n" \
  "v_mfma_f32_16x16x32_bf16 v[48:51], v[168:171], v[236:239], 0\n" \
  "v_and_b32_dpp v241, v229, v29 quad_perm:[2,3,0,1] row_mask:0xf bank_mask:0xf\n" \
  "v_and_b32 v242, v228, v29\n" \
  "v_mfma_f32_16x16x32_bf16 v[52:55], v[176:179], v[236:239], 0\n" \
  "v_and_b32 v243, v229, v29\n" \
  "v_exp_f32 v234, v234\n" \
  "v_cvt_pk_bf16_f32 v216, v184, v185\n" \
  "v_mfma_f32_16x16x32_bf16 v[48:51], v[172:175], v[240:243], v[48:51]\n" \
  "v_mov_b32_dpp v25, v234 quad_perm:[0,1,0,1] row_mask:0xf bank_mask:0xf\n" \
  "v_mov_b32_dpp v26, v234 quad_perm:[2,3,2,3] row_mask:0xf bank_mask:0xf\n" \
  "v_mfma_f32_16x16x32_bf16 v[52:55], v[180:183], v[240:243], v[52:55]\n" \
  "v_cvt_pk_bf16_f32 v25, v25, v26\n" \
  "v_and_b32 v230, v25, v4\n" \
  "v_and_b32 v231, v25, v4\n" \
  "v_and_b32 v232, v25, v29\n" \
  "v_and_b32 v233, v25, v29\n" \
  "v_mfma_f32_16x16x32_bf16 v[56:59], v[252:255], v[228:231], 0\n" \
  "v_cvt_pk_bf16_f32 v217, v186, v187\n" \
  "v_cvt_pk_bf16_f32 v218, v188, v189\n" \
  "v_cvt_pk_bf16_f32 v219, v190, v191\n" \
  "v_cvt_pk_bf16_f32 v220, v192, v193\n" \
  "v_cvt_pk_bf16_f32 v221, v194, v195\n" \
  "v_mfma_f32_16x16x32_bf16 v[48:51], v[216:219], v[230:233], v[48:51]\n" \
  "v_cvt_pk_bf16_f32 v222, v196, v197\n" \
  "v_cvt_pk_bf16_f32 v223, v198, v199\n" \
  "v_add_f32_dpp v56, v56, v56 quad_perm:[2,3,0,1] row_mask:0xf bank_mask:0xf\n" \
  "v_rcp_f32 v56, v56\n" \
  "v_mfma_f32_16x16x32_bf16 v[52:55], v[220:223], v[230:233], v[52:55]\n" \
  "s_nop 2\n" \
  "v_cmp_u_f32 vcc, v48, v48\n" \
  "v_mul_f32 v48, v48, v56\n" \
  "v_mul_f32 v49, v49, v56\n" \
  "v_mul_f32 v50, v50, v56\n" \
  "v_mul_f32 v51, v51, v56\n" \
  "v_pk_mul_f32 v[52:53], v[52:53], v[56:57] op_sel_hi:[1,0]\n" \
  "v_pk_mul_f32 v[54:55], v[54:55], v[56:57] op_sel_hi:[1,0]\n" \
  "v_cvt_pk_bf16_f32 v72, v48, v49\n" \
  "v_cvt_pk_bf16_f32 v73, v50, v51\n" \
  "v_cvt_pk_bf16_f32 v74, v52, v53\n" \
  "v_cvt_pk_bf16_f32 v75, v54, v55\n" \
  "s_cbranch_vccnz LBF_%=\n" \
  "LBG_%=:\n" \
  "buffer_store_dwordx4 v[72:75], v10, s[36:39], s51 offen sc1\n" \
  "s_memtime s[66:67]\n" \
  "s_add_u32 s61, s64, s96\n" \
  "s_cmp_lg_u32 %7, 0\n" \
  "s_cbranch_scc1 LO_%=\n" \
  "s_waitcnt lgkmcnt(0)\n" \
  "s_sub_i32 s61, s61, s66\n" \
  "s_max_i32 s61, s61, " STR(M_C) "\n" \
  "s_cmp_gt_i32 s61, 24\n" \
  "s_cbranch_scc1 LW_c_%=\n" \
  "LB_c_%=:\n" \
  "buffer_load_dwordx4 v[32:35], v11, s[36:39], s51 offen offset:0 sc1\n" \
  "buffer_load_dwordx4 v[36:39], v11, s[36:39], s51 offen offset:1024 sc1\n" \
  "buffer_load_dwordx4 v[40:43], v11, s[36:39], s51 offen offset:2048 sc1\n" \
  "buffer_load_dwordx4 v[44:47], v11, s[36:39], s51 offen offset:3072 sc1\n" \
  "v_mfma_f32_16x16x32_bf16 v[184:187], v[136:139], a[192:195], 0\n" \
  "v_mov_b64 v[48:49], 0\n" \
  "v_mfma_f32_16x16x32_bf16 v[188:191], v[152:155], a[192:195], 0\n" \
  "v_mov_b64 v[50:51], 0\n" \
  "v_mfma_f32_16x16x32_bf16 v[192:195], v[136:139], a[208:211], 0\n" \
  "v_mov_b64 v[52:53], 0\n" \
  "v_mfma_f32_16x16x32_bf16 v[196:199], v[152:155], a[208:211], 0\n" \
  "v_mov_b64 v[54:55], 0\n" \
  "v_mfma_f32_16x16x32_bf16 v[184:187], v[140:143], a[196:199], v[184:187]\n" \
  "v_mov_b64 v[56:57], 0\n" \
  "v_mfma_f32_16x16x32_bf16 v[188:191], v[156:159], a[196:199], v[188:191]\n" \
  "v_mov_b64 v[58:59], 0\n" \
  "v_mfma_f32_16x16x32_bf16 v[192:195], v[140:143], a[212:215], v[192:195]\n" \
  "v_mov_b64 v[60:61], 0\n" \
  "v_mfma_f32_16x16x32_bf16 v[196:199], v[156:159], a[212:215], v[196:199]\n" \
  "v_mov_b64 v[62:63], 0\n" \
  "v_mfma_f32_16x16x32_bf16 v[184:187], v[144:147], a[200:203], v[184:187]\n" \
  "v_mov_b64 v[64:65], 0\n" \
  "ds_read_b128 v[104:107], v84 offset:0\n" \
  "v_mfma_f32_16x16x32_bf16 v[188:191], v[160:163], a[200:203], v[188:191]\n" \
  "v_mov_b64 v[66:67], 0\n" \
  "ds_read_b128 v[108:111], v85 offset:0\n" \
  "v_mfma_f32_16x16x32_bf16 v[192:195], v[144:147], a[216:219], v[192:195]\n" \
  "v_mov_b64 v[68:69], 0\n" \
  "ds_read_b128 v[112:115], v86 offset:0\n" \
  "v_mfma_f32_16x16x32_bf16 v[196:199], v[160:163], a[216:219], v[196:199]\n" \
  "v_mov_b64 v[70:71], 0\n" \
  "ds_read_b128 v[116:119], v87 offset:0\n" \
  "v_mfma_f32_16x16x32_bf16 v[184:187], v[148:151], a[204:207], v[184:187]\n" \
  "ds_read_b128 v[120:123], v84 offset:4096\n" \
  "v_mfma_f32_16x16x32_bf16 v[188:191], v[164:167], a[204:207], v[188:191]\n" \
  "ds_read_b128 v[124:127], v85 offset:4096\n" \
  "v_mfma_f32_16x16x32_bf16 v[192:195], v[148:151], a[220:223], v[192:195]\n" \
  "ds_read_b128 v[128:131], v86 offset:4096\n" \
  "v_mfma_f32_16x16x32_bf16 v[196:199], v[164:167], a[220:223], v[196:199]\n" \
  "ds_read_b128 v[132:135], v87 offset:4096\n" \
  "s_add_u32 s48, s48, 1\n" \
  "s_nop 5\n" \
  "v_cvt_pk_bf16_f32 v168, v184, v185\n" \
  "v_cvt_pk_bf16_f32 v169, v186, v187\n" \
  "v_cvt_pk_bf16_f32 v170, v188, v189\n" \
  "v_cvt_pk_bf16_f32 v171, v190, v191\n" \
  "v_cvt_pk_bf16_f32 v172, v192, v193\n" \
  "v_cvt_pk_bf16_f32 v173, v194, v195\n" \
  "v_cvt_pk_bf16_f32 v174, v196, v197\n" \
  "v_cvt_pk_bf16_f32 v175, v198, v199\n" \
  "LS3_%=:\n" \
  "s_waitcnt vmcnt(3)\n" \
  "v_mfma_f32_16x16x32_bf16 v[224:227], v[248:251], v[32:35], 0\n" \
  "s_waitcnt vmcnt(2)\n" \
  "v_mfma_f32_16x16x32_bf16 v[224:227], v[248:251], v[36:39], v[224:227]\n" \
  "s_waitcnt vmcnt(1)\n" \
  "v_mfma_f32_16x16x32_bf16 v[224:227], v[248:251], v[40:43], v[224:227]\n" \
  "s_waitcnt vmcnt(0)\n" \
  "v_mfma_f32_16x16x32_bf16 v[224:227], v[248:251], v[44:47], v[224:227]\n" \
  "s_nop 7\n" \
  "v_cmp_u_f32 vcc, v224, v224\n" \
  "v_add_f32_dpp v224, v224, v224 row_ror:8 row_mask:0xf bank_mask:0xf\n" \
  "v_add_f32_dpp v225, v225, v225 row_ror:8 row_mask:0xf bank_mask:0xf\n" \
  "v_add_f32_dpp v226, v226, v226 row_ror:8 row_mask:0xf bank_mask:0xf\n" \
  "v_add_f32_dpp v227, v227, v227 row_ror:8 row_mask:0xf bank_mask:0xf\n" \
  "v_cvt_pk_bf16_f32 v228, v224, v225\n" \
  "v_cvt_pk_bf16_f32 v229, v226, v227\n" \
  "s_cbranch_vccnz LC_%=\n" \
  "LC2_%=:\n" \
  "v_permlane16_swap_b32 v228, v230\n" \
  "v_permlane16_swap_b32 v229, v231\n" \
  "buffer_store_dwordx4 v[228:231], v12, s[36:39], s53 offen sc1\n" \
  "s_memtime s[84:85]\n" \
  "LK4_%=:\n" \
  "ds_read_b64 v[92:93], v24 offset:14352\n" \
  "s_memtime s[66:67]\n" \
  "s_waitcnt lgkmcnt(0)\n" \
  "v_readfirstlane_b32 s59, v93\n" \
  "v_readfirstlane_b32 s63, v92\n" \
  "s_cmp_lg_u32 s59, s48\n" \
  "s_cbranch_scc1 LK4_%=\n" \
  "s_sub_i32 s59, s63, s64\n" \
  "s_sub_i32 s59, s59, " STR(F_P) "-300\n" \
  "s_sub_i32 s61, s63, s66\n" \
  "s_sub_i32 s60, s63, s84\n" \
  "s_add_i32 s60, s60, " STR(T_LATE) "\n" \
  "s_min_i32 s59, s59, s60\n" \
  "s_cmp_lt_i32 s59, 0\n" \
  "s_cbranch_scc1 LN_%=\n" \
  "LN2_%=:\n" \
  "v_mov_b32 v92, s63\n" \
  "v_mov_b32 v93, 0\n" \
  "ds_write_b64 v24, v[92:93] offset:14336\n" \
  "s_sub_i32 s59, s84, s66\n" \
  "s_add_i32 s59, s59, " STR(M_A) "-50\n" \
  "s_max_i32 s61, s61, s59\n" \
  "s_cmp_gt_i32 s61, 24\n" \
  "s_cbranch_scc1 LW_a_%=\n" \
  "LB_a_%=:\n" \
  "s_waitcnt lgkmcnt(0)\n" \
  "s_branch LL_%=\n" \
  "LO_%=:\n" \
  "s_cmp_eq_u32 %7, 1\n" \
  "s_waitcnt lgkmcnt(0)\n" \
  "s_cbranch_scc1 LK_%=\n" \
  "s_sub_i32 s61, s61, s66\n" \
  "s_max_i32 s61, s61, " STR(M_C) "\n" \
  "s_add_i32 s61, s61, " STR(D_PF) "\n" \
  "s_add_u32 s56, s48, 3\n" \
  "s_min_u32 s56, s56, 64\n" \
  "s_add_u32 s59, s48, 1\n" \
  "v_mov_b32 v28, s59\n" \
  "buffer_store_dword v28, v0, s[44:47], 0 offen sc1\n" \
  "v_cmp_gt_u32 vcc, s56, v23\n" \
  "v_mfma_f32_16x16x32_bf16 v[184:187], v[136:139], a[192:195], 0\n" \
  "v_mov_b64 v[48:49], 0\n" \
  "v_mfma_f32_16x16x32_bf16 v[188:191], v[152:155], a[192:195], 0\n" \
  "v_mov_b64 v[50:51], 0\n" \
  "v_mfma_f32_16x16x32_bf16 v[192:195], v[136:139], a[208:211], 0\n" \
  "v_mov_b64 v[52:53], 0\n" \
  "v_mfma_f32_16x16x32_bf16 v[196:199], v[152:155], a[208:211], 0\n" \
  "v_mov_b64 v[54:55], 0\n" \
  "v_mfma_f32_16x16x32_bf16 v[184:187], v[140:143], a[196:199], v[184:187]\n" \
  "v_mov_b64 v[56:57], 0\n" \
  "v_mfma_f32_16x16x32_bf16 v[188:191], v[156:159], a[196:199], v[188:191]\n" \
  "v_mov_b64 v[58:59], 0\n" \
  "v_mfma_f32_16x16x32_bf16 v[192:195], v[140:143], a[212:215], v[192:195]\n" \
  "v_mov_b64 v[60:61], 0\n" \
  "v_mfma_f32_16x16x32_bf16 v[196:199], v[156:159], a[212:215], v[196:199]\n" \
  "v_mov_b64 v[62:63], 0\n" \
  "v_mfma_f32_16x16x32_bf16 v[184:187], v[144:147], a[200:203], v[184:187]\n" \
  "v_mov_b64 v[64:65], 0\n" \
  "ds_read_b128 v[104:107], v84 offset:0\n" \
  "v_mfma_f32_16x16x32_bf16 v[188:191], v[160:163], a[200:203], v[188:191]\n" \
  "v_mov_b64 v[66:67], 0\n" \
  "ds_read_b128 v[108:111], v85 offset:0\n" \
  "v_mfma_f32_16x16x32_bf16 v[192:195], v[144:147], a[216:219], v[192:195]\n" \
  "v_mov_b64 v[68:69], 0\n" \
  "ds_read_b128 v[112:115], v86 offset:0\n" \
  "v_mfma_f32_16x16x32_bf16 v[196:199], v[160:163], a[216:219], v[196:199]\n" \
  "v_mov_b64 v[70:71], 0\n" \
  "ds_read_b128 v[116:119], v87 offset:0\n" \
  "v_mfma_f32_16x16x32_bf16 v[184:187], v[148:151], a[204:207], v[184:187]\n" \
  "ds_read_b128 v[120:123], v84 offset:4096\n" \
  "v_mfma_f32_16x16x32_bf16 v[188:191], v[164:167], a[204:207], v[188:191]\n" \
  "ds_read_b128 v[124:127], v85 offset:4096\n" \
  "v_mfma_f32_16x16x32_bf16 v[192:195], v[148:151], a[220:223], v[192:195]\n" \
  "ds_read_b128 v[128:131], v86 offset:4096\n" \
  "v_mfma_f32_16x16x32_bf16 v[196:199], v[164:167], a[220:223], v[196:199]\n" \
  "ds_read_b128 v[132:135], v87 offset:4096\n" \
  "s_add_u32 s48, s48, 1\n" \
  "s_sub_u32 s60, s56, 1\n" \
  "s_lshr_b32 s59, s60, 2\n" \
  "s_xnor_b32 s59, s59, %8\n" \
  "s_and_b32 s59, s59, 1\n" \
  "s_lshl_b32 s59, s59, 13\n" \
  "s_lshl_b32 s55, s60, 19\n" \
  "s_or_b32 s55, s55, s59\n" \
  "s_cbranch_vccnz LF_%=\n" \
  "LF2_%=:\n" \
  "s_cmp_gt_i32 s61, 24\n" \
  "s_cbranch_scc1 LW_p_%=\n" \
  "LB_p_%=:\n" \
  "buffer_load_dwordx4 v[200:203], v247, s[40:43], s55 offen offset:0 sc1\n" \
  "buffer_load_dwordx4 v[204:207], v247, s[40:43], s55 offen offset:1024 sc1\n" \
  "buffer_load_dwordx4 v[208:211], v247, s[40:43], s55 offen offset:2048 sc1\n" \
  "buffer_load_dwordx4 v[212:215], v247, s[40:43], s55 offen offset:3072 sc1\n" \
  "buffer_load_dwordx4 v[216:219], v16, s[40:43], s55 offen offset:0 sc1\n" \
  "buffer_load_dwordx4 v[220:223], v16, s[40:43], s55 offen offset:1024 sc1\n" \
  "buffer_load_dwordx4 v[224:227], v16, s[40:43], s55 offen offset:2048 sc1\n" \
  "buffer_load_dwordx4 v[228:231], v16, s[40:43], s55 offen offset:3072 sc1\n" \
  "v_cvt_pk_bf16_f32 v168, v184, v185\n" \
  "v_cvt_pk_bf16_f32 v169, v186, v187\n" \
  "v_cvt_pk_bf16_f32 v170, v188, v189\n" \
  "v_cvt_pk_bf16_f32 v171, v190, v191\n" \
  "v_cvt_pk_bf16_f32 v172, v192, v193\n" \
  "v_cvt_pk_bf16_f32 v173, v194, v195\n" \
  "v_cvt_pk_bf16_f32 v174, v196, v197\n" \
  "v_cvt_pk_bf16_f32 v175, v198, v199\n" \
  "v_mfma_f32_16x16x32_bf16 v[184:187], v[136:139], a[224:227], 0\n" \
  "v_mfma_f32_16x16x32_bf16 v[188:191], v[152:155], a[224:227], 0\n" \
  "v_mfma_f32_16x16x32_bf16 v[192:195], v[136:139], a[240:243], 0\n" \
  "v_mfma_f32_16x16x32_bf16 v[196:199], v[152:155], a[240:243], 0\n" \
  "v_mfma_f32_16x16x32_bf16 v[184:187], v[140:143], a[228:231], v[184:187]\n" \
  "v_mfma_f32_16x16x32_bf16 v[188:191], v[156:159], a[228:231], v[188:191]\n" \
  "v_mfma_f32_16x16x32_bf16 v[192:195], v[140:143], a[244:247], v[192:195]\n" \
  "v_mfma_f32_16x16x32_bf16 v[196:199], v[156:159], a[244:247], v[196:199]\n" \
  "v_mfma_f32_16x16x32_bf16 v[184:187], v[144:147], a[232:235], v[184:187]\n" \
  "v_mfma_f32_16x16x32_bf16 v[188:191], v[160:163], a[232:235], v[188:191]\n" \
  "v_mfma_f32_16x16x32_bf16 v[192:195], v[144:147], a[248:251], v[192:195]\n" \
  "v_mfma_f32_16x16x32_bf16 v[196:199], v[160:163], a[248:251], v[196:199]\n" \
  "v_mfma_f32_16x16x32_bf16 v[184:187], v[148:151], a[236:239], v[184:187]\n" \
  "v_mfma_f32_16x16x32_bf16 v[188:191], v[164:167], a[236:239], v[188:191]\n" \
  "v_mfma_f32_16x16x32_bf16 v[192:195], v[148:151], a[252:255], v[192:195]\n" \
  "v_mfma_f32_16x16x32_bf16 v[196:199], v[164:167], a[252:255], v[196:199]\n" \
  "s_nop 7\n" \
  "v_cvt_pk_bf16_f32 v176, v184, v185\n" \
  "v_cvt_pk_bf16_f32 v177, v186, v187\n" \
  "v_cvt_pk_bf16_f32 v178, v188, v189\n" \
  "v_cvt_pk_bf16_f32 v179, v190, v191\n" \
  "v_cvt_pk_bf16_f32 v180, v192, v193\n" \
  "v_cvt_pk_bf16_f32 v181, v194, v195\n" \
  "v_cvt_pk_bf16_f32 v182, v196, v197\n" \
  "v_cvt_pk_bf16_f32 v183, v198, v199\n" \
  "s_waitcnt lgkmcnt(0)\n" \
  "s_branch LM_%=\n" \
  "LK_%=:\n" \
  "buffer_load_dwordx2 v[92:93], v13, s[36:39], s50 offen sc1\n" \
  "s_mov_b32 s94, s63\n" \
  "v_mfma_f32_16x16x32_bf16 v[184:187], v[136:139], a[192:195], 0\n" \
  "v_mov_b64 v[48:49], 0\n" \
  "v_mfma_f32_16x16x32_bf16 v[188:191], v[152:155], a[192:195], 0\n" \
  "v_mov_b64 v[50:51], 0\n" \
  "v_mfma_f32_16x16x32_bf16 v[192:195], v[136:139], a[208:211], 0\n" \
  "v_mov_b64 v[52:53], 0\n" \
  "v_mfma_f32_16x16x32_bf16 v[196:199], v[152:155], a[208:211], 0\n" \
  "v_mov_b64 v[54:55], 0\n" \
  "v_mfma_f32_16x16x32_bf16 v[184:187], v[140:143], a[196:199], v[184:187]\n" \
  "v_mov_b64 v[56:57], 0\n" \
  "v_mfma_f32_16x16x32_bf16 v[188:191], v[156:159], a[196:199], v[188:191]\n" \
  "v_mov_b64 v[58:59], 0\n" \
  "v_mfma_f32_16x16x32_bf16 v[192:195], v[140:143], a[212:215], v[192:195]\n" \
  "v_mov_b64 v[60:61], 0\n" \
  "v_mfma_f32_16x16x32_bf16 v[196:199], v[156:159], a[212:215], v[196:199]\n" \
  "v_mov_b64 v[62:63], 0\n" \
  "v_mfma_f32_16x16x32_bf16 v[184:187], v[144:147], a[200:203], v[184:187]\n" \
  "v_mov_b64 v[64:65], 0\n" \
  "ds_read_b128 v[104:107], v84 offset:0\n" \
  "v_mfma_f32_16x16x32_bf16 v[188:191], v[160:163], a[200:203], v[188:191]\n" \
  "v_mov_b64 v[66:67], 0\n" \
  "ds_read_b128 v[108:111], v85 offset:0\n" \
  "v_mfma_f32_16x16x32_bf16 v[192:195], v[144:147], a[216:219], v[192:195]\n" \
  "v_mov_b64 v[68:69], 0\n" \
  "ds_read_b128 v[112:115], v86 offset:0\n" \
  "v_mfma_f32_16x16x32_bf16 v[196:199], v[160:163], a[216:219], v[196:199]\n" \
  "v_mov_b64 v[70:71], 0\n" \
  "ds_read_b128 v[116:119], v87 offset:0\n" \
  "v_mfma_f32_16x16x32_bf16 v[184:187], v[148:151], a[204:207], v[184:187]\n" \
  "ds_read_b128 v[120:123], v84 offset:4096\n" \
  "v_mfma_f32_16x16x32_bf16 v[188:191], v[164:167], a[204:207], v[188:191]\n" \
  "ds_read_b128 v[124:127], v85 offset:4096\n" \
  "v_mfma_f32_16x16x32_bf16 v[192:195], v[148:151], a[220:223], v[192:195]\n" \
  "ds_read_b128 v[128:131], v86 offset:4096\n" \
  "v_mfma_f32_16x16x32_bf16 v[196:199], v[164:167], a[220:223], v[196:199]\n" \
  "ds_read_b128 v[132:135], v87 offset:4096\n" \
  "s_add_u32 s48, s48, 1\n" \
  "s_nop 5\n" \
  "v_cvt_pk_bf16_f32 v168, v184, v185\n" \
  "v_cvt_pk_bf16_f32 v169, v186, v187\n" \
  "v_cvt_pk_bf16_f32 v170, v188, v189\n" \
  "v_cvt_pk_bf16_f32 v171, v190, v191\n" \
  "v_cvt_pk_bf16_f32 v172, v192, v193\n" \
  "v_cvt_pk_bf16_f32 v173, v194, v195\n" \
  "v_cvt_pk_bf16_f32 v174, v196, v197\n" \
  "v_cvt_pk_bf16_f32 v175, v198, v199\n" \
  "s_nop " STR(D_K) "\n" \
  "s_waitcnt lgkmcnt(0)\n" \
  "s_memtime s[72:73]\n" \
  "s_memrealtime s[74:75]\n" \
  "s_nop 1\n" \
  "s_memrealtime s[76:77]\n" \
  "s_nop 1\n" \
  "s_memrealtime s[78:79]\n" \
  "s_nop 1\n" \
  "s_memrealtime s[80:81]\n" \
  "s_nop 1\n" \
  "s_memrealtime s[82:83]\n" \
  "s_nop 1\n" \
  "s_memrealtime s[84:85]\n" \
  "s_nop 1\n" \
  "s_memrealtime s[86:87]\n" \
  "s_nop 1\n" \
  "s_memrealtime s[88:89]\n" \
  "s_nop 1\n" \
  "s_memrealtime s[90:91]\n" \
  "s_mov_b32 s59, 0\n" \
  "s_waitcnt lgkmcnt(0)\n" \
  "s_cmp_eq_u32 s76, s74\n" \
  "s_addc_u32 s59, s59, 0\n" \
  "s_cmp_eq_u32 s78, s74\n" \
  "s_addc_u32 s59, s59, 0\n" \
  "s_cmp_eq_u32 s80, s74\n" \
  "s_addc_u32 s59, s59, 0\n" \
  "s_cmp_eq_u32 s82, s74\n" \
  "s_addc_u32 s59, s59, 0\n" \
  "s_cmp_eq_u32 s84, s74\n" \
  "s_addc_u32 s59, s59, 0\n" \
  "s_cmp_eq_u32 s86, s74\n" \
  "s_addc_u32 s59, s59, 0\n" \
  "s_cmp_eq_u32 s88, s74\n" \
  "s_addc_u32 s59, s59, 0\n" \
  "s_cmp_eq_u32 s90, s74\n" \
  "s_addc_u32 s59, s59, 0\n" \
  "s_mul_i32 s59, s59, 12\n" \
  "s_add_u32 s68, s72, s59\n" \
  "s_add_u32 s59, s74, 4\n" \
  "s_mul_i32 s69, s59, 24\n" \
  "s_sub_u32 s60, s69, s62\n" \
  "s_sub_u32 s59, s68, s56\n" \
  "s_mov_b32 s62, s69\n" \
  "s_mov_b32 s56, s68\n" \
  "v_cvt_f32_u32 v27, s59\n" \
  "v_cvt_f32_u32 v28, s60\n" \
  "v_rcp_f32 v26, v27\n" \
  "v_rcp_f32 v19, v28\n" \
  "v_mul_f32 v26, v26, v28\n" \
  "v_mul_f32 v95, " STR(DECAY) ", v95\n" \
  "v_mul_f32 v19, v19, v27\n" \
  "v_max_f32 v95, v95, v26\n" \
  "v_cvt_f32_u32 v28, " STR(F_P) "\n" \
  "v_mul_f32 v27, v95, v28\n" \
  "v_cvt_u32_f32 v27, v27\n" \
  "v_min_u32 v27, 0x7fff, v27\n" \
  "s_nop 0\n" \
  "v_readfirstlane_b32 s63, v27\n" \
  "LK2_%=:\n" \
  "s_waitcnt vmcnt(0)\n" \
  "v_max_u32 v26, v92, v93\n" \
  "v_cmp_eq_u32 vcc, -1, v26\n" \
  "v_subrev_u32 v25, s62, v92\n" \
  "v_subrev_u32 v26, s94, v93\n" \
  "s_nop 0\n" \
  "v_max_i32_dpp v25, v25, v25 quad_perm:[1,0,3,2] row_mask:0xf bank_mask:0xf\n" \
  "v_max_i32_dpp v26, v26, v26 quad_perm:[1,0,3,2] row_mask:0xf bank_mask:0xf\n" \
  "s_cbranch_vccnz LK3_%=\n" \
  "v_max_i32_dpp v25, v25, v25 quad_perm:[2,3,0,1] row_mask:0xf bank_mask:0xf\n" \
  "v_max_i32_dpp v26, v26, v26 quad_perm:[2,3,0,1] row_mask:0xf bank_mask:0xf\n" \
  "LK5_%=:\n" \
  "v_cvt_f32_i32 v25, v25\n" \
  "v_mov_b32 v93, s48\n" \
  "v_mul_f32 v25, v25, v19\n" \
  "v_cvt_i32_f32 v25, v25\n" \
  "v_add_u32 v92, s68, v25\n" \
  "ds_write_b64 v24, v[92:93] offset:14352\n" \
  "v_max_i32 v26, 0, v26\n" \
  "v_min_i32 v26, 400, v26\n" \
  "ds_write_b32 v24, v26 offset:14344\n" \
  "s_waitcnt lgkmcnt(0)\n" \
  "s_branch LL_%=\n" \
  ".p2align 6\n" \
  "LM_%=:\n" \
  "s_barrier\n" \
  "buffer_load_dwordx4 v[32:35], v80, s[36:39], s49 offen sc1\n" \
  "s_xor_b32 s51, s51, 1<<20\n" \
  "s_xor_b32 s52, s52, 1<<20\n" \
  "s_add_u32 s53, s53, 32768\n" \
  "s_lshl_b32 s60, s48, 19\n" \
  "s_add_u32 s60, s60, " STR(EXP_B) "<<21\n" \
  "s_lshr_b32 s59, s60, 21\n" \
  "s_lshr_b32 s59, %8, s59\n" \
  "s_and_b32 s94, s59, 1\n" \
  "s_nop 3\n" \
  "buffer_load_dwordx4 v[36:39], v81, s[36:39], s49 offen sc1\n" \
  "s_lshl_b32 s94, s94, 13\n" \
  "s_or_b32 s50, s60, s94\n" \
  "s_lshl_b32 s94, s70, 14\n" \
  "s_add_u32 s91, s50, s94\n" \
  "s_and_b32 s94, s59, 2\n" \
  "s_lshl_b32 s94, s94, 12\n" \
  "s_add_u32 s60, s60, 1<<21\n" \
  "s_or_b32 s54, s60, s94\n" \
  "s_nop 3\n" \
  "buffer_load_dwordx4 v[40:43], v82, s[36:39], s49 offen sc1\n" \
  "s_xor_b32 s59, %7, 3\n" \
  "s_lshl_b32 s59, s59, 21\n" \
  "s_add_u32 s59, s59, s53\n" \
  "s_add_u32 s59, s59, 4<<15\n" \
  "s_cmp_lt_u32 s59, 1<<21\n" \
  "s_cselect_b32 s59, s59, 1<<30\n" \
  "s_nop 5\n" \
  "buffer_load_dwordx4 v[44:47], v83, s[36:39], s49 offen sc1\n" \
  "s_waitcnt vmcnt(4)\n" \
  "ds_write_b128 v18, v[200:203] offset:0\n" \
  "ds_write_b128 v18, v[204:207] offset:1024\n" \
  "ds_write_b128 v18, v[208:211] offset:2048\n" \
  "ds_write_b128 v18, v[212:215] offset:3072\n" \
  "v_add_u32 v27, s59, v12\n" \
  "buffer_store_dwordx4 v[88:91], v11, s[36:39], s52 offen sc1\n" \
  "buffer_store_dwordx4 v[88:91], v15, s[36:39], s54 offen sc1\n" \
  "buffer_store_dwordx4 v[88:91], v27, s[36:39], 0 offen sc1\n" \
  "buffer_load_dword v23, v22, s[44:47], 0 offen sc1\n" \
  "ds_write_b128 v18, v[216:219] offset:4096\n" \
  "ds_write_b128 v18, v[220:223] offset:5120\n" \
  "ds_write_b128 v18, v[224:227] offset:6144\n" \
  "ds_write_b128 v18, v[228:231] offset:7168\n" \
  "s_branch LS1_%=\n" \
  "LA_%=:\n" \
  "ds_read_b64 v[92:93], v24 offset:14336\n" \
  "v_max3_u32 v25, v32, v33, v34\n" \
  "v_max3_u32 v25, v25, v35, v36\n" \
  "v_max3_u32 v25, v25, v37, v38\n" \
  "v_max3_u32 v25, v25, v39, v40\n" \
  "v_max3_u32 v25, v25, v41, v42\n" \
  "v_max3_u32 v25, v25, v43, v44\n" \
  "v_max3_u32 v25, v25, v45, v46\n" \
  "v_max_u32 v25, v25, v47\n" \
  "v_cmp_eq_u32 vcc, -1, v25\n" \
  "s_cbranch_vccz LA4_%=\n" \
  "s_add_u32 s58, s58, 1\n" \
  "s_cmp_lt_u32 s58, " STR(SPIN_CAP) "\n" \
  "s_cbranch_scc1 LA1_%=\n" \
  "v_mov_b32 v27, " STR(CW_DEAD) "*4\n" \
  "v_mov_b32 v28, 1\n" \
  "buffer_store_dword v28, v27, s[44:47], 0 offen sc1\n" \
  "s_branch LA4_%=\n" \
  "LA1_%=:\n" \
  "s_waitcnt lgkmcnt(0)\n" \
  "v_readfirstlane_b32 s64, v92\n" \
  "s_memtime s[66:67]\n" \
  "s_waitcnt lgkmcnt(0)\n" \
  "s_sub_u32 s59, " STR(O_A) ", s66\n" \
  "LQ_ra_%=:\n" \
  "s_add_u32 s64, s64, s95\n" \
  "s_add_i32 s61, s64, s59\n" \
  "s_cmp_lt_i32 s61, -" STR(T_TOL) "\n" \
  "s_cbranch_scc1 LQ_ra_%=\n" \
  "s_max_i32 s61, s61, 0\n" \
  "s_min_i32 s61, s61, 4000\n" \
  "s_bitcmp0_b32 s61, 4\n" \
  "s_cbranch_scc1 LX_ra_%=\n" \
  "s_nop 8\n" \
  "LX_ra_%=:\n" \
  "s_lshr_b32 s61, s61, 5\n" \
  "s_cbranch_scc0 LB_ra_%=\n" \
  ".p2align 4\n" \
  "LY_ra_%=:\n" \
  "s_sub_u32 s61, s61, 1\n" \
  "s_cmp_lg_u32 s61, 0\n" \
  "s_cbranch_scc1 LY_ra_%=\n" \
  "LB_ra_%=:\n" \
  "buffer_load_dwordx4 v[32:35], v80, s[36:39], s49 offen sc1\n" \
  "s_nop 11\n" \
  "buffer_load_dwordx4 v[36:39], v81, s[36:39], s49 offen sc1\n" \
  "s_nop 11\n" \
  "buffer_load_dwordx4 v[40:43], v82, s[36:39], s49 offen sc1\n" \
  "s_nop 11\n" \
  "buffer_load_dwordx4 v[44:47], v83, s[36:39], s49 offen sc1\n" \
  "v_mov_b64 v[48:49], 0\n" \
  "v_mov_b64 v[50:51], 0\n" \
  "v_mov_b64 v[52:53], 0\n" \
  "v_mov_b64 v[54:55], 0\n" \
  "v_mov_b64 v[56:57], 0\n" \
  "v_mov_b64 v[58:59], 0\n" \
  "v_mov_b64 v[60:61], 0\n" \
  "v_mov_b64 v[62:63], 0\n" \
  "v_mov_b64 v[64:65], 0\n" \
  "v_mov_b64 v[66:67], 0\n" \
  "v_mov_b64 v[68:69], 0\n" \
  "v_mov_b64 v[70:71], 0\n" \
  "buffer_store_dword v27, v88, s[36:39], 0 offen sc1\n" \
  "buffer_store_dword v27, v88, s[36:39], 0 offen sc1\n" \
  "buffer_store_dword v27, v88, s[36:39], 0 offen sc1\n" \
  "buffer_store_dword v27, v88, s[36:39], 0 offen sc1\n" \
  "v_sub_u32 v27, s64, v92\n" \
  "ds_max_u32 v24, v27 offset:14340\n" \
  "s_branch LS1_%=\n" \
  "LW_b_%=:\n" \
  "s_min_i32 s61, s61, 4000\n" \
  "s_sub_i32 s61, s61, 72\n" \
  "s_max_i32 s61, s61, 0\n" \
  "s_bitcmp0_b32 s61, 4\n" \
  "s_cbranch_scc1 LX_b_%=\n" \
  "s_nop 8\n" \
  "LX_b_%=:\n" \
  "s_lshr_b32 s61, s61, 5\n" \
  "s_cbranch_scc0 LB_b_%=\n" \
  ".p2align 4\n" \
  "LY_b_%=:\n" \
  "s_sub_u32 s61, s61, 1\n" \
  "s_cmp_lg_u32 s61, 0\n" \
  "s_cbranch_scc1 LY_b_%=\n" \
  "s_branch LB_b_%=\n" \
  "LBE_%=:\n" \
  "v_max3_u32 v26, v32, v33, v34\n" \
  "v_max3_u32 v26, v26, v35, v36\n" \
  "v_max3_u32 v26, v26, v37, v38\n" \
  "v_max3_u32 v26, v26, v39, v40\n" \
  "v_max3_u32 v26, v26, v41, v42\n" \
  "v_max3_u32 v26, v26, v43, v44\n" \
  "v_max3_u32 v26, v26, v45, v46\n" \
  "v_max_u32 v26, v26, v47\n" \
  "v_cmp_eq_u32 vcc, -1, v26\n" \
  "s_cbranch_vccz LBH_%=\n" \
  "s_add_u32 s58, s58, 1\n" \
  "s_cmp_lt_u32 s58, " STR(SPIN_CAP) "\n" \
  "s_cbranch_scc1 LB1_%=\n" \
  "v_mov_b32 v27, " STR(CW_DEAD) "*4\n" \
  "v_mov_b32 v28, 1\n" \
  "buffer_store_dword v28, v27, s[44:47], 0 offen sc1\n" \
  "s_branch LBI_%=\n" \
  "LBI_%=:\n" \
  "v_mov_b32 v27, v25\n" \
  "s_nop 1\n" \
  "s_branch LBH_%=\n" \
  "LBF_%=:\n" \
  "v_max3_u32 v25, v32, v33, v34\n" \
  "v_max3_u32 v25, v25, v35, v36\n" \
  "v_max3_u32 v25, v25, v37, v38\n" \
  "v_max3_u32 v25, v25, v39, v40\n" \
  "v_max3_u32 v25, v25, v41, v42\n" \
  "v_max3_u32 v25, v25, v43, v44\n" \
  "v_max3_u32 v25, v25, v45, v46\n" \
  "v_max_u32 v25, v25, v47\n" \
  "v_max3_u32 v25, v25, v96, v97\n" \
  "v_max3_u32 v25, v25, v98, v99\n" \
  "v_max3_u32 v25, v25, v100, v101\n" \
  "v_max3_u32 v25, v25, v102, v103\n" \
  "v_cmp_eq_u32 vcc, -1, v25\n" \
  "s_cbranch_vccz LBG_%=\n" \
  "s_add_u32 s58, s58, 1\n" \
  "s_cmp_lt_u32 s58, " STR(SPIN_CAP) "\n" \
  "s_cbranch_scc1 LB1_%=\n" \
  "v_mov_b32 v27, " STR(CW_DEAD) "*4\n" \
  "v_mov_b32 v28, 1\n" \
  "buffer_store_dword v28, v27, s[44:47], 0 offen sc1\n" \
  "s_branch LBG_%=\n" \
  "LB1_%=:\n" \
  "s_waitcnt vmcnt(0)\n" \
  "v_mov_b64 v[184:185], 0\n" \
  "v_mov_b64 v[186:187], 0\n" \
  "v_mov_b64 v[188:189], 0\n" \
  "v_mov_b64 v[190:191], 0\n" \
  "v_mov_b64 v[192:193], 0\n" \
  "v_mov_b64 v[194:195], 0\n" \
  "v_mov_b64 v[196:197], 0\n" \
  "v_mov_b64 v[198:199], 0\n" \
  "s_memtime s[66:67]\n" \
  "s_waitcnt lgkmcnt(0)\n" \
  "s_sub_u32 s59, " STR(F_B) "+" STR(O_B) ", s66\n" \
  "LQ_rb_%=:\n" \
  "s_add_u32 s64, s64, s95\n" \
  "s_add_i32 s61, s64, s59\n" \
  "s_cmp_lt_i32 s61, -" STR(T_TOL) "\n" \
  "s_cbranch_scc1 LQ_rb_%=\n" \
  "s_max_i32 s61, s61, 0\n" \
  "s_min_i32 s61, s61, 4000\n" \
  "s_bitcmp0_b32 s61, 4\n" \
  "s_cbranch_scc1 LX_rb_%=\n" \
  "s_nop 8\n" \
  "LX_rb_%=:\n" \
  "s_lshr_b32 s61, s61, 5\n" \
  "s_cbranch_scc0 LB_rb_%=\n" \
  ".p2align 4\n" \
  "LY_rb_%=:\n" \
  "s_sub_u32 s61, s61, 1\n" \
  "s_cmp_lg_u32 s61, 0\n" \
  "s_cbranch_scc1 LY_rb_%=\n" \
  "LB_rb_%=:\n" \
  "buffer_load_dwordx4 v[32:35], v80, s[36:39], s91 offen sc1\n" \
  "s_nop 11\n" \
  "buffer_load_dwordx4 v[36:39], v81, s[36:39], s91 offen sc1\n" \
  "s_nop 11\n" \
  "buffer_load_dwordx4 v[40:43], v82, s[36:39], s91 offen sc1\n" \
  "s_nop 11\n" \
  "buffer_load_dwordx4 v[44:47], v83, s[36:39], s91 offen sc1\n" \
  "s_nop 11\n" \
  "buffer_load_dwordx4 v[96:99], v17, s[36:39], s50 offen offset:0 sc1\n" \
  "s_nop 11\n" \
  "buffer_load_dwordx4 v[100:103], v17, s[36:39], s50 offen offset:1024 sc1\n" \
  "buffer_store_dword v27, v88, s[36:39], 0 offen sc1\n" \
  "s_mov_b32 vcc_lo, 0x33333333\n" \
  "s_mov_b32 vcc_hi, 0x33333333\n" \
  "s_branch LS2_%=\n" \
  "LW_c_%=:\n" \
  "s_min_i32 s61, s61, 4000\n" \
  "s_sub_i32 s61, s61, 72\n" \
  "s_max_i32 s61, s61, 0\n" \
  "s_bitcmp0_b32 s61, 4\n" \
  "s_cbranch_scc1 LX_c_%=\n" \
  "s_nop 8\n" \
  "LX_c_%=:\n" \
  "s_lshr_b32 s61, s61, 5\n" \
  "s_cbranch_scc0 LB_c_%=\n" \
  ".p2align 4\n" \
  "LY_c_%=:\n" \
  "s_sub_u32 s61, s61, 1\n" \
  "s_cmp_lg_u32 s61, 0\n" \
  "s_cbranch_scc1 LY_c_%=\n" \
  "s_branch LB_c_%=\n" \
  "LW_a_%=:\n" \
  "s_min_i32 s61, s61, 4000\n" \
  "s_sub_i32 s61, s61, 72\n" \
  "s_max_i32 s61, s61, 0\n" \
  "s_bitcmp0_b32 s61, 4\n" \
  "s_cbranch_scc1 LX_a_%=\n" \
  "s_nop 8\n" \
  "LX_a_%=:\n" \
  "s_lshr_b32 s61, s61, 5\n" \
  "s_cbranch_scc0 LB_a_%=\n" \
  ".p2align 4\n" \
  "LY_a_%=:\n" \
  "s_sub_u32 s61, s61, 1\n" \
  "s_cmp_lg_u32 s61, 0\n" \
  "s_cbranch_scc1 LY_a_%=\n" \
  "s_branch LB_a_%=\n" \
  "LN_%=:\n" \
  "LN3_%=:\n" \
  "s_add_u32 s63, s63, s95\n" \
  "s_add_i32 s61, s61, s95\n" \
  "s_add_i32 s59, s59, s95\n" \
  "s_cmp_lt_i32 s59, 0\n" \
  "s_cbranch_scc1 LN3_%=\n" \
  "s_branch LN2_%=\n" \
  "LC_%=:\n" \
  "v_max3_u32 v25, v32, v33, v34\n" \
  "v_max3_u32 v25, v25, v35, v36\n" \
  "v_max3_u32 v25, v25, v37, v38\n" \
  "v_max3_u32 v25, v25, v39, v40\n" \
  "v_max3_u32 v25, v25, v41, v42\n" \
  "v_max3_u32 v25, v25, v43, v44\n" \
  "v_max3_u32 v25, v25, v45, v46\n" \
  "v_max_u32 v25, v25, v47\n" \
  "v_cmp_eq_u32 vcc, -1, v25\n" \
  "s_cbranch_vccz LC2_%=\n" \
  "s_add_u32 s58, s58, 1\n" \
  "s_cmp_lt_u32 s58, " STR(SPIN_CAP) "\n" \
  "s_cbranch_scc1 LC1_%=\n" \
  "v_mov_b32 v27, " STR(CW_DEAD) "*4\n" \
  "v_mov_b32 v28, 1\n" \
  "buffer_store_dword v28, v27, s[44:47], 0 offen sc1\n" \
  "s_branch LC2_%=\n" \
  "LC1_%=:\n" \
  "s_add_u32 s60, s96, " STR(O_C) "\n" \
  "s_memtime s[66:67]\n" \
  "s_waitcnt lgkmcnt(0)\n" \
  "s_sub_u32 s59, s60, s66\n" \
  "LQ_rc_%=:\n" \
  "s_add_u32 s64, s64, s95\n" \
  "s_add_i32 s61, s64, s59\n" \
  "s_cmp_lt_i32 s61, -" STR(T_TOL) "\n" \
  "s_cbranch_scc1 LQ_rc_%=\n" \
  "s_max_i32 s61, s61, 0\n" \
  "s_min_i32 s61, s61, 4000\n" \
  "s_bitcmp0_b32 s61, 4\n" \
  "s_cbranch_scc1 LX_rc_%=\n" \
  "s_nop 8\n" \
  "LX_rc_%=:\n" \
  "s_lshr_b32 s61, s61, 5\n" \
  "s_cbranch_scc0 LB_rc_%=\n" \
  ".p2align 4\n" \
  "LY_rc_%=:\n" \
  "s_sub_u32 s61, s61, 1\n" \
  "s_cmp_lg_u32 s61, 0\n" \
  "s_cbranch_scc1 LY_rc_%=\n" \
  "LB_rc_%=:\n" \
  "buffer_load_dwordx4 v[32:35], v11, s[36:39], s51 offen offset:0 sc1\n" \
  "buffer_load_dwordx4 v[36:39], v11, s[36:39], s51 offen offset:1024 sc1\n" \
  "buffer_load_dwordx4 v[40:43], v11, s[36:39], s51 offen offset:2048 sc1\n" \
  "buffer_load_dwordx4 v[44:47], v11, s[36:39], s51 offen offset:3072 sc1\n" \
  "s_branch LS3_%=\n" \
  "LW_p_%=:\n" \
  "s_min_i32 s61, s61, 4000\n" \
  "s_sub_i32 s61, s61, 72\n" \
  "s_max_i32 s61, s61, 0\n" \
  "s_bitcmp0_b32 s61, 4\n" \
  "s_cbranch_scc1 LX_p_%=\n" \
  "s_nop 8\n" \
  "LX_p_%=:\n" \
  "s_lshr_b32 s61, s61, 5\n" \
  "s_cbranch_scc0 LB_p_%=\n" \
  ".p2align 4\n" \
  "LY_p_%=:\n" \
  "s_sub_u32 s61, s61, 1\n" \
  "s_cmp_lg_u32 s61, 0\n" \
  "s_cbranch_scc1 LY_p_%=\n" \
  "s_branch LB_p_%=\n" \
  "LF_%=:\n" \
  "buffer_load_dword v23, v22, s[44:47], 0 offen sc1\n" \
  "s_mov_b32 s61, 0\n" \
  "s_waitcnt vmcnt(0)\n" \
  "v_cmp_gt_u32 vcc, s56, v23\n" \
  "s_cbranch_vccz LF2_%=\n" \
  "s_sleep 2\n" \
  "s_add_u32 s58, s58, 1\n" \
  "s_cmp_lt_u32 s58, " STR(SPIN_CAP) "\n" \
  "s_cbranch_scc1 LF_%=\n" \
  "v_mov_b32 v27, " STR(CW_DEAD) "*4\n" \
  "v_mov_b32 v28, 1\n" \
  "buffer_store_dword v28, v27, s[44:47], 0 offen sc1\n" \
  "s_branch LF2_%=\n" \
  "LK3_%=:\n" \
  "s_sleep 2\n" \
  "buffer_load_dwordx2 v[92:93], v13, s[36:39], s50 offen sc1\n" \
  "s_add_u32 s58, s58, 1\n" \
  "s_cmp_lt_u32 s58, " STR(SPIN_CAP) "\n" \
  "s_cbranch_scc1 LK2_%=\n" \
  "v_mov_b32 v27, " STR(CW_DEAD) "*4\n" \
  "v_mov_b32 v28, 1\n" \
  "buffer_store_dword v28, v27, s[44:47], 0 offen sc1\n" \
  "s_branch LK6_%=\n" \
  "LK6_%=:\n" \
  "v_mov_b32 v25, 0\n" \
  "s_branch LK5_%=\n" \
  "LE_%=:\n" \
  "s_add_u32 s59, s48, 1\n" \
  "v_mov_b32 v28, s59\n" \
  "buffer_store_dword v28, v0, s[44:47], 0 offen sc1\n" \
  "s_endpgm\n" \
  ""

#define GEMM_ASM \
  "v_mbcnt_lo_u32_b32 v4, -1, 0\n" \
  "v_mbcnt_hi_u32_b32 v4, -1, v4\n" \
  "v_and_b32 v116, 15, v4\n" \
  "v_lshrrev_b32 v117, 4, v4\n" \
  "s_lshl_b32 s86, %7, 13\n" \
  "s_add_u32 s86, s86, %8\n" \
  "v_lshrrev_b32 v5, 3, v4\n" \
  "v_and_b32 v6, 7, v4\n" \
  "v_lshlrev_b32 v8, 13, v5\n" \
  "v_lshl_add_u32 v8, v6, 4, v8\n" \
  "v_and_b32 v9, 1, v117\n" \
  "v_lshlrev_b32 v9, 5, v9\n" \
  "v_xor_b32 v8, v8, v9\n" \
  "v_xor_b32 v9, 64, v8\n" \
  "s_mov_b64 s[68:69], %1\n" \
  "s_and_b32 s69, s69, 0xffff\n" \
  "s_mov_b32 s70, 8388608\n" \
  "s_mov_b32 s71, 0x00020000\n" \
  "s_lshl_b32 s92, %7, 19\n" \
  "s_add_u32 s92, s92, 0\n" \
  "s_add_u32 m0, s86, 98304\n" \
  "s_nop 0\n" \
  "buffer_load_dwordx4 v8, s[68:71], s92 offen lds\n" \
  "s_add_u32 m0, s86, 99328\n" \
  "s_add_u32 s92, s92, 65536\n" \
  "buffer_load_dwordx4 v9, s[68:71], s92 offen lds\n" \
  "s_add_u32 m0, s86, 100352\n" \
  "s_add_u32 s92, s92, 65536\n" \
  "buffer_load_dwordx4 v8, s[68:71], s92 offen lds\n" \
  "s_add_u32 m0, s86, 101376\n" \
  "s_add_u32 s92, s92, 65536\n" \
  "buffer_load_dwordx4 v9, s[68:71], s92 offen lds\n" \
  "s_add_u32 m0, s86, 102400\n" \
  "s_add_u32 s92, s92, 65536\n" \
  "buffer_load_dwordx4 v8, s[68:71], s92 offen lds\n" \
  "s_add_u32 m0, s86, 103424\n" \
  "s_add_u32 s92, s92, 65536\n" \
  "buffer_load_dwordx4 v9, s[68:71], s92 offen lds\n" \
  "s_add_u32 m0, s86, 104448\n" \
  "s_add_u32 s92, s92, 65536\n" \
  "buffer_load_dwordx4 v8, s[68:71], s92 offen lds\n" \
  "s_add_u32 m0, s86, 105472\n" \
  "s_add_u32 s92, s92, 65536\n" \
  "buffer_load_dwordx4 v9, s[68:71], s92 offen lds\n" \
  "s_lshl_b32 s92, %7, 19\n" \
  "s_add_u32 s92, s92, 2097152\n" \
  "s_add_u32 m0, s86, 131072\n" \
  "s_nop 0\n" \
  "buffer_load_dwordx4 v8, s[68:71], s92 offen lds\n" \
  "s_add_u32 m0, s86, 132096\n" \
  "s_add_u32 s92, s92, 65536\n" \
  "buffer_load_dwordx4 v9, s[68:71], s92 offen lds\n" \
  "s_add_u32 m0, s86, 133120\n" \
  "s_add_u32 s92, s92, 65536\n" \
  "buffer_load_dwordx4 v8, s[68:71], s92 offen lds\n" \
  "s_add_u32 m0, s86, 134144\n" \
  "s_add_u32 s92, s92, 65536\n" \
  "buffer_load_dwordx4 v9, s[68:71], s92 offen lds\n" \
  "s_add_u32 m0, s86, 135168\n" \
  "s_add_u32 s92, s92, 65536\n" \
  "buffer_load_dwordx4 v8, s[68:71], s92 offen lds\n" \
  "s_add_u32 m0, s86, 136192\n" \
  "s_add_u32 s92, s92, 65536\n" \
  "buffer_load_dwordx4 v9, s[68:71], s92 offen lds\n" \
  "s_add_u32 m0, s86, 137216\n" \
  "s_add_u32 s92, s92, 65536\n" \
  "buffer_load_dwordx4 v8, s[68:71], s92 offen lds\n" \
  "s_add_u32 m0, s86, 138240\n" \
  "s_add_u32 s92, s92, 65536\n" \
  "buffer_load_dwordx4 v9, s[68:71], s92 offen lds\n" \
  "s_lshl_b32 s92, %7, 19\n" \
  "s_add_u32 s92, s92, 4194304\n" \
  "s_add_u32 m0, s86, 0\n" \
  "s_nop 0\n" \
  "buffer_load_dwordx4 v8, s[68:71], s92 offen lds\n" \
  "s_add_u32 m0, s86, 1024\n" \
  "s_add_u32 s92, s92, 65536\n" \
  "buffer_load_dwordx4 v9, s[68:71], s92 offen lds\n" \
  "s_add_u32 m0, s86, 2048\n" \
  "s_add_u32 s92, s92, 65536\n" \
  "buffer_load_dwordx4 v8, s[68:71], s92 offen lds\n" \
  "s_add_u32 m0, s86, 3072\n" \
  "s_add_u32 s92, s92, 65536\n" \
  "buffer_load_dwordx4 v9, s[68:71], s92 offen lds\n" \
  "s_add_u32 m0, s86, 4096\n" \
  "s_add_u32 s92, s92, 65536\n" \
  "buffer_load_dwordx4 v8, s[68:71], s92 offen lds\n" \
  "s_add_u32 m0, s86, 5120\n" \
  "s_add_u32 s92, s92, 65536\n" \
  "buffer_load_dwordx4 v9, s[68:71], s92 offen lds\n" \
  "s_add_u32 m0, s86, 6144\n" \
  "s_add_u32 s92, s92, 65536\n" \
  "buffer_load_dwordx4 v8, s[68:71], s92 offen lds\n" \
  "s_add_u32 m0, s86, 7168\n" \
  "s_add_u32 s92, s92, 65536\n" \
  "buffer_load_dwordx4 v9, s[68:71], s92 offen lds\n" \
  "s_lshl_b32 s92, %7, 19\n" \
  "s_add_u32 s92, s92, 6291456\n" \
  "s_add_u32 m0, s86, 32768\n" \
  "s_nop 0\n" \
  "buffer_load_dwordx4 v8, s[68:71], s92 offen lds\n" \
  "s_add_u32 m0, s86, 33792\n" \
  "s_add_u32 s92, s92, 65536\n" \
  "buffer_load_dwordx4 v9, s[68:71], s92 offen lds\n" \
  "s_add_u32 m0, s86, 34816\n" \
  "s_add_u32 s92, s92, 65536\n" \
  "buffer_load_dwordx4 v8, s[68:71], s92 offen lds\n" \
  "s_add_u32 m0, s86, 35840\n" \
  "s_add_u32 s92, s92, 65536\n" \
  "buffer_load_dwordx4 v9, s[68:71], s92 offen lds\n" \
  "s_add_u32 m0, s86, 36864\n" \
  "s_add_u32 s92, s92, 65536\n" \
  "buffer_load_dwordx4 v8, s[68:71], s92 offen lds\n" \
  "s_add_u32 m0, s86, 37888\n" \
  "s_add_u32 s92, s92, 65536\n" \
  "buffer_load_dwordx4 v9, s[68:71], s92 offen lds\n" \
  "s_add_u32 m0, s86, 38912\n" \
  "s_add_u32 s92, s92, 65536\n" \
  "buffer_load_dwordx4 v8, s[68:71], s92 offen lds\n" \
  "s_add_u32 m0, s86, 39936\n" \
  "s_add_u32 s92, s92, 65536\n" \
  "buffer_load_dwordx4 v9, s[68:71], s92 offen lds\n" \
  "s_mov_b64 s[72:73], %3\n" \
  "s_and_b32 s73, s73, 0xffff\n" \
  "s_mov_b32 s74, 4194304\n" \
  "s_mov_b32 s75, 0x00020000\n" \
  "s_mov_b64 s[76:77], %4\n" \
  "s_and_b32 s77, s77, 0xffff\n" \
  "s_mov_b32 s78, 3*" STR(QKV_SZ32) "\n" \
  "s_mov_b32 s79, 0x00020000\n" \
  "s_mov_b64 s[80:81], %5\n" \
  "s_and_b32 s81, s81, 0xffff\n" \
  "s_mov_b32 s82, " STR(CTRL_BYTES) "\n" \
  "s_mov_b32 s83, 0x00020000\n" \
  "v_xor_b32 v6, v6, v117\n" \
  "s_and_b32 s84, %7, 1\n" \
  "s_lshl_b32 s84, s84, 2\n" \
  "v_xor_b32 v6, s84, v6\n" \
  "v_lshlrev_b32 v7, 11, v5\n" \
  "v_lshl_add_u32 v7, v6, 4, v7\n" \
  "s_lshl_b32 s87, %7, 14\n" \
  "s_add_u32 s96, s87, 63*65536\n" \
  "v_lshrrev_b32 v5, 1, v116\n" \
  "v_xor_b32 v5, v5, v117\n" \
  "v_lshlrev_b32 v5, 4, v5\n" \
  "v_lshl_add_u32 v5, v116, 7, v5\n" \
  "v_add_u32 v10, %8, v5\n" \
  "v_xor_b32 v5, 64, v5\n" \
  "v_add_u32 v11, %8, v5\n" \
  "v_add_u32 v12, 0x10000, v10\n" \
  "v_add_u32 v13, 0x10000, v11\n" \
  "s_lshr_b32 s84, %6, 1\n" \
  "s_lshl_b32 s84, s84, 14\n" \
  "s_and_b32 s85, %6, 1\n" \
  "s_lshl_b32 s85, s85, 2\n" \
  "s_add_u32 s85, s85, %7\n" \
  "s_lshl_b32 s91, s85, 9\n" \
  "s_add_u32 s91, s91, s84\n" \
  "v_and_b32 v5, 1, v117\n" \
  "v_lshlrev_b32 v14, 12, v5\n" \
  "v_lshrrev_b32 v5, 1, v117\n" \
  "v_lshl_add_u32 v14, v5, 8, v14\n" \
  "v_lshl_add_u32 v14, v116, 4, v14\n" \
  "v_add_u32 v14, s91, v14\n" \
  "s_lshl_b32 s91, s85, 10\n" \
  "s_add_u32 s91, s91, s84\n" \
  "v_lshlrev_b32 v15, 4, v4\n" \
  "v_add_u32 v15, s91, v15\n" \
  "v_xor_b32 v18, 0x800, v14\n" \
  "v_xor_b32 v15, 0x4000, v15\n" \
  "s_lshl_b32 s84, %6, 7\n" \
  "s_add_u32 s84, s84, " STR(CW_GPROG) "*4\n" \
  "v_or_b32 v5, %7, v4\n" \
  "v_lshl_add_u32 v16, v5, 24, s84\n" \
  "v_mov_b32 v17, 0\n" \
  "v_or_b32 v14, 0x80000000, v14\n" \
  "v_or_b32 v15, 0x80000000, v15\n" \
  "v_or_b32 v18, 0x80000000, v18\n" \
  "v_lshrrev_b32 v5, 2, v116\n" \
  "v_lshl_add_u32 v6, v117, 3, v5\n" \
  "v_lshlrev_b32 v6, 7, v6\n" \
  "v_lshrrev_b32 v5, 1, v5\n" \
  "v_and_b32 v119, 1, v117\n" \
  "v_lshl_add_u32 v5, v119, 1, v5\n" \
  "v_xor_b32 v5, %7, v5\n" \
  "v_lshl_add_u32 v6, v5, 5, v6\n" \
  "v_and_b32 v5, 3, v4\n" \
  "v_lshl_add_u32 v6, v5, 3, v6\n" \
  "v_add_u32 v118, %8, v6\n" \
  "v_add_u32 v119, 0x10000, v118\n" \
  "v_add_u32 v120, 0x20000, v118\n" \
  "s_lshl_b32 s84, %7, 10\n" \
  "s_add_u32 s91, s84, %8\n" \
  "s_waitcnt vmcnt(24) lgkmcnt(0)\n" \
  "s_barrier\n" \
  "s_mov_b64 s[68:69], %2\n" \
  "s_and_b32 s69, s69, 0xffff\n" \
  "s_mov_b32 s70, 8388608\n" \
  "s_mov_b32 s71, 0x00020000\n" \
  "s_lshl_b32 s92, %7, 19\n" \
  "s_add_u32 s92, s92, 0\n" \
  "s_add_u32 m0, s86, 65536\n" \
  "s_nop 0\n" \
  "buffer_load_dwordx4 v8, s[68:71], s92 offen lds\n" \
  "s_add_u32 m0, s86, 66560\n" \
  "s_add_u32 s92, s92, 65536\n" \
  "buffer_load_dwordx4 v9, s[68:71], s92 offen lds\n" \
  "s_add_u32 m0, s86, 67584\n" \
  "s_add_u32 s92, s92, 65536\n" \
  "buffer_load_dwordx4 v8, s[68:71], s92 offen lds\n" \
  "s_add_u32 m0, s86, 68608\n" \
  "s_add_u32 s92, s92, 65536\n" \
  "buffer_load_dwordx4 v9, s[68:71], s92 offen lds\n" \
  "s_add_u32 m0, s86, 69632\n" \
  "s_add_u32 s92, s92, 65536\n" \
  "buffer_load_dwordx4 v8, s[68:71], s92 offen lds\n" \
  "s_add_u32 m0, s86, 70656\n" \
  "s_add_u32 s92, s92, 65536\n" \
  "buffer_load_dwordx4 v9, s[68:71], s92 offen lds\n" \
  "s_add_u32 m0, s86, 71680\n" \
  "s_add_u32 s92, s92, 65536\n" \
  "buffer_load_dwordx4 v8, s[68:71], s92 offen lds\n" \
  "s_add_u32 m0, s86, 72704\n" \
  "s_add_u32 s92, s92, 65536\n" \
  "buffer_load_dwordx4 v9, s[68:71], s92 offen lds\n" \
  "ds_read_b64_tr_b16 a[0:1], v119 offset:32768\n" \
  "ds_read_b64_tr_b16 a[2:3], v119 offset:33280\n" \
  "ds_read_b64_tr_b16 a[4:5], v119 offset:36864\n" \
  "ds_read_b64_tr_b16 a[6:7], v119 offset:37376\n" \
  "ds_read_b64_tr_b16 a[8:9], v119 offset:40960\n" \
  "ds_read_b64_tr_b16 a[10:11], v119 offset:41472\n" \
  "ds_read_b64_tr_b16 a[12:13], v119 offset:45056\n" \
  "ds_read_b64_tr_b16 a[14:15], v119 offset:45568\n" \
  "ds_read_b64_tr_b16 a[16:17], v119 offset:49152\n" \
  "ds_read_b64_tr_b16 a[18:19], v119 offset:49664\n" \
  "ds_read_b64_tr_b16 a[20:21], v119 offset:53248\n" \
  "ds_read_b64_tr_b16 a[22:23], v119 offset:53760\n" \
  "ds_read_b64_tr_b16 a[24:25], v119 offset:57344\n" \
  "ds_read_b64_tr_b16 a[26:27], v119 offset:57856\n" \
  "ds_read_b64_tr_b16 a[28:29], v119 offset:61440\n" \
  "ds_read_b64_tr_b16 a[30:31], v119 offset:61952\n" \
  "s_waitcnt vmcnt(24) lgkmcnt(0)\n" \
  "s_barrier\n" \
  "s_lshl_b32 s92, %7, 19\n" \
  "s_add_u32 s92, s92, 2097152\n" \
  "s_add_u32 m0, s86, 98304\n" \
  "s_nop 0\n" \
  "buffer_load_dwordx4 v8, s[68:71], s92 offen lds\n" \
  "s_add_u32 m0, s86, 99328\n" \
  "s_add_u32 s92, s92, 65536\n" \
  "buffer_load_dwordx4 v9, s[68:71], s92 offen lds\n" \
  "s_add_u32 m0, s86, 100352\n" \
  "s_add_u32 s92, s92, 65536\n" \
  "buffer_load_dwordx4 v8, s[68:71], s92 offen lds\n" \
  "s_add_u32 m0, s86, 101376\n" \
  "s_add_u32 s92, s92, 65536\n" \
  "buffer_load_dwordx4 v9, s[68:71], s92 offen lds\n" \
  "s_add_u32 m0, s86, 102400\n" \
  "s_add_u32 s92, s92, 65536\n" \
  "buffer_load_dwordx4 v8, s[68:71], s92 offen lds\n" \
  "s_add_u32 m0, s86, 103424\n" \
  "s_add_u32 s92, s92, 65536\n" \
  "buffer_load_dwordx4 v9, s[68:71], s92 offen lds\n" \
  "s_add_u32 m0, s86, 104448\n" \
  "s_add_u32 s92, s92, 65536\n" \
  "buffer_load_dwordx4 v8, s[68:71], s92 offen lds\n" \
  "s_add_u32 m0, s86, 105472\n" \
  "s_add_u32 s92, s92, 65536\n" \
  "buffer_load_dwordx4 v9, s[68:71], s92 offen lds\n" \
  "ds_read_b64_tr_b16 a[32:33], v120 offset:0\n" \
  "ds_read_b64_tr_b16 a[34:35], v120 offset:512\n" \
  "ds_read_b64_tr_b16 a[36:37], v120 offset:4096\n" \
  "ds_read_b64_tr_b16 a[38:39], v120 offset:4608\n" \
  "ds_read_b64_tr_b16 a[40:41], v120 offset:8192\n" \
  "ds_read_b64_tr_b16 a[42:43], v120 offset:8704\n" \
  "ds_read_b64_tr_b16 a[44:45], v120 offset:12288\n" \
  "ds_read_b64_tr_b16 a[46:47], v120 offset:12800\n" \
  "ds_read_b64_tr_b16 a[48:49], v120 offset:16384\n" \
  "ds_read_b64_tr_b16 a[50:51], v120 offset:16896\n" \
  "ds_read_b64_tr_b16 a[52:53], v120 offset:20480\n" \
  "ds_read_b64_tr_b16 a[54:55], v120 offset:20992\n" \
  "ds_read_b64_tr_b16 a[56:57], v120 offset:24576\n" \
  "ds_read_b64_tr_b16 a[58:59], v120 offset:25088\n" \
  "ds_read_b64_tr_b16 a[60:61], v120 offset:28672\n" \
  "ds_read_b64_tr_b16 a[62:63], v120 offset:29184\n" \
  "s_waitcnt vmcnt(24) lgkmcnt(0)\n" \
  "s_barrier\n" \
  "s_lshl_b32 s92, %7, 19\n" \
  "s_add_u32 s92, s92, 4194304\n" \
  "s_add_u32 m0, s86, 131072\n" \
  "s_nop 0\n" \
  "buffer_load_dwordx4 v8, s[68:71], s92 offen lds\n" \
  "s_add_u32 m0, s86, 132096\n" \
  "s_add_u32 s92, s92, 65536\n" \
  "buffer_load_dwordx4 v9, s[68:71], s92 offen lds\n" \
  "s_add_u32 m0, s86, 133120\n" \
  "s_add_u32 s92, s92, 65536\n" \
  "buffer_load_dwordx4 v8, s[68:71], s92 offen lds\n" \
  "s_add_u32 m0, s86, 134144\n" \
  "s_add_u32 s92, s92, 65536\n" \
  "buffer_load_dwordx4 v9, s[68:71], s92 offen lds\n" \
  "s_add_u32 m0, s86, 135168\n" \
  "s_add_u32 s92, s92, 65536\n" \
  "buffer_load_dwordx4 v8, s[68:71], s92 offen lds\n" \
  "s_add_u32 m0, s86, 136192\n" \
  "s_add_u32 s92, s92, 65536\n" \
  "buffer_load_dwordx4 v9, s[68:71], s92 offen lds\n" \
  "s_add_u32 m0, s86, 137216\n" \
  "s_add_u32 s92, s92, 65536\n" \
  "buffer_load_dwordx4 v8, s[68:71], s92 offen lds\n" \
  "s_add_u32 m0, s86, 138240\n" \
  "s_add_u32 s92, s92, 65536\n" \
  "buffer_load_dwordx4 v9, s[68:71], s92 offen lds\n" \
  "ds_read_b64_tr_b16 a[64:65], v118 offset:0\n" \
  "ds_read_b64_tr_b16 a[66:67], v118 offset:512\n" \
  "ds_read_b64_tr_b16 a[68:69], v118 offset:4096\n" \
  "ds_read_b64_tr_b16 a[70:71], v118 offset:4608\n" \
  "ds_read_b64_tr_b16 a[72:73], v118 offset:8192\n" \
  "ds_read_b64_tr_b16 a[74:75], v118 offset:8704\n" \
  "ds_read_b64_tr_b16 a[76:77], v118 offset:12288\n" \
  "ds_read_b64_tr_b16 a[78:79], v118 offset:12800\n" \
  "ds_read_b64_tr_b16 a[80:81], v118 offset:16384\n" \
  "ds_read_b64_tr_b16 a[82:83], v118 offset:16896\n" \
  "ds_read_b64_tr_b16 a[84:85], v118 offset:20480\n" \
  "ds_read_b64_tr_b16 a[86:87], v118 offset:20992\n" \
  "ds_read_b64_tr_b16 a[88:89], v118 offset:24576\n" \
  "ds_read_b64_tr_b16 a[90:91], v118 offset:25088\n" \
  "ds_read_b64_tr_b16 a[92:93], v118 offset:28672\n" \
  "ds_read_b64_tr_b16 a[94:95], v118 offset:29184\n" \
  "s_waitcnt vmcnt(24) lgkmcnt(0)\n" \
  "s_barrier\n" \
  "s_lshl_b32 s92, %7, 19\n" \
  "s_add_u32 s92, s92, 6291456\n" \
  "s_add_u32 m0, s86, 0\n" \
  "s_nop 0\n" \
  "buffer_load_dwordx4 v8, s[68:71], s92 offen lds\n" \
  "s_add_u32 m0, s86, 1024\n" \
  "s_add_u32 s92, s92, 65536\n" \
  "buffer_load_dwordx4 v9, s[68:71], s92 offen lds\n" \
  "s_add_u32 m0, s86, 2048\n" \
  "s_add_u32 s92, s92, 65536\n" \
  "buffer_load_dwordx4 v8, s[68:71], s92 offen lds\n" \
  "s_add_u32 m0, s86, 3072\n" \
  "s_add_u32 s92, s92, 65536\n" \
  "buffer_load_dwordx4 v9, s[68:71], s92 offen lds\n" \
  "s_add_u32 m0, s86, 4096\n" \
  "s_add_u32 s92, s92, 65536\n" \
  "buffer_load_dwordx4 v8, s[68:71], s92 offen lds\n" \
  "s_add_u32 m0, s86, 5120\n" \
  "s_add_u32 s92, s92, 65536\n" \
  "buffer_load_dwordx4 v9, s[68:71], s92 offen lds\n" \
  "s_add_u32 m0, s86, 6144\n" \
  "s_add_u32 s92, s92, 65536\n" \
  "buffer_load_dwordx4 v8, s[68:71], s92 offen lds\n" \
  "s_add_u32 m0, s86, 7168\n" \
  "s_add_u32 s92, s92, 65536\n" \
  "buffer_load_dwordx4 v9, s[68:71], s92 offen lds\n" \
  "ds_read_b64_tr_b16 a[96:97], v118 offset:32768\n" \
  "ds_read_b64_tr_b16 a[98:99], v118 offset:33280\n" \
  "ds_read_b64_tr_b16 a[100:101], v118 offset:36864\n" \
  "ds_read_b64_tr_b16 a[102:103], v118 offset:37376\n" \
  "ds_read_b64_tr_b16 a[104:105], v118 offset:40960\n" \
  "ds_read_b64_tr_b16 a[106:107], v118 offset:41472\n" \
  "ds_read_b64_tr_b16 a[108:109], v118 offset:45056\n" \
  "ds_read_b64_tr_b16 a[110:111], v118 offset:45568\n" \
  "ds_read_b64_tr_b16 a[112:113], v118 offset:49152\n" \
  "ds_read_b64_tr_b16 a[114:115], v118 offset:49664\n" \
  "ds_read_b64_tr_b16 a[116:117], v118 offset:53248\n" \
  "ds_read_b64_tr_b16 a[118:119], v118 offset:53760\n" \
  "ds_read_b64_tr_b16 a[120:121], v118 offset:57344\n" \
  "ds_read_b64_tr_b16 a[122:123], v118 offset:57856\n" \
  "ds_read_b64_tr_b16 a[124:125], v118 offset:61440\n" \
  "ds_read_b64_tr_b16 a[126:127], v118 offset:61952\n" \
  "s_waitcnt vmcnt(24) lgkmcnt(0)\n" \
  "s_barrier\n" \
  "s_mov_b64 s[68:69], %0\n" \
  "s_and_b32 s69, s69, 0xffff\n" \
  "s_mov_b32 s70, 8388608\n" \
  "s_mov_b32 s71, 0x00020000\n" \
  "s_lshl_b32 s92, %7, 19\n" \
  "s_add_u32 s92, s92, 0\n" \
  "s_add_u32 m0, s86, 32768\n" \
  "s_nop 0\n" \
  "buffer_load_dwordx4 v8, s[68:71], s92 offen lds\n" \
  "s_add_u32 m0, s86, 33792\n" \
  "s_add_u32 s92, s92, 65536\n" \
  "buffer_load_dwordx4 v9, s[68:71], s92 offen lds\n" \
  "s_add_u32 m0, s86, 34816\n" \
  "s_add_u32 s92, s92, 65536\n" \
  "buffer_load_dwordx4 v8, s[68:71], s92 offen lds\n" \
  "s_add_u32 m0, s86, 35840\n" \
  "s_add_u32 s92, s92, 65536\n" \
  "buffer_load_dwordx4 v9, s[68:71], s92 offen lds\n" \
  "s_add_u32 m0, s86, 36864\n" \
  "s_add_u32 s92, s92, 65536\n" \
  "buffer_load_dwordx4 v8, s[68:71], s92 offen lds\n" \
  "s_add_u32 m0, s86, 37888\n" \
  "s_add_u32 s92, s92, 65536\n" \
  "buffer_load_dwordx4 v9, s[68:71], s92 offen lds\n" \
  "s_add_u32 m0, s86, 38912\n" \
  "s_add_u32 s92, s92, 65536\n" \
  "buffer_load_dwordx4 v8, s[68:71], s92 offen lds\n" \
  "s_add_u32 m0, s86, 39936\n" \
  "s_add_u32 s92, s92, 65536\n" \
  "buffer_load_dwordx4 v9, s[68:71], s92 offen lds\n" \
  "ds_read_b64_tr_b16 a[128:129], v119 offset:0\n" \
  "ds_read_b64_tr_b16 a[130:131], v119 offset:512\n" \
  "ds_read_b64_tr_b16 a[132:133], v119 offset:4096\n" \
  "ds_read_b64_tr_b16 a[134:135], v119 offset:4608\n" \
  "ds_read_b64_tr_b16 a[136:137], v119 offset:8192\n" \
  "ds_read_b64_tr_b16 a[138:139], v119 offset:8704\n" \
  "ds_read_b64_tr_b16 a[140:141], v119 offset:12288\n" \
  "ds_read_b64_tr_b16 a[142:143], v119 offset:12800\n" \
  "ds_read_b64_tr_b16 a[144:145], v119 offset:16384\n" \
  "ds_read_b64_tr_b16 a[146:147], v119 offset:16896\n" \
  "ds_read_b64_tr_b16 a[148:149], v119 offset:20480\n" \
  "ds_read_b64_tr_b16 a[150:151], v119 offset:20992\n" \
  "ds_read_b64_tr_b16 a[152:153], v119 offset:24576\n" \
  "ds_read_b64_tr_b16 a[154:155], v119 offset:25088\n" \
  "ds_read_b64_tr_b16 a[156:157], v119 offset:28672\n" \
  "ds_read_b64_tr_b16 a[158:159], v119 offset:29184\n" \
  "s_waitcnt vmcnt(24) lgkmcnt(0)\n" \
  "s_barrier\n" \
  "s_lshl_b32 s92, %7, 19\n" \
  "s_add_u32 s92, s92, 2097152\n" \
  "s_add_u32 m0, s86, 65536\n" \
  "s_nop 0\n" \
  "buffer_load_dwordx4 v8, s[68:71], s92 offen lds\n" \
  "s_add_u32 m0, s86, 66560\n" \
  "s_add_u32 s92, s92, 65536\n" \
  "buffer_load_dwordx4 v9, s[68:71], s92 offen lds\n" \
  "s_add_u32 m0, s86, 67584\n" \
  "s_add_u32 s92, s92, 65536\n" \
  "buffer_load_dwordx4 v8, s[68:71], s92 offen lds\n" \
  "s_add_u32 m0, s86, 68608\n" \
  "s_add_u32 s92, s92, 65536\n" \
  "buffer_load_dwordx4 v9, s[68:71], s92 offen lds\n" \
  "s_add_u32 m0, s86, 69632\n" \
  "s_add_u32 s92, s92, 65536\n" \
  "buffer_load_dwordx4 v8, s[68:71], s92 offen lds\n" \
  "s_add_u32 m0, s86, 70656\n" \
  "s_add_u32 s92, s92, 65536\n" \
  "buffer_load_dwordx4 v9, s[68:71], s92 offen lds\n" \
  "s_add_u32 m0, s86, 71680\n" \
  "s_add_u32 s92, s92, 65536\n" \
  "buffer_load_dwordx4 v8, s[68:71], s92 offen lds\n" \
  "s_add_u32 m0, s86, 72704\n" \
  "s_add_u32 s92, s92, 65536\n" \
  "buffer_load_dwordx4 v9, s[68:71], s92 offen lds\n" \
  "ds_read_b64_tr_b16 a[160:161], v119 offset:32768\n" \
  "ds_read_b64_tr_b16 a[162:163], v119 offset:33280\n" \
  "ds_read_b64_tr_b16 a[164:165], v119 offset:36864\n" \
  "ds_read_b64_tr_b16 a[166:167], v119 offset:37376\n" \
  "ds_read_b64_tr_b16 a[168:169], v119 offset:40960\n" \
  "ds_read_b64_tr_b16 a[170:171], v119 offset:41472\n" \
  "ds_read_b64_tr_b16 a[172:173], v119 offset:45056\n" \
  "ds_read_b64_tr_b16 a[174:175], v119 offset:45568\n" \
  "ds_read_b64_tr_b16 a[176:177], v119 offset:49152\n" \
  "ds_read_b64_tr_b16 a[178:179], v119 offset:49664\n" \
  "ds_read_b64_tr_b16 a[180:181], v119 offset:53248\n" \
  "ds_read_b64_tr_b16 a[182:183], v119 offset:53760\n" \
  "ds_read_b64_tr_b16 a[184:185], v119 offset:57344\n" \
  "ds_read_b64_tr_b16 a[186:187], v119 offset:57856\n" \
  "ds_read_b64_tr_b16 a[188:189], v119 offset:61440\n" \
  "ds_read_b64_tr_b16 a[190:191], v119 offset:61952\n" \
  "s_waitcnt vmcnt(24) lgkmcnt(0)\n" \
  "s_barrier\n" \
  "s_lshl_b32 s92, %7, 19\n" \
  "s_add_u32 s92, s92, 4194304\n" \
  "s_add_u32 m0, s86, 98304\n" \
  "s_nop 0\n" \
  "buffer_load_dwordx4 v8, s[68:71], s92 offen lds\n" \
  "s_add_u32 m0, s86, 99328\n" \
  "s_add_u32 s92, s92, 65536\n" \
  "buffer_load_dwordx4 v9, s[68:71], s92 offen lds\n" \
  "s_add_u32 m0, s86, 100352\n" \
  "s_add_u32 s92, s92, 65536\n" \
  "buffer_load_dwordx4 v8, s[68:71], s92 offen lds\n" \
  "s_add_u32 m0, s86, 101376\n" \
  "s_add_u32 s92, s92, 65536\n" \
  "buffer_load_dwordx4 v9, s[68:71], s92 offen lds\n" \
  "s_add_u32 m0, s86, 102400\n" \
  "s_add_u32 s92, s92, 65536\n" \
  "buffer_load_dwordx4 v8, s[68:71], s92 offen lds\n" \
  "s_add_u32 m0, s86, 103424\n" \
  "s_add_u32 s92, s92, 65536\n" \
  "buffer_load_dwordx4 v9, s[68:71], s92 offen lds\n" \
  "s_add_u32 m0, s86, 104448\n" \
  "s_add_u32 s92, s92, 65536\n" \
  "buffer_load_dwordx4 v8, s[68:71], s92 offen lds\n" \
  "s_add_u32 m0, s86, 105472\n" \
  "s_add_u32 s92, s92, 65536\n" \
  "buffer_load_dwordx4 v9, s[68:71], s92 offen lds\n" \
  "ds_read_b64_tr_b16 a[192:193], v120 offset:0\n" \
  "ds_read_b64_tr_b16 a[194:195], v120 offset:512\n" \
  "ds_read_b64_tr_b16 a[196:197], v120 offset:4096\n" \
  "ds_read_b64_tr_b16 a[198:199], v120 offset:4608\n" \
  "ds_read_b64_tr_b16 a[200:201], v120 offset:8192\n" \
  "ds_read_b64_tr_b16 a[202:203], v120 offset:8704\n" \
  "ds_read_b64_tr_b16 a[204:205], v120 offset:12288\n" \
  "ds_read_b64_tr_b16 a[206:207], v120 offset:12800\n" \
  "ds_read_b64_tr_b16 a[208:209], v120 offset:16384\n" \
  "ds_read_b64_tr_b16 a[210:211], v120 offset:16896\n" \
  "ds_read_b64_tr_b16 a[212:213], v120 offset:20480\n" \
  "ds_read_b64_tr_b16 a[214:215], v120 offset:20992\n" \
  "ds_read_b64_tr_b16 a[216:217], v120 offset:24576\n" \
  "ds_read_b64_tr_b16 a[218:219], v120 offset:25088\n" \
  "ds_read_b64_tr_b16 a[220:221], v120 offset:28672\n" \
  "ds_read_b64_tr_b16 a[222:223], v120 offset:29184\n" \
  "s_waitcnt vmcnt(24) lgkmcnt(0)\n" \
  "s_barrier\n" \
  "s_lshl_b32 s92, %7, 19\n" \
  "s_add_u32 s92, s92, 6291456\n" \
  "s_add_u32 m0, s86, 131072\n" \
  "s_nop 0\n" \
  "buffer_load_dwordx4 v8, s[68:71], s92 offen lds\n" \
  "s_add_u32 m0, s86, 132096\n" \
  "s_add_u32 s92, s92, 65536\n" \
  "buffer_load_dwordx4 v9, s[68:71], s92 offen lds\n" \
  "s_add_u32 m0, s86, 133120\n" \
  "s_add_u32 s92, s92, 65536\n" \
  "buffer_load_dwordx4 v8, s[68:71], s92 offen lds\n" \
  "s_add_u32 m0, s86, 134144\n" \
  "s_add_u32 s92, s92, 65536\n" \
  "buffer_load_dwordx4 v9, s[68:71], s92 offen lds\n" \
  "s_add_u32 m0, s86, 135168\n" \
  "s_add_u32 s92, s92, 65536\n" \
  "buffer_load_dwordx4 v8, s[68:71], s92 offen lds\n" \
  "s_add_u32 m0, s86, 136192\n" \
  "s_add_u32 s92, s92, 65536\n" \
  "buffer_load_dwordx4 v9, s[68:71], s92 offen lds\n" \
  "s_add_u32 m0, s86, 137216\n" \
  "s_add_u32 s92, s92, 65536\n" \
  "buffer_load_dwordx4 v8, s[68:71], s92 offen lds\n" \
  "s_add_u32 m0, s86, 138240\n" \
  "s_add_u32 s92, s92, 65536\n" \
  "buffer_load_dwordx4 v9, s[68:71], s92 offen lds\n" \
  "ds_read_b64_tr_b16 a[224:225], v118 offset:0\n" \
  "ds_read_b64_tr_b16 a[226:227], v118 offset:512\n" \
  "ds_read_b64_tr_b16 a[228:229], v118 offset:4096\n" \
  "ds_read_b64_tr_b16 a[230:231], v118 offset:4608\n" \
  "ds_read_b64_tr_b16 a[232:233], v118 offset:8192\n" \
  "ds_read_b64_tr_b16 a[234:235], v118 offset:8704\n" \
  "ds_read_b64_tr_b16 a[236:237], v118 offset:12288\n" \
  "ds_read_b64_tr_b16 a[238:239], v118 offset:12800\n" \
  "ds_read_b64_tr_b16 a[240:241], v118 offset:16384\n" \
  "ds_read_b64_tr_b16 a[242:243], v118 offset:16896\n" \
  "ds_read_b64_tr_b16 a[244:245], v118 offset:20480\n" \
  "ds_read_b64_tr_b16 a[246:247], v118 offset:20992\n" \
  "ds_read_b64_tr_b16 a[248:249], v118 offset:24576\n" \
  "ds_read_b64_tr_b16 a[250:251], v118 offset:25088\n" \
  "ds_read_b64_tr_b16 a[252:253], v118 offset:28672\n" \
  "ds_read_b64_tr_b16 a[254:255], v118 offset:29184\n" \
  "s_waitcnt vmcnt(24) lgkmcnt(0)\n" \
  "s_barrier\n" \
  "s_add_u32 m0, s91, 0\n" \
  "s_nop 0\n" \
  "buffer_load_dwordx4 v7, s[72:75], s87 offen lds\n" \
  "s_add_u32 s87, s87, 128\n" \
  "s_add_u32 m0, s91, 4096\n" \
  "s_nop 0\n" \
  "buffer_load_dwordx4 v7, s[72:75], s87 offen lds\n" \
  "s_add_u32 s87, s87, 128\n" \
  "s_add_u32 m0, s91, 8192\n" \
  "s_nop 0\n" \
  "buffer_load_dwordx4 v7, s[72:75], s87 offen lds\n" \
  "s_add_u32 s87, s87, 128\n" \
  "s_add_u32 m0, s91, 12288\n" \
  "s_nop 0\n" \
  "buffer_load_dwordx4 v7, s[72:75], s87 offen lds\n" \
  "s_add_u32 s87, s87, 128\n" \
  "s_add_u32 m0, s91, 16384\n" \
  "s_nop 0\n" \
  "buffer_load_dwordx4 v7, s[72:75], s87 offen lds\n" \
  "s_add_u32 s87, s87, 128\n" \
  "s_add_u32 m0, s91, 20480\n" \
  "s_nop 0\n" \
  "buffer_load_dwordx4 v7, s[72:75], s87 offen lds\n" \
  "s_add_u32 s87, s87, 128\n" \
  "s_add_u32 m0, s91, 24576\n" \
  "s_nop 0\n" \
  "buffer_load_dwordx4 v7, s[72:75], s87 offen lds\n" \
  "s_add_u32 s87, s87, 128\n" \
  "s_add_u32 m0, s91, 28672\n" \
  "s_nop 0\n" \
  "buffer_load_dwordx4 v7, s[72:75], s87 offen lds\n" \
  "s_add_u32 s87, s87, 128\n" \
  "ds_read_b64_tr_b16 v[128:129], v118 offset:32768\n" \
  "ds_read_b64_tr_b16 v[130:131], v118 offset:33280\n" \
  "ds_read_b64_tr_b16 v[132:133], v118 offset:36864\n" \
  "ds_read_b64_tr_b16 v[134:135], v118 offset:37376\n" \
  "ds_read_b64_tr_b16 v[136:137], v118 offset:40960\n" \
  "ds_read_b64_tr_b16 v[138:139], v118 offset:41472\n" \
  "ds_read_b64_tr_b16 v[140:141], v118 offset:45056\n" \
  "ds_read_b64_tr_b16 v[142:143], v118 offset:45568\n" \
  "ds_read_b64_tr_b16 v[144:145], v118 offset:49152\n" \
  "ds_read_b64_tr_b16 v[146:147], v118 offset:49664\n" \
  "ds_read_b64_tr_b16 v[148:149], v118 offset:53248\n" \
  "ds_read_b64_tr_b16 v[150:151], v118 offset:53760\n" \
  "ds_read_b64_tr_b16 v[152:153], v118 offset:57344\n" \
  "ds_read_b64_tr_b16 v[154:155], v118 offset:57856\n" \
  "ds_read_b64_tr_b16 v[156:157], v118 offset:61440\n" \
  "ds_read_b64_tr_b16 v[158:159], v118 offset:61952\n" \
  "s_waitcnt vmcnt(24) lgkmcnt(0)\n" \
  "s_barrier\n" \
  "s_add_u32 m0, s91, 32768\n" \
  "s_nop 0\n" \
  "buffer_load_dwordx4 v7, s[72:75], s87 offen lds\n" \
  "s_add_u32 s87, s87, 128\n" \
  "s_add_u32 m0, s91, 36864\n" \
  "s_nop 0\n" \
  "buffer_load_dwordx4 v7, s[72:75], s87 offen lds\n" \
  "s_add_u32 s87, s87, 128\n" \
  "s_add_u32 m0, s91, 40960\n" \
  "s_nop 0\n" \
  "buffer_load_dwordx4 v7, s[72:75], s87 offen lds\n" \
  "s_add_u32 s87, s87, 128\n" \
  "s_add_u32 m0, s91, 45056\n" \
  "s_nop 0\n" \
  "buffer_load_dwordx4 v7, s[72:75], s87 offen lds\n" \
  "s_add_u32 s87, s87, 128\n" \
  "s_add_u32 m0, s91, 49152\n" \
  "s_nop 0\n" \
  "buffer_load_dwordx4 v7, s[72:75], s87 offen lds\n" \
  "s_add_u32 s87, s87, 128\n" \
  "s_add_u32 m0, s91, 53248\n" \
  "s_nop 0\n" \
  "buffer_load_dwordx4 v7, s[72:75], s87 offen lds\n" \
  "s_add_u32 s87, s87, 128\n" \
  "s_add_u32 m0, s91, 57344\n" \
  "s_nop 0\n" \
  "buffer_load_dwordx4 v7, s[72:75], s87 offen lds\n" \
  "s_add_u32 s87, s87, 128\n" \
  "s_add_u32 m0, s91, 61440\n" \
  "s_nop 0\n" \
  "buffer_load_dwordx4 v7, s[72:75], s87 offen lds\n" \
  "s_add_u32 s87, s87, 63616\n" \
  "s_min_u32 s87, s87, s96\n" \
  "ds_read_b64_tr_b16 v[160:161], v119 offset:0\n" \
  "ds_read_b64_tr_b16 v[162:163], v119 offset:512\n" \
  "ds_read_b64_tr_b16 v[164:165], v119 offset:4096\n" \
  "ds_read_b64_tr_b16 v[166:167], v119 offset:4608\n" \
  "ds_read_b64_tr_b16 v[168:169], v119 offset:8192\n" \
  "ds_read_b64_tr_b16 v[170:171], v119 offset:8704\n" \
  "ds_read_b64_tr_b16 v[172:173], v119 offset:12288\n" \
  "ds_read_b64_tr_b16 v[174:175], v119 offset:12800\n" \
  "ds_read_b64_tr_b16 v[176:177], v119 offset:16384\n" \
  "ds_read_b64_tr_b16 v[178:179], v119 offset:16896\n" \
  "ds_read_b64_tr_b16 v[180:181], v119 offset:20480\n" \
  "ds_read_b64_tr_b16 v[182:183], v119 offset:20992\n" \
  "ds_read_b64_tr_b16 v[184:185], v119 offset:24576\n" \
  "ds_read_b64_tr_b16 v[186:187], v119 offset:25088\n" \
  "ds_read_b64_tr_b16 v[188:189], v119 offset:28672\n" \
  "ds_read_b64_tr_b16 v[190:191], v119 offset:29184\n" \
  "s_waitcnt vmcnt(24) lgkmcnt(0)\n" \
  "s_barrier\n" \
  "s_add_u32 m0, s91, 65536\n" \
  "s_nop 0\n" \
  "buffer_load_dwordx4 v7, s[72:75], s87 offen lds\n" \
  "s_add_u32 s87, s87, 128\n" \
  "s_add_u32 m0, s91, 69632\n" \
  "s_nop 0\n" \
  "buffer_load_dwordx4 v7, s[72:75], s87 offen lds\n" \
  "s_add_u32 s87, s87, 128\n" \
  "s_add_u32 m0, s91, 73728\n" \
  "s_nop 0\n" \
  "buffer_load_dwordx4 v7, s[72:75], s87 offen lds\n" \
  "s_add_u32 s87, s87, 128\n" \
  "s_add_u32 m0, s91, 77824\n" \
  "s_nop 0\n" \
  "buffer_load_dwordx4 v7, s[72:75], s87 offen lds\n" \
  "s_add_u32 s87, s87, 128\n" \
  "s_add_u32 m0, s91, 81920\n" \
  "s_nop 0\n" \
  "buffer_load_dwordx4 v7, s[72:75], s87 offen lds\n" \
  "s_add_u32 s87, s87, 128\n" \
  "s_add_u32 m0, s91, 86016\n" \
  "s_nop 0\n" \
  "buffer_load_dwordx4 v7, s[72:75], s87 offen lds\n" \
  "s_add_u32 s87, s87, 128\n" \
  "s_add_u32 m0, s91, 90112\n" \
  "s_nop 0\n" \
  "buffer_load_dwordx4 v7, s[72:75], s87 offen lds\n" \
  "s_add_u32 s87, s87, 128\n" \
  "s_add_u32 m0, s91, 94208\n" \
  "s_nop 0\n" \
  "buffer_load_dwordx4 v7, s[72:75], s87 offen lds\n" \
  "s_add_u32 s87, s87, 128\n" \
  "ds_read_b64_tr_b16 v[192:193], v119 offset:32768\n" \
  "ds_read_b64_tr_b16 v[194:195], v119 offset:33280\n" \
  "ds_read_b64_tr_b16 v[196:197], v119 offset:36864\n" \
  "ds_read_b64_tr_b16 v[198:199], v119 offset:37376\n" \
  "ds_read_b64_tr_b16 v[200:201], v119 offset:40960\n" \
  "ds_read_b64_tr_b16 v[202:203], v119 offset:41472\n" \
  "ds_read_b64_tr_b16 v[204:205], v119 offset:45056\n" \
  "ds_read_b64_tr_b16 v[206:207], v119 offset:45568\n" \
  "ds_read_b64_tr_b16 v[208:209], v119 offset:49152\n" \
  "ds_read_b64_tr_b16 v[210:211], v119 offset:49664\n" \
  "ds_read_b64_tr_b16 v[212:213], v119 offset:53248\n" \
  "ds_read_b64_tr_b16 v[214:215], v119 offset:53760\n" \
  "ds_read_b64_tr_b16 v[216:217], v119 offset:57344\n" \
  "ds_read_b64_tr_b16 v[218:219], v119 offset:57856\n" \
  "ds_read_b64_tr_b16 v[220:221], v119 offset:61440\n" \
  "ds_read_b64_tr_b16 v[222:223], v119 offset:61952\n" \
  "s_waitcnt vmcnt(24) lgkmcnt(0)\n" \
  "s_barrier\n" \
  "ds_read_b64_tr_b16 v[224:225], v120 offset:0\n" \
  "ds_read_b64_tr_b16 v[226:227], v120 offset:512\n" \
  "ds_read_b64_tr_b16 v[228:229], v120 offset:4096\n" \
  "ds_read_b64_tr_b16 v[230:231], v120 offset:4608\n" \
  "ds_read_b64_tr_b16 v[232:233], v120 offset:8192\n" \
  "ds_read_b64_tr_b16 v[234:235], v120 offset:8704\n" \
  "ds_read_b64_tr_b16 v[236:237], v120 offset:12288\n" \
  "ds_read_b64_tr_b16 v[238:239], v120 offset:12800\n" \
  "ds_read_b64_tr_b16 v[240:241], v120 offset:16384\n" \
  "ds_read_b64_tr_b16 v[242:243], v120 offset:16896\n" \
  "ds_read_b64_tr_b16 v[244:245], v120 offset:20480\n" \
  "ds_read_b64_tr_b16 v[246:247], v120 offset:20992\n" \
  "ds_read_b64_tr_b16 v[248:249], v120 offset:24576\n" \
  "ds_read_b64_tr_b16 v[250:251], v120 offset:25088\n" \
  "ds_read_b64_tr_b16 v[252:253], v120 offset:28672\n" \
  "ds_read_b64_tr_b16 v[254:255], v120 offset:29184\n" \
  "s_waitcnt vmcnt(8) lgkmcnt(0)\n" \
  "s_barrier\n" \
  "ds_read_b128 v[68:71], v10 offset:0\n" \
  "ds_read_b128 v[72:75], v10 offset:2048\n" \
  "ds_read_b128 v[76:79], v11 offset:0\n" \
  "ds_read_b128 v[80:83], v11 offset:2048\n" \
  "ds_read_b128 v[84:87], v10 offset:4096\n" \
  "ds_read_b128 v[88:91], v10 offset:6144\n" \
  "s_mov_b32 s85, 0\n" \
  ".p2align 6\n" \
  "LG_%=:\n" \
  "s_cmp_eq_u32 s85, 64\n" \
  "s_cbranch_scc1 LX0_%=\n" \
  "s_waitcnt vmcnt(8)\n" \
  "s_barrier\n" \
  "s_waitcnt lgkmcnt(4)\n" \
  "v_mfma_f32_16x16x32_bf16 v[20:23], v[128:131], v[68:71], 0\n" \
  "ds_read_b128 v[92:95], v11 offset:4096\n" \
  "s_sub_u32 s89, s85, 1\n" \
  "s_and_b32 s89, s89, 63\n" \
  "v_mfma_f32_16x16x32_bf16 v[28:31], a[0:3], v[68:71], 0\n" \
  "ds_read_b128 v[96:99], v11 offset:6144\n" \
  "s_lshr_b32 s84, s89, 2\n" \
  "s_xnor_b32 s84, s84, %9\n" \
  "v_mfma_f32_16x16x32_bf16 v[36:39], v[68:71], a[128:131], 0\n" \
  "s_and_b32 s84, s84, 1\n" \
  "s_lshl_b32 s84, s84, 13\n" \
  "v_mfma_f32_16x16x32_bf16 v[24:27], v[128:131], v[72:75], 0\n" \
  "s_lshl_b32 s88, s89, 19\n" \
  "s_or_b32 s88, s88, s84\n" \
  "v_mfma_f32_16x16x32_bf16 v[32:35], a[0:3], v[72:75], 0\n" \
  "s_add_u32 s89, s88, " STR(QKV_SZ32) "\n" \
  "s_add_u32 s90, s89, " STR(QKV_SZ32) "\n" \
  "v_mfma_f32_16x16x32_bf16 v[40:43], v[72:75], a[128:131], 0\n" \
  "s_add_u32 s85, s85, 1\n" \
  "s_waitcnt lgkmcnt(4)\n" \
  "v_mfma_f32_16x16x32_bf16 v[20:23], v[132:135], v[76:79], v[20:23]\n" \
  "ds_read_b128 v[68:71], v10 offset:8192\n" \
  "v_cvt_pk_bf16_f32 v116, v44, v45\n" \
  "v_mfma_f32_16x16x32_bf16 v[28:31], a[4:7], v[76:79], v[28:31]\n" \
  "ds_read_b128 v[72:75], v10 offset:10240\n" \
  "v_cvt_pk_bf16_f32 v117, v46, v47\n" \
  "v_mfma_f32_16x16x32_bf16 v[36:39], v[76:79], a[132:135], v[36:39]\n" \
  "v_cvt_pk_bf16_f32 v118, v48, v49\n" \
  "v_mfma_f32_16x16x32_bf16 v[24:27], v[132:135], v[80:83], v[24:27]\n" \
  "v_cvt_pk_bf16_f32 v119, v50, v51\n" \
  "v_mfma_f32_16x16x32_bf16 v[32:35], a[4:7], v[80:83], v[32:35]\n" \
  "v_cvt_pk_bf16_f32 v120, v52, v53\n" \
  "v_mfma_f32_16x16x32_bf16 v[40:43], v[80:83], a[132:135], v[40:43]\n" \
  "v_cvt_pk_bf16_f32 v121, v54, v55\n" \
  "s_waitcnt lgkmcnt(4)\n" \
  "v_mfma_f32_16x16x32_bf16 v[20:23], v[136:139], v[84:87], v[20:23]\n" \
  "ds_read_b128 v[76:79], v11 offset:8192\n" \
  "v_cvt_pk_bf16_f32 v122, v56, v57\n" \
  "v_mfma_f32_16x16x32_bf16 v[28:31], a[8:11], v[84:87], v[28:31]\n" \
  "ds_read_b128 v[80:83], v11 offset:10240\n" \
  "v_cvt_pk_bf16_f32 v123, v58, v59\n" \
  "v_mfma_f32_16x16x32_bf16 v[36:39], v[84:87], a[136:139], v[36:39]\n" \
  "v_cvt_pk_bf16_f32 v124, v60, v61\n" \
  "v_mfma_f32_16x16x32_bf16 v[24:27], v[136:139], v[88:91], v[24:27]\n" \
  "v_cvt_pk_bf16_f32 v125, v62, v63\n" \
  "v_mfma_f32_16x16x32_bf16 v[32:35], a[8:11], v[88:91], v[32:35]\n" \
  "v_cvt_pk_bf16_f32 v126, v64, v65\n" \
  "v_mfma_f32_16x16x32_bf16 v[40:43], v[88:91], a[136:139], v[40:43]\n" \
  "v_cvt_pk_bf16_f32 v127, v66, v67\n" \
  "s_waitcnt lgkmcnt(4)\n" \
  "v_mfma_f32_16x16x32_bf16 v[20:23], v[140:143], v[92:95], v[20:23]\n" \
  "ds_read_b128 v[84:87], v10 offset:12288\n" \
  "v_permlane16_swap_b32 v116, v118\n" \
  "v_mfma_f32_16x16x32_bf16 v[28:31], a[12:15], v[92:95], v[28:31]\n" \
  "ds_read_b128 v[88:91], v10 offset:14336\n" \
  "v_permlane16_swap_b32 v117, v119\n" \
  "v_mfma_f32_16x16x32_bf16 v[36:39], v[92:95], a[140:143], v[36:39]\n" \
  "v_permlane16_swap_b32 v120, v122\n" \
  "v_mfma_f32_16x16x32_bf16 v[24:27], v[140:143], v[96:99], v[24:27]\n" \
  "v_permlane16_swap_b32 v121, v123\n" \
  "v_mfma_f32_16x16x32_bf16 v[32:35], a[12:15], v[96:99], v[32:35]\n" \
  "v_mfma_f32_16x16x32_bf16 v[40:43], v[96:99], a[140:143], v[40:43]\n" \
  "s_waitcnt lgkmcnt(4)\n" \
  "v_mfma_f32_16x16x32_bf16 v[20:23], v[144:147], v[68:71], v[20:23]\n" \
  "ds_read_b128 v[92:95], v11 offset:12288\n" \
  "buffer_store_dwordx4 v[116:119], v14, s[76:79], s88 offen sc1\n" \
  "v_mfma_f32_16x16x32_bf16 v[28:31], a[16:19], v[68:71], v[28:31]\n" \
  "ds_read_b128 v[96:99], v11 offset:14336\n" \
  "buffer_store_dwordx4 v[120:123], v18, s[76:79], s89 offen sc1\n" \
  "v_mfma_f32_16x16x32_bf16 v[36:39], v[68:71], a[144:147], v[36:39]\n" \
  "buffer_store_dwordx4 v[124:127], v15, s[76:79], s90 offen sc1\n" \
  "v_mfma_f32_16x16x32_bf16 v[24:27], v[144:147], v[72:75], v[24:27]\n" \
  "v_mfma_f32_16x16x32_bf16 v[32:35], a[16:19], v[72:75], v[32:35]\n" \
  "v_mfma_f32_16x16x32_bf16 v[40:43], v[72:75], a[144:147], v[40:43]\n" \
  "s_waitcnt lgkmcnt(4)\n" \
  "v_mfma_f32_16x16x32_bf16 v[20:23], v[148:151], v[76:79], v[20:23]\n" \
  "ds_read_b128 v[68:71], v10 offset:16384\n" \
  "v_mfma_f32_16x16x32_bf16 v[28:31], a[20:23], v[76:79], v[28:31]\n" \
  "ds_read_b128 v[72:75], v10 offset:18432\n" \
  "v_mfma_f32_16x16x32_bf16 v[36:39], v[76:79], a[148:151], v[36:39]\n" \
  "v_mfma_f32_16x16x32_bf16 v[24:27], v[148:151], v[80:83], v[24:27]\n" \
  "s_add_u32 m0, s91, 98304\n" \
  "s_nop 0\n" \
  "buffer_load_dwordx4 v7, s[72:75], s87 offen lds\n" \
  "s_add_u32 s87, s87, 128\n" \
  "v_mfma_f32_16x16x32_bf16 v[32:35], a[20:23], v[80:83], v[32:35]\n" \
  "v_mfma_f32_16x16x32_bf16 v[40:43], v[80:83], a[148:151], v[40:43]\n" \
  "s_waitcnt lgkmcnt(4)\n" \
  "v_mfma_f32_16x16x32_bf16 v[20:23], v[152:155], v[84:87], v[20:23]\n" \
  "ds_read_b128 v[76:79], v11 offset:16384\n" \
  "v_mfma_f32_16x16x32_bf16 v[28:31], a[24:27], v[84:87], v[28:31]\n" \
  "ds_read_b128 v[80:83], v11 offset:18432\n" \
  "v_mfma_f32_16x16x32_bf16 v[36:39], v[84:87], a[152:155], v[36:39]\n" \
  "v_mfma_f32_16x16x32_bf16 v[24:27], v[152:155], v[88:91], v[24:27]\n" \
  "s_add_u32 m0, s91, 102400\n" \
  "s_nop 0\n" \
  "buffer_load_dwordx4 v7, s[72:75], s87 offen lds\n" \
  "s_add_u32 s87, s87, 128\n" \
  "v_mfma_f32_16x16x32_bf16 v[32:35], a[24:27], v[88:91], v[32:35]\n" \
  "v_mfma_f32_16x16x32_bf16 v[40:43], v[88:91], a[152:155], v[40:43]\n" \
  "s_waitcnt lgkmcnt(4)\n" \
  "v_mfma_f32_16x16x32_bf16 v[20:23], v[156:159], v[92:95], v[20:23]\n" \
  "ds_read_b128 v[84:87], v10 offset:20480\n" \
  "v_mfma_f32_16x16x32_bf16 v[28:31], a[28:31], v[92:95], v[28:31]\n" \
  "ds_read_b128 v[88:91], v10 offset:22528\n" \
  "v_mfma_f32_16x16x32_bf16 v[36:39], v[92:95], a[156:159], v[36:39]\n" \
  "v_mfma_f32_16x16x32_bf16 v[24:27], v[156:159], v[96:99], v[24:27]\n" \
  "s_add_u32 m0, s91, 106496\n" \
  "s_nop 0\n" \
  "buffer_load_dwordx4 v7, s[72:75], s87 offen lds\n" \
  "s_add_u32 s87, s87, 128\n" \
  "v_mfma_f32_16x16x32_bf16 v[32:35], a[28:31], v[96:99], v[32:35]\n" \
  "v_mfma_f32_16x16x32_bf16 v[40:43], v[96:99], a[156:159], v[40:43]\n" \
  "s_waitcnt lgkmcnt(4)\n" \
  "v_mfma_f32_16x16x32_bf16 v[20:23], v[160:163], v[68:71], v[20:23]\n" \
  "ds_read_b128 v[92:95], v11 offset:20480\n" \
  "v_mfma_f32_16x16x32_bf16 v[28:31], a[32:35], v[68:71], v[28:31]\n" \
  "ds_read_b128 v[96:99], v11 offset:22528\n" \
  "v_mfma_f32_16x16x32_bf16 v[36:39], v[68:71], a[160:163], v[36:39]\n" \
  "v_mfma_f32_16x16x32_bf16 v[24:27], v[160:163], v[72:75], v[24:27]\n" \
  "s_add_u32 m0, s91, 110592\n" \
  "s_nop 0\n" \
  "buffer_load_dwordx4 v7, s[72:75], s87 offen lds\n" \
  "s_add_u32 s87, s87, 128\n" \
  "v_mfma_f32_16x16x32_bf16 v[32:35], a[32:35], v[72:75], v[32:35]\n" \
  "v_mfma_f32_16x16x32_bf16 v[40:43], v[72:75], a[160:163], v[40:43]\n" \
  "s_waitcnt lgkmcnt(4)\n" \
  "v_mfma_f32_16x16x32_bf16 v[20:23], v[164:167], v[76:79], v[20:23]\n" \
  "ds_read_b128 v[68:71], v10 offset:24576\n" \
  "v_mfma_f32_16x16x32_bf16 v[28:31], a[36:39], v[76:79], v[28:31]\n" \
  "ds_read_b128 v[72:75], v10 offset:26624\n" \
  "v_mfma_f32_16x16x32_bf16 v[36:39], v[76:79], a[164:167], v[36:39]\n" \
  "v_mfma_f32_16x16x32_bf16 v[24:27], v[164:167], v[80:83], v[24:27]\n" \
  "s_add_u32 m0, s91, 114688\n" \
  "s_nop 0\n" \
  "buffer_load_dwordx4 v7, s[72:75], s87 offen lds\n" \
  "s_add_u32 s87, s87, 128\n" \
  "v_mfma_f32_16x16x32_bf16 v[32:35], a[36:39], v[80:83], v[32:35]\n" \
  "v_mfma_f32_16x16x32_bf16 v[40:43], v[80:83], a[164:167], v[40:43]\n" \
  "s_waitcnt lgkmcnt(4)\n" \
  "v_mfma_f32_16x16x32_bf16 v[20:23], v[168:171], v[84:87], v[20:23]\n" \
  "ds_read_b128 v[76:79], v11 offset:24576\n" \
  "v_mfma_f32_16x16x32_bf16 v[28:31], a[40:43], v[84:87], v[28:31]\n" \
  "ds_read_b128 v[80:83], v11 offset:26624\n" \
  "v_mfma_f32_16x16x32_bf16 v[36:39], v[84:87], a[168:171], v[36:39]\n" \
  "v_mfma_f32_16x16x32_bf16 v[24:27], v[168:171], v[88:91], v[24:27]\n" \
  "s_add_u32 m0, s91, 118784\n" \
  "s_nop 0\n" \
  "buffer_load_dwordx4 v7, s[72:75], s87 offen lds\n" \
  "s_add_u32 s87, s87, 128\n" \
  "v_mfma_f32_16x16x32_bf16 v[32:35], a[40:43], v[88:91], v[32:35]\n" \
  "v_mfma_f32_16x16x32_bf16 v[40:43], v[88:91], a[168:171], v[40:43]\n" \
  "s_waitcnt lgkmcnt(4)\n" \
  "v_mfma_f32_16x16x32_bf16 v[20:23], v[172:175], v[92:95], v[20:23]\n" \
  "ds_read_b128 v[84:87], v10 offset:28672\n" \
  "v_mfma_f32_16x16x32_bf16 v[28:31], a[44:47], v[92:95], v[28:31]\n" \
  "ds_read_b128 v[88:91], v10 offset:30720\n" \
  "v_mfma_f32_16x16x32_bf16 v[36:39], v[92:95], a[172:175], v[36:39]\n" \
  "v_mfma_f32_16x16x32_bf16 v[24:27], v[172:175], v[96:99], v[24:27]\n" \
  "s_add_u32 m0, s91, 122880\n" \
  "s_nop 0\n" \
  "buffer_load_dwordx4 v7, s[72:75], s87 offen lds\n" \
  "s_add_u32 s87, s87, 128\n" \
  "v_mfma_f32_16x16x32_bf16 v[32:35], a[44:47], v[96:99], v[32:35]\n" \
  "v_mfma_f32_16x16x32_bf16 v[40:43], v[96:99], a[172:175], v[40:43]\n" \
  "s_waitcnt lgkmcnt(4)\n" \
  "v_mfma_f32_16x16x32_bf16 v[20:23], v[176:179], v[68:71], v[20:23]\n" \
  "ds_read_b128 v[92:95], v11 offset:28672\n" \
  "v_mfma_f32_16x16x32_bf16 v[28:31], a[48:51], v[68:71], v[28:31]\n" \
  "ds_read_b128 v[96:99], v11 offset:30720\n" \
  "v_mfma_f32_16x16x32_bf16 v[36:39], v[68:71], a[176:179], v[36:39]\n" \
  "v_mfma_f32_16x16x32_bf16 v[24:27], v[176:179], v[72:75], v[24:27]\n" \
  "s_add_u32 m0, s91, 126976\n" \
  "s_nop 0\n" \
  "buffer_load_dwordx4 v7, s[72:75], s87 offen lds\n" \
  "s_add_u32 s87, s87, 63616\n" \
  "s_min_u32 s87, s87, s96\n" \
  "v_mfma_f32_16x16x32_bf16 v[32:35], a[48:51], v[72:75], v[32:35]\n" \
  "v_mfma_f32_16x16x32_bf16 v[40:43], v[72:75], a[176:179], v[40:43]\n" \
  "s_waitcnt lgkmcnt(4)\n" \
  "v_mfma_f32_16x16x32_bf16 v[20:23], v[180:183], v[76:79], v[20:23]\n" \
  "ds_read_b128 v[68:71], v10 offset:32768\n" \
  "v_mfma_f32_16x16x32_bf16 v[28:31], a[52:55], v[76:79], v[28:31]\n" \
  "ds_read_b128 v[72:75], v10 offset:34816\n" \
  "v_mfma_f32_16x16x32_bf16 v[36:39], v[76:79], a[180:183], v[36:39]\n" \
  "v_mfma_f32_16x16x32_bf16 v[24:27], v[180:183], v[80:83], v[24:27]\n" \
  "v_mfma_f32_16x16x32_bf16 v[32:35], a[52:55], v[80:83], v[32:35]\n" \
  "v_mfma_f32_16x16x32_bf16 v[40:43], v[80:83], a[180:183], v[40:43]\n" \
  "s_waitcnt lgkmcnt(4)\n" \
  "v_mfma_f32_16x16x32_bf16 v[20:23], v[184:187], v[84:87], v[20:23]\n" \
  "ds_read_b128 v[76:79], v11 offset:32768\n" \
  "v_mfma_f32_16x16x32_bf16 v[28:31], a[56:59], v[84:87], v[28:31]\n" \
  "ds_read_b128 v[80:83], v11 offset:34816\n" \
  "v_mfma_f32_16x16x32_bf16 v[36:39], v[84:87], a[184:187], v[36:39]\n" \
  "v_mfma_f32_16x16x32_bf16 v[24:27], v[184:187], v[88:91], v[24:27]\n" \
  "v_mfma_f32_16x16x32_bf16 v[32:35], a[56:59], v[88:91], v[32:35]\n" \
  "v_mfma_f32_16x16x32_bf16 v[40:43], v[88:91], a[184:187], v[40:43]\n" \
  "s_waitcnt lgkmcnt(4)\n" \
  "v_mfma_f32_16x16x32_bf16 v[20:23], v[188:191], v[92:95], v[20:23]\n" \
  "ds_read_b128 v[84:87], v10 offset:36864\n" \
  "v_mfma_f32_16x16x32_bf16 v[28:31], a[60:63], v[92:95], v[28:31]\n" \
  "ds_read_b128 v[88:91], v10 offset:38912\n" \
  "v_mfma_f32_16x16x32_bf16 v[36:39], v[92:95], a[188:191], v[36:39]\n" \
  "v_mfma_f32_16x16x32_bf16 v[24:27], v[188:191], v[96:99], v[24:27]\n" \
  "v_mfma_f32_16x16x32_bf16 v[32:35], a[60:63], v[96:99], v[32:35]\n" \
  "v_mfma_f32_16x16x32_bf16 v[40:43], v[96:99], a[188:191], v[40:43]\n" \
  "s_waitcnt vmcnt(8)\n" \
  "s_barrier\n" \
  "s_waitcnt lgkmcnt(4)\n" \
  "v_mfma_f32_16x16x32_bf16 v[20:23], v[192:195], v[68:71], v[20:23]\n" \
  "ds_read_b128 v[92:95], v11 offset:36864\n" \
  "buffer_store_dword v17, v16, s[80:83], 0 offen sc1\n" \
  "v_mfma_f32_16x16x32_bf16 v[28:31], a[64:67], v[68:71], v[28:31]\n" \
  "ds_read_b128 v[96:99], v11 offset:38912\n" \
  "v_add_u32 v17, 1, v17\n" \
  "v_mfma_f32_16x16x32_bf16 v[36:39], v[68:71], a[192:195], v[36:39]\n" \
  "v_and_b32 v14, 0x7fffffff, v14\n" \
  "v_mfma_f32_16x16x32_bf16 v[24:27], v[192:195], v[72:75], v[24:27]\n" \
  "v_and_b32 v15, 0x7fffffff, v15\n" \
  "v_mfma_f32_16x16x32_bf16 v[32:35], a[64:67], v[72:75], v[32:35]\n" \
  "v_and_b32 v18, 0x7fffffff, v18\n" \
  "v_mfma_f32_16x16x32_bf16 v[40:43], v[72:75], a[192:195], v[40:43]\n" \
  "s_waitcnt lgkmcnt(4)\n" \
  "v_mfma_f32_16x16x32_bf16 v[20:23], v[196:199], v[76:79], v[20:23]\n" \
  "ds_read_b128 v[68:71], v10 offset:40960\n" \
  "v_mfma_f32_16x16x32_bf16 v[28:31], a[68:71], v[76:79], v[28:31]\n" \
  "ds_read_b128 v[72:75], v10 offset:43008\n" \
  "v_mfma_f32_16x16x32_bf16 v[36:39], v[76:79], a[196:199], v[36:39]\n" \
  "v_mfma_f32_16x16x32_bf16 v[24:27], v[196:199], v[80:83], v[24:27]\n" \
  "v_mfma_f32_16x16x32_bf16 v[32:35], a[68:71], v[80:83], v[32:35]\n" \
  "v_mfma_f32_16x16x32_bf16 v[40:43], v[80:83], a[196:199], v[40:43]\n" \
  "s_waitcnt lgkmcnt(4)\n" \
  "v_mfma_f32_16x16x32_bf16 v[20:23], v[200:203], v[84:87], v[20:23]\n" \
  "ds_read_b128 v[76:79], v11 offset:40960\n" \
  "v_mfma_f32_16x16x32_bf16 v[28:31], a[72:75], v[84:87], v[28:31]\n" \
  "ds_read_b128 v[80:83], v11 offset:43008\n" \
  "v_mfma_f32_16x16x32_bf16 v[36:39], v[84:87], a[200:203], v[36:39]\n" \
  "v_mfma_f32_16x16x32_bf16 v[24:27], v[200:203], v[88:91], v[24:27]\n" \
  "v_mfma_f32_16x16x32_bf16 v[32:35], a[72:75], v[88:91], v[32:35]\n" \
  "v_mfma_f32_16x16x32_bf16 v[40:43], v[88:91], a[200:203], v[40:43]\n" \
  "s_waitcnt lgkmcnt(4)\n" \
  "v_mfma_f32_16x16x32_bf16 v[20:23], v[204:207], v[92:95], v[20:23]\n" \
  "ds_read_b128 v[84:87], v10 offset:45056\n" \
  "v_mfma_f32_16x16x32_bf16 v[28:31], a[76:79], v[92:95], v[28:31]\n" \
  "ds_read_b128 v[88:91], v10 offset:47104\n" \
  "v_mfma_f32_16x16x32_bf16 v[36:39], v[92:95], a[204:207], v[36:39]\n" \
  "v_mfma_f32_16x16x32_bf16 v[24:27], v[204:207], v[96:99], v[24:27]\n" \
  "v_mfma_f32_16x16x32_bf16 v[32:35], a[76:79], v[96:99], v[32:35]\n" \
  "v_mfma_f32_16x16x32_bf16 v[40:43], v[96:99], a[204:207], v[40:43]\n" \
  "s_waitcnt lgkmcnt(4)\n" \
  "v_mfma_f32_16x16x32_bf16 v[20:23], v[208:211], v[68:71], v[20:23]\n" \
  "ds_read_b128 v[92:95], v11 offset:45056\n" \
  "v_mfma_f32_16x16x32_bf16 v[28:31], a[80:83], v[68:71], v[28:31]\n" \
  "ds_read_b128 v[96:99], v11 offset:47104\n" \
  "v_mfma_f32_16x16x32_bf16 v[36:39], v[68:71], a[208:211], v[36:39]\n" \
  "v_mfma_f32_16x16x32_bf16 v[24:27], v[208:211], v[72:75], v[24:27]\n" \
  "v_mfma_f32_16x16x32_bf16 v[32:35], a[80:83], v[72:75], v[32:35]\n" \
  "v_mfma_f32_16x16x32_bf16 v[40:43], v[72:75], a[208:211], v[40:43]\n" \
  "s_waitcnt lgkmcnt(4)\n" \
  "v_mfma_f32_16x16x32_bf16 v[20:23], v[212:215], v[76:79], v[20:23]\n" \
  "ds_read_b128 v[68:71], v10 offset:49152\n" \
  "v_mfma_f32_16x16x32_bf16 v[28:31], a[84:87], v[76:79], v[28:31]\n" \
  "ds_read_b128 v[72:75], v10 offset:51200\n" \
  "v_mfma_f32_16x16x32_bf16 v[36:39], v[76:79], a[212:215], v[36:39]\n" \
  "v_mfma_f32_16x16x32_bf16 v[24:27], v[212:215], v[80:83], v[24:27]\n" \
  "s_add_u32 m0, s91, 0\n" \
  "s_nop 0\n" \
  "buffer_load_dwordx4 v7, s[72:75], s87 offen lds\n" \
  "s_add_u32 s87, s87, 128\n" \
  "v_mfma_f32_16x16x32_bf16 v[32:35], a[84:87], v[80:83], v[32:35]\n" \
  "v_mfma_f32_16x16x32_bf16 v[40:43], v[80:83], a[212:215], v[40:43]\n" \
  "s_waitcnt lgkmcnt(4)\n" \
  "v_mfma_f32_16x16x32_bf16 v[20:23], v[216:219], v[84:87], v[20:23]\n" \
  "ds_read_b128 v[76:79], v11 offset:49152\n" \
  "v_mfma_f32_16x16x32_bf16 v[28:31], a[88:91], v[84:87], v[28:31]\n" \
  "ds_read_b128 v[80:83], v11 offset:51200\n" \
  "v_mfma_f32_16x16x32_bf16 v[36:39], v[84:87], a[216:219], v[36:39]\n" \
  "v_mfma_f32_16x16x32_bf16 v[24:27], v[216:219], v[88:91], v[24:27]\n" \
  "s_add_u32 m0, s91, 4096\n" \
  "s_nop 0\n" \
  "buffer_load_dwordx4 v7, s[72:75], s87 offen lds\n" \
  "s_add_u32 s87, s87, 128\n" \
  "v_mfma_f32_16x16x32_bf16 v[32:35], a[88:91], v[88:91], v[32:35]\n" \
  "v_mfma_f32_16x16x32_bf16 v[40:43], v[88:91], a[216:219], v[40:43]\n" \
  "s_waitcnt lgkmcnt(4)\n" \
  "v_mfma_f32_16x16x32_bf16 v[20:23], v[220:223], v[92:95], v[20:23]\n" \
  "ds_read_b128 v[84:87], v10 offset:53248\n" \
  "v_mfma_f32_16x16x32_bf16 v[28:31], a[92:95], v[92:95], v[28:31]\n" \
  "ds_read_b128 v[88:91], v10 offset:55296\n" \
  "v_mfma_f32_16x16x32_bf16 v[36:39], v[92:95], a[220:223], v[36:39]\n" \
  "v_mfma_f32_16x16x32_bf16 v[24:27], v[220:223], v[96:99], v[24:27]\n" \
  "s_add_u32 m0, s91, 8192\n" \
  "s_nop 0\n" \
  "buffer_load_dwordx4 v7, s[72:75], s87 offen lds\n" \
  "s_add_u32 s87, s87, 128\n" \
  "v_mfma_f32_16x16x32_bf16 v[32:35], a[92:95], v[96:99], v[32:35]\n" \
  "v_mfma_f32_16x16x32_bf16 v[40:43], v[96:99], a[220:223], v[40:43]\n" \
  "s_waitcnt lgkmcnt(4)\n" \
  "v_mfma_f32_16x16x32_bf16 v[20:23], v[224:227], v[68:71], v[20:23]\n" \
  "ds_read_b128 v[92:95], v11 offset:53248\n" \
  "v_mfma_f32_16x16x32_bf16 v[28:31], a[96:99], v[68:71], v[28:31]\n" \
  "ds_read_b128 v[96:99], v11 offset:55296\n" \
  "v_mfma_f32_16x16x32_bf16 v[36:39], v[68:71], a[224:227], v[36:39]\n" \
  "v_mfma_f32_16x16x32_bf16 v[24:27], v[224:227], v[72:75], v[24:27]\n" \
  "s_add_u32 m0, s91, 12288\n" \
  "s_nop 0\n" \
  "buffer_load_dwordx4 v7, s[72:75], s87 offen lds\n" \
  "s_add_u32 s87, s87, 128\n" \
  "v_mfma_f32_16x16x32_bf16 v[32:35], a[96:99], v[72:75], v[32:35]\n" \
  "v_mfma_f32_16x16x32_bf16 v[40:43], v[72:75], a[224:227], v[40:43]\n" \
  "s_waitcnt lgkmcnt(4)\n" \
  "v_mfma_f32_16x16x32_bf16 v[20:23], v[228:231], v[76:79], v[20:23]\n" \
  "ds_read_b128 v[68:71], v10 offset:57344\n" \
  "v_mfma_f32_16x16x32_bf16 v[28:31], a[100:103], v[76:79], v[28:31]\n" \
  "ds_read_b128 v[72:75], v10 offset:59392\n" \
  "v_mfma_f32_16x16x32_bf16 v[36:39], v[76:79], a[228:231], v[36:39]\n" \
  "v_mfma_f32_16x16x32_bf16 v[24:27], v[228:231], v[80:83], v[24:27]\n" \
  "s_add_u32 m0, s91, 16384\n" \
  "s_nop 0\n" \
  "buffer_load_dwordx4 v7, s[72:75], s87 offen lds\n" \
  "s_add_u32 s87, s87, 128\n" \
  "v_mfma_f32_16x16x32_bf16 v[32:35], a[100:103], v[80:83], v[32:35]\n" \
  "v_mfma_f32_16x16x32_bf16 v[40:43], v[80:83], a[228:231], v[40:43]\n" \
  "s_waitcnt lgkmcnt(4)\n" \
  "v_mfma_f32_16x16x32_bf16 v[20:23], v[232:235], v[84:87], v[20:23]\n" \
  "ds_read_b128 v[76:79], v11 offset:57344\n" \
  "v_mfma_f32_16x16x32_bf16 v[28:31], a[104:107], v[84:87], v[28:31]\n" \
  "ds_read_b128 v[80:83], v11 offset:59392\n" \
  "v_mfma_f32_16x16x32_bf16 v[36:39], v[84:87], a[232:235], v[36:39]\n" \
  "v_mfma_f32_16x16x32_bf16 v[24:27], v[232:235], v[88:91], v[24:27]\n" \
  "s_add_u32 m0, s91, 20480\n" \
  "s_nop 0\n" \
  "buffer_load_dwordx4 v7, s[72:75], s87 offen lds\n" \
  "s_add_u32 s87, s87, 128\n" \
  "v_mfma_f32_16x16x32_bf16 v[32:35], a[104:107], v[88:91], v[32:35]\n" \
  "v_mfma_f32_16x16x32_bf16 v[40:43], v[88:91], a[232:235], v[40:43]\n" \
  "s_waitcnt lgkmcnt(4)\n" \
  "v_mfma_f32_16x16x32_bf16 v[20:23], v[236:239], v[92:95], v[20:23]\n" \
  "ds_read_b128 v[84:87], v10 offset:61440\n" \
  "v_mfma_f32_16x16x32_bf16 v[28:31], a[108:111], v[92:95], v[28:31]\n" \
  "ds_read_b128 v[88:91], v10 offset:63488\n" \
  "v_mfma_f32_16x16x32_bf16 v[36:39], v[92:95], a[236:239], v[36:39]\n" \
  "v_mfma_f32_16x16x32_bf16 v[24:27], v[236:239], v[96:99], v[24:27]\n" \
  "s_add_u32 m0, s91, 24576\n" \
  "s_nop 0\n" \
  "buffer_load_dwordx4 v7, s[72:75], s87 offen lds\n" \
  "s_add_u32 s87, s87, 128\n" \
  "v_mfma_f32_16x16x32_bf16 v[32:35], a[108:111], v[96:99], v[32:35]\n" \
  "v_mfma_f32_16x16x32_bf16 v[40:43], v[96:99], a[236:239], v[40:43]\n" \
  "s_waitcnt lgkmcnt(4)\n" \
  "v_mfma_f32_16x16x32_bf16 v[20:23], v[240:243], v[68:71], v[20:23]\n" \
  "ds_read_b128 v[92:95], v11 offset:61440\n" \
  "v_mfma_f32_16x16x32_bf16 v[28:31], a[112:115], v[68:71], v[28:31]\n" \
  "ds_read_b128 v[96:99], v11 offset:63488\n" \
  "v_mfma_f32_16x16x32_bf16 v[36:39], v[68:71], a[240:243], v[36:39]\n" \
  "v_mfma_f32_16x16x32_bf16 v[24:27], v[240:243], v[72:75], v[24:27]\n" \
  "s_add_u32 m0, s91, 28672\n" \
  "s_nop 0\n" \
  "buffer_load_dwordx4 v7, s[72:75], s87 offen lds\n" \
  "s_add_u32 s87, s87, 128\n" \
  "v_mfma_f32_16x16x32_bf16 v[32:35], a[112:115], v[72:75], v[32:35]\n" \
  "v_mfma_f32_16x16x32_bf16 v[40:43], v[72:75], a[240:243], v[40:43]\n" \
  "s_waitcnt lgkmcnt(4)\n" \
  "v_mfma_f32_16x16x32_bf16 v[20:23], v[244:247], v[76:79], v[20:23]\n" \
  "ds_read_b128 v[68:71], v12 offset:0\n" \
  "v_mfma_f32_16x16x32_bf16 v[28:31], a[116:119], v[76:79], v[28:31]\n" \
  "ds_read_b128 v[72:75], v12 offset:2048\n" \
  "v_mfma_f32_16x16x32_bf16 v[36:39], v[76:79], a[244:247], v[36:39]\n" \
  "v_mfma_f32_16x16x32_bf16 v[24:27], v[244:247], v[80:83], v[24:27]\n" \
  "v_mfma_f32_16x16x32_bf16 v[32:35], a[116:119], v[80:83], v[32:35]\n" \
  "v_mfma_f32_16x16x32_bf16 v[40:43], v[80:83], a[244:247], v[40:43]\n" \
  "s_waitcnt lgkmcnt(4)\n" \
  "v_mfma_f32_16x16x32_bf16 v[20:23], v[248:251], v[84:87], v[20:23]\n" \
  "ds_read_b128 v[76:79], v13 offset:0\n" \
  "v_mfma_f32_16x16x32_bf16 v[28:31], a[120:123], v[84:87], v[28:31]\n" \
  "ds_read_b128 v[80:83], v13 offset:2048\n" \
  "v_mfma_f32_16x16x32_bf16 v[36:39], v[84:87], a[248:251], v[36:39]\n" \
  "v_mfma_f32_16x16x32_bf16 v[24:27], v[248:251], v[88:91], v[24:27]\n" \
  "v_mfma_f32_16x16x32_bf16 v[32:35], a[120:123], v[88:91], v[32:35]\n" \
  "v_mfma_f32_16x16x32_bf16 v[40:43], v[88:91], a[248:251], v[40:43]\n" \
  "s_waitcnt lgkmcnt(4)\n" \
  "v_mfma_f32_16x16x32_bf16 v[20:23], v[252:255], v[92:95], v[20:23]\n" \
  "ds_read_b128 v[84:87], v12 offset:4096\n" \
  "v_mfma_f32_16x16x32_bf16 v[28:31], a[124:127], v[92:95], v[28:31]\n" \
  "ds_read_b128 v[88:91], v12 offset:6144\n" \
  "v_mfma_f32_16x16x32_bf16 v[36:39], v[92:95], a[252:255], v[36:39]\n" \
  "v_mfma_f32_16x16x32_bf16 v[24:27], v[252:255], v[96:99], v[24:27]\n" \
  "v_mfma_f32_16x16x32_bf16 v[32:35], a[124:127], v[96:99], v[32:35]\n" \
  "v_mfma_f32_16x16x32_bf16 v[40:43], v[96:99], a[252:255], v[40:43]\n" \
  "s_cmp_eq_u32 s85, 64\n" \
  "s_cbranch_scc1 LX1_%=\n" \
  "s_waitcnt vmcnt(8)\n" \
  "s_barrier\n" \
  "s_waitcnt lgkmcnt(4)\n" \
  "v_mfma_f32_16x16x32_bf16 v[44:47], v[128:131], v[68:71], 0\n" \
  "ds_read_b128 v[92:95], v13 offset:4096\n" \
  "s_sub_u32 s89, s85, 1\n" \
  "s_and_b32 s89, s89, 63\n" \
  "v_mfma_f32_16x16x32_bf16 v[52:55], a[0:3], v[68:71], 0\n" \
  "ds_read_b128 v[96:99], v13 offset:6144\n" \
  "s_lshr_b32 s84, s89, 2\n" \
  "s_xnor_b32 s84, s84, %9\n" \
  "v_mfma_f32_16x16x32_bf16 v[60:63], v[68:71], a[128:131], 0\n" \
  "s_and_b32 s84, s84, 1\n" \
  "s_lshl_b32 s84, s84, 13\n" \
  "v_mfma_f32_16x16x32_bf16 v[48:51], v[128:131], v[72:75], 0\n" \
  "s_lshl_b32 s88, s89, 19\n" \
  "s_or_b32 s88, s88, s84\n" \
  "v_mfma_f32_16x16x32_bf16 v[56:59], a[0:3], v[72:75], 0\n" \
  "s_add_u32 s89, s88, " STR(QKV_SZ32) "\n" \
  "s_add_u32 s90, s89, " STR(QKV_SZ32) "\n" \
  "v_mfma_f32_16x16x32_bf16 v[64:67], v[72:75], a[128:131], 0\n" \
  "s_add_u32 s85, s85, 1\n" \
  "s_waitcnt lgkmcnt(4)\n" \
  "v_mfma_f32_16x16x32_bf16 v[44:47], v[132:135], v[76:79], v[44:47]\n" \
  "ds_read_b128 v[68:71], v12 offset:8192\n" \
  "v_cvt_pk_bf16_f32 v116, v20, v21\n" \
  "v_mfma_f32_16x16x32_bf16 v[52:55], a[4:7], v[76:79], v[52:55]\n" \
  "ds_read_b128 v[72:75], v12 offset:10240\n" \
  "v_cvt_pk_bf16_f32 v117, v22, v23\n" \
  "v_mfma_f32_16x16x32_bf16 v[60:63], v[76:79], a[132:135], v[60:63]\n" \
  "v_cvt_pk_bf16_f32 v118, v24, v25\n" \
  "v_mfma_f32_16x16x32_bf16 v[48:51], v[132:135], v[80:83], v[48:51]\n" \
  "v_cvt_pk_bf16_f32 v119, v26, v27\n" \
  "v_mfma_f32_16x16x32_bf16 v[56:59], a[4:7], v[80:83], v[56:59]\n" \
  "v_cvt_pk_bf16_f32 v120, v28, v29\n" \
  "v_mfma_f32_16x16x32_bf16 v[64:67], v[80:83], a[132:135], v[64:67]\n" \
  "v_cvt_pk_bf16_f32 v121, v30, v31\n" \
  "s_waitcnt lgkmcnt(4)\n" \
  "v_mfma_f32_16x16x32_bf16 v[44:47], v[136:139], v[84:87], v[44:47]\n" \
  "ds_read_b128 v[76:79], v13 offset:8192\n" \
  "v_cvt_pk_bf16_f32 v122, v32, v33\n" \
  "v_mfma_f32_16x16x32_bf16 v[52:55], a[8:11], v[84:87], v[52:55]\n" \
  "ds_read_b128 v[80:83], v13 offset:10240\n" \
  "v_cvt_pk_bf16_f32 v123, v34, v35\n" \
  "v_mfma_f32_16x16x32_bf16 v[60:63], v[84:87], a[136:139], v[60:63]\n" \
  "v_cvt_pk_bf16_f32 v124, v36, v37\n" \
  "v_mfma_f32_16x16x32_bf16 v[48:51], v[136:139], v[88:91], v[48:51]\n" \
  "v_cvt_pk_bf16_f32 v125, v38, v39\n" \
  "v_mfma_f32_16x16x32_bf16 v[56:59], a[8:11], v[88:91], v[56:59]\n" \
  "v_cvt_pk_bf16_f32 v126, v40, v41\n" \
  "v_mfma_f32_16x16x32_bf16 v[64:67], v[88:91], a[136:139], v[64:67]\n" \
  "v_cvt_pk_bf16_f32 v127, v42, v43\n" \
  "s_waitcnt lgkmcnt(4)\n" \
  "v_mfma_f32_16x16x32_bf16 v[44:47], v[140:143], v[92:95], v[44:47]\n" \
  "ds_read_b128 v[84:87], v12 offset:12288\n" \
  "v_permlane16_swap_b32 v116, v118\n" \
  "v_mfma_f32_16x16x32_bf16 v[52:55], a[12:15], v[92:95], v[52:55]\n" \
  "ds_read_b128 v[88:91], v12 offset:14336\n" \
  "v_permlane16_swap_b32 v117, v119\n" \
  "v_mfma_f32_16x16x32_bf16 v[60:63], v[92:95], a[140:143], v[60:63]\n" \
  "v_permlane16_swap_b32 v120, v122\n" \
  "v_mfma_f32_16x16x32_bf16 v[48:51], v[140:143], v[96:99], v[48:51]\n" \
  "v_permlane16_swap_b32 v121, v123\n" \
  "v_mfma_f32_16x16x32_bf16 v[56:59], a[12:15], v[96:99], v[56:59]\n" \
  "v_mfma_f32_16x16x32_bf16 v[64:67], v[96:99], a[140:143], v[64:67]\n" \
  "s_waitcnt lgkmcnt(4)\n" \
  "v_mfma_f32_16x16x32_bf16 v[44:47], v[144:147], v[68:71], v[44:47]\n" \
  "ds_read_b128 v[92:95], v13 offset:12288\n" \
  "buffer_store_dwordx4 v[116:119], v14, s[76:79], s88 offen sc1\n" \
  "v_mfma_f32_16x16x32_bf16 v[52:55], a[16:19], v[68:71], v[52:55]\n" \
  "ds_read_b128 v[96:99], v13 offset:14336\n" \
  "buffer_store_dwordx4 v[120:123], v18, s[76:79], s89 offen sc1\n" \
  "v_mfma_f32_16x16x32_bf16 v[60:63], v[68:71], a[144:147], v[60:63]\n" \
  "buffer_store_dwordx4 v[124:127], v15, s[76:79], s90 offen sc1\n" \
  "v_mfma_f32_16x16x32_bf16 v[48:51], v[144:147], v[72:75], v[48:51]\n" \
  "v_mfma_f32_16x16x32_bf16 v[56:59], a[16:19], v[72:75], v[56:59]\n" \
  "v_mfma_f32_16x16x32_bf16 v[64:67], v[72:75], a[144:147], v[64:67]\n" \
  "s_waitcnt lgkmcnt(4)\n" \
  "v_mfma_f32_16x16x32_bf16 v[44:47], v[148:151], v[76:79], v[44:47]\n" \
  "ds_read_b128 v[68:71], v12 offset:16384\n" \
  "v_mfma_f32_16x16x32_bf16 v[52:55], a[20:23], v[76:79], v[52:55]\n" \
  "ds_read_b128 v[72:75], v12 offset:18432\n" \
  "v_mfma_f32_16x16x32_bf16 v[60:63], v[76:79], a[148:151], v[60:63]\n" \
  "v_mfma_f32_16x16x32_bf16 v[48:51], v[148:151], v[80:83], v[48:51]\n" \
  "s_add_u32 m0, s91, 32768\n" \
  "s_nop 0\n" \
  "buffer_load_dwordx4 v7, s[72:75], s87 offen lds\n" \
  "s_add_u32 s87, s87, 128\n" \
  "v_mfma_f32_16x16x32_bf16 v[56:59], a[20:23], v[80:83], v[56:59]\n" \
  "v_mfma_f32_16x16x32_bf16 v[64:67], v[80:83], a[148:151], v[64:67]\n" \
  "s_waitcnt lgkmcnt(4)\n" \
  "v_mfma_f32_16x16x32_bf16 v[44:47], v[152:155], v[84:87], v[44:47]\n" \
  "ds_read_b128 v[76:79], v13 offset:16384\n" \
  "v_mfma_f32_16x16x32_bf16 v[52:55], a[24:27], v[84:87], v[52:55]\n" \
  "ds_read_b128 v[80:83], v13 offset:18432\n" \
  "v_mfma_f32_16x16x32_bf16 v[60:63], v[84:87], a[152:155], v[60:63]\n" \
  "v_mfma_f32_16x16x32_bf16 v[48:51], v[152:155], v[88:91], v[48:51]\n" \
  "s_add_u32 m0, s91, 36864\n" \
  "s_nop 0\n" \
  "buffer_load_dwordx4 v7, s[72:75], s87 offen lds\n" \
  "s_add_u32 s87, s87, 128\n" \
  "v_mfma_f32_16x16x32_bf16 v[56:59], a[24:27], v[88:91], v[56:59]\n" \
  "v_mfma_f32_16x16x32_bf16 v[64:67], v[88:91], a[152:155], v[64:67]\n" \
  "s_waitcnt lgkmcnt(4)\n" \
  "v_mfma_f32_16x16x32_bf16 v[44:47], v[156:159], v[92:95], v[44:47]\n" \
  "ds_read_b128 v[84:87], v12 offset:20480\n" \
  "v_mfma_f32_16x16x32_bf16 v[52:55], a[28:31], v[92:95], v[52:55]\n" \
  "ds_read_b128 v[88:91], v12 offset:22528\n" \
  "v_mfma_f32_16x16x32_bf16 v[60:63], v[92:95], a[156:159], v[60:63]\n" \
  "v_mfma_f32_16x16x32_bf16 v[48:51], v[156:159], v[96:99], v[48:51]\n" \
  "s_add_u32 m0, s91, 40960\n" \
  "s_nop 0\n" \
  "buffer_load_dwordx4 v7, s[72:75], s87 offen lds\n" \
  "s_add_u32 s87, s87, 128\n" \
  "v_mfma_f32_16x16x32_bf16 v[56:59], a[28:31], v[96:99], v[56:59]\n" \
  "v_mfma_f32_16x16x32_bf16 v[64:67], v[96:99], a[156:159], v[64:67]\n" \
  "s_waitcnt lgkmcnt(4)\n" \
  "v_mfma_f32_16x16x32_bf16 v[44:47], v[160:163], v[68:71], v[44:47]\n" \
  "ds_read_b128 v[92:95], v13 offset:20480\n" \
  "v_mfma_f32_16x16x32_bf16 v[52:55], a[32:35], v[68:71], v[52:55]\n" \
  "ds_read_b128 v[96:99], v13 offset:22528\n" \
  "v_mfma_f32_16x16x32_bf16 v[60:63], v[68:71], a[160:163], v[60:63]\n" \
  "v_mfma_f32_16x16x32_bf16 v[48:51], v[160:163], v[72:75], v[48:51]\n" \
  "s_add_u32 m0, s91, 45056\n" \
  "s_nop 0\n" \
  "buffer_load_dwordx4 v7, s[72:75], s87 offen lds\n" \
  "s_add_u32 s87, s87, 128\n" \
  "v_mfma_f32_16x16x32_bf16 v[56:59], a[32:35], v[72:75], v[56:59]\n" \
  "v_mfma_f32_16x16x32_bf16 v[64:67], v[72:75], a[160:163], v[64:67]\n" \
  "s_waitcnt lgkmcnt(4)\n" \
  "v_mfma_f32_16x16x32_bf16 v[44:47], v[164:167], v[76:79], v[44:47]\n" \
  "ds_read_b128 v[68:71], v12 offset:24576\n" \
  "v_mfma_f32_16x16x32_bf16 v[52:55], a[36:39], v[76:79], v[52:55]\n" \
  "ds_read_b128 v[72:75], v12 offset:26624\n" \
  "v_mfma_f32_16x16x32_bf16 v[60:63], v[76:79], a[164:167], v[60:63]\n" \
  "v_mfma_f32_16x16x32_bf16 v[48:51], v[164:167], v[80:83], v[48:51]\n" \
  "s_add_u32 m0, s91, 49152\n" \
  "s_nop 0\n" \
  "buffer_load_dwordx4 v7, s[72:75], s87 offen lds\n" \
  "s_add_u32 s87, s87, 128\n" \
  "v_mfma_f32_16x16x32_bf16 v[56:59], a[36:39], v[80:83], v[56:59]\n" \
  "v_mfma_f32_16x16x32_bf16 v[64:67], v[80:83], a[164:167], v[64:67]\n" \
  "s_waitcnt lgkmcnt(4)\n" \
  "v_mfma_f32_16x16x32_bf16 v[44:47], v[168:171], v[84:87], v[44:47]\n" \
  "ds_read_b128 v[76:79], v13 offset:24576\n" \
  "v_mfma_f32_16x16x32_bf16 v[52:55], a[40:43], v[84:87], v[52:55]\n" \
  "ds_read_b128 v[80:83], v13 offset:26624\n" \
  "v_mfma_f32_16x16x32_bf16 v[60:63], v[84:87], a[168:171], v[60:63]\n" \
  "v_mfma_f32_16x16x32_bf16 v[48:51], v[168:171], v[88:91], v[48:51]\n" \
  "s_add_u32 m0, s91, 53248\n" \
  "s_nop 0\n" \
  "buffer_load_dwordx4 v7, s[72:75], s87 offen lds\n" \
  "s_add_u32 s87, s87, 128\n" \
  "v_mfma_f32_16x16x32_bf16 v[56:59], a[40:43], v[88:91], v[56:59]\n" \
  "v_mfma_f32_16x16x32_bf16 v[64:67], v[88:91], a[168:171], v[64:67]\n" \
  "s_waitcnt lgkmcnt(4)\n" \
  "v_mfma_f32_16x16x32_bf16 v[44:47], v[172:175], v[92:95], v[44:47]\n" \
  "ds_read_b128 v[84:87], v12 offset:28672\n" \
  "v_mfma_f32_16x16x32_bf16 v[52:55], a[44:47], v[92:95], v[52:55]\n" \
  "ds_read_b128 v[88:91], v12 offset:30720\n" \
  "v_mfma_f32_16x16x32_bf16 v[60:63], v[92:95], a[172:175], v[60:63]\n" \
  "v_mfma_f32_16x16x32_bf16 v[48:51], v[172:175], v[96:99], v[48:51]\n" \
  "s_add_u32 m0, s91, 57344\n" \
  "s_nop 0\n" \
  "buffer_load_dwordx4 v7, s[72:75], s87 offen lds\n" \
  "s_add_u32 s87, s87, 128\n" \
  "v_mfma_f32_16x16x32_bf16 v[56:59], a[44:47], v[96:99], v[56:59]\n" \
  "v_mfma_f32_16x16x32_bf16 v[64:67], v[96:99], a[172:175], v[64:67]\n" \
  "s_waitcnt lgkmcnt(4)\n" \
  "v_mfma_f32_16x16x32_bf16 v[44:47], v[176:179], v[68:71], v[44:47]\n" \
  "ds_read_b128 v[92:95], v13 offset:28672\n" \
  "v_mfma_f32_16x16x32_bf16 v[52:55], a[48:51], v[68:71], v[52:55]\n" \
  "ds_read_b128 v[96:99], v13 offset:30720\n" \
  "v_mfma_f32_16x16x32_bf16 v[60:63], v[68:71], a[176:179], v[60:63]\n" \
  "v_mfma_f32_16x16x32_bf16 v[48:51], v[176:179], v[72:75], v[48:51]\n" \
  "s_add_u32 m0, s91, 61440\n" \
  "s_nop 0\n" \
  "buffer_load_dwordx4 v7, s[72:75], s87 offen lds\n" \
  "s_add_u32 s87, s87, 63616\n" \
  "s_min_u32 s87, s87, s96\n" \
  "v_mfma_f32_16x16x32_bf16 v[56:59], a[48:51], v[72:75], v[56:59]\n" \
  "v_mfma_f32_16x16x32_bf16 v[64:67], v[72:75], a[176:179], v[64:67]\n" \
  "s_waitcnt lgkmcnt(4)\n" \
  "v_mfma_f32_16x16x32_bf16 v[44:47], v[180:183], v[76:79], v[44:47]\n" \
  "ds_read_b128 v[68:71], v12 offset:32768\n" \
  "v_mfma_f32_16x16x32_bf16 v[52:55], a[52:55], v[76:79], v[52:55]\n" \
  "ds_read_b128 v[72:75], v12 offset:34816\n" \
  "v_mfma_f32_16x16x32_bf16 v[60:63], v[76:79], a[180:183], v[60:63]\n" \
  "v_mfma_f32_16x16x32_bf16 v[48:51], v[180:183], v[80:83], v[48:51]\n" \
  "v_mfma_f32_16x16x32_bf16 v[56:59], a[52:55], v[80:83], v[56:59]\n" \
  "v_mfma_f32_16x16x32_bf16 v[64:67], v[80:83], a[180:183], v[64:67]\n" \
  "s_waitcnt lgkmcnt(4)\n" \
  "v_mfma_f32_16x16x32_bf16 v[44:47], v[184:187], v[84:87], v[44:47]\n" \
  "ds_read_b128 v[76:79], v13 offset:32768\n" \
  "v_mfma_f32_16x16x32_bf16 v[52:55], a[56:59], v[84:87], v[52:55]\n" \
  "ds_read_b128 v[80:83], v13 offset:34816\n" \
  "v_mfma_f32_16x16x32_bf16 v[60:63], v[84:87], a[184:187], v[60:63]\n" \
  "v_mfma_f32_16x16x32_bf16 v[48:51], v[184:187], v[88:91], v[48:51]\n" \
  "v_mfma_f32_16x16x32_bf16 v[56:59], a[56:59], v[88:91], v[56:59]\n" \
  "v_mfma_f32_16x16x32_bf16 v[64:67], v[88:91], a[184:187], v[64:67]\n" \
  "s_waitcnt lgkmcnt(4)\n" \
  "v_mfma_f32_16x16x32_bf16 v[44:47], v[188:191], v[92:95], v[44:47]\n" \
  "ds_read_b128 v[84:87], v12 offset:36864\n" \
  "v_mfma_f32_16x16x32_bf16 v[52:55], a[60:63], v[92:95], v[52:55]\n" \
  "ds_read_b128 v[88:91], v12 offset:38912\n" \
  "v_mfma_f32_16x16x32_bf16 v[60:63], v[92:95], a[188:191], v[60:63]\n" \
  "v_mfma_f32_16x16x32_bf16 v[48:51], v[188:191], v[96:99], v[48:51]\n" \
  "v_mfma_f32_16x16x32_bf16 v[56:59], a[60:63], v[96:99], v[56:59]\n" \
  "v_mfma_f32_16x16x32_bf16 v[64:67], v[96:99], a[188:191], v[64:67]\n" \
  "s_waitcnt vmcnt(8)\n" \
  "s_barrier\n" \
  "s_waitcnt lgkmcnt(4)\n" \
  "v_mfma_f32_16x16x32_bf16 v[44:47], v[192:195], v[68:71], v[44:47]\n" \
  "ds_read_b128 v[92:95], v13 offset:36864\n" \
  "buffer_store_dword v17, v16, s[80:83], 0 offen sc1\n" \
  "v_mfma_f32_16x16x32_bf16 v[52:55], a[64:67], v[68:71], v[52:55]\n" \
  "ds_read_b128 v[96:99], v13 offset:38912\n" \
  "v_add_u32 v17, 1, v17\n" \
  "v_mfma_f32_16x16x32_bf16 v[60:63], v[68:71], a[192:195], v[60:63]\n" \
  "v_and_b32 v14, 0x7fffffff, v14\n" \
  "v_mfma_f32_16x16x32_bf16 v[48:51], v[192:195], v[72:75], v[48:51]\n" \
  "v_and_b32 v15, 0x7fffffff, v15\n" \
  "v_mfma_f32_16x16x32_bf16 v[56:59], a[64:67], v[72:75], v[56:59]\n" \
  "v_and_b32 v18, 0x7fffffff, v18\n" \
  "v_mfma_f32_16x16x32_bf16 v[64:67], v[72:75], a[192:195], v[64:67]\n" \
  "s_waitcnt lgkmcnt(4)\n" \
  "v_mfma_f32_16x16x32_bf16 v[44:47], v[196:199], v[76:79], v[44:47]\n" \
  "ds_read_b128 v[68:71], v12 offset:40960\n" \
  "v_mfma_f32_16x16x32_bf16 v[52:55], a[68:71], v[76:79], v[52:55]\n" \
  "ds_read_b128 v[72:75], v12 offset:43008\n" \
  "v_mfma_f32_16x16x32_bf16 v[60:63], v[76:79], a[196:199], v[60:63]\n" \
  "v_mfma_f32_16x16x32_bf16 v[48:51], v[196:199], v[80:83], v[48:51]\n" \
  "v_mfma_f32_16x16x32_bf16 v[56:59], a[68:71], v[80:83], v[56:59]\n" \
  "v_mfma_f32_16x16x32_bf16 v[64:67], v[80:83], a[196:199], v[64:67]\n" \
  "s_waitcnt lgkmcnt(4)\n" \
  "v_mfma_f32_16x16x32_bf16 v[44:47], v[200:203], v[84:87], v[44:47]\n" \
  "ds_read_b128 v[76:79], v13 offset:40960\n" \
  "v_mfma_f32_16x16x32_bf16 v[52:55], a[72:75], v[84:87], v[52:55]\n" \
  "ds_read_b128 v[80:83], v13 offset:43008\n" \
  "v_mfma_f32_16x16x32_bf16 v[60:63], v[84:87], a[200:203], v[60:63]\n" \
  "v_mfma_f32_16x16x32_bf16 v[48:51], v[200:203], v[88:91], v[48:51]\n" \
  "v_mfma_f32_16x16x32_bf16 v[56:59], a[72:75], v[88:91], v[56:59]\n" \
  "v_mfma_f32_16x16x32_bf16 v[64:67], v[88:91], a[200:203], v[64:67]\n" \
  "s_waitcnt lgkmcnt(4)\n" \
  "v_mfma_f32_16x16x32_bf16 v[44:47], v[204:207], v[92:95], v[44:47]\n" \
  "ds_read_b128 v[84:87], v12 offset:45056\n" \
  "v_mfma_f32_16x16x32_bf16 v[52:55], a[76:79], v[92:95], v[52:55]\n" \
  "ds_read_b128 v[88:91], v12 offset:47104\n" \
  "v_mfma_f32_16x16x32_bf16 v[60:63], v[92:95], a[204:207], v[60:63]\n" \
  "v_mfma_f32_16x16x32_bf16 v[48:51], v[204:207], v[96:99], v[48:51]\n" \
  "v_mfma_f32_16x16x32_bf16 v[56:59], a[76:79], v[96:99], v[56:59]\n" \
  "v_mfma_f32_16x16x32_bf16 v[64:67], v[96:99], a[204:207], v[64:67]\n" \
  "s_waitcnt lgkmcnt(4)\n" \
  "v_mfma_f32_16x16x32_bf16 v[44:47], v[208:211], v[68:71], v[44:47]\n" \
  "ds_read_b128 v[92:95], v13 offset:45056\n" \
  "v_mfma_f32_16x16x32_bf16 v[52:55], a[80:83], v[68:71], v[52:55]\n" \
  "ds_read_b128 v[96:99], v13 offset:47104\n" \
  "v_mfma_f32_16x16x32_bf16 v[60:63], v[68:71], a[208:211], v[60:63]\n" \
  "v_mfma_f32_16x16x32_bf16 v[48:51], v[208:211], v[72:75], v[48:51]\n" \
  "v_mfma_f32_16x16x32_bf16 v[56:59], a[80:83], v[72:75], v[56:59]\n" \
  "v_mfma_f32_16x16x32_bf16 v[64:67], v[72:75], a[208:211], v[64:67]\n" \
  "s_waitcnt lgkmcnt(4)\n" \
  "v_mfma_f32_16x16x32_bf16 v[44:47], v[212:215], v[76:79], v[44:47]\n" \
  "ds_read_b128 v[68:71], v12 offset:49152\n" \
  "v_mfma_f32_16x16x32_bf16 v[52:55], a[84:87], v[76:79], v[52:55]\n" \
  "ds_read_b128 v[72:75], v12 offset:51200\n" \
  "v_mfma_f32_16x16x32_bf16 v[60:63], v[76:79], a[212:215], v[60:63]\n" \
  "v_mfma_f32_16x16x32_bf16 v[48:51], v[212:215], v[80:83], v[48:51]\n" \
  "s_add_u32 m0, s91, 65536\n" \
  "s_nop 0\n" \
  "buffer_load_dwordx4 v7, s[72:75], s87 offen lds\n" \
  "s_add_u32 s87, s87, 128\n" \
  "v_mfma_f32_16x16x32_bf16 v[56:59], a[84:87], v[80:83], v[56:59]\n" \
  "v_mfma_f32_16x16x32_bf16 v[64:67], v[80:83], a[212:215], v[64:67]\n" \
  "s_waitcnt lgkmcnt(4)\n" \
  "v_mfma_f32_16x16x32_bf16 v[44:47], v[216:219], v[84:87], v[44:47]\n" \
  "ds_read_b128 v[76:79], v13 offset:49152\n" \
  "v_mfma_f32_16x16x32_bf16 v[52:55], a[88:91], v[84:87], v[52:55]\n" \
  "ds_read_b128 v[80:83], v13 offset:51200\n" \
  "v_mfma_f32_16x16x32_bf16 v[60:63], v[84:87], a[216:219], v[60:63]\n" \
  "v_mfma_f32_16x16x32_bf16 v[48:51], v[216:219], v[88:91], v[48:51]\n" \
  "s_add_u32 m0, s91, 69632\n" \
  "s_nop 0\n" \
  "buffer_load_dwordx4 v7, s[72:75], s87 offen lds\n" \
  "s_add_u32 s87, s87, 128\n" \
  "v_mfma_f32_16x16x32_bf16 v[56:59], a[88:91], v[88:91], v[56:59]\n" \
  "v_mfma_f32_16x16x32_bf16 v[64:67], v[88:91], a[216:219], v[64:67]\n" \
  "s_waitcnt lgkmcnt(4)\n" \
  "v_mfma_f32_16x16x32_bf16 v[44:47], v[220:223], v[92:95], v[44:47]\n" \
  "ds_read_b128 v[84:87], v12 offset:53248\n" \
  "v_mfma_f32_16x16x32_bf16 v[52:55], a[92:95], v[92:95], v[52:55]\n" \
  "ds_read_b128 v[88:91], v12 offset:55296\n" \
  "v_mfma_f32_16x16x32_bf16 v[60:63], v[92:95], a[220:223], v[60:63]\n" \
  "v_mfma_f32_16x16x32_bf16 v[48:51], v[220:223], v[96:99], v[48:51]\n" \
  "s_add_u32 m0, s91, 73728\n" \
  "s_nop 0\n" \
  "buffer_load_dwordx4 v7, s[72:75], s87 offen lds\n" \
  "s_add_u32 s87, s87, 128\n" \
  "v_mfma_f32_16x16x32_bf16 v[56:59], a[92:95], v[96:99], v[56:59]\n" \
  "v_mfma_f32_16x16x32_bf16 v[64:67], v[96:99], a[220:223], v[64:67]\n" \
  "s_waitcnt lgkmcnt(4)\n" \
  "v_mfma_f32_16x16x32_bf16 v[44:47], v[224:227], v[68:71], v[44:47]\n" \
  "ds_read_b128 v[92:95], v13 offset:53248\n" \
  "v_mfma_f32_16x16x32_bf16 v[52:55], a[96:99], v[68:71], v[52:55]\n" \
  "ds_read_b128 v[96:99], v13 offset:55296\n" \
  "v_mfma_f32_16x16x32_bf16 v[60:63], v[68:71], a[224:227], v[60:63]\n" \
  "v_mfma_f32_16x16x32_bf16 v[48:51], v[224:227], v[72:75], v[48:51]\n" \
  "s_add_u32 m0, s91, 77824\n" \
  "s_nop 0\n" \
  "buffer_load_dwordx4 v7, s[72:75], s87 offen lds\n" \
  "s_add_u32 s87, s87, 128\n" \
  "v_mfma_f32_16x16x32_bf16 v[56:59], a[96:99], v[72:75], v[56:59]\n" \
  "v_mfma_f32_16x16x32_bf16 v[64:67], v[72:75], a[224:227], v[64:67]\n" \
  "s_waitcnt lgkmcnt(4)\n" \
  "v_mfma_f32_16x16x32_bf16 v[44:47], v[228:231], v[76:79], v[44:47]\n" \
  "ds_read_b128 v[68:71], v12 offset:57344\n" \
  "v_mfma_f32_16x16x32_bf16 v[52:55], a[100:103], v[76:79], v[52:55]\n" \
  "ds_read_b128 v[72:75], v12 offset:59392\n" \
  "v_mfma_f32_16x16x32_bf16 v[60:63], v[76:79], a[228:231], v[60:63]\n" \
  "v_mfma_f32_16x16x32_bf16 v[48:51], v[228:231], v[80:83], v[48:51]\n" \
  "s_add_u32 m0, s91, 81920\n" \
  "s_nop 0\n" \
  "buffer_load_dwordx4 v7, s[72:75], s87 offen lds\n" \
  "s_add_u32 s87, s87, 128\n" \
  "v_mfma_f32_16x16x32_bf16 v[56:59], a[100:103], v[80:83], v[56:59]\n" \
  "v_mfma_f32_16x16x32_bf16 v[64:67], v[80:83], a[228:231], v[64:67]\n" \
  "s_waitcnt lgkmcnt(4)\n" \
  "v_mfma_f32_16x16x32_bf16 v[44:47], v[232:235], v[84:87], v[44:47]\n" \
  "ds_read_b128 v[76:79], v13 offset:57344\n" \
  "v_mfma_f32_16x16x32_bf16 v[52:55], a[104:107], v[84:87], v[52:55]\n" \
  "ds_read_b128 v[80:83], v13 offset:59392\n" \
  "v_mfma_f32_16x16x32_bf16 v[60:63], v[84:87], a[232:235], v[60:63]\n" \
  "v_mfma_f32_16x16x32_bf16 v[48:51], v[232:235], v[88:91], v[48:51]\n" \
  "s_add_u32 m0, s91, 86016\n" \
  "s_nop 0\n" \
  "buffer_load_dwordx4 v7, s[72:75], s87 offen lds\n" \
  "s_add_u32 s87, s87, 128\n" \
  "v_mfma_f32_16x16x32_bf16 v[56:59], a[104:107], v[88:91], v[56:59]\n" \
  "v_mfma_f32_16x16x32_bf16 v[64:67], v[88:91], a[232:235], v[64:67]\n" \
  "s_waitcnt lgkmcnt(4)\n" \
  "v_mfma_f32_16x16x32_bf16 v[44:47], v[236:239], v[92:95], v[44:47]\n" \
  "ds_read_b128 v[84:87], v12 offset:61440\n" \
  "v_mfma_f32_16x16x32_bf16 v[52:55], a[108:111], v[92:95], v[52:55]\n" \
  "ds_read_b128 v[88:91], v12 offset:63488\n" \
  "v_mfma_f32_16x16x32_bf16 v[60:63], v[92:95], a[236:239], v[60:63]\n" \
  "v_mfma_f32_16x16x32_bf16 v[48:51], v[236:239], v[96:99], v[48:51]\n" \
  "s_add_u32 m0, s91, 90112\n" \
  "s_nop 0\n" \
  "buffer_load_dwordx4 v7, s[72:75], s87 offen lds\n" \
  "s_add_u32 s87, s87, 128\n" \
  "v_mfma_f32_16x16x32_bf16 v[56:59], a[108:111], v[96:99], v[56:59]\n" \
  "v_mfma_f32_16x16x32_bf16 v[64:67], v[96:99], a[236:239], v[64:67]\n" \
  "s_waitcnt lgkmcnt(4)\n" \
  "v_mfma_f32_16x16x32_bf16 v[44:47], v[240:243], v[68:71], v[44:47]\n" \
  "ds_read_b128 v[92:95], v13 offset:61440\n" \
  "v_mfma_f32_16x16x32_bf16 v[52:55], a[112:115], v[68:71], v[52:55]\n" \
  "ds_read_b128 v[96:99], v13 offset:63488\n" \
  "v_mfma_f32_16x16x32_bf16 v[60:63], v[68:71], a[240:243], v[60:63]\n" \
  "v_mfma_f32_16x16x32_bf16 v[48:51], v[240:243], v[72:75], v[48:51]\n" \
  "s_add_u32 m0, s91, 94208\n" \
  "s_nop 0\n" \
  "buffer_load_dwordx4 v7, s[72:75], s87 offen lds\n" \
  "s_add_u32 s87, s87, 128\n" \
  "v_mfma_f32_16x16x32_bf16 v[56:59], a[112:115], v[72:75], v[56:59]\n" \
  "v_mfma_f32_16x16x32_bf16 v[64:67], v[72:75], a[240:243], v[64:67]\n" \
  "s_waitcnt lgkmcnt(4)\n" \
  "v_mfma_f32_16x16x32_bf16 v[44:47], v[244:247], v[76:79], v[44:47]\n" \
  "ds_read_b128 v[68:71], v10 offset:0\n" \
  "v_mfma_f32_16x16x32_bf16 v[52:55], a[116:119], v[76:79], v[52:55]\n" \
  "ds_read_b128 v[72:75], v10 offset:2048\n" \
  "v_mfma_f32_16x16x32_bf16 v[60:63], v[76:79], a[244:247], v[60:63]\n" \
  "v_mfma_f32_16x16x32_bf16 v[48:51], v[244:247], v[80:83], v[48:51]\n" \
  "v_mfma_f32_16x16x32_bf16 v[56:59], a[116:119], v[80:83], v[56:59]\n" \
  "v_mfma_f32_16x16x32_bf16 v[64:67], v[80:83], a[244:247], v[64:67]\n" \
  "s_waitcnt lgkmcnt(4)\n" \
  "v_mfma_f32_16x16x32_bf16 v[44:47], v[248:251], v[84:87], v[44:47]\n" \
  "ds_read_b128 v[76:79], v11 offset:0\n" \
  "v_mfma_f32_16x16x32_bf16 v[52:55], a[120:123], v[84:87], v[52:55]\n" \
  "ds_read_b128 v[80:83], v11 offset:2048\n" \
  "v_mfma_f32_16x16x32_bf16 v[60:63], v[84:87], a[248:251], v[60:63]\n" \
  "v_mfma_f32_16x16x32_bf16 v[48:51], v[248:251], v[88:91], v[48:51]\n" \
  "v_mfma_f32_16x16x32_bf16 v[56:59], a[120:123], v[88:91], v[56:59]\n" \
  "v_mfma_f32_16x16x32_bf16 v[64:67], v[88:91], a[248:251], v[64:67]\n" \
  "s_waitcnt lgkmcnt(4)\n" \
  "v_mfma_f32_16x16x32_bf16 v[44:47], v[252:255], v[92:95], v[44:47]\n" \
  "ds_read_b128 v[84:87], v10 offset:4096\n" \
  "v_mfma_f32_16x16x32_bf16 v[52:55], a[124:127], v[92:95], v[52:55]\n" \
  "ds_read_b128 v[88:91], v10 offset:6144\n" \
  "v_mfma_f32_16x16x32_bf16 v[60:63], v[92:95], a[252:255], v[60:63]\n" \
  "v_mfma_f32_16x16x32_bf16 v[48:51], v[252:255], v[96:99], v[48:51]\n" \
  "v_mfma_f32_16x16x32_bf16 v[56:59], a[124:127], v[96:99], v[56:59]\n" \
  "v_mfma_f32_16x16x32_bf16 v[64:67], v[96:99], a[252:255], v[64:67]\n" \
  "s_branch LG_%=\n" \
  "LX0_%=:\n" \
  "s_sub_u32 s89, s85, 1\n" \
  "s_and_b32 s89, s89, 63\n" \
  "s_lshr_b32 s84, s89, 2\n" \
  "s_xnor_b32 s84, s84, %9\n" \
  "s_and_b32 s84, s84, 1\n" \
  "s_lshl_b32 s84, s84, 13\n" \
  "s_lshl_b32 s88, s89, 19\n" \
  "s_or_b32 s88, s88, s84\n" \
  "s_add_u32 s89, s88, " STR(QKV_SZ32) "\n" \
  "s_add_u32 s90, s89, " STR(QKV_SZ32) "\n" \
  "s_add_u32 s85, s85, 1\n" \
  "v_cvt_pk_bf16_f32 v116, v44, v45\n" \
  "v_cvt_pk_bf16_f32 v117, v46, v47\n" \
  "v_cvt_pk_bf16_f32 v118, v48, v49\n" \
  "v_cvt_pk_bf16_f32 v119, v50, v51\n" \
  "v_cvt_pk_bf16_f32 v120, v52, v53\n" \
  "v_cvt_pk_bf16_f32 v121, v54, v55\n" \
  "v_cvt_pk_bf16_f32 v122, v56, v57\n" \
  "v_cvt_pk_bf16_f32 v123, v58, v59\n" \
  "v_cvt_pk_bf16_f32 v124, v60, v61\n" \
  "v_cvt_pk_bf16_f32 v125, v62, v63\n" \
  "v_cvt_pk_bf16_f32 v126, v64, v65\n" \
  "v_cvt_pk_bf16_f32 v127, v66, v67\n" \
  "s_nop 1\n" \
  "v_permlane16_swap_b32 v116, v118\n" \
  "v_permlane16_swap_b32 v117, v119\n" \
  "v_permlane16_swap_b32 v120, v122\n" \
  "v_permlane16_swap_b32 v121, v123\n" \
  "buffer_store_dwordx4 v[116:119], v14, s[76:79], s88 offen sc1\n" \
  "buffer_store_dwordx4 v[120:123], v18, s[76:79], s89 offen sc1\n" \
  "buffer_store_dwordx4 v[124:127], v15, s[76:79], s90 offen sc1\n" \
  "s_branch LZ_%=\n" \
  "LX1_%=:\n" \
  "s_sub_u32 s89, s85, 1\n" \
  "s_and_b32 s89, s89, 63\n" \
  "s_lshr_b32 s84, s89, 2\n" \
  "s_xnor_b32 s84, s84, %9\n" \
  "s_and_b32 s84, s84, 1\n" \
  "s_lshl_b32 s84, s84, 13\n" \
  "s_lshl_b32 s88, s89, 19\n" \
  "s_or_b32 s88, s88, s84\n" \
  "s_add_u32 s89, s88, " STR(QKV_SZ32) "\n" \
  "s_add_u32 s90, s89, " STR(QKV_SZ32) "\n" \
  "s_add_u32 s85, s85, 1\n" \
  "v_cvt_pk_bf16_f32 v116, v20, v21\n" \
  "v_cvt_pk_bf16_f32 v117, v22, v23\n" \
  "v_cvt_pk_bf16_f32 v118, v24, v25\n" \
  "v_cvt_pk_bf16_f32 v119, v26, v27\n" \
  "v_cvt_pk_bf16_f32 v120, v28, v29\n" \
  "v_cvt_pk_bf16_f32 v121, v30, v31\n" \
  "v_cvt_pk_bf16_f32 v122, v32, v33\n" \
  "v_cvt_pk_bf16_f32 v123, v34, v35\n" \
  "v_cvt_pk_bf16_f32 v124, v36, v37\n" \
  "v_cvt_pk_bf16_f32 v125, v38, v39\n" \
  "v_cvt_pk_bf16_f32 v126, v40, v41\n" \
  "v_cvt_pk_bf16_f32 v127, v42, v43\n" \
  "s_nop 1\n" \
  "v_permlane16_swap_b32 v116, v118\n" \
  "v_permlane16_swap_b32 v117, v119\n" \
  "v_permlane16_swap_b32 v120, v122\n" \
  "v_permlane16_swap_b32 v121, v123\n" \
  "buffer_store_dwordx4 v[116:119], v14, s[76:79], s88 offen sc1\n" \
  "buffer_store_dwordx4 v[120:123], v18, s[76:79], s89 offen sc1\n" \
  "buffer_store_dwordx4 v[124:127], v15, s[76:79], s90 offen sc1\n" \
  "s_branch LZ_%=\n" \
  "LZ_%=:\n" \
  "s_waitcnt vmcnt(0) lgkmcnt(0)\n" \
  "s_barrier\n" \
  "buffer_store_dword v17, v16, s[80:83], 0 offen sc1\n" \
  "s_waitcnt vmcnt(0)\n" \
  ""

#define XO_ASM \
  "s_mov_b64 s[68:69], %0\n" \
  "s_and_b32 s69, s69, 0xffff\n" \
  "s_mov_b32 s70, 8388608\n" \
  "s_mov_b32 s71, 0x00020000\n" \
  "s_mov_b64 s[72:73], %1\n" \
  "s_and_b32 s73, s73, 0xffff\n" \
  "s_mov_b32 s74, " STR(QKV_SZ32) "\n" \
  "s_mov_b32 s75, 0x00020000\n" \
  "s_mov_b64 s[76:77], %2\n" \
  "s_and_b32 s77, s77, 0xffff\n" \
  "s_mov_b32 s78, 8388608\n" \
  "s_mov_b32 s79, 0x00020000\n" \
  "s_mov_b64 s[80:81], %3\n" \
  "s_and_b32 s81, s81, 0xffff\n" \
  "s_mov_b32 s82, " STR(CTRL_BYTES) "\n" \
  "s_mov_b32 s83, 0x00020000\n" \
  "v_mbcnt_lo_u32_b32 v4, -1, 0\n" \
  "v_mbcnt_hi_u32_b32 v4, -1, v4\n" \
  "v_and_b32 v20, 15, v4\n" \
  "v_lshrrev_b32 v21, 4, v4\n" \
  "v_lshlrev_b32 v6, 4, v4\n" \
  "s_lshl_b32 s86, %5, 15\n" \
  "s_add_u32 s86, s86, %6\n" \
  "s_lshl_b32 s90, %5, 21\n" \
  "v_lshrrev_b32 v5, 3, v4\n" \
  "v_and_b32 v15, 7, v4\n" \
  "v_lshlrev_b32 v7, 11, v5\n" \
  "v_lshl_add_u32 v7, v15, 4, v7\n" \
  "v_lshlrev_b32 v5, 5, v21\n" \
  "v_xor_b32 v7, v7, v5\n" \
  "s_add_u32 m0, s86, 0\n" \
  "s_nop 0\n" \
  "buffer_load_dwordx4 v7, s[68:71], s90 offen lds\n" \
  "s_add_u32 s90, s90, 16384\n" \
  "s_add_u32 m0, s86, 1024\n" \
  "s_nop 0\n" \
  "buffer_load_dwordx4 v7, s[68:71], s90 offen lds\n" \
  "s_add_u32 s90, s90, 16384\n" \
  "s_add_u32 m0, s86, 2048\n" \
  "s_nop 0\n" \
  "buffer_load_dwordx4 v7, s[68:71], s90 offen lds\n" \
  "s_add_u32 s90, s90, 16384\n" \
  "s_add_u32 m0, s86, 3072\n" \
  "s_nop 0\n" \
  "buffer_load_dwordx4 v7, s[68:71], s90 offen lds\n" \
  "s_add_u32 s90, s90, 16384\n" \
  "s_add_u32 m0, s86, 4096\n" \
  "s_nop 0\n" \
  "buffer_load_dwordx4 v7, s[68:71], s90 offen lds\n" \
  "s_add_u32 s90, s90, 16384\n" \
  "s_add_u32 m0, s86, 5120\n" \
  "s_nop 0\n" \
  "buffer_load_dwordx4 v7, s[68:71], s90 offen lds\n" \
  "s_add_u32 s90, s90, 16384\n" \
  "s_add_u32 m0, s86, 6144\n" \
  "s_nop 0\n" \
  "buffer_load_dwordx4 v7, s[68:71], s90 offen lds\n" \
  "s_add_u32 s90, s90, 16384\n" \
  "s_add_u32 m0, s86, 7168\n" \
  "s_nop 0\n" \
  "buffer_load_dwordx4 v7, s[68:71], s90 offen lds\n" \
  "s_add_u32 s90, s90, 16384\n" \
  "s_add_u32 m0, s86, 8192\n" \
  "s_nop 0\n" \
  "buffer_load_dwordx4 v7, s[68:71], s90 offen lds\n" \
  "s_add_u32 s90, s90, 16384\n" \
  "s_add_u32 m0, s86, 9216\n" \
  "s_nop 0\n" \
  "buffer_load_dwordx4 v7, s[68:71], s90 offen lds\n" \
  "s_add_u32 s90, s90, 16384\n" \
  "s_add_u32 m0, s86, 10240\n" \
  "s_nop 0\n" \
  "buffer_load_dwordx4 v7, s[68:71], s90 offen lds\n" \
  "s_add_u32 s90, s90, 16384\n" \
  "s_add_u32 m0, s86, 11264\n" \
  "s_nop 0\n" \
  "buffer_load_dwordx4 v7, s[68:71], s90 offen lds\n" \
  "s_add_u32 s90, s90, 16384\n" \
  "s_add_u32 m0, s86, 12288\n" \
  "s_nop 0\n" \
  "buffer_load_dwordx4 v7, s[68:71], s90 offen lds\n" \
  "s_add_u32 s90, s90, 16384\n" \
  "s_add_u32 m0, s86, 13312\n" \
  "s_nop 0\n" \
  "buffer_load_dwordx4 v7, s[68:71], s90 offen lds\n" \
  "s_add_u32 s90, s90, 16384\n" \
  "s_add_u32 m0, s86, 14336\n" \
  "s_nop 0\n" \
  "buffer_load_dwordx4 v7, s[68:71], s90 offen lds\n" \
  "s_add_u32 s90, s90, 16384\n" \
  "s_add_u32 m0, s86, 15360\n" \
  "s_nop 0\n" \
  "buffer_load_dwordx4 v7, s[68:71], s90 offen lds\n" \
  "s_add_u32 s90, s90, 16384\n" \
  "s_add_u32 m0, s86, 16384\n" \
  "s_nop 0\n" \
  "buffer_load_dwordx4 v7, s[68:71], s90 offen lds\n" \
  "s_add_u32 s90, s90, 16384\n" \
  "s_add_u32 m0, s86, 17408\n" \
  "s_nop 0\n" \
  "buffer_load_dwordx4 v7, s[68:71], s90 offen lds\n" \
  "s_add_u32 s90, s90, 16384\n" \
  "s_add_u32 m0, s86, 18432\n" \
  "s_nop 0\n" \
  "buffer_load_dwordx4 v7, s[68:71], s90 offen lds\n" \
  "s_add_u32 s90, s90, 16384\n" \
  "s_add_u32 m0, s86, 19456\n" \
  "s_nop 0\n" \
  "buffer_load_dwordx4 v7, s[68:71], s90 offen lds\n" \
  "s_add_u32 s90, s90, 16384\n" \
  "s_add_u32 m0, s86, 20480\n" \
  "s_nop 0\n" \
  "buffer_load_dwordx4 v7, s[68:71], s90 offen lds\n" \
  "s_add_u32 s90, s90, 16384\n" \
  "s_add_u32 m0, s86, 21504\n" \
  "s_nop 0\n" \
  "buffer_load_dwordx4 v7, s[68:71], s90 offen lds\n" \
  "s_add_u32 s90, s90, 16384\n" \
  "s_add_u32 m0, s86, 22528\n" \
  "s_nop 0\n" \
  "buffer_load_dwordx4 v7, s[68:71], s90 offen lds\n" \
  "s_add_u32 s90, s90, 16384\n" \
  "s_add_u32 m0, s86, 23552\n" \
  "s_nop 0\n" \
  "buffer_load_dwordx4 v7, s[68:71], s90 offen lds\n" \
  "s_add_u32 s90, s90, 16384\n" \
  "s_add_u32 m0, s86, 24576\n" \
  "s_nop 0\n" \
  "buffer_load_dwordx4 v7, s[68:71], s90 offen lds\n" \
  "s_add_u32 s90, s90, 16384\n" \
  "s_add_u32 m0, s86, 25600\n" \
  "s_nop 0\n" \
  "buffer_load_dwordx4 v7, s[68:71], s90 offen lds\n" \
  "s_add_u32 s90, s90, 16384\n" \
  "s_add_u32 m0, s86, 26624\n" \
  "s_nop 0\n" \
  "buffer_load_dwordx4 v7, s[68:71], s90 offen lds\n" \
  "s_add_u32 s90, s90, 16384\n" \
  "s_add_u32 m0, s86, 27648\n" \
  "s_nop 0\n" \
  "buffer_load_dwordx4 v7, s[68:71], s90 offen lds\n" \
  "s_add_u32 s90, s90, 16384\n" \
  "s_add_u32 m0, s86, 28672\n" \
  "s_nop 0\n" \
  "buffer_load_dwordx4 v7, s[68:71], s90 offen lds\n" \
  "s_add_u32 s90, s90, 16384\n" \
  "s_add_u32 m0, s86, 29696\n" \
  "s_nop 0\n" \
  "buffer_load_dwordx4 v7, s[68:71], s90 offen lds\n" \
  "s_add_u32 s90, s90, 16384\n" \
  "s_add_u32 m0, s86, 30720\n" \
  "s_nop 0\n" \
  "buffer_load_dwordx4 v7, s[68:71], s90 offen lds\n" \
  "s_add_u32 s90, s90, 16384\n" \
  "s_add_u32 m0, s86, 31744\n" \
  "s_nop 0\n" \
  "buffer_load_dwordx4 v7, s[68:71], s90 offen lds\n" \
  "s_add_u32 s90, s90, 16384\n" \
  "v_lshrrev_b32 v5, 2, v20\n" \
  "v_lshl_add_u32 v5, v21, 2, v5\n" \
  "v_lshlrev_b32 v15, 7, v5\n" \
  "v_lshrrev_b32 v5, 1, v5\n" \
  "v_and_b32 v5, 3, v5\n" \
  "v_lshl_add_u32 v15, v5, 5, v15\n" \
  "v_and_b32 v5, 3, v4\n" \
  "v_lshl_add_u32 v15, v5, 3, v15\n" \
  "v_xor_b32 v5, 0, v15\n" \
  "v_add_u32 v16, s86, v5\n" \
  "v_xor_b32 v5, 32, v15\n" \
  "v_add_u32 v17, s86, v5\n" \
  "v_xor_b32 v5, 64, v15\n" \
  "v_add_u32 v18, s86, v5\n" \
  "v_xor_b32 v5, 96, v15\n" \
  "v_add_u32 v19, s86, v5\n" \
  "s_lshl_b32 s84, %5, 12\n" \
  "s_add_u32 s84, s84, 131072\n" \
  "s_add_u32 s84, s84, %6\n" \
  "v_xor_b32 v5, v21, v20\n" \
  "v_lshlrev_b32 v5, 4, v5\n" \
  "v_lshl_add_u32 v5, v20, 8, v5\n" \
  "v_add_u32 v5, s84, v5\n" \
  "v_xor_b32 v12, 64, v5\n" \
  "v_xor_b32 v13, 128, v5\n" \
  "v_xor_b32 v14, 192, v5\n" \
  "s_lshl_b32 s84, %5, 2\n" \
  "v_add_u32 v5, s84, v21\n" \
  "v_lshlrev_b32 v10, 12, v5\n" \
  "v_lshl_add_u32 v10, v20, 3, v10\n" \
  "v_xor_b32 v15, v5, v20\n" \
  "v_lshlrev_b32 v15, 4, v15\n" \
  "v_lshl_add_u32 v15, v5, 8, v15\n" \
  "s_add_u32 s84, %6, 131072\n" \
  "v_add_u32 v11, s84, v15\n" \
  "s_and_b32 s84, %4, 1\n" \
  "s_lshl_b32 s96, s84, 18\n" \
  "s_lshl_b32 s85, %5, 16\n" \
  "s_add_u32 s96, s96, s85\n" \
  "s_lshl_b32 s88, s84, 16\n" \
  "s_lshr_b32 s84, %4, 1\n" \
  "s_lshl_b32 s84, s84, 7\n" \
  "s_add_u32 s88, s88, s84\n" \
  "v_and_b32 v8, 7, v4\n" \
  "v_lshlrev_b32 v8, 7, v8\n" \
  "s_lshl_b32 s84, %5, 10\n" \
  "s_add_u32 s84, s84, " STR(CW_XPROG) "*4\n" \
  "v_add_u32 v8, s84, v8\n" \
  "s_mov_b32 s91, 0\n" \
  "s_mov_b32 s89, 0\n" \
  "s_lshr_b32 s84, s89, 2\n" \
  "s_xnor_b32 s84, s84, %7\n" \
  "s_and_b32 s84, s84, 1\n" \
  "s_lshl_b32 s84, s84, 13\n" \
  "s_lshl_b32 s87, s89, 19\n" \
  "s_or_b32 s87, s87, s84\n" \
  "s_add_u32 s87, s87, s96\n" \
  "s_waitcnt vmcnt(16)\n" \
  "ds_read_b64_tr_b16 v[128:129], v16 offset:0\n" \
  "ds_read_b64_tr_b16 v[130:131], v16 offset:2048\n" \
  "ds_read_b64_tr_b16 v[132:133], v17 offset:0\n" \
  "ds_read_b64_tr_b16 v[134:135], v17 offset:2048\n" \
  "ds_read_b64_tr_b16 v[136:137], v18 offset:0\n" \
  "ds_read_b64_tr_b16 v[138:139], v18 offset:2048\n" \
  "ds_read_b64_tr_b16 v[140:141], v19 offset:0\n" \
  "ds_read_b64_tr_b16 v[142:143], v19 offset:2048\n" \
  "ds_read_b64_tr_b16 v[144:145], v16 offset:4096\n" \
  "ds_read_b64_tr_b16 v[146:147], v16 offset:6144\n" \
  "ds_read_b64_tr_b16 v[148:149], v17 offset:4096\n" \
  "ds_read_b64_tr_b16 v[150:151], v17 offset:6144\n" \
  "ds_read_b64_tr_b16 v[152:153], v18 offset:4096\n" \
  "ds_read_b64_tr_b16 v[154:155], v18 offset:6144\n" \
  "ds_read_b64_tr_b16 v[156:157], v19 offset:4096\n" \
  "ds_read_b64_tr_b16 v[158:159], v19 offset:6144\n" \
  "ds_read_b64_tr_b16 v[160:161], v16 offset:8192\n" \
  "ds_read_b64_tr_b16 v[162:163], v16 offset:10240\n" \
  "ds_read_b64_tr_b16 v[164:165], v17 offset:8192\n" \
  "ds_read_b64_tr_b16 v[166:167], v17 offset:10240\n" \
  "ds_read_b64_tr_b16 v[168:169], v18 offset:8192\n" \
  "ds_read_b64_tr_b16 v[170:171], v18 offset:10240\n" \
  "ds_read_b64_tr_b16 v[172:173], v19 offset:8192\n" \
  "ds_read_b64_tr_b16 v[174:175], v19 offset:10240\n" \
  "ds_read_b64_tr_b16 v[176:177], v16 offset:12288\n" \
  "ds_read_b64_tr_b16 v[178:179], v16 offset:14336\n" \
  "ds_read_b64_tr_b16 v[180:181], v17 offset:12288\n" \
  "ds_read_b64_tr_b16 v[182:183], v17 offset:14336\n" \
  "ds_read_b64_tr_b16 v[184:185], v18 offset:12288\n" \
  "ds_read_b64_tr_b16 v[186:187], v18 offset:14336\n" \
  "ds_read_b64_tr_b16 v[188:189], v19 offset:12288\n" \
  "ds_read_b64_tr_b16 v[190:191], v19 offset:14336\n" \
  "s_waitcnt lgkmcnt(0)\n" \
  "s_add_u32 m0, s86, 0\n" \
  "s_nop 0\n" \
  "buffer_load_dwordx4 v7, s[68:71], s90 offen lds\n" \
  "s_add_u32 s90, s90, 16384\n" \
  "s_add_u32 m0, s86, 1024\n" \
  "s_nop 0\n" \
  "buffer_load_dwordx4 v7, s[68:71], s90 offen lds\n" \
  "s_add_u32 s90, s90, 16384\n" \
  "s_add_u32 m0, s86, 2048\n" \
  "s_nop 0\n" \
  "buffer_load_dwordx4 v7, s[68:71], s90 offen lds\n" \
  "s_add_u32 s90, s90, 16384\n" \
  "s_add_u32 m0, s86, 3072\n" \
  "s_nop 0\n" \
  "buffer_load_dwordx4 v7, s[68:71], s90 offen lds\n" \
  "s_add_u32 s90, s90, 16384\n" \
  "s_add_u32 m0, s86, 4096\n" \
  "s_nop 0\n" \
  "buffer_load_dwordx4 v7, s[68:71], s90 offen lds\n" \
  "s_add_u32 s90, s90, 16384\n" \
  "s_add_u32 m0, s86, 5120\n" \
  "s_nop 0\n" \
  "buffer_load_dwordx4 v7, s[68:71], s90 offen lds\n" \
  "s_add_u32 s90, s90, 16384\n" \
  "s_add_u32 m0, s86, 6144\n" \
  "s_nop 0\n" \
  "buffer_load_dwordx4 v7, s[68:71], s90 offen lds\n" \
  "s_add_u32 s90, s90, 16384\n" \
  "s_add_u32 m0, s86, 7168\n" \
  "s_nop 0\n" \
  "buffer_load_dwordx4 v7, s[68:71], s90 offen lds\n" \
  "s_add_u32 s90, s90, 16384\n" \
  "s_add_u32 m0, s86, 8192\n" \
  "s_nop 0\n" \
  "buffer_load_dwordx4 v7, s[68:71], s90 offen lds\n" \
  "s_add_u32 s90, s90, 16384\n" \
  "s_add_u32 m0, s86, 9216\n" \
  "s_nop 0\n" \
  "buffer_load_dwordx4 v7, s[68:71], s90 offen lds\n" \
  "s_add_u32 s90, s90, 16384\n" \
  "s_add_u32 m0, s86, 10240\n" \
  "s_nop 0\n" \
  "buffer_load_dwordx4 v7, s[68:71], s90 offen lds\n" \
  "s_add_u32 s90, s90, 16384\n" \
  "s_add_u32 m0, s86, 11264\n" \
  "s_nop 0\n" \
  "buffer_load_dwordx4 v7, s[68:71], s90 offen lds\n" \
  "s_add_u32 s90, s90, 16384\n" \
  "s_add_u32 m0, s86, 12288\n" \
  "s_nop 0\n" \
  "buffer_load_dwordx4 v7, s[68:71], s90 offen lds\n" \
  "s_add_u32 s90, s90, 16384\n" \
  "s_add_u32 m0, s86, 13312\n" \
  "s_nop 0\n" \
  "buffer_load_dwordx4 v7, s[68:71], s90 offen lds\n" \
  "s_add_u32 s90, s90, 16384\n" \
  "s_add_u32 m0, s86, 14336\n" \
  "s_nop 0\n" \
  "buffer_load_dwordx4 v7, s[68:71], s90 offen lds\n" \
  "s_add_u32 s90, s90, 16384\n" \
  "s_add_u32 m0, s86, 15360\n" \
  "s_nop 0\n" \
  "buffer_load_dwordx4 v7, s[68:71], s90 offen lds\n" \
  "s_add_u32 s90, s90, 16384\n" \
  "s_waitcnt vmcnt(16)\n" \
  "ds_read_b64_tr_b16 v[192:193], v16 offset:16384\n" \
  "ds_read_b64_tr_b16 v[194:195], v16 offset:18432\n" \
  "ds_read_b64_tr_b16 v[196:197], v17 offset:16384\n" \
  "ds_read_b64_tr_b16 v[198:199], v17 offset:18432\n" \
  "ds_read_b64_tr_b16 v[200:201], v18 offset:16384\n" \
  "ds_read_b64_tr_b16 v[202:203], v18 offset:18432\n" \
  "ds_read_b64_tr_b16 v[204:205], v19 offset:16384\n" \
  "ds_read_b64_tr_b16 v[206:207], v19 offset:18432\n" \
  "ds_read_b64_tr_b16 v[208:209], v16 offset:20480\n" \
  "ds_read_b64_tr_b16 v[210:211], v16 offset:22528\n" \
  "ds_read_b64_tr_b16 v[212:213], v17 offset:20480\n" \
  "ds_read_b64_tr_b16 v[214:215], v17 offset:22528\n" \
  "ds_read_b64_tr_b16 v[216:217], v18 offset:20480\n" \
  "ds_read_b64_tr_b16 v[218:219], v18 offset:22528\n" \
  "ds_read_b64_tr_b16 v[220:221], v19 offset:20480\n" \
  "ds_read_b64_tr_b16 v[222:223], v19 offset:22528\n" \
  "ds_read_b64_tr_b16 v[224:225], v16 offset:24576\n" \
  "ds_read_b64_tr_b16 v[226:227], v16 offset:26624\n" \
  "ds_read_b64_tr_b16 v[228:229], v17 offset:24576\n" \
  "ds_read_b64_tr_b16 v[230:231], v17 offset:26624\n" \
  "ds_read_b64_tr_b16 v[232:233], v18 offset:24576\n" \
  "ds_read_b64_tr_b16 v[234:235], v18 offset:26624\n" \
  "ds_read_b64_tr_b16 v[236:237], v19 offset:24576\n" \
  "ds_read_b64_tr_b16 v[238:239], v19 offset:26624\n" \
  "ds_read_b64_tr_b16 v[240:241], v16 offset:28672\n" \
  "ds_read_b64_tr_b16 v[242:243], v16 offset:30720\n" \
  "ds_read_b64_tr_b16 v[244:245], v17 offset:28672\n" \
  "ds_read_b64_tr_b16 v[246:247], v17 offset:30720\n" \
  "ds_read_b64_tr_b16 v[248:249], v18 offset:28672\n" \
  "ds_read_b64_tr_b16 v[250:251], v18 offset:30720\n" \
  "ds_read_b64_tr_b16 v[252:253], v19 offset:28672\n" \
  "ds_read_b64_tr_b16 v[254:255], v19 offset:30720\n" \
  "s_waitcnt lgkmcnt(0)\n" \
  "s_add_u32 m0, s86, 16384\n" \
  "s_nop 0\n" \
  "buffer_load_dwordx4 v7, s[68:71], s90 offen lds\n" \
  "s_add_u32 s90, s90, 16384\n" \
  "s_add_u32 m0, s86, 17408\n" \
  "s_nop 0\n" \
  "buffer_load_dwordx4 v7, s[68:71], s90 offen lds\n" \
  "s_add_u32 s90, s90, 16384\n" \
  "s_add_u32 m0, s86, 18432\n" \
  "s_nop 0\n" \
  "buffer_load_dwordx4 v7, s[68:71], s90 offen lds\n" \
  "s_add_u32 s90, s90, 16384\n" \
  "s_add_u32 m0, s86, 19456\n" \
  "s_nop 0\n" \
  "buffer_load_dwordx4 v7, s[68:71], s90 offen lds\n" \
  "s_add_u32 s90, s90, 16384\n" \
  "s_add_u32 m0, s86, 20480\n" \
  "s_nop 0\n" \
  "buffer_load_dwordx4 v7, s[68:71], s90 offen lds\n" \
  "s_add_u32 s90, s90, 16384\n" \
  "s_add_u32 m0, s86, 21504\n" \
  "s_nop 0\n" \
  "buffer_load_dwordx4 v7, s[68:71], s90 offen lds\n" \
  "s_add_u32 s90, s90, 16384\n" \
  "s_add_u32 m0, s86, 22528\n" \
  "s_nop 0\n" \
  "buffer_load_dwordx4 v7, s[68:71], s90 offen lds\n" \
  "s_add_u32 s90, s90, 16384\n" \
  "s_add_u32 m0, s86, 23552\n" \
  "s_nop 0\n" \
  "buffer_load_dwordx4 v7, s[68:71], s90 offen lds\n" \
  "s_add_u32 s90, s90, 16384\n" \
  "s_add_u32 m0, s86, 24576\n" \
  "s_nop 0\n" \
  "buffer_load_dwordx4 v7, s[68:71], s90 offen lds\n" \
  "s_add_u32 s90, s90, 16384\n" \
  "s_add_u32 m0, s86, 25600\n" \
  "s_nop 0\n" \
  "buffer_load_dwordx4 v7, s[68:71], s90 offen lds\n" \
  "s_add_u32 s90, s90, 16384\n" \
  "s_add_u32 m0, s86, 26624\n" \
  "s_nop 0\n" \
  "buffer_load_dwordx4 v7, s[68:71], s90 offen lds\n" \
  "s_add_u32 s90, s90, 16384\n" \
  "s_add_u32 m0, s86, 27648\n" \
  "s_nop 0\n" \
  "buffer_load_dwordx4 v7, s[68:71], s90 offen lds\n" \
  "s_add_u32 s90, s90, 16384\n" \
  "s_add_u32 m0, s86, 28672\n" \
  "s_nop 0\n" \
  "buffer_load_dwordx4 v7, s[68:71], s90 offen lds\n" \
  "s_add_u32 s90, s90, 16384\n" \
  "s_add_u32 m0, s86, 29696\n" \
  "s_nop 0\n" \
  "buffer_load_dwordx4 v7, s[68:71], s90 offen lds\n" \
  "s_add_u32 s90, s90, 16384\n" \
  "s_add_u32 m0, s86, 30720\n" \
  "s_nop 0\n" \
  "buffer_load_dwordx4 v7, s[68:71], s90 offen lds\n" \
  "s_add_u32 s90, s90, 16384\n" \
  "s_add_u32 m0, s86, 31744\n" \
  "s_nop 0\n" \
  "buffer_load_dwordx4 v7, s[68:71], s90 offen lds\n" \
  "s_add_u32 s90, s90, 16384\n" \
  "s_waitcnt vmcnt(16)\n" \
  "ds_read_b64_tr_b16 a[0:1], v16 offset:0\n" \
  "ds_read_b64_tr_b16 a[2:3], v16 offset:2048\n" \
  "ds_read_b64_tr_b16 a[4:5], v17 offset:0\n" \
  "ds_read_b64_tr_b16 a[6:7], v17 offset:2048\n" \
  "ds_read_b64_tr_b16 a[8:9], v18 offset:0\n" \
  "ds_read_b64_tr_b16 a[10:11], v18 offset:2048\n" \
  "ds_read_b64_tr_b16 a[12:13], v19 offset:0\n" \
  "ds_read_b64_tr_b16 a[14:15], v19 offset:2048\n" \
  "ds_read_b64_tr_b16 a[16:17], v16 offset:4096\n" \
  "ds_read_b64_tr_b16 a[18:19], v16 offset:6144\n" \
  "ds_read_b64_tr_b16 a[20:21], v17 offset:4096\n" \
  "ds_read_b64_tr_b16 a[22:23], v17 offset:6144\n" \
  "ds_read_b64_tr_b16 a[24:25], v18 offset:4096\n" \
  "ds_read_b64_tr_b16 a[26:27], v18 offset:6144\n" \
  "ds_read_b64_tr_b16 a[28:29], v19 offset:4096\n" \
  "ds_read_b64_tr_b16 a[30:31], v19 offset:6144\n" \
  "ds_read_b64_tr_b16 a[32:33], v16 offset:8192\n" \
  "ds_read_b64_tr_b16 a[34:35], v16 offset:10240\n" \
  "ds_read_b64_tr_b16 a[36:37], v17 offset:8192\n" \
  "ds_read_b64_tr_b16 a[38:39], v17 offset:10240\n" \
  "ds_read_b64_tr_b16 a[40:41], v18 offset:8192\n" \
  "ds_read_b64_tr_b16 a[42:43], v18 offset:10240\n" \
  "ds_read_b64_tr_b16 a[44:45], v19 offset:8192\n" \
  "ds_read_b64_tr_b16 a[46:47], v19 offset:10240\n" \
  "ds_read_b64_tr_b16 a[48:49], v16 offset:12288\n" \
  "ds_read_b64_tr_b16 a[50:51], v16 offset:14336\n" \
  "ds_read_b64_tr_b16 a[52:53], v17 offset:12288\n" \
  "ds_read_b64_tr_b16 a[54:55], v17 offset:14336\n" \
  "ds_read_b64_tr_b16 a[56:57], v18 offset:12288\n" \
  "ds_read_b64_tr_b16 a[58:59], v18 offset:14336\n" \
  "ds_read_b64_tr_b16 a[60:61], v19 offset:12288\n" \
  "ds_read_b64_tr_b16 a[62:63], v19 offset:14336\n" \
  "s_waitcnt lgkmcnt(0)\n" \
  "s_add_u32 m0, s86, 0\n" \
  "s_nop 0\n" \
  "buffer_load_dwordx4 v7, s[68:71], s90 offen lds\n" \
  "s_add_u32 s90, s90, 16384\n" \
  "s_add_u32 m0, s86, 1024\n" \
  "s_nop 0\n" \
  "buffer_load_dwordx4 v7, s[68:71], s90 offen lds\n" \
  "s_add_u32 s90, s90, 16384\n" \
  "s_add_u32 m0, s86, 2048\n" \
  "s_nop 0\n" \
  "buffer_load_dwordx4 v7, s[68:71], s90 offen lds\n" \
  "s_add_u32 s90, s90, 16384\n" \
  "s_add_u32 m0, s86, 3072\n" \
  "s_nop 0\n" \
  "buffer_load_dwordx4 v7, s[68:71], s90 offen lds\n" \
  "s_add_u32 s90, s90, 16384\n" \
  "s_add_u32 m0, s86, 4096\n" \
  "s_nop 0\n" \
  "buffer_load_dwordx4 v7, s[68:71], s90 offen lds\n" \
  "s_add_u32 s90, s90, 16384\n" \
  "s_add_u32 m0, s86, 5120\n" \
  "s_nop 0\n" \
  "buffer_load_dwordx4 v7, s[68:71], s90 offen lds\n" \
  "s_add_u32 s90, s90, 16384\n" \
  "s_add_u32 m0, s86, 6144\n" \
  "s_nop 0\n" \
  "buffer_load_dwordx4 v7, s[68:71], s90 offen lds\n" \
  "s_add_u32 s90, s90, 16384\n" \
  "s_add_u32 m0, s86, 7168\n" \
  "s_nop 0\n" \
  "buffer_load_dwordx4 v7, s[68:71], s90 offen lds\n" \
  "s_add_u32 s90, s90, 16384\n" \
  "s_add_u32 m0, s86, 8192\n" \
  "s_nop 0\n" \
  "buffer_load_dwordx4 v7, s[68:71], s90 offen lds\n" \
  "s_add_u32 s90, s90, 16384\n" \
  "s_add_u32 m0, s86, 9216\n" \
  "s_nop 0\n" \
  "buffer_load_dwordx4 v7, s[68:71], s90 offen lds\n" \
  "s_add_u32 s90, s90, 16384\n" \
  "s_add_u32 m0, s86, 10240\n" \
  "s_nop 0\n" \
  "buffer_load_dwordx4 v7, s[68:71], s90 offen lds\n" \
  "s_add_u32 s90, s90, 16384\n" \
  "s_add_u32 m0, s86, 11264\n" \
  "s_nop 0\n" \
  "buffer_load_dwordx4 v7, s[68:71], s90 offen lds\n" \
  "s_add_u32 s90, s90, 16384\n" \
  "s_add_u32 m0, s86, 12288\n" \
  "s_nop 0\n" \
  "buffer_load_dwordx4 v7, s[68:71], s90 offen lds\n" \
  "s_add_u32 s90, s90, 16384\n" \
  "s_add_u32 m0, s86, 13312\n" \
  "s_nop 0\n" \
  "buffer_load_dwordx4 v7, s[68:71], s90 offen lds\n" \
  "s_add_u32 s90, s90, 16384\n" \
  "s_add_u32 m0, s86, 14336\n" \
  "s_nop 0\n" \
  "buffer_load_dwordx4 v7, s[68:71], s90 offen lds\n" \
  "s_add_u32 s90, s90, 16384\n" \
  "s_add_u32 m0, s86, 15360\n" \
  "s_nop 0\n" \
  "buffer_load_dwordx4 v7, s[68:71], s90 offen lds\n" \
  "s_add_u32 s90, s90, 16384\n" \
  "s_waitcnt vmcnt(16)\n" \
  "ds_read_b64_tr_b16 a[64:65], v16 offset:16384\n" \
  "ds_read_b64_tr_b16 a[66:67], v16 offset:18432\n" \
  "ds_read_b64_tr_b16 a[68:69], v17 offset:16384\n" \
  "ds_read_b64_tr_b16 a[70:71], v17 offset:18432\n" \
  "ds_read_b64_tr_b16 a[72:73], v18 offset:16384\n" \
  "ds_read_b64_tr_b16 a[74:75], v18 offset:18432\n" \
  "ds_read_b64_tr_b16 a[76:77], v19 offset:16384\n" \
  "ds_read_b64_tr_b16 a[78:79], v19 offset:18432\n" \
  "ds_read_b64_tr_b16 a[80:81], v16 offset:20480\n" \
  "ds_read_b64_tr_b16 a[82:83], v16 offset:22528\n" \
  "ds_read_b64_tr_b16 a[84:85], v17 offset:20480\n" \
  "ds_read_b64_tr_b16 a[86:87], v17 offset:22528\n" \
  "ds_read_b64_tr_b16 a[88:89], v18 offset:20480\n" \
  "ds_read_b64_tr_b16 a[90:91], v18 offset:22528\n" \
  "ds_read_b64_tr_b16 a[92:93], v19 offset:20480\n" \
  "ds_read_b64_tr_b16 a[94:95], v19 offset:22528\n" \
  "ds_read_b64_tr_b16 a[96:97], v16 offset:24576\n" \
  "ds_read_b64_tr_b16 a[98:99], v16 offset:26624\n" \
  "ds_read_b64_tr_b16 a[100:101], v17 offset:24576\n" \
  "ds_read_b64_tr_b16 a[102:103], v17 offset:26624\n" \
  "ds_read_b64_tr_b16 a[104:105], v18 offset:24576\n" \
  "ds_read_b64_tr_b16 a[106:107], v18 offset:26624\n" \
  "ds_read_b64_tr_b16 a[108:109], v19 offset:24576\n" \
  "ds_read_b64_tr_b16 a[110:111], v19 offset:26624\n" \
  "ds_read_b64_tr_b16 a[112:113], v16 offset:28672\n" \
  "ds_read_b64_tr_b16 a[114:115], v16 offset:30720\n" \
  "ds_read_b64_tr_b16 a[116:117], v17 offset:28672\n" \
  "ds_read_b64_tr_b16 a[118:119], v17 offset:30720\n" \
  "ds_read_b64_tr_b16 a[120:121], v18 offset:28672\n" \
  "ds_read_b64_tr_b16 a[122:123], v18 offset:30720\n" \
  "ds_read_b64_tr_b16 a[124:125], v19 offset:28672\n" \
  "ds_read_b64_tr_b16 a[126:127], v19 offset:30720\n" \
  "s_waitcnt lgkmcnt(0)\n" \
  "s_add_u32 m0, s86, 16384\n" \
  "s_nop 0\n" \
  "buffer_load_dwordx4 v7, s[68:71], s90 offen lds\n" \
  "s_add_u32 s90, s90, 16384\n" \
  "s_add_u32 m0, s86, 17408\n" \
  "s_nop 0\n" \
  "buffer_load_dwordx4 v7, s[68:71], s90 offen lds\n" \
  "s_add_u32 s90, s90, 16384\n" \
  "s_add_u32 m0, s86, 18432\n" \
  "s_nop 0\n" \
  "buffer_load_dwordx4 v7, s[68:71], s90 offen lds\n" \
  "s_add_u32 s90, s90, 16384\n" \
  "s_add_u32 m0, s86, 19456\n" \
  "s_nop 0\n" \
  "buffer_load_dwordx4 v7, s[68:71], s90 offen lds\n" \
  "s_add_u32 s90, s90, 16384\n" \
  "s_add_u32 m0, s86, 20480\n" \
  "s_nop 0\n" \
  "buffer_load_dwordx4 v7, s[68:71], s90 offen lds\n" \
  "s_add_u32 s90, s90, 16384\n" \
  "s_add_u32 m0, s86, 21504\n" \
  "s_nop 0\n" \
  "buffer_load_dwordx4 v7, s[68:71], s90 offen lds\n" \
  "s_add_u32 s90, s90, 16384\n" \
  "s_add_u32 m0, s86, 22528\n" \
  "s_nop 0\n" \
  "buffer_load_dwordx4 v7, s[68:71], s90 offen lds\n" \
  "s_add_u32 s90, s90, 16384\n" \
  "s_add_u32 m0, s86, 23552\n" \
  "s_nop 0\n" \
  "buffer_load_dwordx4 v7, s[68:71], s90 offen lds\n" \
  "s_add_u32 s90, s90, 16384\n" \
  "s_add_u32 m0, s86, 24576\n" \
  "s_nop 0\n" \
  "buffer_load_dwordx4 v7, s[68:71], s90 offen lds\n" \
  "s_add_u32 s90, s90, 16384\n" \
  "s_add_u32 m0, s86, 25600\n" \
  "s_nop 0\n" \
  "buffer_load_dwordx4 v7, s[68:71], s90 offen lds\n" \
  "s_add_u32 s90, s90, 16384\n" \
  "s_add_u32 m0, s86, 26624\n" \
  "s_nop 0\n" \
  "buffer_load_dwordx4 v7, s[68:71], s90 offen lds\n" \
  "s_add_u32 s90, s90, 16384\n" \
  "s_add_u32 m0, s86, 27648\n" \
  "s_nop 0\n" \
  "buffer_load_dwordx4 v7, s[68:71], s90 offen lds\n" \
  "s_add_u32 s90, s90, 16384\n" \
  "s_add_u32 m0, s86, 28672\n" \
  "s_nop 0\n" \
  "buffer_load_dwordx4 v7, s[68:71], s90 offen lds\n" \
  "s_add_u32 s90, s90, 16384\n" \
  "s_add_u32 m0, s86, 29696\n" \
  "s_nop 0\n" \
  "buffer_load_dwordx4 v7, s[68:71], s90 offen lds\n" \
  "s_add_u32 s90, s90, 16384\n" \
  "s_add_u32 m0, s86, 30720\n" \
  "s_nop 0\n" \
  "buffer_load_dwordx4 v7, s[68:71], s90 offen lds\n" \
  "s_add_u32 s90, s90, 16384\n" \
  "s_add_u32 m0, s86, 31744\n" \
  "s_nop 0\n" \
  "buffer_load_dwordx4 v7, s[68:71], s90 offen lds\n" \
  "s_add_u32 s90, s90, 16384\n" \
  "s_waitcnt vmcnt(16)\n" \
  "ds_read_b64_tr_b16 a[128:129], v16 offset:0\n" \
  "ds_read_b64_tr_b16 a[130:131], v16 offset:2048\n" \
  "ds_read_b64_tr_b16 a[132:133], v17 offset:0\n" \
  "ds_read_b64_tr_b16 a[134:135], v17 offset:2048\n" \
  "ds_read_b64_tr_b16 a[136:137], v18 offset:0\n" \
  "ds_read_b64_tr_b16 a[138:139], v18 offset:2048\n" \
  "ds_read_b64_tr_b16 a[140:141], v19 offset:0\n" \
  "ds_read_b64_tr_b16 a[142:143], v19 offset:2048\n" \
  "ds_read_b64_tr_b16 a[144:145], v16 offset:4096\n" \
  "ds_read_b64_tr_b16 a[146:147], v16 offset:6144\n" \
  "ds_read_b64_tr_b16 a[148:149], v17 offset:4096\n" \
  "ds_read_b64_tr_b16 a[150:151], v17 offset:6144\n" \
  "ds_read_b64_tr_b16 a[152:153], v18 offset:4096\n" \
  "ds_read_b64_tr_b16 a[154:155], v18 offset:6144\n" \
  "ds_read_b64_tr_b16 a[156:157], v19 offset:4096\n" \
  "ds_read_b64_tr_b16 a[158:159], v19 offset:6144\n" \
  "ds_read_b64_tr_b16 a[160:161], v16 offset:8192\n" \
  "ds_read_b64_tr_b16 a[162:163], v16 offset:10240\n" \
  "ds_read_b64_tr_b16 a[164:165], v17 offset:8192\n" \
  "ds_read_b64_tr_b16 a[166:167], v17 offset:10240\n" \
  "ds_read_b64_tr_b16 a[168:169], v18 offset:8192\n" \
  "ds_read_b64_tr_b16 a[170:171], v18 offset:10240\n" \
  "ds_read_b64_tr_b16 a[172:173], v19 offset:8192\n" \
  "ds_read_b64_tr_b16 a[174:175], v19 offset:10240\n" \
  "ds_read_b64_tr_b16 a[176:177], v16 offset:12288\n" \
  "ds_read_b64_tr_b16 a[178:179], v16 offset:14336\n" \
  "ds_read_b64_tr_b16 a[180:181], v17 offset:12288\n" \
  "ds_read_b64_tr_b16 a[182:183], v17 offset:14336\n" \
  "ds_read_b64_tr_b16 a[184:185], v18 offset:12288\n" \
  "ds_read_b64_tr_b16 a[186:187], v18 offset:14336\n" \
  "ds_read_b64_tr_b16 a[188:189], v19 offset:12288\n" \
  "ds_read_b64_tr_b16 a[190:191], v19 offset:14336\n" \
  "s_waitcnt lgkmcnt(0)\n" \
  "s_add_u32 m0, s86, 0\n" \
  "s_nop 0\n" \
  "buffer_load_dwordx4 v7, s[68:71], s90 offen lds\n" \
  "s_add_u32 s90, s90, 16384\n" \
  "s_add_u32 m0, s86, 1024\n" \
  "s_nop 0\n" \
  "buffer_load_dwordx4 v7, s[68:71], s90 offen lds\n" \
  "s_add_u32 s90, s90, 16384\n" \
  "s_add_u32 m0, s86, 2048\n" \
  "s_nop 0\n" \
  "buffer_load_dwordx4 v7, s[68:71], s90 offen lds\n" \
  "s_add_u32 s90, s90, 16384\n" \
  "s_add_u32 m0, s86, 3072\n" \
  "s_nop 0\n" \
  "buffer_load_dwordx4 v7, s[68:71], s90 offen lds\n" \
  "s_add_u32 s90, s90, 16384\n" \
  "s_add_u32 m0, s86, 4096\n" \
  "s_nop 0\n" \
  "buffer_load_dwordx4 v7, s[68:71], s90 offen lds\n" \
  "s_add_u32 s90, s90, 16384\n" \
  "s_add_u32 m0, s86, 5120\n" \
  "s_nop 0\n" \
  "buffer_load_dwordx4 v7, s[68:71], s90 offen lds\n" \
  "s_add_u32 s90, s90, 16384\n" \
  "s_add_u32 m0, s86, 6144\n" \
  "s_nop 0\n" \
  "buffer_load_dwordx4 v7, s[68:71], s90 offen lds\n" \
  "s_add_u32 s90, s90, 16384\n" \
  "s_add_u32 m0, s86, 7168\n" \
  "s_nop 0\n" \
  "buffer_load_dwordx4 v7, s[68:71], s90 offen lds\n" \
  "s_add_u32 s90, s90, 16384\n" \
  "s_add_u32 m0, s86, 8192\n" \
  "s_nop 0\n" \
  "buffer_load_dwordx4 v7, s[68:71], s90 offen lds\n" \
  "s_add_u32 s90, s90, 16384\n" \
  "s_add_u32 m0, s86, 9216\n" \
  "s_nop 0\n" \
  "buffer_load_dwordx4 v7, s[68:71], s90 offen lds\n" \
  "s_add_u32 s90, s90, 16384\n" \
  "s_add_u32 m0, s86, 10240\n" \
  "s_nop 0\n" \
  "buffer_load_dwordx4 v7, s[68:71], s90 offen lds\n" \
  "s_add_u32 s90, s90, 16384\n" \
  "s_add_u32 m0, s86, 11264\n" \
  "s_nop 0\n" \
  "buffer_load_dwordx4 v7, s[68:71], s90 offen lds\n" \
  "s_add_u32 s90, s90, 16384\n" \
  "s_add_u32 m0, s86, 12288\n" \
  "s_nop 0\n" \
  "buffer_load_dwordx4 v7, s[68:71], s90 offen lds\n" \
  "s_add_u32 s90, s90, 16384\n" \
  "s_add_u32 m0, s86, 13312\n" \
  "s_nop 0\n" \
  "buffer_load_dwordx4 v7, s[68:71], s90 offen lds\n" \
  "s_add_u32 s90, s90, 16384\n" \
  "s_add_u32 m0, s86, 14336\n" \
  "s_nop 0\n" \
  "buffer_load_dwordx4 v7, s[68:71], s90 offen lds\n" \
  "s_add_u32 s90, s90, 16384\n" \
  "s_add_u32 m0, s86, 15360\n" \
  "s_nop 0\n" \
  "buffer_load_dwordx4 v7, s[68:71], s90 offen lds\n" \
  "s_add_u32 s90, s90, 16384\n" \
  "s_waitcnt vmcnt(16)\n" \
  "ds_read_b64_tr_b16 a[192:193], v16 offset:16384\n" \
  "ds_read_b64_tr_b16 a[194:195], v16 offset:18432\n" \
  "ds_read_b64_tr_b16 a[196:197], v17 offset:16384\n" \
  "ds_read_b64_tr_b16 a[198:199], v17 offset:18432\n" \
  "ds_read_b64_tr_b16 a[200:201], v18 offset:16384\n" \
  "ds_read_b64_tr_b16 a[202:203], v18 offset:18432\n" \
  "ds_read_b64_tr_b16 a[204:205], v19 offset:16384\n" \
  "ds_read_b64_tr_b16 a[206:207], v19 offset:18432\n" \
  "ds_read_b64_tr_b16 a[208:209], v16 offset:20480\n" \
  "ds_read_b64_tr_b16 a[210:211], v16 offset:22528\n" \
  "ds_read_b64_tr_b16 a[212:213], v17 offset:20480\n" \
  "ds_read_b64_tr_b16 a[214:215], v17 offset:22528\n" \
  "ds_read_b64_tr_b16 a[216:217], v18 offset:20480\n" \
  "ds_read_b64_tr_b16 a[218:219], v18 offset:22528\n" \
  "ds_read_b64_tr_b16 a[220:221], v19 offset:20480\n" \
  "ds_read_b64_tr_b16 a[222:223], v19 offset:22528\n" \
  "ds_read_b64_tr_b16 a[224:225], v16 offset:24576\n" \
  "ds_read_b64_tr_b16 a[226:227], v16 offset:26624\n" \
  "ds_read_b64_tr_b16 a[228:229], v17 offset:24576\n" \
  "ds_read_b64_tr_b16 a[230:231], v17 offset:26624\n" \
  "ds_read_b64_tr_b16 a[232:233], v18 offset:24576\n" \
  "ds_read_b64_tr_b16 a[234:235], v18 offset:26624\n" \
  "ds_read_b64_tr_b16 a[236:237], v19 offset:24576\n" \
  "ds_read_b64_tr_b16 a[238:239], v19 offset:26624\n" \
  "ds_read_b64_tr_b16 a[240:241], v16 offset:28672\n" \
  "ds_read_b64_tr_b16 a[242:243], v16 offset:30720\n" \
  "ds_read_b64_tr_b16 a[244:245], v17 offset:28672\n" \
  "ds_read_b64_tr_b16 a[246:247], v17 offset:30720\n" \
  "ds_read_b64_tr_b16 a[248:249], v18 offset:28672\n" \
  "ds_read_b64_tr_b16 a[250:251], v18 offset:30720\n" \
  "ds_read_b64_tr_b16 a[252:253], v19 offset:28672\n" \
  "ds_read_b64_tr_b16 a[254:255], v19 offset:30720\n" \
  "s_waitcnt lgkmcnt(0)\n" \
  "s_add_u32 m0, s86, 16384\n" \
  "s_nop 0\n" \
  "buffer_load_dwordx4 v7, s[68:71], s90 offen lds\n" \
  "s_add_u32 s90, s90, 16384\n" \
  "s_add_u32 m0, s86, 17408\n" \
  "s_nop 0\n" \
  "buffer_load_dwordx4 v7, s[68:71], s90 offen lds\n" \
  "s_add_u32 s90, s90, 16384\n" \
  "s_add_u32 m0, s86, 18432\n" \
  "s_nop 0\n" \
  "buffer_load_dwordx4 v7, s[68:71], s90 offen lds\n" \
  "s_add_u32 s90, s90, 16384\n" \
  "s_add_u32 m0, s86, 19456\n" \
  "s_nop 0\n" \
  "buffer_load_dwordx4 v7, s[68:71], s90 offen lds\n" \
  "s_add_u32 s90, s90, 16384\n" \
  "s_add_u32 m0, s86, 20480\n" \
  "s_nop 0\n" \
  "buffer_load_dwordx4 v7, s[68:71], s90 offen lds\n" \
  "s_add_u32 s90, s90, 16384\n" \
  "s_add_u32 m0, s86, 21504\n" \
  "s_nop 0\n" \
  "buffer_load_dwordx4 v7, s[68:71], s90 offen lds\n" \
  "s_add_u32 s90, s90, 16384\n" \
  "s_add_u32 m0, s86, 22528\n" \
  "s_nop 0\n" \
  "buffer_load_dwordx4 v7, s[68:71], s90 offen lds\n" \
  "s_add_u32 s90, s90, 16384\n" \
  "s_add_u32 m0, s86, 23552\n" \
  "s_nop 0\n" \
  "buffer_load_dwordx4 v7, s[68:71], s90 offen lds\n" \
  "s_add_u32 s90, s90, 16384\n" \
  "s_add_u32 m0, s86, 24576\n" \
  "s_nop 0\n" \
  "buffer_load_dwordx4 v7, s[68:71], s90 offen lds\n" \
  "s_add_u32 s90, s90, 16384\n" \
  "s_add_u32 m0, s86, 25600\n" \
  "s_nop 0\n" \
  "buffer_load_dwordx4 v7, s[68:71], s90 offen lds\n" \
  "s_add_u32 s90, s90, 16384\n" \
  "s_add_u32 m0, s86, 26624\n" \
  "s_nop 0\n" \
  "buffer_load_dwordx4 v7, s[68:71], s90 offen lds\n" \
  "s_add_u32 s90, s90, 16384\n" \
  "s_add_u32 m0, s86, 27648\n" \
  "s_nop 0\n" \
  "buffer_load_dwordx4 v7, s[68:71], s90 offen lds\n" \
  "s_add_u32 s90, s90, 16384\n" \
  "s_add_u32 m0, s86, 28672\n" \
  "s_nop 0\n" \
  "buffer_load_dwordx4 v7, s[68:71], s90 offen lds\n" \
  "s_add_u32 s90, s90, 16384\n" \
  "s_add_u32 m0, s86, 29696\n" \
  "s_nop 0\n" \
  "buffer_load_dwordx4 v7, s[68:71], s90 offen lds\n" \
  "s_add_u32 s90, s90, 16384\n" \
  "s_add_u32 m0, s86, 30720\n" \
  "s_nop 0\n" \
  "buffer_load_dwordx4 v7, s[68:71], s90 offen lds\n" \
  "s_add_u32 s90, s90, 16384\n" \
  "s_add_u32 m0, s86, 31744\n" \
  "s_nop 0\n" \
  "buffer_load_dwordx4 v7, s[68:71], s90 offen lds\n" \
  "s_add_u32 s90, s90, 16384\n" \
  "s_mov_b32 s92, 1\n" \
  "v_xor_b32 v7, 64, v12\n" \
  "LP0_%=:\n" \
  "buffer_load_dword v9, v8, s[80:83], 0 offen sc1\n" \
  "s_waitcnt vmcnt(0)\n" \
  "v_cmp_gt_u32 vcc, s92, v9\n" \
  "s_cbranch_vccz LP1_%=\n" \
  "s_sleep 8\n" \
  "s_add_u32 s91, s91, 1\n" \
  "s_cmp_lt_u32 s91, " STR(SPIN_CAP) "\n" \
  "s_cbranch_scc1 LP0_%=\n" \
  "v_mov_b32 v5, " STR(CW_DEAD) "*4\n" \
  "v_mov_b32 v15, 1\n" \
  "buffer_store_dword v15, v5, s[80:83], 0 offen sc1\n" \
  "s_branch LP1_%=\n" \
  "LP1_%=:\n" \
  "s_add_u32 s84, s87, 0\n" \
  "buffer_load_dwordx4 v[64:67], v6, s[72:75], s84 offen sc1\n" \
  "s_add_u32 s84, s87, 1024\n" \
  "buffer_load_dwordx4 v[68:71], v6, s[72:75], s84 offen sc1\n" \
  "s_add_u32 s84, s87, 2048\n" \
  "buffer_load_dwordx4 v[72:75], v6, s[72:75], s84 offen sc1\n" \
  "s_add_u32 s84, s87, 49152\n" \
  "buffer_load_dwordx4 v[76:79], v6, s[72:75], s84 offen sc1\n" \
  "s_add_u32 s84, s87, 3072\n" \
  "buffer_load_dwordx4 v[80:83], v6, s[72:75], s84 offen sc1\n" \
  "s_add_u32 s84, s87, 4096\n" \
  "buffer_load_dwordx4 v[84:87], v6, s[72:75], s84 offen sc1\n" \
  "s_add_u32 s84, s87, 5120\n" \
  "buffer_load_dwordx4 v[88:91], v6, s[72:75], s84 offen sc1\n" \
  "s_add_u32 s84, s87, 50176\n" \
  "buffer_load_dwordx4 v[92:95], v6, s[72:75], s84 offen sc1\n" \
  "s_add_u32 s84, s87, 6144\n" \
  "buffer_load_dwordx4 v[96:99], v6, s[72:75], s84 offen sc1\n" \
  "s_add_u32 s84, s87, 7168\n" \
  "buffer_load_dwordx4 v[100:103], v6, s[72:75], s84 offen sc1\n" \
  "s_add_u32 s84, s87, 16384\n" \
  "buffer_load_dwordx4 v[104:107], v6, s[72:75], s84 offen sc1\n" \
  "s_add_u32 s84, s87, 51200\n" \
  "buffer_load_dwordx4 v[108:111], v6, s[72:75], s84 offen sc1\n" \
  "s_add_u32 s84, s87, 17408\n" \
  "buffer_load_dwordx4 v[112:115], v6, s[72:75], s84 offen sc1\n" \
  "s_add_u32 s84, s87, 18432\n" \
  "buffer_load_dwordx4 v[116:119], v6, s[72:75], s84 offen sc1\n" \
  "s_add_u32 s84, s87, 19456\n" \
  "buffer_load_dwordx4 v[120:123], v6, s[72:75], s84 offen sc1\n" \
  "s_add_u32 s84, s87, 52224\n" \
  "buffer_load_dwordx4 v[124:127], v6, s[72:75], s84 offen sc1\n" \
  "v_or_b32 v10, 0x80000000, v10\n" \
  "s_sub_u32 s88, s88, 131072\n" \
  ".p2align 6\n" \
  "LX_%=:\n" \
  "s_waitcnt vmcnt(15)\n" \
  "v_mfma_f32_16x16x32_bf16 v[20:23], v[128:131], v[64:67], 0\n" \
  "v_mfma_f32_16x16x32_bf16 v[24:27], v[132:135], v[64:67], 0\n" \
  "v_mfma_f32_16x16x32_bf16 v[28:31], v[136:139], v[64:67], 0\n" \
  "v_mfma_f32_16x16x32_bf16 v[32:35], v[140:143], v[64:67], 0\n" \
  "s_add_u32 s84, s87, 20480\n" \
  "buffer_load_dwordx4 v[64:67], v6, s[72:75], s84 offen sc1\n" \
  "s_waitcnt vmcnt(15)\n" \
  "v_mfma_f32_16x16x32_bf16 v[20:23], v[144:147], v[68:71], v[20:23]\n" \
  "v_mfma_f32_16x16x32_bf16 v[24:27], v[148:151], v[68:71], v[24:27]\n" \
  "v_mfma_f32_16x16x32_bf16 v[28:31], v[152:155], v[68:71], v[28:31]\n" \
  "v_mfma_f32_16x16x32_bf16 v[32:35], v[156:159], v[68:71], v[32:35]\n" \
  "s_add_u32 s84, s87, 21504\n" \
  "buffer_load_dwordx4 v[68:71], v6, s[72:75], s84 offen sc1\n" \
  "s_waitcnt vmcnt(15)\n" \
  "v_mfma_f32_16x16x32_bf16 v[20:23], v[160:163], v[72:75], v[20:23]\n" \
  "ds_read_b64_tr_b16 v[40:41], v16 offset:0\n" \
  "ds_read_b64_tr_b16 v[42:43], v16 offset:2048\n" \
  "v_mfma_f32_16x16x32_bf16 v[24:27], v[164:167], v[72:75], v[24:27]\n" \
  "ds_read_b64_tr_b16 v[44:45], v17 offset:0\n" \
  "ds_read_b64_tr_b16 v[46:47], v17 offset:2048\n" \
  "v_mfma_f32_16x16x32_bf16 v[28:31], v[168:171], v[72:75], v[28:31]\n" \
  "ds_read_b64_tr_b16 v[48:49], v18 offset:0\n" \
  "ds_read_b64_tr_b16 v[50:51], v18 offset:2048\n" \
  "v_mfma_f32_16x16x32_bf16 v[32:35], v[172:175], v[72:75], v[32:35]\n" \
  "ds_read_b64_tr_b16 v[52:53], v19 offset:0\n" \
  "ds_read_b64_tr_b16 v[54:55], v19 offset:2048\n" \
  "s_add_u32 s84, s87, 22528\n" \
  "buffer_load_dwordx4 v[72:75], v6, s[72:75], s84 offen sc1\n" \
  "s_waitcnt vmcnt(15)\n" \
  "s_waitcnt lgkmcnt(6)\n" \
  "v_mfma_f32_16x16x32_bf16 v[20:23], v[40:43], v[76:79], v[20:23]\n" \
  "s_waitcnt lgkmcnt(4)\n" \
  "v_mfma_f32_16x16x32_bf16 v[24:27], v[44:47], v[76:79], v[24:27]\n" \
  "s_waitcnt lgkmcnt(2)\n" \
  "v_mfma_f32_16x16x32_bf16 v[28:31], v[48:51], v[76:79], v[28:31]\n" \
  "s_waitcnt lgkmcnt(0)\n" \
  "v_mfma_f32_16x16x32_bf16 v[32:35], v[52:55], v[76:79], v[32:35]\n" \
  "s_add_u32 s84, s87, 53248\n" \
  "buffer_load_dwordx4 v[76:79], v6, s[72:75], s84 offen sc1\n" \
  "s_waitcnt lgkmcnt(0)\n" \
  "s_barrier\n" \
  "ds_read_b128 v[52:55], v11 offset:0\n" \
  "ds_read_b128 v[56:59], v11 offset:4096\n" \
  "ds_read_b128 v[60:63], v11 offset:8192\n" \
  "ds_read_b128 v[36:39], v11 offset:12288\n" \
  "s_waitcnt vmcnt(15)\n" \
  "v_mfma_f32_16x16x32_bf16 v[20:23], v[176:179], v[80:83], v[20:23]\n" \
  "v_mfma_f32_16x16x32_bf16 v[24:27], v[180:183], v[80:83], v[24:27]\n" \
  "v_mfma_f32_16x16x32_bf16 v[28:31], v[184:187], v[80:83], v[28:31]\n" \
  "v_mfma_f32_16x16x32_bf16 v[32:35], v[188:191], v[80:83], v[32:35]\n" \
  "s_add_u32 s84, s87, 23552\n" \
  "buffer_load_dwordx4 v[80:83], v6, s[72:75], s84 offen sc1\n" \
  "s_waitcnt lgkmcnt(0)\n" \
  "s_waitcnt vmcnt(15)\n" \
  "v_mfma_f32_16x16x32_bf16 v[20:23], v[192:195], v[84:87], v[20:23]\n" \
  "v_add_f32 v36, v36, v52\n" \
  "v_add_f32 v37, v37, v53\n" \
  "v_add_f32 v38, v38, v54\n" \
  "v_add_f32 v39, v39, v55\n" \
  "v_mfma_f32_16x16x32_bf16 v[24:27], v[196:199], v[84:87], v[24:27]\n" \
  "v_add_f32 v56, v56, v60\n" \
  "v_add_f32 v57, v57, v61\n" \
  "v_add_f32 v58, v58, v62\n" \
  "v_add_f32 v59, v59, v63\n" \
  "v_mfma_f32_16x16x32_bf16 v[28:31], v[200:203], v[84:87], v[28:31]\n" \
  "v_add_f32 v36, v36, v56\n" \
  "v_add_f32 v37, v37, v57\n" \
  "v_add_f32 v38, v38, v58\n" \
  "v_add_f32 v39, v39, v59\n" \
  "v_mfma_f32_16x16x32_bf16 v[32:35], v[204:207], v[84:87], v[32:35]\n" \
  "v_cvt_pk_bf16_f32 v36, v36, v37\n" \
  "v_cvt_pk_bf16_f32 v37, v38, v39\n" \
  "buffer_store_dwordx2 v[36:37], v10, s[76:79], s88 offen\n" \
  "s_add_u32 s88, s88, 131072\n" \
  "v_and_b32 v10, 0x7fffffff, v10\n" \
  "s_add_u32 s84, s87, 32768\n" \
  "buffer_load_dwordx4 v[84:87], v6, s[72:75], s84 offen sc1\n" \
  "s_waitcnt vmcnt(16)\n" \
  "v_mfma_f32_16x16x32_bf16 v[20:23], v[208:211], v[88:91], v[20:23]\n" \
  "ds_read_b64_tr_b16 v[40:41], v16 offset:4096\n" \
  "ds_read_b64_tr_b16 v[42:43], v16 offset:6144\n" \
  "v_mfma_f32_16x16x32_bf16 v[24:27], v[212:215], v[88:91], v[24:27]\n" \
  "ds_read_b64_tr_b16 v[44:45], v17 offset:4096\n" \
  "ds_read_b64_tr_b16 v[46:47], v17 offset:6144\n" \
  "v_mfma_f32_16x16x32_bf16 v[28:31], v[216:219], v[88:91], v[28:31]\n" \
  "ds_read_b64_tr_b16 v[48:49], v18 offset:4096\n" \
  "ds_read_b64_tr_b16 v[50:51], v18 offset:6144\n" \
  "v_mfma_f32_16x16x32_bf16 v[32:35], v[220:223], v[88:91], v[32:35]\n" \
  "ds_read_b64_tr_b16 v[52:53], v19 offset:4096\n" \
  "ds_read_b64_tr_b16 v[54:55], v19 offset:6144\n" \
  "s_add_u32 s84, s87, 33792\n" \
  "buffer_load_dwordx4 v[88:91], v6, s[72:75], s84 offen sc1\n" \
  "s_waitcnt vmcnt(16)\n" \
  "s_waitcnt lgkmcnt(6)\n" \
  "v_mfma_f32_16x16x32_bf16 v[20:23], v[40:43], v[92:95], v[20:23]\n" \
  "s_waitcnt lgkmcnt(4)\n" \
  "v_mfma_f32_16x16x32_bf16 v[24:27], v[44:47], v[92:95], v[24:27]\n" \
  "s_waitcnt lgkmcnt(2)\n" \
  "v_mfma_f32_16x16x32_bf16 v[28:31], v[48:51], v[92:95], v[28:31]\n" \
  "s_waitcnt lgkmcnt(0)\n" \
  "v_mfma_f32_16x16x32_bf16 v[32:35], v[52:55], v[92:95], v[32:35]\n" \
  "s_add_u32 s84, s87, 54272\n" \
  "buffer_load_dwordx4 v[92:95], v6, s[72:75], s84 offen sc1\n" \
  "s_waitcnt vmcnt(16)\n" \
  "v_mfma_f32_16x16x32_bf16 v[20:23], v[224:227], v[96:99], v[20:23]\n" \
  "buffer_load_dword v9, v8, s[80:83], 0 offen sc1\n" \
  "v_mfma_f32_16x16x32_bf16 v[24:27], v[228:231], v[96:99], v[24:27]\n" \
  "v_xor_b32 v11, 0x4000, v11\n" \
  "v_xor_b32 v7, 0x4000, v7\n" \
  "v_mfma_f32_16x16x32_bf16 v[28:31], v[232:235], v[96:99], v[28:31]\n" \
  "v_xor_b32 v12, 0x4000, v12\n" \
  "v_xor_b32 v13, 0x4000, v13\n" \
  "v_xor_b32 v14, 0x4000, v14\n" \
  "v_mfma_f32_16x16x32_bf16 v[32:35], v[236:239], v[96:99], v[32:35]\n" \
  "s_add_u32 s84, s87, 34816\n" \
  "buffer_load_dwordx4 v[96:99], v6, s[72:75], s84 offen sc1\n" \
  "s_waitcnt vmcnt(17)\n" \
  "v_mfma_f32_16x16x32_bf16 v[20:23], v[240:243], v[100:103], v[20:23]\n" \
  "v_mfma_f32_16x16x32_bf16 v[24:27], v[244:247], v[100:103], v[24:27]\n" \
  "v_mfma_f32_16x16x32_bf16 v[28:31], v[248:251], v[100:103], v[28:31]\n" \
  "v_mfma_f32_16x16x32_bf16 v[32:35], v[252:255], v[100:103], v[32:35]\n" \
  "s_add_u32 s84, s87, 35840\n" \
  "buffer_load_dwordx4 v[100:103], v6, s[72:75], s84 offen sc1\n" \
  "s_waitcnt vmcnt(17)\n" \
  "v_mfma_f32_16x16x32_bf16 v[20:23], a[0:3], v[104:107], v[20:23]\n" \
  "ds_read_b64_tr_b16 v[40:41], v16 offset:8192\n" \
  "ds_read_b64_tr_b16 v[42:43], v16 offset:10240\n" \
  "v_mfma_f32_16x16x32_bf16 v[24:27], a[4:7], v[104:107], v[24:27]\n" \
  "ds_read_b64_tr_b16 v[44:45], v17 offset:8192\n" \
  "ds_read_b64_tr_b16 v[46:47], v17 offset:10240\n" \
  "v_mfma_f32_16x16x32_bf16 v[28:31], a[8:11], v[104:107], v[28:31]\n" \
  "ds_read_b64_tr_b16 v[48:49], v18 offset:8192\n" \
  "ds_read_b64_tr_b16 v[50:51], v18 offset:10240\n" \
  "v_mfma_f32_16x16x32_bf16 v[32:35], a[12:15], v[104:107], v[32:35]\n" \
  "ds_read_b64_tr_b16 v[52:53], v19 offset:8192\n" \
  "ds_read_b64_tr_b16 v[54:55], v19 offset:10240\n" \
  "s_add_u32 s84, s87, 36864\n" \
  "buffer_load_dwordx4 v[104:107], v6, s[72:75], s84 offen sc1\n" \
  "s_waitcnt vmcnt(17)\n" \
  "s_waitcnt lgkmcnt(6)\n" \
  "v_mfma_f32_16x16x32_bf16 v[20:23], v[40:43], v[108:111], v[20:23]\n" \
  "s_waitcnt lgkmcnt(4)\n" \
  "v_mfma_f32_16x16x32_bf16 v[24:27], v[44:47], v[108:111], v[24:27]\n" \
  "s_waitcnt lgkmcnt(2)\n" \
  "v_mfma_f32_16x16x32_bf16 v[28:31], v[48:51], v[108:111], v[28:31]\n" \
  "s_waitcnt lgkmcnt(0)\n" \
  "v_mfma_f32_16x16x32_bf16 v[32:35], v[52:55], v[108:111], v[32:35]\n" \
  "s_add_u32 s84, s87, 55296\n" \
  "buffer_load_dwordx4 v[108:111], v6, s[72:75], s84 offen sc1\n" \
  "s_waitcnt vmcnt(17)\n" \
  "v_mfma_f32_16x16x32_bf16 v[20:23], a[16:19], v[112:115], v[20:23]\n" \
  "v_mfma_f32_16x16x32_bf16 v[24:27], a[20:23], v[112:115], v[24:27]\n" \
  "v_mfma_f32_16x16x32_bf16 v[28:31], a[24:27], v[112:115], v[28:31]\n" \
  "v_mfma_f32_16x16x32_bf16 v[32:35], a[28:31], v[112:115], v[32:35]\n" \
  "s_add_u32 s84, s87, 37888\n" \
  "buffer_load_dwordx4 v[112:115], v6, s[72:75], s84 offen sc1\n" \
  "s_waitcnt vmcnt(17)\n" \
  "v_mfma_f32_16x16x32_bf16 v[20:23], a[32:35], v[116:119], v[20:23]\n" \
  "v_mfma_f32_16x16x32_bf16 v[24:27], a[36:39], v[116:119], v[24:27]\n" \
  "v_mfma_f32_16x16x32_bf16 v[28:31], a[40:43], v[116:119], v[28:31]\n" \
  "v_mfma_f32_16x16x32_bf16 v[32:35], a[44:47], v[116:119], v[32:35]\n" \
  "s_add_u32 s84, s87, 38912\n" \
  "buffer_load_dwordx4 v[116:119], v6, s[72:75], s84 offen sc1\n" \
  "s_waitcnt vmcnt(17)\n" \
  "v_mfma_f32_16x16x32_bf16 v[20:23], a[48:51], v[120:123], v[20:23]\n" \
  "ds_read_b64_tr_b16 v[40:41], v16 offset:12288\n" \
  "ds_read_b64_tr_b16 v[42:43], v16 offset:14336\n" \
  "v_mfma_f32_16x16x32_bf16 v[24:27], a[52:55], v[120:123], v[24:27]\n" \
  "ds_read_b64_tr_b16 v[44:45], v17 offset:12288\n" \
  "ds_read_b64_tr_b16 v[46:47], v17 offset:14336\n" \
  "v_mfma_f32_16x16x32_bf16 v[28:31], a[56:59], v[120:123], v[28:31]\n" \
  "ds_read_b64_tr_b16 v[48:49], v18 offset:12288\n" \
  "ds_read_b64_tr_b16 v[50:51], v18 offset:14336\n" \
  "v_mfma_f32_16x16x32_bf16 v[32:35], a[60:63], v[120:123], v[32:35]\n" \
  "ds_read_b64_tr_b16 v[52:53], v19 offset:12288\n" \
  "ds_read_b64_tr_b16 v[54:55], v19 offset:14336\n" \
  "s_add_u32 s84, s87, 39936\n" \
  "buffer_load_dwordx4 v[120:123], v6, s[72:75], s84 offen sc1\n" \
  "s_waitcnt vmcnt(17)\n" \
  "s_waitcnt lgkmcnt(6)\n" \
  "v_mfma_f32_16x16x32_bf16 v[20:23], v[40:43], v[124:127], v[20:23]\n" \
  "s_waitcnt lgkmcnt(4)\n" \
  "v_mfma_f32_16x16x32_bf16 v[24:27], v[44:47], v[124:127], v[24:27]\n" \
  "s_waitcnt lgkmcnt(2)\n" \
  "v_mfma_f32_16x16x32_bf16 v[28:31], v[48:51], v[124:127], v[28:31]\n" \
  "s_waitcnt lgkmcnt(0)\n" \
  "v_mfma_f32_16x16x32_bf16 v[32:35], v[52:55], v[124:127], v[32:35]\n" \
  "s_add_u32 s84, s87, 56320\n" \
  "buffer_load_dwordx4 v[124:127], v6, s[72:75], s84 offen sc1\n" \
  "s_waitcnt vmcnt(8)\n" \
  "s_add_u32 s92, s89, 2\n" \
  "s_min_u32 s92, s92, 64\n" \
  "v_cmp_gt_u32 vcc, s92, v9\n" \
  "s_sub_u32 s85, s92, 1\n" \
  "s_lshr_b32 s84, s85, 2\n" \
  "s_xnor_b32 s84, s84, %7\n" \
  "s_and_b32 s84, s84, 1\n" \
  "s_lshl_b32 s84, s84, 13\n" \
  "s_lshl_b32 s87, s85, 19\n" \
  "s_or_b32 s87, s87, s84\n" \
  "s_add_u32 s87, s87, s96\n" \
  "s_cbranch_vccnz LP2_%=\n" \
  "LP3_%=:\n" \
  "s_waitcnt vmcnt(17)\n" \
  "v_mfma_f32_16x16x32_bf16 v[20:23], a[64:67], v[64:67], v[20:23]\n" \
  "v_mfma_f32_16x16x32_bf16 v[24:27], a[68:71], v[64:67], v[24:27]\n" \
  "v_mfma_f32_16x16x32_bf16 v[28:31], a[72:75], v[64:67], v[28:31]\n" \
  "v_mfma_f32_16x16x32_bf16 v[32:35], a[76:79], v[64:67], v[32:35]\n" \
  "s_add_u32 s84, s87, 0\n" \
  "buffer_load_dwordx4 v[64:67], v6, s[72:75], s84 offen sc1\n" \
  "s_waitcnt vmcnt(17)\n" \
  "v_mfma_f32_16x16x32_bf16 v[20:23], a[80:83], v[68:71], v[20:23]\n" \
  "v_mfma_f32_16x16x32_bf16 v[24:27], a[84:87], v[68:71], v[24:27]\n" \
  "v_mfma_f32_16x16x32_bf16 v[28:31], a[88:91], v[68:71], v[28:31]\n" \
  "v_mfma_f32_16x16x32_bf16 v[32:35], a[92:95], v[68:71], v[32:35]\n" \
  "s_add_u32 s84, s87, 1024\n" \
  "buffer_load_dwordx4 v[68:71], v6, s[72:75], s84 offen sc1\n" \
  "s_waitcnt vmcnt(17)\n" \
  "v_mfma_f32_16x16x32_bf16 v[20:23], a[96:99], v[72:75], v[20:23]\n" \
  "ds_read_b64_tr_b16 v[40:41], v16 offset:16384\n" \
  "ds_read_b64_tr_b16 v[42:43], v16 offset:18432\n" \
  "v_mfma_f32_16x16x32_bf16 v[24:27], a[100:103], v[72:75], v[24:27]\n" \
  "ds_read_b64_tr_b16 v[44:45], v17 offset:16384\n" \
  "ds_read_b64_tr_b16 v[46:47], v17 offset:18432\n" \
  "v_mfma_f32_16x16x32_bf16 v[28:31], a[104:107], v[72:75], v[28:31]\n" \
  "ds_read_b64_tr_b16 v[48:49], v18 offset:16384\n" \
  "ds_read_b64_tr_b16 v[50:51], v18 offset:18432\n" \
  "v_mfma_f32_16x16x32_bf16 v[32:35], a[108:111], v[72:75], v[32:35]\n" \
  "ds_read_b64_tr_b16 v[52:53], v19 offset:16384\n" \
  "ds_read_b64_tr_b16 v[54:55], v19 offset:18432\n" \
  "s_add_u32 s84, s87, 2048\n" \
  "buffer_load_dwordx4 v[72:75], v6, s[72:75], s84 offen sc1\n" \
  "s_waitcnt vmcnt(17)\n" \
  "s_waitcnt lgkmcnt(6)\n" \
  "v_mfma_f32_16x16x32_bf16 v[20:23], v[40:43], v[76:79], v[20:23]\n" \
  "s_waitcnt lgkmcnt(4)\n" \
  "v_mfma_f32_16x16x32_bf16 v[24:27], v[44:47], v[76:79], v[24:27]\n" \
  "s_waitcnt lgkmcnt(2)\n" \
  "v_mfma_f32_16x16x32_bf16 v[28:31], v[48:51], v[76:79], v[28:31]\n" \
  "s_waitcnt lgkmcnt(0)\n" \
  "v_mfma_f32_16x16x32_bf16 v[32:35], v[52:55], v[76:79], v[32:35]\n" \
  "s_add_u32 s84, s87, 49152\n" \
  "buffer_load_dwordx4 v[76:79], v6, s[72:75], s84 offen sc1\n" \
  "s_waitcnt vmcnt(17)\n" \
  "v_mfma_f32_16x16x32_bf16 v[20:23], a[112:115], v[80:83], v[20:23]\n" \
  "v_mfma_f32_16x16x32_bf16 v[24:27], a[116:119], v[80:83], v[24:27]\n" \
  "v_mfma_f32_16x16x32_bf16 v[28:31], a[120:123], v[80:83], v[28:31]\n" \
  "v_mfma_f32_16x16x32_bf16 v[32:35], a[124:127], v[80:83], v[32:35]\n" \
  "s_add_u32 s84, s87, 3072\n" \
  "buffer_load_dwordx4 v[80:83], v6, s[72:75], s84 offen sc1\n" \
  "s_waitcnt vmcnt(16)\n" \
  "v_mfma_f32_16x16x32_bf16 v[20:23], a[128:131], v[84:87], v[20:23]\n" \
  "v_mfma_f32_16x16x32_bf16 v[24:27], a[132:135], v[84:87], v[24:27]\n" \
  "v_mfma_f32_16x16x32_bf16 v[28:31], a[136:139], v[84:87], v[28:31]\n" \
  "v_mfma_f32_16x16x32_bf16 v[32:35], a[140:143], v[84:87], v[32:35]\n" \
  "s_add_u32 s84, s87, 4096\n" \
  "buffer_load_dwordx4 v[84:87], v6, s[72:75], s84 offen sc1\n" \
  "s_waitcnt vmcnt(16)\n" \
  "v_mfma_f32_16x16x32_bf16 v[20:23], a[144:147], v[88:91], v[20:23]\n" \
  "ds_read_b64_tr_b16 v[40:41], v16 offset:20480\n" \
  "ds_read_b64_tr_b16 v[42:43], v16 offset:22528\n" \
  "v_mfma_f32_16x16x32_bf16 v[24:27], a[148:151], v[88:91], v[24:27]\n" \
  "ds_read_b64_tr_b16 v[44:45], v17 offset:20480\n" \
  "ds_read_b64_tr_b16 v[46:47], v17 offset:22528\n" \
  "v_mfma_f32_16x16x32_bf16 v[28:31], a[152:155], v[88:91], v[28:31]\n" \
  "ds_read_b64_tr_b16 v[48:49], v18 offset:20480\n" \
  "ds_read_b64_tr_b16 v[50:51], v18 offset:22528\n" \
  "v_mfma_f32_16x16x32_bf16 v[32:35], a[156:159], v[88:91], v[32:35]\n" \
  "ds_read_b64_tr_b16 v[52:53], v19 offset:20480\n" \
  "ds_read_b64_tr_b16 v[54:55], v19 offset:22528\n" \
  "s_add_u32 s84, s87, 5120\n" \
  "buffer_load_dwordx4 v[88:91], v6, s[72:75], s84 offen sc1\n" \
  "s_waitcnt vmcnt(16)\n" \
  "s_waitcnt lgkmcnt(6)\n" \
  "v_mfma_f32_16x16x32_bf16 v[20:23], v[40:43], v[92:95], v[20:23]\n" \
  "s_waitcnt lgkmcnt(4)\n" \
  "v_mfma_f32_16x16x32_bf16 v[24:27], v[44:47], v[92:95], v[24:27]\n" \
  "s_waitcnt lgkmcnt(2)\n" \
  "v_mfma_f32_16x16x32_bf16 v[28:31], v[48:51], v[92:95], v[28:31]\n" \
  "s_waitcnt lgkmcnt(0)\n" \
  "v_mfma_f32_16x16x32_bf16 v[32:35], v[52:55], v[92:95], v[32:35]\n" \
  "s_add_u32 s84, s87, 50176\n" \
  "buffer_load_dwordx4 v[92:95], v6, s[72:75], s84 offen sc1\n" \
  "s_waitcnt vmcnt(15)\n" \
  "v_mfma_f32_16x16x32_bf16 v[20:23], a[160:163], v[96:99], v[20:23]\n" \
  "v_mfma_f32_16x16x32_bf16 v[24:27], a[164:167], v[96:99], v[24:27]\n" \
  "v_mfma_f32_16x16x32_bf16 v[28:31], a[168:171], v[96:99], v[28:31]\n" \
  "v_mfma_f32_16x16x32_bf16 v[32:35], a[172:175], v[96:99], v[32:35]\n" \
  "s_add_u32 s84, s87, 6144\n" \
  "buffer_load_dwordx4 v[96:99], v6, s[72:75], s84 offen sc1\n" \
  "s_waitcnt vmcnt(15)\n" \
  "v_mfma_f32_16x16x32_bf16 v[20:23], a[176:179], v[100:103], v[20:23]\n" \
  "v_mfma_f32_16x16x32_bf16 v[24:27], a[180:183], v[100:103], v[24:27]\n" \
  "v_mfma_f32_16x16x32_bf16 v[28:31], a[184:187], v[100:103], v[28:31]\n" \
  "v_mfma_f32_16x16x32_bf16 v[32:35], a[188:191], v[100:103], v[32:35]\n" \
  "s_add_u32 s84, s87, 7168\n" \
  "buffer_load_dwordx4 v[100:103], v6, s[72:75], s84 offen sc1\n" \
  "s_waitcnt vmcnt(15)\n" \
  "v_mfma_f32_16x16x32_bf16 v[20:23], a[192:195], v[104:107], v[20:23]\n" \
  "ds_read_b64_tr_b16 v[40:41], v16 offset:24576\n" \
  "ds_read_b64_tr_b16 v[42:43], v16 offset:26624\n" \
  "v_mfma_f32_16x16x32_bf16 v[24:27], a[196:199], v[104:107], v[24:27]\n" \
  "ds_read_b64_tr_b16 v[44:45], v17 offset:24576\n" \
  "ds_read_b64_tr_b16 v[46:47], v17 offset:26624\n" \
  "v_mfma_f32_16x16x32_bf16 v[28:31], a[200:203], v[104:107], v[28:31]\n" \
  "ds_read_b64_tr_b16 v[48:49], v18 offset:24576\n" \
  "ds_read_b64_tr_b16 v[50:51], v18 offset:26624\n" \
  "v_mfma_f32_16x16x32_bf16 v[32:35], a[204:207], v[104:107], v[32:35]\n" \
  "ds_read_b64_tr_b16 v[52:53], v19 offset:24576\n" \
  "ds_read_b64_tr_b16 v[54:55], v19 offset:26624\n" \
  "s_add_u32 s84, s87, 16384\n" \
  "buffer_load_dwordx4 v[104:107], v6, s[72:75], s84 offen sc1\n" \
  "s_waitcnt vmcnt(15)\n" \
  "s_waitcnt lgkmcnt(6)\n" \
  "v_mfma_f32_16x16x32_bf16 v[20:23], v[40:43], v[108:111], v[20:23]\n" \
  "s_waitcnt lgkmcnt(4)\n" \
  "v_mfma_f32_16x16x32_bf16 v[24:27], v[44:47], v[108:111], v[24:27]\n" \
  "s_waitcnt lgkmcnt(2)\n" \
  "v_mfma_f32_16x16x32_bf16 v[28:31], v[48:51], v[108:111], v[28:31]\n" \
  "s_waitcnt lgkmcnt(0)\n" \
  "v_mfma_f32_16x16x32_bf16 v[32:35], v[52:55], v[108:111], v[32:35]\n" \
  "s_add_u32 s84, s87, 51200\n" \
  "buffer_load_dwordx4 v[108:111], v6, s[72:75], s84 offen sc1\n" \
  "s_waitcnt vmcnt(15)\n" \
  "v_mfma_f32_16x16x32_bf16 v[20:23], a[208:211], v[112:115], v[20:23]\n" \
  "v_mfma_f32_16x16x32_bf16 v[24:27], a[212:215], v[112:115], v[24:27]\n" \
  "v_mfma_f32_16x16x32_bf16 v[28:31], a[216:219], v[112:115], v[28:31]\n" \
  "v_mfma_f32_16x16x32_bf16 v[32:35], a[220:223], v[112:115], v[32:35]\n" \
  "s_add_u32 s84, s87, 17408\n" \
  "buffer_load_dwordx4 v[112:115], v6, s[72:75], s84 offen sc1\n" \
  "s_waitcnt vmcnt(15)\n" \
  "v_mfma_f32_16x16x32_bf16 v[20:23], a[224:227], v[116:119], v[20:23]\n" \
  "v_mfma_f32_16x16x32_bf16 v[24:27], a[228:231], v[116:119], v[24:27]\n" \
  "v_mfma_f32_16x16x32_bf16 v[28:31], a[232:235], v[116:119], v[28:31]\n" \
  "v_mfma_f32_16x16x32_bf16 v[32:35], a[236:239], v[116:119], v[32:35]\n" \
  "s_add_u32 s84, s87, 18432\n" \
  "buffer_load_dwordx4 v[116:119], v6, s[72:75], s84 offen sc1\n" \
  "s_waitcnt vmcnt(15)\n" \
  "v_mfma_f32_16x16x32_bf16 v[20:23], a[240:243], v[120:123], v[20:23]\n" \
  "ds_read_b64_tr_b16 v[40:41], v16 offset:28672\n" \
  "ds_read_b64_tr_b16 v[42:43], v16 offset:30720\n" \
  "v_mfma_f32_16x16x32_bf16 v[24:27], a[244:247], v[120:123], v[24:27]\n" \
  "ds_read_b64_tr_b16 v[44:45], v17 offset:28672\n" \
  "ds_read_b64_tr_b16 v[46:47], v17 offset:30720\n" \
  "v_mfma_f32_16x16x32_bf16 v[28:31], a[248:251], v[120:123], v[28:31]\n" \
  "ds_read_b64_tr_b16 v[48:49], v18 offset:28672\n" \
  "ds_read_b64_tr_b16 v[50:51], v18 offset:30720\n" \
  "v_mfma_f32_16x16x32_bf16 v[32:35], a[252:255], v[120:123], v[32:35]\n" \
  "ds_read_b64_tr_b16 v[52:53], v19 offset:28672\n" \
  "ds_read_b64_tr_b16 v[54:55], v19 offset:30720\n" \
  "s_add_u32 s84, s87, 19456\n" \
  "buffer_load_dwordx4 v[120:123], v6, s[72:75], s84 offen sc1\n" \
  "s_waitcnt vmcnt(15)\n" \
  "s_waitcnt lgkmcnt(6)\n" \
  "v_mfma_f32_16x16x32_bf16 v[20:23], v[40:43], v[124:127], v[20:23]\n" \
  "s_waitcnt lgkmcnt(4)\n" \
  "v_mfma_f32_16x16x32_bf16 v[24:27], v[44:47], v[124:127], v[24:27]\n" \
  "s_waitcnt lgkmcnt(2)\n" \
  "v_mfma_f32_16x16x32_bf16 v[28:31], v[48:51], v[124:127], v[28:31]\n" \
  "s_waitcnt lgkmcnt(0)\n" \
  "v_mfma_f32_16x16x32_bf16 v[32:35], v[52:55], v[124:127], v[32:35]\n" \
  "s_add_u32 s84, s87, 52224\n" \
  "buffer_load_dwordx4 v[124:127], v6, s[72:75], s84 offen sc1\n" \
  "s_add_u32 s89, s89, 1\n" \
  "s_cmp_eq_u32 s89, 64\n" \
  "ds_write_b128 v7, v[20:23]\n" \
  "ds_write_b128 v12, v[24:27]\n" \
  "ds_write_b128 v13, v[28:31]\n" \
  "s_nop 0\n" \
  "ds_write_b128 v14, v[32:35]\n" \
  "s_cbranch_scc0 LX_%=\n" \
  "s_waitcnt lgkmcnt(0)\n" \
  "s_barrier\n" \
  "ds_read_b128 v[52:55], v11 offset:0\n" \
  "ds_read_b128 v[56:59], v11 offset:4096\n" \
  "ds_read_b128 v[60:63], v11 offset:8192\n" \
  "ds_read_b128 v[36:39], v11 offset:12288\n" \
  "s_waitcnt lgkmcnt(0)\n" \
  "v_add_f32 v36, v36, v52\n" \
  "v_add_f32 v37, v37, v53\n" \
  "v_add_f32 v38, v38, v54\n" \
  "v_add_f32 v39, v39, v55\n" \
  "v_add_f32 v56, v56, v60\n" \
  "v_add_f32 v57, v57, v61\n" \
  "v_add_f32 v58, v58, v62\n" \
  "v_add_f32 v59, v59, v63\n" \
  "v_add_f32 v36, v36, v56\n" \
  "v_add_f32 v37, v37, v57\n" \
  "v_add_f32 v38, v38, v58\n" \
  "v_add_f32 v39, v39, v59\n" \
  "v_cvt_pk_bf16_f32 v36, v36, v37\n" \
  "v_cvt_pk_bf16_f32 v37, v38, v39\n" \
  "buffer_store_dwordx2 v[36:37], v10, s[76:79], s88 offen\n" \
  "s_add_u32 s88, s88, 131072\n" \
  "v_and_b32 v10, 0x7fffffff, v10\n" \
  "s_waitcnt vmcnt(0)\n" \
  "s_branch LE_%=\n" \
  "LP2_%=:\n" \
  "buffer_load_dword v9, v8, s[80:83], 0 offen sc1\n" \
  "s_waitcnt vmcnt(0)\n" \
  "v_cmp_gt_u32 vcc, s92, v9\n" \
  "s_cbranch_vccz LP3_%=\n" \
  "s_sleep 2\n" \
  "s_add_u32 s91, s91, 1\n" \
  "s_cmp_lt_u32 s91, " STR(SPIN_CAP) "\n" \
  "s_cbranch_scc1 LP2_%=\n" \
  "v_mov_b32 v5, " STR(CW_DEAD) "*4\n" \
  "v_mov_b32 v15, 1\n" \
  "buffer_store_dword v15, v5, s[80:83], 0 offen sc1\n" \
  "s_branch LP3_%=\n" \
  "LE_%=:\n" \
  ""

#define XA_ASM \
  "s_mov_b64 s[68:69], %0\n" \
  "s_and_b32 s69, s69, 0xffff\n" \
  "s_mov_b32 s70, " STR(EX_BYTES32) "\n" \
  "s_mov_b32 s71, 0x00020000\n" \
  "s_mov_b64 s[72:73], %1\n" \
  "s_and_b32 s73, s73, 0xffff\n" \
  "s_mov_b32 s74, 4*" STR(QKV_SZ32) "\n" \
  "s_mov_b32 s75, 0x00020000\n" \
  "s_mov_b64 s[76:77], %2\n" \
  "s_and_b32 s77, s77, 0xffff\n" \
  "s_mov_b32 s78, " STR(CTRL_BYTES) "\n" \
  "s_mov_b32 s79, 0x00020000\n" \
  "v_mbcnt_lo_u32_b32 v4, -1, 0\n" \
  "v_mbcnt_hi_u32_b32 v4, -1, v4\n" \
  "v_and_b32 v116, 15, v4\n" \
  "v_lshrrev_b32 v117, 4, v4\n" \
  "v_lshlrev_b32 v7, 4, v4\n" \
  "v_add_u32 v8, 4096, v7\n" \
  "v_add_u32 v17, 0x40000, v7\n" \
  "v_add_u32 v11, %5, v7\n" \
  "v_lshrrev_b32 v5, 2, v116\n" \
  "v_and_b32 v5, 1, v5\n" \
  "v_lshl_add_u32 v5, v117, 2, v5\n" \
  "v_and_b32 v6, 2, v4\n" \
  "v_add_lshl_u32 v5, v5, v6, 4\n" \
  "v_and_b32 v6, 1, v4\n" \
  "v_lshl_add_u32 v5, v6, 3, v5\n" \
  "v_add_u32 v12, %5, v5\n" \
  "s_lshl_b32 s80, %3, 7\n" \
  "s_add_u32 s81, s80, " STR(CW_XPROG) "*4\n" \
  "v_lshl_add_u32 v15, v4, 24, s81\n" \
  "s_add_u32 s81, s80, " STR(CW_PROG) "*4\n" \
  "v_mov_b32 v13, s81\n" \
  "s_lshr_b32 s80, %3, 1\n" \
  "s_lshl_b32 s80, s80, 9\n" \
  "s_add_u32 s80, s80, " STR(CW_GPROG) "*4\n" \
  "v_writelane_b32 v13, s80, 1\n" \
  "s_add_u32 s80, s80, 128\n" \
  "v_writelane_b32 v13, s80, 2\n" \
  "s_add_u32 s80, s80, 128\n" \
  "v_writelane_b32 v13, s80, 3\n" \
  "s_add_u32 s80, s80, 128\n" \
  "v_writelane_b32 v13, s80, 4\n" \
  "v_mov_b32 v16, 0\n" \
  "v_mov_b32 v6, 0xff800000\n" \
  "v_lshlrev_b32 v5, 2, v117\n" \
  "v_cmp_gt_u32 vcc, v5, v116\n" \
  "v_mov_b32 v24, 0x3f803f80\n" \
  "v_add_u32 v5, 1, v5\n" \
  "v_cndmask_b32 v20, 0, v6, vcc\n" \
  "v_cmp_gt_u32 vcc, v5, v116\n" \
  "v_mov_b32 v25, 0x3f803f80\n" \
  "v_add_u32 v5, 1, v5\n" \
  "v_cndmask_b32 v21, 0, v6, vcc\n" \
  "v_cmp_gt_u32 vcc, v5, v116\n" \
  "v_mov_b32 v26, 0x3f803f80\n" \
  "v_add_u32 v5, 1, v5\n" \
  "v_cndmask_b32 v22, 0, v6, vcc\n" \
  "v_cmp_gt_u32 vcc, v5, v116\n" \
  "v_mov_b32 v27, 0x3f803f80\n" \
  "v_add_u32 v5, 1, v5\n" \
  "v_cndmask_b32 v23, 0, v6, vcc\n" \
  "v_mov_b64 v[130:131], 0\n" \
  "v_mov_b32 v156, 0\n" \
  "v_mov_b32 v158, 0\n" \
  "s_mov_b32 s89, 0x3e0293ee\n" \
  "s_mov_b32 s88, 0\n" \
  "s_mov_b32 s82, 0\n" \
  "s_mov_b32 s90, 0\n" \
  "s_lshr_b32 s80, s82, 2\n" \
  "s_xnor_b32 s80, s80, %4\n" \
  "s_and_b32 s80, s80, 1\n" \
  "s_lshl_b32 s80, s80, 13\n" \
  "s_lshl_b32 s83, s82, 19\n" \
  "s_or_b32 s83, s83, s80\n" \
  "s_lshl_b32 s80, %3, 14\n" \
  "s_add_u32 s83, s83, s80\n" \
  "s_add_u32 s84, s83, " STR(QKV_SZ32) "\n" \
  "s_add_u32 s85, s84, " STR(QKV_SZ32) "\n" \
  "s_mov_b32 s81, 1\n" \
  "LP0_%=:\n" \
  "buffer_load_dword v14, v13, s[76:79], 0 offen sc1\n" \
  "s_waitcnt vmcnt(0)\n" \
  "v_readlane_b32 s91, v14, 0\n" \
  "v_readlane_b32 s92, v14, 1\n" \
  "v_readlane_b32 s80, v14, 2\n" \
  "s_min_u32 s92, s92, s80\n" \
  "v_readlane_b32 s80, v14, 3\n" \
  "s_min_u32 s92, s92, s80\n" \
  "v_readlane_b32 s80, v14, 4\n" \
  "s_min_u32 s92, s92, s80\n" \
  "s_cmp_ge_u32 s92, s81\n" \
  "s_cbranch_scc1 LP1_%=\n" \
  "s_sleep 16\n" \
  "s_add_u32 s88, s88, 1\n" \
  "s_cmp_lt_u32 s88, " STR(SPIN_CAP) "\n" \
  "s_cbranch_scc1 LP0_%=\n" \
  "v_mov_b32 v5, " STR(CW_DEAD) "*4\n" \
  "v_mov_b32 v6, 1\n" \
  "buffer_store_dword v6, v5, s[76:79], 0 offen sc1\n" \
  "s_branch LP1_%=\n" \
  "LP1_%=:\n" \
  "buffer_load_dwordx4 a[64:67], v7, s[72:75], s84 offen offset:0 sc1\n" \
  "buffer_load_dwordx4 a[68:71], v7, s[72:75], s84 offen offset:1024 sc1\n" \
  "buffer_load_dwordx4 a[72:75], v7, s[72:75], s84 offen offset:2048 sc1\n" \
  "buffer_load_dwordx4 a[76:79], v7, s[72:75], s84 offen offset:3072 sc1\n" \
  "buffer_load_dwordx4 a[80:83], v8, s[72:75], s84 offen offset:0 sc1\n" \
  "buffer_load_dwordx4 a[84:87], v8, s[72:75], s84 offen offset:1024 sc1\n" \
  "buffer_load_dwordx4 a[88:91], v8, s[72:75], s84 offen offset:2048 sc1\n" \
  "buffer_load_dwordx4 a[92:95], v8, s[72:75], s84 offen offset:3072 sc1\n" \
  "buffer_load_dwordx4 a[0:3], v7, s[72:75], s83 offen offset:0 sc1\n" \
  "buffer_load_dwordx4 a[4:7], v7, s[72:75], s83 offen offset:1024 sc1\n" \
  "buffer_load_dwordx4 a[8:11], v7, s[72:75], s83 offen offset:2048 sc1\n" \
  "buffer_load_dwordx4 a[12:15], v7, s[72:75], s83 offen offset:3072 sc1\n" \
  "buffer_load_dwordx4 a[16:19], v8, s[72:75], s83 offen offset:0 sc1\n" \
  "buffer_load_dwordx4 a[20:23], v8, s[72:75], s83 offen offset:1024 sc1\n" \
  "buffer_load_dwordx4 a[24:27], v8, s[72:75], s83 offen offset:2048 sc1\n" \
  "buffer_load_dwordx4 a[28:31], v8, s[72:75], s83 offen offset:3072 sc1\n" \
  "buffer_load_dwordx4 a[96:99], v7, s[72:75], s85 offen offset:0 sc1\n" \
  "buffer_load_dwordx4 a[100:103], v7, s[72:75], s85 offen offset:1024 sc1\n" \
  "buffer_load_dwordx4 a[104:107], v7, s[72:75], s85 offen offset:2048 sc1\n" \
  "buffer_load_dwordx4 a[108:111], v7, s[72:75], s85 offen offset:3072 sc1\n" \
  "buffer_load_dwordx4 a[112:115], v8, s[72:75], s85 offen offset:0 sc1\n" \
  "buffer_load_dwordx4 a[116:119], v8, s[72:75], s85 offen offset:1024 sc1\n" \
  "buffer_load_dwordx4 a[120:123], v8, s[72:75], s85 offen offset:2048 sc1\n" \
  "buffer_load_dwordx4 a[124:127], v8, s[72:75], s85 offen offset:3072 sc1\n" \
  "s_add_u32 s80, s82, 0\n" \
  "s_lshl_b32 s80, s80, 5\n" \
  "s_add_u32 s80, s80, %3\n" \
  "s_lshl_b32 s87, s80, 14\n" \
  "s_add_u32 s87, s87, " STR(EXP_B) "<<21\n" \
  "s_lshr_b32 s80, s87, 21\n" \
  "s_lshr_b32 s80, %4, s80\n" \
  "s_and_b32 s80, s80, 1\n" \
  "s_lshl_b32 s80, s80, 13\n" \
  "s_or_b32 s87, s87, s80\n" \
  "s_waitcnt vmcnt(0)\n" \
  ".p2align 6\n" \
  "LA_%=:\n" \
  "s_waitcnt vmcnt(24)\n" \
  "s_add_u32 s81, s82, 2\n" \
  "s_min_u32 s81, s81, 64\n" \
  "s_sub_u32 s84, s81, 1\n" \
  "v_mfma_f32_16x16x32_bf16 v[116:119], a[64:67], a[0:3], v[20:23]\n" \
  "s_lshr_b32 s80, s84, 2\n" \
  "s_xnor_b32 s80, s80, %4\n" \
  "v_mfma_f32_16x16x32_bf16 v[120:123], a[64:67], a[16:19], 0\n" \
  "s_and_b32 s80, s80, 1\n" \
  "s_lshl_b32 s80, s80, 13\n" \
  "v_mfma_f32_16x16x32_bf16 v[124:127], a[80:83], a[16:19], v[20:23]\n" \
  "s_lshl_b32 s83, s84, 19\n" \
  "s_or_b32 s83, s83, s80\n" \
  "v_mfma_f32_16x16x32_bf16 v[116:119], a[68:71], a[4:7], v[116:119]\n" \
  "s_lshl_b32 s80, %3, 14\n" \
  "s_add_u32 s83, s83, s80\n" \
  "v_mfma_f32_16x16x32_bf16 v[120:123], a[68:71], a[20:23], v[120:123]\n" \
  "s_add_u32 s84, s83, " STR(QKV_SZ32) "\n" \
  "s_add_u32 s85, s84, " STR(QKV_SZ32) "\n" \
  "v_mfma_f32_16x16x32_bf16 v[124:127], a[84:87], a[20:23], v[124:127]\n" \
  "s_lshr_b32 s80, s82, 2\n" \
  "s_xnor_b32 s80, s80, %4\n" \
  "v_mfma_f32_16x16x32_bf16 v[116:119], a[72:75], a[8:11], v[116:119]\n" \
  "s_and_b32 s80, s80, 1\n" \
  "s_lshl_b32 s80, s80, 13\n" \
  "v_mfma_f32_16x16x32_bf16 v[120:123], a[72:75], a[24:27], v[120:123]\n" \
  "s_lshl_b32 s86, s82, 19\n" \
  "s_or_b32 s86, s86, s80\n" \
  "v_mfma_f32_16x16x32_bf16 v[124:127], a[88:91], a[24:27], v[124:127]\n" \
  "s_lshr_b32 s80, %3, 1\n" \
  "s_lshl_b32 s80, s80, 14\n" \
  "v_mfma_f32_16x16x32_bf16 v[116:119], a[76:79], a[12:15], v[116:119]\n" \
  "s_add_u32 s86, s86, s80\n" \
  "s_and_b32 s80, %3, 1\n" \
  "v_mfma_f32_16x16x32_bf16 v[120:123], a[76:79], a[28:31], v[120:123]\n" \
  "s_lshl_b32 s80, s80, 12\n" \
  "s_add_u32 s86, s86, s80\n" \
  "v_mfma_f32_16x16x32_bf16 v[124:127], a[92:95], a[28:31], v[124:127]\n" \
  "s_add_u32 s86, s86, 3*" STR(QKV_SZ32) "\n" \
  "s_cmp_lt_u32 s92, s81\n" \
  "s_cbranch_scc1 LG0_%=\n" \
  "LH0_%=:\n" \
  "buffer_load_dwordx4 a[64:67], v7, s[72:75], s84 offen offset:0 sc1\n" \
  "buffer_load_dwordx4 a[68:71], v7, s[72:75], s84 offen offset:1024 sc1\n" \
  "buffer_load_dwordx4 a[72:75], v7, s[72:75], s84 offen offset:2048 sc1\n" \
  "buffer_load_dwordx4 a[76:79], v7, s[72:75], s84 offen offset:3072 sc1\n" \
  "buffer_load_dwordx4 a[80:83], v8, s[72:75], s84 offen offset:0 sc1\n" \
  "buffer_load_dwordx4 a[84:87], v8, s[72:75], s84 offen offset:1024 sc1\n" \
  "v_max3_f32 v144, v116, v117, v118\n" \
  "v_max3_f32 v145, v120, v121, v122\n" \
  "v_max_f32 v144, v144, v119\n" \
  "v_max3_f32 v145, v145, v123, v124\n" \
  "v_max3_f32 v145, v145, v125, v126\n" \
  "v_max_f32 v145, v145, v127\n" \
  "v_mov_b32 v5, v144\n" \
  "v_mov_b32 v6, v145\n" \
  "s_nop 0\n" \
  "v_permlane16_swap_b32 v144, v5\n" \
  "v_permlane16_swap_b32 v145, v6\n" \
  "v_max_f32 v144, v144, v5\n" \
  "v_max_f32 v145, v145, v6\n" \
  "v_mov_b32 v5, v144\n" \
  "v_mov_b32 v6, v145\n" \
  "s_nop 0\n" \
  "v_permlane32_swap_b32 v144, v5\n" \
  "v_permlane32_swap_b32 v145, v6\n" \
  "v_max_f32 v144, v144, v5\n" \
  "v_max_f32 v145, v145, v6\n" \
  "v_mul_f32 v144, s89, v144\n" \
  "v_mul_f32 v145, s89, v145\n" \
  "v_fma_f32 v116, v116, s89, -v144\n" \
  "v_exp_f32 v116, v116\n" \
  "buffer_load_dwordx4 a[88:91], v8, s[72:75], s84 offen offset:2048 sc1\n" \
  "v_fma_f32 v117, v117, s89, -v144\n" \
  "v_exp_f32 v117, v117\n" \
  "buffer_load_dwordx4 a[92:95], v8, s[72:75], s84 offen offset:3072 sc1\n" \
  "v_fma_f32 v118, v118, s89, -v144\n" \
  "v_exp_f32 v118, v118\n" \
  "buffer_load_dwordx4 a[32:35], v7, s[72:75], s83 offen offset:0 sc1\n" \
  "v_fma_f32 v119, v119, s89, -v144\n" \
  "v_exp_f32 v119, v119\n" \
  "buffer_load_dwordx4 a[36:39], v7, s[72:75], s83 offen offset:1024 sc1\n" \
  "v_fma_f32 v120, v120, s89, -v145\n" \
  "v_exp_f32 v120, v120\n" \
  "buffer_load_dwordx4 a[40:43], v7, s[72:75], s83 offen offset:2048 sc1\n" \
  "v_fma_f32 v121, v121, s89, -v145\n" \
  "v_exp_f32 v121, v121\n" \
  "buffer_load_dwordx4 a[44:47], v7, s[72:75], s83 offen offset:3072 sc1\n" \
  "v_fma_f32 v122, v122, s89, -v145\n" \
  "v_exp_f32 v122, v122\n" \
  "buffer_load_dwordx4 a[48:51], v8, s[72:75], s83 offen offset:0 sc1\n" \
  "v_fma_f32 v123, v123, s89, -v145\n" \
  "v_exp_f32 v123, v123\n" \
  "buffer_load_dwordx4 a[52:55], v8, s[72:75], s83 offen offset:1024 sc1\n" \
  "v_fma_f32 v124, v124, s89, -v145\n" \
  "v_exp_f32 v124, v124\n" \
  "buffer_load_dwordx4 a[56:59], v8, s[72:75], s83 offen offset:2048 sc1\n" \
  "v_fma_f32 v125, v125, s89, -v145\n" \
  "v_exp_f32 v125, v125\n" \
  "buffer_load_dwordx4 a[60:63], v8, s[72:75], s83 offen offset:3072 sc1\n" \
  "v_fma_f32 v126, v126, s89, -v145\n" \
  "v_exp_f32 v126, v126\n" \
  "v_fma_f32 v127, v127, s89, -v145\n" \
  "v_exp_f32 v127, v127\n" \
  "s_nop 0\n" \
  "v_cvt_pk_bf16_f32 v128, v116, v117\n" \
  "v_cvt_pk_bf16_f32 v129, v118, v119\n" \
  "v_cvt_pk_bf16_f32 v132, v120, v121\n" \
  "v_cvt_pk_bf16_f32 v133, v122, v123\n" \
  "v_cvt_pk_bf16_f32 v134, v124, v125\n" \
  "v_cvt_pk_bf16_f32 v135, v126, v127\n" \
  "s_waitcnt vmcnt(22)\n" \
  "buffer_store_dword v16, v15, s[76:79], 0 offen sc1\n" \
  "v_mfma_f32_16x16x32_bf16 v[136:139], v[24:27], v[128:131], 0\n" \
  "v_mfma_f32_16x16x32_bf16 v[140:143], v[24:27], v[132:135], 0\n" \
  "v_add_u32 v16, 1, v16\n" \
  "v_mfma_f32_16x16x32_bf16 v[52:55], a[96:99], v[128:131], 0\n" \
  "v_mfma_f32_16x16x32_bf16 v[84:87], a[96:99], v[132:135], 0\n" \
  "buffer_load_dwordx4 a[96:99], v7, s[72:75], s85 offen offset:0 sc1\n" \
  "v_mfma_f32_16x16x32_bf16 v[56:59], a[100:103], v[128:131], 0\n" \
  "v_mfma_f32_16x16x32_bf16 v[88:91], a[100:103], v[132:135], 0\n" \
  "buffer_load_dwordx4 a[100:103], v7, s[72:75], s85 offen offset:1024 sc1\n" \
  "v_mfma_f32_16x16x32_bf16 v[60:63], a[104:107], v[128:131], 0\n" \
  "v_mfma_f32_16x16x32_bf16 v[92:95], a[104:107], v[132:135], 0\n" \
  "buffer_load_dwordx4 a[104:107], v7, s[72:75], s85 offen offset:2048 sc1\n" \
  "v_mfma_f32_16x16x32_bf16 v[64:67], a[108:111], v[128:131], 0\n" \
  "v_mfma_f32_16x16x32_bf16 v[96:99], a[108:111], v[132:135], 0\n" \
  "buffer_load_dwordx4 a[108:111], v7, s[72:75], s85 offen offset:3072 sc1\n" \
  "v_mfma_f32_16x16x32_bf16 v[68:71], a[112:115], v[128:131], 0\n" \
  "v_mfma_f32_16x16x32_bf16 v[100:103], a[112:115], v[132:135], 0\n" \
  "buffer_load_dwordx4 a[112:115], v8, s[72:75], s85 offen offset:0 sc1\n" \
  "v_mfma_f32_16x16x32_bf16 v[72:75], a[116:119], v[128:131], 0\n" \
  "v_mfma_f32_16x16x32_bf16 v[104:107], a[116:119], v[132:135], 0\n" \
  "buffer_load_dwordx4 a[116:119], v8, s[72:75], s85 offen offset:1024 sc1\n" \
  "v_mfma_f32_16x16x32_bf16 v[76:79], a[120:123], v[128:131], 0\n" \
  "v_mfma_f32_16x16x32_bf16 v[108:111], a[120:123], v[132:135], 0\n" \
  "buffer_load_dwordx4 a[120:123], v8, s[72:75], s85 offen offset:2048 sc1\n" \
  "v_mfma_f32_16x16x32_bf16 v[80:83], a[124:127], v[128:131], 0\n" \
  "v_mfma_f32_16x16x32_bf16 v[112:115], a[124:127], v[132:135], 0\n" \
  "buffer_load_dwordx4 a[124:127], v8, s[72:75], s85 offen offset:3072 sc1\n" \
  "buffer_load_dword v14, v13, s[76:79], 0 offen sc1\n" \
  "s_cmp_eq_u32 s90, 0\n" \
  "s_cbranch_scc1 LW0_%=\n" \
  "s_waitcnt vmcnt(26)\n" \
  "LM0_%=:\n" \
  "ds_write_b128 v11, v[44:47]\n" \
  "ds_write_b128 v11, v[48:51] offset:1024\n" \
  "v_mfma_f32_16x16x32_bf16 v[148:151], v[28:31], a[0:3], 0\n" \
  "v_max3_u32 v5, v28, v29, v30\n" \
  "v_max3_u32 v5, v5, v31, v32\n" \
  "v_mfma_f32_16x16x32_bf16 v[152:155], v[28:31], a[16:19], 0\n" \
  "v_max3_u32 v5, v5, v33, v34\n" \
  "v_max3_u32 v5, v5, v35, v36\n" \
  "v_mfma_f32_16x16x32_bf16 v[148:151], v[32:35], a[4:7], v[148:151]\n" \
  "v_max3_u32 v5, v5, v37, v38\n" \
  "v_max3_u32 v5, v5, v39, v40\n" \
  "v_mfma_f32_16x16x32_bf16 v[152:155], v[32:35], a[20:23], v[152:155]\n" \
  "v_max3_u32 v5, v5, v41, v42\n" \
  "v_max3_u32 v5, v5, v43, v44\n" \
  "v_mfma_f32_16x16x32_bf16 v[148:151], v[36:39], a[8:11], v[148:151]\n" \
  "v_max3_u32 v5, v5, v45, v46\n" \
  "v_max3_u32 v5, v5, v47, v48\n" \
  "v_mfma_f32_16x16x32_bf16 v[152:155], v[36:39], a[24:27], v[152:155]\n" \
  "v_max3_u32 v5, v5, v49, v50\n" \
  "v_max_u32 v5, v5, v51\n" \
  "v_mfma_f32_16x16x32_bf16 v[148:151], v[40:43], a[12:15], v[148:151]\n" \
  "v_cmp_eq_u32 vcc, -1, v5\n" \
  "v_mfma_f32_16x16x32_bf16 v[152:155], v[40:43], a[28:31], v[152:155]\n" \
  "ds_read_b64_tr_b16 a[128:129], v12 offset:0\n" \
  "ds_read_b64_tr_b16 a[130:131], v12 offset:256\n" \
  "ds_read_b64_tr_b16 a[132:133], v12 offset:512\n" \
  "ds_read_b64_tr_b16 a[134:135], v12 offset:768\n" \
  "ds_read_b64_tr_b16 a[136:137], v12 offset:1024\n" \
  "ds_read_b64_tr_b16 a[138:139], v12 offset:1280\n" \
  "ds_read_b64_tr_b16 a[140:141], v12 offset:1536\n" \
  "ds_read_b64_tr_b16 a[142:143], v12 offset:1792\n" \
  "s_cbranch_vccnz LR0_%=\n" \
  "v_max_f32 v146, v150, v151\n" \
  "v_max_f32 v147, v154, v155\n" \
  "v_mov_b32 v5, v146\n" \
  "v_mov_b32 v6, v147\n" \
  "s_nop 0\n" \
  "v_permlane16_swap_b32 v146, v5\n" \
  "v_permlane16_swap_b32 v147, v6\n" \
  "v_max_f32 v146, v146, v5\n" \
  "v_max_f32 v147, v147, v6\n" \
  "v_mov_b32 v5, v146\n" \
  "v_mov_b32 v6, v147\n" \
  "s_nop 0\n" \
  "v_permlane32_swap_b32 v146, v5\n" \
  "v_permlane32_swap_b32 v147, v6\n" \
  "v_max_f32 v146, v146, v5\n" \
  "v_max_f32 v147, v147, v6\n" \
  "v_mul_f32 v146, s89, v146\n" \
  "v_mul_f32 v147, s89, v147\n" \
  "v_max_f32 v146, v146, v144\n" \
  "v_max_f32 v147, v147, v145\n" \
  "v_sub_f32 v160, v144, v146\n" \
  "v_sub_f32 v162, v145, v147\n" \
  "v_fma_f32 v150, v150, s89, -v146\n" \
  "v_fma_f32 v151, v151, s89, -v146\n" \
  "v_fma_f32 v154, v154, s89, -v147\n" \
  "v_fma_f32 v155, v155, s89, -v147\n" \
  "v_exp_f32 v150, v150\n" \
  "v_exp_f32 v151, v151\n" \
  "v_exp_f32 v154, v154\n" \
  "v_exp_f32 v155, v155\n" \
  "v_exp_f32 v160, v160\n" \
  "v_exp_f32 v162, v162\n" \
  "v_add_f32 v146, v150, v151\n" \
  "v_add_f32 v147, v154, v155\n" \
  "v_mov_b32 v5, v146\n" \
  "v_mov_b32 v6, v147\n" \
  "s_nop 0\n" \
  "v_permlane16_swap_b32 v146, v5\n" \
  "v_permlane16_swap_b32 v147, v6\n" \
  "v_add_f32 v146, v146, v5\n" \
  "v_add_f32 v147, v147, v6\n" \
  "v_mov_b32 v5, v146\n" \
  "v_mov_b32 v6, v147\n" \
  "s_nop 0\n" \
  "v_permlane32_swap_b32 v146, v5\n" \
  "v_permlane32_swap_b32 v147, v6\n" \
  "v_add_f32 v146, v146, v5\n" \
  "v_add_f32 v147, v147, v6\n" \
  "v_fma_f32 v146, v160, v136, v146\n" \
  "v_fma_f32 v147, v162, v140, v147\n" \
  "v_rcp_f32 v146, v146\n" \
  "v_rcp_f32 v147, v147\n" \
  "v_mul_f32 v160, v160, v146\n" \
  "v_mul_f32 v162, v162, v147\n" \
  "v_mul_f32 v150, v150, v146\n" \
  "v_mul_f32 v151, v151, v146\n" \
  "v_mul_f32 v154, v154, v147\n" \
  "v_mul_f32 v155, v155, v147\n" \
  "v_cvt_pk_bf16_f32 v157, v150, v151\n" \
  "v_cvt_pk_bf16_f32 v159, v154, v155\n" \
  "v_pk_mul_f32 v[52:53], v[52:53], v[160:161] op_sel_hi:[1,0]\n" \
  "v_pk_mul_f32 v[54:55], v[54:55], v[160:161] op_sel_hi:[1,0]\n" \
  "v_pk_mul_f32 v[56:57], v[56:57], v[160:161] op_sel_hi:[1,0]\n" \
  "v_pk_mul_f32 v[58:59], v[58:59], v[160:161] op_sel_hi:[1,0]\n" \
  "v_pk_mul_f32 v[60:61], v[60:61], v[160:161] op_sel_hi:[1,0]\n" \
  "v_pk_mul_f32 v[62:63], v[62:63], v[160:161] op_sel_hi:[1,0]\n" \
  "v_pk_mul_f32 v[64:65], v[64:65], v[160:161] op_sel_hi:[1,0]\n" \
  "v_pk_mul_f32 v[66:67], v[66:67], v[160:161] op_sel_hi:[1,0]\n" \
  "v_pk_mul_f32 v[68:69], v[68:69], v[160:161] op_sel_hi:[1,0]\n" \
  "v_pk_mul_f32 v[70:71], v[70:71], v[160:161] op_sel_hi:[1,0]\n" \
  "v_pk_mul_f32 v[72:73], v[72:73], v[160:161] op_sel_hi:[1,0]\n" \
  "v_pk_mul_f32 v[74:75], v[74:75], v[160:161] op_sel_hi:[1,0]\n" \
  "v_pk_mul_f32 v[76:77], v[76:77], v[160:161] op_sel_hi:[1,0]\n" \
  "v_pk_mul_f32 v[78:79], v[78:79], v[160:161] op_sel_hi:[1,0]\n" \
  "v_pk_mul_f32 v[80:81], v[80:81], v[160:161] op_sel_hi:[1,0]\n" \
  "v_pk_mul_f32 v[82:83], v[82:83], v[160:161] op_sel_hi:[1,0]\n" \
  "v_pk_mul_f32 v[84:85], v[84:85], v[162:163] op_sel_hi:[1,0]\n" \
  "v_pk_mul_f32 v[86:87], v[86:87], v[162:163] op_sel_hi:[1,0]\n" \
  "v_pk_mul_f32 v[88:89], v[88:89], v[162:163] op_sel_hi:[1,0]\n" \
  "v_pk_mul_f32 v[90:91], v[90:91], v[162:163] op_sel_hi:[1,0]\n" \
  "v_pk_mul_f32 v[92:93], v[92:93], v[162:163] op_sel_hi:[1,0]\n" \
  "v_pk_mul_f32 v[94:95], v[94:95], v[162:163] op_sel_hi:[1,0]\n" \
  "v_pk_mul_f32 v[96:97], v[96:97], v[162:163] op_sel_hi:[1,0]\n" \
  "v_pk_mul_f32 v[98:99], v[98:99], v[162:163] op_sel_hi:[1,0]\n" \
  "v_pk_mul_f32 v[100:101], v[100:101], v[162:163] op_sel_hi:[1,0]\n" \
  "v_pk_mul_f32 v[102:103], v[102:103], v[162:163] op_sel_hi:[1,0]\n" \
  "v_pk_mul_f32 v[104:105], v[104:105], v[162:163] op_sel_hi:[1,0]\n" \
  "v_pk_mul_f32 v[106:107], v[106:107], v[162:163] op_sel_hi:[1,0]\n" \
  "v_pk_mul_f32 v[108:109], v[108:109], v[162:163] op_sel_hi:[1,0]\n" \
  "v_pk_mul_f32 v[110:111], v[110:111], v[162:163] op_sel_hi:[1,0]\n" \
  "v_pk_mul_f32 v[112:113], v[112:113], v[162:163] op_sel_hi:[1,0]\n" \
  "v_pk_mul_f32 v[114:115], v[114:115], v[162:163] op_sel_hi:[1,0]\n" \
  "s_waitcnt lgkmcnt(0)\n" \
  "v_mfma_f32_16x16x16_bf16 v[52:55], a[128:129], v[156:157], v[52:55]\n" \
  "v_mfma_f32_16x16x16_bf16 v[56:59], a[130:131], v[156:157], v[56:59]\n" \
  "v_mfma_f32_16x16x16_bf16 v[60:63], a[132:133], v[156:157], v[60:63]\n" \
  "v_mfma_f32_16x16x16_bf16 v[64:67], a[134:135], v[156:157], v[64:67]\n" \
  "v_mfma_f32_16x16x16_bf16 v[68:71], a[136:137], v[156:157], v[68:71]\n" \
  "v_cvt_pk_bf16_f32 v164, v52, v53\n" \
  "v_cvt_pk_bf16_f32 v165, v54, v55\n" \
  "v_mfma_f32_16x16x16_bf16 v[72:75], a[138:139], v[156:157], v[72:75]\n" \
  "v_cvt_pk_bf16_f32 v166, v56, v57\n" \
  "v_cvt_pk_bf16_f32 v167, v58, v59\n" \
  "buffer_store_dwordx4 v[164:167], v7, s[72:75], s86 offen offset:0 sc1\n" \
  "v_mfma_f32_16x16x16_bf16 v[76:79], a[140:141], v[156:157], v[76:79]\n" \
  "v_cvt_pk_bf16_f32 v168, v60, v61\n" \
  "v_cvt_pk_bf16_f32 v169, v62, v63\n" \
  "v_mfma_f32_16x16x16_bf16 v[80:83], a[142:143], v[156:157], v[80:83]\n" \
  "v_cvt_pk_bf16_f32 v170, v64, v65\n" \
  "v_cvt_pk_bf16_f32 v171, v66, v67\n" \
  "buffer_store_dwordx4 v[168:171], v7, s[72:75], s86 offen offset:1024 sc1\n" \
  "v_mfma_f32_16x16x16_bf16 v[84:87], a[128:129], v[158:159], v[84:87]\n" \
  "v_cvt_pk_bf16_f32 v172, v68, v69\n" \
  "v_cvt_pk_bf16_f32 v173, v70, v71\n" \
  "v_mfma_f32_16x16x16_bf16 v[88:91], a[130:131], v[158:159], v[88:91]\n" \
  "v_cvt_pk_bf16_f32 v174, v72, v73\n" \
  "v_cvt_pk_bf16_f32 v175, v74, v75\n" \
  "buffer_store_dwordx4 v[172:175], v7, s[72:75], s86 offen offset:2048 sc1\n" \
  "v_mfma_f32_16x16x16_bf16 v[92:95], a[132:133], v[158:159], v[92:95]\n" \
  "v_cvt_pk_bf16_f32 v176, v76, v77\n" \
  "v_cvt_pk_bf16_f32 v177, v78, v79\n" \
  "v_mfma_f32_16x16x16_bf16 v[96:99], a[134:135], v[158:159], v[96:99]\n" \
  "v_cvt_pk_bf16_f32 v178, v80, v81\n" \
  "v_cvt_pk_bf16_f32 v179, v82, v83\n" \
  "buffer_store_dwordx4 v[176:179], v7, s[72:75], s86 offen offset:3072 sc1\n" \
  "v_mfma_f32_16x16x16_bf16 v[100:103], a[136:137], v[158:159], v[100:103]\n" \
  "v_cvt_pk_bf16_f32 v180, v84, v85\n" \
  "v_cvt_pk_bf16_f32 v181, v86, v87\n" \
  "v_mfma_f32_16x16x16_bf16 v[104:107], a[138:139], v[158:159], v[104:107]\n" \
  "v_cvt_pk_bf16_f32 v182, v88, v89\n" \
  "v_cvt_pk_bf16_f32 v183, v90, v91\n" \
  "buffer_store_dwordx4 v[180:183], v17, s[72:75], s86 offen offset:0 sc1\n" \
  "v_mfma_f32_16x16x16_bf16 v[108:111], a[140:141], v[158:159], v[108:111]\n" \
  "v_cvt_pk_bf16_f32 v184, v92, v93\n" \
  "v_cvt_pk_bf16_f32 v185, v94, v95\n" \
  "v_mfma_f32_16x16x16_bf16 v[112:115], a[142:143], v[158:159], v[112:115]\n" \
  "v_cvt_pk_bf16_f32 v186, v96, v97\n" \
  "v_cvt_pk_bf16_f32 v187, v98, v99\n" \
  "buffer_store_dwordx4 v[184:187], v17, s[72:75], s86 offen offset:1024 sc1\n" \
  "v_cvt_pk_bf16_f32 v188, v100, v101\n" \
  "v_cvt_pk_bf16_f32 v189, v102, v103\n" \
  "v_cvt_pk_bf16_f32 v190, v104, v105\n" \
  "v_cvt_pk_bf16_f32 v191, v106, v107\n" \
  "buffer_store_dwordx4 v[188:191], v17, s[72:75], s86 offen offset:2048 sc1\n" \
  "s_nop 1\n" \
  "v_cvt_pk_bf16_f32 v192, v108, v109\n" \
  "v_cvt_pk_bf16_f32 v193, v110, v111\n" \
  "v_cvt_pk_bf16_f32 v194, v112, v113\n" \
  "v_cvt_pk_bf16_f32 v195, v114, v115\n" \
  "buffer_store_dwordx4 v[192:195], v17, s[72:75], s86 offen offset:3072 sc1\n" \
  "s_add_u32 s82, s82, 1\n" \
  "s_cmp_eq_u32 s82, 64\n" \
  "s_cbranch_scc1 LZ_%=\n" \
  "s_add_u32 s80, s82, 0\n" \
  "s_lshl_b32 s80, s80, 5\n" \
  "s_add_u32 s80, s80, %3\n" \
  "s_lshl_b32 s87, s80, 14\n" \
  "s_add_u32 s87, s87, " STR(EXP_B) "<<21\n" \
  "s_lshr_b32 s80, s87, 21\n" \
  "s_lshr_b32 s80, %4, s80\n" \
  "s_and_b32 s80, s80, 1\n" \
  "s_lshl_b32 s80, s80, 13\n" \
  "s_or_b32 s87, s87, s80\n" \
  "s_waitcnt vmcnt(8)\n" \
  "v_readlane_b32 s91, v14, 0\n" \
  "v_readlane_b32 s92, v14, 1\n" \
  "v_readlane_b32 s80, v14, 2\n" \
  "s_min_u32 s92, s92, s80\n" \
  "v_readlane_b32 s80, v14, 3\n" \
  "s_min_u32 s92, s92, s80\n" \
  "v_readlane_b32 s80, v14, 4\n" \
  "s_min_u32 s92, s92, s80\n" \
  "s_cmp_gt_u32 s91, s82\n" \
  "s_cselect_b32 s90, 1, 0\n" \
  "s_cselect_b32 s80, 0, 0x80000000\n" \
  "v_or_b32 v9, s80, v7\n" \
  "v_or_b32 v10, s80, v8\n" \
  "buffer_load_dwordx4 v[28:31], v9, s[68:71], s87 offen offset:0 sc1\n" \
  "buffer_load_dwordx4 v[32:35], v9, s[68:71], s87 offen offset:1024 sc1\n" \
  "buffer_load_dwordx4 v[36:39], v9, s[68:71], s87 offen offset:2048 sc1\n" \
  "buffer_load_dwordx4 v[40:43], v9, s[68:71], s87 offen offset:3072 sc1\n" \
  "buffer_load_dwordx4 v[44:47], v10, s[68:71], s87 offen offset:0 sc1\n" \
  "buffer_load_dwordx4 v[48:51], v10, s[68:71], s87 offen offset:1024 sc1\n" \
  "s_waitcnt vmcnt(24)\n" \
  "s_add_u32 s81, s82, 2\n" \
  "s_min_u32 s81, s81, 64\n" \
  "s_sub_u32 s84, s81, 1\n" \
  "v_mfma_f32_16x16x32_bf16 v[116:119], a[64:67], a[32:35], v[20:23]\n" \
  "s_lshr_b32 s80, s84, 2\n" \
  "s_xnor_b32 s80, s80, %4\n" \
  "v_mfma_f32_16x16x32_bf16 v[120:123], a[64:67], a[48:51], 0\n" \
  "s_and_b32 s80, s80, 1\n" \
  "s_lshl_b32 s80, s80, 13\n" \
  "v_mfma_f32_16x16x32_bf16 v[124:127], a[80:83], a[48:51], v[20:23]\n" \
  "s_lshl_b32 s83, s84, 19\n" \
  "s_or_b32 s83, s83, s80\n" \
  "v_mfma_f32_16x16x32_bf16 v[116:119], a[68:71], a[36:39], v[116:119]\n" \
  "s_lshl_b32 s80, %3, 14\n" \
  "s_add_u32 s83, s83, s80\n" \
  "v_mfma_f32_16x16x32_bf16 v[120:123], a[68:71], a[52:55], v[120:123]\n" \
  "s_add_u32 s84, s83, " STR(QKV_SZ32) "\n" \
  "s_add_u32 s85, s84, " STR(QKV_SZ32) "\n" \
  "v_mfma_f32_16x16x32_bf16 v[124:127], a[84:87], a[52:55], v[124:127]\n" \
  "s_lshr_b32 s80, s82, 2\n" \
  "s_xnor_b32 s80, s80, %4\n" \
  "v_mfma_f32_16x16x32_bf16 v[116:119], a[72:75], a[40:43], v[116:119]\n" \
  "s_and_b32 s80, s80, 1\n" \
  "s_lshl_b32 s80, s80, 13\n" \
  "v_mfma_f32_16x16x32_bf16 v[120:123], a[72:75], a[56:59], v[120:123]\n" \
  "s_lshl_b32 s86, s82, 19\n" \
  "s_or_b32 s86, s86, s80\n" \
  "v_mfma_f32_16x16x32_bf16 v[124:127], a[88:91], a[56:59], v[124:127]\n" \
  "s_lshr_b32 s80, %3, 1\n" \
  "s_lshl_b32 s80, s80, 14\n" \
  "v_mfma_f32_16x16x32_bf16 v[116:119], a[76:79], a[44:47], v[116:119]\n" \
  "s_add_u32 s86, s86, s80\n" \
  "s_and_b32 s80, %3, 1\n" \
  "v_mfma_f32_16x16x32_bf16 v[120:123], a[76:79], a[60:63], v[120:123]\n" \
  "s_lshl_b32 s80, s80, 12\n" \
  "s_add_u32 s86, s86, s80\n" \
  "v_mfma_f32_16x16x32_bf16 v[124:127], a[92:95], a[60:63], v[124:127]\n" \
  "s_add_u32 s86, s86, 3*" STR(QKV_SZ32) "\n" \
  "s_cmp_lt_u32 s92, s81\n" \
  "s_cbranch_scc1 LG1_%=\n" \
  "LH1_%=:\n" \
  "buffer_load_dwordx4 a[64:67], v7, s[72:75], s84 offen offset:0 sc1\n" \
  "buffer_load_dwordx4 a[68:71], v7, s[72:75], s84 offen offset:1024 sc1\n" \
  "buffer_load_dwordx4 a[72:75], v7, s[72:75], s84 offen offset:2048 sc1\n" \
  "buffer_load_dwordx4 a[76:79], v7, s[72:75], s84 offen offset:3072 sc1\n" \
  "buffer_load_dwordx4 a[80:83], v8, s[72:75], s84 offen offset:0 sc1\n" \
  "buffer_load_dwordx4 a[84:87], v8, s[72:75], s84 offen offset:1024 sc1\n" \
  "v_max3_f32 v144, v116, v117, v118\n" \
  "v_max3_f32 v145, v120, v121, v122\n" \
  "v_max_f32 v144, v144, v119\n" \
  "v_max3_f32 v145, v145, v123, v124\n" \
  "v_max3_f32 v145, v145, v125, v126\n" \
  "v_max_f32 v145, v145, v127\n" \
  "v_mov_b32 v5, v144\n" \
  "v_mov_b32 v6, v145\n" \
  "s_nop 0\n" \
  "v_permlane16_swap_b32 v144, v5\n" \
  "v_permlane16_swap_b32 v145, v6\n" \
  "v_max_f32 v144, v144, v5\n" \
  "v_max_f32 v145, v145, v6\n" \
  "v_mov_b32 v5, v144\n" \
  "v_mov_b32 v6, v145\n" \
  "s_nop 0\n" \
  "v_permlane32_swap_b32 v144, v5\n" \
  "v_permlane32_swap_b32 v145, v6\n" \
  "v_max_f32 v144, v144, v5\n" \
  "v_max_f32 v145, v145, v6\n" \
  "v_mul_f32 v144, s89, v144\n" \
  "v_mul_f32 v145, s89, v145\n" \
  "v_fma_f32 v116, v116, s89, -v144\n" \
  "v_exp_f32 v116, v116\n" \
  "buffer_load_dwordx4 a[88:91], v8, s[72:75], s84 offen offset:2048 sc1\n" \
  "v_fma_f32 v117, v117, s89, -v144\n" \
  "v_exp_f32 v117, v117\n" \
  "buffer_load_dwordx4 a[92:95], v8, s[72:75], s84 offen offset:3072 sc1\n" \
  "v_fma_f32 v118, v118, s89, -v144\n" \
  "v_exp_f32 v118, v118\n" \
  "buffer_load_dwordx4 a[0:3], v7, s[72:75], s83 offen offset:0 sc1\n" \
  "v_fma_f32 v119, v119, s89, -v144\n" \
  "v_exp_f32 v119, v119\n" \
  "buffer_load_dwordx4 a[4:7], v7, s[72:75], s83 offen offset:1024 sc1\n" \
  "v_fma_f32 v120, v120, s89, -v145\n" \
  "v_exp_f32 v120, v120\n" \
  "buffer_load_dwordx4 a[8:11], v7, s[72:75], s83 offen offset:2048 sc1\n" \
  "v_fma_f32 v121, v121, s89, -v145\n" \
  "v_exp_f32 v121, v121\n" \
  "buffer_load_dwordx4 a[12:15], v7, s[72:75], s83 offen offset:3072 sc1\n" \
  "v_fma_f32 v122, v122, s89, -v145\n" \
  "v_exp_f32 v122, v122\n" \
  "buffer_load_dwordx4 a[16:19], v8, s[72:75], s83 offen offset:0 sc1\n" \
  "v_fma_f32 v123, v123, s89, -v145\n" \
  "v_exp_f32 v123, v123\n" \
  "buffer_load_dwordx4 a[20:23], v8, s[72:75], s83 offen offset:1024 sc1\n" \
  "v_fma_f32 v124, v124, s89, -v145\n" \
  "v_exp_f32 v124, v124\n" \
  "buffer_load_dwordx4 a[24:27], v8, s[72:75], s83 offen offset:2048 sc1\n" \
  "v_fma_f32 v125, v125, s89, -v145\n" \
  "v_exp_f32 v125, v125\n" \
  "buffer_load_dwordx4 a[28:31], v8, s[72:75], s83 offen offset:3072 sc1\n" \
  "v_fma_f32 v126, v126, s89, -v145\n" \
  "v_exp_f32 v126, v126\n" \
  "v_fma_f32 v127, v127, s89, -v145\n" \
  "v_exp_f32 v127, v127\n" \
  "s_nop 0\n" \
  "v_cvt_pk_bf16_f32 v128, v116, v117\n" \
  "v_cvt_pk_bf16_f32 v129, v118, v119\n" \
  "v_cvt_pk_bf16_f32 v132, v120, v121\n" \
  "v_cvt_pk_bf16_f32 v133, v122, v123\n" \
  "v_cvt_pk_bf16_f32 v134, v124, v125\n" \
  "v_cvt_pk_bf16_f32 v135, v126, v127\n" \
  "s_waitcnt vmcnt(22)\n" \
  "buffer_store_dword v16, v15, s[76:79], 0 offen sc1\n" \
  "v_mfma_f32_16x16x32_bf16 v[136:139], v[24:27], v[128:131], 0\n" \
  "v_mfma_f32_16x16x32_bf16 v[140:143], v[24:27], v[132:135], 0\n" \
  "v_add_u32 v16, 1, v16\n" \
  "v_mfma_f32_16x16x32_bf16 v[52:55], a[96:99], v[128:131], 0\n" \
  "v_mfma_f32_16x16x32_bf16 v[84:87], a[96:99], v[132:135], 0\n" \
  "buffer_load_dwordx4 a[96:99], v7, s[72:75], s85 offen offset:0 sc1\n" \
  "v_mfma_f32_16x16x32_bf16 v[56:59], a[100:103], v[128:131], 0\n" \
  "v_mfma_f32_16x16x32_bf16 v[88:91], a[100:103], v[132:135], 0\n" \
  "buffer_load_dwordx4 a[100:103], v7, s[72:75], s85 offen offset:1024 sc1\n" \
  "v_mfma_f32_16x16x32_bf16 v[60:63], a[104:107], v[128:131], 0\n" \
  "v_mfma_f32_16x16x32_bf16 v[92:95], a[104:107], v[132:135], 0\n" \
  "buffer_load_dwordx4 a[104:107], v7, s[72:75], s85 offen offset:2048 sc1\n" \
  "v_mfma_f32_16x16x32_bf16 v[64:67], a[108:111], v[128:131], 0\n" \
  "v_mfma_f32_16x16x32_bf16 v[96:99], a[108:111], v[132:135], 0\n" \
  "buffer_load_dwordx4 a[108:111], v7, s[72:75], s85 offen offset:3072 sc1\n" \
  "v_mfma_f32_16x16x32_bf16 v[68:71], a[112:115], v[128:131], 0\n" \
  "v_mfma_f32_16x16x32_bf16 v[100:103], a[112:115], v[132:135], 0\n" \
  "buffer_load_dwordx4 a[112:115], v8, s[72:75], s85 offen offset:0 sc1\n" \
  "v_mfma_f32_16x16x32_bf16 v[72:75], a[116:119], v[128:131], 0\n" \
  "v_mfma_f32_16x16x32_bf16 v[104:107], a[116:119], v[132:135], 0\n" \
  "buffer_load_dwordx4 a[116:119], v8, s[72:75], s85 offen offset:1024 sc1\n" \
  "v_mfma_f32_16x16x32_bf16 v[76:79], a[120:123], v[128:131], 0\n" \
  "v_mfma_f32_16x16x32_bf16 v[108:111], a[120:123], v[132:135], 0\n" \
  "buffer_load_dwordx4 a[120:123], v8, s[72:75], s85 offen offset:2048 sc1\n" \
  "v_mfma_f32_16x16x32_bf16 v[80:83], a[124:127], v[128:131], 0\n" \
  "v_mfma_f32_16x16x32_bf16 v[112:115], a[124:127], v[132:135], 0\n" \
  "buffer_load_dwordx4 a[124:127], v8, s[72:75], s85 offen offset:3072 sc1\n" \
  "buffer_load_dword v14, v13, s[76:79], 0 offen sc1\n" \
  "s_cmp_eq_u32 s90, 0\n" \
  "s_cbranch_scc1 LW1_%=\n" \
  "s_waitcnt vmcnt(26)\n" \
  "LM1_%=:\n" \
  "ds_write_b128 v11, v[44:47]\n" \
  "ds_write_b128 v11, v[48:51] offset:1024\n" \
  "v_mfma_f32_16x16x32_bf16 v[148:151], v[28:31], a[32:35], 0\n" \
  "v_max3_u32 v5, v28, v29, v30\n" \
  "v_max3_u32 v5, v5, v31, v32\n" \
  "v_mfma_f32_16x16x32_bf16 v[152:155], v[28:31], a[48:51], 0\n" \
  "v_max3_u32 v5, v5, v33, v34\n" \
  "v_max3_u32 v5, v5, v35, v36\n" \
  "v_mfma_f32_16x16x32_bf16 v[148:151], v[32:35], a[36:39], v[148:151]\n" \
  "v_max3_u32 v5, v5, v37, v38\n" \
  "v_max3_u32 v5, v5, v39, v40\n" \
  "v_mfma_f32_16x16x32_bf16 v[152:155], v[32:35], a[52:55], v[152:155]\n" \
  "v_max3_u32 v5, v5, v41, v42\n" \
  "v_max3_u32 v5, v5, v43, v44\n" \
  "v_mfma_f32_16x16x32_bf16 v[148:151], v[36:39], a[40:43], v[148:151]\n" \
  "v_max3_u32 v5, v5, v45, v46\n" \
  "v_max3_u32 v5, v5, v47, v48\n" \
  "v_mfma_f32_16x16x32_bf16 v[152:155], v[36:39], a[56:59], v[152:155]\n" \
  "v_max3_u32 v5, v5, v49, v50\n" \
  "v_max_u32 v5, v5, v51\n" \
  "v_mfma_f32_16x16x32_bf16 v[148:151], v[40:43], a[44:47], v[148:151]\n" \
  "v_cmp_eq_u32 vcc, -1, v5\n" \
  "v_mfma_f32_16x16x32_bf16 v[152:155], v[40:43], a[60:63], v[152:155]\n" \
  "ds_read_b64_tr_b16 a[128:129], v12 offset:0\n" \
  "ds_read_b64_tr_b16 a[130:131], v12 offset:256\n" \
  "ds_read_b64_tr_b16 a[132:133], v12 offset:512\n" \
  "ds_read_b64_tr_b16 a[134:135], v12 offset:768\n" \
  "ds_read_b64_tr_b16 a[136:137], v12 offset:1024\n" \
  "ds_read_b64_tr_b16 a[138:139], v12 offset:1280\n" \
  "ds_read_b64_tr_b16 a[140:141], v12 offset:1536\n" \
  "ds_read_b64_tr_b16 a[142:143], v12 offset:1792\n" \
  "s_cbranch_vccnz LR1_%=\n" \
  "v_max_f32 v146, v150, v151\n" \
  "v_max_f32 v147, v154, v155\n" \
  "v_mov_b32 v5, v146\n" \
  "v_mov_b32 v6, v147\n" \
  "s_nop 0\n" \
  "v_permlane16_swap_b32 v146, v5\n" \
  "v_permlane16_swap_b32 v147, v6\n" \
  "v_max_f32 v146, v146, v5\n" \
  "v_max_f32 v147, v147, v6\n" \
  "v_mov_b32 v5, v146\n" \
  "v_mov_b32 v6, v147\n" \
  "s_nop 0\n" \
  "v_permlane32_swap_b32 v146, v5\n" \
  "v_permlane32_swap_b32 v147, v6\n" \
  "v_max_f32 v146, v146, v5\n" \
  "v_max_f32 v147, v147, v6\n" \
  "v_mul_f32 v146, s89, v146\n" \
  "v_mul_f32 v147, s89, v147\n" \
  "v_max_f32 v146, v146, v144\n" \
  "v_max_f32 v147, v147, v145\n" \
  "v_sub_f32 v160, v144, v146\n" \
  "v_sub_f32 v162, v145, v147\n" \
  "v_fma_f32 v150, v150, s89, -v146\n" \
  "v_fma_f32 v151, v151, s89, -v146\n" \
  "v_fma_f32 v154, v154, s89, -v147\n" \
  "v_fma_f32 v155, v155, s89, -v147\n" \
  "v_exp_f32 v150, v150\n" \
  "v_exp_f32 v151, v151\n" \
  "v_exp_f32 v154, v154\n" \
  "v_exp_f32 v155, v155\n" \
  "v_exp_f32 v160, v160\n" \
  "v_exp_f32 v162, v162\n" \
  "v_add_f32 v146, v150, v151\n" \
  "v_add_f32 v147, v154, v155\n" \
  "v_mov_b32 v5, v146\n" \
  "v_mov_b32 v6, v147\n" \
  "s_nop 0\n" \
  "v_permlane16_swap_b32 v146, v5\n" \
  "v_permlane16_swap_b32 v147, v6\n" \
  "v_add_f32 v146, v146, v5\n" \
  "v_add_f32 v147, v147, v6\n" \
  "v_mov_b32 v5, v146\n" \
  "v_mov_b32 v6, v147\n" \
  "s_nop 0\n" \
  "v_permlane32_swap_b32 v146, v5\n" \
  "v_permlane32_swap_b32 v147, v6\n" \
  "v_add_f32 v146, v146, v5\n" \
  "v_add_f32 v147, v147, v6\n" \
  "v_fma_f32 v146, v160, v136, v146\n" \
  "v_fma_f32 v147, v162, v140, v147\n" \
  "v_rcp_f32 v146, v146\n" \
  "v_rcp_f32 v147, v147\n" \
  "v_mul_f32 v160, v160, v146\n" \
  "v_mul_f32 v162, v162, v147\n" \
  "v_mul_f32 v150, v150, v146\n" \
  "v_mul_f32 v151, v151, v146\n" \
  "v_mul_f32 v154, v154, v147\n" \
  "v_mul_f32 v155, v155, v147\n" \
  "v_cvt_pk_bf16_f32 v157, v150, v151\n" \
  "v_cvt_pk_bf16_f32 v159, v154, v155\n" \
  "v_pk_mul_f32 v[52:53], v[52:53], v[160:161] op_sel_hi:[1,0]\n" \
  "v_pk_mul_f32 v[54:55], v[54:55], v[160:161] op_sel_hi:[1,0]\n" \
  "v_pk_mul_f32 v[56:57], v[56:57], v[160:161] op_sel_hi:[1,0]\n" \
  "v_pk_mul_f32 v[58:59], v[58:59], v[160:161] op_sel_hi:[1,0]\n" \
  "v_pk_mul_f32 v[60:61], v[60:61], v[160:161] op_sel_hi:[1,0]\n" \
  "v_pk_mul_f32 v[62:63], v[62:63], v[160:161] op_sel_hi:[1,0]\n" \
  "v_pk_mul_f32 v[64:65], v[64:65], v[160:161] op_sel_hi:[1,0]\n" \
  "v_pk_mul_f32 v[66:67], v[66:67], v[160:161] op_sel_hi:[1,0]\n" \
  "v_pk_mul_f32 v[68:69], v[68:69], v[160:161] op_sel_hi:[1,0]\n" \
  "v_pk_mul_f32 v[70:71], v[70:71], v[160:161] op_sel_hi:[1,0]\n" \
  "v_pk_mul_f32 v[72:73], v[72:73], v[160:161] op_sel_hi:[1,0]\n" \
  "v_pk_mul_f32 v[74:75], v[74:75], v[160:161] op_sel_hi:[1,0]\n" \
  "v_pk_mul_f32 v[76:77], v[76:77], v[160:161] op_sel_hi:[1,0]\n" \
  "v_pk_mul_f32 v[78:79], v[78:79], v[160:161] op_sel_hi:[1,0]\n" \
  "v_pk_mul_f32 v[80:81], v[80:81], v[160:161] op_sel_hi:[1,0]\n" \
  "v_pk_mul_f32 v[82:83], v[82:83], v[160:161] op_sel_hi:[1,0]\n" \
  "v_pk_mul_f32 v[84:85], v[84:85], v[162:163] op_sel_hi:[1,0]\n" \
  "v_pk_mul_f32 v[86:87], v[86:87], v[162:163] op_sel_hi:[1,0]\n" \
  "v_pk_mul_f32 v[88:89], v[88:89], v[162:163] op_sel_hi:[1,0]\n" \
  "v_pk_mul_f32 v[90:91], v[90:91], v[162:163] op_sel_hi:[1,0]\n" \
  "v_pk_mul_f32 v[92:93], v[92:93], v[162:163] op_sel_hi:[1,0]\n" \
  "v_pk_mul_f32 v[94:95], v[94:95], v[162:163] op_sel_hi:[1,0]\n" \
  "v_pk_mul_f32 v[96:97], v[96:97], v[162:163] op_sel_hi:[1,0]\n" \
  "v_pk_mul_f32 v[98:99], v[98:99], v[162:163] op_sel_hi:[1,0]\n" \
  "v_pk_mul_f32 v[100:101], v[100:101], v[162:163] op_sel_hi:[1,0]\n" \
  "v_pk_mul_f32 v[102:103], v[102:103], v[162:163] op_sel_hi:[1,0]\n" \
  "v_pk_mul_f32 v[104:105], v[104:105], v[162:163] op_sel_hi:[1,0]\n" \
  "v_pk_mul_f32 v[106:107], v[106:107], v[162:163] op_sel_hi:[1,0]\n" \
  "v_pk_mul_f32 v[108:109], v[108:109], v[162:163] op_sel_hi:[1,0]\n" \
  "v_pk_mul_f32 v[110:111], v[110:111], v[162:163] op_sel_hi:[1,0]\n" \
  "v_pk_mul_f32 v[112:113], v[112:113], v[162:163] op_sel_hi:[1,0]\n" \
  "v_pk_mul_f32 v[114:115], v[114:115], v[162:163] op_sel_hi:[1,0]\n" \
  "s_waitcnt lgkmcnt(0)\n" \
  "v_mfma_f32_16x16x16_bf16 v[52:55], a[128:129], v[156:157], v[52:55]\n" \
  "v_mfma_f32_16x16x16_bf16 v[56:59], a[130:131], v[156:157], v[56:59]\n" \
  "v_mfma_f32_16x16x16_bf16 v[60:63], a[132:133], v[156:157], v[60:63]\n" \
  "v_mfma_f32_16x16x16_bf16 v[64:67], a[134:135], v[156:157], v[64:67]\n" \
  "v_mfma_f32_16x16x16_bf16 v[68:71], a[136:137], v[156:157], v[68:71]\n" \
  "v_cvt_pk_bf16_f32 v164, v52, v53\n" \
  "v_cvt_pk_bf16_f32 v165, v54, v55\n" \
  "v_mfma_f32_16x16x16_bf16 v[72:75], a[138:139], v[156:157], v[72:75]\n" \
  "v_cvt_pk_bf16_f32 v166, v56, v57\n" \
  "v_cvt_pk_bf16_f32 v167, v58, v59\n" \
  "buffer_store_dwordx4 v[164:167], v7, s[72:75], s86 offen offset:0 sc1\n" \
  "v_mfma_f32_16x16x16_bf16 v[76:79], a[140:141], v[156:157], v[76:79]\n" \
  "v_cvt_pk_bf16_f32 v168, v60, v61\n" \
  "v_cvt_pk_bf16_f32 v169, v62, v63\n" \
  "v_mfma_f32_16x16x16_bf16 v[80:83], a[142:143], v[156:157], v[80:83]\n" \
  "v_cvt_pk_bf16_f32 v170, v64, v65\n" \
  "v_cvt_pk_bf16_f32 v171, v66, v67\n" \
  "buffer_store_dwordx4 v[168:171], v7, s[72:75], s86 offen offset:1024 sc1\n" \
  "v_mfma_f32_16x16x16_bf16 v[84:87], a[128:129], v[158:159], v[84:87]\n" \
  "v_cvt_pk_bf16_f32 v172, v68, v69\n" \
  "v_cvt_pk_bf16_f32 v173, v70, v71\n" \
  "v_mfma_f32_16x16x16_bf16 v[88:91], a[130:131], v[158:159], v[88:91]\n" \
  "v_cvt_pk_bf16_f32 v174, v72, v73\n" \
  "v_cvt_pk_bf16_f32 v175, v74, v75\n" \
  "buffer_store_dwordx4 v[172:175], v7, s[72:75], s86 offen offset:2048 sc1\n" \
  "v_mfma_f32_16x16x16_bf16 v[92:95], a[132:133], v[158:159], v[92:95]\n" \
  "v_cvt_pk_bf16_f32 v176, v76, v77\n" \
  "v_cvt_pk_bf16_f32 v177, v78, v79\n" \
  "v_mfma_f32_16x16x16_bf16 v[96:99], a[134:135], v[158:159], v[96:99]\n" \
  "v_cvt_pk_bf16_f32 v178, v80, v81\n" \
  "v_cvt_pk_bf16_f32 v179, v82, v83\n" \
  "buffer_store_dwordx4 v[176:179], v7, s[72:75], s86 offen offset:3072 sc1\n" \
  "v_mfma_f32_16x16x16_bf16 v[100:103], a[136:137], v[158:159], v[100:103]\n" \
  "v_cvt_pk_bf16_f32 v180, v84, v85\n" \
  "v_cvt_pk_bf16_f32 v181, v86, v87\n" \
  "v_mfma_f32_16x16x16_bf16 v[104:107], a[138:139], v[158:159], v[104:107]\n" \
  "v_cvt_pk_bf16_f32 v182, v88, v89\n" \
  "v_cvt_pk_bf16_f32 v183, v90, v91\n" \
  "buffer_store_dwordx4 v[180:183], v17, s[72:75], s86 offen offset:0 sc1\n" \
  "v_mfma_f32_16x16x16_bf16 v[108:111], a[140:141], v[158:159], v[108:111]\n" \
  "v_cvt_pk_bf16_f32 v184, v92, v93\n" \
  "v_cvt_pk_bf16_f32 v185, v94, v95\n" \
  "v_mfma_f32_16x16x16_bf16 v[112:115], a[142:143], v[158:159], v[112:115]\n" \
  "v_cvt_pk_bf16_f32 v186, v96, v97\n" \
  "v_cvt_pk_bf16_f32 v187, v98, v99\n" \
  "buffer_store_dwordx4 v[184:187], v17, s[72:75], s86 offen offset:1024 sc1\n" \
  "v_cvt_pk_bf16_f32 v188, v100, v101\n" \
  "v_cvt_pk_bf16_f32 v189, v102, v103\n" \
  "v_cvt_pk_bf16_f32 v190, v104, v105\n" \
  "v_cvt_pk_bf16_f32 v191, v106, v107\n" \
  "buffer_store_dwordx4 v[188:191], v17, s[72:75], s86 offen offset:2048 sc1\n" \
  "s_nop 1\n" \
  "v_cvt_pk_bf16_f32 v192, v108, v109\n" \
  "v_cvt_pk_bf16_f32 v193, v110, v111\n" \
  "v_cvt_pk_bf16_f32 v194, v112, v113\n" \
  "v_cvt_pk_bf16_f32 v195, v114, v115\n" \
  "buffer_store_dwordx4 v[192:195], v17, s[72:75], s86 offen offset:3072 sc1\n" \
  "s_add_u32 s82, s82, 1\n" \
  "s_cmp_eq_u32 s82, 64\n" \
  "s_cbranch_scc1 LZ_%=\n" \
  "s_add_u32 s80, s82, 0\n" \
  "s_lshl_b32 s80, s80, 5\n" \
  "s_add_u32 s80, s80, %3\n" \
  "s_lshl_b32 s87, s80, 14\n" \
  "s_add_u32 s87, s87, " STR(EXP_B) "<<21\n" \
  "s_lshr_b32 s80, s87, 21\n" \
  "s_lshr_b32 s80, %4, s80\n" \
  "s_and_b32 s80, s80, 1\n" \
  "s_lshl_b32 s80, s80, 13\n" \
  "s_or_b32 s87, s87, s80\n" \
  "s_waitcnt vmcnt(8)\n" \
  "v_readlane_b32 s91, v14, 0\n" \
  "v_readlane_b32 s92, v14, 1\n" \
  "v_readlane_b32 s80, v14, 2\n" \
  "s_min_u32 s92, s92, s80\n" \
  "v_readlane_b32 s80, v14, 3\n" \
  "s_min_u32 s92, s92, s80\n" \
  "v_readlane_b32 s80, v14, 4\n" \
  "s_min_u32 s92, s92, s80\n" \
  "s_cmp_gt_u32 s91, s82\n" \
  "s_cselect_b32 s90, 1, 0\n" \
  "s_cselect_b32 s80, 0, 0x80000000\n" \
  "v_or_b32 v9, s80, v7\n" \
  "v_or_b32 v10, s80, v8\n" \
  "buffer_load_dwordx4 v[28:31], v9, s[68:71], s87 offen offset:0 sc1\n" \
  "buffer_load_dwordx4 v[32:35], v9, s[68:71], s87 offen offset:1024 sc1\n" \
  "buffer_load_dwordx4 v[36:39], v9, s[68:71], s87 offen offset:2048 sc1\n" \
  "buffer_load_dwordx4 v[40:43], v9, s[68:71], s87 offen offset:3072 sc1\n" \
  "buffer_load_dwordx4 v[44:47], v10, s[68:71], s87 offen offset:0 sc1\n" \
  "buffer_load_dwordx4 v[48:51], v10, s[68:71], s87 offen offset:1024 sc1\n" \
  "s_branch LA_%=\n" \
  "LG0_%=:\n" \
  "buffer_load_dword v14, v13, s[76:79], 0 offen sc1\n" \
  "s_waitcnt vmcnt(0)\n" \
  "v_readlane_b32 s91, v14, 0\n" \
  "v_readlane_b32 s92, v14, 1\n" \
  "v_readlane_b32 s80, v14, 2\n" \
  "s_min_u32 s92, s92, s80\n" \
  "v_readlane_b32 s80, v14, 3\n" \
  "s_min_u32 s92, s92, s80\n" \
  "v_readlane_b32 s80, v14, 4\n" \
  "s_min_u32 s92, s92, s80\n" \
  "s_cmp_ge_u32 s92, s81\n" \
  "s_cbranch_scc1 LH0_%=\n" \
  "s_sleep 4\n" \
  "s_add_u32 s88, s88, 1\n" \
  "s_cmp_lt_u32 s88, " STR(SPIN_CAP) "\n" \
  "s_cbranch_scc1 LG0_%=\n" \
  "v_mov_b32 v5, " STR(CW_DEAD) "*4\n" \
  "v_mov_b32 v6, 1\n" \
  "buffer_store_dword v6, v5, s[76:79], 0 offen sc1\n" \
  "s_branch LH0_%=\n" \
  "LW0_%=:\n" \
  "s_add_u32 s81, s82, 1\n" \
  "LQ0_%=:\n" \
  "buffer_load_dword v14, v13, s[76:79], 0 offen sc1\n" \
  "s_waitcnt vmcnt(0)\n" \
  "v_readlane_b32 s91, v14, 0\n" \
  "v_readlane_b32 s92, v14, 1\n" \
  "v_readlane_b32 s80, v14, 2\n" \
  "s_min_u32 s92, s92, s80\n" \
  "v_readlane_b32 s80, v14, 3\n" \
  "s_min_u32 s92, s92, s80\n" \
  "v_readlane_b32 s80, v14, 4\n" \
  "s_min_u32 s92, s92, s80\n" \
  "s_cmp_ge_u32 s91, s81\n" \
  "s_cbranch_scc1 LR0_%=\n" \
  "s_sleep 2\n" \
  "s_add_u32 s88, s88, 1\n" \
  "s_cmp_lt_u32 s88, " STR(SPIN_CAP) "\n" \
  "s_cbranch_scc1 LQ0_%=\n" \
  "v_mov_b32 v5, " STR(CW_DEAD) "*4\n" \
  "v_mov_b32 v6, 1\n" \
  "buffer_store_dword v6, v5, s[76:79], 0 offen sc1\n" \
  "s_branch LR0_%=\n" \
  "LR0_%=:\n" \
  "buffer_load_dwordx4 v[28:31], v7, s[68:71], s87 offen offset:0 sc1\n" \
  "buffer_load_dwordx4 v[32:35], v7, s[68:71], s87 offen offset:1024 sc1\n" \
  "buffer_load_dwordx4 v[36:39], v7, s[68:71], s87 offen offset:2048 sc1\n" \
  "buffer_load_dwordx4 v[40:43], v7, s[68:71], s87 offen offset:3072 sc1\n" \
  "buffer_load_dwordx4 v[44:47], v8, s[68:71], s87 offen offset:0 sc1\n" \
  "buffer_load_dwordx4 v[48:51], v8, s[68:71], s87 offen offset:1024 sc1\n" \
  "s_waitcnt vmcnt(0)\n" \
  "v_max3_u32 v5, v28, v29, v30\n" \
  "v_max3_u32 v5, v5, v31, v32\n" \
  "v_max3_u32 v5, v5, v33, v34\n" \
  "v_max3_u32 v5, v5, v35, v36\n" \
  "v_max3_u32 v5, v5, v37, v38\n" \
  "v_max3_u32 v5, v5, v39, v40\n" \
  "v_max3_u32 v5, v5, v41, v42\n" \
  "v_max3_u32 v5, v5, v43, v44\n" \
  "v_max3_u32 v5, v5, v45, v46\n" \
  "v_max3_u32 v5, v5, v47, v48\n" \
  "v_max3_u32 v5, v5, v49, v50\n" \
  "v_max_u32 v5, v5, v51\n" \
  "v_cmp_eq_u32 vcc, -1, v5\n" \
  "s_cbranch_vccz LU0_%=\n" \
  "s_sleep 1\n" \
  "s_add_u32 s88, s88, 1\n" \
  "s_cmp_lt_u32 s88, " STR(SPIN_CAP) "\n" \
  "s_cbranch_scc1 LR0_%=\n" \
  "v_mov_b32 v5, " STR(CW_DEAD) "*4\n" \
  "v_mov_b32 v6, 1\n" \
  "buffer_store_dword v6, v5, s[76:79], 0 offen sc1\n" \
  "s_branch LV0_%=\n" \
  "LV0_%=:\n" \
  "v_mov_b64 v[28:29], 0\n" \
  "v_mov_b64 v[30:31], 0\n" \
  "v_mov_b64 v[32:33], 0\n" \
  "v_mov_b64 v[34:35], 0\n" \
  "v_mov_b64 v[36:37], 0\n" \
  "v_mov_b64 v[38:39], 0\n" \
  "v_mov_b64 v[40:41], 0\n" \
  "v_mov_b64 v[42:43], 0\n" \
  "v_mov_b64 v[44:45], 0\n" \
  "v_mov_b64 v[46:47], 0\n" \
  "v_mov_b64 v[48:49], 0\n" \
  "v_mov_b64 v[50:51], 0\n" \
  "LU0_%=:\n" \
  "s_nop 1\n" \
  "s_branch LM0_%=\n" \
  "LG1_%=:\n" \
  "buffer_load_dword v14, v13, s[76:79], 0 offen sc1\n" \
  "s_waitcnt vmcnt(0)\n" \
  "v_readlane_b32 s91, v14, 0\n" \
  "v_readlane_b32 s92, v14, 1\n" \
  "v_readlane_b32 s80, v14, 2\n" \
  "s_min_u32 s92, s92, s80\n" \
  "v_readlane_b32 s80, v14, 3\n" \
  "s_min_u32 s92, s92, s80\n" \
  "v_readlane_b32 s80, v14, 4\n" \
  "s_min_u32 s92, s92, s80\n" \
  "s_cmp_ge_u32 s92, s81\n" \
  "s_cbranch_scc1 LH1_%=\n" \
  "s_sleep 4\n" \
  "s_add_u32 s88, s88, 1\n" \
  "s_cmp_lt_u32 s88, " STR(SPIN_CAP) "\n" \
  "s_cbranch_scc1 LG1_%=\n" \
  "v_mov_b32 v5, " STR(CW_DEAD) "*4\n" \
  "v_mov_b32 v6, 1\n" \
  "buffer_store_dword v6, v5, s[76:79], 0 offen sc1\n" \
  "s_branch LH1_%=\n" \
  "LW1_%=:\n" \
  "s_add_u32 s81, s82, 1\n" \
  "LQ1_%=:\n" \
  "buffer_load_dword v14, v13, s[76:79], 0 offen sc1\n" \
  "s_waitcnt vmcnt(0)\n" \
  "v_readlane_b32 s91, v14, 0\n" \
  "v_readlane_b32 s92, v14, 1\n" \
  "v_readlane_b32 s80, v14, 2\n" \
  "s_min_u32 s92, s92, s80\n" \
  "v_readlane_b32 s80, v14, 3\n" \
  "s_min_u32 s92, s92, s80\n" \
  "v_readlane_b32 s80, v14, 4\n" \
  "s_min_u32 s92, s92, s80\n" \
  "s_cmp_ge_u32 s91, s81\n" \
  "s_cbranch_scc1 LR1_%=\n" \
  "s_sleep 2\n" \
  "s_add_u32 s88, s88, 1\n" \
  "s_cmp_lt_u32 s88, " STR(SPIN_CAP) "\n" \
  "s_cbranch_scc1 LQ1_%=\n" \
  "v_mov_b32 v5, " STR(CW_DEAD) "*4\n" \
  "v_mov_b32 v6, 1\n" \
  "buffer_store_dword v6, v5, s[76:79], 0 offen sc1\n" \
  "s_branch LR1_%=\n" \
  "LR1_%=:\n" \
  "buffer_load_dwordx4 v[28:31], v7, s[68:71], s87 offen offset:0 sc1\n" \
  "buffer_load_dwordx4 v[32:35], v7, s[68:71], s87 offen offset:1024 sc1\n" \
  "buffer_load_dwordx4 v[36:39], v7, s[68:71], s87 offen offset:2048 sc1\n" \
  "buffer_load_dwordx4 v[40:43], v7, s[68:71], s87 offen offset:3072 sc1\n" \
  "buffer_load_dwordx4 v[44:47], v8, s[68:71], s87 offen offset:0 sc1\n" \
  "buffer_load_dwordx4 v[48:51], v8, s[68:71], s87 offen offset:1024 sc1\n" \
  "s_waitcnt vmcnt(0)\n" \
  "v_max3_u32 v5, v28, v29, v30\n" \
  "v_max3_u32 v5, v5, v31, v32\n" \
  "v_max3_u32 v5, v5, v33, v34\n" \
  "v_max3_u32 v5, v5, v35, v36\n" \
  "v_max3_u32 v5, v5, v37, v38\n" \
  "v_max3_u32 v5, v5, v39, v40\n" \
  "v_max3_u32 v5, v5, v41, v42\n" \
  "v_max3_u32 v5, v5, v43, v44\n" \
  "v_max3_u32 v5, v5, v45, v46\n" \
  "v_max3_u32 v5, v5, v47, v48\n" \
  "v_max3_u32 v5, v5, v49, v50\n" \
  "v_max_u32 v5, v5, v51\n" \
  "v_cmp_eq_u32 vcc, -1, v5\n" \
  "s_cbranch_vccz LU1_%=\n" \
  "s_sleep 1\n" \
  "s_add_u32 s88, s88, 1\n" \
  "s_cmp_lt_u32 s88, " STR(SPIN_CAP) "\n" \
  "s_cbranch_scc1 LR1_%=\n" \
  "v_mov_b32 v5, " STR(CW_DEAD) "*4\n" \
  "v_mov_b32 v6, 1\n" \
  "buffer_store_dword v6, v5, s[76:79], 0 offen sc1\n" \
  "s_branch LV1_%=\n" \
  "LV1_%=:\n" \
  "v_mov_b64 v[28:29], 0\n" \
  "v_mov_b64 v[30:31], 0\n" \
  "v_mov_b64 v[32:33], 0\n" \
  "v_mov_b64 v[34:35], 0\n" \
  "v_mov_b64 v[36:37], 0\n" \
  "v_mov_b64 v[38:39], 0\n" \
  "v_mov_b64 v[40:41], 0\n" \
  "v_mov_b64 v[42:43], 0\n" \
  "v_mov_b64 v[44:45], 0\n" \
  "v_mov_b64 v[46:47], 0\n" \
  "v_mov_b64 v[48:49], 0\n" \
  "v_mov_b64 v[50:51], 0\n" \
  "LU1_%=:\n" \
  "s_nop 1\n" \
  "s_branch LM1_%=\n" \
  "LZ_%=:\n" \
  "s_waitcnt vmcnt(0)\n" \
  "buffer_store_dword v16, v15, s[76:79], 0 offen sc1\n" \
  "s_waitcnt vmcnt(0)\n" \
  ""

#define CONV_ASM \
  "s_waitcnt lgkmcnt(0)\n" \
  "ds_read_b64 a[0:1], %2 offset:32\n" \
  "ds_read_b64 a[2:3], %2 offset:40\n" \
  "ds_read_b128 a[4:7], %3 offset:5040\n" \
  "ds_read_b128 a[8:11], %3 offset:2480\n" \
  "ds_read_b64 a[12:13], %2 offset:2080\n" \
  "ds_read_b64 a[14:15], %2 offset:2088\n" \
  "ds_read_b64 a[16:17], %2 offset:0\n" \
  "ds_read_b64 a[18:19], %2 offset:8\n" \
  "ds_read_b128 a[20:23], %3 offset:5072\n" \
  "ds_read_b128 a[24:27], %3 offset:2512\n" \
  "ds_read_b64 a[28:29], %2 offset:2048\n" \
  "ds_read_b64 a[30:31], %2 offset:2056\n" \
  "s_waitcnt lgkmcnt(6)\n" \
  "v_mfma_f32_32x32x16_bf16 %1, a[0:3], a[4:7], 0\n" \
  "ds_read_b64 a[32:33], %2 offset:96\n" \
  "ds_read_b64 a[34:35], %2 offset:104\n" \
  "v_mfma_f32_32x32x16_bf16 %0, a[0:3], a[8:11], 0\n" \
  "ds_read_b128 a[36:39], %3 offset:4960\n" \
  "ds_read_b128 a[40:43], %3 offset:2400\n" \
  "v_mfma_f32_32x32x16_bf16 %1, a[12:15], a[8:11], %1\n" \
  "ds_read_b64 a[44:45], %2 offset:2144\n" \
  "ds_read_b64 a[46:47], %2 offset:2152\n" \
  "s_waitcnt lgkmcnt(6)\n" \
  "v_mfma_f32_32x32x16_bf16 %1, a[16:19], a[20:23], %1\n" \
  "ds_read_b64 a[0:1], %2 offset:64\n" \
  "ds_read_b64 a[2:3], %2 offset:72\n" \
  "v_mfma_f32_32x32x16_bf16 %0, a[16:19], a[24:27], %0\n" \
  "ds_read_b128 a[4:7], %3 offset:4992\n" \
  "ds_read_b128 a[8:11], %3 offset:2432\n" \
  "v_mfma_f32_32x32x16_bf16 %1, a[28:31], a[24:27], %1\n" \
  "ds_read_b64 a[12:13], %2 offset:2112\n" \
  "ds_read_b64 a[14:15], %2 offset:2120\n" \
  "s_waitcnt lgkmcnt(6)\n" \
  "v_mfma_f32_32x32x16_bf16 %1, a[32:35], a[36:39], %1\n" \
  "ds_read_b64 a[16:17], %2 offset:160\n" \
  "ds_read_b64 a[18:19], %2 offset:168\n" \
  "v_mfma_f32_32x32x16_bf16 %0, a[32:35], a[40:43], %0\n" \
  "ds_read_b128 a[20:23], %3 offset:4880\n" \
  "ds_read_b128 a[24:27], %3 offset:2320\n" \
  "v_mfma_f32_32x32x16_bf16 %1, a[44:47], a[40:43], %1\n" \
  "ds_read_b64 a[28:29], %2 offset:2208\n" \
  "ds_read_b64 a[30:31], %2 offset:2216\n" \
  "s_waitcnt lgkmcnt(6)\n" \
  "v_mfma_f32_32x32x16_bf16 %1, a[0:3], a[4:7], %1\n" \
  "ds_read_b64 a[32:33], %2 offset:128\n" \
  "ds_read_b64 a[34:35], %2 offset:136\n" \
  "v_mfma_f32_32x32x16_bf16 %0, a[0:3], a[8:11], %0\n" \
  "ds_read_b128 a[36:39], %3 offset:4912\n" \
  "ds_read_b128 a[40:43], %3 offset:2352\n" \
  "v_mfma_f32_32x32x16_bf16 %1, a[12:15], a[8:11], %1\n" \
  "ds_read_b64 a[44:45], %2 offset:2176\n" \
  "ds_read_b64 a[46:47], %2 offset:2184\n" \
  "s_waitcnt lgkmcnt(6)\n" \
  "v_mfma_f32_32x32x16_bf16 %1, a[16:19], a[20:23], %1\n" \
  "ds_read_b64 a[0:1], %2 offset:224\n" \
  "ds_read_b64 a[2:3], %2 offset:232\n" \
  "v_mfma_f32_32x32x16_bf16 %0, a[16:19], a[24:27], %0\n" \
  "ds_read_b128 a[4:7], %3 offset:4800\n" \
  "ds_read_b128 a[8:11], %3 offset:2240\n" \
  "v_mfma_f32_32x32x16_bf16 %1, a[28:31], a[24:27], %1\n" \
  "ds_read_b64 a[12:13], %2 offset:2272\n" \
  "ds_read_b64 a[14:15], %2 offset:2280\n" \
  "s_waitcnt lgkmcnt(6)\n" \
  "v_mfma_f32_32x32x16_bf16 %1, a[32:35], a[36:39], %1\n" \
  "ds_read_b64 a[16:17], %2 offset:192\n" \
  "ds_read_b64 a[18:19], %2 offset:200\n" \
  "v_mfma_f32_32x32x16_bf16 %0, a[32:35], a[40:43], %0\n" \
  "ds_read_b128 a[20:23], %3 offset:4832\n" \
  "ds_read_b128 a[24:27], %3 offset:2272\n" \
  "v_mfma_f32_32x32x16_bf16 %1, a[44:47], a[40:43], %1\n" \
  "ds_read_b64 a[28:29], %2 offset:2240\n" \
  "ds_read_b64 a[30:31], %2 offset:2248\n" \
  "s_waitcnt lgkmcnt(6)\n" \
  "v_mfma_f32_32x32x16_bf16 %1, a[0:3], a[4:7], %1\n" \
  "ds_read_b64 a[32:33], %2 offset:288\n" \
  "ds_read_b64 a[34:35], %2 offset:296\n" \
  "v_mfma_f32_32x32x16_bf16 %0, a[0:3], a[8:11], %0\n" \
  "ds_read_b128 a[36:39], %3 offset:4720\n" \
  "ds_read_b128 a[40:43], %3 offset:2160\n" \
  "v_mfma_f32_32x32x16_bf16 %1, a[12:15], a[8:11], %1\n" \
  "ds_read_b64 a[44:45], %2 offset:2336\n" \
  "ds_read_b64 a[46:47], %2 offset:2344\n" \
  "s_waitcnt lgkmcnt(6)\n" \
  "v_mfma_f32_32x32x16_bf16 %1, a[16:19], a[20:23], %1\n" \
  "ds_read_b64 a[0:1], %2 offset:256\n" \
  "ds_read_b64 a[2:3], %2 offset:264\n" \
  "v_mfma_f32_32x32x16_bf16 %0, a[16:19], a[24:27], %0\n" \
  "ds_read_b128 a[4:7], %3 offset:4752\n" \
  "ds_read_b128 a[8:11], %3 offset:2192\n" \
  "v_mfma_f32_32x32x16_bf16 %1, a[28:31], a[24:27], %1\n" \
  "ds_read_b64 a[12:13], %2 offset:2304\n" \
  "ds_read_b64 a[14:15], %2 offset:2312\n" \
  "s_waitcnt lgkmcnt(6)\n" \
  "v_mfma_f32_32x32x16_bf16 %1, a[32:35], a[36:39], %1\n" \
  "ds_read_b64 a[16:17], %2 offset:352\n" \
  "ds_read_b64 a[18:19], %2 offset:360\n" \
  "v_mfma_f32_32x32x16_bf16 %0, a[32:35], a[40:43], %0\n" \
  "ds_read_b128 a[20:23], %3 offset:4640\n" \
  "ds_read_b128 a[24:27], %3 offset:2080\n" \
  "v_mfma_f32_32x32x16_bf16 %1, a[44:47], a[40:43], %1\n" \
  "ds_read_b64 a[28:29], %2 offset:2400\n" \
  "ds_read_b64 a[30:31], %2 offset:2408\n" \
  "s_waitcnt lgkmcnt(6)\n" \
  "v_mfma_f32_32x32x16_bf16 %1, a[0:3], a[4:7], %1\n" \
  "ds_read_b64 a[32:33], %2 offset:320\n" \
  "ds_read_b64 a[34:35], %2 offset:328\n" \
  "v_mfma_f32_32x32x16_bf16 %0, a[0:3], a[8:11], %0\n" \
  "ds_read_b128 a[36:39], %3 offset:4672\n" \
  "ds_read_b128 a[40:43], %3 offset:2112\n" \
  "v_mfma_f32_32x32x16_bf16 %1, a[12:15], a[8:11], %1\n" \
  "ds_read_b64 a[44:45], %2 offset:2368\n" \
  "ds_read_b64 a[46:47], %2 offset:2376\n" \
  "s_waitcnt lgkmcnt(6)\n" \
  "v_mfma_f32_32x32x16_bf16 %1, a[16:19], a[20:23], %1\n" \
  "ds_read_b64 a[0:1], %2 offset:416\n" \
  "ds_read_b64 a[2:3], %2 offset:424\n" \
  "v_mfma_f32_32x32x16_bf16 %0, a[16:19], a[24:27], %0\n" \
  "ds_read_b128 a[4:7], %3 offset:4560\n" \
  "ds_read_b128 a[8:11], %3 offset:2000\n" \
  "v_mfma_f32_32x32x16_bf16 %1, a[28:31], a[24:27], %1\n" \
  "ds_read_b64 a[12:13], %2 offset:2464\n" \
  "ds_read_b64 a[14:15], %2 offset:2472\n" \
  "s_waitcnt lgkmcnt(6)\n" \
  "v_mfma_f32_32x32x16_bf16 %1, a[32:35], a[36:39], %1\n" \
  "ds_read_b64 a[16:17], %2 offset:384\n" \
  "ds_read_b64 a[18:19], %2 offset:392\n" \
  "v_mfma_f32_32x32x16_bf16 %0, a[32:35], a[40:43], %0\n" \
  "ds_read_b128 a[20:23], %3 offset:4592\n" \
  "ds_read_b128 a[24:27], %3 offset:2032\n" \
  "v_mfma_f32_32x32x16_bf16 %1, a[44:47], a[40:43], %1\n" \
  "ds_read_b64 a[28:29], %2 offset:2432\n" \
  "ds_read_b64 a[30:31], %2 offset:2440\n" \
  "s_waitcnt lgkmcnt(6)\n" \
  "v_mfma_f32_32x32x16_bf16 %1, a[0:3], a[4:7], %1\n" \
  "ds_read_b64 a[32:33], %2 offset:480\n" \
  "ds_read_b64 a[34:35], %2 offset:488\n" \
  "v_mfma_f32_32x32x16_bf16 %0, a[0:3], a[8:11], %0\n" \
  "ds_read_b128 a[36:39], %3 offset:4480\n" \
  "ds_read_b128 a[40:43], %3 offset:1920\n" \
  "v_mfma_f32_32x32x16_bf16 %1, a[12:15], a[8:11], %1\n" \
  "ds_read_b64 a[44:45], %2 offset:2528\n" \
  "ds_read_b64 a[46:47], %2 offset:2536\n" \
  "s_waitcnt lgkmcnt(6)\n" \
  "v_mfma_f32_32x32x16_bf16 %1, a[16:19], a[20:23], %1\n" \
  "ds_read_b64 a[0:1], %2 offset:448\n" \
  "ds_read_b64 a[2:3], %2 offset:456\n" \
  "v_mfma_f32_32x32x16_bf16 %0, a[16:19], a[24:27], %0\n" \
  "ds_read_b128 a[4:7], %3 offset:4512\n" \
  "ds_read_b128 a[8:11], %3 offset:1952\n" \
  "v_mfma_f32_32x32x16_bf16 %1, a[28:31], a[24:27], %1\n" \
  "ds_read_b64 a[12:13], %2 offset:2496\n" \
  "ds_read_b64 a[14:15], %2 offset:2504\n" \
  "s_waitcnt lgkmcnt(6)\n" \
  "v_mfma_f32_32x32x16_bf16 %1, a[32:35], a[36:39], %1\n" \
  "ds_read_b64 a[16:17], %2 offset:544\n" \
  "ds_read_b64 a[18:19], %2 offset:552\n" \
  "v_mfma_f32_32x32x16_bf16 %0, a[32:35], a[40:43], %0\n" \
  "ds_read_b128 a[20:23], %3 offset:4400\n" \
  "ds_read_b128 a[24:27], %3 offset:1840\n" \
  "v_mfma_f32_32x32x16_bf16 %1, a[44:47], a[40:43], %1\n" \
  "ds_read_b64 a[28:29], %2 offset:2592\n" \
  "ds_read_b64 a[30:31], %2 offset:2600\n" \
  "s_waitcnt lgkmcnt(6)\n" \
  "v_mfma_f32_32x32x16_bf16 %1, a[0:3], a[4:7], %1\n" \
  "ds_read_b64 a[32:33], %2 offset:512\n" \
  "ds_read_b64 a[34:35], %2 offset:520\n" \
  "v_mfma_f32_32x32x16_bf16 %0, a[0:3], a[8:11], %0\n" \
  "ds_read_b128 a[36:39], %3 offset:4432\n" \
  "ds_read_b128 a[40:43], %3 offset:1872\n" \
  "v_mfma_f32_32x32x16_bf16 %1, a[12:15], a[8:11], %1\n" \
  "ds_read_b64 a[44:45], %2 offset:2560\n" \
  "ds_read_b64 a[46:47], %2 offset:2568\n" \
  "s_waitcnt lgkmcnt(6)\n" \
  "v_mfma_f32_32x32x16_bf16 %1, a[16:19], a[20:23], %1\n" \
  "ds_read_b64 a[0:1], %2 offset:608\n" \
  "ds_read_b64 a[2:3], %2 offset:616\n" \
  "v_mfma_f32_32x32x16_bf16 %0, a[16:19], a[24:27], %0\n" \
  "ds_read_b128 a[4:7], %3 offset:4320\n" \
  "ds_read_b128 a[8:11], %3 offset:1760\n" \
  "v_mfma_f32_32x32x16_bf16 %1, a[28:31], a[24:27], %1\n" \
  "ds_read_b64 a[12:13], %2 offset:2656\n" \
  "ds_read_b64 a[14:15], %2 offset:2664\n" \
  "s_waitcnt lgkmcnt(6)\n" \
  "v_mfma_f32_32x32x16_bf16 %1, a[32:35], a[36:39], %1\n" \
  "ds_read_b64 a[16:17], %2 offset:576\n" \
  "ds_read_b64 a[18:19], %2 offset:584\n" \
  "v_mfma_f32_32x32x16_bf16 %0, a[32:35], a[40:43], %0\n" \
  "ds_read_b128 a[20:23], %3 offset:4352\n" \
  "ds_read_b128 a[24:27], %3 offset:1792\n" \
  "v_mfma_f32_32x32x16_bf16 %1, a[44:47], a[40:43], %1\n" \
  "ds_read_b64 a[28:29], %2 offset:2624\n" \
  "ds_read_b64 a[30:31], %2 offset:2632\n" \
  "s_waitcnt lgkmcnt(6)\n" \
  "v_mfma_f32_32x32x16_bf16 %1, a[0:3], a[4:7], %1\n" \
  "ds_read_b64 a[32:33], %2 offset:672\n" \
  "ds_read_b64 a[34:35], %2 offset:680\n" \
  "v_mfma_f32_32x32x16_bf16 %0, a[0:3], a[8:11], %0\n" \
  "ds_read_b128 a[36:39], %3 offset:4240\n" \
  "ds_read_b128 a[40:43], %3 offset:1680\n" \
  "v_mfma_f32_32x32x16_bf16 %1, a[12:15], a[8:11], %1\n" \
  "ds_read_b64 a[44:45], %2 offset:2720\n" \
  "ds_read_b64 a[46:47], %2 offset:2728\n" \
  "s_waitcnt lgkmcnt(6)\n" \
  "v_mfma_f32_32x32x16_bf16 %1, a[16:19], a[20:23], %1\n" \
  "ds_read_b64 a[0:1], %2 offset:640\n" \
  "ds_read_b64 a[2:3], %2 offset:648\n" \
  "v_mfma_f32_32x32x16_bf16 %0, a[16:19], a[24:27], %0\n" \
  "ds_read_b128 a[4:7], %3 offset:4272\n" \
  "ds_read_b128 a[8:11], %3 offset:1712\n" \
  "v_mfma_f32_32x32x16_bf16 %1, a[28:31], a[24:27], %1\n" \
  "ds_read_b64 a[12:13], %2 offset:2688\n" \
  "ds_read_b64 a[14:15], %2 offset:2696\n" \
  "s_waitcnt lgkmcnt(6)\n" \
  "v_mfma_f32_32x32x16_bf16 %1, a[32:35], a[36:39], %1\n" \
  "ds_read_b64 a[16:17], %2 offset:736\n" \
  "ds_read_b64 a[18:19], %2 offset:744\n" \
  "v_mfma_f32_32x32x16_bf16 %0, a[32:35], a[40:43], %0\n" \
  "ds_read_b128 a[20:23], %3 offset:4160\n" \
  "ds_read_b128 a[24:27], %3 offset:1600\n" \
  "v_mfma_f32_32x32x16_bf16 %1, a[44:47], a[40:43], %1\n" \
  "ds_read_b64 a[28:29], %2 offset:2784\n" \
  "ds_read_b64 a[30:31], %2 offset:2792\n" \
  "s_waitcnt lgkmcnt(6)\n" \
  "v_mfma_f32_32x32x16_bf16 %1, a[0:3], a[4:7], %1\n" \
  "ds_read_b64 a[32:33], %2 offset:704\n" \
  "ds_read_b64 a[34:35], %2 offset:712\n" \
  "v_mfma_f32_32x32x16_bf16 %0, a[0:3], a[8:11], %0\n" \
  "ds_read_b128 a[36:39], %3 offset:4192\n" \
  "ds_read_b128 a[40:43], %3 offset:1632\n" \
  "v_mfma_f32_32x32x16_bf16 %1, a[12:15], a[8:11], %1\n" \
  "ds_read_b64 a[44:45], %2 offset:2752\n" \
  "ds_read_b64 a[46:47], %2 offset:2760\n" \
  "s_waitcnt lgkmcnt(6)\n" \
  "v_mfma_f32_32x32x16_bf16 %1, a[16:19], a[20:23], %1\n" \
  "ds_read_b64 a[0:1], %2 offset:800\n" \
  "ds_read_b64 a[2:3], %2 offset:808\n" \
  "v_mfma_f32_32x32x16_bf16 %0, a[16:19], a[24:27], %0\n" \
  "ds_read_b128 a[4:7], %3 offset:4080\n" \
  "ds_read_b128 a[8:11], %3 offset:1520\n" \
  "v_mfma_f32_32x32x16_bf16 %1, a[28:31], a[24:27], %1\n" \
  "ds_read_b64 a[12:13], %2 offset:2848\n" \
  "ds_read_b64 a[14:15], %2 offset:2856\n" \
  "s_waitcnt lgkmcnt(6)\n" \
  "v_mfma_f32_32x32x16_bf16 %1, a[32:35], a[36:39], %1\n" \
  "ds_read_b64 a[16:17], %2 offset:768\n" \
  "ds_read_b64 a[18:19], %2 offset:776\n" \
  "v_mfma_f32_32x32x16_bf16 %0, a[32:35], a[40:43], %0\n" \
  "ds_read_b128 a[20:23], %3 offset:4112\n" \
  "ds_read_b128 a[24:27], %3 offset:1552\n" \
  "v_mfma_f32_32x32x16_bf16 %1, a[44:47], a[40:43], %1\n" \
  "ds_read_b64 a[28:29], %2 offset:2816\n" \
  "ds_read_b64 a[30:31], %2 offset:2824\n" \
  "s_waitcnt lgkmcnt(6)\n" \
  "v_mfma_f32_32x32x16_bf16 %1, a[0:3], a[4:7], %1\n" \
  "ds_read_b64 a[32:33], %2 offset:864\n" \
  "ds_read_b64 a[34:35], %2 offset:872\n" \
  "v_mfma_f32_32x32x16_bf16 %0, a[0:3], a[8:11], %0\n" \
  "ds_read_b128 a[36:39], %3 offset:4000\n" \
  "ds_read_b128 a[40:43], %3 offset:1440\n" \
  "v_mfma_f32_32x32x16_bf16 %1, a[12:15], a[8:11], %1\n" \
  "ds_read_b64 a[44:45], %2 offset:2912\n" \
  "ds_read_b64 a[46:47], %2 offset:2920\n" \
  "s_waitcnt lgkmcnt(6)\n" \
  "v_mfma_f32_32x32x16_bf16 %1, a[16:19], a[20:23], %1\n" \
  "ds_read_b64 a[0:1], %2 offset:832\n" \
  "ds_read_b64 a[2:3], %2 offset:840\n" \
  "v_mfma_f32_32x32x16_bf16 %0, a[16:19], a[24:27], %0\n" \
  "ds_read_b128 a[4:7], %3 offset:4032\n" \
  "ds_read_b128 a[8:11], %3 offset:1472\n" \
  "v_mfma_f32_32x32x16_bf16 %1, a[28:31], a[24:27], %1\n" \
  "ds_read_b64 a[12:13], %2 offset:2880\n" \
  "ds_read_b64 a[14:15], %2 offset:2888\n" \
  "s_waitcnt lgkmcnt(6)\n" \
  "v_mfma_f32_32x32x16_bf16 %1, a[32:35], a[36:39], %1\n" \
  "ds_read_b64 a[16:17], %2 offset:928\n" \
  "ds_read_b64 a[18:19], %2 offset:936\n" \
  "v_mfma_f32_32x32x16_bf16 %0, a[32:35], a[40:43], %0\n" \
  "ds_read_b128 a[20:23], %3 offset:3920\n" \
  "ds_read_b128 a[24:27], %3 offset:1360\n" \
  "v_mfma_f32_32x32x16_bf16 %1, a[44:47], a[40:43], %1\n" \
  "ds_read_b64 a[28:29], %2 offset:2976\n" \
  "ds_read_b64 a[30:31], %2 offset:2984\n" \
  "s_waitcnt lgkmcnt(6)\n" \
  "v_mfma_f32_32x32x16_bf16 %1, a[0:3], a[4:7], %1\n" \
  "ds_read_b64 a[32:33], %2 offset:896\n" \
  "ds_read_b64 a[34:35], %2 offset:904\n" \
  "v_mfma_f32_32x32x16_bf16 %0, a[0:3], a[8:11], %0\n" \
  "ds_read_b128 a[36:39], %3 offset:3952\n" \
  "ds_read_b128 a[40:43], %3 offset:1392\n" \
  "v_mfma_f32_32x32x16_bf16 %1, a[12:15], a[8:11], %1\n" \
  "ds_read_b64 a[44:45], %2 offset:2944\n" \
  "ds_read_b64 a[46:47], %2 offset:2952\n" \
  "s_waitcnt lgkmcnt(6)\n" \
  "v_mfma_f32_32x32x16_bf16 %1, a[16:19], a[20:23], %1\n" \
  "ds_read_b64 a[0:1], %2 offset:992\n" \
  "ds_read_b64 a[2:3], %2 offset:1000\n" \
  "v_mfma_f32_32x32x16_bf16 %0, a[16:19], a[24:27], %0\n" \
  "ds_read_b128 a[4:7], %3 offset:3840\n" \
  "ds_read_b128 a[8:11], %3 offset:1280\n" \
  "v_mfma_f32_32x32x16_bf16 %1, a[28:31], a[24:27], %1\n" \
  "ds_read_b64 a[12:13], %2 offset:3040\n" \
  "ds_read_b64 a[14:15], %2 offset:3048\n" \
  "s_waitcnt lgkmcnt(6)\n" \
  "v_mfma_f32_32x32x16_bf16 %1, a[32:35], a[36:39], %1\n" \
  "ds_read_b64 a[16:17], %2 offset:960\n" \
  "ds_read_b64 a[18:19], %2 offset:968\n" \
  "v_mfma_f32_32x32x16_bf16 %0, a[32:35], a[40:43], %0\n" \
  "ds_read_b128 a[20:23], %3 offset:3872\n" \
  "ds_read_b128 a[24:27], %3 offset:1312\n" \
  "v_mfma_f32_32x32x16_bf16 %1, a[44:47], a[40:43], %1\n" \
  "ds_read_b64 a[28:29], %2 offset:3008\n" \
  "ds_read_b64 a[30:31], %2 offset:3016\n" \
  "s_waitcnt lgkmcnt(6)\n" \
  "v_mfma_f32_32x32x16_bf16 %1, a[0:3], a[4:7], %1\n" \
  "ds_read_b64 a[32:33], %2 offset:1056\n" \
  "ds_read_b64 a[34:35], %2 offset:1064\n" \
  "v_mfma_f32_32x32x16_bf16 %0, a[0:3], a[8:11], %0\n" \
  "ds_read_b128 a[36:39], %3 offset:3760\n" \
  "ds_read_b128 a[40:43], %3 offset:1200\n" \
  "v_mfma_f32_32x32x16_bf16 %1, a[12:15], a[8:11], %1\n" \
  "ds_read_b64 a[44:45], %2 offset:3104\n" \
  "ds_read_b64 a[46:47], %2 offset:3112\n" \
  "s_waitcnt lgkmcnt(6)\n" \
  "v_mfma_f32_32x32x16_bf16 %1, a[16:19], a[20:23], %1\n" \
  "ds_read_b64 a[0:1], %2 offset:1024\n" \
  "ds_read_b64 a[2:3], %2 offset:1032\n" \
  "v_mfma_f32_32x32x16_bf16 %0, a[16:19], a[24:27], %0\n" \
  "ds_read_b128 a[4:7], %3 offset:3792\n" \
  "ds_read_b128 a[8:11], %3 offset:1232\n" \
  "v_mfma_f32_32x32x16_bf16 %1, a[28:31], a[24:27], %1\n" \
  "ds_read_b64 a[12:13], %2 offset:3072\n" \
  "ds_read_b64 a[14:15], %2 offset:3080\n" \
  "s_waitcnt lgkmcnt(6)\n" \
  "v_mfma_f32_32x32x16_bf16 %1, a[32:35], a[36:39], %1\n" \
  "ds_read_b64 a[16:17], %2 offset:1120\n" \
  "ds_read_b64 a[18:19], %2 offset:1128\n" \
  "v_mfma_f32_32x32x16_bf16 %0, a[32:35], a[40:43], %0\n" \
  "ds_read_b128 a[20:23], %3 offset:3680\n" \
  "ds_read_b128 a[24:27], %3 offset:1120\n" \
  "v_mfma_f32_32x32x16_bf16 %1, a[44:47], a[40:43], %1\n" \
  "ds_read_b64 a[28:29], %2 offset:3168\n" \
  "ds_read_b64 a[30:31], %2 offset:3176\n" \
  "s_waitcnt lgkmcnt(6)\n" \
  "v_mfma_f32_32x32x16_bf16 %1, a[0:3], a[4:7], %1\n" \
  "ds_read_b64 a[32:33], %2 offset:1088\n" \
  "ds_read_b64 a[34:35], %2 offset:1096\n" \
  "v_mfma_f32_32x32x16_bf16 %0, a[0:3], a[8:11], %0\n" \
  "ds_read_b128 a[36:39], %3 offset:3712\n" \
  "ds_read_b128 a[40:43], %3 offset:1152\n" \
  "v_mfma_f32_32x32x16_bf16 %1, a[12:15], a[8:11], %1\n" \
  "ds_read_b64 a[44:45], %2 offset:3136\n" \
  "ds_read_b64 a[46:47], %2 offset:3144\n" \
  "s_waitcnt lgkmcnt(6)\n" \
  "v_mfma_f32_32x32x16_bf16 %1, a[16:19], a[20:23], %1\n" \
  "ds_read_b64 a[0:1], %2 offset:1184\n" \
  "ds_read_b64 a[2:3], %2 offset:1192\n" \
  "v_mfma_f32_32x32x16_bf16 %0, a[16:19], a[24:27], %0\n" \
  "ds_read_b128 a[4:7], %3 offset:3600\n" \
  "ds_read_b128 a[8:11], %3 offset:1040\n" \
  "v_mfma_f32_32x32x16_bf16 %1, a[28:31], a[24:27], %1\n" \
  "ds_read_b64 a[12:13], %2 offset:3232\n" \
  "ds_read_b64 a[14:15], %2 offset:3240\n" \
  "s_waitcnt lgkmcnt(6)\n" \
  "v_mfma_f32_32x32x16_bf16 %1, a[32:35], a[36:39], %1\n" \
  "ds_read_b64 a[16:17], %2 offset:1152\n" \
  "ds_read_b64 a[18:19], %2 offset:1160\n" \
  "v_mfma_f32_32x32x16_bf16 %0, a[32:35], a[40:43], %0\n" \
  "ds_read_b128 a[20:23], %3 offset:3632\n" \
  "ds_read_b128 a[24:27], %3 offset:1072\n" \
  "v_mfma_f32_32x32x16_bf16 %1, a[44:47], a[40:43], %1\n" \
  "ds_read_b64 a[28:29], %2 offset:3200\n" \
  "ds_read_b64 a[30:31], %2 offset:3208\n" \
  "s_waitcnt lgkmcnt(6)\n" \
  "v_mfma_f32_32x32x16_bf16 %1, a[0:3], a[4:7], %1\n" \
  "ds_read_b64 a[32:33], %2 offset:1248\n" \
  "ds_read_b64 a[34:35], %2 offset:1256\n" \
  "v_mfma_f32_32x32x16_bf16 %0, a[0:3], a[8:11], %0\n" \
  "ds_read_b128 a[36:39], %3 offset:3520\n" \
  "ds_read_b128 a[40:43], %3 offset:960\n" \
  "v_mfma_f32_32x32x16_bf16 %1, a[12:15], a[8:11], %1\n" \
  "ds_read_b64 a[44:45], %2 offset:3296\n" \
  "ds_read_b64 a[46:47], %2 offset:3304\n" \
  "s_waitcnt lgkmcnt(6)\n" \
  "v_mfma_f32_32x32x16_bf16 %1, a[16:19], a[20:23], %1\n" \
  "ds_read_b64 a[0:1], %2 offset:1216\n" \
  "ds_read_b64 a[2:3], %2 offset:1224\n" \
  "v_mfma_f32_32x32x16_bf16 %0, a[16:19], a[24:27], %0\n" \
  "ds_read_b128 a[4:7], %3 offset:3552\n" \
  "ds_read_b128 a[8:11], %3 offset:992\n" \
  "v_mfma_f32_32x32x16_bf16 %1, a[28:31], a[24:27], %1\n" \
  "ds_read_b64 a[12:13], %2 offset:3264\n" \
  "ds_read_b64 a[14:15], %2 offset:3272\n" \
  "s_waitcnt lgkmcnt(6)\n" \
  "v_mfma_f32_32x32x16_bf16 %1, a[32:35], a[36:39], %1\n" \
  "ds_read_b64 a[16:17], %2 offset:1312\n" \
  "ds_read_b64 a[18:19], %2 offset:1320\n" \
  "v_mfma_f32_32x32x16_bf16 %0, a[32:35], a[40:43], %0\n" \
  "ds_read_b128 a[20:23], %3 offset:3440\n" \
  "ds_read_b128 a[24:27], %3 offset:880\n" \
  "v_mfma_f32_32x32x16_bf16 %1, a[44:47], a[40:43], %1\n" \
  "ds_read_b64 a[28:29], %2 offset:3360\n" \
  "ds_read_b64 a[30:31], %2 offset:3368\n" \
  "s_waitcnt lgkmcnt(6)\n" \
  "v_mfma_f32_32x32x16_bf16 %1, a[0:3], a[4:7], %1\n" \
  "ds_read_b64 a[32:33], %2 offset:1280\n" \
  "ds_read_b64 a[34:35], %2 offset:1288\n" \
  "v_mfma_f32_32x32x16_bf16 %0, a[0:3], a[8:11], %0\n" \
  "ds_read_b128 a[36:39], %3 offset:3472\n" \
  "ds_read_b128 a[40:43], %3 offset:912\n" \
  "v_mfma_f32_32x32x16_bf16 %1, a[12:15], a[8:11], %1\n" \
  "ds_read_b64 a[44:45], %2 offset:3328\n" \
  "ds_read_b64 a[46:47], %2 offset:3336\n" \
  "s_waitcnt lgkmcnt(6)\n" \
  "v_mfma_f32_32x32x16_bf16 %1, a[16:19], a[20:23], %1\n" \
  "ds_read_b64 a[0:1], %2 offset:1376\n" \
  "ds_read_b64 a[2:3], %2 offset:1384\n" \
  "v_mfma_f32_32x32x16_bf16 %0, a[16:19], a[24:27], %0\n" \
  "ds_read_b128 a[4:7], %3 offset:3360\n" \
  "ds_read_b128 a[8:11], %3 offset:800\n" \
  "v_mfma_f32_32x32x16_bf16 %1, a[28:31], a[24:27], %1\n" \
  "ds_read_b64 a[12:13], %2 offset:3424\n" \
  "ds_read_b64 a[14:15], %2 offset:3432\n" \
  "s_waitcnt lgkmcnt(6)\n" \
  "v_mfma_f32_32x32x16_bf16 %1, a[32:35], a[36:39], %1\n" \
  "ds_read_b64 a[16:17], %2 offset:1344\n" \
  "ds_read_b64 a[18:19], %2 offset:1352\n" \
  "v_mfma_f32_32x32x16_bf16 %0, a[32:35], a[40:43], %0\n" \
  "ds_read_b128 a[20:23], %3 offset:3392\n" \
  "ds_read_b128 a[24:27], %3 offset:832\n" \
  "v_mfma_f32_32x32x16_bf16 %1, a[44:47], a[40:43], %1\n" \
  "ds_read_b64 a[28:29], %2 offset:3392\n" \
  "ds_read_b64 a[30:31], %2 offset:3400\n" \
  "s_waitcnt lgkmcnt(6)\n" \
  "v_mfma_f32_32x32x16_bf16 %1, a[0:3], a[4:7], %1\n" \
  "ds_read_b64 a[32:33], %2 offset:1440\n" \
  "ds_read_b64 a[34:35], %2 offset:1448\n" \
  "v_mfma_f32_32x32x16_bf16 %0, a[0:3], a[8:11], %0\n" \
  "ds_read_b128 a[36:39], %3 offset:3280\n" \
  "ds_read_b128 a[40:43], %3 offset:720\n" \
  "v_mfma_f32_32x32x16_bf16 %1, a[12:15], a[8:11], %1\n" \
  "ds_read_b64 a[44:45], %2 offset:3488\n" \
  "ds_read_b64 a[46:47], %2 offset:3496\n" \
  "s_waitcnt lgkmcnt(6)\n" \
  "v_mfma_f32_32x32x16_bf16 %1, a[16:19], a[20:23], %1\n" \
  "ds_read_b64 a[0:1], %2 offset:1408\n" \
  "ds_read_b64 a[2:3], %2 offset:1416\n" \
  "v_mfma_f32_32x32x16_bf16 %0, a[16:19], a[24:27], %0\n" \
  "ds_read_b128 a[4:7], %3 offset:3312\n" \
  "ds_read_b128 a[8:11], %3 offset:752\n" \
  "v_mfma_f32_32x32x16_bf16 %1, a[28:31], a[24:27], %1\n" \
  "ds_read_b64 a[12:13], %2 offset:3456\n" \
  "ds_read_b64 a[14:15], %2 offset:3464\n" \
  "s_waitcnt lgkmcnt(6)\n" \
  "v_mfma_f32_32x32x16_bf16 %1, a[32:35], a[36:39], %1\n" \
  "ds_read_b64 a[16:17], %2 offset:1504\n" \
  "ds_read_b64 a[18:19], %2 offset:1512\n" \
  "v_mfma_f32_32x32x16_bf16 %0, a[32:35], a[40:43], %0\n" \
  "ds_read_b128 a[20:23], %3 offset:3200\n" \
  "ds_read_b128 a[24:27], %3 offset:640\n" \
  "v_mfma_f32_32x32x16_bf16 %1, a[44:47], a[40:43], %1\n" \
  "ds_read_b64 a[28:29], %2 offset:3552\n" \
  "ds_read_b64 a[30:31], %2 offset:3560\n" \
  "s_waitcnt lgkmcnt(6)\n" \
  "v_mfma_f32_32x32x16_bf16 %1, a[0:3], a[4:7], %1\n" \
  "ds_read_b64 a[32:33], %2 offset:1472\n" \
  "ds_read_b64 a[34:35], %2 offset:1480\n" \
  "v_mfma_f32_32x32x16_bf16 %0, a[0:3], a[8:11], %0\n" \
  "ds_read_b128 a[36:39], %3 offset:3232\n" \
  "ds_read_b128 a[40:43], %3 offset:672\n" \
  "v_mfma_f32_32x32x16_bf16 %1, a[12:15], a[8:11], %1\n" \
  "ds_read_b64 a[44:45], %2 offset:3520\n" \
  "ds_read_b64 a[46:47], %2 offset:3528\n" \
  "s_waitcnt lgkmcnt(6)\n" \
  "v_mfma_f32_32x32x16_bf16 %1, a[16:19], a[20:23], %1\n" \
  "ds_read_b64 a[0:1], %2 offset:1568\n" \
  "ds_read_b64 a[2:3], %2 offset:1576\n" \
  "v_mfma_f32_32x32x16_bf16 %0, a[16:19], a[24:27], %0\n" \
  "ds_read_b128 a[4:7], %3 offset:3120\n" \
  "ds_read_b128 a[8:11], %3 offset:560\n" \
  "v_mfma_f32_32x32x16_bf16 %1, a[28:31], a[24:27], %1\n" \
  "ds_read_b64 a[12:13], %2 offset:3616\n" \
  "ds_read_b64 a[14:15], %2 offset:3624\n" \
  "s_waitcnt lgkmcnt(6)\n" \
  "v_mfma_f32_32x32x16_bf16 %1, a[32:35], a[36:39], %1\n" \
  "ds_read_b64 a[16:17], %2 offset:1536\n" \
  "ds_read_b64 a[18:19], %2 offset:1544\n" \
  "v_mfma_f32_32x32x16_bf16 %0, a[32:35], a[40:43], %0\n" \
  "ds_read_b128 a[20:23], %3 offset:3152\n" \
  "ds_read_b128 a[24:27], %3 offset:592\n" \
  "v_mfma_f32_32x32x16_bf16 %1, a[44:47], a[40:43], %1\n" \
  "ds_read_b64 a[28:29], %2 offset:3584\n" \
  "ds_read_b64 a[30:31], %2 offset:3592\n" \
  "s_waitcnt lgkmcnt(6)\n" \
  "v_mfma_f32_32x32x16_bf16 %1, a[0:3], a[4:7], %1\n" \
  "ds_read_b64 a[32:33], %2 offset:1632\n" \
  "ds_read_b64 a[34:35], %2 offset:1640\n" \
  "v_mfma_f32_32x32x16_bf16 %0, a[0:3], a[8:11], %0\n" \
  "ds_read_b128 a[36:39], %3 offset:3040\n" \
  "ds_read_b128 a[40:43], %3 offset:480\n" \
  "v_mfma_f32_32x32x16_bf16 %1, a[12:15], a[8:11], %1\n" \
  "ds_read_b64 a[44:45], %2 offset:3680\n" \
  "ds_read_b64 a[46:47], %2 offset:3688\n" \
  "s_waitcnt lgkmcnt(6)\n" \
  "v_mfma_f32_32x32x16_bf16 %1, a[16:19], a[20:23], %1\n" \
  "ds_read_b64 a[0:1], %2 offset:1600\n" \
  "ds_read_b64 a[2:3], %2 offset:1608\n" \
  "v_mfma_f32_32x32x16_bf16 %0, a[16:19], a[24:27], %0\n" \
  "ds_read_b128 a[4:7], %3 offset:3072\n" \
  "ds_read_b128 a[8:11], %3 offset:512\n" \
  "v_mfma_f32_32x32x16_bf16 %1, a[28:31], a[24:27], %1\n" \
  "ds_read_b64 a[12:13], %2 offset:3648\n" \
  "ds_read_b64 a[14:15], %2 offset:3656\n" \
  "s_waitcnt lgkmcnt(6)\n" \
  "v_mfma_f32_32x32x16_bf16 %1, a[32:35], a[36:39], %1\n" \
  "ds_read_b64 a[16:17], %2 offset:1696\n" \
  "ds_read_b64 a[18:19], %2 offset:1704\n" \
  "v_mfma_f32_32x32x16_bf16 %0, a[32:35], a[40:43], %0\n" \
  "ds_read_b128 a[20:23], %3 offset:2960\n" \
  "ds_read_b128 a[24:27], %3 offset:400\n" \
  "v_mfma_f32_32x32x16_bf16 %1, a[44:47], a[40:43], %1\n" \
  "ds_read_b64 a[28:29], %2 offset:3744\n" \
  "ds_read_b64 a[30:31], %2 offset:3752\n" \
  "s_waitcnt lgkmcnt(6)\n" \
  "v_mfma_f32_32x32x16_bf16 %1, a[0:3], a[4:7], %1\n" \
  "ds_read_b64 a[32:33], %2 offset:1664\n" \
  "ds_read_b64 a[34:35], %2 offset:1672\n" \
  "v_mfma_f32_32x32x16_bf16 %0, a[0:3], a[8:11], %0\n" \
  "ds_read_b128 a[36:39], %3 offset:2992\n" \
  "ds_read_b128 a[40:43], %3 offset:432\n" \
  "v_mfma_f32_32x32x16_bf16 %1, a[12:15], a[8:11], %1\n" \
  "ds_read_b64 a[44:45], %2 offset:3712\n" \
  "ds_read_b64 a[46:47], %2 offset:3720\n" \
  "s_waitcnt lgkmcnt(6)\n" \
  "v_mfma_f32_32x32x16_bf16 %1, a[16:19], a[20:23], %1\n" \
  "ds_read_b64 a[0:1], %2 offset:1760\n" \
  "ds_read_b64 a[2:3], %2 offset:1768\n" \
  "v_mfma_f32_32x32x16_bf16 %0, a[16:19], a[24:27], %0\n" \
  "ds_read_b128 a[4:7], %3 offset:2880\n" \
  "ds_read_b128 a[8:11], %3 offset:320\n" \
  "v_mfma_f32_32x32x16_bf16 %1, a[28:31], a[24:27], %1\n" \
  "ds_read_b64 a[12:13], %2 offset:3808\n" \
  "ds_read_b64 a[14:15], %2 offset:3816\n" \
  "s_waitcnt lgkmcnt(6)\n" \
  "v_mfma_f32_32x32x16_bf16 %1, a[32:35], a[36:39], %1\n" \
  "ds_read_b64 a[16:17], %2 offset:1728\n" \
  "ds_read_b64 a[18:19], %2 offset:1736\n" \
  "v_mfma_f32_32x32x16_bf16 %0, a[32:35], a[40:43], %0\n" \
  "ds_read_b128 a[20:23], %3 offset:2912\n" \
  "ds_read_b128 a[24:27], %3 offset:352\n" \
  "v_mfma_f32_32x32x16_bf16 %1, a[44:47], a[40:43], %1\n" \
  "ds_read_b64 a[28:29], %2 offset:3776\n" \
  "ds_read_b64 a[30:31], %2 offset:3784\n" \
  "s_waitcnt lgkmcnt(6)\n" \
  "v_mfma_f32_32x32x16_bf16 %1, a[0:3], a[4:7], %1\n" \
  "ds_read_b64 a[32:33], %2 offset:1824\n" \
  "ds_read_b64 a[34:35], %2 offset:1832\n" \
  "v_mfma_f32_32x32x16_bf16 %0, a[0:3], a[8:11], %0\n" \
  "ds_read_b128 a[36:39], %3 offset:2800\n" \
  "ds_read_b128 a[40:43], %3 offset:240\n" \
  "v_mfma_f32_32x32x16_bf16 %1, a[12:15], a[8:11], %1\n" \
  "ds_read_b64 a[44:45], %2 offset:3872\n" \
  "ds_read_b64 a[46:47], %2 offset:3880\n" \
  "s_waitcnt lgkmcnt(6)\n" \
  "v_mfma_f32_32x32x16_bf16 %1, a[16:19], a[20:23], %1\n" \
  "ds_read_b64 a[0:1], %2 offset:1792\n" \
  "ds_read_b64 a[2:3], %2 offset:1800\n" \
  "v_mfma_f32_32x32x16_bf16 %0, a[16:19], a[24:27], %0\n" \
  "ds_read_b128 a[4:7], %3 offset:2832\n" \
  "ds_read_b128 a[8:11], %3 offset:272\n" \
  "v_mfma_f32_32x32x16_bf16 %1, a[28:31], a[24:27], %1\n" \
  "ds_read_b64 a[12:13], %2 offset:3840\n" \
  "ds_read_b64 a[14:15], %2 offset:3848\n" \
  "s_waitcnt lgkmcnt(6)\n" \
  "v_mfma_f32_32x32x16_bf16 %1, a[32:35], a[36:39], %1\n" \
  "ds_read_b64 a[16:17], %2 offset:1888\n" \
  "ds_read_b64 a[18:19], %2 offset:1896\n" \
  "v_mfma_f32_32x32x16_bf16 %0, a[32:35], a[40:43], %0\n" \
  "ds_read_b128 a[20:23], %3 offset:2720\n" \
  "ds_read_b128 a[24:27], %3 offset:160\n" \
  "v_mfma_f32_32x32x16_bf16 %1, a[44:47], a[40:43], %1\n" \
  "ds_read_b64 a[28:29], %2 offset:3936\n" \
  "ds_read_b64 a[30:31], %2 offset:3944\n" \
  "s_waitcnt lgkmcnt(6)\n" \
  "v_mfma_f32_32x32x16_bf16 %1, a[0:3], a[4:7], %1\n" \
  "ds_read_b64 a[32:33], %2 offset:1856\n" \
  "ds_read_b64 a[34:35], %2 offset:1864\n" \
  "v_mfma_f32_32x32x16_bf16 %0, a[0:3], a[8:11], %0\n" \
  "ds_read_b128 a[36:39], %3 offset:2752\n" \
  "ds_read_b128 a[40:43], %3 offset:192\n" \
  "v_mfma_f32_32x32x16_bf16 %1, a[12:15], a[8:11], %1\n" \
  "ds_read_b64 a[44:45], %2 offset:3904\n" \
  "ds_read_b64 a[46:47], %2 offset:3912\n" \
  "s_waitcnt lgkmcnt(6)\n" \
  "v_mfma_f32_32x32x16_bf16 %1, a[16:19], a[20:23], %1\n" \
  "ds_read_b64 a[0:1], %2 offset:1952\n" \
  "ds_read_b64 a[2:3], %2 offset:1960\n" \
  "v_mfma_f32_32x32x16_bf16 %0, a[16:19], a[24:27], %0\n" \
  "ds_read_b128 a[4:7], %3 offset:2640\n" \
  "ds_read_b128 a[8:11], %3 offset:80\n" \
  "v_mfma_f32_32x32x16_bf16 %1, a[28:31], a[24:27], %1\n" \
  "ds_read_b64 a[12:13], %2 offset:4000\n" \
  "ds_read_b64 a[14:15], %2 offset:4008\n" \
  "s_waitcnt lgkmcnt(6)\n" \
  "v_mfma_f32_32x32x16_bf16 %1, a[32:35], a[36:39], %1\n" \
  "ds_read_b64 a[16:17], %2 offset:1920\n" \
  "ds_read_b64 a[18:19], %2 offset:1928\n" \
  "v_mfma_f32_32x32x16_bf16 %0, a[32:35], a[40:43], %0\n" \
  "ds_read_b128 a[20:23], %3 offset:2672\n" \
  "ds_read_b128 a[24:27], %3 offset:112\n" \
  "v_mfma_f32_32x32x16_bf16 %1, a[44:47], a[40:43], %1\n" \
  "ds_read_b64 a[28:29], %2 offset:3968\n" \
  "ds_read_b64 a[30:31], %2 offset:3976\n" \
  "s_waitcnt lgkmcnt(6)\n" \
  "v_mfma_f32_32x32x16_bf16 %1, a[0:3], a[4:7], %1\n" \
  "ds_read_b64 a[32:33], %2 offset:2016\n" \
  "ds_read_b64 a[34:35], %2 offset:2024\n" \
  "v_mfma_f32_32x32x16_bf16 %0, a[0:3], a[8:11], %0\n" \
  "ds_read_b128 a[36:39], %3 offset:2560\n" \
  "ds_read_b128 a[40:43], %3 offset:0\n" \
  "v_mfma_f32_32x32x16_bf16 %1, a[12:15], a[8:11], %1\n" \
  "ds_read_b64 a[44:45], %2 offset:4064\n" \
  "ds_read_b64 a[46:47], %2 offset:4072\n" \
  "s_waitcnt lgkmcnt(6)\n" \
  "v_mfma_f32_32x32x16_bf16 %1, a[16:19], a[20:23], %1\n" \
  "ds_read_b64 a[0:1], %2 offset:1984\n" \
  "ds_read_b64 a[2:3], %2 offset:1992\n" \
  "v_mfma_f32_32x32x16_bf16 %0, a[16:19], a[24:27], %0\n" \
  "ds_read_b128 a[4:7], %3 offset:2592\n" \
  "ds_read_b128 a[8:11], %3 offset:32\n" \
  "v_mfma_f32_32x32x16_bf16 %1, a[28:31], a[24:27], %1\n" \
  "ds_read_b64 a[12:13], %2 offset:4032\n" \
  "ds_read_b64 a[14:15], %2 offset:4040\n" \
  "s_waitcnt lgkmcnt(6)\n" \
  "v_mfma_f32_32x32x16_bf16 %1, a[32:35], a[36:39], %1\n" \
  "v_mfma_f32_32x32x16_bf16 %0, a[32:35], a[40:43], %0\n" \
  "v_mfma_f32_32x32x16_bf16 %1, a[44:47], a[40:43], %1\n" \
  "s_waitcnt lgkmcnt(0)\n" \
  "v_mfma_f32_32x32x16_bf16 %1, a[0:3], a[4:7], %1\n" \
  "v_mfma_f32_32x32x16_bf16 %0, a[0:3], a[8:11], %0\n" \
  "v_mfma_f32_32x32x16_bf16 %1, a[12:15], a[8:11], %1\n" \
  "s_nop 15\n" \
  ""

#define CONV_CLOBBERS "a0", "a1", "a2", "a3", "a4", "a5", "a6", "a7", "a8", "a9", "a10", "a11", "a12", "a13", "a14", "a15", "a16", "a17", "a18", "a19", "a20", "a21", "a22", "a23", "a24", "a25", "a26", "a27", "a28", "a29", "a30", "a31", "a32", "a33", "a34", "a35", "a36", "a37", "a38", "a39", "a40", "a41", "a42", "a43", "a44", "a45", "a46", "a47", "memory"
#define REC_CLOBBERS \
  "v0", "v1", "v2", "v3", "v4", "v5", "v6", "v7", "v8", "v9", "v10", "v11", "v12", "v13", "v14", "v15", "v16", "v17", "v18", "v19", "v20", "v21", "v22",\
  "v23", "v24", "v25", "v26", "v27", "v28", "v29", "v30", "v31", "v32", "v33", "v34", "v35", "v36", "v37", "v38", "v39", "v40", "v41", "v42", "v43",\
  "v44", "v45", "v46", "v47", "v48", "v49", "v50", "v51", "v52", "v53", "v54", "v55", "v56", "v57", "v58", "v59", "v60", "v61", "v62", "v63", "v64",\
  "v65", "v66", "v67", "v68", "v69", "v70", "v71", "v72", "v73", "v74", "v75", "v76", "v77", "v78", "v79", "v80", "v81", "v82", "v83", "v84", "v85",\
  "v86", "v87", "v88", "v89", "v90", "v91", "v92", "v93", "v94", "v95", "v96", "v97", "v98", "v99", "v100", "v101", "v102", "v103", "v104", "v105",\
  "v106", "v107", "v108", "v109", "v110", "v111", "v112", "v113", "v114", "v115", "v116", "v117", "v118", "v119", "v120", "v121", "v122", "v123",\
  "v124", "v125", "v126", "v127", "v128", "v129", "v130", "v131", "v132", "v133", "v134", "v135", "v136", "v137", "v138", "v139", "v140", "v141",\
  "v142", "v143", "v144", "v145", "v146", "v147", "v148", "v149", "v150", "v151", "v152", "v153", "v154", "v155", "v156", "v157", "v158", "v159",\
  "v160", "v161", "v162", "v163", "v164", "v165", "v166", "v167", "v168", "v169", "v170", "v171", "v172", "v173", "v174", "v175", "v176", "v177",\
  "v178", "v179", "v180", "v181", "v182", "v183", "v184", "v185", "v186", "v187", "v188", "v189", "v190", "v191", "v192", "v193", "v194", "v195",\
  "v196", "v197", "v198", "v199", "v200", "v201", "v202", "v203", "v204", "v205", "v206", "v207", "v208", "v209", "v210", "v211", "v212", "v213",\
  "v214", "v215", "v216", "v217", "v218", "v219", "v220", "v221", "v222", "v223", "v224", "v225", "v226", "v227", "v228", "v229", "v230", "v231",\
  "v232", "v233", "v234", "v235", "v236", "v237", "v238", "v239", "v240", "v241", "v242", "v243", "v244", "v245", "v246", "v247", "v248", "v249",\
  "v250", "v251", "v252", "v253", "v254", "v255", "a0", "a1", "a2", "a3", "a4", "a5", "a6", "a7", "a8", "a9", "a10", "a11", "a12", "a13", "a14", "a15",\
  "a16", "a17", "a18", "a19", "a20", "a21", "a22", "a23", "a24", "a25", "a26", "a27", "a28", "a29", "a30", "a31", "a32", "a33", "a34", "a35", "a36",\
  "a37", "a38", "a39", "a40", "a41", "a42", "a43", "a44", "a45", "a46", "a47", "a48", "a49", "a50", "a51", "a52", "a53", "a54", "a55", "a56", "a57",\
  "a58", "a59", "a60", "a61", "a62", "a63", "a64", "a65", "a66", "a67", "a68", "a69", "a70", "a71", "a72", "a73", "a74", "a75", "a76", "a77", "a78",\
  "a79", "a80", "a81", "a82", "a83", "a84", "a85", "a86", "a87", "a88", "a89", "a90", "a91", "a92", "a93", "a94", "a95", "a96", "a97", "a98", "a99",\
  "a100", "a101", "a102", "a103", "a104", "a105", "a106", "a107", "a108", "a109", "a110", "a111", "a112", "a113", "a114", "a115", "a116", "a117",\
  "a118", "a119", "a120", "a121", "a122", "a123", "a124", "a125", "a126", "a127", "a128", "a129", "a130", "a131", "a132", "a133", "a134", "a135",\
  "a136", "a137", "a138", "a139", "a140", "a141", "a142", "a143", "a144", "a145", "a146", "a147", "a148", "a149", "a150", "a151", "a152", "a153",\
  "a154", "a155", "a156", "a157", "a158", "a159", "a160", "a161", "a162", "a163", "a164", "a165", "a166", "a167", "a168", "a169", "a170", "a171",\
  "a172", "a173", "a174", "a175", "a176", "a177", "a178", "a179", "a180", "a181", "a182", "a183", "a184", "a185", "a186", "a187", "a188", "a189",\
  "a190", "a191", "a192", "a193", "a194", "a195", "a196", "a197", "a198", "a199", "a200", "a201", "a202", "a203", "a204", "a205", "a206", "a207",\
  "a208", "a209", "a210", "a211", "a212", "a213", "a214", "a215", "a216", "a217", "a218", "a219", "a220", "a221", "a222", "a223", "a224", "a225",\
  "a226", "a227", "a228", "a229", "a230", "a231", "a232", "a233", "a234", "a235", "a236", "a237", "a238", "a239", "a240", "a241", "a242", "a243",\
  "a244", "a245", "a246", "a247", "a248", "a249", "a250", "a251", "a252", "a253", "a254", "a255", "s34", "s35", "s36", "s37", "s38", "s39", "s40", "s41", "s42",\
  "s43", "s44", "s45", "s46", "s47", "s48", "s49", "s50", "s51", "s52", "s53", "s54", "s55", "s56", "s57", "s58", "s59", "s60", "s61", "s62", "s63",\
  "s64", "s65", "s66", "s67", "s68", "s69", "s70", "s71", "s72", "s73", "s74", "s75", "s76", "s77", "s78", "s79", "s80", "s81", "s82", "s83", "s84",\
  "s85", "s86", "s87", "s88", "s89", "s90", "s91", "s92", "s93", "s94", "s95", "s96", "s97", "s98", "s99", "vcc", "scc", "memory"
__device__ __forceinline__ void role_rec(const Args& a, char LAS* smem, const int rk, const u32 pol) {
  const int cu = rk;
  const int tid = threadIdx.x, w = __builtin_amdgcn_readfirstlane(tid >> 6);
  const int head = cu >> 2, j = cu & 3;
  const u16* pq = a.Wq + head * DH + 64 * (j >> 1);
  const u16* pk = a.Wk + head * DH + 64 * (j >> 1);
  const u16* pv = a.Wv + head * DH + 64 * ((j >> 1) ^ 1);
  const u16* po = a.Wo + (size_t)head * DH * DM + 256 * j;
  asm volatile(REC_ASM
               :: "s"(pq), "s"(pk), "s"(pv), "s"(po), "s"(a.ex), "s"(a.ctrl), "s"(cu), "s"(w), "s"(pol), "s"((u32)(uintptr_t)smem), "s"(rk), "s"(a.mem0 + (size_t)(4 * (cu & 1)) * DM + 16 * (cu >> 1))
               : REC_CLOBBERS);
  __builtin_unreachable();
}
#define S_ROW 4160
#define S_WSZ (4 * S_ROW + 96 * 80 + 2048 + 64)
#define T_LD 132
__device__ __forceinline__ u32 offT(u32 pol, u32 ch) { return (ch >> 1) * 16384 + ((~(pol >> (ch >> 8)) & 1u) << 13) + (ch & 1) * 4096; }
__device__ __forceinline__ void wait_all(u32* ctrl, int word, int n, int slp) {
  const int lane = threadIdx.x & 63;
  for (int spins = 0;; ++spins) {
    asm volatile("" ::: "memory");
    const u32 v = __hip_atomic_load(ctrl + word + 32 * min(lane, n - 1), RLX_AGENT);
    if (__builtin_amdgcn_ballot_w64(v == 0) == 0) break;
    if (spins > SPIN_CAP) { set_dead(ctrl); break; }
    if ((spins & 63) == 63 && __builtin_amdgcn_readfirstlane((int)is_dead(ctrl))) break;
    sleepn(slp);
  }
  asm volatile("" ::: "memory");
}
__device__ __forceinline__ void ssm_in(const Args& a, char LAS* smem, const int rb, const u32 pol) {
  const int tid = threadIdx.x;
  const __amdgpu_buffer_rsrc_t rt = __builtin_amdgcn_make_buffer_rsrc(a.ex + WS_UT_OFF, 0, 8 << 20, 0x00020000);
#pragma unroll
  for (int q = 0; q < 4; ++q) {
    u32x4 x[8];
#pragma unroll
    for (int i = 0; i < 4; ++i) {
      const u16* p = a.u + (size_t)(64 * rb + 2 * (8 * q + 2 * i + (tid >> 7))) * SSM_D + (tid & 127) * 8;
      x[2 * i] = *(const u32x4*)p; x[2 * i + 1] = *(const u32x4*)(p + SSM_D);
    }
#pragma unroll
    for (int i = 0; i < 4; ++i)
#pragma unroll
      for (int e = 0; e < 4; ++e) {
        char LAS* o = smem + ((tid & 127) + 256 * e) * T_LD + (8 * q + 2 * i + (tid >> 7)) * 4;
        *(LAS u32*)o = __builtin_amdgcn_perm(x[2 * i + 1][e], x[2 * i][e], 0x05040100u);
        *(LAS u32*)(o + 128 * T_LD) = __builtin_amdgcn_perm(x[2 * i + 1][e], x[2 * i][e], 0x07060302u);
      }
  }
  __syncthreads();
#pragma unroll 4
  for (int i = 0; i < 32; ++i) {
    const int sl = 32 * i + (tid >> 3), ch = (sl & 127) * 8 + (sl >> 7);
    const LAS u32* p = (const LAS u32*)(smem + sl * T_LD + (tid & 7) * 16);
    __builtin_amdgcn_raw_buffer_store_b128((u32x4){p[0], p[1], p[2], p[3]}, rt, offT(pol, ch) + rb * 128 + (tid & 7) * 16, 0, SC1);
  }
  WAIT_VM0();
  __syncthreads();
  if (tid == 0) __hip_atomic_store(a.ctrl + CW_UIN + rb * 32, 1u, RLX_AGENT);
}
__device__ __forceinline__ void ssm_out(const Args& a, char LAS* smem, const int rb, const int hb, const u32 pol) {
  const int tid = threadIdx.x;
  const __amdgpu_buffer_rsrc_t rt = __builtin_amdgcn_make_buffer_rsrc(a.ex + WS_UT_OFF, 0, 8 << 20, 0x00020000);
  wait_all(a.ctrl, CW_YOUT + hb * 48 * 32, 48, 8);
  u32x4 x[16];
#pragma unroll
  for (int i = 0; i < 16; ++i) {
    const int sl = 32 * i + (tid >> 3), ch = (sl & 63) * 8 + (sl >> 6);
    x[i] = __builtin_amdgcn_raw_buffer_load_b128(rt, offT(pol, 512 * hb + ch) + rb * 128 + (tid & 7) * 16, 0, SC1);
  }
#pragma unroll
  for (int i = 0; i < 16; ++i) {
    LAS u32* p = (LAS u32*)(smem + (32 * i + (tid >> 3)) * T_LD + (tid & 7) * 16);
#pragma unroll
    for (int e = 0; e < 4; ++e) p[e] = x[i][e];
  }
  __syncthreads();
#pragma unroll 2
  for (int i = 0; i < 8; ++i) {
    const int rp = 4 * i + (tid >> 6);
    u32x4 v0, v1;
#pragma unroll
    for (int e = 0; e < 4; ++e) {
      const char LAS* o = smem + ((tid & 63) + 128 * e) * T_LD + rp * 4;
      const u32 x0 = *(const LAS u32*)o, x1 = *(const LAS u32*)(o + 64 * T_LD);
      v0[e] = __builtin_amdgcn_perm(x1, x0, 0x05040100u); v1[e] = __builtin_amdgcn_perm(x1, x0, 0x07060302u);
    }
    u16* p = a.out + (size_t)(64 * rb + 2 * rp) * OUTLD + DM + 512 * hb + (tid & 63) * 8;
    *(u32x4*)p = v0; *(u32x4*)(p + OUTLD) = v1;
  }
}
__device__ __forceinline__ void split2(float a, float b, u32& hi, u32& lo) {
  hi = pk2(a, b);
  lo = pk2(a - __uint_as_float(hi << 16), b - __uint_as_float(hi & 0xffff0000u));
}
__device__ __forceinline__ void cexp1(float ar, float ai, float l, float& xr, float& xi) {
  const float e = __builtin_amdgcn_exp2f(ar * l);
  const float rv = __builtin_amdgcn_fractf(ai * l);
  xr = e * __builtin_amdgcn_cosf(rv); xi = e * __builtin_amdgcn_sinf(rv);
}
__device__ __forceinline__ u32 lo_hi(u32 lo, u32 hi) { return __builtin_amdgcn_perm(hi, lo, 0x07060100u); }
__device__ __forceinline__ void role_ssm(const Args& a, char LAS* smem, const int cx, const int rank, const u32 pol) {
  const int tid = threadIdx.x, lane = tid & 63, w = __builtin_amdgcn_readfirstlane(tid >> 6);
  const int c = lane & 15, g = lane >> 4, b = lane & 31, h = lane >> 5;
  const __amdgpu_buffer_rsrc_t rt = __builtin_amdgcn_make_buffer_rsrc(a.ex + WS_UT_OFF, 0, 8 << 20, 0x00020000);
  char LAS* lu = smem + w * S_WSZ;
  char LAS* lw = lu + S_ROW;
  char LAS* lk = lw + 3 * S_ROW + 31 * 80;
  char LAS* lp = lk + 65 * 80;
  if (lane < 4) *(LAS u32x4*)(lu + lane * 16) = (u32x4){0, 0, 0, 0};
  for (int i = lane; i < 160; i += 64) *(LAS u32x4*)(lw + 3 * S_ROW + i * 16) = (u32x4){0, 0, 0, 0};
  float la[3];
  la[0] = (float)c;
  la[1] = c < 8 ? (float)(256 + 2 * c) : (float)(512 + 256 * ((c - 8) >> 2) + 4 * ((c - 8) & 3));
  la[2] = c < 8 ? (float)(1024 + 256 * (c >> 1) + 8 * (c & 1)) : (c == 8 ? 2048.f : 0.f);
  const float ly = (float)(16 * c);
  u32x4 un[4];
  auto fetch_u = [&](int d) {
#pragma unroll
    for (int i = 0; i < 4; ++i) un[i] = __builtin_amdgcn_raw_buffer_load_b128(rt, lane * 16 + i * 1024, offT(pol, d), SC1);
  };
  u32 pr[6]; u16 pdt, pds;
  auto fetch_p = [&](int d) {
    const size_t o = (size_t)d * SSM_N + 2 * lane;
    pr[0] = *(const u32*)(a.A_re + o); pr[1] = *(const u32*)(a.A_im + o); pr[2] = *(const u32*)(a.B_re + o);
    pr[3] = *(const u32*)(a.B_im + o); pr[4] = *(const u32*)(a.C_re + o); pr[5] = *(const u32*)(a.C_im + o);
    pdt = a.log_dt[d]; pds = a.Dskip[d];
  };
#define SST(i)
  const int first = cx * 256 + rank * 3 + w, last = cx * 256 + 256;
  fetch_p(first);
  {
    for (int d = first; d < last; d += 24) {
      const int nxt = d + 24 < last ? d + 24 : d;
      SST(0);
      const float dt = rbf(expf(bf2f(pdt))), ds = bf2f(pds);
#pragma unroll
      for (int i = 0; i < 2; ++i) {
        auto el = [&](int k) { return __uint_as_float(i ? (pr[k] & 0xffff0000u) : (pr[k] << 16)); };
        const float br = dt * el(2), bi = dt * el(3), cr = el(4), ci = el(5);
        *(LAS f32x4*)(lp + (2 * lane + i) * 16) = (f32x4){dt * el(0) * 1.4426950408889634f, dt * el(1) * 0.15915494309189535f, cr * br - ci * bi, cr * bi + ci * br};
      }
      fetch_p(nxt);
      SST(1);
      f32x4 ka[3];
#pragma unroll
      for (int t = 0; t < 3; ++t) ka[t] = (f32x4){0, 0, 0, 0};
      f32x4 pn[4];
#pragma unroll
      for (int i = 0; i < 4; ++i) pn[i] = *(const LAS f32x4*)(lp + (4 * g + i) * 16);
      for (int k = 0; k < 8; ++k) {
        f32x4 p[4];
#pragma unroll
        for (int i = 0; i < 4; ++i) { p[i] = pn[i]; pn[i] = *(const LAS f32x4*)(lp + (16 * ((k + 1) & 7) + 4 * g + i) * 16); }
        u32x4 yh, yl, xh[3], xl[3];
#pragma unroll
        for (int i = 0; i < 4; ++i) {
          float er, ei; u32 hi, lo;
          cexp1(p[i][0], p[i][1], ly, er, ei);
          split2(p[i][2] * er - p[i][3] * ei, -(p[i][2] * ei + p[i][3] * er), hi, lo); yh[i] = hi; yl[i] = lo;
#pragma unroll
          for (int t = 0; t < 3; ++t) { cexp1(p[i][0], p[i][1], la[t], er, ei); split2(er, ei, hi, lo); xh[t][i] = hi; xl[t][i] = lo; }
        }
        __builtin_amdgcn_sched_barrier(0);
#pragma unroll
        for (int t = 0; t < 3; ++t) ka[t] = MFMA32(asb(xh[t]), asb(yh), ka[t]);
#pragma unroll
        for (int t = 0; t < 3; ++t) ka[t] = MFMA32(asb(xh[t]), asb(yl), ka[t]);
#pragma unroll
        for (int t = 0; t < 3; ++t) ka[t] = MFMA32(asb(xl[t]), asb(yh), ka[t]);
        __builtin_amdgcn_sched_barrier(0);
      }
      SST(2);
      {
        const int l = 16 * c + 4 * g;
        *(LAS u32x2*)(lk + (1 + (l >> 5)) * 80 + ((l & 31) ^ 4) * 2) = (u32x2){pk2(ka[0][3], ka[0][2]), pk2(ka[0][1], ka[0][0])};
        const int j1 = g < 2 ? 8 * c + 4 * g : 128 + 64 * (g - 2) + 4 * c;
        *(LAS u32x4*)(lp + j1 * 4) = (u32x4){pk2(ka[1][0], ka[1][0]), pk2(ka[1][1], ka[1][1]), pk2(ka[1][2], ka[1][2]), pk2(ka[1][3], ka[1][3])};
        if (g < 2) {
          *(LAS u32x2*)(lp + (256 + 64 * g + 2 * c) * 4) = (u32x2){pk2(ka[2][0], ka[2][0]), pk2(ka[2][1], ka[2][1])};
          *(LAS u32x2*)(lp + (256 + 64 * g + 32 + 2 * c) * 4) = (u32x2){pk2(ka[2][2], ka[2][2]), pk2(ka[2][3], ka[2][3])};
        } else if (lane == 32) *(LAS u32*)(lp + 384 * 4) = pk2(ka[2][0], ka[2][0]);
        if (lane < 32) {
          const u32x4 v = *(const LAS u32x4*)(lp + lane * 16);
          const u32 v4 = *(const LAS u32*)(lp + lane * 16 + 16);
          *(LAS u32x4*)(lk + (9 + (lane >> 2)) * 80 + (lane & 3) * 16) = (u32x4){lo_hi(v4, v[3]), v[2], lo_hi(v[2], v[1]), v[0]};
        }
        {
          const u32x2 v = *(const LAS u32x2*)(lp + 512 + lane * 8);
          const u32 v2 = *(const LAS u32*)(lp + 512 + lane * 8 + 8);
          *(LAS u32x4*)(lk + (17 + (lane >> 2)) * 80 + (lane & 3) * 16) = (u32x4){v2, v[1], lo_hi(v[1], v[0]), v[0]};
        }
        {
          const u32x2 v = *(const LAS u32x2*)(lp + 1024 + lane * 8);
          const u32 v2 = *(const LAS u32*)(lp + 1024 + lane * 8 + 8);
          char LAS* o = lk + (33 + (lane >> 1)) * 80 + (lane & 1) * 32;
          *(LAS u32x4*)o = (u32x4){v[1], lo_hi(v[1], v[0]), v[0], v[0]};
          *(LAS u32x4*)(o + 16) = (u32x4){v2, v2, v[1], v[1]};
        }
      }
      if (d == first) { wait_all(a.ctrl, CW_UIN, 32, 8); fetch_u(d); }
#pragma unroll
      for (int i = 0; i < 4; ++i) *(LAS u32x4*)(lu + 64 + lane * 16 + i * 1024) = un[i];
      if (nxt != d) fetch_u(nxt);
#pragma unroll
      for (int i = 0; i < 5; ++i) {
        const int q = lane + 64 * i;
        if (i < 4 || lane < 4) {
          const u32x4 x = *(const LAS u32x4*)(lu + q * 16);
          u32x2 y = *(const LAS u32x2*)(lu + q * 16 + 16);
          *(LAS u32x4*)(lw + q * 16) = (u32x4){__builtin_amdgcn_alignbit(x[1], x[0], 16), __builtin_amdgcn_alignbit(x[2], x[1], 16), __builtin_amdgcn_alignbit(x[3], x[2], 16), __builtin_amdgcn_alignbit(y[0], x[3], 16)};
          *(LAS u32x4*)(lw + S_ROW + q * 16) = (u32x4){x[1], x[2], x[3], y[0]};
          *(LAS u32x4*)(lw + 2 * S_ROW + q * 16) = (u32x4){__builtin_amdgcn_alignbit(x[2], x[1], 16), __builtin_amdgcn_alignbit(x[3], x[2], 16), __builtin_amdgcn_alignbit(y[0], x[3], 16), __builtin_amdgcn_alignbit(y[1], y[0], 16)};
        }
      }
      SST(3);
      const int p0 = 32 + b - 8 * h - 7, cp = p0 & 3;
      const char LAS* ra = lu + cp * S_ROW + (p0 - cp) * 2 - 32;
      const char LAS* rb = lk + 16 * h + 80 * (b - 30);
      f32x16 y0, y1;
      asm volatile(CONV_ASM : "=&v"(y0), "=&v"(y1) : "v"((u32)(uintptr_t)ra), "v"((u32)(uintptr_t)rb) : CONV_CLOBBERS);
      SST(4);
#pragma unroll
      for (int cg = 0; cg < 2; ++cg)
#pragma unroll
        for (int i4 = 0; i4 < 4; ++i4) {
          LAS u32x2* up = (LAS u32x2*)(lu + 64 + (32 * (32 * cg + b) + 8 * i4 + 4 * h) * 2);
          const u32x2 uv = *up;
          float v[4];
#pragma unroll
          for (int e = 0; e < 4; ++e) v[e] = cg ? y1[4 * i4 + e] : y0[4 * i4 + e];
          const u32 ya = pk2(v[0], v[1]), yb = pk2(v[2], v[3]);
          const u32 sa = pk2(__uint_as_float(uv[0] << 16) * ds, __uint_as_float(uv[0] & 0xffff0000u) * ds);
          const u32 sb = pk2(__uint_as_float(uv[1] << 16) * ds, __uint_as_float(uv[1] & 0xffff0000u) * ds);
          *up = (u32x2){pk2(__uint_as_float(ya << 16) + __uint_as_float(sa << 16), __uint_as_float(ya & 0xffff0000u) + __uint_as_float(sa & 0xffff0000u)),
                        pk2(__uint_as_float(yb << 16) + __uint_as_float(sb << 16), __uint_as_float(yb & 0xffff0000u) + __uint_as_float(sb & 0xffff0000u))};
        }
#pragma unroll
      for (int i = 0; i < 4; ++i) __builtin_amdgcn_raw_buffer_store_b128(*(const LAS u32x4*)(lu + 64 + lane * 16 + i * 1024), rt, lane * 16 + i * 1024, offT(pol, d), SC1);
      SST(5);
    }
    WAIT_VM0();
    if (lane == 0) __hip_atomic_store(a.ctrl + CW_YOUT + (cx * 24 + rank * 3 + w) * 32, 1u, RLX_AGENT);
  }
}
#define BULK_CLOBBERS \
  "v4", "v5", "v6", "v7", "v8", "v9", "v10", "v11", "v12", "v13", "v14", "v15", "v16", "v17", "v18", "v19", "v20", "v21", "v22", "v23", "v24", "v25",\
  "v26", "v27", "v28", "v29", "v30", "v31", "v32", "v33", "v34", "v35", "v36", "v37", "v38", "v39", "v40", "v41", "v42", "v43", "v44", "v45", "v46",\
  "v47", "v48", "v49", "v50", "v51", "v52", "v53", "v54", "v55", "v56", "v57", "v58", "v59", "v60", "v61", "v62", "v63", "v64", "v65", "v66", "v67",\
  "v68", "v69", "v70", "v71", "v72", "v73", "v74", "v75", "v76", "v77", "v78", "v79", "v80", "v81", "v82", "v83", "v84", "v85", "v86", "v87", "v88",\
  "v89", "v90", "v91", "v92", "v93", "v94", "v95", "v96", "v97", "v98", "v99", "v100", "v101", "v102", "v103", "v104", "v105", "v106", "v107", "v108",\
  "v109", "v110", "v111", "v112", "v113", "v114", "v115", "v116", "v117", "v118", "v119", "v120", "v121", "v122", "v123", "v124", "v125", "v126",\
  "v127", "v128", "v129", "v130", "v131", "v132", "v133", "v134", "v135", "v136", "v137", "v138", "v139", "v140", "v141", "v142", "v143", "v144",\
  "v145", "v146", "v147", "v148", "v149", "v150", "v151", "v152", "v153", "v154", "v155", "v156", "v157", "v158", "v159", "v160", "v161", "v162",\
  "v163", "v164", "v165", "v166", "v167", "v168", "v169", "v170", "v171", "v172", "v173", "v174", "v175", "v176", "v177", "v178", "v179", "v180",\
  "v181", "v182", "v183", "v184", "v185", "v186", "v187", "v188", "v189", "v190", "v191", "v192", "v193", "v194", "v195", "v196", "v197", "v198",\
  "v199", "v200", "v201", "v202", "v203", "v204", "v205", "v206", "v207", "v208", "v209", "v210", "v211", "v212", "v213", "v214", "v215", "v216",\
  "v217", "v218", "v219", "v220", "v221", "v222", "v223", "v224", "v225", "v226", "v227", "v228", "v229", "v230", "v231", "v232", "v233", "v234",\
  "v235", "v236", "v237", "v238", "v239", "v240", "v241", "v242", "v243", "v244", "v245", "v246", "v247", "v248", "v249", "v250", "v251", "v252",\
  "v253", "v254", "v255", "a0", "a1", "a2", "a3", "a4", "a5", "a6", "a7", "a8", "a9", "a10", "a11", "a12", "a13", "a14", "a15", "a16", "a17", "a18",\
  "a19", "a20", "a21", "a22", "a23", "a24", "a25", "a26", "a27", "a28", "a29", "a30", "a31", "a32", "a33", "a34", "a35", "a36", "a37", "a38", "a39",\
  "a40", "a41", "a42", "a43", "a44", "a45", "a46", "a47", "a48", "a49", "a50", "a51", "a52", "a53", "a54", "a55", "a56", "a57", "a58", "a59", "a60",\
  "a61", "a62", "a63", "a64", "a65", "a66", "a67", "a68", "a69", "a70", "a71", "a72", "a73", "a74", "a75", "a76", "a77", "a78", "a79", "a80", "a81",\
  "a82", "a83", "a84", "a85", "a86", "a87", "a88", "a89", "a90", "a91", "a92", "a93", "a94", "a95", "a96", "a97", "a98", "a99", "a100", "a101", "a102",\
  "a103", "a104", "a105", "a106", "a107", "a108", "a109", "a110", "a111", "a112", "a113", "a114", "a115", "a116", "a117", "a118", "a119", "a120",\
  "a121", "a122", "a123", "a124", "a125", "a126", "a127", "a128", "a129", "a130", "a131", "a132", "a133", "a134", "a135", "a136", "a137", "a138",\
  "a139", "a140", "a141", "a142", "a143", "a144", "a145", "a146", "a147", "a148", "a149", "a150", "a151", "a152", "a153", "a154", "a155", "a156",\
  "a157", "a158", "a159", "a160", "a161", "a162", "a163", "a164", "a165", "a166", "a167", "a168", "a169", "a170", "a171", "a172", "a173", "a174",\
  "a175", "a176", "a177", "a178", "a179", "a180", "a181", "a182", "a183", "a184", "a185", "a186", "a187", "a188", "a189", "a190", "a191", "a192",\
  "a193", "a194", "a195", "a196", "a197", "a198", "a199", "a200", "a201", "a202", "a203", "a204", "a205", "a206", "a207", "a208", "a209", "a210",\
  "a211", "a212", "a213", "a214", "a215", "a216", "a217", "a218", "a219", "a220", "a221", "a222", "a223", "a224", "a225", "a226", "a227", "a228",\
  "a229", "a230", "a231", "a232", "a233", "a234", "a235", "a236", "a237", "a238", "a239", "a240", "a241", "a242", "a243", "a244", "a245", "a246",\
  "a247", "a248", "a249", "a250", "a251", "a252", "a253", "a254", "a255", "s68", "s69", "s70", "s71", "s72", "s73", "s74", "s75", "s76", "s77", "s78",\
  "s79", "s80", "s81", "s82", "s83", "s84", "s85", "s86", "s87", "s88", "s89", "s90", "s91", "s92", "s93", "s94", "s95", "s96", "vcc", "scc", "memory"
#define GEMM_CLOBBERS \
  "v4", "v5", "v6", "v7", "v8", "v9", "v10", "v11", "v12", "v13", "v14", "v15", "v16", "v17", "v18", "v19", "v20", "v21", "v22", "v23", "v24", "v25",\
  "v26", "v27", "v28", "v29", "v30", "v31", "v32", "v33", "v34", "v35", "v36", "v37", "v38", "v39", "v40", "v41", "v42", "v43", "v44", "v45", "v46",\
  "v47", "v48", "v49", "v50", "v51", "v52", "v53", "v54", "v55", "v56", "v57", "v58", "v59", "v60", "v61", "v62", "v63", "v64", "v65", "v66", "v67",\
  "v68", "v69", "v70", "v71", "v72", "v73", "v74", "v75", "v76", "v77", "v78", "v79", "v80", "v81", "v82", "v83", "v84", "v85", "v86", "v87", "v88",\
  "v89", "v90", "v91", "v92", "v93", "v94", "v95", "v96", "v97", "v98", "v99", "v116", "v117", "v118", "v119", "v120", "v121", "v122", "v123", "v124",\
  "v125", "v126", "v127", "v128", "v129", "v130", "v131", "v132", "v133", "v134", "v135", "v136", "v137", "v138", "v139", "v140", "v141", "v142",\
  "v143", "v144", "v145", "v146", "v147", "v148", "v149", "v150", "v151", "v152", "v153", "v154", "v155", "v156", "v157", "v158", "v159", "v160",\
  "v161", "v162", "v163", "v164", "v165", "v166", "v167", "v168", "v169", "v170", "v171", "v172", "v173", "v174", "v175", "v176", "v177", "v178",\
  "v179", "v180", "v181", "v182", "v183", "v184", "v185", "v186", "v187", "v188", "v189", "v190", "v191", "v192", "v193", "v194", "v195", "v196",\
  "v197", "v198", "v199", "v200", "v201", "v202", "v203", "v204", "v205", "v206", "v207", "v208", "v209", "v210", "v211", "v212", "v213", "v214",\
  "v215", "v216", "v217", "v218", "v219", "v220", "v221", "v222", "v223", "v224", "v225", "v226", "v227", "v228", "v229", "v230", "v231", "v232",\
  "v233", "v234", "v235", "v236", "v237", "v238", "v239", "v240", "v241", "v242", "v243", "v244", "v245", "v246", "v247", "v248", "v249", "v250",\
  "v251", "v252", "v253", "v254", "v255", "a0", "a1", "a2", "a3", "a4", "a5", "a6", "a7", "a8", "a9", "a10", "a11", "a12", "a13", "a14", "a15", "a16",\
  "a17", "a18", "a19", "a20", "a21", "a22", "a23", "a24", "a25", "a26", "a27", "a28", "a29", "a30", "a31", "a32", "a33", "a34", "a35", "a36", "a37",\
  "a38", "a39", "a40", "a41", "a42", "a43", "a44", "a45", "a46", "a47", "a48", "a49", "a50", "a51", "a52", "a53", "a54", "a55", "a56", "a57", "a58",\
  "a59", "a60", "a61", "a62", "a63", "a64", "a65", "a66", "a67", "a68", "a69", "a70", "a71", "a72", "a73", "a74", "a75", "a76", "a77", "a78", "a79",\
  "a80", "a81", "a82", "a83", "a84", "a85", "a86", "a87", "a88", "a89", "a90", "a91", "a92", "a93", "a94", "a95", "a96", "a97", "a98", "a99", "a100",\
  "a101", "a102", "a103", "a104", "a105", "a106", "a107", "a108", "a109", "a110", "a111", "a112", "a113", "a114", "a115", "a116", "a117", "a118",\
  "a119", "a120", "a121", "a122", "a123", "a124", "a125", "a126", "a127", "a128", "a129", "a130", "a131", "a132", "a133", "a134", "a135", "a136",\
  "a137", "a138", "a139", "a140", "a141", "a142", "a143", "a144", "a145", "a146", "a147", "a148", "a149", "a150", "a151", "a152", "a153", "a154",\
  "a155", "a156", "a157", "a158", "a159", "a160", "a161", "a162", "a163", "a164", "a165", "a166", "a167", "a168", "a169", "a170", "a171", "a172",\
  "a173", "a174", "a175", "a176", "a177", "a178", "a179", "a180", "a181", "a182", "a183", "a184", "a185", "a186", "a187", "a188", "a189", "a190",\
  "a191", "a192", "a193", "a194", "a195", "a196", "a197", "a198", "a199", "a200", "a201", "a202", "a203", "a204", "a205", "a206", "a207", "a208",\
  "a209", "a210", "a211", "a212", "a213", "a214", "a215", "a216", "a217", "a218", "a219", "a220", "a221", "a222", "a223", "a224", "a225", "a226",\
  "a227", "a228", "a229", "a230", "a231", "a232", "a233", "a234", "a235", "a236", "a237", "a238", "a239", "a240", "a241", "a242", "a243", "a244",\
  "a245", "a246", "a247", "a248", "a249", "a250", "a251", "a252", "a253", "a254", "a255", "s68", "s69", "s70", "s71", "s72", "s73", "s74", "s75", "s76",\
  "s77", "s78", "s79", "s80", "s81", "s82", "s83", "s84", "s85", "s86", "s87", "s88", "s89", "s90", "s91", "s92", "s93", "s94", "s95", "s96", "vcc", "scc", "memory"
__device__ __forceinline__ void role_gemm(const Args& a, char LAS* smem, const int gi, const u32 pol) {
  const int w = __builtin_amdgcn_readfirstlane(threadIdx.x >> 6);
  asm volatile(GEMM_ASM
               :: "s"(a.Wq + 64 * gi), "s"(a.Wk + 64 * (gi ^ 1)), "s"(a.Wv + 64 * (gi ^ 2)), "s"(a.x), "s"(a.ex + WS_Q_OFF), "s"(a.ctrl), "s"(gi), "s"(w), "s"((u32)(uintptr_t)smem), "s"(pol)
               : GEMM_CLOBBERS);
  ssm_out(a, smem, gi & 31, gi >> 5, pol);
}
__device__ __forceinline__ void role_xo(const Args& a, char LAS* smem, const int xi, const u32 pol) {
  const int w = __builtin_amdgcn_readfirstlane(threadIdx.x >> 6);
  sleepn(XO_SLEEP);
  ssm_in(a, smem, xi, pol);
  asm volatile(XO_ASM
               :: "s"(a.Wo + 64 * (xi >> 1)), "s"(a.ex + WS_OX_OFF), "s"(a.out), "s"(a.ctrl), "s"(xi), "s"(w), "s"((u32)(uintptr_t)smem), "s"(pol)
               : BULK_CLOBBERS);
}
__device__ __forceinline__ void role_xa(const Args& a, char LAS* smem, const int head, const u32 pol) {
  const int lane = threadIdx.x & 63;
  sleepn(XPRE);
  {
    u32 acc = 0;
    for (int i = 0; i < 16; ++i) acc |= *(const u32*)((const char*)a.x + ((size_t)(i * 32 + head) * 64 + lane) * 128);
    const u16* pp[6] = {a.A_re, a.A_im, a.B_re, a.B_im, a.C_re, a.C_im};
#pragma unroll
    for (int i = 0; i < 6; ++i) acc |= *(const u32*)((const char*)pp[i] + ((size_t)head * 64 + lane) * 128);
    asm volatile("" :: "v"(acc));
  }
  asm volatile(XA_ASM
               :: "s"(a.ex), "s"(a.ex + WS_Q_OFF), "s"(a.ctrl), "s"(head), "s"(pol), "s"((u32)(uintptr_t)smem + 3 * S_WSZ)
               : BULK_CLOBBERS);
}
__global__ void __launch_bounds__(128) prep_kernel(u32* ctrl, char* ex) {
  __shared__ u32 best[2];
  const int b = blockIdx.x, tid = threadIdx.x, w = tid >> 6;
  for (int i = b * 128 + tid; i < CTRL_BYTES / 4; i += EX_PAGES * 128)
    if ((i < CW_POL || i >= CW_POL + 32) && (i < 6000 || i >= 6064)) ctrl[i] = 0;
  if (w == 0) {
    u32* p0 = (u32*)(ex + ((size_t)b << 21));
    u32 b0 = 0xffffffffu, b1 = 0xffffffffu;
    for (int rep = 0; rep < NPROBE; ++rep) {
      u32* pp = p0 + ((rep * 5) & 31) * 64;
      u64 t0 = __builtin_amdgcn_s_memtime();
      u32 r0 = __hip_atomic_fetch_or(pp, 0u, RLX_AGENT);
      asm volatile("s_waitcnt vmcnt(0)" :: "v"(r0) : "memory");
      u64 t1 = __builtin_amdgcn_s_memtime();
      u32 r1 = __hip_atomic_fetch_or(pp + 2048, 0u, RLX_AGENT);
      asm volatile("s_waitcnt vmcnt(0)" :: "v"(r1) : "memory");
      u64 t2 = __builtin_amdgcn_s_memtime();
      b0 = min(b0, (u32)(t1 - t0)); b1 = min(b1, (u32)(t2 - t1));
    }
    if (tid == 0) { best[0] = b0; best[1] = b1; }
  }
  if (tid == 0) {
    const u32 xcc = (u32)__builtin_amdgcn_s_getreg(20 | (3 << 11)) & 7u;
    const u32 fast1 = best[1] < best[0] ? 1u : 0u;
    ctrl[CW_POL + b] = xcc < 4 ? fast1 : (fast1 ^ 1u);
  }
}

__global__ void __launch_bounds__(256, 1) mega_kernel(Args a) {
  __shared__ __attribute__((aligned(16))) char smem_raw[163840];
  char LAS* smem = (char LAS*)smem_raw;
  const int tid = threadIdx.x;
  const u32 pw = (tid & 63) < EX_PAGES ? a.ctrl[CW_POL + (tid & 63)] : 0u;
  if (tid == 0) {
    const u32 xcc = (u32)__builtin_amdgcn_s_getreg(20 | (3 << 11)) & 7u;
    int role = -1;
    for (int k = 0; k < 8 && role < 0; ++k) {
      const u32 cc = (xcc + k) & 7u;
      const u32 t = __hip_atomic_fetch_add(a.ctrl + CW_TICKET + cc * 32, 1u, RLX_AGENT);
      if (t < 32) role = (int)(cc * 32 + t);
    }
    *(LAS int*)smem = role;
  }
  __syncthreads();
  const int role = __builtin_amdgcn_readfirstlane(*(LAS int*)smem);
  __syncthreads();
  if (role < 0) return;
  const u32 pol = vote_pol(pw);
  if (role < 128) role_rec(a, smem, role, pol);
  else {
    const int cx = (role - 128) >> 5, t = role & 31;
    if (t < 8) role_xo(a, smem, ((cx >> 1) * 8 + t) * 2 + (cx & 1), pol);
    else if (t < 16) {
      if (__builtin_amdgcn_readfirstlane(tid >> 6) == 3) role_xa(a, smem, cx * 8 + t - 8, pol);
      else role_ssm(a, smem, cx, t - 8, pol);
    }
    else role_gemm(a, smem, (cx >> 1) * 32 + (t - 16) * 2 + (cx & 1), pol);
  }
}

extern "C" void kernel_launch(void* const* d_in, const int* in_sizes, int n_in, void* d_out, int out_size,
                              void* d_ws, size_t ws_size, hipStream_t stream) {
  Args a;
  a.x = (const u16*)d_in[0]; a.mem0 = (const u16*)d_in[1];
  a.Wq = (const u16*)d_in[2]; a.Wk = (const u16*)d_in[3]; a.Wv = (const u16*)d_in[4]; a.Wo = (const u16*)d_in[5];
  a.u = (const u16*)d_in[7]; a.A_re = (const u16*)d_in[8]; a.A_im = (const u16*)d_in[9];
  a.B_re = (const u16*)d_in[10]; a.B_im = (const u16*)d_in[11]; a.C_re = (const u16*)d_in[12]; a.C_im = (const u16*)d_in[13];
  a.log_dt = (const u16*)d_in[14]; a.Dskip = (const u16*)d_in[15];
  a.out = (u16*)d_out;
  char* ws = (char*)d_ws;
  a.ctrl = (u32*)ws;
  a.ex = (char*)(((uintptr_t)ws + CTRL_BYTES + (2u << 20) - 1) & ~(uintptr_t)((2u << 20) - 1));
  prep_kernel<<<EX_PAGES, 128, 0, stream>>>(a.ctrl, a.ex);
  mega_kernel<<<256, 256, 0, stream>>>(a);
}
```

```cpp
#include <hip/hip_runtime.h>
#include <hip/hip_bf16.h>
#include <stdint.h>

#define S_LEN 2048
#define DM 1024
#define NH 32
#define DH 128
#define HD 4096
#define NSEG 64
#define SSM_L 2048
#define SSM_D 1024
#define SSM_N 128
#define OUTLD 2048

typedef unsigned short u16;
typedef unsigned int u32;
typedef unsigned long long u64;
typedef __attribute__((ext_vector_type(8))) short bf16x8;
typedef __attribute__((ext_vector_type(4))) short bf16x4;
typedef __attribute__((ext_vector_type(4))) float f32x4;
typedef __attribute__((ext_vector_type(16))) float f32x16;
typedef __attribute__((ext_vector_type(4))) unsigned int u32x4;
typedef __attribute__((ext_vector_type(2))) unsigned int u32x2;
typedef __attribute__((ext_vector_type(2))) float f32x2;
typedef __attribute__((ext_vector_type(2))) __bf16 bf2_t;
typedef short v4i16_t __attribute__((ext_vector_type(4)));
#define LAS __attribute__((address_space(3)))

#define SENT 0xFFFFFFFFu
#define RLX_AGENT __ATOMIC_RELAXED, __HIP_MEMORY_SCOPE_AGENT
#define SC1 16

__device__ __forceinline__ float bf2f(u16 v) { return __uint_as_float(((u32)v) << 16); }
__device__ __forceinline__ u32 pk2(float a, float b) {
  f32x2 f = {a, b};
  bf2_t h = __builtin_convertvector(f, bf2_t);
  return __builtin_bit_cast(u32, h);
}
__device__ __forceinline__ float rbf(float f) { return __uint_as_float(pk2(f, 0.f) << 16); }
__device__ __forceinline__ u32 nosent(u32 v) { return v < 0xFFFFFFFEu ? v : 0xFFFFFFFEu; }
__device__ __forceinline__ bool okv(u32x4 v) { return v[0] != SENT && v[1] != SENT && v[2] != SENT && v[3] != SENT; }
__device__ __forceinline__ bf16x8 asb(u32x4 v) { return __builtin_bit_cast(bf16x8, v); }
__device__ __forceinline__ void sleepn(int n) { for (int k = 0; k < n; ++k) __builtin_amdgcn_s_sleep(1); }
#define WAIT_VM0() asm volatile("s_waitcnt vmcnt(0)" ::: "memory")
#define BAR() do { asm volatile("s_waitcnt lgkmcnt(0)" ::: "memory"); __builtin_amdgcn_s_barrier(); asm volatile("" ::: "memory"); } while (0)

__device__ __forceinline__ float xmax16(float x) {
  auto r = __builtin_amdgcn_permlane16_swap(__float_as_uint(x), __float_as_uint(x), false, false);
  return fmaxf(__uint_as_float(r[0]), __uint_as_float(r[1]));
}
__device__ __forceinline__ float xmax32(float x) {
  auto r = __builtin_amdgcn_permlane32_swap(__float_as_uint(x), __float_as_uint(x), false, false);
  return fmaxf(__uint_as_float(r[0]), __uint_as_float(r[1]));
}
__device__ __forceinline__ float xadd16(float x) {
  auto r = __builtin_amdgcn_permlane16_swap(__float_as_uint(x), __float_as_uint(x), false, false);
  return __uint_as_float(r[0]) + __uint_as_float(r[1]);
}
__device__ __forceinline__ float xadd32(float x) {
  auto r = __builtin_amdgcn_permlane32_swap(__float_as_uint(x), __float_as_uint(x), false, false);
  return __uint_as_float(r[0]) + __uint_as_float(r[1]);
}
__device__ __forceinline__ u32 ror8(u32 x) { return (u32)__builtin_amdgcn_update_dpp(0, (int)x, 0x128, 0xf, 0xf, true); }
template <int N> __device__ __forceinline__ float rorf(float x) {
  return __uint_as_float((u32)__builtin_amdgcn_update_dpp(0, (int)__float_as_uint(x), 0x120 + N, 0xf, 0xf, true));
}
__device__ __forceinline__ float ror8f(float x) { return __uint_as_float(ror8(__float_as_uint(x))); }

#define MFMA32(a, b, c) __builtin_amdgcn_mfma_f32_16x16x32_bf16(a, b, c, 0, 0, 0)
#define MFMA16(a, b, c) __builtin_amdgcn_mfma_f32_16x16x16bf16_1k(a, b, c, 0, 0, 0)
#define MFMA_AV0(acc, wa, xb) asm volatile("v_mfma_f32_16x16x32_bf16 %0, %1, %2, 0" : "=&v"(acc) : "a"(wa), "v"(xb))
#define MFMA_AV(acc, wa, xb) asm volatile("v_mfma_f32_16x16x32_bf16 %0, %1, %2, %0" : "+v"(acc) : "a"(wa), "v"(xb))
#define MFMA_VA0(acc, xa, wb) asm volatile("v_mfma_f32_16x16x32_bf16 %0, %1, %2, 0" : "=&v"(acc) : "v"(xa), "a"(wb))
#define MFMA_VA(acc, xa, wb) asm volatile("v_mfma_f32_16x16x32_bf16 %0, %1, %2, %0" : "+v"(acc) : "v"(xa), "a"(wb))
#define MFMA_VV0(acc, xa, xb) asm volatile("v_mfma_f32_16x16x32_bf16 %0, %1, %2, 0" : "=&v"(acc) : "v"(xa), "v"(xb))
#define MFMA16_VV(acc, xa, xb) asm volatile("v_mfma_f32_16x16x16_bf16 %0, %1, %2, %0" : "+v"(acc) : "v"(xa), "v"(xb))
#define MFMA_ACC(acc, xa, xb) asm volatile("v_mfma_f32_16x16x32_bf16 %0, %1, %2, %0" : "+a"(acc) : "v"(xa), "v"(xb))
#define NOP_IN() asm volatile("s_nop 1" ::: "memory")
#define PIN(x) asm volatile("" : "+v"(x))
#define PINA(x) asm volatile("" : "+a"(x))

__device__ __forceinline__ bf16x4 trread(const char LAS* p) {
  return __builtin_amdgcn_ds_read_tr16_b64_v4i16((LAS v4i16_t*)p);
}
__device__ __forceinline__ bf16x8 cat8(bf16x4 a, bf16x4 b) {
  return __builtin_shufflevector(a, b, 0, 1, 2, 3, 4, 5, 6, 7);
}

#define CW_TICKET 0
#define CW_POL 5600
#define CW_INIT 288
#define CW_SSMQ 352
#define CW_DEAD 320
#define CW_PROG 384
#define CW_XPROG 1408
#define CW_GPROG 3456
#define CW_UIN 2432
#define CW_YOUT 8192
#define CTRL_BYTES 49152
#define EX_PAGES 20
#define EX_BYTES ((u64)EX_PAGES << 21)
#define EXP_C 0
#define EXP_A 1
#define EXP_B 2
#define EXP_DBG 19
#define QKV_SZ ((u64)S_LEN * HD * 4)
#define WS_Q_OFF (EX_BYTES)
#define WS_K_OFF (WS_Q_OFF + QKV_SZ)
#define WS_VT_OFF (WS_K_OFF + QKV_SZ)
#define WS_OX_OFF (WS_VT_OFF + QKV_SZ)
#define WS_UT_OFF (WS_OX_OFF + QKV_SZ)
#define EX_BYTES32 41943040
#define QKV_SZ32 33554432
#define WS_K_OFF32 75497472
static_assert(EX_BYTES32 == EX_BYTES && QKV_SZ32 == QKV_SZ && WS_K_OFF32 == WS_K_OFF, "layout");

#define SPIN_CAP 300000
#define NPROBE 2
#define F_B 1460
#define F_C 3330
#define F_P 4310
#ifndef M_C
#define M_C 150
#endif
#ifndef M_A
#define M_A 150
#endif
#define T_Q 1300
#define T_TOL 100
#define D_K 0
#define DECAY 0.996
#define RI0 1.06
#define XPRE 300
#define O_N 0
#define T_LATE 0
#define O_A 110
#define O_B 130
#define O_C 40
#define GAP_B 4
#define GAP_V 4
#define GAP_0 2
#define XO_SLEEP 250
#define D_PF (-200)
#define STR_(x) #x
#define STR(x) STR_(x)

struct Args {
  const u16 *x, *mem0, *Wq, *Wk, *Wv, *Wo;
  const u16 *u, *A_re, *A_im, *B_re, *B_im, *C_re, *C_im, *log_dt, *Dskip;
  u16* out;
  u32* ctrl;
  char* ex;
};

__device__ __forceinline__ bool is_dead(u32* ctrl) { return __hip_atomic_load(ctrl + CW_DEAD, RLX_AGENT) != 0; }
__device__ __forceinline__ void set_dead(u32* ctrl) { __hip_atomic_store(ctrl + CW_DEAD, 1u, RLX_AGENT); }
__device__ __forceinline__ void wait_ge(u32* ctrl, int word, u32 want, int slp) {
  for (int spins = 0;; ++spins) {
    asm volatile("" ::: "memory");
    u32 v = __builtin_amdgcn_readfirstlane(__hip_atomic_load(ctrl + word, RLX_AGENT));
    if (v >= want) break;
    if (spins > SPIN_CAP) { set_dead(ctrl); break; }
    if ((spins & 63) == 63 && __builtin_amdgcn_readfirstlane((int)is_dead(ctrl))) break;
    sleepn(slp);
  }
}
__device__ __forceinline__ u32 vote_code(u32x4 a, u32x4 b, u32& pol) {
  const u32 n = ((a[0] + a[1]) + (a[2] + a[3])) + ((b[0] + b[1]) + (b[2] + b[3]));
  const int t0 = n & 0xffff, t1 = n >> 16;
  int s = 0, d[2][2] = {{0, 0}, {0, 0}};
#pragma unroll
  for (int pg = 0; pg < EX_PAGES; ++pg) {
    const int x0 = __builtin_amdgcn_readlane(t0, pg), x1 = __builtin_amdgcn_readlane(t1, pg), same = ((pg >> 1) ^ pg ^ 1) & 1;
    s += same ? x0 + x1 : -(x0 + x1);
    d[same][(pg >> 2) & 1] += x1 < x0 ? 1 : -1;
  }
  pol = s < 0 ? 0xAAAAAAAAu : 0x55555555u;
  const int d12 = s < 0 ? d[1][0] : d[0][0], d14 = s < 0 ? d[1][1] : d[0][1];
  return (d12 > 0 ? 0x1000u : 0u) | (d14 > 0 ? 0x4000u : 0u) | (((pol >> (threadIdx.x & 31)) & 1u) << 13);
}
__device__ __forceinline__ u32 max4(u32x4 v) { return max(max(v[0], v[1]), max(v[2], v[3])); }
#define POLL_FAIL(spins) ((++(spins) > SPIN_CAP) ? (set_dead(ctrl), true) : (((spins) & 255) == 255 && __builtin_amdgcn_readfirstlane((int)is_dead(ctrl))))
#define REC_ASM \
  "s_mov_b64 exec, -1\n" \
  "v_mbcnt_lo_u32_b32 v0, -1, 0\n" \
  "v_mbcnt_hi_u32_b32 v0, -1, v0\n" \
  "s_lshr_b32 s70, %6, 2\n" \
  "s_and_b32 s71, %6, 3\n" \
  "v_lshlrev_b32 v1, 4, v0\n" \
  "v_and_b32 v27, 7, v0\n" \
  "v_lshrrev_b32 v28, 3, v0\n" \
  "v_lshlrev_b32 v34, 11, v28\n" \
  "v_lshl_add_u32 v34, v27, 4, v34\n" \
  "v_lshlrev_b32 v32, 13, v28\n" \
  "v_lshl_add_u32 v32, v27, 4, v32\n" \
  "v_and_b32 v27, 32, v0\n" \
  "v_xor_b32 v32, v32, v27\n" \
  "v_xor_b32 v33, 64, v32\n" \
  "s_mul_i32 s88, %7, 40960\n" \
  "s_add_u32 s88, s88, %9\n" \
  "s_lshl_b32 s60, %7, 21\n" \
  "s_mov_b64 s[72:73], %0\n" \
  "s_and_b32 s73, s73, 0xffff\n" \
  "s_mov_b32 s74, 8388608\n" \
  "s_mov_b32 s75, 0x00020000\n" \
  "s_mov_b64 s[76:77], %1\n" \
  "s_and_b32 s77, s77, 0xffff\n" \
  "s_mov_b32 s78, 8388608\n" \
  "s_mov_b32 s79, 0x00020000\n" \
  "s_mov_b64 s[80:81], %2\n" \
  "s_and_b32 s81, s81, 0xffff\n" \
  "s_mov_b32 s82, 8388608\n" \
  "s_mov_b32 s83, 0x00020000\n" \
  "s_mov_b64 s[84:85], %3\n" \
  "s_and_b32 s85, s85, 0xffff\n" \
  "s_mov_b32 s86, 262144\n" \
  "s_mov_b32 s87, 0x00020000\n" \
  "s_mov_b64 s[90:91], %11\n" \
  "v_and_b32 v27, 1, v0\n" \
  "v_lshlrev_b32 v27, 11, v27\n" \
  "v_and_b32 v28, 4, v0\n" \
  "v_lshl_add_u32 v27, v28, 10, v27\n" \
  "v_and_b32 v28, 2, v0\n" \
  "v_lshl_add_u32 v27, v28, 3, v27\n" \
  "global_load_dwordx4 v[64:67], v27, s[90:91]\n" \
  "s_lshr_b32 s59, %10, 1\n" \
  "s_add_u32 s59, s59, %7\n" \
  "s_add_u32 s59, s59, 0\n" \
  "s_and_b32 s59, s59, 3\n" \
  "s_lshl_b32 s59, s59, 19\n" \
  "s_lshl_b32 s60, %7, 21\n" \
  "s_add_u32 s59, s59, s60\n" \
  "s_add_u32 s59, s59, 0\n" \
  "s_add_u32 m0, s88, 0\n" \
  "s_nop 0\n" \
  "buffer_load_dwordx4 v32, s[72:75], s59 offen lds\n" \
  "s_add_u32 m0, s88, 4096\n" \
  "s_nop 0\n" \
  "buffer_load_dwordx4 v32, s[76:79], s59 offen lds\n" \
  "s_add_u32 s59, s59, 65536\n" \
  "s_add_u32 m0, s88, 1024\n" \
  "s_nop 0\n" \
  "buffer_load_dwordx4 v33, s[72:75], s59 offen lds\n" \
  "s_add_u32 m0, s88, 5120\n" \
  "s_nop 0\n" \
  "buffer_load_dwordx4 v33, s[76:79], s59 offen lds\n" \
  "s_add_u32 s59, s59, 65536\n" \
  "s_add_u32 m0, s88, 2048\n" \
  "s_nop 0\n" \
  "buffer_load_dwordx4 v32, s[72:75], s59 offen lds\n" \
  "s_add_u32 m0, s88, 6144\n" \
  "s_nop 0\n" \
  "buffer_load_dwordx4 v32, s[76:79], s59 offen lds\n" \
  "s_add_u32 s59, s59, 65536\n" \
  "s_add_u32 m0, s88, 3072\n" \
  "s_nop 0\n" \
  "buffer_load_dwordx4 v33, s[72:75], s59 offen lds\n" \
  "s_add_u32 m0, s88, 7168\n" \
  "s_nop 0\n" \
  "buffer_load_dwordx4 v33, s[76:79], s59 offen lds\n" \
  "s_lshr_b32 s59, %10, 1\n" \
  "s_add_u32 s59, s59, %7\n" \
  "s_add_u32 s59, s59, 0\n" \
  "s_and_b32 s59, s59, 3\n" \
  "s_lshl_b32 s59, s59, 19\n" \
  "s_lshl_b32 s60, %7, 21\n" \
  "s_add_u32 s59, s59, s60\n" \
  "s_add_u32 s59, s59, 262144\n" \
  "s_add_u32 m0, s88, 8192\n" \
  "s_nop 0\n" \
  "buffer_load_dwordx4 v32, s[72:75], s59 offen lds\n" \
  "s_add_u32 m0, s88, 12288\n" \
  "s_nop 0\n" \
  "buffer_load_dwordx4 v32, s[76:79], s59 offen lds\n" \
  "s_add_u32 s59, s59, 65536\n" \
  "s_add_u32 m0, s88, 9216\n" \
  "s_nop 0\n" \
  "buffer_load_dwordx4 v33, s[72:75], s59 offen lds\n" \
  "s_add_u32 m0, s88, 13312\n" \
  "s_nop 0\n" \
  "buffer_load_dwordx4 v33, s[76:79], s59 offen lds\n" \
  "s_add_u32 s59, s59, 65536\n" \
  "s_add_u32 m0, s88, 10240\n" \
  "s_nop 0\n" \
  "buffer_load_dwordx4 v32, s[72:75], s59 offen lds\n" \
  "s_add_u32 m0, s88, 14336\n" \
  "s_nop 0\n" \
  "buffer_load_dwordx4 v32, s[76:79], s59 offen lds\n" \
  "s_add_u32 s59, s59, 65536\n" \
  "s_add_u32 m0, s88, 11264\n" \
  "s_nop 0\n" \
  "buffer_load_dwordx4 v33, s[72:75], s59 offen lds\n" \
  "s_add_u32 m0, s88, 15360\n" \
  "s_nop 0\n" \
  "buffer_load_dwordx4 v33, s[76:79], s59 offen lds\n" \
  "s_lshr_b32 s59, %10, 1\n" \
  "s_add_u32 s59, s59, %7\n" \
  "s_add_u32 s59, s59, 1\n" \
  "s_and_b32 s59, s59, 3\n" \
  "s_lshl_b32 s59, s59, 19\n" \
  "s_lshl_b32 s60, %7, 21\n" \
  "s_add_u32 s59, s59, s60\n" \
  "s_add_u32 s59, s59, 0\n" \
  "s_add_u32 m0, s88, 16384\n" \
  "s_nop 0\n" \
  "buffer_load_dwordx4 v32, s[72:75], s59 offen lds\n" \
  "s_add_u32 m0, s88, 20480\n" \
  "s_nop 0\n" \
  "buffer_load_dwordx4 v32, s[76:79], s59 offen lds\n" \
  "s_add_u32 s59, s59, 65536\n" \
  "s_add_u32 m0, s88, 17408\n" \
  "s_nop 0\n" \
  "buffer_load_dwordx4 v33, s[72:75], s59 offen lds\n" \
  "s_add_u32 m0, s88, 21504\n" \
  "s_nop 0\n" \
  "buffer_load_dwordx4 v33, s[76:79], s59 offen lds\n" \
  "s_add_u32 s59, s59, 65536\n" \
  "s_add_u32 m0, s88, 18432\n" \
  "s_nop 0\n" \
  "buffer_load_dwordx4 v32, s[72:75], s59 offen lds\n" \
  "s_add_u32 m0, s88, 22528\n" \
  "s_nop 0\n" \
  "buffer_load_dwordx4 v32, s[76:79], s59 offen lds\n" \
  "s_add_u32 s59, s59, 65536\n" \
  "s_add_u32 m0, s88, 19456\n" \
  "s_nop 0\n" \
  "buffer_load_dwordx4 v33, s[72:75], s59 offen lds\n" \
  "s_add_u32 m0, s88, 23552\n" \
  "s_nop 0\n" \
  "buffer_load_dwordx4 v33, s[76:79], s59 offen lds\n" \
  "v_and_b32 v36, 15, v0\n" \
  "v_lshrrev_b32 v37, 4, v0\n" \
  "v_lshrrev_b32 v38, 2, v36\n" \
  "v_and_b32 v39, 3, v0\n" \
  "v_lshrrev_b32 v46, 1, v37\n" \
  "v_lshlrev_b32 v46, 4, v46\n" \
  "v_and_b32 v27, 1, v37\n" \
  "v_lshl_add_u32 v46, v27, 2, v46\n" \
  "v_and_b32 v28, 1, v38\n" \
  "v_add_u32 v46, v46, v28\n" \
  "v_lshrrev_b32 v28, 1, v38\n" \
  "v_lshl_add_u32 v46, v28, 3, v46\n" \
  "v_lshl_add_u32 v27, v28, 1, v27\n" \
  "v_lshlrev_b32 v27, 5, v27\n" \
  "s_and_b32 s59, s71, 1\n" \
  "s_lshl_b32 s59, s59, 6\n" \
  "v_xor_b32 v27, s59, v27\n" \
  "v_lshl_add_u32 v27, v46, 7, v27\n" \
  "v_lshl_add_u32 v28, v39, 3, v27\n" \
  "v_add_u32 v42, s88, v28\n" \
  "v_xor_b32 v28, 32, v28\n" \
  "v_add_u32 v43, s88, v28\n" \
  "v_and_b32 v28, 1, v39\n" \
  "v_lshl_add_u32 v27, v28, 3, v27\n" \
  "v_lshrrev_b32 v28, 1, v39\n" \
  "v_lshl_add_u32 v27, v28, 12, v27\n" \
  "s_add_u32 s59, %7, 0\n" \
  "s_and_b32 s59, s59, 3\n" \
  "s_lshl_b32 s59, s59, 4\n" \
  "v_xor_b32 v28, s59, v27\n" \
  "v_add_u32 v52, s88, v28\n" \
  "s_add_u32 s59, %7, 1\n" \
  "s_and_b32 s59, s59, 3\n" \
  "s_lshl_b32 s59, s59, 4\n" \
  "v_xor_b32 v28, s59, v27\n" \
  "v_add_u32 v53, s88, v28\n" \
  "s_add_u32 s59, %7, 2\n" \
  "s_and_b32 s59, s59, 3\n" \
  "s_lshl_b32 s59, s59, 4\n" \
  "v_xor_b32 v28, s59, v27\n" \
  "v_add_u32 v54, s88, v28\n" \
  "s_add_u32 s59, %7, 3\n" \
  "s_and_b32 s59, s59, 3\n" \
  "s_lshl_b32 s59, s59, 4\n" \
  "v_xor_b32 v28, s59, v27\n" \
  "v_add_u32 v55, s88, v28\n" \
  "v_lshlrev_b32 v49, 7, v46\n" \
  "v_lshl_add_u32 v49, v39, 5, v49\n" \
  "v_add_u32 v49, s88, v49\n" \
  "s_mov_b64 s[44:45], %5\n" \
  "s_and_b32 s45, s45, 0xffff\n" \
  "s_mov_b32 s46, " STR(CTRL_BYTES) "\n" \
  "s_mov_b32 s47, 0x00020000\n" \
  "s_mov_b64 s[36:37], %4\n" \
  "s_and_b32 s37, s37, 0xffff\n" \
  "s_mov_b32 s38, " STR(EX_BYTES32) "\n" \
  "s_mov_b32 s39, 0x00020000\n" \
  "s_mov_b64 s[40:41], %4\n" \
  "s_add_u32 s40, s40, " STR(WS_K_OFF32) "\n" \
  "s_addc_u32 s41, s41, 0\n" \
  "s_and_b32 s41, s41, 0xffff\n" \
  "s_mov_b32 s42, 3*" STR(QKV_SZ32) "\n" \
  "s_mov_b32 s43, 0x00020000\n" \
  "v_lshrrev_b32 v50, 5, v0\n" \
  "v_and_b32 v51, 31, v0\n" \
  "v_mov_b32 v2, 0x4444\n" \
  "v_mov_b32 v27, 0xeeee\n" \
  "v_cmp_lt_u32 vcc, 1, v39\n" \
  "s_mul_i32 s59, %7, 1280\n" \
  "s_add_u32 s59, s59, %9\n" \
  "v_cndmask_b32 v2, v2, v27, vcc\n" \
  "v_cndmask_b32 v29, 0, -1, vcc\n" \
  "v_cndmask_b32 v4, -1, 0, vcc\n" \
  "v_mul_u32_u24 v27, 80, v37\n" \
  "v_add_lshl_u32 v27, v27, v36, 2\n" \
  "v_add_u32 v5, s59, v27\n" \
  "v_lshlrev_b32 v27, 3, v0\n" \
  "s_add_u32 s60, %9, 8192\n" \
  "v_add_u32 v27, s60, v27\n" \
  "s_mul_i32 s59, %7, 1536\n" \
  "v_add_u32 v6, s59, v27\n" \
  "s_add_u32 s59, %7, 1\n" \
  "s_and_b32 s59, s59, 3\n" \
  "s_mul_i32 s59, s59, 1536\n" \
  "v_add_u32 v3, s59, v27\n" \
  "s_add_u32 s59, %7, 2\n" \
  "s_and_b32 s59, s59, 3\n" \
  "s_mul_i32 s59, s59, 1536\n" \
  "v_add_u32 v244, s59, v27\n" \
  "s_add_u32 s59, %7, 3\n" \
  "s_and_b32 s59, s59, 3\n" \
  "s_mul_i32 s59, s59, 1536\n" \
  "v_add_u32 v245, s59, v27\n" \
  "v_and_b32 v27, 1, v37\n" \
  "v_lshlrev_b32 v27, 1, v27\n" \
  "v_and_b32 v28, 1, v38\n" \
  "v_add_u32 v27, v27, v28\n" \
  "v_mul_u32_u24 v27, 160, v27\n" \
  "v_lshrrev_b32 v28, 1, v38\n" \
  "v_lshl_add_u32 v27, v28, 5, v27\n" \
  "v_lshl_add_u32 v27, v39, 3, v27\n" \
  "s_and_b32 s59, %7, 1\n" \
  "s_mul_i32 s59, s59, 640\n" \
  "s_lshr_b32 s60, %7, 1\n" \
  "s_lshl_b32 s60, s60, 6\n" \
  "s_add_u32 s59, s59, s60\n" \
  "s_add_u32 s59, s59, %9\n" \
  "v_add_u32 v7, s59, v27\n" \
  "s_and_b32 s94, s70, 1\n" \
  "s_lshl_b32 s94, s94, 11\n" \
  "s_add_u32 s94, s94, 32768\n" \
  "v_add_u32 v17, s94, v1\n" \
  "s_lshl_b32 s59, s71, 2\n" \
  "s_add_u32 s59, s59, %7\n" \
  "s_lshl_b32 s59, s59, 8\n" \
  "v_and_b32 v8, 0x38, v0\n" \
  "v_lshlrev_b32 v8, 2, v8\n" \
  "v_and_b32 v27, 1, v0\n" \
  "v_lshl_add_u32 v8, v27, 4, v8\n" \
  "v_and_b32 v27, 6, v0\n" \
  "v_lshl_add_u32 v8, v27, 1, v8\n" \
  "v_add_u32 v8, s59, v8\n" \
  "v_and_b32 v27, 7, v0\n" \
  "v_lshlrev_b32 v27, 4, v27\n" \
  "v_lshrrev_b32 v28, 3, v0\n" \
  "v_add_u32 v15, s59, v1\n" \
  "s_xor_b32 s59, s71, 2\n" \
  "s_lshl_b32 s59, s59, 2\n" \
  "s_add_u32 s59, s59, %7\n" \
  "s_lshl_b32 s59, s59, 7\n" \
  "s_add_u32 s59, s59, s94\n" \
  "v_lshlrev_b32 v9, 2, v51\n" \
  "v_add_u32 v9, s59, v9\n" \
  "v_lshl_add_u32 v9, v50, 31, v9\n" \
  "v_add_u32 v25, s59, v27\n" \
  "v_cmp_eq_u32 vcc, 2, v28\n" \
  "s_and_b32 s60, %6, 31\n" \
  "s_lshl_b32 s60, s60, 2\n" \
  "s_or_b32 s60, s60, %7\n" \
  "v_cndmask_b32 v15, v15, v25, vcc\n" \
  "s_cmp_eq_u32 s60, 1\n" \
  "s_cselect_b32 s59, 0, 0x80000000\n" \
  "s_or_b32 s59, s59, 32768+2048\n" \
  "v_mov_b32 v25, 0x80000000\n" \
  "v_cmp_eq_u32 vcc, 3, v28\n" \
  "v_lshl_add_u32 v14, v0, 3, s59\n" \
  "s_nop 0\n" \
  "v_cndmask_b32 v15, v15, v25, vcc\n" \
  "v_lshl_add_u32 v25, v51, 4, s59\n" \
  "v_cmp_eq_u32 vcc, 1, v50\n" \
  "s_lshr_b32 s59, %6, 5\n" \
  "s_and_b32 s93, s59, 1\n" \
  "s_lshl_b32 s93, s93, 14\n" \
  "s_lshr_b32 s60, s59, 1\n" \
  "s_lshl_b32 s60, s60, 12\n" \
  "s_or_b32 s93, s93, s60\n" \
  "s_lshl_b32 s59, s59, 7\n" \
  "s_add_u32 s59, s59, 32768+2048\n" \
  "s_or_b32 s59, s59, s93\n" \
  "v_cndmask_b32 v15, v15, v25, vcc\n" \
  "v_and_b32 v25, 1, v39\n" \
  "v_lshlrev_b32 v25, 14, v25\n" \
  "v_lshrrev_b32 v13, 1, v39\n" \
  "v_lshl_or_b32 v13, v13, 12, v25\n" \
  "v_xor_b32 v13, s59, v13\n" \
  "s_xor_b32 s59, %7, 1\n" \
  "s_lshl_b32 s59, s59, 29\n" \
  "v_or_b32 v13, s59, v13\n" \
  "s_lshl_b32 s59, %7, 18\n" \
  "v_lshl_add_u32 v10, v28, 15, v27\n" \
  "v_add_u32 v10, s59, v10\n" \
  "s_lshl_b32 s60, %7, 4\n" \
  "s_add_u32 s60, s60, s70\n" \
  "v_lshl_add_u32 v25, v28, 1, s60\n" \
  "v_and_b32 v25, 31, v25\n" \
  "v_lshl_add_u32 v10, v25, 7, v10\n" \
  "s_and_b32 s59, %6, 31\n" \
  "s_lshl_b32 s59, s59, 15\n" \
  "s_lshl_b32 s60, %7, 10\n" \
  "s_add_u32 s59, s59, s60\n" \
  "v_add_u32 v11, s59, v1\n" \
  "v_readlane_b32 s49, v24, " STR(EXP_A) "\n" \
  "v_readlane_b32 s51, v24, " STR(EXP_C) "\n" \
  "s_lshr_b32 s59, %6, 5\n" \
  "s_lshl_b32 s59, s59, 15\n" \
  "s_and_b32 s60, %6, 31\n" \
  "s_lshl_b32 s60, s60, 7\n" \
  "s_add_u32 s59, s59, s60\n" \
  "s_or_b32 s49, s49, " STR(EXP_A) "<<21\n" \
  "s_or_b32 s59, s59, s49\n" \
  "v_lshrrev_b32 v25, 3, v36\n" \
  "v_lshlrev_b32 v25, 14, v25\n" \
  "v_lshl_or_b32 v25, v50, 12, v25\n" \
  "v_xor_b32 v12, s59, v25\n" \
  "v_add_u32 v12, v12, v27\n" \
  "v_lshl_add_u32 v12, v37, 31, v12\n" \
  "v_and_b32 v27, 1, v37\n" \
  "v_lshlrev_b32 v27, 5, v27\n" \
  "v_xor_b32 v27, v1, v27\n" \
  "s_add_u32 s59, %9, 32768\n" \
  "v_add_u32 v19, s59, v27\n" \
  "s_lshl_b32 s60, %7, 12\n" \
  "v_add_u32 v18, s60, v19\n" \
  "v_and_b32 v20, 15, v46\n" \
  "v_lshlrev_b32 v20, 4, v20\n" \
  "v_lshl_add_u32 v20, v50, 10, v20\n" \
  "v_lshl_add_u32 v20, v39, 8, v20\n" \
  "v_and_b32 v27, 1, v39\n" \
  "v_lshlrev_b32 v27, 5, v27\n" \
  "v_xor_b32 v20, v20, v27\n" \
  "v_xor_b32 v21, 32, v20\n" \
  "s_add_u32 s59, s59, 8192\n" \
  "v_add_u32 v20, s59, v20\n" \
  "v_add_u32 v21, s59, v21\n" \
  "v_and_b32 v22, 1, v0\n" \
  "v_lshlrev_b32 v22, 7, v22\n" \
  "s_lshr_b32 s59, %7, 1\n" \
  "s_xor_b32 s59, s59, s70\n" \
  "s_lshl_b32 s59, s59, 8\n" \
  "s_add_u32 s59, s59, " STR(CW_GPROG) "*4\n" \
  "v_add_u32 v22, s59, v22\n" \
  "s_lshr_b32 s59, %10, 1\n" \
  "s_add_u32 s59, s59, %7\n" \
  "s_add_u32 s59, s59, 0\n" \
  "s_and_b32 s59, s59, 3\n" \
  "s_lshl_b32 s59, s59, 10\n" \
  "v_add_u32 v80, s59, v1\n" \
  "v_add_u32 v84, s59, v19\n" \
  "v_mov_b32 v88, -1\n" \
  "s_lshr_b32 s59, %10, 1\n" \
  "s_add_u32 s59, s59, %7\n" \
  "s_add_u32 s59, s59, 1\n" \
  "s_and_b32 s59, s59, 3\n" \
  "s_lshl_b32 s59, s59, 10\n" \
  "v_add_u32 v81, s59, v1\n" \
  "v_add_u32 v85, s59, v19\n" \
  "v_mov_b32 v89, -1\n" \
  "s_lshr_b32 s59, %10, 1\n" \
  "s_add_u32 s59, s59, %7\n" \
  "s_add_u32 s59, s59, 2\n" \
  "s_and_b32 s59, s59, 3\n" \
  "s_lshl_b32 s59, s59, 10\n" \
  "v_add_u32 v82, s59, v1\n" \
  "v_add_u32 v86, s59, v19\n" \
  "v_mov_b32 v90, -1\n" \
  "s_lshr_b32 s59, %10, 1\n" \
  "s_add_u32 s59, s59, %7\n" \
  "s_add_u32 s59, s59, 3\n" \
  "s_and_b32 s59, s59, 3\n" \
  "s_lshl_b32 s59, s59, 10\n" \
  "v_add_u32 v83, s59, v1\n" \
  "v_add_u32 v87, s59, v19\n" \
  "v_mov_b32 v91, -1\n" \
  "v_mov_b32 v248, 0\n" \
  "v_mov_b32 v27, 0x3f80\n" \
  "v_and_b32 v25, 7, v36\n" \
  "v_cmp_eq_u32 vcc, 0, v25\n" \
  "v_mov_b32 v28, 1.0\n" \
  "s_nop 0\n" \
  "v_cndmask_b32 v248, v248, v27, vcc\n" \
  "v_cmp_eq_u32 vcc, 1, v25\n" \
  "s_nop 1\n" \
  "v_cndmask_b32 v248, v248, v28, vcc\n" \
  "v_mov_b32 v249, 0\n" \
  "v_mov_b32 v27, 0x3f80\n" \
  "v_and_b32 v25, 7, v36\n" \
  "v_cmp_eq_u32 vcc, 2, v25\n" \
  "v_mov_b32 v28, 1.0\n" \
  "s_nop 0\n" \
  "v_cndmask_b32 v249, v249, v27, vcc\n" \
  "v_cmp_eq_u32 vcc, 3, v25\n" \
  "s_nop 1\n" \
  "v_cndmask_b32 v249, v249, v28, vcc\n" \
  "v_mov_b32 v250, 0\n" \
  "v_mov_b32 v27, 0x3f80\n" \
  "v_and_b32 v25, 7, v36\n" \
  "v_cmp_eq_u32 vcc, 4, v25\n" \
  "v_mov_b32 v28, 1.0\n" \
  "s_nop 0\n" \
  "v_cndmask_b32 v250, v250, v27, vcc\n" \
  "v_cmp_eq_u32 vcc, 5, v25\n" \
  "s_nop 1\n" \
  "v_cndmask_b32 v250, v250, v28, vcc\n" \
  "v_mov_b32 v251, 0\n" \
  "v_mov_b32 v27, 0x3f80\n" \
  "v_and_b32 v25, 7, v36\n" \
  "v_cmp_eq_u32 vcc, 6, v25\n" \
  "v_mov_b32 v28, 1.0\n" \
  "s_nop 0\n" \
  "v_cndmask_b32 v251, v251, v27, vcc\n" \
  "v_cmp_eq_u32 vcc, 7, v25\n" \
  "s_nop 1\n" \
  "v_cndmask_b32 v251, v251, v28, vcc\n" \
  "v_and_b32 v27, 1, v0\n" \
  "v_cmp_eq_u32 vcc, 1, v27\n" \
  "v_mov_b32 v246, 0x05040100\n" \
  "v_mov_b32 v28, 0x03020706\n" \
  "v_cndmask_b32 v246, v246, v28, vcc\n" \
  "v_mov_b32 v252, 0x3f803f80\n" \
  "v_mov_b32 v253, 0x3f803f80\n" \
  "v_mov_b32 v254, 0x3f803f80\n" \
  "v_mov_b32 v255, 0\n" \
  "v_mov_b32 v30, 0\n" \
  "s_mov_b32 s92, 0x3e0293ee\n" \
  "s_mov_b32 s58, 0\n" \
  "s_mov_b32 s48, 0\n" \
  "s_xor_b32 s49, s49, s93\n" \
  "s_lshl_b32 s59, %7, 15\n" \
  "s_add_u32 s49, s49, s59\n" \
  "s_mov_b32 s53, 0\n" \
  "s_or_b32 s51, s51, " STR(EXP_C) "<<21\n" \
  "s_xor_b32 s52, s51, s93\n" \
  "s_and_b32 s59, %6, 16\n" \
  "s_lshl_b32 s59, s59, 10\n" \
  "s_xor_b32 s59, s59, 1052672\n" \
  "s_xor_b32 s52, s52, s59\n" \
  "s_lshr_b32 s59, %7, 1\n" \
  "s_xor_b32 s59, s59, s71\n" \
  "s_and_b32 s59, s59, 1\n" \
  "s_lshl_b32 s59, s59, 14\n" \
  "s_xor_b32 s51, s51, s59\n" \
  "s_lshr_b32 s59, s71, 1\n" \
  "s_lshl_b32 s59, s59, 12\n" \
  "s_xor_b32 s51, s51, s59\n" \
  "s_xor_b32 s51, s51, 1052672\n" \
  "s_and_b32 s59, s70, 7\n" \
  "s_lshl_b32 s59, s59, 16\n" \
  "s_or_b32 s93, s93, s59\n" \
  "s_lshr_b32 s59, %7, 1\n" \
  "s_mul_i32 s59, s59, " STR(QKV_SZ32) "\n" \
  "s_lshl_b32 s60, s70, 14\n" \
  "s_add_u32 s59, s59, s60\n" \
  "s_and_b32 s60, %7, 1\n" \
  "s_lshl_b32 s60, s60, 12\n" \
  "s_add_u32 s59, s59, s60\n" \
  "v_add_u32 v247, s59, v1\n" \
  "s_lshr_b32 s59, s48, 2\n" \
  "s_xnor_b32 s59, s59, %8\n" \
  "s_and_b32 s59, s59, 1\n" \
  "s_lshl_b32 s59, s59, 13\n" \
  "s_lshl_b32 s55, s48, 19\n" \
  "s_or_b32 s55, s55, s59\n" \
  "s_waitcnt vmcnt(24)\n" \
  "s_mov_b32 s60, 0\n" \
  "s_lshr_b32 s59, s60, 2\n" \
  "s_add_u32 s59, s59, " STR(EXP_B) "\n" \
  "v_readlane_b32 s94, v24, s59\n" \
  "s_lshl_b32 s50, s60, 19\n" \
  "s_add_u32 s50, s50, " STR(EXP_B) "<<21\n" \
  "s_xor_b32 s50, s50, s93\n" \
  "s_xor_b32 s50, s50, s94\n" \
  "s_and_b32 s59, s60, 3\n" \
  "s_mul_i32 s59, s59, 0x4800\n" \
  "s_and_b32 s59, s59, 0x5000\n" \
  "s_xor_b32 s50, s50, s59\n" \
  "buffer_store_dwordx4 v[88:91], v15, s[36:39], s50 offen sc1\n" \
  "s_mov_b32 s60, 1\n" \
  "s_lshr_b32 s59, s60, 2\n" \
  "s_add_u32 s59, s59, " STR(EXP_B) "\n" \
  "v_readlane_b32 s94, v24, s59\n" \
  "s_lshl_b32 s50, s60, 19\n" \
  "s_add_u32 s50, s50, " STR(EXP_B) "<<21\n" \
  "s_xor_b32 s50, s50, s93\n" \
  "s_xor_b32 s50, s50, s94\n" \
  "s_and_b32 s59, s60, 3\n" \
  "s_mul_i32 s59, s59, 0x4800\n" \
  "s_and_b32 s59, s59, 0x5000\n" \
  "s_xor_b32 s50, s50, s59\n" \
  "buffer_store_dwordx4 v[88:91], v15, s[36:39], s50 offen sc1\n" \
  "s_mov_b32 s60, 2\n" \
  "s_lshr_b32 s59, s60, 2\n" \
  "s_add_u32 s59, s59, " STR(EXP_B) "\n" \
  "v_readlane_b32 s94, v24, s59\n" \
  "s_lshl_b32 s50, s60, 19\n" \
  "s_add_u32 s50, s50, " STR(EXP_B) "<<21\n" \
  "s_xor_b32 s50, s50, s93\n" \
  "s_xor_b32 s50, s50, s94\n" \
  "s_and_b32 s59, s60, 3\n" \
  "s_mul_i32 s59, s59, 0x4800\n" \
  "s_and_b32 s59, s59, 0x5000\n" \
  "s_xor_b32 s50, s50, s59\n" \
  "buffer_store_dwordx4 v[88:91], v15, s[36:39], s50 offen sc1\n" \
  "s_mov_b32 s60, 3\n" \
  "s_lshr_b32 s59, s60, 2\n" \
  "s_add_u32 s59, s59, " STR(EXP_B) "\n" \
  "v_readlane_b32 s94, v24, s59\n" \
  "s_lshl_b32 s50, s60, 19\n" \
  "s_add_u32 s50, s50, " STR(EXP_B) "<<21\n" \
  "s_xor_b32 s50, s50, s93\n" \
  "s_xor_b32 s50, s50, s94\n" \
  "s_and_b32 s59, s60, 3\n" \
  "s_mul_i32 s59, s59, 0x4800\n" \
  "s_and_b32 s59, s59, 0x5000\n" \
  "s_xor_b32 s50, s50, s59\n" \
  "buffer_store_dwordx4 v[88:91], v15, s[36:39], s50 offen sc1\n" \
  "s_xor_b32 s59, s52, 1052672\n" \
  "buffer_store_dwordx4 v[88:91], v11, s[36:39], s59 offen sc1\n" \
  "buffer_store_dwordx4 v[88:91], v11, s[36:39], s52 offen sc1\n" \
  "s_max_u32 s59, %7, 1\n" \
  "s_lshl_b32 s59, s59, 17\n" \
  "buffer_store_dwordx4 v[88:91], v12, s[36:39], s59 offen sc1\n" \
  "v_min_u32 v64, -2, v64\n" \
  "v_min_u32 v65, -2, v65\n" \
  "v_min_u32 v66, -2, v66\n" \
  "v_min_u32 v67, -2, v67\n" \
  "s_lshl_b32 s59, %7, 29\n" \
  "v_or_b32 v27, s59, v12\n" \
  "buffer_store_dwordx4 v[64:67], v27, s[36:39], 0 offen sc1\n" \
  "s_lshr_b32 s59, %10, 1\n" \
  "s_add_u32 s59, s59, %7\n" \
  "s_add_u32 s59, s59, 1\n" \
  "s_and_b32 s59, s59, 3\n" \
  "s_lshl_b32 s59, s59, 19\n" \
  "s_lshl_b32 s60, %7, 21\n" \
  "s_add_u32 s59, s59, s60\n" \
  "s_add_u32 s59, s59, 262144\n" \
  "s_add_u32 m0, s88, 24576\n" \
  "s_nop 0\n" \
  "buffer_load_dwordx4 v32, s[72:75], s59 offen lds\n" \
  "s_add_u32 m0, s88, 28672\n" \
  "s_nop 0\n" \
  "buffer_load_dwordx4 v32, s[76:79], s59 offen lds\n" \
  "s_add_u32 s59, s59, 65536\n" \
  "s_add_u32 m0, s88, 25600\n" \
  "s_nop 0\n" \
  "buffer_load_dwordx4 v33, s[72:75], s59 offen lds\n" \
  "s_add_u32 m0, s88, 29696\n" \
  "s_nop 0\n" \
  "buffer_load_dwordx4 v33, s[76:79], s59 offen lds\n" \
  "s_add_u32 s59, s59, 65536\n" \
  "s_add_u32 m0, s88, 26624\n" \
  "s_nop 0\n" \
  "buffer_load_dwordx4 v32, s[72:75], s59 offen lds\n" \
  "s_add_u32 m0, s88, 30720\n" \
  "s_nop 0\n" \
  "buffer_load_dwordx4 v32, s[76:79], s59 offen lds\n" \
  "s_add_u32 s59, s59, 65536\n" \
  "s_add_u32 m0, s88, 27648\n" \
  "s_nop 0\n" \
  "buffer_load_dwordx4 v33, s[72:75], s59 offen lds\n" \
  "s_add_u32 m0, s88, 31744\n" \
  "s_nop 0\n" \
  "buffer_load_dwordx4 v33, s[76:79], s59 offen lds\n" \
  "s_lshr_b32 s59, %10, 1\n" \
  "s_add_u32 s59, s59, %7\n" \
  "s_add_u32 s59, s59, 2\n" \
  "s_and_b32 s59, s59, 3\n" \
  "s_lshl_b32 s59, s59, 19\n" \
  "s_lshl_b32 s60, %7, 21\n" \
  "s_add_u32 s59, s59, s60\n" \
  "s_add_u32 s59, s59, 0\n" \
  "s_add_u32 m0, s88, 32768\n" \
  "s_nop 0\n" \
  "buffer_load_dwordx4 v32, s[72:75], s59 offen lds\n" \
  "s_add_u32 m0, s88, 36864\n" \
  "s_nop 0\n" \
  "buffer_load_dwordx4 v32, s[76:79], s59 offen lds\n" \
  "s_add_u32 s59, s59, 65536\n" \
  "s_add_u32 m0, s88, 33792\n" \
  "s_nop 0\n" \
  "buffer_load_dwordx4 v33, s[72:75], s59 offen lds\n" \
  "s_add_u32 m0, s88, 37888\n" \
  "s_nop 0\n" \
  "buffer_load_dwordx4 v33, s[76:79], s59 offen lds\n" \
  "s_add_u32 s59, s59, 65536\n" \
  "s_add_u32 m0, s88, 34816\n" \
  "s_nop 0\n" \
  "buffer_load_dwordx4 v32, s[72:75], s59 offen lds\n" \
  "s_add_u32 m0, s88, 38912\n" \
  "s_nop 0\n" \
  "buffer_load_dwordx4 v32, s[76:79], s59 offen lds\n" \
  "s_add_u32 s59, s59, 65536\n" \
  "s_add_u32 m0, s88, 35840\n" \
  "s_nop 0\n" \
  "buffer_load_dwordx4 v33, s[72:75], s59 offen lds\n" \
  "s_add_u32 m0, s88, 39936\n" \
  "s_nop 0\n" \
  "buffer_load_dwordx4 v33, s[76:79], s59 offen lds\n" \
  "s_waitcnt vmcnt(40)\n" \
  "ds_read_b64_tr_b16 a[0:1], v52 offset:0\n" \
  "ds_read_b64_tr_b16 a[2:3], v52 offset:256\n" \
  "ds_read_b64_tr_b16 a[8:9], v53 offset:0\n" \
  "ds_read_b64_tr_b16 a[10:11], v53 offset:256\n" \
  "ds_read_b64_tr_b16 a[16:17], v54 offset:0\n" \
  "ds_read_b64_tr_b16 a[18:19], v54 offset:256\n" \
  "ds_read_b64_tr_b16 a[24:25], v55 offset:0\n" \
  "ds_read_b64_tr_b16 a[26:27], v55 offset:256\n" \
  "s_waitcnt lgkmcnt(0)\n" \
  "s_lshr_b32 s59, %10, 1\n" \
  "s_add_u32 s59, s59, %7\n" \
  "s_add_u32 s59, s59, 2\n" \
  "s_and_b32 s59, s59, 3\n" \
  "s_lshl_b32 s59, s59, 19\n" \
  "s_lshl_b32 s60, %7, 21\n" \
  "s_add_u32 s59, s59, s60\n" \
  "s_add_u32 s59, s59, 262144\n" \
  "s_add_u32 m0, s88, 0\n" \
  "s_nop 0\n" \
  "buffer_load_dwordx4 v32, s[72:75], s59 offen lds\n" \
  "s_add_u32 m0, s88, 4096\n" \
  "s_nop 0\n" \
  "buffer_load_dwordx4 v32, s[76:79], s59 offen lds\n" \
  "s_add_u32 s59, s59, 65536\n" \
  "s_add_u32 m0, s88, 1024\n" \
  "s_nop 0\n" \
  "buffer_load_dwordx4 v33, s[72:75], s59 offen lds\n" \
  "s_add_u32 m0, s88, 5120\n" \
  "s_nop 0\n" \
  "buffer_load_dwordx4 v33, s[76:79], s59 offen lds\n" \
  "s_add_u32 s59, s59, 65536\n" \
  "s_add_u32 m0, s88, 2048\n" \
  "s_nop 0\n" \
  "buffer_load_dwordx4 v32, s[72:75], s59 offen lds\n" \
  "s_add_u32 m0, s88, 6144\n" \
  "s_nop 0\n" \
  "buffer_load_dwordx4 v32, s[76:79], s59 offen lds\n" \
  "s_add_u32 s59, s59, 65536\n" \
  "s_add_u32 m0, s88, 3072\n" \
  "s_nop 0\n" \
  "buffer_load_dwordx4 v33, s[72:75], s59 offen lds\n" \
  "s_add_u32 m0, s88, 7168\n" \
  "s_nop 0\n" \
  "buffer_load_dwordx4 v33, s[76:79], s59 offen lds\n" \
  "s_waitcnt vmcnt(40)\n" \
  "ds_read_b64_tr_b16 a[4:5], v52 offset:8192\n" \
  "ds_read_b64_tr_b16 a[6:7], v52 offset:8448\n" \
  "ds_read_b64_tr_b16 a[12:13], v53 offset:8192\n" \
  "ds_read_b64_tr_b16 a[14:15], v53 offset:8448\n" \
  "ds_read_b64_tr_b16 a[20:21], v54 offset:8192\n" \
  "ds_read_b64_tr_b16 a[22:23], v54 offset:8448\n" \
  "ds_read_b64_tr_b16 a[28:29], v55 offset:8192\n" \
  "ds_read_b64_tr_b16 a[30:31], v55 offset:8448\n" \
  "s_waitcnt lgkmcnt(0)\n" \
  "s_lshr_b32 s59, %10, 1\n" \
  "s_add_u32 s59, s59, %7\n" \
  "s_add_u32 s59, s59, 3\n" \
  "s_and_b32 s59, s59, 3\n" \
  "s_lshl_b32 s59, s59, 19\n" \
  "s_lshl_b32 s60, %7, 21\n" \
  "s_add_u32 s59, s59, s60\n" \
  "s_add_u32 s59, s59, 0\n" \
  "s_add_u32 m0, s88, 8192\n" \
  "s_nop 0\n" \
  "buffer_load_dwordx4 v32, s[72:75], s59 offen lds\n" \
  "s_add_u32 m0, s88, 12288\n" \
  "s_nop 0\n" \
  "buffer_load_dwordx4 v32, s[76:79], s59 offen lds\n" \
  "s_add_u32 s59, s59, 65536\n" \
  "s_add_u32 m0, s88, 9216\n" \
  "s_nop 0\n" \
  "buffer_load_dwordx4 v33, s[72:75], s59 offen lds\n" \
  "s_add_u32 m0, s88, 13312\n" \
  "s_nop 0\n" \
  "buffer_load_dwordx4 v33, s[76:79], s59 offen lds\n" \
  "s_add_u32 s59, s59, 65536\n" \
  "s_add_u32 m0, s88, 10240\n" \
  "s_nop 0\n" \
  "buffer_load_dwordx4 v32, s[72:75], s59 offen lds\n" \
  "s_add_u32 m0, s88, 14336\n" \
  "s_nop 0\n" \
  "buffer_load_dwordx4 v32, s[76:79], s59 offen lds\n" \
  "s_add_u32 s59, s59, 65536\n" \
  "s_add_u32 m0, s88, 11264\n" \
  "s_nop 0\n" \
  "buffer_load_dwordx4 v33, s[72:75], s59 offen lds\n" \
  "s_add_u32 m0, s88, 15360\n" \
  "s_nop 0\n" \
  "buffer_load_dwordx4 v33, s[76:79], s59 offen lds\n" \
  "s_waitcnt vmcnt(40)\n" \
  "ds_read_b64_tr_b16 a[32:33], v52 offset:16384\n" \
  "ds_read_b64_tr_b16 a[34:35], v52 offset:16640\n" \
  "ds_read_b64_tr_b16 a[40:41], v53 offset:16384\n" \
  "ds_read_b64_tr_b16 a[42:43], v53 offset:16640\n" \
  "ds_read_b64_tr_b16 a[48:49], v54 offset:16384\n" \
  "ds_read_b64_tr_b16 a[50:51], v54 offset:16640\n" \
  "ds_read_b64_tr_b16 a[56:57], v55 offset:16384\n" \
  "ds_read_b64_tr_b16 a[58:59], v55 offset:16640\n" \
  "s_waitcnt lgkmcnt(0)\n" \
  "s_lshr_b32 s59, %10, 1\n" \
  "s_add_u32 s59, s59, %7\n" \
  "s_add_u32 s59, s59, 3\n" \
  "s_and_b32 s59, s59, 3\n" \
  "s_lshl_b32 s59, s59, 19\n" \
  "s_lshl_b32 s60, %7, 21\n" \
  "s_add_u32 s59, s59, s60\n" \
  "s_add_u32 s59, s59, 262144\n" \
  "s_add_u32 m0, s88, 16384\n" \
  "s_nop 0\n" \
  "buffer_load_dwordx4 v32, s[72:75], s59 offen lds\n" \
  "s_add_u32 m0, s88, 20480\n" \
  "s_nop 0\n" \
  "buffer_load_dwordx4 v32, s[76:79], s59 offen lds\n" \
  "s_add_u32 s59, s59, 65536\n" \
  "s_add_u32 m0, s88, 17408\n" \
  "s_nop 0\n" \
  "buffer_load_dwordx4 v33, s[72:75], s59 offen lds\n" \
  "s_add_u32 m0, s88, 21504\n" \
  "s_nop 0\n" \
  "buffer_load_dwordx4 v33, s[76:79], s59 offen lds\n" \
  "s_add_u32 s59, s59, 65536\n" \
  "s_add_u32 m0, s88, 18432\n" \
  "s_nop 0\n" \
  "buffer_load_dwordx4 v32, s[72:75], s59 offen lds\n" \
  "s_add_u32 m0, s88, 22528\n" \
  "s_nop 0\n" \
  "buffer_load_dwordx4 v32, s[76:79], s59 offen lds\n" \
  "s_add_u32 s59, s59, 65536\n" \
  "s_add_u32 m0, s88, 19456\n" \
  "s_nop 0\n" \
  "buffer_load_dwordx4 v33, s[72:75], s59 offen lds\n" \
  "s_add_u32 m0, s88, 23552\n" \
  "s_nop 0\n" \
  "buffer_load_dwordx4 v33, s[76:79], s59 offen lds\n" \
  "s_waitcnt vmcnt(32)\n" \
  "ds_read_b64_tr_b16 a[36:37], v52 offset:24576\n" \
  "ds_read_b64_tr_b16 a[38:39], v52 offset:24832\n" \
  "ds_read_b64_tr_b16 a[44:45], v53 offset:24576\n" \
  "ds_read_b64_tr_b16 a[46:47], v53 offset:24832\n" \
  "ds_read_b64_tr_b16 a[52:53], v54 offset:24576\n" \
  "ds_read_b64_tr_b16 a[54:55], v54 offset:24832\n" \
  "ds_read_b64_tr_b16 a[60:61], v55 offset:24576\n" \
  "ds_read_b64_tr_b16 a[62:63], v55 offset:24832\n" \
  "s_barrier\n" \
  "v_mov_b32 v27, " STR(CW_INIT) "*4\n" \
  "v_mov_b32 v28, 1\n" \
  "v_or_b32 v25, %7, v0\n" \
  "v_lshl_add_u32 v27, v25, 24, v27\n" \
  "buffer_atomic_add v28, v27, s[44:47], 0 offen\n" \
  "s_waitcnt lgkmcnt(0)\n" \
  "s_lshr_b32 s59, %10, 1\n" \
  "s_add_u32 s59, s59, %7\n" \
  "s_add_u32 s59, s59, 0\n" \
  "s_and_b32 s59, s59, 3\n" \
  "s_lshl_b32 s59, s59, 19\n" \
  "s_lshl_b32 s60, %7, 21\n" \
  "s_add_u32 s59, s59, s60\n" \
  "s_add_u32 s59, s59, 0\n" \
  "s_add_u32 m0, s88, 24576\n" \
  "s_nop 0\n" \
  "buffer_load_dwordx4 v32, s[80:83], s59 offen lds\n" \
  "s_add_u32 s59, s59, 65536\n" \
  "s_add_u32 m0, s88, 25600\n" \
  "s_nop 0\n" \
  "buffer_load_dwordx4 v33, s[80:83], s59 offen lds\n" \
  "s_add_u32 s59, s59, 65536\n" \
  "s_add_u32 m0, s88, 26624\n" \
  "s_nop 0\n" \
  "buffer_load_dwordx4 v32, s[80:83], s59 offen lds\n" \
  "s_add_u32 s59, s59, 65536\n" \
  "s_add_u32 m0, s88, 27648\n" \
  "s_nop 0\n" \
  "buffer_load_dwordx4 v33, s[80:83], s59 offen lds\n" \
  "s_add_u32 s59, s59, 65536\n" \
  "s_add_u32 m0, s88, 28672\n" \
  "s_nop 0\n" \
  "buffer_load_dwordx4 v32, s[80:83], s59 offen lds\n" \
  "s_add_u32 s59, s59, 65536\n" \
  "s_add_u32 m0, s88, 29696\n" \
  "s_nop 0\n" \
  "buffer_load_dwordx4 v33, s[80:83], s59 offen lds\n" \
  "s_add_u32 s59, s59, 65536\n" \
  "s_add_u32 m0, s88, 30720\n" \
  "s_nop 0\n" \
  "buffer_load_dwordx4 v32, s[80:83], s59 offen lds\n" \
  "s_add_u32 s59, s59, 65536\n" \
  "s_add_u32 m0, s88, 31744\n" \
  "s_nop 0\n" \
  "buffer_load_dwordx4 v33, s[80:83], s59 offen lds\n" \
  "s_waitcnt vmcnt(33)\n" \
  "ds_read_b64_tr_b16 a[64:65], v52 offset:32768\n" \
  "ds_read_b64_tr_b16 a[66:67], v52 offset:33024\n" \
  "ds_read_b64_tr_b16 a[72:73], v53 offset:32768\n" \
  "ds_read_b64_tr_b16 a[74:75], v53 offset:33024\n" \
  "ds_read_b64_tr_b16 a[80:81], v54 offset:32768\n" \
  "ds_read_b64_tr_b16 a[82:83], v54 offset:33024\n" \
  "ds_read_b64_tr_b16 a[88:89], v55 offset:32768\n" \
  "ds_read_b64_tr_b16 a[90:91], v55 offset:33024\n" \
  "s_waitcnt lgkmcnt(0)\n" \
  "s_lshr_b32 s59, %10, 1\n" \
  "s_add_u32 s59, s59, %7\n" \
  "s_add_u32 s59, s59, 1\n" \
  "s_and_b32 s59, s59, 3\n" \
  "s_lshl_b32 s59, s59, 19\n" \
  "s_lshl_b32 s60, %7, 21\n" \
  "s_add_u32 s59, s59, s60\n" \
  "s_add_u32 s59, s59, 0\n" \
  "s_add_u32 m0, s88, 32768\n" \
  "s_nop 0\n" \
  "buffer_load_dwordx4 v32, s[80:83], s59 offen lds\n" \
  "s_add_u32 s59, s59, 65536\n" \
  "s_add_u32 m0, s88, 33792\n" \
  "s_nop 0\n" \
  "buffer_load_dwordx4 v33, s[80:83], s59 offen lds\n" \
  "s_add_u32 s59, s59, 65536\n" \
  "s_add_u32 m0, s88, 34816\n" \
  "s_nop 0\n" \
  "buffer_load_dwordx4 v32, s[80:83], s59 offen lds\n" \
  "s_add_u32 s59, s59, 65536\n" \
  "s_add_u32 m0, s88, 35840\n" \
  "s_nop 0\n" \
  "buffer_load_dwordx4 v33, s[80:83], s59 offen lds\n" \
  "s_add_u32 s59, s59, 65536\n" \
  "s_add_u32 m0, s88, 36864\n" \
  "s_nop 0\n" \
  "buffer_load_dwordx4 v32, s[80:83], s59 offen lds\n" \
  "s_add_u32 s59, s59, 65536\n" \
  "s_add_u32 m0, s88, 37888\n" \
  "s_nop 0\n" \
  "buffer_load_dwordx4 v33, s[80:83], s59 offen lds\n" \
  "s_add_u32 s59, s59, 65536\n" \
  "s_add_u32 m0, s88, 38912\n" \
  "s_nop 0\n" \
  "buffer_load_dwordx4 v32, s[80:83], s59 offen lds\n" \
  "s_add_u32 s59, s59, 65536\n" \
  "s_add_u32 m0, s88, 39936\n" \
  "s_nop 0\n" \
  "buffer_load_dwordx4 v33, s[80:83], s59 offen lds\n" \
  "s_waitcnt vmcnt(33)\n" \
  "ds_read_b64_tr_b16 a[68:69], v52 offset:0\n" \
  "ds_read_b64_tr_b16 a[70:71], v52 offset:256\n" \
  "ds_read_b64_tr_b16 a[76:77], v53 offset:0\n" \
  "ds_read_b64_tr_b16 a[78:79], v53 offset:256\n" \
  "ds_read_b64_tr_b16 a[84:85], v54 offset:0\n" \
  "ds_read_b64_tr_b16 a[86:87], v54 offset:256\n" \
  "ds_read_b64_tr_b16 a[92:93], v55 offset:0\n" \
  "ds_read_b64_tr_b16 a[94:95], v55 offset:256\n" \
  "s_waitcnt lgkmcnt(0)\n" \
  "s_lshr_b32 s59, %10, 1\n" \
  "s_add_u32 s59, s59, %7\n" \
  "s_add_u32 s59, s59, 2\n" \
  "s_and_b32 s59, s59, 3\n" \
  "s_lshl_b32 s59, s59, 19\n" \
  "s_lshl_b32 s60, %7, 21\n" \
  "s_add_u32 s59, s59, s60\n" \
  "s_add_u32 s59, s59, 0\n" \
  "s_add_u32 m0, s88, 0\n" \
  "s_nop 0\n" \
  "buffer_load_dwordx4 v32, s[80:83], s59 offen lds\n" \
  "s_add_u32 s59, s59, 65536\n" \
  "s_add_u32 m0, s88, 1024\n" \
  "s_nop 0\n" \
  "buffer_load_dwordx4 v33, s[80:83], s59 offen lds\n" \
  "s_add_u32 s59, s59, 65536\n" \
  "s_add_u32 m0, s88, 2048\n" \
  "s_nop 0\n" \
  "buffer_load_dwordx4 v32, s[80:83], s59 offen lds\n" \
  "s_add_u32 s59, s59, 65536\n" \
  "s_add_u32 m0, s88, 3072\n" \
  "s_nop 0\n" \
  "buffer_load_dwordx4 v33, s[80:83], s59 offen lds\n" \
  "s_add_u32 s59, s59, 65536\n" \
  "s_add_u32 m0, s88, 4096\n" \
  "s_nop 0\n" \
  "buffer_load_dwordx4 v32, s[80:83], s59 offen lds\n" \
  "s_add_u32 s59, s59, 65536\n" \
  "s_add_u32 m0, s88, 5120\n" \
  "s_nop 0\n" \
  "buffer_load_dwordx4 v33, s[80:83], s59 offen lds\n" \
  "s_add_u32 s59, s59, 65536\n" \
  "s_add_u32 m0, s88, 6144\n" \
  "s_nop 0\n" \
  "buffer_load_dwordx4 v32, s[80:83], s59 offen lds\n" \
  "s_add_u32 s59, s59, 65536\n" \
  "s_add_u32 m0, s88, 7168\n" \
  "s_nop 0\n" \
  "buffer_load_dwordx4 v33, s[80:83], s59 offen lds\n" \
  "s_waitcnt vmcnt(33)\n" \
  "ds_read_b64_tr_b16 a[96:97], v52 offset:8192\n" \
  "ds_read_b64_tr_b16 a[98:99], v52 offset:8448\n" \
  "ds_read_b64_tr_b16 a[104:105], v53 offset:8192\n" \
  "ds_read_b64_tr_b16 a[106:107], v53 offset:8448\n" \
  "ds_read_b64_tr_b16 a[112:113], v54 offset:8192\n" \
  "ds_read_b64_tr_b16 a[114:115], v54 offset:8448\n" \
  "ds_read_b64_tr_b16 a[120:121], v55 offset:8192\n" \
  "ds_read_b64_tr_b16 a[122:123], v55 offset:8448\n" \
  "s_waitcnt lgkmcnt(0)\n" \
  "s_lshr_b32 s59, %10, 1\n" \
  "s_add_u32 s59, s59, %7\n" \
  "s_add_u32 s59, s59, 3\n" \
  "s_and_b32 s59, s59, 3\n" \
  "s_lshl_b32 s59, s59, 19\n" \
  "s_lshl_b32 s60, %7, 21\n" \
  "s_add_u32 s59, s59, s60\n" \
  "s_add_u32 s59, s59, 0\n" \
  "s_add_u32 m0, s88, 8192\n" \
  "s_nop 0\n" \
  "buffer_load_dwordx4 v32, s[80:83], s59 offen lds\n" \
  "s_add_u32 s59, s59, 65536\n" \
  "s_add_u32 m0, s88, 9216\n" \
  "s_nop 0\n" \
  "buffer_load_dwordx4 v33, s[80:83], s59 offen lds\n" \
  "s_add_u32 s59, s59, 65536\n" \
  "s_add_u32 m0, s88, 10240\n" \
  "s_nop 0\n" \
  "buffer_load_dwordx4 v32, s[80:83], s59 offen lds\n" \
  "s_add_u32 s59, s59, 65536\n" \
  "s_add_u32 m0, s88, 11264\n" \
  "s_nop 0\n" \
  "buffer_load_dwordx4 v33, s[80:83], s59 offen lds\n" \
  "s_add_u32 s59, s59, 65536\n" \
  "s_add_u32 m0, s88, 12288\n" \
  "s_nop 0\n" \
  "buffer_load_dwordx4 v32, s[80:83], s59 offen lds\n" \
  "s_add_u32 s59, s59, 65536\n" \
  "s_add_u32 m0, s88, 13312\n" \
  "s_nop 0\n" \
  "buffer_load_dwordx4 v33, s[80:83], s59 offen lds\n" \
  "s_add_u32 s59, s59, 65536\n" \
  "s_add_u32 m0, s88, 14336\n" \
  "s_nop 0\n" \
  "buffer_load_dwordx4 v32, s[80:83], s59 offen lds\n" \
  "s_add_u32 s59, s59, 65536\n" \
  "s_add_u32 m0, s88, 15360\n" \
  "s_nop 0\n" \
  "buffer_load_dwordx4 v33, s[80:83], s59 offen lds\n" \
  "s_waitcnt vmcnt(33)\n" \
  "ds_read_b64_tr_b16 a[100:101], v52 offset:16384\n" \
  "ds_read_b64_tr_b16 a[102:103], v52 offset:16640\n" \
  "ds_read_b64_tr_b16 a[108:109], v53 offset:16384\n" \
  "ds_read_b64_tr_b16 a[110:111], v53 offset:16640\n" \
  "ds_read_b64_tr_b16 a[116:117], v54 offset:16384\n" \
  "ds_read_b64_tr_b16 a[118:119], v54 offset:16640\n" \
  "ds_read_b64_tr_b16 a[124:125], v55 offset:16384\n" \
  "ds_read_b64_tr_b16 a[126:127], v55 offset:16640\n" \
  "s_waitcnt lgkmcnt(0)\n" \
  "s_lshl_b32 s59, %7, 7\n" \
  "s_add_u32 s59, s59, 0\n" \
  "s_add_u32 m0, s88, 16384\n" \
  "s_nop 0\n" \
  "buffer_load_dwordx4 v34, s[84:87], s59 offen lds\n" \
  "s_add_u32 s59, s59, 16384\n" \
  "s_add_u32 m0, s88, 17408\n" \
  "s_nop 0\n" \
  "buffer_load_dwordx4 v34, s[84:87], s59 offen lds\n" \
  "s_add_u32 s59, s59, 16384\n" \
  "s_add_u32 m0, s88, 18432\n" \
  "s_nop 0\n" \
  "buffer_load_dwordx4 v34, s[84:87], s59 offen lds\n" \
  "s_add_u32 s59, s59, 16384\n" \
  "s_add_u32 m0, s88, 19456\n" \
  "s_nop 0\n" \
  "buffer_load_dwordx4 v34, s[84:87], s59 offen lds\n" \
  "s_add_u32 s59, s59, 16384\n" \
  "s_add_u32 m0, s88, 20480\n" \
  "s_nop 0\n" \
  "buffer_load_dwordx4 v34, s[84:87], s59 offen lds\n" \
  "s_add_u32 s59, s59, 16384\n" \
  "s_add_u32 m0, s88, 21504\n" \
  "s_nop 0\n" \
  "buffer_load_dwordx4 v34, s[84:87], s59 offen lds\n" \
  "s_add_u32 s59, s59, 16384\n" \
  "s_add_u32 m0, s88, 22528\n" \
  "s_nop 0\n" \
  "buffer_load_dwordx4 v34, s[84:87], s59 offen lds\n" \
  "s_add_u32 s59, s59, 16384\n" \
  "s_add_u32 m0, s88, 23552\n" \
  "s_nop 0\n" \
  "buffer_load_dwordx4 v34, s[84:87], s59 offen lds\n" \
  "s_waitcnt vmcnt(32)\n" \
  "ds_read_b64_tr_b16 a[128:129], v42 offset:24576\n" \
  "ds_read_b64_tr_b16 a[130:131], v42 offset:24832\n" \
  "ds_read_b64_tr_b16 a[132:133], v42 offset:28672\n" \
  "ds_read_b64_tr_b16 a[134:135], v42 offset:28928\n" \
  "ds_read_b64_tr_b16 a[136:137], v43 offset:24576\n" \
  "ds_read_b64_tr_b16 a[138:139], v43 offset:24832\n" \
  "ds_read_b64_tr_b16 a[140:141], v43 offset:28672\n" \
  "ds_read_b64_tr_b16 a[142:143], v43 offset:28928\n" \
  "s_waitcnt lgkmcnt(0)\n" \
  "s_lshl_b32 s59, %7, 7\n" \
  "s_add_u32 s59, s59, 131072\n" \
  "s_add_u32 m0, s88, 24576\n" \
  "s_nop 0\n" \
  "buffer_load_dwordx4 v34, s[84:87], s59 offen lds\n" \
  "s_add_u32 s59, s59, 16384\n" \
  "s_add_u32 m0, s88, 25600\n" \
  "s_nop 0\n" \
  "buffer_load_dwordx4 v34, s[84:87], s59 offen lds\n" \
  "s_add_u32 s59, s59, 16384\n" \
  "s_add_u32 m0, s88, 26624\n" \
  "s_nop 0\n" \
  "buffer_load_dwordx4 v34, s[84:87], s59 offen lds\n" \
  "s_add_u32 s59, s59, 16384\n" \
  "s_add_u32 m0, s88, 27648\n" \
  "s_nop 0\n" \
  "buffer_load_dwordx4 v34, s[84:87], s59 offen lds\n" \
  "s_add_u32 s59, s59, 16384\n" \
  "s_add_u32 m0, s88, 28672\n" \
  "s_nop 0\n" \
  "buffer_load_dwordx4 v34, s[84:87], s59 offen lds\n" \
  "s_add_u32 s59, s59, 16384\n" \
  "s_add_u32 m0, s88, 29696\n" \
  "s_nop 0\n" \
  "buffer_load_dwordx4 v34, s[84:87], s59 offen lds\n" \
  "s_add_u32 s59, s59, 16384\n" \
  "s_add_u32 m0, s88, 30720\n" \
  "s_nop 0\n" \
  "buffer_load_dwordx4 v34, s[84:87], s59 offen lds\n" \
  "s_add_u32 s59, s59, 16384\n" \
  "s_add_u32 m0, s88, 31744\n" \
  "s_nop 0\n" \
  "buffer_load_dwordx4 v34, s[84:87], s59 offen lds\n" \
  "s_memtime s[72:73]\n" \
  "s_memrealtime s[74:75]\n" \
  "s_nop 1\n" \
  "s_memrealtime s[76:77]\n" \
  "s_nop 1\n" \
  "s_memrealtime s[78:79]\n" \
  "s_nop 1\n" \
  "s_memrealtime s[80:81]\n" \
  "s_nop 1\n" \
  "s_memrealtime s[82:83]\n" \
  "s_nop 1\n" \
  "s_memrealtime s[84:85]\n" \
  "s_nop 1\n" \
  "s_memrealtime s[86:87]\n" \
  "s_nop 1\n" \
  "s_memrealtime s[88:89]\n" \
  "s_nop 1\n" \
  "s_memrealtime s[90:91]\n" \
  "s_mov_b32 s59, 0\n" \
  "s_waitcnt lgkmcnt(0)\n" \
  "s_cmp_eq_u32 s76, s74\n" \
  "s_addc_u32 s59, s59, 0\n" \
  "s_cmp_eq_u32 s78, s74\n" \
  "s_addc_u32 s59, s59, 0\n" \
  "s_cmp_eq_u32 s80, s74\n" \
  "s_addc_u32 s59, s59, 0\n" \
  "s_cmp_eq_u32 s82, s74\n" \
  "s_addc_u32 s59, s59, 0\n" \
  "s_cmp_eq_u32 s84, s74\n" \
  "s_addc_u32 s59, s59, 0\n" \
  "s_cmp_eq_u32 s86, s74\n" \
  "s_addc_u32 s59, s59, 0\n" \
  "s_cmp_eq_u32 s88, s74\n" \
  "s_addc_u32 s59, s59, 0\n" \
  "s_cmp_eq_u32 s90, s74\n" \
  "s_addc_u32 s59, s59, 0\n" \
  "s_mul_i32 s59, s59, 12\n" \
  "s_add_u32 s68, s72, s59\n" \
  "s_add_u32 s59, s74, 4\n" \
  "s_mul_i32 s69, s59, 24\n" \
  "s_mov_b32 s62, s69\n" \
  "s_mov_b32 s56, s68\n" \
  "s_waitcnt vmcnt(32)\n" \
  "ds_read_b64_tr_b16 a[144:145], v42 offset:32768\n" \
  "ds_read_b64_tr_b16 a[146:147], v42 offset:33024\n" \
  "ds_read_b64_tr_b16 a[148:149], v42 offset:36864\n" \
  "ds_read_b64_tr_b16 a[150:151], v42 offset:37120\n" \
  "ds_read_b64_tr_b16 a[152:153], v43 offset:32768\n" \
  "ds_read_b64_tr_b16 a[154:155], v43 offset:33024\n" \
  "ds_read_b64_tr_b16 a[156:157], v43 offset:36864\n" \
  "ds_read_b64_tr_b16 a[158:159], v43 offset:37120\n" \
  "s_waitcnt vmcnt(24)\n" \
  "ds_read_b64_tr_b16 a[160:161], v42 offset:0\n" \
  "ds_read_b64_tr_b16 a[162:163], v42 offset:256\n" \
  "ds_read_b64_tr_b16 a[164:165], v42 offset:4096\n" \
  "ds_read_b64_tr_b16 a[166:167], v42 offset:4352\n" \
  "ds_read_b64_tr_b16 a[168:169], v43 offset:0\n" \
  "ds_read_b64_tr_b16 a[170:171], v43 offset:256\n" \
  "ds_read_b64_tr_b16 a[172:173], v43 offset:4096\n" \
  "ds_read_b64_tr_b16 a[174:175], v43 offset:4352\n" \
  "s_waitcnt vmcnt(16)\n" \
  "ds_read_b64_tr_b16 a[176:177], v42 offset:8192\n" \
  "ds_read_b64_tr_b16 a[178:179], v42 offset:8448\n" \
  "ds_read_b64_tr_b16 a[180:181], v42 offset:12288\n" \
  "ds_read_b64_tr_b16 a[182:183], v42 offset:12544\n" \
  "ds_read_b64_tr_b16 a[184:185], v43 offset:8192\n" \
  "ds_read_b64_tr_b16 a[186:187], v43 offset:8448\n" \
  "ds_read_b64_tr_b16 a[188:189], v43 offset:12288\n" \
  "ds_read_b64_tr_b16 a[190:191], v43 offset:12544\n" \
  "s_waitcnt vmcnt(8)\n" \
  "ds_read_b64_tr_b16 a[192:193], v49 offset:16384\n" \
  "ds_read_b64_tr_b16 a[194:195], v49 offset:16640\n" \
  "ds_read_b64_tr_b16 a[196:197], v49 offset:20480\n" \
  "ds_read_b64_tr_b16 a[198:199], v49 offset:20736\n" \
  "ds_read_b64_tr_b16 a[208:209], v49 offset:16400\n" \
  "ds_read_b64_tr_b16 a[210:211], v49 offset:16656\n" \
  "ds_read_b64_tr_b16 a[212:213], v49 offset:20496\n" \
  "ds_read_b64_tr_b16 a[214:215], v49 offset:20752\n" \
  "ds_read_b64_tr_b16 a[224:225], v49 offset:16392\n" \
  "ds_read_b64_tr_b16 a[226:227], v49 offset:16648\n" \
  "ds_read_b64_tr_b16 a[228:229], v49 offset:20488\n" \
  "ds_read_b64_tr_b16 a[230:231], v49 offset:20744\n" \
  "ds_read_b64_tr_b16 a[240:241], v49 offset:16408\n" \
  "ds_read_b64_tr_b16 a[242:243], v49 offset:16664\n" \
  "ds_read_b64_tr_b16 a[244:245], v49 offset:20504\n" \
  "ds_read_b64_tr_b16 a[246:247], v49 offset:20760\n" \
  "s_waitcnt vmcnt(0)\n" \
  "ds_read_b64_tr_b16 a[200:201], v49 offset:24576\n" \
  "ds_read_b64_tr_b16 a[202:203], v49 offset:24832\n" \
  "ds_read_b64_tr_b16 a[204:205], v49 offset:28672\n" \
  "ds_read_b64_tr_b16 a[206:207], v49 offset:28928\n" \
  "ds_read_b64_tr_b16 a[216:217], v49 offset:24592\n" \
  "ds_read_b64_tr_b16 a[218:219], v49 offset:24848\n" \
  "ds_read_b64_tr_b16 a[220:221], v49 offset:28688\n" \
  "ds_read_b64_tr_b16 a[222:223], v49 offset:28944\n" \
  "ds_read_b64_tr_b16 a[232:233], v49 offset:24584\n" \
  "ds_read_b64_tr_b16 a[234:235], v49 offset:24840\n" \
  "ds_read_b64_tr_b16 a[236:237], v49 offset:28680\n" \
  "ds_read_b64_tr_b16 a[238:239], v49 offset:28936\n" \
  "ds_read_b64_tr_b16 a[248:249], v49 offset:24600\n" \
  "ds_read_b64_tr_b16 a[250:251], v49 offset:24856\n" \
  "ds_read_b64_tr_b16 a[252:253], v49 offset:28696\n" \
  "ds_read_b64_tr_b16 a[254:255], v49 offset:28952\n" \
  "s_waitcnt lgkmcnt(0)\n" \
  "s_barrier\n" \
  "s_cmp_lg_u32 %7, 2\n" \
  "s_cbranch_scc1 LI6_%=\n" \
  "v_mov_b32 v25, " STR(CW_INIT) "*4\n" \
  "LI1a_%=:\n" \
  "buffer_load_dword v23, v25, s[44:47], 0 offen sc1\n" \
  "s_waitcnt vmcnt(0)\n" \
  "v_cmp_gt_u32 vcc, 128, v23\n" \
  "s_cbranch_vccz LI1b_%=\n" \
  "s_sleep 24\n" \
  "s_add_u32 s58, s58, 1\n" \
  "s_cmp_lt_u32 s58, " STR(SPIN_CAP) "\n" \
  "s_cbranch_scc1 LI1a_%=\n" \
  "v_mov_b32 v27, " STR(CW_DEAD) "*4\n" \
  "v_mov_b32 v28, 1\n" \
  "buffer_store_dword v28, v27, s[44:47], 0 offen sc1\n" \
  "s_branch LI1b_%=\n" \
  "LI1b_%=:\n" \
  "LI6_%=:\n" \
  "LI2a_%=:\n" \
  "buffer_load_dword v23, v22, s[44:47], 0 offen sc1\n" \
  "s_waitcnt vmcnt(0)\n" \
  "v_cmp_gt_u32 vcc, 1, v23\n" \
  "s_cbranch_vccz LI2b_%=\n" \
  "s_sleep 8\n" \
  "s_add_u32 s58, s58, 1\n" \
  "s_cmp_lt_u32 s58, " STR(SPIN_CAP) "\n" \
  "s_cbranch_scc1 LI2a_%=\n" \
  "v_mov_b32 v27, " STR(CW_DEAD) "*4\n" \
  "v_mov_b32 v28, 1\n" \
  "buffer_store_dword v28, v27, s[44:47], 0 offen sc1\n" \
  "s_branch LI2b_%=\n" \
  "LI2b_%=:\n" \
  "buffer_load_dwordx4 v[200:203], v247, s[40:43], s55 offen offset:0 sc1\n" \
  "buffer_load_dwordx4 v[204:207], v247, s[40:43], s55 offen offset:1024 sc1\n" \
  "buffer_load_dwordx4 v[208:211], v247, s[40:43], s55 offen offset:2048 sc1\n" \
  "buffer_load_dwordx4 v[212:215], v247, s[40:43], s55 offen offset:3072 sc1\n" \
  "s_add_u32 s55, s55, 1<<19\n" \
  "s_waitcnt vmcnt(0)\n" \
  "ds_write_b128 v18, v[200:203] offset:0\n" \
  "ds_write_b128 v18, v[204:207] offset:1024\n" \
  "ds_write_b128 v18, v[208:211] offset:2048\n" \
  "ds_write_b128 v18, v[212:215] offset:3072\n" \
  "v_xor_b32 v18, 16384, v18\n" \
  "s_waitcnt lgkmcnt(0)\n" \
  "s_barrier\n" \
  "ds_read_b64_tr_b16 v[136:137], v20 offset:0\n" \
  "ds_read_b64_tr_b16 v[138:139], v21 offset:0\n" \
  "ds_read_b64_tr_b16 v[152:153], v20 offset:8\n" \
  "ds_read_b64_tr_b16 v[154:155], v21 offset:8\n" \
  "ds_read_b64_tr_b16 v[140:141], v20 offset:2048\n" \
  "ds_read_b64_tr_b16 v[142:143], v21 offset:2048\n" \
  "ds_read_b64_tr_b16 v[156:157], v20 offset:2056\n" \
  "ds_read_b64_tr_b16 v[158:159], v21 offset:2056\n" \
  "ds_read_b64_tr_b16 v[144:145], v20 offset:4096\n" \
  "ds_read_b64_tr_b16 v[146:147], v21 offset:4096\n" \
  "ds_read_b64_tr_b16 v[160:161], v20 offset:4104\n" \
  "ds_read_b64_tr_b16 v[162:163], v21 offset:4104\n" \
  "ds_read_b64_tr_b16 v[148:149], v20 offset:6144\n" \
  "ds_read_b64_tr_b16 v[150:151], v21 offset:6144\n" \
  "ds_read_b64_tr_b16 v[164:165], v20 offset:6152\n" \
  "ds_read_b64_tr_b16 v[166:167], v21 offset:6152\n" \
  "ds_read_b128 v[104:107], v84 offset:0\n" \
  "ds_read_b128 v[108:111], v85 offset:0\n" \
  "ds_read_b128 v[112:115], v86 offset:0\n" \
  "ds_read_b128 v[116:119], v87 offset:0\n" \
  "ds_read_b128 v[120:123], v84 offset:4096\n" \
  "ds_read_b128 v[124:127], v85 offset:4096\n" \
  "ds_read_b128 v[128:131], v86 offset:4096\n" \
  "ds_read_b128 v[132:135], v87 offset:4096\n" \
  "v_xor_b32 v20, 16384, v20\n" \
  "v_xor_b32 v21, 16384, v21\n" \
  "v_xor_b32 v84, 16384, v84\n" \
  "v_xor_b32 v85, 16384, v85\n" \
  "v_xor_b32 v86, 16384, v86\n" \
  "v_xor_b32 v87, 16384, v87\n" \
  "s_waitcnt lgkmcnt(0)\n" \
  "v_mfma_f32_16x16x32_bf16 v[184:187], v[136:139], a[192:195], 0\n" \
  "v_mfma_f32_16x16x32_bf16 v[188:191], v[152:155], a[192:195], 0\n" \
  "v_mfma_f32_16x16x32_bf16 v[192:195], v[136:139], a[208:211], 0\n" \
  "v_mfma_f32_16x16x32_bf16 v[196:199], v[152:155], a[208:211], 0\n" \
  "v_mfma_f32_16x16x32_bf16 v[184:187], v[140:143], a[196:199], v[184:187]\n" \
  "v_mfma_f32_16x16x32_bf16 v[188:191], v[156:159], a[196:199], v[188:191]\n" \
  "v_mfma_f32_16x16x32_bf16 v[192:195], v[140:143], a[212:215], v[192:195]\n" \
  "v_mfma_f32_16x16x32_bf16 v[196:199], v[156:159], a[212:215], v[196:199]\n" \
  "v_mfma_f32_16x16x32_bf16 v[184:187], v[144:147], a[200:203], v[184:187]\n" \
  "v_mfma_f32_16x16x32_bf16 v[188:191], v[160:163], a[200:203], v[188:191]\n" \
  "v_mfma_f32_16x16x32_bf16 v[192:195], v[144:147], a[216:219], v[192:195]\n" \
  "v_mfma_f32_16x16x32_bf16 v[196:199], v[160:163], a[216:219], v[196:199]\n" \
  "v_mfma_f32_16x16x32_bf16 v[184:187], v[148:151], a[204:207], v[184:187]\n" \
  "v_mfma_f32_16x16x32_bf16 v[188:191], v[164:167], a[204:207], v[188:191]\n" \
  "v_mfma_f32_16x16x32_bf16 v[192:195], v[148:151], a[220:223], v[192:195]\n" \
  "v_mfma_f32_16x16x32_bf16 v[196:199], v[164:167], a[220:223], v[196:199]\n" \
  "s_memtime s[72:73]\n" \
  "s_memrealtime s[74:75]\n" \
  "s_nop 1\n" \
  "s_memrealtime s[76:77]\n" \
  "s_nop 1\n" \
  "s_memrealtime s[78:79]\n" \
  "s_nop 1\n" \
  "s_memrealtime s[80:81]\n" \
  "s_nop 1\n" \
  "s_memrealtime s[82:83]\n" \
  "s_nop 1\n" \
  "s_memrealtime s[84:85]\n" \
  "s_nop 1\n" \
  "s_memrealtime s[86:87]\n" \
  "s_nop 1\n" \
  "s_memrealtime s[88:89]\n" \
  "s_nop 1\n" \
  "s_memrealtime s[90:91]\n" \
  "s_mov_b32 s59, 0\n" \
  "s_waitcnt lgkmcnt(0)\n" \
  "s_cmp_eq_u32 s76, s74\n" \
  "s_addc_u32 s59, s59, 0\n" \
  "s_cmp_eq_u32 s78, s74\n" \
  "s_addc_u32 s59, s59, 0\n" \
  "s_cmp_eq_u32 s80, s74\n" \
  "s_addc_u32 s59, s59, 0\n" \
  "s_cmp_eq_u32 s82, s74\n" \
  "s_addc_u32 s59, s59, 0\n" \
  "s_cmp_eq_u32 s84, s74\n" \
  "s_addc_u32 s59, s59, 0\n" \
  "s_cmp_eq_u32 s86, s74\n" \
  "s_addc_u32 s59, s59, 0\n" \
  "s_cmp_eq_u32 s88, s74\n" \
  "s_addc_u32 s59, s59, 0\n" \
  "s_cmp_eq_u32 s90, s74\n" \
  "s_addc_u32 s59, s59, 0\n" \
  "s_mul_i32 s59, s59, 12\n" \
  "s_add_u32 s68, s72, s59\n" \
  "s_add_u32 s59, s74, 4\n" \
  "s_mul_i32 s69, s59, 24\n" \
  "v_cvt_pk_bf16_f32 v168, v184, v185\n" \
  "v_cvt_pk_bf16_f32 v169, v186, v187\n" \
  "v_cvt_pk_bf16_f32 v170, v188, v189\n" \
  "v_cvt_pk_bf16_f32 v171, v190, v191\n" \
  "v_cvt_pk_bf16_f32 v172, v192, v193\n" \
  "v_cvt_pk_bf16_f32 v173, v194, v195\n" \
  "v_cvt_pk_bf16_f32 v174, v196, v197\n" \
  "v_cvt_pk_bf16_f32 v175, v198, v199\n" \
  "v_mov_b32 v95, " STR(RI0) "\n" \
  "s_sub_u32 s60, s69, s62\n" \
  "s_sub_u32 s59, s68, s56\n" \
  "s_mov_b32 s62, s69\n" \
  "s_mov_b32 s56, s68\n" \
  "v_cvt_f32_u32 v27, s59\n" \
  "v_cvt_f32_u32 v28, s60\n" \
  "v_rcp_f32 v26, v27\n" \
  "v_rcp_f32 v19, v28\n" \
  "v_mul_f32 v26, v26, v28\n" \
  "v_mul_f32 v95, " STR(DECAY) ", v95\n" \
  "v_mul_f32 v19, v19, v27\n" \
  "v_max_f32 v95, v95, v26\n" \
  "v_cvt_f32_u32 v28, " STR(F_P) "\n" \
  "v_mul_f32 v27, v95, v28\n" \
  "v_cvt_u32_f32 v27, v27\n" \
  "v_min_u32 v27, 0x7fff, v27\n" \
  "s_nop 0\n" \
  "v_readfirstlane_b32 s63, v27\n" \
  "s_add_u32 s59, s74, 4+63\n" \
  "s_and_b32 s59, s59, -64\n" \
  "s_mul_i32 s59, s59, 24\n" \
  "s_sub_u32 s59, s59, s69\n" \
  "v_cvt_f32_u32 v27, s59\n" \
  "v_mul_f32 v28, 0x44c00000, v19\n" \
  "v_mul_f32 v27, v27, v19\n" \
  "v_cvt_u32_f32 v28, v28\n" \
  "v_cvt_u32_f32 v27, v27\n" \
  "v_max_u32 v28, " STR(T_Q) ", v28\n" \
  "v_min_u32 v28, 2047, v28\n" \
  "v_readfirstlane_b32 s59, v27\n" \
  "v_readfirstlane_b32 s95, v28\n" \
  "v_subrev_u32 v28, " STR(T_Q) ", v28\n" \
  "v_lshlrev_b32 v28, 2, v28\n" \
  "v_mov_b32 v27, 0\n" \
  "s_add_u32 s59, s59, s68\n" \
  "s_cmp_lg_u32 %7, 1\n" \
  "s_cbranch_scc1 LI8_%=\n" \
  "ds_write_b32 v255, v28 offset:14344\n" \
  "ds_write_b32 v255, v27 offset:14356\n" \
  "v_mov_b32 v92, s59\n" \
  "v_mov_b32 v93, 0\n" \
  "ds_write_b64 v255, v[92:93] offset:14336\n" \
  "LI8_%=:\n" \
  "s_waitcnt lgkmcnt(0)\n" \
  "s_lshl_b32 s59, s70, 7\n" \
  "s_add_u32 s59, s59, " STR(CW_PROG) "*4\n" \
  "s_lshl_b32 s60, s71, 2\n" \
  "s_or_b32 s60, s60, %7\n" \
  "s_cmp_eq_u32 s60, 3\n" \
  "s_cselect_b32 s59, s59, 0x80000000\n" \
  "v_lshl_add_u32 v0, v0, 24, s59\n" \
  "v_mov_b64 v[48:49], 0\n" \
  "v_mov_b64 v[50:51], 0\n" \
  "v_mov_b64 v[52:53], 0\n" \
  "v_mov_b64 v[54:55], 0\n" \
  "v_mov_b64 v[56:57], 0\n" \
  "v_mov_b64 v[58:59], 0\n" \
  "v_mov_b64 v[60:61], 0\n" \
  "v_mov_b64 v[62:63], 0\n" \
  "v_mov_b64 v[64:65], 0\n" \
  "v_mov_b64 v[66:67], 0\n" \
  "v_mov_b64 v[68:69], 0\n" \
  "v_mov_b64 v[70:71], 0\n" \
  "LI7a_%=:\n" \
  "buffer_load_dword v23, v22, s[44:47], 0 offen sc1\n" \
  "s_waitcnt vmcnt(0)\n" \
  "v_cmp_gt_u32 vcc, 2, v23\n" \
  "s_cbranch_vccz LI7b_%=\n" \
  "s_sleep 4\n" \
  "s_add_u32 s58, s58, 1\n" \
  "s_cmp_lt_u32 s58, " STR(SPIN_CAP) "\n" \
  "s_cbranch_scc1 LI7a_%=\n" \
  "v_mov_b32 v27, " STR(CW_DEAD) "*4\n" \
  "v_mov_b32 v28, 1\n" \
  "buffer_store_dword v28, v27, s[44:47], 0 offen sc1\n" \
  "s_branch LI7b_%=\n" \
  "LI7b_%=:\n" \
  "buffer_load_dwordx4 v[200:203], v247, s[40:43], s55 offen offset:0 sc1\n" \
  "buffer_load_dwordx4 v[204:207], v247, s[40:43], s55 offen offset:1024 sc1\n" \
  "buffer_load_dwordx4 v[208:211], v247, s[40:43], s55 offen offset:2048 sc1\n" \
  "buffer_load_dwordx4 v[212:215], v247, s[40:43], s55 offen offset:3072 sc1\n" \
  "s_waitcnt vmcnt(0)\n" \
  "ds_write_b128 v18, v[200:203] offset:0\n" \
  "ds_write_b128 v18, v[204:207] offset:1024\n" \
  "ds_write_b128 v18, v[208:211] offset:2048\n" \
  "ds_write_b128 v18, v[212:215] offset:3072\n" \
  "v_xor_b32 v18, 16384, v18\n" \
  "s_waitcnt lgkmcnt(0)\n" \
  "s_branch LL_%=\n" \
  ".p2align 6\n" \
  "s_nop 1\n" \
  "s_nop 1\n" \
  "s_nop 1\n" \
  "s_nop 1\n" \
  "s_nop 1\n" \
  "s_nop 1\n" \
  "s_nop 1\n" \
  "s_nop 1\n" \
  "s_nop 1\n" \
  "s_nop 1\n" \
  "s_nop 1\n" \
  "s_nop 1\n" \
  "s_nop 1\n" \
  "s_nop 1\n" \
  "s_nop 1\n" \
  "s_nop 1\n" \
  "s_nop 1\n" \
  "s_nop 1\n" \
  "s_nop 1\n" \
  "s_nop 1\n" \
  "s_nop 1\n" \
  "s_nop 1\n" \
  "s_nop 1\n" \
  "s_nop 1\n" \
  "s_nop 1\n" \
  "s_nop 1\n" \
  "s_nop 1\n" \
  "s_nop 1\n" \
  "s_nop 1\n" \
  "s_nop 1\n" \
  "s_nop 1\n" \
  "s_nop 1\n" \
  "s_nop 1\n" \
  "s_nop 1\n" \
  "s_nop 1\n" \
  "s_nop 1\n" \
  "s_nop 1\n" \
  "s_nop 1\n" \
  "s_nop 1\n" \
  "s_nop 1\n" \
  "s_nop 1\n" \
  "s_nop 1\n" \
  "s_nop 1\n" \
  "s_nop 1\n" \
  "s_nop 1\n" \
  "s_nop 1\n" \
  "s_nop 1\n" \
  "s_nop 1\n" \
  "s_nop 1\n" \
  "s_nop 1\n" \
  "s_nop 1\n" \
  "s_nop 1\n" \
  "s_nop 1\n" \
  "s_nop 1\n" \
  "s_nop 1\n" \
  "s_nop 1\n" \
  "s_nop 1\n" \
  "s_nop 1\n" \
  "s_nop 1\n" \
  "s_nop 1\n" \
  "s_nop 1\n" \
  "s_nop 1\n" \
  "s_nop 1\n" \
  "s_nop 1\n" \
  "s_nop 1\n" \
  "s_nop 1\n" \
  "s_nop 1\n" \
  "s_nop 1\n" \
  "s_nop 1\n" \
  "s_nop 1\n" \
  "s_nop 1\n" \
  "s_nop 1\n" \
  "s_nop 1\n" \
  "s_nop 1\n" \
  "s_nop 1\n" \
  "s_nop 1\n" \
  "s_nop 1\n" \
  "s_nop 1\n" \
  "s_nop 1\n" \
  "s_nop 1\n" \
  "s_nop 1\n" \
  "s_nop 1\n" \
  "s_nop 1\n" \
  "s_nop 1\n" \
  "s_nop 1\n" \
  "s_nop 1\n" \
  "s_nop 1\n" \
  "s_nop 1\n" \
  "s_nop 1\n" \
  "s_nop 1\n" \
  "s_nop 1\n" \
  "s_nop 1\n" \
  "s_nop 1\n" \
  "s_nop 1\n" \
  "s_nop 1\n" \
  "s_nop 1\n" \
  "LB_a_%=:\n" \
  "LL_%=:\n" \
  "s_barrier\n" \
  "buffer_load_dwordx4 v[32:35], v80, s[36:39], s49 offen sc1\n" \
  "v_mfma_f32_16x16x32_bf16 v[184:187], v[136:139], a[224:227], 0\n" \
  "s_xor_b32 s51, s51, 1052672\n" \
  "s_add_u32 s53, s53, 1<<17\n" \
  "v_mfma_f32_16x16x32_bf16 v[188:191], v[152:155], a[224:227], 0\n" \
  "s_and_b32 s53, s53, 0x60000\n" \
  "s_lshr_b32 s59, s48, 2\n" \
  "v_mfma_f32_16x16x32_bf16 v[192:195], v[136:139], a[240:243], 0\n" \
  "s_add_u32 s59, s59, " STR(EXP_B) "\n" \
  "v_readlane_b32 s94, v24, s59\n" \
  "buffer_load_dwordx4 v[36:39], v81, s[36:39], s49 offen sc1\n" \
  "v_mfma_f32_16x16x32_bf16 v[196:199], v[152:155], a[240:243], 0\n" \
  "s_lshl_b32 s50, s48, 19\n" \
  "s_add_u32 s50, s50, " STR(EXP_B) "<<21\n" \
  "v_mfma_f32_16x16x32_bf16 v[184:187], v[140:143], a[228:231], v[184:187]\n" \
  "s_xor_b32 s50, s50, s93\n" \
  "s_xor_b32 s50, s50, s94\n" \
  "v_mfma_f32_16x16x32_bf16 v[188:191], v[156:159], a[228:231], v[188:191]\n" \
  "s_and_b32 s59, s48, 3\n" \
  "s_mul_i32 s59, s59, 0x4800\n" \
  "buffer_load_dwordx4 v[40:43], v82, s[36:39], s49 offen sc1\n" \
  "v_mfma_f32_16x16x32_bf16 v[192:195], v[140:143], a[244:247], v[192:195]\n" \
  "s_and_b32 s59, s59, 0x5000\n" \
  "s_xor_b32 s50, s50, s59\n" \
  "v_mfma_f32_16x16x32_bf16 v[196:199], v[156:159], a[244:247], v[196:199]\n" \
  "s_add_u32 s60, s48, 4\n" \
  "s_lshr_b32 s59, s60, 2\n" \
  "v_mfma_f32_16x16x32_bf16 v[184:187], v[144:147], a[232:235], v[184:187]\n" \
  "s_add_u32 s59, s59, " STR(EXP_B) "\n" \
  "v_readlane_b32 s94, v24, s59\n" \
  "buffer_load_dwordx4 v[44:47], v83, s[36:39], s49 offen sc1\n" \
  "v_mfma_f32_16x16x32_bf16 v[188:191], v[160:163], a[232:235], v[188:191]\n" \
  "s_lshl_b32 s54, s60, 19\n" \
  "s_add_u32 s54, s54, " STR(EXP_B) "<<21\n" \
  "v_mfma_f32_16x16x32_bf16 v[192:195], v[144:147], a[248:251], v[192:195]\n" \
  "s_xor_b32 s54, s54, s93\n" \
  "s_xor_b32 s54, s54, s94\n" \
  "ds_read_b32 v236, v255 offset:14336\n" \
  "v_mfma_f32_16x16x32_bf16 v[196:199], v[160:163], a[248:251], v[196:199]\n" \
  "s_and_b32 s59, s60, 3\n" \
  "s_mul_i32 s59, s59, 0x4800\n" \
  "v_mfma_f32_16x16x32_bf16 v[184:187], v[148:151], a[236:239], v[184:187]\n" \
  "s_and_b32 s59, s59, 0x5000\n" \
  "s_xor_b32 s54, s54, s59\n" \
  "v_mfma_f32_16x16x32_bf16 v[188:191], v[164:167], a[236:239], v[188:191]\n" \
  "s_xor_b32 s59, %7, 3\n" \
  "s_lshl_b32 s59, s59, 29\n" \
  "v_mfma_f32_16x16x32_bf16 v[192:195], v[148:151], a[252:255], v[192:195]\n" \
  "s_xor_b32 s60, s53, 0x40000\n" \
  "s_or_b32 s59, s59, s60\n" \
  "v_mfma_f32_16x16x32_bf16 v[196:199], v[164:167], a[252:255], v[196:199]\n" \
  "v_add_u32 v26, s59, v12\n" \
  "s_sub_u32 s59, " STR(F_P) ", s56\n" \
  "s_waitcnt lgkmcnt(0)\n" \
  "v_add_u32 v236, s59, v236\n" \
  "v_cvt_f32_i32 v236, v236\n" \
  "v_mov_b32 v237, s63\n" \
  "v_mul_f32 v236, v236, v95\n" \
  "v_cvt_i32_f32 v236, v236\n" \
  "v_add_u32 v236, s62, v236\n" \
  "v_min_u32 v236, -2, v236\n" \
  "v_cvt_pk_bf16_f32 v176, v184, v185\n" \
  "v_cvt_pk_bf16_f32 v177, v186, v187\n" \
  "v_cvt_pk_bf16_f32 v178, v188, v189\n" \
  "v_cvt_pk_bf16_f32 v179, v190, v191\n" \
  "v_cvt_pk_bf16_f32 v180, v192, v193\n" \
  "v_cvt_pk_bf16_f32 v181, v194, v195\n" \
  "v_cvt_pk_bf16_f32 v182, v196, v197\n" \
  "v_cvt_pk_bf16_f32 v183, v198, v199\n" \
  "LS1_%=:\n" \
  "s_waitcnt vmcnt(3)\n" \
  "v_smfmac_f32_16x16x64_bf16 v[52:55], v[32:35], a[8:15], v2\n" \
  "buffer_store_dwordx2 v[236:237], v14, s[36:39], s50 offen sc1\n" \
  "v_smfmac_f32_16x16x64_bf16 v[56:59], v[32:35], a[16:23], v2\n" \
  "v_smfmac_f32_16x16x64_bf16 v[60:63], v[32:35], a[24:31], v2\n" \
  "buffer_store_dwordx4 v[88:91], v11, s[36:39], s52 offen sc1\n" \
  "v_smfmac_f32_16x16x64_bf16 v[48:51], v[32:35], a[0:7], v2\n" \
  "s_waitcnt vmcnt(4)\n" \
  "v_smfmac_f32_16x16x64_bf16 v[52:55], v[36:39], a[40:47], v2\n" \
  "buffer_store_dwordx4 v[88:91], v15, s[36:39], s54 offen sc1\n" \
  "v_smfmac_f32_16x16x64_bf16 v[56:59], v[36:39], a[48:55], v2\n" \
  "v_smfmac_f32_16x16x64_bf16 v[60:63], v[36:39], a[56:63], v2\n" \
  "buffer_load_dword v23, v22, s[44:47], 0 offen sc1\n" \
  "v_smfmac_f32_16x16x64_bf16 v[48:51], v[36:39], a[32:39], v2\n" \
  "s_waitcnt vmcnt(5)\n" \
  "v_smfmac_f32_16x16x64_bf16 v[52:55], v[40:43], a[72:79], v2\n" \
  "v_smfmac_f32_16x16x64_bf16 v[56:59], v[40:43], a[80:87], v2\n" \
  "buffer_store_dwordx4 v[88:91], v26, s[36:39], 0 offen sc1\n" \
  "v_smfmac_f32_16x16x64_bf16 v[60:63], v[40:43], a[88:95], v2\n" \
  "v_smfmac_f32_16x16x64_bf16 v[48:51], v[40:43], a[64:71], v2\n" \
  "s_waitcnt vmcnt(5)\n" \
  "v_smfmac_f32_16x16x64_bf16 v[52:55], v[44:47], a[104:111], v2\n" \
  "v_smfmac_f32_16x16x64_bf16 v[56:59], v[44:47], a[112:119], v2\n" \
  "v_smfmac_f32_16x16x64_bf16 v[60:63], v[44:47], a[120:127], v2\n" \
  "v_add_f32 v52, v52, v54\n" \
  "v_add_f32 v53, v53, v55\n" \
  "v_smfmac_f32_16x16x64_bf16 v[48:51], v[44:47], a[96:103], v2\n" \
  "v_add_f32 v56, v56, v58\n" \
  "v_add_f32 v57, v57, v59\n" \
  "ds_write_b64 v3, v[52:53] offset:0\n" \
  "v_add_f32 v60, v60, v62\n" \
  "v_add_f32 v61, v61, v63\n" \
  "v_smfmac_f32_16x16x64_bf16 v[64:67], v[32:35], a[128:135], v2\n" \
  "ds_write_b64 v244, v[56:57] offset:512\n" \
  "v_smfmac_f32_16x16x64_bf16 v[68:71], v[32:35], a[136:143], v2\n" \
  "ds_write_b64 v245, v[60:61] offset:1024\n" \
  "v_cmp_u_f32 vcc, v52, v53\n" \
  "v_smfmac_f32_16x16x64_bf16 v[64:67], v[36:39], a[144:151], v2\n" \
  "v_add_f32 v48, v48, v50\n" \
  "v_add_f32 v49, v49, v51\n" \
  "v_smfmac_f32_16x16x64_bf16 v[68:71], v[36:39], a[152:159], v2\n" \
  "v_smfmac_f32_16x16x64_bf16 v[64:67], v[40:43], a[160:167], v2\n" \
  "s_cbranch_vccnz LA_%=\n" \
  "LA4_%=:\n" \
  "s_waitcnt lgkmcnt(0)\n" \
  "s_barrier\n" \
  "ds_read_b64 v[72:73], v6 offset:0\n" \
  "ds_read_b64 v[74:75], v6 offset:512\n" \
  "ds_read_b64 v[76:77], v6 offset:1024\n" \
  "ds_read_b96 v[92:94], v255 offset:14336\n" \
  "v_smfmac_f32_16x16x64_bf16 v[68:71], v[40:43], a[168:175], v2\n" \
  "v_smfmac_f32_16x16x64_bf16 v[64:67], v[44:47], a[176:183], v2\n" \
  "s_waitcnt lgkmcnt(3)\n" \
  "v_add_f32 v48, v48, v72\n" \
  "v_add_f32 v49, v49, v73\n" \
  "s_waitcnt lgkmcnt(1)\n" \
  "v_add_f32 v74, v74, v76\n" \
  "v_add_f32 v75, v75, v77\n" \
  "v_add_f32 v48, v48, v74\n" \
  "v_add_f32 v49, v49, v75\n" \
  "v_cvt_pk_bf16_f32 v27, v48, v49\n" \
  "v_smfmac_f32_16x16x64_bf16 v[68:71], v[44:47], a[184:191], v2\n" \
  "v_mov_b32_dpp v28, v27 quad_perm:[1,0,3,2] row_mask:0xf bank_mask:0xf\n" \
  "v_perm_b32 v27, v28, v27, v246\n" \
  "buffer_store_dword v27, v8, s[36:39], s50 offen sc1\n" \
  "v_add_f32 v64, v64, v66\n" \
  "v_add_f32 v65, v65, v67\n" \
  "s_nop 2\n" \
  "v_add_f32 v68, v68, v70\n" \
  "v_add_f32 v69, v69, v71\n" \
  "ds_write2_b32 v5, v64, v65 offset0:0 offset1:40\n" \
  "ds_write2_b32 v5, v68, v69 offset0:16 offset1:56\n" \
  "v_mov_b64 v[184:185], 0\n" \
  "s_andn2_b32 s49, s49, 0x60000\n" \
  "s_or_b32 s49, s49, s53\n" \
  "s_xor_b32 s52, s52, 1052672\n" \
  "v_mov_b64 v[186:187], 0\n" \
  "s_getpc_b64 s[82:83]\n" \
  "LG_b_%=:\n" \
  "s_add_u32 s82, s82, LB_b_%=-LG_b_%=\n" \
  "s_addc_u32 s83, s83, 0\n" \
  "v_mov_b64 v[188:189], 0\n" \
  "s_waitcnt lgkmcnt(0)\n" \
  "s_barrier\n" \
  "s_memtime s[66:67]\n" \
  "ds_read_b64 v[72:73], v7 offset:0\n" \
  "ds_read_b64 v[74:75], v7 offset:1280\n" \
  "ds_read_b64 v[76:77], v7 offset:2560\n" \
  "ds_read_b64 v[78:79], v7 offset:3840\n" \
  "v_readfirstlane_b32 s64, v92\n" \
  "v_readfirstlane_b32 s59, v93\n" \
  "v_readfirstlane_b32 s60, v94\n" \
  "v_mov_b64 v[190:191], 0\n" \
  "s_add_u32 s64, s64, s59\n" \
  "s_add_u32 s61, s64, " STR(F_B) "\n" \
  "s_cmp_ge_u32 s48, 63\n" \
  "s_cbranch_scc1 LD_%=\n" \
  "s_waitcnt lgkmcnt(0)\n" \
  "s_sub_i32 s61, s61, s66\n" \
  "v_pk_add_f32 v[72:73], v[72:73], v[74:75]\n" \
  "v_pk_add_f32 v[76:77], v[76:77], v[78:79]\n" \
  "v_pk_add_f32 v[72:73], v[72:73], v[76:77]\n" \
  "v_cvt_pk_bf16_f32 v27, v72, v73\n" \
  "buffer_store_dword v27, v9, s[36:39], s50 offen sc1\n" \
  "s_cmp_gt_i32 s61, 768\n" \
  "s_cbranch_scc1 LW_b_%=\n" \
  "LV_b_%=:\n" \
  "s_max_i32 s61, s61, 0\n" \
  "s_lshr_b32 s61, s61, 3\n" \
  "s_lshl_b32 s61, s61, 2\n" \
  "s_sub_u32 s82, s82, s61\n" \
  "s_subb_u32 s83, s83, 0\n" \
  "s_setpc_b64 s[82:83]\n" \
  "s_nop 1\n" \
  "s_nop 1\n" \
  "s_nop 1\n" \
  "s_nop 1\n" \
  "s_nop 1\n" \
  "s_nop 1\n" \
  "s_nop 1\n" \
  "s_nop 1\n" \
  "s_nop 1\n" \
  "s_nop 1\n" \
  "s_nop 1\n" \
  "s_nop 1\n" \
  "s_nop 1\n" \
  "s_nop 1\n" \
  "s_nop 1\n" \
  "s_nop 1\n" \
  "s_nop 1\n" \
  "s_nop 1\n" \
  "s_nop 1\n" \
  "s_nop 1\n" \
  "s_nop 1\n" \
  "s_nop 1\n" \
  "s_nop 1\n" \
  "s_nop 1\n" \
  "s_nop 1\n" \
  "s_nop 1\n" \
  "s_nop 1\n" \
  "s_nop 1\n" \
  "s_nop 1\n" \
  "s_nop 1\n" \
  "s_nop 1\n" \
  "s_nop 1\n" \
  "s_nop 1\n" \
  "s_nop 1\n" \
  "s_nop 1\n" \
  "s_nop 1\n" \
  "s_nop 1\n" \
  "s_nop 1\n" \
  "s_nop 1\n" \
  "s_nop 1\n" \
  "s_nop 1\n" \
  "s_nop 1\n" \
  "s_nop 1\n" \
  "s_nop 1\n" \
  "s_nop 1\n" \
  "s_nop 1\n" \
  "s_nop 1\n" \
  "s_nop 1\n" \
  "s_nop 1\n" \
  "s_nop 1\n" \
  "s_nop 1\n" \
  "s_nop 1\n" \
  "s_nop 1\n" \
  "s_nop 1\n" \
  "s_nop 1\n" \
  "s_nop 1\n" \
  "s_nop 1\n" \
  "s_nop 1\n" \
  "s_nop 1\n" \
  "s_nop 1\n" \
  "s_nop 1\n" \
  "s_nop 1\n" \
  "s_nop 1\n" \
  "s_nop 1\n" \
  "s_nop 1\n" \
  "s_nop 1\n" \
  "s_nop 1\n" \
  "s_nop 1\n" \
  "s_nop 1\n" \
  "s_nop 1\n" \
  "s_nop 1\n" \
  "s_nop 1\n" \
  "s_nop 1\n" \
  "s_nop 1\n" \
  "s_nop 1\n" \
  "s_nop 1\n" \
  "s_nop 1\n" \
  "s_nop 1\n" \
  "s_nop 1\n" \
  "s_nop 1\n" \
  "s_nop 1\n" \
  "s_nop 1\n" \
  "s_nop 1\n" \
  "s_nop 1\n" \
  "s_nop 1\n" \
  "s_nop 1\n" \
  "s_nop 1\n" \
  "s_nop 1\n" \
  "s_nop 1\n" \
  "s_nop 1\n" \
  "s_nop 1\n" \
  "s_nop 1\n" \
  "s_nop 1\n" \
  "s_nop 1\n" \
  "s_nop 1\n" \
  "s_nop 1\n" \
  "LB_b_%=:\n" \
  "buffer_load_dwordx4 v[32:35], v80, s[36:39], s50 offen sc1\n" \
  "v_mov_b64 v[192:193], 0\n" \
  "s_nop " STR(GAP_0) "\n" \
  "s_lshr_b32 s95, s60, 2\n" \
  "s_sub_u32 s96, s60, s95\n" \
  "s_add_u32 s95, s95, " STR(T_Q) "\n" \
  "s_add_u32 s96, s96, " STR(F_C) "\n" \
  "s_nop 1\n" \
  "v_mov_b64 v[194:195], 0\n" \
  "v_mov_b64 v[196:197], 0\n" \
  "v_mov_b64 v[198:199], 0\n" \
  "buffer_load_dwordx4 v[36:39], v81, s[36:39], s50 offen sc1\n" \
  "ds_read_b64_tr_b16 v[136:137], v20 offset:0\n" \
  "ds_read_b64_tr_b16 v[138:139], v21 offset:0\n" \
  "ds_read_b64_tr_b16 v[152:153], v20 offset:8\n" \
  "ds_read_b64_tr_b16 v[154:155], v21 offset:8\n" \
  "s_nop " STR(GAP_B) "\n" \
  "buffer_load_dwordx4 v[40:43], v82, s[36:39], s50 offen sc1\n" \
  "ds_read_b64_tr_b16 v[140:141], v20 offset:2048\n" \
  "ds_read_b64_tr_b16 v[142:143], v21 offset:2048\n" \
  "ds_read_b64_tr_b16 v[156:157], v20 offset:2056\n" \
  "ds_read_b64_tr_b16 v[158:159], v21 offset:2056\n" \
  "s_nop " STR(GAP_B) "\n" \
  "buffer_load_dwordx4 v[44:47], v83, s[36:39], s50 offen sc1\n" \
  "ds_read_b64_tr_b16 v[144:145], v20 offset:4096\n" \
  "ds_read_b64_tr_b16 v[146:147], v21 offset:4096\n" \
  "ds_read_b64_tr_b16 v[160:161], v20 offset:4104\n" \
  "ds_read_b64_tr_b16 v[162:163], v21 offset:4104\n" \
  "s_nop " STR(GAP_V) "\n" \
  "buffer_load_dwordx4 v[96:99], v17, s[36:39], s50 offen offset:0 sc1\n" \
  "ds_read_b64_tr_b16 v[148:149], v20 offset:6144\n" \
  "ds_read_b64_tr_b16 v[150:151], v21 offset:6144\n" \
  "ds_read_b64_tr_b16 v[164:165], v20 offset:6152\n" \
  "ds_read_b64_tr_b16 v[166:167], v21 offset:6152\n" \
  "s_nop " STR(GAP_B) "\n" \
  "buffer_load_dwordx4 v[100:103], v17, s[36:39], s50 offen offset:1024 sc1\n" \
  "s_andn2_b32 s59, s50, 0x70000\n" \
  "v_xor_b32 v28, s59, v13\n" \
  "buffer_load_dwordx2 v[92:93], v28, s[36:39], 0 offen sc1\n" \
  "s_add_u32 s60, s48, 2\n" \
  "s_min_u32 s60, s60, 63\n" \
  "s_waitcnt vmcnt(9)\n" \
  "v_cmp_ge_u32 vcc, s60, v23\n" \
  "s_lshr_b32 s59, s60, 2\n" \
  "s_xnor_b32 s59, s59, %8\n" \
  "s_and_b32 s59, s59, 1\n" \
  "s_lshl_b32 s59, s59, 13\n" \
  "s_lshl_b32 s55, s60, 19\n" \
  "s_or_b32 s55, s55, s59\n" \
  "s_cmp_lg_u64 vcc, 0\n" \
  "s_cselect_b32 s54, 0x80000000, 0\n" \
  "v_or_b32 v16, s54, v247\n" \
  "s_mov_b32 vcc_lo, 0x33333333\n" \
  "s_mov_b32 vcc_hi, 0x33333333\n" \
  "LS2_%=:\n" \
  "s_waitcnt vmcnt(6)\n" \
  "v_mfma_f32_16x16x32_bf16 v[228:231], v[120:123], v[32:35], 0\n" \
  "v_mfma_f32_16x16x32_bf16 v[224:227], v[104:107], v[32:35], 0\n" \
  "v_mfma_f32_16x16x32_bf16 v[232:235], v[32:35], v[32:35], 0\n" \
  "buffer_load_dwordx4 v[200:203], v16, s[40:43], s55 offen offset:0 sc1\n" \
  "s_waitcnt vmcnt(6)\n" \
  "v_mfma_f32_16x16x32_bf16 v[228:231], v[124:127], v[36:39], v[228:231]\n" \
  "v_mfma_f32_16x16x32_bf16 v[224:227], v[108:111], v[36:39], v[224:227]\n" \
  "v_mfma_f32_16x16x32_bf16 v[232:235], v[36:39], v[36:39], v[232:235]\n" \
  "buffer_load_dwordx4 v[204:207], v16, s[40:43], s55 offen offset:1024 sc1\n" \
  "s_waitcnt vmcnt(6)\n" \
  "v_mfma_f32_16x16x32_bf16 v[228:231], v[128:131], v[40:43], v[228:231]\n" \
  "v_mfma_f32_16x16x32_bf16 v[224:227], v[112:115], v[40:43], v[224:227]\n" \
  "v_mfma_f32_16x16x32_bf16 v[232:235], v[40:43], v[40:43], v[232:235]\n" \
  "buffer_load_dwordx4 v[208:211], v16, s[40:43], s55 offen offset:2048 sc1\n" \
  "s_waitcnt vmcnt(6)\n" \
  "v_mfma_f32_16x16x32_bf16 v[228:231], v[132:135], v[44:47], v[228:231]\n" \
  "v_mfma_f32_16x16x32_bf16 v[224:227], v[116:119], v[44:47], v[224:227]\n" \
  "v_mfma_f32_16x16x32_bf16 v[232:235], v[44:47], v[44:47], v[232:235]\n" \
  "buffer_load_dwordx4 v[212:215], v16, s[40:43], s55 offen offset:3072 sc1\n" \
  "s_nop 2\n" \
  "v_cndmask_b32_dpp v224, v228, v224, vcc quad_perm:[2,3,0,1] row_mask:0xf bank_mask:0xf\n" \
  "v_cndmask_b32_dpp v225, v229, v225, vcc quad_perm:[2,3,0,1] row_mask:0xf bank_mask:0xf\n" \
  "v_cndmask_b32_dpp v226, v230, v226, vcc quad_perm:[2,3,0,1] row_mask:0xf bank_mask:0xf\n" \
  "v_cndmask_b32_dpp v227, v231, v227, vcc quad_perm:[2,3,0,1] row_mask:0xf bank_mask:0xf\n" \
  "v_cndmask_b32_dpp v234, v235, v234, vcc quad_perm:[2,3,0,1] row_mask:0xf bank_mask:0xf\n" \
  "v_cmp_u_f32 vcc, v228, v228\n" \
  "v_max3_f32 v25, v224, v225, v226\n" \
  "v_max3_f32 v25, v25, v227, v234\n" \
  "s_waitcnt vmcnt(6)\n" \
  "v_smfmac_f32_16x16x64_bf16 v[184:187], v[96:99], a[192:199], v2\n" \
  "v_max_f32_dpp v26, v25, v25 quad_perm:[2,3,0,1] row_mask:0xf bank_mask:0xf\n" \
  "v_max_f32_dpp v25, v25, v25 quad_perm:[2,3,0,1] row_mask:0xf bank_mask:0xf\n" \
  "v_smfmac_f32_16x16x64_bf16 v[188:191], v[96:99], a[208:215], v2\n" \
  "v_permlane16_swap_b32 v25, v26\n" \
  "v_max_f32 v27, v25, v26\n" \
  "v_max_f32 v25, v25, v26\n" \
  "v_smfmac_f32_16x16x64_bf16 v[192:195], v[96:99], a[224:231], v2\n" \
  "s_cbranch_vccnz LBE_%=\n" \
  "LBH_%=:\n" \
  "v_permlane32_swap_b32 v25, v27\n" \
  "v_max_f32 v25, v25, v27\n" \
  "v_mul_f32 v25, s92, v25\n" \
  "v_fma_f32 v224, v224, s92, -v25\n" \
  "v_smfmac_f32_16x16x64_bf16 v[196:199], v[96:99], a[240:247], v2\n" \
  "v_fma_f32 v225, v225, s92, -v25\n" \
  "v_exp_f32 v224, v224\n" \
  "v_exp_f32 v225, v225\n" \
  "s_waitcnt vmcnt(5)\n" \
  "v_smfmac_f32_16x16x64_bf16 v[184:187], v[100:103], a[200:207], v2\n" \
  "v_fma_f32 v226, v226, s92, -v25\n" \
  "v_smfmac_f32_16x16x64_bf16 v[188:191], v[100:103], a[216:223], v2\n" \
  "v_fma_f32 v227, v227, s92, -v25\n" \
  "v_smfmac_f32_16x16x64_bf16 v[192:195], v[100:103], a[232:239], v2\n" \
  "v_exp_f32 v226, v226\n" \
  "v_smfmac_f32_16x16x64_bf16 v[196:199], v[100:103], a[248:255], v2\n" \
  "v_exp_f32 v227, v227\n" \
  "v_cvt_pk_bf16_f32 v228, v224, v225\n" \
  "v_cvt_pk_bf16_f32 v229, v226, v227\n" \
  "v_and_b32 v236, v228, v4\n" \
  "v_and_b32 v237, v229, v4\n" \
  "v_and_b32_dpp v238, v228, v4 quad_perm:[2,3,0,1] row_mask:0xf bank_mask:0xf\n" \
  "v_and_b32_dpp v239, v229, v4 quad_perm:[2,3,0,1] row_mask:0xf bank_mask:0xf\n" \
  "v_fma_f32 v234, v234, s92, -v25\n" \
  "v_and_b32_dpp v240, v228, v29 quad_perm:[2,3,0,1] row_mask:0xf bank_mask:0xf\n" \
  "v_mfma_f32_16x16x32_bf16 v[48:51], v[168:171], v[236:239], 0\n" \
  "v_and_b32_dpp v241, v229, v29 quad_perm:[2,3,0,1] row_mask:0xf bank_mask:0xf\n" \
  "v_and_b32 v242, v228, v29\n" \
  "v_mfma_f32_16x16x32_bf16 v[52:55], v[176:179], v[236:239], 0\n" \
  "v_and_b32 v243, v229, v29\n" \
  "v_exp_f32 v234, v234\n" \
  "v_cvt_pk_bf16_f32 v216, v184, v185\n" \
  "v_mfma_f32_16x16x32_bf16 v[48:51], v[172:175], v[240:243], v[48:51]\n" \
  "v_mov_b32_dpp v25, v234 quad_perm:[0,1,0,1] row_mask:0xf bank_mask:0xf\n" \
  "v_mov_b32_dpp v26, v234 quad_perm:[2,3,2,3] row_mask:0xf bank_mask:0xf\n" \
  "v_mfma_f32_16x16x32_bf16 v[52:55], v[180:183], v[240:243], v[52:55]\n" \
  "v_cvt_pk_bf16_f32 v25, v25, v26\n" \
  "v_and_b32 v230, v25, v4\n" \
  "v_and_b32 v231, v25, v4\n" \
  "v_and_b32 v232, v25, v29\n" \
  "v_and_b32 v233, v25, v29\n" \
  "v_mfma_f32_16x16x32_bf16 v[56:59], v[252:255], v[228:231], 0\n" \
  "v_cvt_pk_bf16_f32 v217, v186, v187\n" \
  "v_cvt_pk_bf16_f32 v218, v188, v189\n" \
  "v_cvt_pk_bf16_f32 v219, v190, v191\n" \
  "v_cvt_pk_bf16_f32 v220, v192, v193\n" \
  "v_cvt_pk_bf16_f32 v221, v194, v195\n" \
  "v_mfma_f32_16x16x32_bf16 v[48:51], v[216:219], v[230:233], v[48:51]\n" \
  "v_cvt_pk_bf16_f32 v222, v196, v197\n" \
  "v_cvt_pk_bf16_f32 v223, v198, v199\n" \
  "v_add_f32_dpp v56, v56, v56 quad_perm:[2,3,0,1] row_mask:0xf bank_mask:0xf\n" \
  "v_rcp_f32 v56, v56\n" \
  "v_mfma_f32_16x16x32_bf16 v[52:55], v[220:223], v[230:233], v[52:55]\n" \
  "s_nop 2\n" \
  "v_cmp_u_f32 vcc, v48, v48\n" \
  "v_mul_f32 v48, v48, v56\n" \
  "v_mul_f32 v49, v49, v56\n" \
  "v_mul_f32 v50, v50, v56\n" \
  "v_mul_f32 v51, v51, v56\n" \
  "v_pk_mul_f32 v[52:53], v[52:53], v[56:57] op_sel_hi:[1,0]\n" \
  "v_pk_mul_f32 v[54:55], v[54:55], v[56:57] op_sel_hi:[1,0]\n" \
  "v_cvt_pk_bf16_f32 v72, v48, v49\n" \
  "v_cvt_pk_bf16_f32 v73, v50, v51\n" \
  "v_cvt_pk_bf16_f32 v74, v52, v53\n" \
  "v_cvt_pk_bf16_f32 v75, v54, v55\n" \
  "s_cbranch_vccnz LBF_%=\n" \
  "LBG_%=:\n" \
  "buffer_store_dwordx4 v[72:75], v10, s[36:39], s51 offen sc1\n" \
  "s_memtime s[66:67]\n" \
  "s_add_u32 s61, s64, s96\n" \
  "s_cmp_lg_u32 %7, 0\n" \
  "s_cbranch_scc1 LO_%=\n" \
  "s_getpc_b64 s[82:83]\n" \
  "LG_c_%=:\n" \
  "s_add_u32 s82, s82, LB_c_%=-LG_c_%=\n" \
  "s_addc_u32 s83, s83, 0\n" \
  "s_waitcnt lgkmcnt(0)\n" \
  "s_sub_i32 s61, s61, s66\n" \
  "s_sub_i32 s61, s61, 200\n" \
  "v_mfma_f32_16x16x32_bf16 v[184:187], v[136:139], a[192:195], 0\n" \
  "v_mov_b64 v[48:49], 0\n" \
  "ds_read_b128 v[104:107], v84 offset:0\n" \
  "v_mfma_f32_16x16x32_bf16 v[188:191], v[152:155], a[192:195], 0\n" \
  "v_mov_b64 v[50:51], 0\n" \
  "ds_read_b128 v[108:111], v85 offset:0\n" \
  "v_mfma_f32_16x16x32_bf16 v[192:195], v[136:139], a[208:211], 0\n" \
  "v_mov_b64 v[52:53], 0\n" \
  "ds_read_b128 v[112:115], v86 offset:0\n" \
  "v_mfma_f32_16x16x32_bf16 v[196:199], v[152:155], a[208:211], 0\n" \
  "v_mov_b64 v[54:55], 0\n" \
  "ds_read_b128 v[116:119], v87 offset:0\n" \
  "v_mfma_f32_16x16x32_bf16 v[184:187], v[140:143], a[196:199], v[184:187]\n" \
  "v_mov_b64 v[56:57], 0\n" \
  "ds_read_b128 v[120:123], v84 offset:4096\n" \
  "v_mfma_f32_16x16x32_bf16 v[188:191], v[156:159], a[196:199], v[188:191]\n" \
  "v_mov_b64 v[58:59], 0\n" \
  "ds_read_b128 v[124:127], v85 offset:4096\n" \
  "v_mfma_f32_16x16x32_bf16 v[192:195], v[140:143], a[212:215], v[192:195]\n" \
  "v_mov_b64 v[60:61], 0\n" \
  "ds_read_b128 v[128:131], v86 offset:4096\n" \
  "v_mfma_f32_16x16x32_bf16 v[196:199], v[156:159], a[212:215], v[196:199]\n" \
  "v_mov_b64 v[62:63], 0\n" \
  "ds_read_b128 v[132:135], v87 offset:4096\n" \
  "v_mfma_f32_16x16x32_bf16 v[184:187], v[144:147], a[200:203], v[184:187]\n" \
  "v_mov_b64 v[64:65], 0\n" \
  "v_mfma_f32_16x16x32_bf16 v[188:191], v[160:163], a[200:203], v[188:191]\n" \
  "v_mov_b64 v[66:67], 0\n" \
  "v_mfma_f32_16x16x32_bf16 v[192:195], v[144:147], a[216:219], v[192:195]\n" \
  "v_mov_b64 v[68:69], 0\n" \
  "v_xor_b32 v20, 16384, v20\n" \
  "v_mfma_f32_16x16x32_bf16 v[196:199], v[160:163], a[216:219], v[196:199]\n" \
  "v_mov_b64 v[70:71], 0\n" \
  "v_xor_b32 v21, 16384, v21\n" \
  "s_cmp_gt_i32 s61, 768\n" \
  "s_cbranch_scc1 LW_c_%=\n" \
  "LV_c_%=:\n" \
  "s_max_i32 s61, s61, 0\n" \
  "s_lshr_b32 s61, s61, 3\n" \
  "s_lshl_b32 s61, s61, 2\n" \
  "s_sub_u32 s82, s82, s61\n" \
  "s_subb_u32 s83, s83, 0\n" \
  "s_setpc_b64 s[82:83]\n" \
  "s_nop 1\n" \
  "s_nop 1\n" \
  "s_nop 1\n" \
  "s_nop 1\n" \
  "s_nop 1\n" \
  "s_nop 1\n" \
  "s_nop 1\n" \
  "s_nop 1\n" \
  "s_nop 1\n" \
  "s_nop 1\n" \
  "s_nop 1\n" \
  "s_nop 1\n" \
  "s_nop 1\n" \
  "s_nop 1\n" \
  "s_nop 1\n" \
  "s_nop 1\n" \
  "s_nop 1\n" \
  "s_nop 1\n" \
  "s_nop 1\n" \
  "s_nop 1\n" \
  "s_nop 1\n" \
  "s_nop 1\n" \
  "s_nop 1\n" \
  "s_nop 1\n" \
  "s_nop 1\n" \
  "s_nop 1\n" \
  "s_nop 1\n" \
  "s_nop 1\n" \
  "s_nop 1\n" \
  "s_nop 1\n" \
  "s_nop 1\n" \
  "s_nop 1\n" \
  "s_nop 1\n" \
  "s_nop 1\n" \
  "s_nop 1\n" \
  "s_nop 1\n" \
  "s_nop 1\n" \
  "s_nop 1\n" \
  "s_nop 1\n" \
  "s_nop 1\n" \
  "s_nop 1\n" \
  "s_nop 1\n" \
  "s_nop 1\n" \
  "s_nop 1\n" \
  "s_nop 1\n" \
  "s_nop 1\n" \
  "s_nop 1\n" \
  "s_nop 1\n" \
  "s_nop 1\n" \
  "s_nop 1\n" \
  "s_nop 1\n" \
  "s_nop 1\n" \
  "s_nop 1\n" \
  "s_nop 1\n" \
  "s_nop 1\n" \
  "s_nop 1\n" \
  "s_nop 1\n" \
  "s_nop 1\n" \
  "s_nop 1\n" \
  "s_nop 1\n" \
  "s_nop 1\n" \
  "s_nop 1\n" \
  "s_nop 1\n" \
  "s_nop 1\n" \
  "s_nop 1\n" \
  "s_nop 1\n" \
  "s_nop 1\n" \
  "s_nop 1\n" \
  "s_nop 1\n" \
  "s_nop 1\n" \
  "s_nop 1\n" \
  "s_nop 1\n" \
  "s_nop 1\n" \
  "s_nop 1\n" \
  "s_nop 1\n" \
  "s_nop 1\n" \
  "s_nop 1\n" \
  "s_nop 1\n" \
  "s_nop 1\n" \
  "s_nop 1\n" \
  "s_nop 1\n" \
  "s_nop 1\n" \
  "s_nop 1\n" \
  "s_nop 1\n" \
  "s_nop 1\n" \
  "s_nop 1\n" \
  "s_nop 1\n" \
  "s_nop 1\n" \
  "s_nop 1\n" \
  "s_nop 1\n" \
  "s_nop 1\n" \
  "s_nop 1\n" \
  "s_nop 1\n" \
  "s_nop 1\n" \
  "s_nop 1\n" \
  "s_nop 1\n" \
  "LB_c_%=:\n" \
  "buffer_load_dwordx4 v[32:35], v11, s[36:39], s52 offen offset:0 sc1\n" \
  "buffer_load_dwordx4 v[36:39], v11, s[36:39], s52 offen offset:1024 sc1\n" \
  "buffer_load_dwordx4 v[40:43], v11, s[36:39], s52 offen offset:2048 sc1\n" \
  "buffer_load_dwordx4 v[44:47], v11, s[36:39], s52 offen offset:3072 sc1\n" \
  "v_mfma_f32_16x16x32_bf16 v[184:187], v[148:151], a[204:207], v[184:187]\n" \
  "v_xor_b32 v84, 16384, v84\n" \
  "v_mfma_f32_16x16x32_bf16 v[188:191], v[164:167], a[204:207], v[188:191]\n" \
  "v_xor_b32 v85, 16384, v85\n" \
  "v_mfma_f32_16x16x32_bf16 v[192:195], v[148:151], a[220:223], v[192:195]\n" \
  "v_xor_b32 v86, 16384, v86\n" \
  "v_mfma_f32_16x16x32_bf16 v[196:199], v[164:167], a[220:223], v[196:199]\n" \
  "v_xor_b32 v87, 16384, v87\n" \
  "s_add_u32 s48, s48, 1\n" \
  "s_nop 4\n" \
  "v_cvt_pk_bf16_f32 v168, v184, v185\n" \
  "v_cvt_pk_bf16_f32 v169, v186, v187\n" \
  "v_cvt_pk_bf16_f32 v170, v188, v189\n" \
  "v_cvt_pk_bf16_f32 v171, v190, v191\n" \
  "v_cvt_pk_bf16_f32 v172, v192, v193\n" \
  "v_cvt_pk_bf16_f32 v173, v194, v195\n" \
  "v_cvt_pk_bf16_f32 v174, v196, v197\n" \
  "v_cvt_pk_bf16_f32 v175, v198, v199\n" \
  "s_cmp_lg_u32 s54, 0\n" \
  "s_cbranch_scc1 LS3_%=\n" \
  "s_waitcnt vmcnt(5)\n" \
  "ds_write_b128 v18, v[200:203] offset:0\n" \
  "ds_write_b128 v18, v[204:207] offset:1024\n" \
  "ds_write_b128 v18, v[208:211] offset:2048\n" \
  "ds_write_b128 v18, v[212:215] offset:3072\n" \
  "v_xor_b32 v18, 16384, v18\n" \
  "LS3_%=:\n" \
  "s_nop " STR(D_K) "\n" \
  "ds_read_b64 v[92:93], v255 offset:14352\n" \
  "s_waitcnt lgkmcnt(0)\n" \
  "v_readfirstlane_b32 s62, v93\n" \
  "v_readfirstlane_b32 s63, v92\n" \
  "s_getpc_b64 s[82:83]\n" \
  "LG_a_%=:\n" \
  "s_sub_u32 s82, s82, LG_a_%=-LB_a_%=\n" \
  "s_subb_u32 s83, s83, 0\n" \
  "s_xor_b32 s62, s62, s48\n" \
  "s_or_b32 s62, s62, s54\n" \
  "s_sub_i32 s59, s63, s64\n" \
  "s_cmp_lt_i32 s59, " STR(F_P) "-300\n" \
  "s_cselect_b32 s62, 1, s62\n" \
  "s_movk_i32 s68, -(" STR(T_LATE) ")\n" \
  "s_cmp_eq_u32 s62, 0\n" \
  "s_cselect_b32 s68, s68, 0x7fffffff\n" \
  "s_cselect_b32 s63, s63, s64\n" \
  "v_mov_b32 v92, s63\n" \
  "v_mov_b32 v93, 0\n" \
  "ds_write_b64 v255, v[92:93] offset:14336\n" \
  "s_waitcnt vmcnt(3)\n" \
  "v_mfma_f32_16x16x32_bf16 v[224:227], v[248:251], v[32:35], 0\n" \
  "s_waitcnt vmcnt(2)\n" \
  "v_mfma_f32_16x16x32_bf16 v[224:227], v[248:251], v[36:39], v[224:227]\n" \
  "s_waitcnt vmcnt(1)\n" \
  "v_mfma_f32_16x16x32_bf16 v[224:227], v[248:251], v[40:43], v[224:227]\n" \
  "s_waitcnt vmcnt(0)\n" \
  "v_mfma_f32_16x16x32_bf16 v[224:227], v[248:251], v[44:47], v[224:227]\n" \
  "s_nop 6\n" \
  "s_memtime s[66:67]\n" \
  "v_cmp_u_f32 vcc, v224, v224\n" \
  "v_add_f32_dpp v224, v224, v224 row_ror:8 row_mask:0xf bank_mask:0xf\n" \
  "v_add_f32_dpp v225, v225, v225 row_ror:8 row_mask:0xf bank_mask:0xf\n" \
  "v_add_f32_dpp v226, v226, v226 row_ror:8 row_mask:0xf bank_mask:0xf\n" \
  "v_add_f32_dpp v227, v227, v227 row_ror:8 row_mask:0xf bank_mask:0xf\n" \
  "v_cvt_pk_bf16_f32 v228, v224, v225\n" \
  "v_cvt_pk_bf16_f32 v229, v226, v227\n" \
  "s_cbranch_vccnz LC_%=\n" \
  "LC2_%=:\n" \
  "v_permlane16_swap_b32 v228, v230\n" \
  "v_permlane16_swap_b32 v229, v231\n" \
  "buffer_store_dwordx4 v[228:231], v12, s[36:39], s53 offen sc1\n" \
  "s_waitcnt lgkmcnt(0)\n" \
  "s_sub_i32 s61, s63, s66\n" \
  "s_cmp_lt_i32 s61, s68\n" \
  "s_cbranch_scc1 LN_%=\n" \
  "LN2_%=:\n" \
  "s_cmp_gt_i32 s61, 768\n" \
  "s_cbranch_scc1 LW_a_%=\n" \
  "LV_a_%=:\n" \
  "s_max_i32 s61, s61, 0\n" \
  "s_lshr_b32 s61, s61, 3\n" \
  "s_lshl_b32 s61, s61, 2\n" \
  "s_sub_u32 s82, s82, s61\n" \
  "s_subb_u32 s83, s83, 0\n" \
  "s_setpc_b64 s[82:83]\n" \
  "LN_%=:\n" \
  "s_mov_b32 s68, s66\n" \
  "s_cmp_lg_u32 s54, 0\n" \
  "s_cbranch_scc1 LFa_%=\n" \
  "LFa2_%=:\n" \
  "LK4_%=:\n" \
  "ds_read_b64 v[92:93], v255 offset:14352\n" \
  "s_waitcnt lgkmcnt(0)\n" \
  "v_readfirstlane_b32 s62, v93\n" \
  "v_readfirstlane_b32 s63, v92\n" \
  "s_cmp_lg_u32 s62, s48\n" \
  "s_cbranch_scc1 LK4_%=\n" \
  "s_sub_i32 s59, s63, s64\n" \
  "s_sub_i32 s59, s59, " STR(F_P) "-300\n" \
  "s_sub_i32 s60, s63, s68\n" \
  "s_add_i32 s60, s60, " STR(T_LATE) "\n" \
  "s_min_i32 s59, s59, s60\n" \
  "s_sub_u32 s63, s63, s95\n" \
  "LN3_%=:\n" \
  "s_add_u32 s63, s63, s95\n" \
  "s_add_i32 s59, s59, s95\n" \
  "s_cmp_lt_i32 s59, s95\n" \
  "s_cbranch_scc1 LN3_%=\n" \
  "v_mov_b32 v92, s63\n" \
  "v_mov_b32 v93, 0\n" \
  "ds_write_b64 v255, v[92:93] offset:14336\n" \
  "s_memtime s[66:67]\n" \
  "s_waitcnt lgkmcnt(0)\n" \
  "s_sub_i32 s61, s63, s66\n" \
  "s_add_i32 s61, s61, " STR(O_N) "\n" \
  "s_branch LN2_%=\n" \
  "LO_%=:\n" \
  "s_cmp_eq_u32 %7, 1\n" \
  "s_waitcnt lgkmcnt(0)\n" \
  "s_cbranch_scc1 LK_%=\n" \
  "s_add_u32 s59, s48, 1\n" \
  "v_mov_b32 v28, s59\n" \
  "buffer_store_dword v28, v0, s[44:47], 0 offen sc1\n" \
  "v_mfma_f32_16x16x32_bf16 v[184:187], v[136:139], a[192:195], 0\n" \
  "v_mov_b64 v[48:49], 0\n" \
  "ds_read_b128 v[104:107], v84 offset:0\n" \
  "v_mfma_f32_16x16x32_bf16 v[188:191], v[152:155], a[192:195], 0\n" \
  "v_mov_b64 v[50:51], 0\n" \
  "ds_read_b128 v[108:111], v85 offset:0\n" \
  "v_mfma_f32_16x16x32_bf16 v[192:195], v[136:139], a[208:211], 0\n" \
  "v_mov_b64 v[52:53], 0\n" \
  "ds_read_b128 v[112:115], v86 offset:0\n" \
  "v_mfma_f32_16x16x32_bf16 v[196:199], v[152:155], a[208:211], 0\n" \
  "v_mov_b64 v[54:55], 0\n" \
  "ds_read_b128 v[116:119], v87 offset:0\n" \
  "v_mfma_f32_16x16x32_bf16 v[184:187], v[140:143], a[196:199], v[184:187]\n" \
  "v_mov_b64 v[56:57], 0\n" \
  "ds_read_b128 v[120:123], v84 offset:4096\n" \
  "v_mfma_f32_16x16x32_bf16 v[188:191], v[156:159], a[196:199], v[188:191]\n" \
  "v_mov_b64 v[58:59], 0\n" \
  "ds_read_b128 v[124:127], v85 offset:4096\n" \
  "v_mfma_f32_16x16x32_bf16 v[192:195], v[140:143], a[212:215], v[192:195]\n" \
  "v_mov_b64 v[60:61], 0\n" \
  "ds_read_b128 v[128:131], v86 offset:4096\n" \
  "v_mfma_f32_16x16x32_bf16 v[196:199], v[156:159], a[212:215], v[196:199]\n" \
  "v_mov_b64 v[62:63], 0\n" \
  "ds_read_b128 v[132:135], v87 offset:4096\n" \
  "v_mfma_f32_16x16x32_bf16 v[184:187], v[144:147], a[200:203], v[184:187]\n" \
  "v_mov_b64 v[64:65], 0\n" \
  "v_mfma_f32_16x16x32_bf16 v[188:191], v[160:163], a[200:203], v[188:191]\n" \
  "v_mov_b64 v[66:67], 0\n" \
  "v_mfma_f32_16x16x32_bf16 v[192:195], v[144:147], a[216:219], v[192:195]\n" \
  "v_mov_b64 v[68:69], 0\n" \
  "v_xor_b32 v20, 16384, v20\n" \
  "v_mfma_f32_16x16x32_bf16 v[196:199], v[160:163], a[216:219], v[196:199]\n" \
  "v_mov_b64 v[70:71], 0\n" \
  "v_xor_b32 v21, 16384, v21\n" \
  "v_mfma_f32_16x16x32_bf16 v[184:187], v[148:151], a[204:207], v[184:187]\n" \
  "v_xor_b32 v84, 16384, v84\n" \
  "v_mfma_f32_16x16x32_bf16 v[188:191], v[164:167], a[204:207], v[188:191]\n" \
  "v_xor_b32 v85, 16384, v85\n" \
  "v_mfma_f32_16x16x32_bf16 v[192:195], v[148:151], a[220:223], v[192:195]\n" \
  "v_xor_b32 v86, 16384, v86\n" \
  "v_mfma_f32_16x16x32_bf16 v[196:199], v[164:167], a[220:223], v[196:199]\n" \
  "v_xor_b32 v87, 16384, v87\n" \
  "s_add_u32 s48, s48, 1\n" \
  "s_nop 4\n" \
  "v_cvt_pk_bf16_f32 v168, v184, v185\n" \
  "v_cvt_pk_bf16_f32 v169, v186, v187\n" \
  "v_cvt_pk_bf16_f32 v170, v188, v189\n" \
  "v_cvt_pk_bf16_f32 v171, v190, v191\n" \
  "v_cvt_pk_bf16_f32 v172, v192, v193\n" \
  "v_cvt_pk_bf16_f32 v173, v194, v195\n" \
  "v_cvt_pk_bf16_f32 v174, v196, v197\n" \
  "v_cvt_pk_bf16_f32 v175, v198, v199\n" \
  "s_cmp_lg_u32 s54, 0\n" \
  "s_cbranch_scc1 LFb_%=\n" \
  "s_waitcnt vmcnt(2)\n" \
  "ds_write_b128 v18, v[200:203] offset:0\n" \
  "ds_write_b128 v18, v[204:207] offset:1024\n" \
  "ds_write_b128 v18, v[208:211] offset:2048\n" \
  "ds_write_b128 v18, v[212:215] offset:3072\n" \
  "v_xor_b32 v18, 16384, v18\n" \
  "LFb2_%=:\n" \
  "s_waitcnt lgkmcnt(0)\n" \
  "s_branch LL_%=\n" \
  "LK_%=:\n" \
  "s_mov_b32 s94, s63\n" \
  "s_memtime s[72:73]\n" \
  "s_memrealtime s[74:75]\n" \
  "s_nop 1\n" \
  "s_memrealtime s[76:77]\n" \
  "s_nop 1\n" \
  "s_memrealtime s[78:79]\n" \
  "s_nop 1\n" \
  "s_memrealtime s[80:81]\n" \
  "s_nop 1\n" \
  "s_memrealtime s[82:83]\n" \
  "s_nop 1\n" \
  "s_memrealtime s[84:85]\n" \
  "s_nop 1\n" \
  "s_memrealtime s[86:87]\n" \
  "s_nop 1\n" \
  "s_memrealtime s[88:89]\n" \
  "s_nop 1\n" \
  "s_memrealtime s[90:91]\n" \
  "s_mov_b32 s59, 0\n" \
  "s_waitcnt lgkmcnt(0)\n" \
  "s_cmp_eq_u32 s76, s74\n" \
  "s_addc_u32 s59, s59, 0\n" \
  "s_cmp_eq_u32 s78, s74\n" \
  "s_addc_u32 s59, s59, 0\n" \
  "s_cmp_eq_u32 s80, s74\n" \
  "s_addc_u32 s59, s59, 0\n" \
  "s_cmp_eq_u32 s82, s74\n" \
  "s_addc_u32 s59, s59, 0\n" \
  "s_cmp_eq_u32 s84, s74\n" \
  "s_addc_u32 s59, s59, 0\n" \
  "s_cmp_eq_u32 s86, s74\n" \
  "s_addc_u32 s59, s59, 0\n" \
  "s_cmp_eq_u32 s88, s74\n" \
  "s_addc_u32 s59, s59, 0\n" \
  "s_cmp_eq_u32 s90, s74\n" \
  "s_addc_u32 s59, s59, 0\n" \
  "s_mul_i32 s59, s59, 12\n" \
  "s_add_u32 s68, s72, s59\n" \
  "s_add_u32 s59, s74, 4\n" \
  "s_mul_i32 s69, s59, 24\n" \
  "s_sub_u32 s60, s69, s62\n" \
  "s_sub_u32 s59, s68, s56\n" \
  "s_mov_b32 s62, s69\n" \
  "s_mov_b32 s56, s68\n" \
  "v_cvt_f32_u32 v27, s59\n" \
  "v_cvt_f32_u32 v28, s60\n" \
  "v_rcp_f32 v26, v27\n" \
  "v_rcp_f32 v19, v28\n" \
  "v_mul_f32 v26, v26, v28\n" \
  "v_mul_f32 v95, " STR(DECAY) ", v95\n" \
  "v_mul_f32 v19, v19, v27\n" \
  "v_max_f32 v95, v95, v26\n" \
  "v_cvt_f32_u32 v28, " STR(F_P) "\n" \
  "v_mul_f32 v27, v95, v28\n" \
  "v_cvt_u32_f32 v27, v27\n" \
  "v_min_u32 v27, 0x7fff, v27\n" \
  "s_nop 0\n" \
  "v_readfirstlane_b32 s63, v27\n" \
  "s_waitcnt vmcnt(5)\n" \
  "LK2_%=:\n" \
  "v_max_u32 v26, v92, v93\n" \
  "v_cmp_eq_u32 vcc, -1, v26\n" \
  "v_subrev_u32 v25, s62, v92\n" \
  "v_subrev_u32 v26, s94, v93\n" \
  "s_nop 0\n" \
  "v_max_i32_dpp v25, v25, v25 quad_perm:[1,0,3,2] row_mask:0xf bank_mask:0xf\n" \
  "v_max_i32_dpp v26, v26, v26 quad_perm:[1,0,3,2] row_mask:0xf bank_mask:0xf\n" \
  "s_cbranch_vccnz LK3_%=\n" \
  "v_max_i32_dpp v25, v25, v25 quad_perm:[2,3,0,1] row_mask:0xf bank_mask:0xf\n" \
  "v_max_i32_dpp v26, v26, v26 quad_perm:[2,3,0,1] row_mask:0xf bank_mask:0xf\n" \
  "LK5_%=:\n" \
  "v_cvt_f32_i32 v25, v25\n" \
  "s_add_u32 s59, s48, 1\n" \
  "v_mov_b32 v93, s59\n" \
  "v_mul_f32 v25, v25, v19\n" \
  "v_cvt_i32_f32 v25, v25\n" \
  "v_add_u32 v92, s68, v25\n" \
  "ds_write_b64 v255, v[92:93] offset:14352\n" \
  "v_max_i32 v26, 0, v26\n" \
  "v_min_i32 v26, 400, v26\n" \
  "ds_write_b32 v255, v26 offset:14344\n" \
  "v_mfma_f32_16x16x32_bf16 v[184:187], v[136:139], a[192:195], 0\n" \
  "v_mov_b64 v[48:49], 0\n" \
  "ds_read_b128 v[104:107], v84 offset:0\n" \
  "v_mfma_f32_16x16x32_bf16 v[188:191], v[152:155], a[192:195], 0\n" \
  "v_mov_b64 v[50:51], 0\n" \
  "ds_read_b128 v[108:111], v85 offset:0\n" \
  "v_mfma_f32_16x16x32_bf16 v[192:195], v[136:139], a[208:211], 0\n" \
  "v_mov_b64 v[52:53], 0\n" \
  "ds_read_b128 v[112:115], v86 offset:0\n" \
  "v_mfma_f32_16x16x32_bf16 v[196:199], v[152:155], a[208:211], 0\n" \
  "v_mov_b64 v[54:55], 0\n" \
  "ds_read_b128 v[116:119], v87 offset:0\n" \
  "v_mfma_f32_16x16x32_bf16 v[184:187], v[140:143], a[196:199], v[184:187]\n" \
  "v_mov_b64 v[56:57], 0\n" \
  "ds_read_b128 v[120:123], v84 offset:4096\n" \
  "v_mfma_f32_16x16x32_bf16 v[188:191], v[156:159], a[196:199], v[188:191]\n" \
  "v_mov_b64 v[58:59], 0\n" \
  "ds_read_b128 v[124:127], v85 offset:4096\n" \
  "v_mfma_f32_16x16x32_bf16 v[192:195], v[140:143], a[212:215], v[192:195]\n" \
  "v_mov_b64 v[60:61], 0\n" \
  "ds_read_b128 v[128:131], v86 offset:4096\n" \
  "v_mfma_f32_16x16x32_bf16 v[196:199], v[156:159], a[212:215], v[196:199]\n" \
  "v_mov_b64 v[62:63], 0\n" \
  "ds_read_b128 v[132:135], v87 offset:4096\n" \
  "v_mfma_f32_16x16x32_bf16 v[184:187], v[144:147], a[200:203], v[184:187]\n" \
  "v_mov_b64 v[64:65], 0\n" \
  "v_mfma_f32_16x16x32_bf16 v[188:191], v[160:163], a[200:203], v[188:191]\n" \
  "v_mov_b64 v[66:67], 0\n" \
  "v_mfma_f32_16x16x32_bf16 v[192:195], v[144:147], a[216:219], v[192:195]\n" \
  "v_mov_b64 v[68:69], 0\n" \
  "v_xor_b32 v20, 16384, v20\n" \
  "v_mfma_f32_16x16x32_bf16 v[196:199], v[160:163], a[216:219], v[196:199]\n" \
  "v_mov_b64 v[70:71], 0\n" \
  "v_xor_b32 v21, 16384, v21\n" \
  "v_mfma_f32_16x16x32_bf16 v[184:187], v[148:151], a[204:207], v[184:187]\n" \
  "v_xor_b32 v84, 16384, v84\n" \
  "v_mfma_f32_16x16x32_bf16 v[188:191], v[164:167], a[204:207], v[188:191]\n" \
  "v_xor_b32 v85, 16384, v85\n" \
  "v_mfma_f32_16x16x32_bf16 v[192:195], v[148:151], a[220:223], v[192:195]\n" \
  "v_xor_b32 v86, 16384, v86\n" \
  "v_mfma_f32_16x16x32_bf16 v[196:199], v[164:167], a[220:223], v[196:199]\n" \
  "v_xor_b32 v87, 16384, v87\n" \
  "s_add_u32 s48, s48, 1\n" \
  "s_nop 4\n" \
  "v_cvt_pk_bf16_f32 v168, v184, v185\n" \
  "v_cvt_pk_bf16_f32 v169, v186, v187\n" \
  "v_cvt_pk_bf16_f32 v170, v188, v189\n" \
  "v_cvt_pk_bf16_f32 v171, v190, v191\n" \
  "v_cvt_pk_bf16_f32 v172, v192, v193\n" \
  "v_cvt_pk_bf16_f32 v173, v194, v195\n" \
  "v_cvt_pk_bf16_f32 v174, v196, v197\n" \
  "v_cvt_pk_bf16_f32 v175, v198, v199\n" \
  "s_cmp_lg_u32 s54, 0\n" \
  "s_cbranch_scc1 LFc_%=\n" \
  "s_waitcnt vmcnt(1)\n" \
  "ds_write_b128 v18, v[200:203] offset:0\n" \
  "ds_write_b128 v18, v[204:207] offset:1024\n" \
  "ds_write_b128 v18, v[208:211] offset:2048\n" \
  "ds_write_b128 v18, v[212:215] offset:3072\n" \
  "v_xor_b32 v18, 16384, v18\n" \
  "LFc2_%=:\n" \
  "s_waitcnt lgkmcnt(0)\n" \
  "s_branch LL_%=\n" \
  "LA_%=:\n" \
  "ds_read_b64 v[92:93], v255 offset:14336\n" \
  "v_max3_u32 v25, v32, v33, v34\n" \
  "v_max3_u32 v25, v25, v35, v36\n" \
  "v_max3_u32 v25, v25, v37, v38\n" \
  "v_max3_u32 v25, v25, v39, v40\n" \
  "v_max3_u32 v25, v25, v41, v42\n" \
  "v_max3_u32 v25, v25, v43, v44\n" \
  "v_max3_u32 v25, v25, v45, v46\n" \
  "v_max_u32 v25, v25, v47\n" \
  "v_cmp_eq_u32 vcc, -1, v25\n" \
  "s_cbranch_vccz LA4_%=\n" \
  "s_add_u32 s58, s58, 1\n" \
  "s_cmp_lt_u32 s58, " STR(SPIN_CAP) "\n" \
  "s_cbranch_scc1 LA1_%=\n" \
  "v_mov_b32 v27, " STR(CW_DEAD) "*4\n" \
  "v_mov_b32 v28, 1\n" \
  "buffer_store_dword v28, v27, s[44:47], 0 offen sc1\n" \
  "s_branch LA4_%=\n" \
  "LA1_%=:\n" \
  "s_waitcnt lgkmcnt(0)\n" \
  "v_readfirstlane_b32 s64, v92\n" \
  "s_memtime s[66:67]\n" \
  "s_waitcnt lgkmcnt(0)\n" \
  "s_sub_u32 s59, " STR(O_A) ", s66\n" \
  "LQ_ra_%=:\n" \
  "s_add_u32 s64, s64, s95\n" \
  "s_add_i32 s61, s64, s59\n" \
  "s_cmp_lt_i32 s61, -" STR(T_TOL) "\n" \
  "s_cbranch_scc1 LQ_ra_%=\n" \
  "s_max_i32 s61, s61, 0\n" \
  "s_min_i32 s61, s61, 4000\n" \
  "s_bitcmp0_b32 s61, 4\n" \
  "s_cbranch_scc1 LX_ra_%=\n" \
  "s_nop 8\n" \
  "LX_ra_%=:\n" \
  "s_lshr_b32 s61, s61, 5\n" \
  "s_cbranch_scc0 LB_ra_%=\n" \
  ".p2align 4\n" \
  "LY_ra_%=:\n" \
  "s_sub_u32 s61, s61, 1\n" \
  "s_cmp_lg_u32 s61, 0\n" \
  "s_cbranch_scc1 LY_ra_%=\n" \
  "LB_ra_%=:\n" \
  "buffer_load_dwordx4 v[32:35], v80, s[36:39], s49 offen sc1\n" \
  "s_nop 11\n" \
  "buffer_load_dwordx4 v[36:39], v81, s[36:39], s49 offen sc1\n" \
  "s_nop 11\n" \
  "buffer_load_dwordx4 v[40:43], v82, s[36:39], s49 offen sc1\n" \
  "s_nop 11\n" \
  "buffer_load_dwordx4 v[44:47], v83, s[36:39], s49 offen sc1\n" \
  "v_mov_b64 v[48:49], 0\n" \
  "v_mov_b64 v[50:51], 0\n" \
  "v_mov_b64 v[52:53], 0\n" \
  "v_mov_b64 v[54:55], 0\n" \
  "v_mov_b64 v[56:57], 0\n" \
  "v_mov_b64 v[58:59], 0\n" \
  "v_mov_b64 v[60:61], 0\n" \
  "v_mov_b64 v[62:63], 0\n" \
  "v_mov_b64 v[64:65], 0\n" \
  "v_mov_b64 v[66:67], 0\n" \
  "v_mov_b64 v[68:69], 0\n" \
  "v_mov_b64 v[70:71], 0\n" \
  "v_sub_u32 v27, s64, v92\n" \
  "ds_max_u32 v255, v27 offset:14340\n" \
  "s_branch LS1_%=\n" \
  "LW_b_%=:\n" \
  "s_min_i32 s61, s61, 4000\n" \
  "s_sub_i32 s61, s61, 56\n" \
  ".p2align 4\n" \
  "LY_b_%=:\n" \
  "s_sub_i32 s61, s61, 32\n" \
  "s_cmp_gt_i32 s61, 704\n" \
  "s_cbranch_scc1 LY_b_%=\n" \
  "s_branch LV_b_%=\n" \
  "LBE_%=:\n" \
  "v_max3_u32 v26, v32, v33, v34\n" \
  "v_max3_u32 v26, v26, v35, v36\n" \
  "v_max3_u32 v26, v26, v37, v38\n" \
  "v_max3_u32 v26, v26, v39, v40\n" \
  "v_max3_u32 v26, v26, v41, v42\n" \
  "v_max3_u32 v26, v26, v43, v44\n" \
  "v_max3_u32 v26, v26, v45, v46\n" \
  "v_max_u32 v26, v26, v47\n" \
  "v_cmp_eq_u32 vcc, -1, v26\n" \
  "s_cbranch_vccz LBH_%=\n" \
  "s_add_u32 s58, s58, 1\n" \
  "s_cmp_lt_u32 s58, " STR(SPIN_CAP) "\n" \
  "s_cbranch_scc1 LB1_%=\n" \
  "v_mov_b32 v27, " STR(CW_DEAD) "*4\n" \
  "v_mov_b32 v28, 1\n" \
  "buffer_store_dword v28, v27, s[44:47], 0 offen sc1\n" \
  "s_branch LBI_%=\n" \
  "LBI_%=:\n" \
  "v_mov_b32 v27, v25\n" \
  "s_nop 1\n" \
  "s_branch LBH_%=\n" \
  "LBF_%=:\n" \
  "v_max3_u32 v25, v32, v33, v34\n" \
  "v_max3_u32 v25, v25, v35, v36\n" \
  "v_max3_u32 v25, v25, v37, v38\n" \
  "v_max3_u32 v25, v25, v39, v40\n" \
  "v_max3_u32 v25, v25, v41, v42\n" \
  "v_max3_u32 v25, v25, v43, v44\n" \
  "v_max3_u32 v25, v25, v45, v46\n" \
  "v_max_u32 v25, v25, v47\n" \
  "v_max3_u32 v25, v25, v96, v97\n" \
  "v_max3_u32 v25, v25, v98, v99\n" \
  "v_max3_u32 v25, v25, v100, v101\n" \
  "v_max3_u32 v25, v25, v102, v103\n" \
  "v_cmp_eq_u32 vcc, -1, v25\n" \
  "s_cbranch_vccz LBG_%=\n" \
  "s_add_u32 s58, s58, 1\n" \
  "s_cmp_lt_u32 s58, " STR(SPIN_CAP) "\n" \
  "s_cbranch_scc1 LB1_%=\n" \
  "v_mov_b32 v27, " STR(CW_DEAD) "*4\n" \
  "v_mov_b32 v28, 1\n" \
  "buffer_store_dword v28, v27, s[44:47], 0 offen sc1\n" \
  "s_branch LBG_%=\n" \
  "LB1_%=:\n" \
  "s_waitcnt vmcnt(0)\n" \
  "v_mov_b64 v[184:185], 0\n" \
  "v_mov_b64 v[186:187], 0\n" \
  "v_mov_b64 v[188:189], 0\n" \
  "v_mov_b64 v[190:191], 0\n" \
  "v_mov_b64 v[192:193], 0\n" \
  "v_mov_b64 v[194:195], 0\n" \
  "v_mov_b64 v[196:197], 0\n" \
  "v_mov_b64 v[198:199], 0\n" \
  "s_memtime s[66:67]\n" \
  "s_waitcnt lgkmcnt(0)\n" \
  "s_sub_u32 s59, " STR(F_B) "+" STR(O_B) ", s66\n" \
  "LQ_rb_%=:\n" \
  "s_add_u32 s64, s64, s95\n" \
  "s_add_i32 s61, s64, s59\n" \
  "s_cmp_lt_i32 s61, -" STR(T_TOL) "\n" \
  "s_cbranch_scc1 LQ_rb_%=\n" \
  "s_max_i32 s61, s61, 0\n" \
  "s_min_i32 s61, s61, 4000\n" \
  "s_bitcmp0_b32 s61, 4\n" \
  "s_cbranch_scc1 LX_rb_%=\n" \
  "s_nop 8\n" \
  "LX_rb_%=:\n" \
  "s_lshr_b32 s61, s61, 5\n" \
  "s_cbranch_scc0 LB_rb_%=\n" \
  ".p2align 4\n" \
  "LY_rb_%=:\n" \
  "s_sub_u32 s61, s61, 1\n" \
  "s_cmp_lg_u32 s61, 0\n" \
  "s_cbranch_scc1 LY_rb_%=\n" \
  "LB_rb_%=:\n" \
  "buffer_load_dwordx4 v[32:35], v80, s[36:39], s50 offen sc1\n" \
  "s_nop 11\n" \
  "buffer_load_dwordx4 v[36:39], v81, s[36:39], s50 offen sc1\n" \
  "s_nop 11\n" \
  "buffer_load_dwordx4 v[40:43], v82, s[36:39], s50 offen sc1\n" \
  "s_nop 11\n" \
  "buffer_load_dwordx4 v[44:47], v83, s[36:39], s50 offen sc1\n" \
  "s_nop 11\n" \
  "buffer_load_dwordx4 v[96:99], v17, s[36:39], s50 offen offset:0 sc1\n" \
  "s_nop 11\n" \
  "buffer_load_dwordx4 v[100:103], v17, s[36:39], s50 offen offset:1024 sc1\n" \
  "buffer_store_dword v27, v88, s[36:39], 0 offen sc1\n" \
  "s_mov_b32 vcc_lo, 0x33333333\n" \
  "s_mov_b32 vcc_hi, 0x33333333\n" \
  "s_branch LS2_%=\n" \
  "LW_c_%=:\n" \
  "s_min_i32 s61, s61, 4000\n" \
  "s_sub_i32 s61, s61, 56\n" \
  ".p2align 4\n" \
  "LY_c_%=:\n" \
  "s_sub_i32 s61, s61, 32\n" \
  "s_cmp_gt_i32 s61, 704\n" \
  "s_cbranch_scc1 LY_c_%=\n" \
  "s_branch LV_c_%=\n" \
  "LW_a_%=:\n" \
  "s_min_i32 s61, s61, 4000\n" \
  "s_sub_i32 s61, s61, 56\n" \
  ".p2align 4\n" \
  "LY_a_%=:\n" \
  "s_sub_i32 s61, s61, 32\n" \
  "s_cmp_gt_i32 s61, 704\n" \
  "s_cbranch_scc1 LY_a_%=\n" \
  "s_branch LV_a_%=\n" \
  "LFa_%=:\n" \
  "buffer_load_dword v23, v22, s[44:47], 0 offen sc1\n" \
  "s_add_u32 s60, s48, 1\n" \
  "s_min_u32 s60, s60, 63\n" \
  "s_waitcnt vmcnt(0)\n" \
  "v_cmp_ge_u32 vcc, s60, v23\n" \
  "s_cbranch_vccz LFa3_%=\n" \
  "s_sleep 2\n" \
  "s_add_u32 s58, s58, 1\n" \
  "s_cmp_lt_u32 s58, " STR(SPIN_CAP) "\n" \
  "s_cbranch_scc1 LFa_%=\n" \
  "v_mov_b32 v27, " STR(CW_DEAD) "*4\n" \
  "v_mov_b32 v28, 1\n" \
  "buffer_store_dword v28, v27, s[44:47], 0 offen sc1\n" \
  "s_branch LFa3_%=\n" \
  "LFa3_%=:\n" \
  "buffer_load_dwordx4 v[200:203], v247, s[40:43], s55 offen offset:0 sc1\n" \
  "buffer_load_dwordx4 v[204:207], v247, s[40:43], s55 offen offset:1024 sc1\n" \
  "buffer_load_dwordx4 v[208:211], v247, s[40:43], s55 offen offset:2048 sc1\n" \
  "buffer_load_dwordx4 v[212:215], v247, s[40:43], s55 offen offset:3072 sc1\n" \
  "s_waitcnt vmcnt(0)\n" \
  "ds_write_b128 v18, v[200:203] offset:0\n" \
  "ds_write_b128 v18, v[204:207] offset:1024\n" \
  "ds_write_b128 v18, v[208:211] offset:2048\n" \
  "ds_write_b128 v18, v[212:215] offset:3072\n" \
  "v_xor_b32 v18, 16384, v18\n" \
  "s_branch LFa2_%=\n" \
  "LC_%=:\n" \
  "v_max3_u32 v25, v32, v33, v34\n" \
  "v_max3_u32 v25, v25, v35, v36\n" \
  "v_max3_u32 v25, v25, v37, v38\n" \
  "v_max3_u32 v25, v25, v39, v40\n" \
  "v_max3_u32 v25, v25, v41, v42\n" \
  "v_max3_u32 v25, v25, v43, v44\n" \
  "v_max3_u32 v25, v25, v45, v46\n" \
  "v_max_u32 v25, v25, v47\n" \
  "v_cmp_eq_u32 vcc, -1, v25\n" \
  "s_cbranch_vccz LC2_%=\n" \
  "s_add_u32 s58, s58, 1\n" \
  "s_cmp_lt_u32 s58, " STR(SPIN_CAP) "\n" \
  "s_cbranch_scc1 LC1_%=\n" \
  "v_mov_b32 v27, " STR(CW_DEAD) "*4\n" \
  "v_mov_b32 v28, 1\n" \
  "buffer_store_dword v28, v27, s[44:47], 0 offen sc1\n" \
  "s_branch LC2_%=\n" \
  "LC1_%=:\n" \
  "s_add_u32 s60, s96, " STR(O_C) "\n" \
  "s_memtime s[66:67]\n" \
  "s_waitcnt lgkmcnt(0)\n" \
  "s_sub_u32 s59, s60, s66\n" \
  "LQ_rc_%=:\n" \
  "s_add_u32 s64, s64, s95\n" \
  "s_add_i32 s61, s64, s59\n" \
  "s_cmp_lt_i32 s61, -" STR(T_TOL) "\n" \
  "s_cbranch_scc1 LQ_rc_%=\n" \
  "s_max_i32 s61, s61, 0\n" \
  "s_min_i32 s61, s61, 4000\n" \
  "s_bitcmp0_b32 s61, 4\n" \
  "s_cbranch_scc1 LX_rc_%=\n" \
  "s_nop 8\n" \
  "LX_rc_%=:\n" \
  "s_lshr_b32 s61, s61, 5\n" \
  "s_cbranch_scc0 LB_rc_%=\n" \
  ".p2align 4\n" \
  "LY_rc_%=:\n" \
  "s_sub_u32 s61, s61, 1\n" \
  "s_cmp_lg_u32 s61, 0\n" \
  "s_cbranch_scc1 LY_rc_%=\n" \
  "LB_rc_%=:\n" \
  "buffer_load_dwordx4 v[32:35], v11, s[36:39], s52 offen offset:0 sc1\n" \
  "buffer_load_dwordx4 v[36:39], v11, s[36:39], s52 offen offset:1024 sc1\n" \
  "buffer_load_dwordx4 v[40:43], v11, s[36:39], s52 offen offset:2048 sc1\n" \
  "buffer_load_dwordx4 v[44:47], v11, s[36:39], s52 offen offset:3072 sc1\n" \
  "s_branch LS3_%=\n" \
  "LFb_%=:\n" \
  "buffer_load_dword v23, v22, s[44:47], 0 offen sc1\n" \
  "s_add_u32 s60, s48, 1\n" \
  "s_min_u32 s60, s60, 63\n" \
  "s_waitcnt vmcnt(0)\n" \
  "v_cmp_ge_u32 vcc, s60, v23\n" \
  "s_cbranch_vccz LFb3_%=\n" \
  "s_sleep 2\n" \
  "s_add_u32 s58, s58, 1\n" \
  "s_cmp_lt_u32 s58, " STR(SPIN_CAP) "\n" \
  "s_cbranch_scc1 LFb_%=\n" \
  "v_mov_b32 v27, " STR(CW_DEAD) "*4\n" \
  "v_mov_b32 v28, 1\n" \
  "buffer_store_dword v28, v27, s[44:47], 0 offen sc1\n" \
  "s_branch LFb3_%=\n" \
  "LFb3_%=:\n" \
  "buffer_load_dwordx4 v[200:203], v247, s[40:43], s55 offen offset:0 sc1\n" \
  "buffer_load_dwordx4 v[204:207], v247, s[40:43], s55 offen offset:1024 sc1\n" \
  "buffer_load_dwordx4 v[208:211], v247, s[40:43], s55 offen offset:2048 sc1\n" \
  "buffer_load_dwordx4 v[212:215], v247, s[40:43], s55 offen offset:3072 sc1\n" \
  "s_waitcnt vmcnt(0)\n" \
  "ds_write_b128 v18, v[200:203] offset:0\n" \
  "ds_write_b128 v18, v[204:207] offset:1024\n" \
  "ds_write_b128 v18, v[208:211] offset:2048\n" \
  "ds_write_b128 v18, v[212:215] offset:3072\n" \
  "v_xor_b32 v18, 16384, v18\n" \
  "s_branch LFb2_%=\n" \
  "LFc_%=:\n" \
  "buffer_load_dword v23, v22, s[44:47], 0 offen sc1\n" \
  "s_add_u32 s60, s48, 1\n" \
  "s_min_u32 s60, s60, 63\n" \
  "s_waitcnt vmcnt(0)\n" \
  "v_cmp_ge_u32 vcc, s60, v23\n" \
  "s_cbranch_vccz LFc3_%=\n" \
  "s_sleep 2\n" \
  "s_add_u32 s58, s58, 1\n" \
  "s_cmp_lt_u32 s58, " STR(SPIN_CAP) "\n" \
  "s_cbranch_scc1 LFc_%=\n" \
  "v_mov_b32 v27, " STR(CW_DEAD) "*4\n" \
  "v_mov_b32 v28, 1\n" \
  "buffer_store_dword v28, v27, s[44:47], 0 offen sc1\n" \
  "s_branch LFc3_%=\n" \
  "LFc3_%=:\n" \
  "buffer_load_dwordx4 v[200:203], v247, s[40:43], s55 offen offset:0 sc1\n" \
  "buffer_load_dwordx4 v[204:207], v247, s[40:43], s55 offen offset:1024 sc1\n" \
  "buffer_load_dwordx4 v[208:211], v247, s[40:43], s55 offen offset:2048 sc1\n" \
  "buffer_load_dwordx4 v[212:215], v247, s[40:43], s55 offen offset:3072 sc1\n" \
  "s_waitcnt vmcnt(0)\n" \
  "ds_write_b128 v18, v[200:203] offset:0\n" \
  "ds_write_b128 v18, v[204:207] offset:1024\n" \
  "ds_write_b128 v18, v[208:211] offset:2048\n" \
  "ds_write_b128 v18, v[212:215] offset:3072\n" \
  "v_xor_b32 v18, 16384, v18\n" \
  "s_branch LFc2_%=\n" \
  "LK3_%=:\n" \
  "s_sleep 2\n" \
  "s_andn2_b32 s59, s50, 0x70000\n" \
  "v_xor_b32 v28, s59, v13\n" \
  "buffer_load_dwordx2 v[92:93], v28, s[36:39], 0 offen sc1\n" \
  "s_waitcnt vmcnt(0)\n" \
  "s_add_u32 s58, s58, 1\n" \
  "s_cmp_lt_u32 s58, " STR(SPIN_CAP) "\n" \
  "s_cbranch_scc1 LK2_%=\n" \
  "v_mov_b32 v27, " STR(CW_DEAD) "*4\n" \
  "v_mov_b32 v28, 1\n" \
  "buffer_store_dword v28, v27, s[44:47], 0 offen sc1\n" \
  "s_branch LK6_%=\n" \
  "LK6_%=:\n" \
  "v_mov_b32 v25, 0\n" \
  "s_branch LK5_%=\n" \
  "LD_%=:\n" \
  "s_waitcnt lgkmcnt(0)\n" \
  "v_pk_add_f32 v[72:73], v[72:73], v[74:75]\n" \
  "v_pk_add_f32 v[76:77], v[76:77], v[78:79]\n" \
  "v_pk_add_f32 v[72:73], v[72:73], v[76:77]\n" \
  "v_cvt_pk_bf16_f32 v27, v72, v73\n" \
  "buffer_store_dword v27, v9, s[36:39], s50 offen sc1\n" \
  "LE_%=:\n" \
  "s_add_u32 s59, s48, 1\n" \
  "v_mov_b32 v28, s59\n" \
  "buffer_store_dword v28, v0, s[44:47], 0 offen sc1\n" \
  "s_endpgm\n" \
  ""

#define GEMM_ASM \
  "v_mbcnt_lo_u32_b32 v4, -1, 0\n" \
  "v_mbcnt_hi_u32_b32 v4, -1, v4\n" \
  "v_and_b32 v116, 15, v4\n" \
  "v_lshrrev_b32 v117, 4, v4\n" \
  "s_lshl_b32 s86, %7, 13\n" \
  "s_add_u32 s86, s86, %8\n" \
  "v_lshrrev_b32 v5, 3, v4\n" \
  "v_and_b32 v6, 7, v4\n" \
  "v_lshlrev_b32 v8, 13, v5\n" \
  "v_lshl_add_u32 v8, v6, 4, v8\n" \
  "v_and_b32 v9, 1, v117\n" \
  "v_lshlrev_b32 v9, 5, v9\n" \
  "v_xor_b32 v8, v8, v9\n" \
  "v_xor_b32 v9, 64, v8\n" \
  "s_mov_b64 s[68:69], %1\n" \
  "s_and_b32 s69, s69, 0xffff\n" \
  "s_mov_b32 s70, 8388608\n" \
  "s_mov_b32 s71, 0x00020000\n" \
  "s_lshl_b32 s92, %7, 19\n" \
  "s_add_u32 s92, s92, 0\n" \
  "s_add_u32 m0, s86, 98304\n" \
  "s_nop 0\n" \
  "buffer_load_dwordx4 v8, s[68:71], s92 offen lds\n" \
  "s_add_u32 m0, s86, 99328\n" \
  "s_add_u32 s92, s92, 65536\n" \
  "buffer_load_dwordx4 v9, s[68:71], s92 offen lds\n" \
  "s_add_u32 m0, s86, 100352\n" \
  "s_add_u32 s92, s92, 65536\n" \
  "buffer_load_dwordx4 v8, s[68:71], s92 offen lds\n" \
  "s_add_u32 m0, s86, 101376\n" \
  "s_add_u32 s92, s92, 65536\n" \
  "buffer_load_dwordx4 v9, s[68:71], s92 offen lds\n" \
  "s_add_u32 m0, s86, 102400\n" \
  "s_add_u32 s92, s92, 65536\n" \
  "buffer_load_dwordx4 v8, s[68:71], s92 offen lds\n" \
  "s_add_u32 m0, s86, 103424\n" \
  "s_add_u32 s92, s92, 65536\n" \
  "buffer_load_dwordx4 v9, s[68:71], s92 offen lds\n" \
  "s_add_u32 m0, s86, 104448\n" \
  "s_add_u32 s92, s92, 65536\n" \
  "buffer_load_dwordx4 v8, s[68:71], s92 offen lds\n" \
  "s_add_u32 m0, s86, 105472\n" \
  "s_add_u32 s92, s92, 65536\n" \
  "buffer_load_dwordx4 v9, s[68:71], s92 offen lds\n" \
  "s_lshl_b32 s92, %7, 19\n" \
  "s_add_u32 s92, s92, 2097152\n" \
  "s_add_u32 m0, s86, 131072\n" \
  "s_nop 0\n" \
  "buffer_load_dwordx4 v8, s[68:71], s92 offen lds\n" \
  "s_add_u32 m0, s86, 132096\n" \
  "s_add_u32 s92, s92, 65536\n" \
  "buffer_load_dwordx4 v9, s[68:71], s92 offen lds\n" \
  "s_add_u32 m0, s86, 133120\n" \
  "s_add_u32 s92, s92, 65536\n" \
  "buffer_load_dwordx4 v8, s[68:71], s92 offen lds\n" \
  "s_add_u32 m0, s86, 134144\n" \
  "s_add_u32 s92, s92, 65536\n" \
  "buffer_load_dwordx4 v9, s[68:71], s92 offen lds\n" \
  "s_add_u32 m0, s86, 135168\n" \
  "s_add_u32 s92, s92, 65536\n" \
  "buffer_load_dwordx4 v8, s[68:71], s92 offen lds\n" \
  "s_add_u32 m0, s86, 136192\n" \
  "s_add_u32 s92, s92, 65536\n" \
  "buffer_load_dwordx4 v9, s[68:71], s92 offen lds\n" \
  "s_add_u32 m0, s86, 137216\n" \
  "s_add_u32 s92, s92, 65536\n" \
  "buffer_load_dwordx4 v8, s[68:71], s92 offen lds\n" \
  "s_add_u32 m0, s86, 138240\n" \
  "s_add_u32 s92, s92, 65536\n" \
  "buffer_load_dwordx4 v9, s[68:71], s92 offen lds\n" \
  "s_lshl_b32 s92, %7, 19\n" \
  "s_add_u32 s92, s92, 4194304\n" \
  "s_add_u32 m0, s86, 0\n" \
  "s_nop 0\n" \
  "buffer_load_dwordx4 v8, s[68:71], s92 offen lds\n" \
  "s_add_u32 m0, s86, 1024\n" \
  "s_add_u32 s92, s92, 65536\n" \
  "buffer_load_dwordx4 v9, s[68:71], s92 offen lds\n" \
  "s_add_u32 m0, s86, 2048\n" \
  "s_add_u32 s92, s92, 65536\n" \
  "buffer_load_dwordx4 v8, s[68:71], s92 offen lds\n" \
  "s_add_u32 m0, s86, 3072\n" \
  "s_add_u32 s92, s92, 65536\n" \
  "buffer_load_dwordx4 v9, s[68:71], s92 offen lds\n" \
  "s_add_u32 m0, s86, 4096\n" \
  "s_add_u32 s92, s92, 65536\n" \
  "buffer_load_dwordx4 v8, s[68:71], s92 offen lds\n" \
  "s_add_u32 m0, s86, 5120\n" \
  "s_add_u32 s92, s92, 65536\n" \
  "buffer_load_dwordx4 v9, s[68:71], s92 offen lds\n" \
  "s_add_u32 m0, s86, 6144\n" \
  "s_add_u32 s92, s92, 65536\n" \
  "buffer_load_dwordx4 v8, s[68:71], s92 offen lds\n" \
  "s_add_u32 m0, s86, 7168\n" \
  "s_add_u32 s92, s92, 65536\n" \
  "buffer_load_dwordx4 v9, s[68:71], s92 offen lds\n" \
  "s_lshl_b32 s92, %7, 19\n" \
  "s_add_u32 s92, s92, 6291456\n" \
  "s_add_u32 m0, s86, 32768\n" \
  "s_nop 0\n" \
  "buffer_load_dwordx4 v8, s[68:71], s92 offen lds\n" \
  "s_add_u32 m0, s86, 33792\n" \
  "s_add_u32 s92, s92, 65536\n" \
  "buffer_load_dwordx4 v9, s[68:71], s92 offen lds\n" \
  "s_add_u32 m0, s86, 34816\n" \
  "s_add_u32 s92, s92, 65536\n" \
  "buffer_load_dwordx4 v8, s[68:71], s92 offen lds\n" \
  "s_add_u32 m0, s86, 35840\n" \
  "s_add_u32 s92, s92, 65536\n" \
  "buffer_load_dwordx4 v9, s[68:71], s92 offen lds\n" \
  "s_add_u32 m0, s86, 36864\n" \
  "s_add_u32 s92, s92, 65536\n" \
  "buffer_load_dwordx4 v8, s[68:71], s92 offen lds\n" \
  "s_add_u32 m0, s86, 37888\n" \
  "s_add_u32 s92, s92, 65536\n" \
  "buffer_load_dwordx4 v9, s[68:71], s92 offen lds\n" \
  "s_add_u32 m0, s86, 38912\n" \
  "s_add_u32 s92, s92, 65536\n" \
  "buffer_load_dwordx4 v8, s[68:71], s92 offen lds\n" \
  "s_add_u32 m0, s86, 39936\n" \
  "s_add_u32 s92, s92, 65536\n" \
  "buffer_load_dwordx4 v9, s[68:71], s92 offen lds\n" \
  "s_mov_b64 s[72:73], %3\n" \
  "s_and_b32 s73, s73, 0xffff\n" \
  "s_mov_b32 s74, 4194304\n" \
  "s_mov_b32 s75, 0x00020000\n" \
  "s_mov_b64 s[76:77], %4\n" \
  "s_and_b32 s77, s77, 0xffff\n" \
  "s_mov_b32 s78, 3*" STR(QKV_SZ32) "\n" \
  "s_mov_b32 s79, 0x00020000\n" \
  "s_mov_b64 s[80:81], %5\n" \
  "s_and_b32 s81, s81, 0xffff\n" \
  "s_mov_b32 s82, " STR(CTRL_BYTES) "\n" \
  "s_mov_b32 s83, 0x00020000\n" \
  "v_xor_b32 v6, v6, v117\n" \
  "s_and_b32 s84, %7, 1\n" \
  "s_lshl_b32 s84, s84, 2\n" \
  "v_xor_b32 v6, s84, v6\n" \
  "v_lshlrev_b32 v7, 11, v5\n" \
  "v_lshl_add_u32 v7, v6, 4, v7\n" \
  "s_lshl_b32 s87, %7, 14\n" \
  "s_add_u32 s96, s87, 63*65536\n" \
  "v_lshrrev_b32 v5, 1, v116\n" \
  "v_xor_b32 v5, v5, v117\n" \
  "v_lshlrev_b32 v5, 4, v5\n" \
  "v_lshl_add_u32 v5, v116, 7, v5\n" \
  "v_add_u32 v10, %8, v5\n" \
  "v_xor_b32 v5, 64, v5\n" \
  "v_add_u32 v11, %8, v5\n" \
  "v_add_u32 v12, 0x10000, v10\n" \
  "v_add_u32 v13, 0x10000, v11\n" \
  "s_lshr_b32 s84, %6, 1\n" \
  "s_lshl_b32 s84, s84, 14\n" \
  "s_and_b32 s85, %6, 1\n" \
  "s_lshl_b32 s85, s85, 2\n" \
  "s_add_u32 s85, s85, %7\n" \
  "s_lshl_b32 s91, s85, 9\n" \
  "s_add_u32 s91, s91, s84\n" \
  "v_and_b32 v5, 1, v117\n" \
  "v_lshlrev_b32 v14, 12, v5\n" \
  "v_lshrrev_b32 v5, 1, v117\n" \
  "v_lshl_add_u32 v14, v5, 8, v14\n" \
  "v_lshl_add_u32 v14, v116, 4, v14\n" \
  "v_add_u32 v14, s91, v14\n" \
  "s_lshl_b32 s91, s85, 10\n" \
  "s_add_u32 s91, s91, s84\n" \
  "v_lshlrev_b32 v15, 4, v4\n" \
  "v_add_u32 v15, s91, v15\n" \
  "v_xor_b32 v18, 0x800, v14\n" \
  "v_xor_b32 v15, 0x4000, v15\n" \
  "s_lshl_b32 s84, %6, 7\n" \
  "s_add_u32 s84, s84, " STR(CW_GPROG) "*4\n" \
  "v_or_b32 v5, %7, v4\n" \
  "v_lshl_add_u32 v16, v5, 24, s84\n" \
  "v_mov_b32 v17, 0\n" \
  "v_or_b32 v14, 0x80000000, v14\n" \
  "v_or_b32 v15, 0x80000000, v15\n" \
  "v_or_b32 v18, 0x80000000, v18\n" \
  "v_lshrrev_b32 v5, 2, v116\n" \
  "v_lshl_add_u32 v6, v117, 3, v5\n" \
  "v_lshlrev_b32 v6, 7, v6\n" \
  "v_lshrrev_b32 v5, 1, v5\n" \
  "v_and_b32 v119, 1, v117\n" \
  "v_lshl_add_u32 v5, v119, 1, v5\n" \
  "v_xor_b32 v5, %7, v5\n" \
  "v_lshl_add_u32 v6, v5, 5, v6\n" \
  "v_and_b32 v5, 3, v4\n" \
  "v_lshl_add_u32 v6, v5, 3, v6\n" \
  "v_add_u32 v118, %8, v6\n" \
  "v_add_u32 v119, 0x10000, v118\n" \
  "v_add_u32 v120, 0x20000, v118\n" \
  "s_lshl_b32 s84, %7, 10\n" \
  "s_add_u32 s91, s84, %8\n" \
  "s_waitcnt vmcnt(24) lgkmcnt(0)\n" \
  "s_barrier\n" \
  "s_mov_b64 s[68:69], %2\n" \
  "s_and_b32 s69, s69, 0xffff\n" \
  "s_mov_b32 s70, 8388608\n" \
  "s_mov_b32 s71, 0x00020000\n" \
  "s_lshl_b32 s92, %7, 19\n" \
  "s_add_u32 s92, s92, 0\n" \
  "s_add_u32 m0, s86, 65536\n" \
  "s_nop 0\n" \
  "buffer_load_dwordx4 v8, s[68:71], s92 offen lds\n" \
  "s_add_u32 m0, s86, 66560\n" \
  "s_add_u32 s92, s92, 65536\n" \
  "buffer_load_dwordx4 v9, s[68:71], s92 offen lds\n" \
  "s_add_u32 m0, s86, 67584\n" \
  "s_add_u32 s92, s92, 65536\n" \
  "buffer_load_dwordx4 v8, s[68:71], s92 offen lds\n" \
  "s_add_u32 m0, s86, 68608\n" \
  "s_add_u32 s92, s92, 65536\n" \
  "buffer_load_dwordx4 v9, s[68:71], s92 offen lds\n" \
  "s_add_u32 m0, s86, 69632\n" \
  "s_add_u32 s92, s92, 65536\n" \
  "buffer_load_dwordx4 v8, s[68:71], s92 offen lds\n" \
  "s_add_u32 m0, s86, 70656\n" \
  "s_add_u32 s92, s92, 65536\n" \
  "buffer_load_dwordx4 v9, s[68:71], s92 offen lds\n" \
  "s_add_u32 m0, s86, 71680\n" \
  "s_add_u32 s92, s92, 65536\n" \
  "buffer_load_dwordx4 v8, s[68:71], s92 offen lds\n" \
  "s_add_u32 m0, s86, 72704\n" \
  "s_add_u32 s92, s92, 65536\n" \
  "buffer_load_dwordx4 v9, s[68:71], s92 offen lds\n" \
  "ds_read_b64_tr_b16 a[0:1], v119 offset:32768\n" \
  "ds_read_b64_tr_b16 a[2:3], v119 offset:33280\n" \
  "ds_read_b64_tr_b16 a[4:5], v119 offset:36864\n" \
  "ds_read_b64_tr_b16 a[6:7], v119 offset:37376\n" \
  "ds_read_b64_tr_b16 a[8:9], v119 offset:40960\n" \
  "ds_read_b64_tr_b16 a[10:11], v119 offset:41472\n" \
  "ds_read_b64_tr_b16 a[12:13], v119 offset:45056\n" \
  "ds_read_b64_tr_b16 a[14:15], v119 offset:45568\n" \
  "ds_read_b64_tr_b16 a[16:17], v119 offset:49152\n" \
  "ds_read_b64_tr_b16 a[18:19], v119 offset:49664\n" \
  "ds_read_b64_tr_b16 a[20:21], v119 offset:53248\n" \
  "ds_read_b64_tr_b16 a[22:23], v119 offset:53760\n" \
  "ds_read_b64_tr_b16 a[24:25], v119 offset:57344\n" \
  "ds_read_b64_tr_b16 a[26:27], v119 offset:57856\n" \
  "ds_read_b64_tr_b16 a[28:29], v119 offset:61440\n" \
  "ds_read_b64_tr_b16 a[30:31], v119 offset:61952\n" \
  "s_waitcnt vmcnt(24) lgkmcnt(0)\n" \
  "s_barrier\n" \
  "s_lshl_b32 s92, %7, 19\n" \
  "s_add_u32 s92, s92, 2097152\n" \
  "s_add_u32 m0, s86, 98304\n" \
  "s_nop 0\n" \
  "buffer_load_dwordx4 v8, s[68:71], s92 offen lds\n" \
  "s_add_u32 m0, s86, 99328\n" \
  "s_add_u32 s92, s92, 65536\n" \
  "buffer_load_dwordx4 v9, s[68:71], s92 offen lds\n" \
  "s_add_u32 m0, s86, 100352\n" \
  "s_add_u32 s92, s92, 65536\n" \
  "buffer_load_dwordx4 v8, s[68:71], s92 offen lds\n" \
  "s_add_u32 m0, s86, 101376\n" \
  "s_add_u32 s92, s92, 65536\n" \
  "buffer_load_dwordx4 v9, s[68:71], s92 offen lds\n" \
  "s_add_u32 m0, s86, 102400\n" \
  "s_add_u32 s92, s92, 65536\n" \
  "buffer_load_dwordx4 v8, s[68:71], s92 offen lds\n" \
  "s_add_u32 m0, s86, 103424\n" \
  "s_add_u32 s92, s92, 65536\n" \
  "buffer_load_dwordx4 v9, s[68:71], s92 offen lds\n" \
  "s_add_u32 m0, s86, 104448\n" \
  "s_add_u32 s92, s92, 65536\n" \
  "buffer_load_dwordx4 v8, s[68:71], s92 offen lds\n" \
  "s_add_u32 m0, s86, 105472\n" \
  "s_add_u32 s92, s92, 65536\n" \
  "buffer_load_dwordx4 v9, s[68:71], s92 offen lds\n" \
  "ds_read_b64_tr_b16 a[32:33], v120 offset:0\n" \
  "ds_read_b64_tr_b16 a[34:35], v120 offset:512\n" \
  "ds_read_b64_tr_b16 a[36:37], v120 offset:4096\n" \
  "ds_read_b64_tr_b16 a[38:39], v120 offset:4608\n" \
  "ds_read_b64_tr_b16 a[40:41], v120 offset:8192\n" \
  "ds_read_b64_tr_b16 a[42:43], v120 offset:8704\n" \
  "ds_read_b64_tr_b16 a[44:45], v120 offset:12288\n" \
  "ds_read_b64_tr_b16 a[46:47], v120 offset:12800\n" \
  "ds_read_b64_tr_b16 a[48:49], v120 offset:16384\n" \
  "ds_read_b64_tr_b16 a[50:51], v120 offset:16896\n" \
  "ds_read_b64_tr_b16 a[52:53], v120 offset:20480\n" \
  "ds_read_b64_tr_b16 a[54:55], v120 offset:20992\n" \
  "ds_read_b64_tr_b16 a[56:57], v120 offset:24576\n" \
  "ds_read_b64_tr_b16 a[58:59], v120 offset:25088\n" \
  "ds_read_b64_tr_b16 a[60:61], v120 offset:28672\n" \
  "ds_read_b64_tr_b16 a[62:63], v120 offset:29184\n" \
  "s_waitcnt vmcnt(24) lgkmcnt(0)\n" \
  "s_barrier\n" \
  "s_lshl_b32 s92, %7, 19\n" \
  "s_add_u32 s92, s92, 4194304\n" \
  "s_add_u32 m0, s86, 131072\n" \
  "s_nop 0\n" \
  "buffer_load_dwordx4 v8, s[68:71], s92 offen lds\n" \
  "s_add_u32 m0, s86, 132096\n" \
  "s_add_u32 s92, s92, 65536\n" \
  "buffer_load_dwordx4 v9, s[68:71], s92 offen lds\n" \
  "s_add_u32 m0, s86, 133120\n" \
  "s_add_u32 s92, s92, 65536\n" \
  "buffer_load_dwordx4 v8, s[68:71], s92 offen lds\n" \
  "s_add_u32 m0, s86, 134144\n" \
  "s_add_u32 s92, s92, 65536\n" \
  "buffer_load_dwordx4 v9, s[68:71], s92 offen lds\n" \
  "s_add_u32 m0, s86, 135168\n" \
  "s_add_u32 s92, s92, 65536\n" \
  "buffer_load_dwordx4 v8, s[68:71], s92 offen lds\n" \
  "s_add_u32 m0, s86, 136192\n" \
  "s_add_u32 s92, s92, 65536\n" \
  "buffer_load_dwordx4 v9, s[68:71], s92 offen lds\n" \
  "s_add_u32 m0, s86, 137216\n" \
  "s_add_u32 s92, s92, 65536\n" \
  "buffer_load_dwordx4 v8, s[68:71], s92 offen lds\n" \
  "s_add_u32 m0, s86, 138240\n" \
  "s_add_u32 s92, s92, 65536\n" \
  "buffer_load_dwordx4 v9, s[68:71], s92 offen lds\n" \
  "ds_read_b64_tr_b16 a[64:65], v118 offset:0\n" \
  "ds_read_b64_tr_b16 a[66:67], v118 offset:512\n" \
  "ds_read_b64_tr_b16 a[68:69], v118 offset:4096\n" \
  "ds_read_b64_tr_b16 a[70:71], v118 offset:4608\n" \
  "ds_read_b64_tr_b16 a[72:73], v118 offset:8192\n" \
  "ds_read_b64_tr_b16 a[74:75], v118 offset:8704\n" \
  "ds_read_b64_tr_b16 a[76:77], v118 offset:12288\n" \
  "ds_read_b64_tr_b16 a[78:79], v118 offset:12800\n" \
  "ds_read_b64_tr_b16 a[80:81], v118 offset:16384\n" \
  "ds_read_b64_tr_b16 a[82:83], v118 offset:16896\n" \
  "ds_read_b64_tr_b16 a[84:85], v118 offset:20480\n" \
  "ds_read_b64_tr_b16 a[86:87], v118 offset:20992\n" \
  "ds_read_b64_tr_b16 a[88:89], v118 offset:24576\n" \
  "ds_read_b64_tr_b16 a[90:91], v118 offset:25088\n" \
  "ds_read_b64_tr_b16 a[92:93], v118 offset:28672\n" \
  "ds_read_b64_tr_b16 a[94:95], v118 offset:29184\n" \
  "s_waitcnt vmcnt(24) lgkmcnt(0)\n" \
  "s_barrier\n" \
  "s_lshl_b32 s92, %7, 19\n" \
  "s_add_u32 s92, s92, 6291456\n" \
  "s_add_u32 m0, s86, 0\n" \
  "s_nop 0\n" \
  "buffer_load_dwordx4 v8, s[68:71], s92 offen lds\n" \
  "s_add_u32 m0, s86, 1024\n" \
  "s_add_u32 s92, s92, 65536\n" \
  "buffer_load_dwordx4 v9, s[68:71], s92 offen lds\n" \
  "s_add_u32 m0, s86, 2048\n" \
  "s_add_u32 s92, s92, 65536\n" \
  "buffer_load_dwordx4 v8, s[68:71], s92 offen lds\n" \
  "s_add_u32 m0, s86, 3072\n" \
  "s_add_u32 s92, s92, 65536\n" \
  "buffer_load_dwordx4 v9, s[68:71], s92 offen lds\n" \
  "s_add_u32 m0, s86, 4096\n" \
  "s_add_u32 s92, s92, 65536\n" \
  "buffer_load_dwordx4 v8, s[68:71], s92 offen lds\n" \
  "s_add_u32 m0, s86, 5120\n" \
  "s_add_u32 s92, s92, 65536\n" \
  "buffer_load_dwordx4 v9, s[68:71], s92 offen lds\n" \
  "s_add_u32 m0, s86, 6144\n" \
  "s_add_u32 s92, s92, 65536\n" \
  "buffer_load_dwordx4 v8, s[68:71], s92 offen lds\n" \
  "s_add_u32 m0, s86, 7168\n" \
  "s_add_u32 s92, s92, 65536\n" \
  "buffer_load_dwordx4 v9, s[68:71], s92 offen lds\n" \
  "ds_read_b64_tr_b16 a[96:97], v118 offset:32768\n" \
  "ds_read_b64_tr_b16 a[98:99], v118 offset:33280\n" \
  "ds_read_b64_tr_b16 a[100:101], v118 offset:36864\n" \
  "ds_read_b64_tr_b16 a[102:103], v118 offset:37376\n" \
  "ds_read_b64_tr_b16 a[104:105], v118 offset:40960\n" \
  "ds_read_b64_tr_b16 a[106:107], v118 offset:41472\n" \
  "ds_read_b64_tr_b16 a[108:109], v118 offset:45056\n" \
  "ds_read_b64_tr_b16 a[110:111], v118 offset:45568\n" \
  "ds_read_b64_tr_b16 a[112:113], v118 offset:49152\n" \
  "ds_read_b64_tr_b16 a[114:115], v118 offset:49664\n" \
  "ds_read_b64_tr_b16 a[116:117], v118 offset:53248\n" \
  "ds_read_b64_tr_b16 a[118:119], v118 offset:53760\n" \
  "ds_read_b64_tr_b16 a[120:121], v118 offset:57344\n" \
  "ds_read_b64_tr_b16 a[122:123], v118 offset:57856\n" \
  "ds_read_b64_tr_b16 a[124:125], v118 offset:61440\n" \
  "ds_read_b64_tr_b16 a[126:127], v118 offset:61952\n" \
  "s_waitcnt vmcnt(24) lgkmcnt(0)\n" \
  "s_barrier\n" \
  "s_mov_b64 s[68:69], %0\n" \
  "s_and_b32 s69, s69, 0xffff\n" \
  "s_mov_b32 s70, 8388608\n" \
  "s_mov_b32 s71, 0x00020000\n" \
  "s_lshl_b32 s92, %7, 19\n" \
  "s_add_u32 s92, s92, 0\n" \
  "s_add_u32 m0, s86, 32768\n" \
  "s_nop 0\n" \
  "buffer_load_dwordx4 v8, s[68:71], s92 offen lds\n" \
  "s_add_u32 m0, s86, 33792\n" \
  "s_add_u32 s92, s92, 65536\n" \
  "buffer_load_dwordx4 v9, s[68:71], s92 offen lds\n" \
  "s_add_u32 m0, s86, 34816\n" \
  "s_add_u32 s92, s92, 65536\n" \
  "buffer_load_dwordx4 v8, s[68:71], s92 offen lds\n" \
  "s_add_u32 m0, s86, 35840\n" \
  "s_add_u32 s92, s92, 65536\n" \
  "buffer_load_dwordx4 v9, s[68:71], s92 offen lds\n" \
  "s_add_u32 m0, s86, 36864\n" \
  "s_add_u32 s92, s92, 65536\n" \
  "buffer_load_dwordx4 v8, s[68:71], s92 offen lds\n" \
  "s_add_u32 m0, s86, 37888\n" \
  "s_add_u32 s92, s92, 65536\n" \
  "buffer_load_dwordx4 v9, s[68:71], s92 offen lds\n" \
  "s_add_u32 m0, s86, 38912\n" \
  "s_add_u32 s92, s92, 65536\n" \
  "buffer_load_dwordx4 v8, s[68:71], s92 offen lds\n" \
  "s_add_u32 m0, s86, 39936\n" \
  "s_add_u32 s92, s92, 65536\n" \
  "buffer_load_dwordx4 v9, s[68:71], s92 offen lds\n" \
  "ds_read_b64_tr_b16 a[128:129], v119 offset:0\n" \
  "ds_read_b64_tr_b16 a[130:131], v119 offset:512\n" \
  "ds_read_b64_tr_b16 a[132:133], v119 offset:4096\n" \
  "ds_read_b64_tr_b16 a[134:135], v119 offset:4608\n" \
  "ds_read_b64_tr_b16 a[136:137], v119 offset:8192\n" \
  "ds_read_b64_tr_b16 a[138:139], v119 offset:8704\n" \
  "ds_read_b64_tr_b16 a[140:141], v119 offset:12288\n" \
  "ds_read_b64_tr_b16 a[142:143], v119 offset:12800\n" \
  "ds_read_b64_tr_b16 a[144:145], v119 offset:16384\n" \
  "ds_read_b64_tr_b16 a[146:147], v119 offset:16896\n" \
  "ds_read_b64_tr_b16 a[148:149], v119 offset:20480\n" \
  "ds_read_b64_tr_b16 a[150:151], v119 offset:20992\n" \
  "ds_read_b64_tr_b16 a[152:153], v119 offset:24576\n" \
  "ds_read_b64_tr_b16 a[154:155], v119 offset:25088\n" \
  "ds_read_b64_tr_b16 a[156:157], v119 offset:28672\n" \
  "ds_read_b64_tr_b16 a[158:159], v119 offset:29184\n" \
  "s_waitcnt vmcnt(24) lgkmcnt(0)\n" \
  "s_barrier\n" \
  "s_lshl_b32 s92, %7, 19\n" \
  "s_add_u32 s92, s92, 2097152\n" \
  "s_add_u32 m0, s86, 65536\n" \
  "s_nop 0\n" \
  "buffer_load_dwordx4 v8, s[68:71], s92 offen lds\n" \
  "s_add_u32 m0, s86, 66560\n" \
  "s_add_u32 s92, s92, 65536\n" \
  "buffer_load_dwordx4 v9, s[68:71], s92 offen lds\n" \
  "s_add_u32 m0, s86, 67584\n" \
  "s_add_u32 s92, s92, 65536\n" \
  "buffer_load_dwordx4 v8, s[68:71], s92 offen lds\n" \
  "s_add_u32 m0, s86, 68608\n" \
  "s_add_u32 s92, s92, 65536\n" \
  "buffer_load_dwordx4 v9, s[68:71], s92 offen lds\n" \
  "s_add_u32 m0, s86, 69632\n" \
  "s_add_u32 s92, s92, 65536\n" \
  "buffer_load_dwordx4 v8, s[68:71], s92 offen lds\n" \
  "s_add_u32 m0, s86, 70656\n" \
  "s_add_u32 s92, s92, 65536\n" \
  "buffer_load_dwordx4 v9, s[68:71], s92 offen lds\n" \
  "s_add_u32 m0, s86, 71680\n" \
  "s_add_u32 s92, s92, 65536\n" \
  "buffer_load_dwordx4 v8, s[68:71], s92 offen lds\n" \
  "s_add_u32 m0, s86, 72704\n" \
  "s_add_u32 s92, s92, 65536\n" \
  "buffer_load_dwordx4 v9, s[68:71], s92 offen lds\n" \
  "ds_read_b64_tr_b16 a[160:161], v119 offset:32768\n" \
  "ds_read_b64_tr_b16 a[162:163], v119 offset:33280\n" \
  "ds_read_b64_tr_b16 a[164:165], v119 offset:36864\n" \
  "ds_read_b64_tr_b16 a[166:167], v119 offset:37376\n" \
  "ds_read_b64_tr_b16 a[168:169], v119 offset:40960\n" \
  "ds_read_b64_tr_b16 a[170:171], v119 offset:41472\n" \
  "ds_read_b64_tr_b16 a[172:173], v119 offset:45056\n" \
  "ds_read_b64_tr_b16 a[174:175], v119 offset:45568\n" \
  "ds_read_b64_tr_b16 a[176:177], v119 offset:49152\n" \
  "ds_read_b64_tr_b16 a[178:179], v119 offset:49664\n" \
  "ds_read_b64_tr_b16 a[180:181], v119 offset:53248\n" \
  "ds_read_b64_tr_b16 a[182:183], v119 offset:53760\n" \
  "ds_read_b64_tr_b16 a[184:185], v119 offset:57344\n" \
  "ds_read_b64_tr_b16 a[186:187], v119 offset:57856\n" \
  "ds_read_b64_tr_b16 a[188:189], v119 offset:61440\n" \
  "ds_read_b64_tr_b16 a[190:191], v119 offset:61952\n" \
  "s_waitcnt vmcnt(24) lgkmcnt(0)\n" \
  "s_barrier\n" \
  "s_lshl_b32 s92, %7, 19\n" \
  "s_add_u32 s92, s92, 4194304\n" \
  "s_add_u32 m0, s86, 98304\n" \
  "s_nop 0\n" \
  "buffer_load_dwordx4 v8, s[68:71], s92 offen lds\n" \
  "s_add_u32 m0, s86, 99328\n" \
  "s_add_u32 s92, s92, 65536\n" \
  "buffer_load_dwordx4 v9, s[68:71], s92 offen lds\n" \
  "s_add_u32 m0, s86, 100352\n" \
  "s_add_u32 s92, s92, 65536\n" \
  "buffer_load_dwordx4 v8, s[68:71], s92 offen lds\n" \
  "s_add_u32 m0, s86, 101376\n" \
  "s_add_u32 s92, s92, 65536\n" \
  "buffer_load_dwordx4 v9, s[68:71], s92 offen lds\n" \
  "s_add_u32 m0, s86, 102400\n" \
  "s_add_u32 s92, s92, 65536\n" \
  "buffer_load_dwordx4 v8, s[68:71], s92 offen lds\n" \
  "s_add_u32 m0, s86, 103424\n" \
  "s_add_u32 s92, s92, 65536\n" \
  "buffer_load_dwordx4 v9, s[68:71], s92 offen lds\n" \
  "s_add_u32 m0, s86, 104448\n" \
  "s_add_u32 s92, s92, 65536\n" \
  "buffer_load_dwordx4 v8, s[68:71], s92 offen lds\n" \
  "s_add_u32 m0, s86, 105472\n" \
  "s_add_u32 s92, s92, 65536\n" \
  "buffer_load_dwordx4 v9, s[68:71], s92 offen lds\n" \
  "ds_read_b64_tr_b16 a[192:193], v120 offset:0\n" \
  "ds_read_b64_tr_b16 a[194:195], v120 offset:512\n" \
  "ds_read_b64_tr_b16 a[196:197], v120 offset:4096\n" \
  "ds_read_b64_tr_b16 a[198:199], v120 offset:4608\n" \
  "ds_read_b64_tr_b16 a[200:201], v120 offset:8192\n" \
  "ds_read_b64_tr_b16 a[202:203], v120 offset:8704\n" \
  "ds_read_b64_tr_b16 a[204:205], v120 offset:12288\n" \
  "ds_read_b64_tr_b16 a[206:207], v120 offset:12800\n" \
  "ds_read_b64_tr_b16 a[208:209], v120 offset:16384\n" \
  "ds_read_b64_tr_b16 a[210:211], v120 offset:16896\n" \
  "ds_read_b64_tr_b16 a[212:213], v120 offset:20480\n" \
  "ds_read_b64_tr_b16 a[214:215], v120 offset:20992\n" \
  "ds_read_b64_tr_b16 a[216:217], v120 offset:24576\n" \
  "ds_read_b64_tr_b16 a[218:219], v120 offset:25088\n" \
  "ds_read_b64_tr_b16 a[220:221], v120 offset:28672\n" \
  "ds_read_b64_tr_b16 a[222:223], v120 offset:29184\n" \
  "s_waitcnt vmcnt(24) lgkmcnt(0)\n" \
  "s_barrier\n" \
  "s_lshl_b32 s92, %7, 19\n" \
  "s_add_u32 s92, s92, 6291456\n" \
  "s_add_u32 m0, s86, 131072\n" \
  "s_nop 0\n" \
  "buffer_load_dwordx4 v8, s[68:71], s92 offen lds\n" \
  "s_add_u32 m0, s86, 132096\n" \
  "s_add_u32 s92, s92, 65536\n" \
  "buffer_load_dwordx4 v9, s[68:71], s92 offen lds\n" \
  "s_add_u32 m0, s86, 133120\n" \
  "s_add_u32 s92, s92, 65536\n" \
  "buffer_load_dwordx4 v8, s[68:71], s92 offen lds\n" \
  "s_add_u32 m0, s86, 134144\n" \
  "s_add_u32 s92, s92, 65536\n" \
  "buffer_load_dwordx4 v9, s[68:71], s92 offen lds\n" \
  "s_add_u32 m0, s86, 135168\n" \
  "s_add_u32 s92, s92, 65536\n" \
  "buffer_load_dwordx4 v8, s[68:71], s92 offen lds\n" \
  "s_add_u32 m0, s86, 136192\n" \
  "s_add_u32 s92, s92, 65536\n" \
  "buffer_load_dwordx4 v9, s[68:71], s92 offen lds\n" \
  "s_add_u32 m0, s86, 137216\n" \
  "s_add_u32 s92, s92, 65536\n" \
  "buffer_load_dwordx4 v8, s[68:71], s92 offen lds\n" \
  "s_add_u32 m0, s86, 138240\n" \
  "s_add_u32 s92, s92, 65536\n" \
  "buffer_load_dwordx4 v9, s[68:71], s92 offen lds\n" \
  "ds_read_b64_tr_b16 a[224:225], v118 offset:0\n" \
  "ds_read_b64_tr_b16 a[226:227], v118 offset:512\n" \
  "ds_read_b64_tr_b16 a[228:229], v118 offset:4096\n" \
  "ds_read_b64_tr_b16 a[230:231], v118 offset:4608\n" \
  "ds_read_b64_tr_b16 a[232:233], v118 offset:8192\n" \
  "ds_read_b64_tr_b16 a[234:235], v118 offset:8704\n" \
  "ds_read_b64_tr_b16 a[236:237], v118 offset:12288\n" \
  "ds_read_b64_tr_b16 a[238:239], v118 offset:12800\n" \
  "ds_read_b64_tr_b16 a[240:241], v118 offset:16384\n" \
  "ds_read_b64_tr_b16 a[242:243], v118 offset:16896\n" \
  "ds_read_b64_tr_b16 a[244:245], v118 offset:20480\n" \
  "ds_read_b64_tr_b16 a[246:247], v118 offset:20992\n" \
  "ds_read_b64_tr_b16 a[248:249], v118 offset:24576\n" \
  "ds_read_b64_tr_b16 a[250:251], v118 offset:25088\n" \
  "ds_read_b64_tr_b16 a[252:253], v118 offset:28672\n" \
  "ds_read_b64_tr_b16 a[254:255], v118 offset:29184\n" \
  "s_waitcnt vmcnt(24) lgkmcnt(0)\n" \
  "s_barrier\n" \
  "s_add_u32 m0, s91, 0\n" \
  "s_nop 0\n" \
  "buffer_load_dwordx4 v7, s[72:75], s87 offen lds\n" \
  "s_add_u32 s87, s87, 128\n" \
  "s_add_u32 m0, s91, 4096\n" \
  "s_nop 0\n" \
  "buffer_load_dwordx4 v7, s[72:75], s87 offen lds\n" \
  "s_add_u32 s87, s87, 128\n" \
  "s_add_u32 m0, s91, 8192\n" \
  "s_nop 0\n" \
  "buffer_load_dwordx4 v7, s[72:75], s87 offen lds\n" \
  "s_add_u32 s87, s87, 128\n" \
  "s_add_u32 m0, s91, 12288\n" \
  "s_nop 0\n" \
  "buffer_load_dwordx4 v7, s[72:75], s87 offen lds\n" \
  "s_add_u32 s87, s87, 128\n" \
  "s_add_u32 m0, s91, 16384\n" \
  "s_nop 0\n" \
  "buffer_load_dwordx4 v7, s[72:75], s87 offen lds\n" \
  "s_add_u32 s87, s87, 128\n" \
  "s_add_u32 m0, s91, 20480\n" \
  "s_nop 0\n" \
  "buffer_load_dwordx4 v7, s[72:75], s87 offen lds\n" \
  "s_add_u32 s87, s87, 128\n" \
  "s_add_u32 m0, s91, 24576\n" \
  "s_nop 0\n" \
  "buffer_load_dwordx4 v7, s[72:75], s87 offen lds\n" \
  "s_add_u32 s87, s87, 128\n" \
  "s_add_u32 m0, s91, 28672\n" \
  "s_nop 0\n" \
  "buffer_load_dwordx4 v7, s[72:75], s87 offen lds\n" \
  "s_add_u32 s87, s87, 128\n" \
  "ds_read_b64_tr_b16 v[128:129], v118 offset:32768\n" \
  "ds_read_b64_tr_b16 v[130:131], v118 offset:33280\n" \
  "ds_read_b64_tr_b16 v[132:133], v118 offset:36864\n" \
  "ds_read_b64_tr_b16 v[134:135], v118 offset:37376\n" \
  "ds_read_b64_tr_b16 v[136:137], v118 offset:40960\n" \
  "ds_read_b64_tr_b16 v[138:139], v118 offset:41472\n" \
  "ds_read_b64_tr_b16 v[140:141], v118 offset:45056\n" \
  "ds_read_b64_tr_b16 v[142:143], v118 offset:45568\n" \
  "ds_read_b64_tr_b16 v[144:145], v118 offset:49152\n" \
  "ds_read_b64_tr_b16 v[146:147], v118 offset:49664\n" \
  "ds_read_b64_tr_b16 v[148:149], v118 offset:53248\n" \
  "ds_read_b64_tr_b16 v[150:151], v118 offset:53760\n" \
  "ds_read_b64_tr_b16 v[152:153], v118 offset:57344\n" \
  "ds_read_b64_tr_b16 v[154:155], v118 offset:57856\n" \
  "ds_read_b64_tr_b16 v[156:157], v118 offset:61440\n" \
  "ds_read_b64_tr_b16 v[158:159], v118 offset:61952\n" \
  "s_waitcnt vmcnt(24) lgkmcnt(0)\n" \
  "s_barrier\n" \
  "s_add_u32 m0, s91, 32768\n" \
  "s_nop 0\n" \
  "buffer_load_dwordx4 v7, s[72:75], s87 offen lds\n" \
  "s_add_u32 s87, s87, 128\n" \
  "s_add_u32 m0, s91, 36864\n" \
  "s_nop 0\n" \
  "buffer_load_dwordx4 v7, s[72:75], s87 offen lds\n" \
  "s_add_u32 s87, s87, 128\n" \
  "s_add_u32 m0, s91, 40960\n" \
  "s_nop 0\n" \
  "buffer_load_dwordx4 v7, s[72:75], s87 offen lds\n" \
  "s_add_u32 s87, s87, 128\n" \
  "s_add_u32 m0, s91, 45056\n" \
  "s_nop 0\n" \
  "buffer_load_dwordx4 v7, s[72:75], s87 offen lds\n" \
  "s_add_u32 s87, s87, 128\n" \
  "s_add_u32 m0, s91, 49152\n" \
  "s_nop 0\n" \
  "buffer_load_dwordx4 v7, s[72:75], s87 offen lds\n" \
  "s_add_u32 s87, s87, 128\n" \
  "s_add_u32 m0, s91, 53248\n" \
  "s_nop 0\n" \
  "buffer_load_dwordx4 v7, s[72:75], s87 offen lds\n" \
  "s_add_u32 s87, s87, 128\n" \
  "s_add_u32 m0, s91, 57344\n" \
  "s_nop 0\n" \
  "buffer_load_dwordx4 v7, s[72:75], s87 offen lds\n" \
  "s_add_u32 s87, s87, 128\n" \
  "s_add_u32 m0, s91, 61440\n" \
  "s_nop 0\n" \
  "buffer_load_dwordx4 v7, s[72:75], s87 offen lds\n" \
  "s_add_u32 s87, s87, 63616\n" \
  "s_min_u32 s87, s87, s96\n" \
  "ds_read_b64_tr_b16 v[160:161], v119 offset:0\n" \
  "ds_read_b64_tr_b16 v[162:163], v119 offset:512\n" \
  "ds_read_b64_tr_b16 v[164:165], v119 offset:4096\n" \
  "ds_read_b64_tr_b16 v[166:167], v119 offset:4608\n" \
  "ds_read_b64_tr_b16 v[168:169], v119 offset:8192\n" \
  "ds_read_b64_tr_b16 v[170:171], v119 offset:8704\n" \
  "ds_read_b64_tr_b16 v[172:173], v119 offset:12288\n" \
  "ds_read_b64_tr_b16 v[174:175], v119 offset:12800\n" \
  "ds_read_b64_tr_b16 v[176:177], v119 offset:16384\n" \
  "ds_read_b64_tr_b16 v[178:179], v119 offset:16896\n" \
  "ds_read_b64_tr_b16 v[180:181], v119 offset:20480\n" \
  "ds_read_b64_tr_b16 v[182:183], v119 offset:20992\n" \
  "ds_read_b64_tr_b16 v[184:185], v119 offset:24576\n" \
  "ds_read_b64_tr_b16 v[186:187], v119 offset:25088\n" \
  "ds_read_b64_tr_b16 v[188:189], v119 offset:28672\n" \
  "ds_read_b64_tr_b16 v[190:191], v119 offset:29184\n" \
  "s_waitcnt vmcnt(24) lgkmcnt(0)\n" \
  "s_barrier\n" \
  "s_add_u32 m0, s91, 65536\n" \
  "s_nop 0\n" \
  "buffer_load_dwordx4 v7, s[72:75], s87 offen lds\n" \
  "s_add_u32 s87, s87, 128\n" \
  "s_add_u32 m0, s91, 69632\n" \
  "s_nop 0\n" \
  "buffer_load_dwordx4 v7, s[72:75], s87 offen lds\n" \
  "s_add_u32 s87, s87, 128\n" \
  "s_add_u32 m0, s91, 73728\n" \
  "s_nop 0\n" \
  "buffer_load_dwordx4 v7, s[72:75], s87 offen lds\n" \
  "s_add_u32 s87, s87, 128\n" \
  "s_add_u32 m0, s91, 77824\n" \
  "s_nop 0\n" \
  "buffer_load_dwordx4 v7, s[72:75], s87 offen lds\n" \
  "s_add_u32 s87, s87, 128\n" \
  "s_add_u32 m0, s91, 81920\n" \
  "s_nop 0\n" \
  "buffer_load_dwordx4 v7, s[72:75], s87 offen lds\n" \
  "s_add_u32 s87, s87, 128\n" \
  "s_add_u32 m0, s91, 86016\n" \
  "s_nop 0\n" \
  "buffer_load_dwordx4 v7, s[72:75], s87 offen lds\n" \
  "s_add_u32 s87, s87, 128\n" \
  "s_add_u32 m0, s91, 90112\n" \
  "s_nop 0\n" \
  "buffer_load_dwordx4 v7, s[72:75], s87 offen lds\n" \
  "s_add_u32 s87, s87, 128\n" \
  "s_add_u32 m0, s91, 94208\n" \
  "s_nop 0\n" \
  "buffer_load_dwordx4 v7, s[72:75], s87 offen lds\n" \
  "s_add_u32 s87, s87, 128\n" \
  "ds_read_b64_tr_b16 v[192:193], v119 offset:32768\n" \
  "ds_read_b64_tr_b16 v[194:195], v119 offset:33280\n" \
  "ds_read_b64_tr_b16 v[196:197], v119 offset:36864\n" \
  "ds_read_b64_tr_b16 v[198:199], v119 offset:37376\n" \
  "ds_read_b64_tr_b16 v[200:201], v119 offset:40960\n" \
  "ds_read_b64_tr_b16 v[202:203], v119 offset:41472\n" \
  "ds_read_b64_tr_b16 v[204:205], v119 offset:45056\n" \
  "ds_read_b64_tr_b16 v[206:207], v119 offset:45568\n" \
  "ds_read_b64_tr_b16 v[208:209], v119 offset:49152\n" \
  "ds_read_b64_tr_b16 v[210:211], v119 offset:49664\n" \
  "ds_read_b64_tr_b16 v[212:213], v119 offset:53248\n" \
  "ds_read_b64_tr_b16 v[214:215], v119 offset:53760\n" \
  "ds_read_b64_tr_b16 v[216:217], v119 offset:57344\n" \
  "ds_read_b64_tr_b16 v[218:219], v119 offset:57856\n" \
  "ds_read_b64_tr_b16 v[220:221], v119 offset:61440\n" \
  "ds_read_b64_tr_b16 v[222:223], v119 offset:61952\n" \
  "s_waitcnt vmcnt(24) lgkmcnt(0)\n" \
  "s_barrier\n" \
  "ds_read_b64_tr_b16 v[224:225], v120 offset:0\n" \
  "ds_read_b64_tr_b16 v[226:227], v120 offset:512\n" \
  "ds_read_b64_tr_b16 v[228:229], v120 offset:4096\n" \
  "ds_read_b64_tr_b16 v[230:231], v120 offset:4608\n" \
  "ds_read_b64_tr_b16 v[232:233], v120 offset:8192\n" \
  "ds_read_b64_tr_b16 v[234:235], v120 offset:8704\n" \
  "ds_read_b64_tr_b16 v[236:237], v120 offset:12288\n" \
  "ds_read_b64_tr_b16 v[238:239], v120 offset:12800\n" \
  "ds_read_b64_tr_b16 v[240:241], v120 offset:16384\n" \
  "ds_read_b64_tr_b16 v[242:243], v120 offset:16896\n" \
  "ds_read_b64_tr_b16 v[244:245], v120 offset:20480\n" \
  "ds_read_b64_tr_b16 v[246:247], v120 offset:20992\n" \
  "ds_read_b64_tr_b16 v[248:249], v120 offset:24576\n" \
  "ds_read_b64_tr_b16 v[250:251], v120 offset:25088\n" \
  "ds_read_b64_tr_b16 v[252:253], v120 offset:28672\n" \
  "ds_read_b64_tr_b16 v[254:255], v120 offset:29184\n" \
  "s_waitcnt vmcnt(8) lgkmcnt(0)\n" \
  "s_barrier\n" \
  "ds_read_b128 v[68:71], v10 offset:0\n" \
  "ds_read_b128 v[72:75], v10 offset:2048\n" \
  "ds_read_b128 v[76:79], v11 offset:0\n" \
  "ds_read_b128 v[80:83], v11 offset:2048\n" \
  "ds_read_b128 v[84:87], v10 offset:4096\n" \
  "ds_read_b128 v[88:91], v10 offset:6144\n" \
  "s_mov_b32 s85, 0\n" \
  ".p2align 6\n" \
  "LG_%=:\n" \
  "s_cmp_eq_u32 s85, 64\n" \
  "s_cbranch_scc1 LX0_%=\n" \
  "s_waitcnt vmcnt(8)\n" \
  "s_barrier\n" \
  "s_waitcnt lgkmcnt(4)\n" \
  "v_mfma_f32_16x16x32_bf16 v[20:23], v[128:131], v[68:71], 0\n" \
  "ds_read_b128 v[92:95], v11 offset:4096\n" \
  "s_sub_u32 s89, s85, 1\n" \
  "s_and_b32 s89, s89, 63\n" \
  "v_mfma_f32_16x16x32_bf16 v[28:31], a[0:3], v[68:71], 0\n" \
  "ds_read_b128 v[96:99], v11 offset:6144\n" \
  "s_lshr_b32 s84, s89, 2\n" \
  "s_xnor_b32 s84, s84, %9\n" \
  "v_mfma_f32_16x16x32_bf16 v[36:39], v[68:71], a[128:131], 0\n" \
  "s_and_b32 s84, s84, 1\n" \
  "s_lshl_b32 s84, s84, 13\n" \
  "v_mfma_f32_16x16x32_bf16 v[24:27], v[128:131], v[72:75], 0\n" \
  "s_lshl_b32 s88, s89, 19\n" \
  "s_or_b32 s88, s88, s84\n" \
  "v_mfma_f32_16x16x32_bf16 v[32:35], a[0:3], v[72:75], 0\n" \
  "s_add_u32 s89, s88, " STR(QKV_SZ32) "\n" \
  "s_add_u32 s90, s89, " STR(QKV_SZ32) "\n" \
  "v_mfma_f32_16x16x32_bf16 v[40:43], v[72:75], a[128:131], 0\n" \
  "s_add_u32 s85, s85, 1\n" \
  "s_waitcnt lgkmcnt(4)\n" \
  "v_mfma_f32_16x16x32_bf16 v[20:23], v[132:135], v[76:79], v[20:23]\n" \
  "ds_read_b128 v[68:71], v10 offset:8192\n" \
  "v_cvt_pk_bf16_f32 v116, v44, v45\n" \
  "v_mfma_f32_16x16x32_bf16 v[28:31], a[4:7], v[76:79], v[28:31]\n" \
  "ds_read_b128 v[72:75], v10 offset:10240\n" \
  "v_cvt_pk_bf16_f32 v117, v46, v47\n" \
  "v_mfma_f32_16x16x32_bf16 v[36:39], v[76:79], a[132:135], v[36:39]\n" \
  "v_cvt_pk_bf16_f32 v118, v48, v49\n" \
  "v_mfma_f32_16x16x32_bf16 v[24:27], v[132:135], v[80:83], v[24:27]\n" \
  "v_cvt_pk_bf16_f32 v119, v50, v51\n" \
  "v_mfma_f32_16x16x32_bf16 v[32:35], a[4:7], v[80:83], v[32:35]\n" \
  "v_cvt_pk_bf16_f32 v120, v52, v53\n" \
  "v_mfma_f32_16x16x32_bf16 v[40:43], v[80:83], a[132:135], v[40:43]\n" \
  "v_cvt_pk_bf16_f32 v121, v54, v55\n" \
  "s_waitcnt lgkmcnt(4)\n" \
  "v_mfma_f32_16x16x32_bf16 v[20:23], v[136:139], v[84:87], v[20:23]\n" \
  "ds_read_b128 v[76:79], v11 offset:8192\n" \
  "v_cvt_pk_bf16_f32 v122, v56, v57\n" \
  "v_mfma_f32_16x16x32_bf16 v[28:31], a[8:11], v[84:87], v[28:31]\n" \
  "ds_read_b128 v[80:83], v11 offset:10240\n" \
  "v_cvt_pk_bf16_f32 v123, v58, v59\n" \
  "v_mfma_f32_16x16x32_bf16 v[36:39], v[84:87], a[136:139], v[36:39]\n" \
  "v_cvt_pk_bf16_f32 v124, v60, v61\n" \
  "v_mfma_f32_16x16x32_bf16 v[24:27], v[136:139], v[88:91], v[24:27]\n" \
  "v_cvt_pk_bf16_f32 v125, v62, v63\n" \
  "v_mfma_f32_16x16x32_bf16 v[32:35], a[8:11], v[88:91], v[32:35]\n" \
  "v_cvt_pk_bf16_f32 v126, v64, v65\n" \
  "v_mfma_f32_16x16x32_bf16 v[40:43], v[88:91], a[136:139], v[40:43]\n" \
  "v_cvt_pk_bf16_f32 v127, v66, v67\n" \
  "s_waitcnt lgkmcnt(4)\n" \
  "v_mfma_f32_16x16x32_bf16 v[20:23], v[140:143], v[92:95], v[20:23]\n" \
  "ds_read_b128 v[84:87], v10 offset:12288\n" \
  "v_permlane16_swap_b32 v116, v118\n" \
  "v_mfma_f32_16x16x32_bf16 v[28:31], a[12:15], v[92:95], v[28:31]\n" \
  "ds_read_b128 v[88:91], v10 offset:14336\n" \
  "v_permlane16_swap_b32 v117, v119\n" \
  "v_mfma_f32_16x16x32_bf16 v[36:39], v[92:95], a[140:143], v[36:39]\n" \
  "v_permlane16_swap_b32 v120, v122\n" \
  "v_mfma_f32_16x16x32_bf16 v[24:27], v[140:143], v[96:99], v[24:27]\n" \
  "v_permlane16_swap_b32 v121, v123\n" \
  "v_mfma_f32_16x16x32_bf16 v[32:35], a[12:15], v[96:99], v[32:35]\n" \
  "v_mfma_f32_16x16x32_bf16 v[40:43], v[96:99], a[140:143], v[40:43]\n" \
  "s_waitcnt lgkmcnt(4)\n" \
  "v_mfma_f32_16x16x32_bf16 v[20:23], v[144:147], v[68:71], v[20:23]\n" \
  "ds_read_b128 v[92:95], v11 offset:12288\n" \
  "buffer_store_dwordx4 v[116:119], v14, s[76:79], s88 offen sc1\n" \
  "v_mfma_f32_16x16x32_bf16 v[28:31], a[16:19], v[68:71], v[28:31]\n" \
  "ds_read_b128 v[96:99], v11 offset:14336\n" \
  "buffer_store_dwordx4 v[120:123], v18, s[76:79], s89 offen sc1\n" \
  "v_mfma_f32_16x16x32_bf16 v[36:39], v[68:71], a[144:147], v[36:39]\n" \
  "buffer_store_dwordx4 v[124:127], v15, s[76:79], s90 offen sc1\n" \
  "v_mfma_f32_16x16x32_bf16 v[24:27], v[144:147], v[72:75], v[24:27]\n" \
  "v_mfma_f32_16x16x32_bf16 v[32:35], a[16:19], v[72:75], v[32:35]\n" \
  "v_mfma_f32_16x16x32_bf16 v[40:43], v[72:75], a[144:147], v[40:43]\n" \
  "s_waitcnt lgkmcnt(4)\n" \
  "v_mfma_f32_16x16x32_bf16 v[20:23], v[148:151], v[76:79], v[20:23]\n" \
  "ds_read_b128 v[68:71], v10 offset:16384\n" \
  "v_mfma_f32_16x16x32_bf16 v[28:31], a[20:23], v[76:79], v[28:31]\n" \
  "ds_read_b128 v[72:75], v10 offset:18432\n" \
  "v_mfma_f32_16x16x32_bf16 v[36:39], v[76:79], a[148:151], v[36:39]\n" \
  "v_mfma_f32_16x16x32_bf16 v[24:27], v[148:151], v[80:83], v[24:27]\n" \
  "s_add_u32 m0, s91, 98304\n" \
  "s_nop 0\n" \
  "buffer_load_dwordx4 v7, s[72:75], s87 offen lds\n" \
  "s_add_u32 s87, s87, 128\n" \
  "v_mfma_f32_16x16x32_bf16 v[32:35], a[20:23], v[80:83], v[32:35]\n" \
  "v_mfma_f32_16x16x32_bf16 v[40:43], v[80:83], a[148:151], v[40:43]\n" \
  "s_waitcnt lgkmcnt(4)\n" \
  "v_mfma_f32_16x16x32_bf16 v[20:23], v[152:155], v[84:87], v[20:23]\n" \
  "ds_read_b128 v[76:79], v11 offset:16384\n" \
  "v_mfma_f32_16x16x32_bf16 v[28:31], a[24:27], v[84:87], v[28:31]\n" \
  "ds_read_b128 v[80:83], v11 offset:18432\n" \
  "v_mfma_f32_16x16x32_bf16 v[36:39], v[84:87], a[152:155], v[36:39]\n" \
  "v_mfma_f32_16x16x32_bf16 v[24:27], v[152:155], v[88:91], v[24:27]\n" \
  "s_add_u32 m0, s91, 102400\n" \
  "s_nop 0\n" \
  "buffer_load_dwordx4 v7, s[72:75], s87 offen lds\n" \
  "s_add_u32 s87, s87, 128\n" \
  "v_mfma_f32_16x16x32_bf16 v[32:35], a[24:27], v[88:91], v[32:35]\n" \
  "v_mfma_f32_16x16x32_bf16 v[40:43], v[88:91], a[152:155], v[40:43]\n" \
  "s_waitcnt lgkmcnt(4)\n" \
  "v_mfma_f32_16x16x32_bf16 v[20:23], v[156:159], v[92:95], v[20:23]\n" \
  "ds_read_b128 v[84:87], v10 offset:20480\n" \
  "v_mfma_f32_16x16x32_bf16 v[28:31], a[28:31], v[92:95], v[28:31]\n" \
  "ds_read_b128 v[88:91], v10 offset:22528\n" \
  "v_mfma_f32_16x16x32_bf16 v[36:39], v[92:95], a[156:159], v[36:39]\n" \
  "v_mfma_f32_16x16x32_bf16 v[24:27], v[156:159], v[96:99], v[24:27]\n" \
  "s_add_u32 m0, s91, 106496\n" \
  "s_nop 0\n" \
  "buffer_load_dwordx4 v7, s[72:75], s87 offen lds\n" \
  "s_add_u32 s87, s87, 128\n" \
  "v_mfma_f32_16x16x32_bf16 v[32:35], a[28:31], v[96:99], v[32:35]\n" \
  "v_mfma_f32_16x16x32_bf16 v[40:43], v[96:99], a[156:159], v[40:43]\n" \
  "s_waitcnt lgkmcnt(4)\n" \
  "v_mfma_f32_16x16x32_bf16 v[20:23], v[160:163], v[68:71], v[20:23]\n" \
  "ds_read_b128 v[92:95], v11 offset:20480\n" \
  "v_mfma_f32_16x16x32_bf16 v[28:31], a[32:35], v[68:71], v[28:31]\n" \
  "ds_read_b128 v[96:99], v11 offset:22528\n" \
  "v_mfma_f32_16x16x32_bf16 v[36:39], v[68:71], a[160:163], v[36:39]\n" \
  "v_mfma_f32_16x16x32_bf16 v[24:27], v[160:163], v[72:75], v[24:27]\n" \
  "s_add_u32 m0, s91, 110592\n" \
  "s_nop 0\n" \
  "buffer_load_dwordx4 v7, s[72:75], s87 offen lds\n" \
  "s_add_u32 s87, s87, 128\n" \
  "v_mfma_f32_16x16x32_bf16 v[32:35], a[32:35], v[72:75], v[32:35]\n" \
  "v_mfma_f32_16x16x32_bf16 v[40:43], v[72:75], a[160:163], v[40:43]\n" \
  "s_waitcnt lgkmcnt(4)\n" \
  "v_mfma_f32_16x16x32_bf16 v[20:23], v[164:167], v[76:79], v[20:23]\n" \
  "ds_read_b128 v[68:71], v10 offset:24576\n" \
  "v_mfma_f32_16x16x32_bf16 v[28:31], a[36:39], v[76:79], v[28:31]\n" \
  "ds_read_b128 v[72:75], v10 offset:26624\n" \
  "v_mfma_f32_16x16x32_bf16 v[36:39], v[76:79], a[164:167], v[36:39]\n" \
  "v_mfma_f32_16x16x32_bf16 v[24:27], v[164:167], v[80:83], v[24:27]\n" \
  "s_add_u32 m0, s91, 114688\n" \
  "s_nop 0\n" \
  "buffer_load_dwordx4 v7, s[72:75], s87 offen lds\n" \
  "s_add_u32 s87, s87, 128\n" \
  "v_mfma_f32_16x16x32_bf16 v[32:35], a[36:39], v[80:83], v[32:35]\n" \
  "v_mfma_f32_16x16x32_bf16 v[40:43], v[80:83], a[164:167], v[40:43]\n" \
  "s_waitcnt lgkmcnt(4)\n" \
  "v_mfma_f32_16x16x32_bf16 v[20:23], v[168:171], v[84:87], v[20:23]\n" \
  "ds_read_b128 v[76:79], v11 offset:24576\n" \
  "v_mfma_f32_16x16x32_bf16 v[28:31], a[40:43], v[84:87], v[28:31]\n" \
  "ds_read_b128 v[80:83], v11 offset:26624\n" \
  "v_mfma_f32_16x16x32_bf16 v[36:39], v[84:87], a[168:171], v[36:39]\n" \
  "v_mfma_f32_16x16x32_bf16 v[24:27], v[168:171], v[88:91], v[24:27]\n" \
  "s_add_u32 m0, s91, 118784\n" \
  "s_nop 0\n" \
  "buffer_load_dwordx4 v7, s[72:75], s87 offen lds\n" \
  "s_add_u32 s87, s87, 128\n" \
  "v_mfma_f32_16x16x32_bf16 v[32:35], a[40:43], v[88:91], v[32:35]\n" \
  "v_mfma_f32_16x16x32_bf16 v[40:43], v[88:91], a[168:171], v[40:43]\n" \
  "s_waitcnt lgkmcnt(4)\n" \
  "v_mfma_f32_16x16x32_bf16 v[20:23], v[172:175], v[92:95], v[20:23]\n" \
  "ds_read_b128 v[84:87], v10 offset:28672\n" \
  "v_mfma_f32_16x16x32_bf16 v[28:31], a[44:47], v[92:95], v[28:31]\n" \
  "ds_read_b128 v[88:91], v10 offset:30720\n" \
  "v_mfma_f32_16x16x32_bf16 v[36:39], v[92:95], a[172:175], v[36:39]\n" \
  "v_mfma_f32_16x16x32_bf16 v[24:27], v[172:175], v[96:99], v[24:27]\n" \
  "s_add_u32 m0, s91, 122880\n" \
  "s_nop 0\n" \
  "buffer_load_dwordx4 v7, s[72:75], s87 offen lds\n" \
  "s_add_u32 s87, s87, 128\n" \
  "v_mfma_f32_16x16x32_bf16 v[32:35], a[44:47], v[96:99], v[32:35]\n" \
  "v_mfma_f32_16x16x32_bf16 v[40:43], v[96:99], a[172:175], v[40:43]\n" \
  "s_waitcnt lgkmcnt(4)\n" \
  "v_mfma_f32_16x16x32_bf16 v[20:23], v[176:179], v[68:71], v[20:23]\n" \
  "ds_read_b128 v[92:95], v11 offset:28672\n" \
  "v_mfma_f32_16x16x32_bf16 v[28:31], a[48:51], v[68:71], v[28:31]\n" \
  "ds_read_b128 v[96:99], v11 offset:30720\n" \
  "v_mfma_f32_16x16x32_bf16 v[36:39], v[68:71], a[176:179], v[36:39]\n" \
  "v_mfma_f32_16x16x32_bf16 v[24:27], v[176:179], v[72:75], v[24:27]\n" \
  "s_add_u32 m0, s91, 126976\n" \
  "s_nop 0\n" \
  "buffer_load_dwordx4 v7, s[72:75], s87 offen lds\n" \
  "s_add_u32 s87, s87, 63616\n" \
  "s_min_u32 s87, s87, s96\n" \
  "v_mfma_f32_16x16x32_bf16 v[32:35], a[48:51], v[72:75], v[32:35]\n" \
  "v_mfma_f32_16x16x32_bf16 v[40:43], v[72:75], a[176:179], v[40:43]\n" \
  "s_waitcnt lgkmcnt(4)\n" \
  "v_mfma_f32_16x16x32_bf16 v[20:23], v[180:183], v[76:79], v[20:23]\n" \
  "ds_read_b128 v[68:71], v10 offset:32768\n" \
  "v_mfma_f32_16x16x32_bf16 v[28:31], a[52:55], v[76:79], v[28:31]\n" \
  "ds_read_b128 v[72:75], v10 offset:34816\n" \
  "v_mfma_f32_16x16x32_bf16 v[36:39], v[76:79], a[180:183], v[36:39]\n" \
  "v_mfma_f32_16x16x32_bf16 v[24:27], v[180:183], v[80:83], v[24:27]\n" \
  "v_mfma_f32_16x16x32_bf16 v[32:35], a[52:55], v[80:83], v[32:35]\n" \
  "v_mfma_f32_16x16x32_bf16 v[40:43], v[80:83], a[180:183], v[40:43]\n" \
  "s_waitcnt lgkmcnt(4)\n" \
  "v_mfma_f32_16x16x32_bf16 v[20:23], v[184:187], v[84:87], v[20:23]\n" \
  "ds_read_b128 v[76:79], v11 offset:32768\n" \
  "v_mfma_f32_16x16x32_bf16 v[28:31], a[56:59], v[84:87], v[28:31]\n" \
  "ds_read_b128 v[80:83], v11 offset:34816\n" \
  "v_mfma_f32_16x16x32_bf16 v[36:39], v[84:87], a[184:187], v[36:39]\n" \
  "v_mfma_f32_16x16x32_bf16 v[24:27], v[184:187], v[88:91], v[24:27]\n" \
  "v_mfma_f32_16x16x32_bf16 v[32:35], a[56:59], v[88:91], v[32:35]\n" \
  "v_mfma_f32_16x16x32_bf16 v[40:43], v[88:91], a[184:187], v[40:43]\n" \
  "s_waitcnt lgkmcnt(4)\n" \
  "v_mfma_f32_16x16x32_bf16 v[20:23], v[188:191], v[92:95], v[20:23]\n" \
  "ds_read_b128 v[84:87], v10 offset:36864\n" \
  "v_mfma_f32_16x16x32_bf16 v[28:31], a[60:63], v[92:95], v[28:31]\n" \
  "ds_read_b128 v[88:91], v10 offset:38912\n" \
  "v_mfma_f32_16x16x32_bf16 v[36:39], v[92:95], a[188:191], v[36:39]\n" \
  "v_mfma_f32_16x16x32_bf16 v[24:27], v[188:191], v[96:99], v[24:27]\n" \
  "v_mfma_f32_16x16x32_bf16 v[32:35], a[60:63], v[96:99], v[32:35]\n" \
  "v_mfma_f32_16x16x32_bf16 v[40:43], v[96:99], a[188:191], v[40:43]\n" \
  "s_waitcnt vmcnt(8)\n" \
  "s_barrier\n" \
  "s_waitcnt lgkmcnt(4)\n" \
  "v_mfma_f32_16x16x32_bf16 v[20:23], v[192:195], v[68:71], v[20:23]\n" \
  "ds_read_b128 v[92:95], v11 offset:36864\n" \
  "buffer_store_dword v17, v16, s[80:83], 0 offen sc1\n" \
  "v_mfma_f32_16x16x32_bf16 v[28:31], a[64:67], v[68:71], v[28:31]\n" \
  "ds_read_b128 v[96:99], v11 offset:38912\n" \
  "v_add_u32 v17, 1, v17\n" \
  "v_mfma_f32_16x16x32_bf16 v[36:39], v[68:71], a[192:195], v[36:39]\n" \
  "v_and_b32 v14, 0x7fffffff, v14\n" \
  "v_mfma_f32_16x16x32_bf16 v[24:27], v[192:195], v[72:75], v[24:27]\n" \
  "v_and_b32 v15, 0x7fffffff, v15\n" \
  "v_mfma_f32_16x16x32_bf16 v[32:35], a[64:67], v[72:75], v[32:35]\n" \
  "v_and_b32 v18, 0x7fffffff, v18\n" \
  "v_mfma_f32_16x16x32_bf16 v[40:43], v[72:75], a[192:195], v[40:43]\n" \
  "s_waitcnt lgkmcnt(4)\n" \
  "v_mfma_f32_16x16x32_bf16 v[20:23], v[196:199], v[76:79], v[20:23]\n" \
  "ds_read_b128 v[68:71], v10 offset:40960\n" \
  "v_mfma_f32_16x16x32_bf16 v[28:31], a[68:71], v[76:79], v[28:31]\n" \
  "ds_read_b128 v[72:75], v10 offset:43008\n" \
  "v_mfma_f32_16x16x32_bf16 v[36:39], v[76:79], a[196:199], v[36:39]\n" \
  "v_mfma_f32_16x16x32_bf16 v[24:27], v[196:199], v[80:83], v[24:27]\n" \
  "v_mfma_f32_16x16x32_bf16 v[32:35], a[68:71], v[80:83], v[32:35]\n" \
  "v_mfma_f32_16x16x32_bf16 v[40:43], v[80:83], a[196:199], v[40:43]\n" \
  "s_waitcnt lgkmcnt(4)\n" \
  "v_mfma_f32_16x16x32_bf16 v[20:23], v[200:203], v[84:87], v[20:23]\n" \
  "ds_read_b128 v[76:79], v11 offset:40960\n" \
  "v_mfma_f32_16x16x32_bf16 v[28:31], a[72:75], v[84:87], v[28:31]\n" \
  "ds_read_b128 v[80:83], v11 offset:43008\n" \
  "v_mfma_f32_16x16x32_bf16 v[36:39], v[84:87], a[200:203], v[36:39]\n" \
  "v_mfma_f32_16x16x32_bf16 v[24:27], v[200:203], v[88:91], v[24:27]\n" \
  "v_mfma_f32_16x16x32_bf16 v[32:35], a[72:75], v[88:91], v[32:35]\n" \
  "v_mfma_f32_16x16x32_bf16 v[40:43], v[88:91], a[200:203], v[40:43]\n" \
  "s_waitcnt lgkmcnt(4)\n" \
  "v_mfma_f32_16x16x32_bf16 v[20:23], v[204:207], v[92:95], v[20:23]\n" \
  "ds_read_b128 v[84:87], v10 offset:45056\n" \
  "v_mfma_f32_16x16x32_bf16 v[28:31], a[76:79], v[92:95], v[28:31]\n" \
  "ds_read_b128 v[88:91], v10 offset:47104\n" \
  "v_mfma_f32_16x16x32_bf16 v[36:39], v[92:95], a[204:207], v[36:39]\n" \
  "v_mfma_f32_16x16x32_bf16 v[24:27], v[204:207], v[96:99], v[24:27]\n" \
  "v_mfma_f32_16x16x32_bf16 v[32:35], a[76:79], v[96:99], v[32:35]\n" \
  "v_mfma_f32_16x16x32_bf16 v[40:43], v[96:99], a[204:207], v[40:43]\n" \
  "s_waitcnt lgkmcnt(4)\n" \
  "v_mfma_f32_16x16x32_bf16 v[20:23], v[208:211], v[68:71], v[20:23]\n" \
  "ds_read_b128 v[92:95], v11 offset:45056\n" \
  "v_mfma_f32_16x16x32_bf16 v[28:31], a[80:83], v[68:71], v[28:31]\n" \
  "ds_read_b128 v[96:99], v11 offset:47104\n" \
  "v_mfma_f32_16x16x32_bf16 v[36:39], v[68:71], a[208:211], v[36:39]\n" \
  "v_mfma_f32_16x16x32_bf16 v[24:27], v[208:211], v[72:75], v[24:27]\n" \
  "v_mfma_f32_16x16x32_bf16 v[32:35], a[80:83], v[72:75], v[32:35]\n" \
  "v_mfma_f32_16x16x32_bf16 v[40:43], v[72:75], a[208:211], v[40:43]\n" \
  "s_waitcnt lgkmcnt(4)\n" \
  "v_mfma_f32_16x16x32_bf16 v[20:23], v[212:215], v[76:79], v[20:23]\n" \
  "ds_read_b128 v[68:71], v10 offset:49152\n" \
  "v_mfma_f32_16x16x32_bf16 v[28:31], a[84:87], v[76:79], v[28:31]\n" \
  "ds_read_b128 v[72:75], v10 offset:51200\n" \
  "v_mfma_f32_16x16x32_bf16 v[36:39], v[76:79], a[212:215], v[36:39]\n" \
  "v_mfma_f32_16x16x32_bf16 v[24:27], v[212:215], v[80:83], v[24:27]\n" \
  "s_add_u32 m0, s91, 0\n" \
  "s_nop 0\n" \
  "buffer_load_dwordx4 v7, s[72:75], s87 offen lds\n" \
  "s_add_u32 s87, s87, 128\n" \
  "v_mfma_f32_16x16x32_bf16 v[32:35], a[84:87], v[80:83], v[32:35]\n" \
  "v_mfma_f32_16x16x32_bf16 v[40:43], v[80:83], a[212:215], v[40:43]\n" \
  "s_waitcnt lgkmcnt(4)\n" \
  "v_mfma_f32_16x16x32_bf16 v[20:23], v[216:219], v[84:87], v[20:23]\n" \
  "ds_read_b128 v[76:79], v11 offset:49152\n" \
  "v_mfma_f32_16x16x32_bf16 v[28:31], a[88:91], v[84:87], v[28:31]\n" \
  "ds_read_b128 v[80:83], v11 offset:51200\n" \
  "v_mfma_f32_16x16x32_bf16 v[36:39], v[84:87], a[216:219], v[36:39]\n" \
  "v_mfma_f32_16x16x32_bf16 v[24:27], v[216:219], v[88:91], v[24:27]\n" \
  "s_add_u32 m0, s91, 4096\n" \
  "s_nop 0\n" \
  "buffer_load_dwordx4 v7, s[72:75], s87 offen lds\n" \
  "s_add_u32 s87, s87, 128\n" \
  "v_mfma_f32_16x16x32_bf16 v[32:35], a[88:91], v[88:91], v[32:35]\n" \
  "v_mfma_f32_16x16x32_bf16 v[40:43], v[88:91], a[216:219], v[40:43]\n" \
  "s_waitcnt lgkmcnt(4)\n" \
  "v_mfma_f32_16x16x32_bf16 v[20:23], v[220:223], v[92:95], v[20:23]\n" \
  "ds_read_b128 v[84:87], v10 offset:53248\n" \
  "v_mfma_f32_16x16x32_bf16 v[28:31], a[92:95], v[92:95], v[28:31]\n" \
  "ds_read_b128 v[88:91], v10 offset:55296\n" \
  "v_mfma_f32_16x16x32_bf16 v[36:39], v[92:95], a[220:223], v[36:39]\n" \
  "v_mfma_f32_16x16x32_bf16 v[24:27], v[220:223], v[96:99], v[24:27]\n" \
  "s_add_u32 m0, s91, 8192\n" \
  "s_nop 0\n" \
  "buffer_load_dwordx4 v7, s[72:75], s87 offen lds\n" \
  "s_add_u32 s87, s87, 128\n" \
  "v_mfma_f32_16x16x32_bf16 v[32:35], a[92:95], v[96:99], v[32:35]\n" \
  "v_mfma_f32_16x16x32_bf16 v[40:43], v[96:99], a[220:223], v[40:43]\n" \
  "s_waitcnt lgkmcnt(4)\n" \
  "v_mfma_f32_16x16x32_bf16 v[20:23], v[224:227], v[68:71], v[20:23]\n" \
  "ds_read_b128 v[92:95], v11 offset:53248\n" \
  "v_mfma_f32_16x16x32_bf16 v[28:31], a[96:99], v[68:71], v[28:31]\n" \
  "ds_read_b128 v[96:99], v11 offset:55296\n" \
  "v_mfma_f32_16x16x32_bf16 v[36:39], v[68:71], a[224:227], v[36:39]\n" \
  "v_mfma_f32_16x16x32_bf16 v[24:27], v[224:227], v[72:75], v[24:27]\n" \
  "s_add_u32 m0, s91, 12288\n" \
  "s_nop 0\n" \
  "buffer_load_dwordx4 v7, s[72:75], s87 offen lds\n" \
  "s_add_u32 s87, s87, 128\n" \
  "v_mfma_f32_16x16x32_bf16 v[32:35], a[96:99], v[72:75], v[32:35]\n" \
  "v_mfma_f32_16x16x32_bf16 v[40:43], v[72:75], a[224:227], v[40:43]\n" \
  "s_waitcnt lgkmcnt(4)\n" \
  "v_mfma_f32_16x16x32_bf16 v[20:23], v[228:231], v[76:79], v[20:23]\n" \
  "ds_read_b128 v[68:71], v10 offset:57344\n" \
  "v_mfma_f32_16x16x32_bf16 v[28:31], a[100:103], v[76:79], v[28:31]\n" \
  "ds_read_b128 v[72:75], v10 offset:59392\n" \
  "v_mfma_f32_16x16x32_bf16 v[36:39], v[76:79], a[228:231], v[36:39]\n" \
  "v_mfma_f32_16x16x32_bf16 v[24:27], v[228:231], v[80:83], v[24:27]\n" \
  "s_add_u32 m0, s91, 16384\n" \
  "s_nop 0\n" \
  "buffer_load_dwordx4 v7, s[72:75], s87 offen lds\n" \
  "s_add_u32 s87, s87, 128\n" \
  "v_mfma_f32_16x16x32_bf16 v[32:35], a[100:103], v[80:83], v[32:35]\n" \
  "v_mfma_f32_16x16x32_bf16 v[40:43], v[80:83], a[228:231], v[40:43]\n" \
  "s_waitcnt lgkmcnt(4)\n" \
  "v_mfma_f32_16x16x32_bf16 v[20:23], v[232:235], v[84:87], v[20:23]\n" \
  "ds_read_b128 v[76:79], v11 offset:57344\n" \
  "v_mfma_f32_16x16x32_bf16 v[28:31], a[104:107], v[84:87], v[28:31]\n" \
  "ds_read_b128 v[80:83], v11 offset:59392\n" \
  "v_mfma_f32_16x16x32_bf16 v[36:39], v[84:87], a[232:235], v[36:39]\n" \
  "v_mfma_f32_16x16x32_bf16 v[24:27], v[232:235], v[88:91], v[24:27]\n" \
  "s_add_u32 m0, s91, 20480\n" \
  "s_nop 0\n" \
  "buffer_load_dwordx4 v7, s[72:75], s87 offen lds\n" \
  "s_add_u32 s87, s87, 128\n" \
  "v_mfma_f32_16x16x32_bf16 v[32:35], a[104:107], v[88:91], v[32:35]\n" \
  "v_mfma_f32_16x16x32_bf16 v[40:43], v[88:91], a[232:235], v[40:43]\n" \
  "s_waitcnt lgkmcnt(4)\n" \
  "v_mfma_f32_16x16x32_bf16 v[20:23], v[236:239], v[92:95], v[20:23]\n" \
  "ds_read_b128 v[84:87], v10 offset:61440\n" \
  "v_mfma_f32_16x16x32_bf16 v[28:31], a[108:111], v[92:95], v[28:31]\n" \
  "ds_read_b128 v[88:91], v10 offset:63488\n" \
  "v_mfma_f32_16x16x32_bf16 v[36:39], v[92:95], a[236:239], v[36:39]\n" \
  "v_mfma_f32_16x16x32_bf16 v[24:27], v[236:239], v[96:99], v[24:27]\n" \
  "s_add_u32 m0, s91, 24576\n" \
  "s_nop 0\n" \
  "buffer_load_dwordx4 v7, s[72:75], s87 offen lds\n" \
  "s_add_u32 s87, s87, 128\n" \
  "v_mfma_f32_16x16x32_bf16 v[32:35], a[108:111], v[96:99], v[32:35]\n" \
  "v_mfma_f32_16x16x32_bf16 v[40:43], v[96:99], a[236:239], v[40:43]\n" \
  "s_waitcnt lgkmcnt(4)\n" \
  "v_mfma_f32_16x16x32_bf16 v[20:23], v[240:243], v[68:71], v[20:23]\n" \
  "ds_read_b128 v[92:95], v11 offset:61440\n" \
  "v_mfma_f32_16x16x32_bf16 v[28:31], a[112:115], v[68:71], v[28:31]\n" \
  "ds_read_b128 v[96:99], v11 offset:63488\n" \
  "v_mfma_f32_16x16x32_bf16 v[36:39], v[68:71], a[240:243], v[36:39]\n" \
  "v_mfma_f32_16x16x32_bf16 v[24:27], v[240:243], v[72:75], v[24:27]\n" \
  "s_add_u32 m0, s91, 28672\n" \
  "s_nop 0\n" \
  "buffer_load_dwordx4 v7, s[72:75], s87 offen lds\n" \
  "s_add_u32 s87, s87, 128\n" \
  "v_mfma_f32_16x16x32_bf16 v[32:35], a[112:115], v[72:75], v[32:35]\n" \
  "v_mfma_f32_16x16x32_bf16 v[40:43], v[72:75], a[240:243], v[40:43]\n" \
  "s_waitcnt lgkmcnt(4)\n" \
  "v_mfma_f32_16x16x32_bf16 v[20:23], v[244:247], v[76:79], v[20:23]\n" \
  "ds_read_b128 v[68:71], v12 offset:0\n" \
  "v_mfma_f32_16x16x32_bf16 v[28:31], a[116:119], v[76:79], v[28:31]\n" \
  "ds_read_b128 v[72:75], v12 offset:2048\n" \
  "v_mfma_f32_16x16x32_bf16 v[36:39], v[76:79], a[244:247], v[36:39]\n" \
  "v_mfma_f32_16x16x32_bf16 v[24:27], v[244:247], v[80:83], v[24:27]\n" \
  "v_mfma_f32_16x16x32_bf16 v[32:35], a[116:119], v[80:83], v[32:35]\n" \
  "v_mfma_f32_16x16x32_bf16 v[40:43], v[80:83], a[244:247], v[40:43]\n" \
  "s_waitcnt lgkmcnt(4)\n" \
  "v_mfma_f32_16x16x32_bf16 v[20:23], v[248:251], v[84:87], v[20:23]\n" \
  "ds_read_b128 v[76:79], v13 offset:0\n" \
  "v_mfma_f32_16x16x32_bf16 v[28:31], a[120:123], v[84:87], v[28:31]\n" \
  "ds_read_b128 v[80:83], v13 offset:2048\n" \
  "v_mfma_f32_16x16x32_bf16 v[36:39], v[84:87], a[248:251], v[36:39]\n" \
  "v_mfma_f32_16x16x32_bf16 v[24:27], v[248:251], v[88:91], v[24:27]\n" \
  "v_mfma_f32_16x16x32_bf16 v[32:35], a[120:123], v[88:91], v[32:35]\n" \
  "v_mfma_f32_16x16x32_bf16 v[40:43], v[88:91], a[248:251], v[40:43]\n" \
  "s_waitcnt lgkmcnt(4)\n" \
  "v_mfma_f32_16x16x32_bf16 v[20:23], v[252:255], v[92:95], v[20:23]\n" \
  "ds_read_b128 v[84:87], v12 offset:4096\n" \
  "v_mfma_f32_16x16x32_bf16 v[28:31], a[124:127], v[92:95], v[28:31]\n" \
  "ds_read_b128 v[88:91], v12 offset:6144\n" \
  "v_mfma_f32_16x16x32_bf16 v[36:39], v[92:95], a[252:255], v[36:39]\n" \
  "v_mfma_f32_16x16x32_bf16 v[24:27], v[252:255], v[96:99], v[24:27]\n" \
  "v_mfma_f32_16x16x32_bf16 v[32:35], a[124:127], v[96:99], v[32:35]\n" \
  "v_mfma_f32_16x16x32_bf16 v[40:43], v[96:99], a[252:255], v[40:43]\n" \
  "s_cmp_eq_u32 s85, 64\n" \
  "s_cbranch_scc1 LX1_%=\n" \
  "s_waitcnt vmcnt(8)\n" \
  "s_barrier\n" \
  "s_waitcnt lgkmcnt(4)\n" \
  "v_mfma_f32_16x16x32_bf16 v[44:47], v[128:131], v[68:71], 0\n" \
  "ds_read_b128 v[92:95], v13 offset:4096\n" \
  "s_sub_u32 s89, s85, 1\n" \
  "s_and_b32 s89, s89, 63\n" \
  "v_mfma_f32_16x16x32_bf16 v[52:55], a[0:3], v[68:71], 0\n" \
  "ds_read_b128 v[96:99], v13 offset:6144\n" \
  "s_lshr_b32 s84, s89, 2\n" \
  "s_xnor_b32 s84, s84, %9\n" \
  "v_mfma_f32_16x16x32_bf16 v[60:63], v[68:71], a[128:131], 0\n" \
  "s_and_b32 s84, s84, 1\n" \
  "s_lshl_b32 s84, s84, 13\n" \
  "v_mfma_f32_16x16x32_bf16 v[48:51], v[128:131], v[72:75], 0\n" \
  "s_lshl_b32 s88, s89, 19\n" \
  "s_or_b32 s88, s88, s84\n" \
  "v_mfma_f32_16x16x32_bf16 v[56:59], a[0:3], v[72:75], 0\n" \
  "s_add_u32 s89, s88, " STR(QKV_SZ32) "\n" \
  "s_add_u32 s90, s89, " STR(QKV_SZ32) "\n" \
  "v_mfma_f32_16x16x32_bf16 v[64:67], v[72:75], a[128:131], 0\n" \
  "s_add_u32 s85, s85, 1\n" \
  "s_waitcnt lgkmcnt(4)\n" \
  "v_mfma_f32_16x16x32_bf16 v[44:47], v[132:135], v[76:79], v[44:47]\n" \
  "ds_read_b128 v[68:71], v12 offset:8192\n" \
  "v_cvt_pk_bf16_f32 v116, v20, v21\n" \
  "v_mfma_f32_16x16x32_bf16 v[52:55], a[4:7], v[76:79], v[52:55]\n" \
  "ds_read_b128 v[72:75], v12 offset:10240\n" \
  "v_cvt_pk_bf16_f32 v117, v22, v23\n" \
  "v_mfma_f32_16x16x32_bf16 v[60:63], v[76:79], a[132:135], v[60:63]\n" \
  "v_cvt_pk_bf16_f32 v118, v24, v25\n" \
  "v_mfma_f32_16x16x32_bf16 v[48:51], v[132:135], v[80:83], v[48:51]\n" \
  "v_cvt_pk_bf16_f32 v119, v26, v27\n" \
  "v_mfma_f32_16x16x32_bf16 v[56:59], a[4:7], v[80:83], v[56:59]\n" \
  "v_cvt_pk_bf16_f32 v120, v28, v29\n" \
  "v_mfma_f32_16x16x32_bf16 v[64:67], v[80:83], a[132:135], v[64:67]\n" \
  "v_cvt_pk_bf16_f32 v121, v30, v31\n" \
  "s_waitcnt lgkmcnt(4)\n" \
  "v_mfma_f32_16x16x32_bf16 v[44:47], v[136:139], v[84:87], v[44:47]\n" \
  "ds_read_b128 v[76:79], v13 offset:8192\n" \
  "v_cvt_pk_bf16_f32 v122, v32, v33\n" \
  "v_mfma_f32_16x16x32_bf16 v[52:55], a[8:11], v[84:87], v[52:55]\n" \
  "ds_read_b128 v[80:83], v13 offset:10240\n" \
  "v_cvt_pk_bf16_f32 v123, v34, v35\n" \
  "v_mfma_f32_16x16x32_bf16 v[60:63], v[84:87], a[136:139], v[60:63]\n" \
  "v_cvt_pk_bf16_f32 v124, v36, v37\n" \
  "v_mfma_f32_16x16x32_bf16 v[48:51], v[136:139], v[88:91], v[48:51]\n" \
  "v_cvt_pk_bf16_f32 v125, v38, v39\n" \
  "v_mfma_f32_16x16x32_bf16 v[56:59], a[8:11], v[88:91], v[56:59]\n" \
  "v_cvt_pk_bf16_f32 v126, v40, v41\n" \
  "v_mfma_f32_16x16x32_bf16 v[64:67], v[88:91], a[136:139], v[64:67]\n" \
  "v_cvt_pk_bf16_f32 v127, v42, v43\n" \
  "s_waitcnt lgkmcnt(4)\n" \
  "v_mfma_f32_16x16x32_bf16 v[44:47], v[140:143], v[92:95], v[44:47]\n" \
  "ds_read_b128 v[84:87], v12 offset:12288\n" \
  "v_permlane16_swap_b32 v116, v118\n" \
  "v_mfma_f32_16x16x32_bf16 v[52:55], a[12:15], v[92:95], v[52:55]\n" \
  "ds_read_b128 v[88:91], v12 offset:14336\n" \
  "v_permlane16_swap_b32 v117, v119\n" \
  "v_mfma_f32_16x16x32_bf16 v[60:63], v[92:95], a[140:143], v[60:63]\n" \
  "v_permlane16_swap_b32 v120, v122\n" \
  "v_mfma_f32_16x16x32_bf16 v[48:51], v[140:143], v[96:99], v[48:51]\n" \
  "v_permlane16_swap_b32 v121, v123\n" \
  "v_mfma_f32_16x16x32_bf16 v[56:59], a[12:15], v[96:99], v[56:59]\n" \
  "v_mfma_f32_16x16x32_bf16 v[64:67], v[96:99], a[140:143], v[64:67]\n" \
  "s_waitcnt lgkmcnt(4)\n" \
  "v_mfma_f32_16x16x32_bf16 v[44:47], v[144:147], v[68:71], v[44:47]\n" \
  "ds_read_b128 v[92:95], v13 offset:12288\n" \
  "buffer_store_dwordx4 v[116:119], v14, s[76:79], s88 offen sc1\n" \
  "v_mfma_f32_16x16x32_bf16 v[52:55], a[16:19], v[68:71], v[52:55]\n" \
  "ds_read_b128 v[96:99], v13 offset:14336\n" \
  "buffer_store_dwordx4 v[120:123], v18, s[76:79], s89 offen sc1\n" \
  "v_mfma_f32_16x16x32_bf16 v[60:63], v[68:71], a[144:147], v[60:63]\n" \
  "buffer_store_dwordx4 v[124:127], v15, s[76:79], s90 offen sc1\n" \
  "v_mfma_f32_16x16x32_bf16 v[48:51], v[144:147], v[72:75], v[48:51]\n" \
  "v_mfma_f32_16x16x32_bf16 v[56:59], a[16:19], v[72:75], v[56:59]\n" \
  "v_mfma_f32_16x16x32_bf16 v[64:67], v[72:75], a[144:147], v[64:67]\n" \
  "s_waitcnt lgkmcnt(4)\n" \
  "v_mfma_f32_16x16x32_bf16 v[44:47], v[148:151], v[76:79], v[44:47]\n" \
  "ds_read_b128 v[68:71], v12 offset:16384\n" \
  "v_mfma_f32_16x16x32_bf16 v[52:55], a[20:23], v[76:79], v[52:55]\n" \
  "ds_read_b128 v[72:75], v12 offset:18432\n" \
  "v_mfma_f32_16x16x32_bf16 v[60:63], v[76:79], a[148:151], v[60:63]\n" \
  "v_mfma_f32_16x16x32_bf16 v[48:51], v[148:151], v[80:83], v[48:51]\n" \
  "s_add_u32 m0, s91, 32768\n" \
  "s_nop 0\n" \
  "buffer_load_dwordx4 v7, s[72:75], s87 offen lds\n" \
  "s_add_u32 s87, s87, 128\n" \
  "v_mfma_f32_16x16x32_bf16 v[56:59], a[20:23], v[80:83], v[56:59]\n" \
  "v_mfma_f32_16x16x32_bf16 v[64:67], v[80:83], a[148:151], v[64:67]\n" \
  "s_waitcnt lgkmcnt(4)\n" \
  "v_mfma_f32_16x16x32_bf16 v[44:47], v[152:155], v[84:87], v[44:47]\n" \
  "ds_read_b128 v[76:79], v13 offset:16384\n" \
  "v_mfma_f32_16x16x32_bf16 v[52:55], a[24:27], v[84:87], v[52:55]\n" \
  "ds_read_b128 v[80:83], v13 offset:18432\n" \
  "v_mfma_f32_16x16x32_bf16 v[60:63], v[84:87], a[152:155], v[60:63]\n" \
  "v_mfma_f32_16x16x32_bf16 v[48:51], v[152:155], v[88:91], v[48:51]\n" \
  "s_add_u32 m0, s91, 36864\n" \
  "s_nop 0\n" \
  "buffer_load_dwordx4 v7, s[72:75], s87 offen lds\n" \
  "s_add_u32 s87, s87, 128\n" \
  "v_mfma_f32_16x16x32_bf16 v[56:59], a[24:27], v[88:91], v[56:59]\n" \
  "v_mfma_f32_16x16x32_bf16 v[64:67], v[88:91], a[152:155], v[64:67]\n" \
  "s_waitcnt lgkmcnt(4)\n" \
  "v_mfma_f32_16x16x32_bf16 v[44:47], v[156:159], v[92:95], v[44:47]\n" \
  "ds_read_b128 v[84:87], v12 offset:20480\n" \
  "v_mfma_f32_16x16x32_bf16 v[52:55], a[28:31], v[92:95], v[52:55]\n" \
  "ds_read_b128 v[88:91], v12 offset:22528\n" \
  "v_mfma_f32_16x16x32_bf16 v[60:63], v[92:95], a[156:159], v[60:63]\n" \
  "v_mfma_f32_16x16x32_bf16 v[48:51], v[156:159], v[96:99], v[48:51]\n" \
  "s_add_u32 m0, s91, 40960\n" \
  "s_nop 0\n" \
  "buffer_load_dwordx4 v7, s[72:75], s87 offen lds\n" \
  "s_add_u32 s87, s87, 128\n" \
  "v_mfma_f32_16x16x32_bf16 v[56:59], a[28:31], v[96:99], v[56:59]\n" \
  "v_mfma_f32_16x16x32_bf16 v[64:67], v[96:99], a[156:159], v[64:67]\n" \
  "s_waitcnt lgkmcnt(4)\n" \
  "v_mfma_f32_16x16x32_bf16 v[44:47], v[160:163], v[68:71], v[44:47]\n" \
  "ds_read_b128 v[92:95], v13 offset:20480\n" \
  "v_mfma_f32_16x16x32_bf16 v[52:55], a[32:35], v[68:71], v[52:55]\n" \
  "ds_read_b128 v[96:99], v13 offset:22528\n" \
  "v_mfma_f32_16x16x32_bf16 v[60:63], v[68:71], a[160:163], v[60:63]\n" \
  "v_mfma_f32_16x16x32_bf16 v[48:51], v[160:163], v[72:75], v[48:51]\n" \
  "s_add_u32 m0, s91, 45056\n" \
  "s_nop 0\n" \
  "buffer_load_dwordx4 v7, s[72:75], s87 offen lds\n" \
  "s_add_u32 s87, s87, 128\n" \
  "v_mfma_f32_16x16x32_bf16 v[56:59], a[32:35], v[72:75], v[56:59]\n" \
  "v_mfma_f32_16x16x32_bf16 v[64:67], v[72:75], a[160:163], v[64:67]\n" \
  "s_waitcnt lgkmcnt(4)\n" \
  "v_mfma_f32_16x16x32_bf16 v[44:47], v[164:167], v[76:79], v[44:47]\n" \
  "ds_read_b128 v[68:71], v12 offset:24576\n" \
  "v_mfma_f32_16x16x32_bf16 v[52:55], a[36:39], v[76:79], v[52:55]\n" \
  "ds_read_b128 v[72:75], v12 offset:26624\n" \
  "v_mfma_f32_16x16x32_bf16 v[60:63], v[76:79], a[164:167], v[60:63]\n" \
  "v_mfma_f32_16x16x32_bf16 v[48:51], v[164:167], v[80:83], v[48:51]\n" \
  "s_add_u32 m0, s91, 49152\n" \
  "s_nop 0\n" \
  "buffer_load_dwordx4 v7, s[72:75], s87 offen lds\n" \
  "s_add_u32 s87, s87, 128\n" \
  "v_mfma_f32_16x16x32_bf16 v[56:59], a[36:39], v[80:83], v[56:59]\n" \
  "v_mfma_f32_16x16x32_bf16 v[64:67], v[80:83], a[164:167], v[64:67]\n" \
  "s_waitcnt lgkmcnt(4)\n" \
  "v_mfma_f32_16x16x32_bf16 v[44:47], v[168:171], v[84:87], v[44:47]\n" \
  "ds_read_b128 v[76:79], v13 offset:24576\n" \
  "v_mfma_f32_16x16x32_bf16 v[52:55], a[40:43], v[84:87], v[52:55]\n" \
  "ds_read_b128 v[80:83], v13 offset:26624\n" \
  "v_mfma_f32_16x16x32_bf16 v[60:63], v[84:87], a[168:171], v[60:63]\n" \
  "v_mfma_f32_16x16x32_bf16 v[48:51], v[168:171], v[88:91], v[48:51]\n" \
  "s_add_u32 m0, s91, 53248\n" \
  "s_nop 0\n" \
  "buffer_load_dwordx4 v7, s[72:75], s87 offen lds\n" \
  "s_add_u32 s87, s87, 128\n" \
  "v_mfma_f32_16x16x32_bf16 v[56:59], a[40:43], v[88:91], v[56:59]\n" \
  "v_mfma_f32_16x16x32_bf16 v[64:67], v[88:91], a[168:171], v[64:67]\n" \
  "s_waitcnt lgkmcnt(4)\n" \
  "v_mfma_f32_16x16x32_bf16 v[44:47], v[172:175], v[92:95], v[44:47]\n" \
  "ds_read_b128 v[84:87], v12 offset:28672\n" \
  "v_mfma_f32_16x16x32_bf16 v[52:55], a[44:47], v[92:95], v[52:55]\n" \
  "ds_read_b128 v[88:91], v12 offset:30720\n" \
  "v_mfma_f32_16x16x32_bf16 v[60:63], v[92:95], a[172:175], v[60:63]\n" \
  "v_mfma_f32_16x16x32_bf16 v[48:51], v[172:175], v[96:99], v[48:51]\n" \
  "s_add_u32 m0, s91, 57344\n" \
  "s_nop 0\n" \
  "buffer_load_dwordx4 v7, s[72:75], s87 offen lds\n" \
  "s_add_u32 s87, s87, 128\n" \
  "v_mfma_f32_16x16x32_bf16 v[56:59], a[44:47], v[96:99], v[56:59]\n" \
  "v_mfma_f32_16x16x32_bf16 v[64:67], v[96:99], a[172:175], v[64:67]\n" \
  "s_waitcnt lgkmcnt(4)\n" \
  "v_mfma_f32_16x16x32_bf16 v[44:47], v[176:179], v[68:71], v[44:47]\n" \
  "ds_read_b128 v[92:95], v13 offset:28672\n" \
  "v_mfma_f32_16x16x32_bf16 v[52:55], a[48:51], v[68:71], v[52:55]\n" \
  "ds_read_b128 v[96:99], v13 offset:30720\n" \
  "v_mfma_f32_16x16x32_bf16 v[60:63], v[68:71], a[176:179], v[60:63]\n" \
  "v_mfma_f32_16x16x32_bf16 v[48:51], v[176:179], v[72:75], v[48:51]\n" \
  "s_add_u32 m0, s91, 61440\n" \
  "s_nop 0\n" \
  "buffer_load_dwordx4 v7, s[72:75], s87 offen lds\n" \
  "s_add_u32 s87, s87, 63616\n" \
  "s_min_u32 s87, s87, s96\n" \
  "v_mfma_f32_16x16x32_bf16 v[56:59], a[48:51], v[72:75], v[56:59]\n" \
  "v_mfma_f32_16x16x32_bf16 v[64:67], v[72:75], a[176:179], v[64:67]\n" \
  "s_waitcnt lgkmcnt(4)\n" \
  "v_mfma_f32_16x16x32_bf16 v[44:47], v[180:183], v[76:79], v[44:47]\n" \
  "ds_read_b128 v[68:71], v12 offset:32768\n" \
  "v_mfma_f32_16x16x32_bf16 v[52:55], a[52:55], v[76:79], v[52:55]\n" \
  "ds_read_b128 v[72:75], v12 offset:34816\n" \
  "v_mfma_f32_16x16x32_bf16 v[60:63], v[76:79], a[180:183], v[60:63]\n" \
  "v_mfma_f32_16x16x32_bf16 v[48:51], v[180:183], v[80:83], v[48:51]\n" \
  "v_mfma_f32_16x16x32_bf16 v[56:59], a[52:55], v[80:83], v[56:59]\n" \
  "v_mfma_f32_16x16x32_bf16 v[64:67], v[80:83], a[180:183], v[64:67]\n" \
  "s_waitcnt lgkmcnt(4)\n" \
  "v_mfma_f32_16x16x32_bf16 v[44:47], v[184:187], v[84:87], v[44:47]\n" \
  "ds_read_b128 v[76:79], v13 offset:32768\n" \
  "v_mfma_f32_16x16x32_bf16 v[52:55], a[56:59], v[84:87], v[52:55]\n" \
  "ds_read_b128 v[80:83], v13 offset:34816\n" \
  "v_mfma_f32_16x16x32_bf16 v[60:63], v[84:87], a[184:187], v[60:63]\n" \
  "v_mfma_f32_16x16x32_bf16 v[48:51], v[184:187], v[88:91], v[48:51]\n" \
  "v_mfma_f32_16x16x32_bf16 v[56:59], a[56:59], v[88:91], v[56:59]\n" \
  "v_mfma_f32_16x16x32_bf16 v[64:67], v[88:91], a[184:187], v[64:67]\n" \
  "s_waitcnt lgkmcnt(4)\n" \
  "v_mfma_f32_16x16x32_bf16 v[44:47], v[188:191], v[92:95], v[44:47]\n" \
  "ds_read_b128 v[84:87], v12 offset:36864\n" \
  "v_mfma_f32_16x16x32_bf16 v[52:55], a[60:63], v[92:95], v[52:55]\n" \
  "ds_read_b128 v[88:91], v12 offset:38912\n" \
  "v_mfma_f32_16x16x32_bf16 v[60:63], v[92:95], a[188:191], v[60:63]\n" \
  "v_mfma_f32_16x16x32_bf16 v[48:51], v[188:191], v[96:99], v[48:51]\n" \
  "v_mfma_f32_16x16x32_bf16 v[56:59], a[60:63], v[96:99], v[56:59]\n" \
  "v_mfma_f32_16x16x32_bf16 v[64:67], v[96:99], a[188:191], v[64:67]\n" \
  "s_waitcnt vmcnt(8)\n" \
  "s_barrier\n" \
  "s_waitcnt lgkmcnt(4)\n" \
  "v_mfma_f32_16x16x32_bf16 v[44:47], v[192:195], v[68:71], v[44:47]\n" \
  "ds_read_b128 v[92:95], v13 offset:36864\n" \
  "buffer_store_dword v17, v16, s[80:83], 0 offen sc1\n" \
  "v_mfma_f32_16x16x32_bf16 v[52:55], a[64:67], v[68:71], v[52:55]\n" \
  "ds_read_b128 v[96:99], v13 offset:38912\n" \
  "v_add_u32 v17, 1, v17\n" \
  "v_mfma_f32_16x16x32_bf16 v[60:63], v[68:71], a[192:195], v[60:63]\n" \
  "v_and_b32 v14, 0x7fffffff, v14\n" \
  "v_mfma_f32_16x16x32_bf16 v[48:51], v[192:195], v[72:75], v[48:51]\n" \
  "v_and_b32 v15, 0x7fffffff, v15\n" \
  "v_mfma_f32_16x16x32_bf16 v[56:59], a[64:67], v[72:75], v[56:59]\n" \
  "v_and_b32 v18, 0x7fffffff, v18\n" \
  "v_mfma_f32_16x16x32_bf16 v[64:67], v[72:75], a[192:195], v[64:67]\n" \
  "s_waitcnt lgkmcnt(4)\n" \
  "v_mfma_f32_16x16x32_bf16 v[44:47], v[196:199], v[76:79], v[44:47]\n" \
  "ds_read_b128 v[68:71], v12 offset:40960\n" \
  "v_mfma_f32_16x16x32_bf16 v[52:55], a[68:71], v[76:79], v[52:55]\n" \
  "ds_read_b128 v[72:75], v12 offset:43008\n" \
  "v_mfma_f32_16x16x32_bf16 v[60:63], v[76:79], a[196:199], v[60:63]\n" \
  "v_mfma_f32_16x16x32_bf16 v[48:51], v[196:199], v[80:83], v[48:51]\n" \
  "v_mfma_f32_16x16x32_bf16 v[56:59], a[68:71], v[80:83], v[56:59]\n" \
  "v_mfma_f32_16x16x32_bf16 v[64:67], v[80:83], a[196:199], v[64:67]\n" \
  "s_waitcnt lgkmcnt(4)\n" \
  "v_mfma_f32_16x16x32_bf16 v[44:47], v[200:203], v[84:87], v[44:47]\n" \
  "ds_read_b128 v[76:79], v13 offset:40960\n" \
  "v_mfma_f32_16x16x32_bf16 v[52:55], a[72:75], v[84:87], v[52:55]\n" \
  "ds_read_b128 v[80:83], v13 offset:43008\n" \
  "v_mfma_f32_16x16x32_bf16 v[60:63], v[84:87], a[200:203], v[60:63]\n" \
  "v_mfma_f32_16x16x32_bf16 v[48:51], v[200:203], v[88:91], v[48:51]\n" \
  "v_mfma_f32_16x16x32_bf16 v[56:59], a[72:75], v[88:91], v[56:59]\n" \
  "v_mfma_f32_16x16x32_bf16 v[64:67], v[88:91], a[200:203], v[64:67]\n" \
  "s_waitcnt lgkmcnt(4)\n" \
  "v_mfma_f32_16x16x32_bf16 v[44:47], v[204:207], v[92:95], v[44:47]\n" \
  "ds_read_b128 v[84:87], v12 offset:45056\n" \
  "v_mfma_f32_16x16x32_bf16 v[52:55], a[76:79], v[92:95], v[52:55]\n" \
  "ds_read_b128 v[88:91], v12 offset:47104\n" \
  "v_mfma_f32_16x16x32_bf16 v[60:63], v[92:95], a[204:207], v[60:63]\n" \
  "v_mfma_f32_16x16x32_bf16 v[48:51], v[204:207], v[96:99], v[48:51]\n" \
  "v_mfma_f32_16x16x32_bf16 v[56:59], a[76:79], v[96:99], v[56:59]\n" \
  "v_mfma_f32_16x16x32_bf16 v[64:67], v[96:99], a[204:207], v[64:67]\n" \
  "s_waitcnt lgkmcnt(4)\n" \
  "v_mfma_f32_16x16x32_bf16 v[44:47], v[208:211], v[68:71], v[44:47]\n" \
  "ds_read_b128 v[92:95], v13 offset:45056\n" \
  "v_mfma_f32_16x16x32_bf16 v[52:55], a[80:83], v[68:71], v[52:55]\n" \
  "ds_read_b128 v[96:99], v13 offset:47104\n" \
  "v_mfma_f32_16x16x32_bf16 v[60:63], v[68:71], a[208:211], v[60:63]\n" \
  "v_mfma_f32_16x16x32_bf16 v[48:51], v[208:211], v[72:75], v[48:51]\n" \
  "v_mfma_f32_16x16x32_bf16 v[56:59], a[80:83], v[72:75], v[56:59]\n" \
  "v_mfma_f32_16x16x32_bf16 v[64:67], v[72:75], a[208:211], v[64:67]\n" \
  "s_waitcnt lgkmcnt(4)\n" \
  "v_mfma_f32_16x16x32_bf16 v[44:47], v[212:215], v[76:79], v[44:47]\n" \
  "ds_read_b128 v[68:71], v12 offset:49152\n" \
  "v_mfma_f32_16x16x32_bf16 v[52:55], a[84:87], v[76:79], v[52:55]\n" \
  "ds_read_b128 v[72:75], v12 offset:51200\n" \
  "v_mfma_f32_16x16x32_bf16 v[60:63], v[76:79], a[212:215], v[60:63]\n" \
  "v_mfma_f32_16x16x32_bf16 v[48:51], v[212:215], v[80:83], v[48:51]\n" \
  "s_add_u32 m0, s91, 65536\n" \
  "s_nop 0\n" \
  "buffer_load_dwordx4 v7, s[72:75], s87 offen lds\n" \
  "s_add_u32 s87, s87, 128\n" \
  "v_mfma_f32_16x16x32_bf16 v[56:59], a[84:87], v[80:83], v[56:59]\n" \
  "v_mfma_f32_16x16x32_bf16 v[64:67], v[80:83], a[212:215], v[64:67]\n" \
  "s_waitcnt lgkmcnt(4)\n" \
  "v_mfma_f32_16x16x32_bf16 v[44:47], v[216:219], v[84:87], v[44:47]\n" \
  "ds_read_b128 v[76:79], v13 offset:49152\n" \
  "v_mfma_f32_16x16x32_bf16 v[52:55], a[88:91], v[84:87], v[52:55]\n" \
  "ds_read_b128 v[80:83], v13 offset:51200\n" \
  "v_mfma_f32_16x16x32_bf16 v[60:63], v[84:87], a[216:219], v[60:63]\n" \
  "v_mfma_f32_16x16x32_bf16 v[48:51], v[216:219], v[88:91], v[48:51]\n" \
  "s_add_u32 m0, s91, 69632\n" \
  "s_nop 0\n" \
  "buffer_load_dwordx4 v7, s[72:75], s87 offen lds\n" \
  "s_add_u32 s87, s87, 128\n" \
  "v_mfma_f32_16x16x32_bf16 v[56:59], a[88:91], v[88:91], v[56:59]\n" \
  "v_mfma_f32_16x16x32_bf16 v[64:67], v[88:91], a[216:219], v[64:67]\n" \
  "s_waitcnt lgkmcnt(4)\n" \
  "v_mfma_f32_16x16x32_bf16 v[44:47], v[220:223], v[92:95], v[44:47]\n" \
  "ds_read_b128 v[84:87], v12 offset:53248\n" \
  "v_mfma_f32_16x16x32_bf16 v[52:55], a[92:95], v[92:95], v[52:55]\n" \
  "ds_read_b128 v[88:91], v12 offset:55296\n" \
  "v_mfma_f32_16x16x32_bf16 v[60:63], v[92:95], a[220:223], v[60:63]\n" \
  "v_mfma_f32_16x16x32_bf16 v[48:51], v[220:223], v[96:99], v[48:51]\n" \
  "s_add_u32 m0, s91, 73728\n" \
  "s_nop 0\n" \
  "buffer_load_dwordx4 v7, s[72:75], s87 offen lds\n" \
  "s_add_u32 s87, s87, 128\n" \
  "v_mfma_f32_16x16x32_bf16 v[56:59], a[92:95], v[96:99], v[56:59]\n" \
  "v_mfma_f32_16x16x32_bf16 v[64:67], v[96:99], a[220:223], v[64:67]\n" \
  "s_waitcnt lgkmcnt(4)\n" \
  "v_mfma_f32_16x16x32_bf16 v[44:47], v[224:227], v[68:71], v[44:47]\n" \
  "ds_read_b128 v[92:95], v13 offset:53248\n" \
  "v_mfma_f32_16x16x32_bf16 v[52:55], a[96:99], v[68:71], v[52:55]\n" \
  "ds_read_b128 v[96:99], v13 offset:55296\n" \
  "v_mfma_f32_16x16x32_bf16 v[60:63], v[68:71], a[224:227], v[60:63]\n" \
  "v_mfma_f32_16x16x32_bf16 v[48:51], v[224:227], v[72:75], v[48:51]\n" \
  "s_add_u32 m0, s91, 77824\n" \
  "s_nop 0\n" \
  "buffer_load_dwordx4 v7, s[72:75], s87 offen lds\n" \
  "s_add_u32 s87, s87, 128\n" \
  "v_mfma_f32_16x16x32_bf16 v[56:59], a[96:99], v[72:75], v[56:59]\n" \
  "v_mfma_f32_16x16x32_bf16 v[64:67], v[72:75], a[224:227], v[64:67]\n" \
  "s_waitcnt lgkmcnt(4)\n" \
  "v_mfma_f32_16x16x32_bf16 v[44:47], v[228:231], v[76:79], v[44:47]\n" \
  "ds_read_b128 v[68:71], v12 offset:57344\n" \
  "v_mfma_f32_16x16x32_bf16 v[52:55], a[100:103], v[76:79], v[52:55]\n" \
  "ds_read_b128 v[72:75], v12 offset:59392\n" \
  "v_mfma_f32_16x16x32_bf16 v[60:63], v[76:79], a[228:231], v[60:63]\n" \
  "v_mfma_f32_16x16x32_bf16 v[48:51], v[228:231], v[80:83], v[48:51]\n" \
  "s_add_u32 m0, s91, 81920\n" \
  "s_nop 0\n" \
  "buffer_load_dwordx4 v7, s[72:75], s87 offen lds\n" \
  "s_add_u32 s87, s87, 128\n" \
  "v_mfma_f32_16x16x32_bf16 v[56:59], a[100:103], v[80:83], v[56:59]\n" \
  "v_mfma_f32_16x16x32_bf16 v[64:67], v[80:83], a[228:231], v[64:67]\n" \
  "s_waitcnt lgkmcnt(4)\n" \
  "v_mfma_f32_16x16x32_bf16 v[44:47], v[232:235], v[84:87], v[44:47]\n" \
  "ds_read_b128 v[76:79], v13 offset:57344\n" \
  "v_mfma_f32_16x16x32_bf16 v[52:55], a[104:107], v[84:87], v[52:55]\n" \
  "ds_read_b128 v[80:83], v13 offset:59392\n" \
  "v_mfma_f32_16x16x32_bf16 v[60:63], v[84:87], a[232:235], v[60:63]\n" \
  "v_mfma_f32_16x16x32_bf16 v[48:51], v[232:235], v[88:91], v[48:51]\n" \
  "s_add_u32 m0, s91, 86016\n" \
  "s_nop 0\n" \
  "buffer_load_dwordx4 v7, s[72:75], s87 offen lds\n" \
  "s_add_u32 s87, s87, 128\n" \
  "v_mfma_f32_16x16x32_bf16 v[56:59], a[104:107], v[88:91], v[56:59]\n" \
  "v_mfma_f32_16x16x32_bf16 v[64:67], v[88:91], a[232:235], v[64:67]\n" \
  "s_waitcnt lgkmcnt(4)\n" \
  "v_mfma_f32_16x16x32_bf16 v[44:47], v[236:239], v[92:95], v[44:47]\n" \
  "ds_read_b128 v[84:87], v12 offset:61440\n" \
  "v_mfma_f32_16x16x32_bf16 v[52:55], a[108:111], v[92:95], v[52:55]\n" \
  "ds_read_b128 v[88:91], v12 offset:63488\n" \
  "v_mfma_f32_16x16x32_bf16 v[60:63], v[92:95], a[236:239], v[60:63]\n" \
  "v_mfma_f32_16x16x32_bf16 v[48:51], v[236:239], v[96:99], v[48:51]\n" \
  "s_add_u32 m0, s91, 90112\n" \
  "s_nop 0\n" \
  "buffer_load_dwordx4 v7, s[72:75], s87 offen lds\n" \
  "s_add_u32 s87, s87, 128\n" \
  "v_mfma_f32_16x16x32_bf16 v[56:59], a[108:111], v[96:99], v[56:59]\n" \
  "v_mfma_f32_16x16x32_bf16 v[64:67], v[96:99], a[236:239], v[64:67]\n" \
  "s_waitcnt lgkmcnt(4)\n" \
  "v_mfma_f32_16x16x32_bf16 v[44:47], v[240:243], v[68:71], v[44:47]\n" \
  "ds_read_b128 v[92:95], v13 offset:61440\n" \
  "v_mfma_f32_16x16x32_bf16 v[52:55], a[112:115], v[68:71], v[52:55]\n" \
  "ds_read_b128 v[96:99], v13 offset:63488\n" \
  "v_mfma_f32_16x16x32_bf16 v[60:63], v[68:71], a[240:243], v[60:63]\n" \
  "v_mfma_f32_16x16x32_bf16 v[48:51], v[240:243], v[72:75], v[48:51]\n" \
  "s_add_u32 m0, s91, 94208\n" \
  "s_nop 0\n" \
  "buffer_load_dwordx4 v7, s[72:75], s87 offen lds\n" \
  "s_add_u32 s87, s87, 128\n" \
  "v_mfma_f32_16x16x32_bf16 v[56:59], a[112:115], v[72:75], v[56:59]\n" \
  "v_mfma_f32_16x16x32_bf16 v[64:67], v[72:75], a[240:243], v[64:67]\n" \
  "s_waitcnt lgkmcnt(4)\n" \
  "v_mfma_f32_16x16x32_bf16 v[44:47], v[244:247], v[76:79], v[44:47]\n" \
  "ds_read_b128 v[68:71], v10 offset:0\n" \
  "v_mfma_f32_16x16x32_bf16 v[52:55], a[116:119], v[76:79], v[52:55]\n" \
  "ds_read_b128 v[72:75], v10 offset:2048\n" \
  "v_mfma_f32_16x16x32_bf16 v[60:63], v[76:79], a[244:247], v[60:63]\n" \
  "v_mfma_f32_16x16x32_bf16 v[48:51], v[244:247], v[80:83], v[48:51]\n" \
  "v_mfma_f32_16x16x32_bf16 v[56:59], a[116:119], v[80:83], v[56:59]\n" \
  "v_mfma_f32_16x16x32_bf16 v[64:67], v[80:83], a[244:247], v[64:67]\n" \
  "s_waitcnt lgkmcnt(4)\n" \
  "v_mfma_f32_16x16x32_bf16 v[44:47], v[248:251], v[84:87], v[44:47]\n" \
  "ds_read_b128 v[76:79], v11 offset:0\n" \
  "v_mfma_f32_16x16x32_bf16 v[52:55], a[120:123], v[84:87], v[52:55]\n" \
  "ds_read_b128 v[80:83], v11 offset:2048\n" \
  "v_mfma_f32_16x16x32_bf16 v[60:63], v[84:87], a[248:251], v[60:63]\n" \
  "v_mfma_f32_16x16x32_bf16 v[48:51], v[248:251], v[88:91], v[48:51]\n" \
  "v_mfma_f32_16x16x32_bf16 v[56:59], a[120:123], v[88:91], v[56:59]\n" \
  "v_mfma_f32_16x16x32_bf16 v[64:67], v[88:91], a[248:251], v[64:67]\n" \
  "s_waitcnt lgkmcnt(4)\n" \
  "v_mfma_f32_16x16x32_bf16 v[44:47], v[252:255], v[92:95], v[44:47]\n" \
  "ds_read_b128 v[84:87], v10 offset:4096\n" \
  "v_mfma_f32_16x16x32_bf16 v[52:55], a[124:127], v[92:95], v[52:55]\n" \
  "ds_read_b128 v[88:91], v10 offset:6144\n" \
  "v_mfma_f32_16x16x32_bf16 v[60:63], v[92:95], a[252:255], v[60:63]\n" \
  "v_mfma_f32_16x16x32_bf16 v[48:51], v[252:255], v[96:99], v[48:51]\n" \
  "v_mfma_f32_16x16x32_bf16 v[56:59], a[124:127], v[96:99], v[56:59]\n" \
  "v_mfma_f32_16x16x32_bf16 v[64:67], v[96:99], a[252:255], v[64:67]\n" \
  "s_branch LG_%=\n" \
  "LX0_%=:\n" \
  "s_sub_u32 s89, s85, 1\n" \
  "s_and_b32 s89, s89, 63\n" \
  "s_lshr_b32 s84, s89, 2\n" \
  "s_xnor_b32 s84, s84, %9\n" \
  "s_and_b32 s84, s84, 1\n" \
  "s_lshl_b32 s84, s84, 13\n" \
  "s_lshl_b32 s88, s89, 19\n" \
  "s_or_b32 s88, s88, s84\n" \
  "s_add_u32 s89, s88, " STR(QKV_SZ32) "\n" \
  "s_add_u32 s90, s89, " STR(QKV_SZ32) "\n" \
  "s_add_u32 s85, s85, 1\n" \
  "v_cvt_pk_bf16_f32 v116, v44, v45\n" \
  "v_cvt_pk_bf16_f32 v117, v46, v47\n" \
  "v_cvt_pk_bf16_f32 v118, v48, v49\n" \
  "v_cvt_pk_bf16_f32 v119, v50, v51\n" \
  "v_cvt_pk_bf16_f32 v120, v52, v53\n" \
  "v_cvt_pk_bf16_f32 v121, v54, v55\n" \
  "v_cvt_pk_bf16_f32 v122, v56, v57\n" \
  "v_cvt_pk_bf16_f32 v123, v58, v59\n" \
  "v_cvt_pk_bf16_f32 v124, v60, v61\n" \
  "v_cvt_pk_bf16_f32 v125, v62, v63\n" \
  "v_cvt_pk_bf16_f32 v126, v64, v65\n" \
  "v_cvt_pk_bf16_f32 v127, v66, v67\n" \
  "s_nop 1\n" \
  "v_permlane16_swap_b32 v116, v118\n" \
  "v_permlane16_swap_b32 v117, v119\n" \
  "v_permlane16_swap_b32 v120, v122\n" \
  "v_permlane16_swap_b32 v121, v123\n" \
  "buffer_store_dwordx4 v[116:119], v14, s[76:79], s88 offen sc1\n" \
  "buffer_store_dwordx4 v[120:123], v18, s[76:79], s89 offen sc1\n" \
  "buffer_store_dwordx4 v[124:127], v15, s[76:79], s90 offen sc1\n" \
  "s_branch LZ_%=\n" \
  "LX1_%=:\n" \
  "s_sub_u32 s89, s85, 1\n" \
  "s_and_b32 s89, s89, 63\n" \
  "s_lshr_b32 s84, s89, 2\n" \
  "s_xnor_b32 s84, s84, %9\n" \
  "s_and_b32 s84, s84, 1\n" \
  "s_lshl_b32 s84, s84, 13\n" \
  "s_lshl_b32 s88, s89, 19\n" \
  "s_or_b32 s88, s88, s84\n" \
  "s_add_u32 s89, s88, " STR(QKV_SZ32) "\n" \
  "s_add_u32 s90, s89, " STR(QKV_SZ32) "\n" \
  "s_add_u32 s85, s85, 1\n" \
  "v_cvt_pk_bf16_f32 v116, v20, v21\n" \
  "v_cvt_pk_bf16_f32 v117, v22, v23\n" \
  "v_cvt_pk_bf16_f32 v118, v24, v25\n" \
  "v_cvt_pk_bf16_f32 v119, v26, v27\n" \
  "v_cvt_pk_bf16_f32 v120, v28, v29\n" \
  "v_cvt_pk_bf16_f32 v121, v30, v31\n" \
  "v_cvt_pk_bf16_f32 v122, v32, v33\n" \
  "v_cvt_pk_bf16_f32 v123, v34, v35\n" \
  "v_cvt_pk_bf16_f32 v124, v36, v37\n" \
  "v_cvt_pk_bf16_f32 v125, v38, v39\n" \
  "v_cvt_pk_bf16_f32 v126, v40, v41\n" \
  "v_cvt_pk_bf16_f32 v127, v42, v43\n" \
  "s_nop 1\n" \
  "v_permlane16_swap_b32 v116, v118\n" \
  "v_permlane16_swap_b32 v117, v119\n" \
  "v_permlane16_swap_b32 v120, v122\n" \
  "v_permlane16_swap_b32 v121, v123\n" \
  "buffer_store_dwordx4 v[116:119], v14, s[76:79], s88 offen sc1\n" \
  "buffer_store_dwordx4 v[120:123], v18, s[76:79], s89 offen sc1\n" \
  "buffer_store_dwordx4 v[124:127], v15, s[76:79], s90 offen sc1\n" \
  "s_branch LZ_%=\n" \
  "LZ_%=:\n" \
  "s_waitcnt vmcnt(0) lgkmcnt(0)\n" \
  "s_barrier\n" \
  "buffer_store_dword v17, v16, s[80:83], 0 offen sc1\n" \
  "s_waitcnt vmcnt(0)\n" \
  ""

#define XO_ASM \
  "s_mov_b64 s[68:69], %0\n" \
  "s_and_b32 s69, s69, 0xffff\n" \
  "s_mov_b32 s70, 8388608\n" \
  "s_mov_b32 s71, 0x00020000\n" \
  "s_mov_b64 s[72:73], %1\n" \
  "s_and_b32 s73, s73, 0xffff\n" \
  "s_mov_b32 s74, " STR(QKV_SZ32) "\n" \
  "s_mov_b32 s75, 0x00020000\n" \
  "s_mov_b64 s[76:77], %2\n" \
  "s_and_b32 s77, s77, 0xffff\n" \
  "s_mov_b32 s78, 8388608\n" \
  "s_mov_b32 s79, 0x00020000\n" \
  "s_mov_b64 s[80:81], %3\n" \
  "s_and_b32 s81, s81, 0xffff\n" \
  "s_mov_b32 s82, " STR(CTRL_BYTES) "\n" \
  "s_mov_b32 s83, 0x00020000\n" \
  "v_mbcnt_lo_u32_b32 v4, -1, 0\n" \
  "v_mbcnt_hi_u32_b32 v4, -1, v4\n" \
  "v_and_b32 v20, 15, v4\n" \
  "v_lshrrev_b32 v21, 4, v4\n" \
  "v_lshlrev_b32 v6, 4, v4\n" \
  "s_lshl_b32 s86, %5, 15\n" \
  "s_add_u32 s86, s86, %6\n" \
  "s_lshl_b32 s90, %5, 21\n" \
  "v_lshrrev_b32 v5, 3, v4\n" \
  "v_and_b32 v15, 7, v4\n" \
  "v_lshlrev_b32 v7, 11, v5\n" \
  "v_lshl_add_u32 v7, v15, 4, v7\n" \
  "v_lshlrev_b32 v5, 5, v21\n" \
  "v_xor_b32 v7, v7, v5\n" \
  "s_add_u32 m0, s86, 0\n" \
  "s_nop 0\n" \
  "buffer_load_dwordx4 v7, s[68:71], s90 offen lds\n" \
  "s_add_u32 s90, s90, 16384\n" \
  "s_add_u32 m0, s86, 1024\n" \
  "s_nop 0\n" \
  "buffer_load_dwordx4 v7, s[68:71], s90 offen lds\n" \
  "s_add_u32 s90, s90, 16384\n" \
  "s_add_u32 m0, s86, 2048\n" \
  "s_nop 0\n" \
  "buffer_load_dwordx4 v7, s[68:71], s90 offen lds\n" \
  "s_add_u32 s90, s90, 16384\n" \
  "s_add_u32 m0, s86, 3072\n" \
  "s_nop 0\n" \
  "buffer_load_dwordx4 v7, s[68:71], s90 offen lds\n" \
  "s_add_u32 s90, s90, 16384\n" \
  "s_add_u32 m0, s86, 4096\n" \
  "s_nop 0\n" \
  "buffer_load_dwordx4 v7, s[68:71], s90 offen lds\n" \
  "s_add_u32 s90, s90, 16384\n" \
  "s_add_u32 m0, s86, 5120\n" \
  "s_nop 0\n" \
  "buffer_load_dwordx4 v7, s[68:71], s90 offen lds\n" \
  "s_add_u32 s90, s90, 16384\n" \
  "s_add_u32 m0, s86, 6144\n" \
  "s_nop 0\n" \
  "buffer_load_dwordx4 v7, s[68:71], s90 offen lds\n" \
  "s_add_u32 s90, s90, 16384\n" \
  "s_add_u32 m0, s86, 7168\n" \
  "s_nop 0\n" \
  "buffer_load_dwordx4 v7, s[68:71], s90 offen lds\n" \
  "s_add_u32 s90, s90, 16384\n" \
  "s_add_u32 m0, s86, 8192\n" \
  "s_nop 0\n" \
  "buffer_load_dwordx4 v7, s[68:71], s90 offen lds\n" \
  "s_add_u32 s90, s90, 16384\n" \
  "s_add_u32 m0, s86, 9216\n" \
  "s_nop 0\n" \
  "buffer_load_dwordx4 v7, s[68:71], s90 offen lds\n" \
  "s_add_u32 s90, s90, 16384\n" \
  "s_add_u32 m0, s86, 10240\n" \
  "s_nop 0\n" \
  "buffer_load_dwordx4 v7, s[68:71], s90 offen lds\n" \
  "s_add_u32 s90, s90, 16384\n" \
  "s_add_u32 m0, s86, 11264\n" \
  "s_nop 0\n" \
  "buffer_load_dwordx4 v7, s[68:71], s90 offen lds\n" \
  "s_add_u32 s90, s90, 16384\n" \
  "s_add_u32 m0, s86, 12288\n" \
  "s_nop 0\n" \
  "buffer_load_dwordx4 v7, s[68:71], s90 offen lds\n" \
  "s_add_u32 s90, s90, 16384\n" \
  "s_add_u32 m0, s86, 13312\n" \
  "s_nop 0\n" \
  "buffer_load_dwordx4 v7, s[68:71], s90 offen lds\n" \
  "s_add_u32 s90, s90, 16384\n" \
  "s_add_u32 m0, s86, 14336\n" \
  "s_nop 0\n" \
  "buffer_load_dwordx4 v7, s[68:71], s90 offen lds\n" \
  "s_add_u32 s90, s90, 16384\n" \
  "s_add_u32 m0, s86, 15360\n" \
  "s_nop 0\n" \
  "buffer_load_dwordx4 v7, s[68:71], s90 offen lds\n" \
  "s_add_u32 s90, s90, 16384\n" \
  "s_add_u32 m0, s86, 16384\n" \
  "s_nop 0\n" \
  "buffer_load_dwordx4 v7, s[68:71], s90 offen lds\n" \
  "s_add_u32 s90, s90, 16384\n" \
  "s_add_u32 m0, s86, 17408\n" \
  "s_nop 0\n" \
  "buffer_load_dwordx4 v7, s[68:71], s90 offen lds\n" \
  "s_add_u32 s90, s90, 16384\n" \
  "s_add_u32 m0, s86, 18432\n" \
  "s_nop 0\n" \
  "buffer_load_dwordx4 v7, s[68:71], s90 offen lds\n" \
  "s_add_u32 s90, s90, 16384\n" \
  "s_add_u32 m0, s86, 19456\n" \
  "s_nop 0\n" \
  "buffer_load_dwordx4 v7, s[68:71], s90 offen lds\n" \
  "s_add_u32 s90, s90, 16384\n" \
  "s_add_u32 m0, s86, 20480\n" \
  "s_nop 0\n" \
  "buffer_load_dwordx4 v7, s[68:71], s90 offen lds\n" \
  "s_add_u32 s90, s90, 16384\n" \
  "s_add_u32 m0, s86, 21504\n" \
  "s_nop 0\n" \
  "buffer_load_dwordx4 v7, s[68:71], s90 offen lds\n" \
  "s_add_u32 s90, s90, 16384\n" \
  "s_add_u32 m0, s86, 22528\n" \
  "s_nop 0\n" \
  "buffer_load_dwordx4 v7, s[68:71], s90 offen lds\n" \
  "s_add_u32 s90, s90, 16384\n" \
  "s_add_u32 m0, s86, 23552\n" \
  "s_nop 0\n" \
  "buffer_load_dwordx4 v7, s[68:71], s90 offen lds\n" \
  "s_add_u32 s90, s90, 16384\n" \
  "s_add_u32 m0, s86, 24576\n" \
  "s_nop 0\n" \
  "buffer_load_dwordx4 v7, s[68:71], s90 offen lds\n" \
  "s_add_u32 s90, s90, 16384\n" \
  "s_add_u32 m0, s86, 25600\n" \
  "s_nop 0\n" \
  "buffer_load_dwordx4 v7, s[68:71], s90 offen lds\n" \
  "s_add_u32 s90, s90, 16384\n" \
  "s_add_u32 m0, s86, 26624\n" \
  "s_nop 0\n" \
  "buffer_load_dwordx4 v7, s[68:71], s90 offen lds\n" \
  "s_add_u32 s90, s90, 16384\n" \
  "s_add_u32 m0, s86, 27648\n" \
  "s_nop 0\n" \
  "buffer_load_dwordx4 v7, s[68:71], s90 offen lds\n" \
  "s_add_u32 s90, s90, 16384\n" \
  "s_add_u32 m0, s86, 28672\n" \
  "s_nop 0\n" \
  "buffer_load_dwordx4 v7, s[68:71], s90 offen lds\n" \
  "s_add_u32 s90, s90, 16384\n" \
  "s_add_u32 m0, s86, 29696\n" \
  "s_nop 0\n" \
  "buffer_load_dwordx4 v7, s[68:71], s90 offen lds\n" \
  "s_add_u32 s90, s90, 16384\n" \
  "s_add_u32 m0, s86, 30720\n" \
  "s_nop 0\n" \
  "buffer_load_dwordx4 v7, s[68:71], s90 offen lds\n" \
  "s_add_u32 s90, s90, 16384\n" \
  "s_add_u32 m0, s86, 31744\n" \
  "s_nop 0\n" \
  "buffer_load_dwordx4 v7, s[68:71], s90 offen lds\n" \
  "s_add_u32 s90, s90, 16384\n" \
  "v_lshrrev_b32 v5, 2, v20\n" \
  "v_lshl_add_u32 v5, v21, 2, v5\n" \
  "v_lshlrev_b32 v15, 7, v5\n" \
  "v_lshrrev_b32 v5, 1, v5\n" \
  "v_and_b32 v5, 3, v5\n" \
  "v_lshl_add_u32 v15, v5, 5, v15\n" \
  "v_and_b32 v5, 3, v4\n" \
  "v_lshl_add_u32 v15, v5, 3, v15\n" \
  "v_xor_b32 v5, 0, v15\n" \
  "v_add_u32 v16, s86, v5\n" \
  "v_xor_b32 v5, 32, v15\n" \
  "v_add_u32 v17, s86, v5\n" \
  "v_xor_b32 v5, 64, v15\n" \
  "v_add_u32 v18, s86, v5\n" \
  "v_xor_b32 v5, 96, v15\n" \
  "v_add_u32 v19, s86, v5\n" \
  "s_lshl_b32 s84, %5, 12\n" \
  "s_add_u32 s84, s84, 131072\n" \
  "s_add_u32 s84, s84, %6\n" \
  "v_xor_b32 v5, v21, v20\n" \
  "v_lshlrev_b32 v5, 4, v5\n" \
  "v_lshl_add_u32 v5, v20, 8, v5\n" \
  "v_add_u32 v5, s84, v5\n" \
  "v_xor_b32 v12, 64, v5\n" \
  "v_xor_b32 v13, 128, v5\n" \
  "v_xor_b32 v14, 192, v5\n" \
  "s_lshl_b32 s84, %5, 2\n" \
  "v_add_u32 v5, s84, v21\n" \
  "v_lshlrev_b32 v10, 12, v5\n" \
  "v_lshl_add_u32 v10, v20, 3, v10\n" \
  "v_xor_b32 v15, v5, v20\n" \
  "v_lshlrev_b32 v15, 4, v15\n" \
  "v_lshl_add_u32 v15, v5, 8, v15\n" \
  "s_add_u32 s84, %6, 131072\n" \
  "v_add_u32 v11, s84, v15\n" \
  "s_and_b32 s84, %4, 1\n" \
  "s_lshl_b32 s96, s84, 18\n" \
  "s_lshl_b32 s85, %5, 16\n" \
  "s_add_u32 s96, s96, s85\n" \
  "s_lshl_b32 s88, s84, 16\n" \
  "s_lshr_b32 s84, %4, 1\n" \
  "s_lshl_b32 s84, s84, 7\n" \
  "s_add_u32 s88, s88, s84\n" \
  "v_and_b32 v8, 7, v4\n" \
  "v_lshlrev_b32 v8, 7, v8\n" \
  "s_lshl_b32 s84, %5, 10\n" \
  "s_add_u32 s84, s84, " STR(CW_XPROG) "*4\n" \
  "v_add_u32 v8, s84, v8\n" \
  "s_mov_b32 s91, 0\n" \
  "s_mov_b32 s89, 0\n" \
  "s_lshr_b32 s84, s89, 2\n" \
  "s_xnor_b32 s84, s84, %7\n" \
  "s_and_b32 s84, s84, 1\n" \
  "s_lshl_b32 s84, s84, 13\n" \
  "s_lshl_b32 s87, s89, 19\n" \
  "s_or_b32 s87, s87, s84\n" \
  "s_add_u32 s87, s87, s96\n" \
  "s_waitcnt vmcnt(16)\n" \
  "ds_read_b64_tr_b16 v[128:129], v16 offset:0\n" \
  "ds_read_b64_tr_b16 v[130:131], v16 offset:2048\n" \
  "ds_read_b64_tr_b16 v[132:133], v17 offset:0\n" \
  "ds_read_b64_tr_b16 v[134:135], v17 offset:2048\n" \
  "ds_read_b64_tr_b16 v[136:137], v18 offset:0\n" \
  "ds_read_b64_tr_b16 v[138:139], v18 offset:2048\n" \
  "ds_read_b64_tr_b16 v[140:141], v19 offset:0\n" \
  "ds_read_b64_tr_b16 v[142:143], v19 offset:2048\n" \
  "ds_read_b64_tr_b16 v[144:145], v16 offset:4096\n" \
  "ds_read_b64_tr_b16 v[146:147], v16 offset:6144\n" \
  "ds_read_b64_tr_b16 v[148:149], v17 offset:4096\n" \
  "ds_read_b64_tr_b16 v[150:151], v17 offset:6144\n" \
  "ds_read_b64_tr_b16 v[152:153], v18 offset:4096\n" \
  "ds_read_b64_tr_b16 v[154:155], v18 offset:6144\n" \
  "ds_read_b64_tr_b16 v[156:157], v19 offset:4096\n" \
  "ds_read_b64_tr_b16 v[158:159], v19 offset:6144\n" \
  "ds_read_b64_tr_b16 v[160:161], v16 offset:8192\n" \
  "ds_read_b64_tr_b16 v[162:163], v16 offset:10240\n" \
  "ds_read_b64_tr_b16 v[164:165], v17 offset:8192\n" \
  "ds_read_b64_tr_b16 v[166:167], v17 offset:10240\n" \
  "ds_read_b64_tr_b16 v[168:169], v18 offset:8192\n" \
  "ds_read_b64_tr_b16 v[170:171], v18 offset:10240\n" \
  "ds_read_b64_tr_b16 v[172:173], v19 offset:8192\n" \
  "ds_read_b64_tr_b16 v[174:175], v19 offset:10240\n" \
  "ds_read_b64_tr_b16 v[176:177], v16 offset:12288\n" \
  "ds_read_b64_tr_b16 v[178:179], v16 offset:14336\n" \
  "ds_read_b64_tr_b16 v[180:181], v17 offset:12288\n" \
  "ds_read_b64_tr_b16 v[182:183], v17 offset:14336\n" \
  "ds_read_b64_tr_b16 v[184:185], v18 offset:12288\n" \
  "ds_read_b64_tr_b16 v[186:187], v18 offset:14336\n" \
  "ds_read_b64_tr_b16 v[188:189], v19 offset:12288\n" \
  "ds_read_b64_tr_b16 v[190:191], v19 offset:14336\n" \
  "s_waitcnt lgkmcnt(0)\n" \
  "s_add_u32 m0, s86, 0\n" \
  "s_nop 0\n" \
  "buffer_load_dwordx4 v7, s[68:71], s90 offen lds\n" \
  "s_add_u32 s90, s90, 16384\n" \
  "s_add_u32 m0, s86, 1024\n" \
  "s_nop 0\n" \
  "buffer_load_dwordx4 v7, s[68:71], s90 offen lds\n" \
  "s_add_u32 s90, s90, 16384\n" \
  "s_add_u32 m0, s86, 2048\n" \
  "s_nop 0\n" \
  "buffer_load_dwordx4 v7, s[68:71], s90 offen lds\n" \
  "s_add_u32 s90, s90, 16384\n" \
  "s_add_u32 m0, s86, 3072\n" \
  "s_nop 0\n" \
  "buffer_load_dwordx4 v7, s[68:71], s90 offen lds\n" \
  "s_add_u32 s90, s90, 16384\n" \
  "s_add_u32 m0, s86, 4096\n" \
  "s_nop 0\n" \
  "buffer_load_dwordx4 v7, s[68:71], s90 offen lds\n" \
  "s_add_u32 s90, s90, 16384\n" \
  "s_add_u32 m0, s86, 5120\n" \
  "s_nop 0\n" \
  "buffer_load_dwordx4 v7, s[68:71], s90 offen lds\n" \
  "s_add_u32 s90, s90, 16384\n" \
  "s_add_u32 m0, s86, 6144\n" \
  "s_nop 0\n" \
  "buffer_load_dwordx4 v7, s[68:71], s90 offen lds\n" \
  "s_add_u32 s90, s90, 16384\n" \
  "s_add_u32 m0, s86, 7168\n" \
  "s_nop 0\n" \
  "buffer_load_dwordx4 v7, s[68:71], s90 offen lds\n" \
  "s_add_u32 s90, s90, 16384\n" \
  "s_add_u32 m0, s86, 8192\n" \
  "s_nop 0\n" \
  "buffer_load_dwordx4 v7, s[68:71], s90 offen lds\n" \
  "s_add_u32 s90, s90, 16384\n" \
  "s_add_u32 m0, s86, 9216\n" \
  "s_nop 0\n" \
  "buffer_load_dwordx4 v7, s[68:71], s90 offen lds\n" \
  "s_add_u32 s90, s90, 16384\n" \
  "s_add_u32 m0, s86, 10240\n" \
  "s_nop 0\n" \
  "buffer_load_dwordx4 v7, s[68:71], s90 offen lds\n" \
  "s_add_u32 s90, s90, 16384\n" \
  "s_add_u32 m0, s86, 11264\n" \
  "s_nop 0\n" \
  "buffer_load_dwordx4 v7, s[68:71], s90 offen lds\n" \
  "s_add_u32 s90, s90, 16384\n" \
  "s_add_u32 m0, s86, 12288\n" \
  "s_nop 0\n" \
  "buffer_load_dwordx4 v7, s[68:71], s90 offen lds\n" \
  "s_add_u32 s90, s90, 16384\n" \
  "s_add_u32 m0, s86, 13312\n" \
  "s_nop 0\n" \
  "buffer_load_dwordx4 v7, s[68:71], s90 offen lds\n" \
  "s_add_u32 s90, s90, 16384\n" \
  "s_add_u32 m0, s86, 14336\n" \
  "s_nop 0\n" \
  "buffer_load_dwordx4 v7, s[68:71], s90 offen lds\n" \
  "s_add_u32 s90, s90, 16384\n" \
  "s_add_u32 m0, s86, 15360\n" \
  "s_nop 0\n" \
  "buffer_load_dwordx4 v7, s[68:71], s90 offen lds\n" \
  "s_add_u32 s90, s90, 16384\n" \
  "s_waitcnt vmcnt(16)\n" \
  "ds_read_b64_tr_b16 v[192:193], v16 offset:16384\n" \
  "ds_read_b64_tr_b16 v[194:195], v16 offset:18432\n" \
  "ds_read_b64_tr_b16 v[196:197], v17 offset:16384\n" \
  "ds_read_b64_tr_b16 v[198:199], v17 offset:18432\n" \
  "ds_read_b64_tr_b16 v[200:201], v18 offset:16384\n" \
  "ds_read_b64_tr_b16 v[202:203], v18 offset:18432\n" \
  "ds_read_b64_tr_b16 v[204:205], v19 offset:16384\n" \
  "ds_read_b64_tr_b16 v[206:207], v19 offset:18432\n" \
  "ds_read_b64_tr_b16 v[208:209], v16 offset:20480\n" \
  "ds_read_b64_tr_b16 v[210:211], v16 offset:22528\n" \
  "ds_read_b64_tr_b16 v[212:213], v17 offset:20480\n" \
  "ds_read_b64_tr_b16 v[214:215], v17 offset:22528\n" \
  "ds_read_b64_tr_b16 v[216:217], v18 offset:20480\n" \
  "ds_read_b64_tr_b16 v[218:219], v18 offset:22528\n" \
  "ds_read_b64_tr_b16 v[220:221], v19 offset:20480\n" \
  "ds_read_b64_tr_b16 v[222:223], v19 offset:22528\n" \
  "ds_read_b64_tr_b16 v[224:225], v16 offset:24576\n" \
  "ds_read_b64_tr_b16 v[226:227], v16 offset:26624\n" \
  "ds_read_b64_tr_b16 v[228:229], v17 offset:24576\n" \
  "ds_read_b64_tr_b16 v[230:231], v17 offset:26624\n" \
  "ds_read_b64_tr_b16 v[232:233], v18 offset:24576\n" \
  "ds_read_b64_tr_b16 v[234:235], v18 offset:26624\n" \
  "ds_read_b64_tr_b16 v[236:237], v19 offset:24576\n" \
  "ds_read_b64_tr_b16 v[238:239], v19 offset:26624\n" \
  "ds_read_b64_tr_b16 v[240:241], v16 offset:28672\n" \
  "ds_read_b64_tr_b16 v[242:243], v16 offset:30720\n" \
  "ds_read_b64_tr_b16 v[244:245], v17 offset:28672\n" \
  "ds_read_b64_tr_b16 v[246:247], v17 offset:30720\n" \
  "ds_read_b64_tr_b16 v[248:249], v18 offset:28672\n" \
  "ds_read_b64_tr_b16 v[250:251], v18 offset:30720\n" \
  "ds_read_b64_tr_b16 v[252:253], v19 offset:28672\n" \
  "ds_read_b64_tr_b16 v[254:255], v19 offset:30720\n" \
  "s_waitcnt lgkmcnt(0)\n" \
  "s_add_u32 m0, s86, 16384\n" \
  "s_nop 0\n" \
  "buffer_load_dwordx4 v7, s[68:71], s90 offen lds\n" \
  "s_add_u32 s90, s90, 16384\n" \
  "s_add_u32 m0, s86, 17408\n" \
  "s_nop 0\n" \
  "buffer_load_dwordx4 v7, s[68:71], s90 offen lds\n" \
  "s_add_u32 s90, s90, 16384\n" \
  "s_add_u32 m0, s86, 18432\n" \
  "s_nop 0\n" \
  "buffer_load_dwordx4 v7, s[68:71], s90 offen lds\n" \
  "s_add_u32 s90, s90, 16384\n" \
  "s_add_u32 m0, s86, 19456\n" \
  "s_nop 0\n" \
  "buffer_load_dwordx4 v7, s[68:71], s90 offen lds\n" \
  "s_add_u32 s90, s90, 16384\n" \
  "s_add_u32 m0, s86, 20480\n" \
  "s_nop 0\n" \
  "buffer_load_dwordx4 v7, s[68:71], s90 offen lds\n" \
  "s_add_u32 s90, s90, 16384\n" \
  "s_add_u32 m0, s86, 21504\n" \
  "s_nop 0\n" \
  "buffer_load_dwordx4 v7, s[68:71], s90 offen lds\n" \
  "s_add_u32 s90, s90, 16384\n" \
  "s_add_u32 m0, s86, 22528\n" \
  "s_nop 0\n" \
  "buffer_load_dwordx4 v7, s[68:71], s90 offen lds\n" \
  "s_add_u32 s90, s90, 16384\n" \
  "s_add_u32 m0, s86, 23552\n" \
  "s_nop 0\n" \
  "buffer_load_dwordx4 v7, s[68:71], s90 offen lds\n" \
  "s_add_u32 s90, s90, 16384\n" \
  "s_add_u32 m0, s86, 24576\n" \
  "s_nop 0\n" \
  "buffer_load_dwordx4 v7, s[68:71], s90 offen lds\n" \
  "s_add_u32 s90, s90, 16384\n" \
  "s_add_u32 m0, s86, 25600\n" \
  "s_nop 0\n" \
  "buffer_load_dwordx4 v7, s[68:71], s90 offen lds\n" \
  "s_add_u32 s90, s90, 16384\n" \
  "s_add_u32 m0, s86, 26624\n" \
  "s_nop 0\n" \
  "buffer_load_dwordx4 v7, s[68:71], s90 offen lds\n" \
  "s_add_u32 s90, s90, 16384\n" \
  "s_add_u32 m0, s86, 27648\n" \
  "s_nop 0\n" \
  "buffer_load_dwordx4 v7, s[68:71], s90 offen lds\n" \
  "s_add_u32 s90, s90, 16384\n" \
  "s_add_u32 m0, s86, 28672\n" \
  "s_nop 0\n" \
  "buffer_load_dwordx4 v7, s[68:71], s90 offen lds\n" \
  "s_add_u32 s90, s90, 16384\n" \
  "s_add_u32 m0, s86, 29696\n" \
  "s_nop 0\n" \
  "buffer_load_dwordx4 v7, s[68:71], s90 offen lds\n" \
  "s_add_u32 s90, s90, 16384\n" \
  "s_add_u32 m0, s86, 30720\n" \
  "s_nop 0\n" \
  "buffer_load_dwordx4 v7, s[68:71], s90 offen lds\n" \
  "s_add_u32 s90, s90, 16384\n" \
  "s_add_u32 m0, s86, 31744\n" \
  "s_nop 0\n" \
  "buffer_load_dwordx4 v7, s[68:71], s90 offen lds\n" \
  "s_add_u32 s90, s90, 16384\n" \
  "s_waitcnt vmcnt(16)\n" \
  "ds_read_b64_tr_b16 a[0:1], v16 offset:0\n" \
  "ds_read_b64_tr_b16 a[2:3], v16 offset:2048\n" \
  "ds_read_b64_tr_b16 a[4:5], v17 offset:0\n" \
  "ds_read_b64_tr_b16 a[6:7], v17 offset:2048\n" \
  "ds_read_b64_tr_b16 a[8:9], v18 offset:0\n" \
  "ds_read_b64_tr_b16 a[10:11], v18 offset:2048\n" \
  "ds_read_b64_tr_b16 a[12:13], v19 offset:0\n" \
  "ds_read_b64_tr_b16 a[14:15], v19 offset:2048\n" \
  "ds_read_b64_tr_b16 a[16:17], v16 offset:4096\n" \
  "ds_read_b64_tr_b16 a[18:19], v16 offset:6144\n" \
  "ds_read_b64_tr_b16 a[20:21], v17 offset:4096\n" \
  "ds_read_b64_tr_b16 a[22:23], v17 offset:6144\n" \
  "ds_read_b64_tr_b16 a[24:25], v18 offset:4096\n" \
  "ds_read_b64_tr_b16 a[26:27], v18 offset:6144\n" \
  "ds_read_b64_tr_b16 a[28:29], v19 offset:4096\n" \
  "ds_read_b64_tr_b16 a[30:31], v19 offset:6144\n" \
  "ds_read_b64_tr_b16 a[32:33], v16 offset:8192\n" \
  "ds_read_b64_tr_b16 a[34:35], v16 offset:10240\n" \
  "ds_read_b64_tr_b16 a[36:37], v17 offset:8192\n" \
  "ds_read_b64_tr_b16 a[38:39], v17 offset:10240\n" \
  "ds_read_b64_tr_b16 a[40:41], v18 offset:8192\n" \
  "ds_read_b64_tr_b16 a[42:43], v18 offset:10240\n" \
  "ds_read_b64_tr_b16 a[44:45], v19 offset:8192\n" \
  "ds_read_b64_tr_b16 a[46:47], v19 offset:10240\n" \
  "ds_read_b64_tr_b16 a[48:49], v16 offset:12288\n" \
  "ds_read_b64_tr_b16 a[50:51], v16 offset:14336\n" \
  "ds_read_b64_tr_b16 a[52:53], v17 offset:12288\n" \
  "ds_read_b64_tr_b16 a[54:55], v17 offset:14336\n" \
  "ds_read_b64_tr_b16 a[56:57], v18 offset:12288\n" \
  "ds_read_b64_tr_b16 a[58:59], v18 offset:14336\n" \
  "ds_read_b64_tr_b16 a[60:61], v19 offset:12288\n" \
  "ds_read_b64_tr_b16 a[62:63], v19 offset:14336\n" \
  "s_waitcnt lgkmcnt(0)\n" \
  "s_add_u32 m0, s86, 0\n" \
  "s_nop 0\n" \
  "buffer_load_dwordx4 v7, s[68:71], s90 offen lds\n" \
  "s_add_u32 s90, s90, 16384\n" \
  "s_add_u32 m0, s86, 1024\n" \
  "s_nop 0\n" \
  "buffer_load_dwordx4 v7, s[68:71], s90 offen lds\n" \
  "s_add_u32 s90, s90, 16384\n" \
  "s_add_u32 m0, s86, 2048\n" \
  "s_nop 0\n" \
  "buffer_load_dwordx4 v7, s[68:71], s90 offen lds\n" \
  "s_add_u32 s90, s90, 16384\n" \
  "s_add_u32 m0, s86, 3072\n" \
  "s_nop 0\n" \
  "buffer_load_dwordx4 v7, s[68:71], s90 offen lds\n" \
  "s_add_u32 s90, s90, 16384\n" \
  "s_add_u32 m0, s86, 4096\n" \
  "s_nop 0\n" \
  "buffer_load_dwordx4 v7, s[68:71], s90 offen lds\n" \
  "s_add_u32 s90, s90, 16384\n" \
  "s_add_u32 m0, s86, 5120\n" \
  "s_nop 0\n" \
  "buffer_load_dwordx4 v7, s[68:71], s90 offen lds\n" \
  "s_add_u32 s90, s90, 16384\n" \
  "s_add_u32 m0, s86, 6144\n" \
  "s_nop 0\n" \
  "buffer_load_dwordx4 v7, s[68:71], s90 offen lds\n" \
  "s_add_u32 s90, s90, 16384\n" \
  "s_add_u32 m0, s86, 7168\n" \
  "s_nop 0\n" \
  "buffer_load_dwordx4 v7, s[68:71], s90 offen lds\n" \
  "s_add_u32 s90, s90, 16384\n" \
  "s_add_u32 m0, s86, 8192\n" \
  "s_nop 0\n" \
  "buffer_load_dwordx4 v7, s[68:71], s90 offen lds\n" \
  "s_add_u32 s90, s90, 16384\n" \
  "s_add_u32 m0, s86, 9216\n" \
  "s_nop 0\n" \
  "buffer_load_dwordx4 v7, s[68:71], s90 offen lds\n" \
  "s_add_u32 s90, s90, 16384\n" \
  "s_add_u32 m0, s86, 10240\n" \
  "s_nop 0\n" \
  "buffer_load_dwordx4 v7, s[68:71], s90 offen lds\n" \
  "s_add_u32 s90, s90, 16384\n" \
  "s_add_u32 m0, s86, 11264\n" \
  "s_nop 0\n" \
  "buffer_load_dwordx4 v7, s[68:71], s90 offen lds\n" \
  "s_add_u32 s90, s90, 16384\n" \
  "s_add_u32 m0, s86, 12288\n" \
  "s_nop 0\n" \
  "buffer_load_dwordx4 v7, s[68:71], s90 offen lds\n" \
  "s_add_u32 s90, s90, 16384\n" \
  "s_add_u32 m0, s86, 13312\n" \
  "s_nop 0\n" \
  "buffer_load_dwordx4 v7, s[68:71], s90 offen lds\n" \
  "s_add_u32 s90, s90, 16384\n" \
  "s_add_u32 m0, s86, 14336\n" \
  "s_nop 0\n" \
  "buffer_load_dwordx4 v7, s[68:71], s90 offen lds\n" \
  "s_add_u32 s90, s90, 16384\n" \
  "s_add_u32 m0, s86, 15360\n" \
  "s_nop 0\n" \
  "buffer_load_dwordx4 v7, s[68:71], s90 offen lds\n" \
  "s_add_u32 s90, s90, 16384\n" \
  "s_waitcnt vmcnt(16)\n" \
  "ds_read_b64_tr_b16 a[64:65], v16 offset:16384\n" \
  "ds_read_b64_tr_b16 a[66:67], v16 offset:18432\n" \
  "ds_read_b64_tr_b16 a[68:69], v17 offset:16384\n" \
  "ds_read_b64_tr_b16 a[70:71], v17 offset:18432\n" \
  "ds_read_b64_tr_b16 a[72:73], v18 offset:16384\n" \
  "ds_read_b64_tr_b16 a[74:75], v18 offset:18432\n" \
  "ds_read_b64_tr_b16 a[76:77], v19 offset:16384\n" \
  "ds_read_b64_tr_b16 a[78:79], v19 offset:18432\n" \
  "ds_read_b64_tr_b16 a[80:81], v16 offset:20480\n" \
  "ds_read_b64_tr_b16 a[82:83], v16 offset:22528\n" \
  "ds_read_b64_tr_b16 a[84:85], v17 offset:20480\n" \
  "ds_read_b64_tr_b16 a[86:87], v17 offset:22528\n" \
  "ds_read_b64_tr_b16 a[88:89], v18 offset:20480\n" \
  "ds_read_b64_tr_b16 a[90:91], v18 offset:22528\n" \
  "ds_read_b64_tr_b16 a[92:93], v19 offset:20480\n" \
  "ds_read_b64_tr_b16 a[94:95], v19 offset:22528\n" \
  "ds_read_b64_tr_b16 a[96:97], v16 offset:24576\n" \
  "ds_read_b64_tr_b16 a[98:99], v16 offset:26624\n" \
  "ds_read_b64_tr_b16 a[100:101], v17 offset:24576\n" \
  "ds_read_b64_tr_b16 a[102:103], v17 offset:26624\n" \
  "ds_read_b64_tr_b16 a[104:105], v18 offset:24576\n" \
  "ds_read_b64_tr_b16 a[106:107], v18 offset:26624\n" \
  "ds_read_b64_tr_b16 a[108:109], v19 offset:24576\n" \
  "ds_read_b64_tr_b16 a[110:111], v19 offset:26624\n" \
  "ds_read_b64_tr_b16 a[112:113], v16 offset:28672\n" \
  "ds_read_b64_tr_b16 a[114:115], v16 offset:30720\n" \
  "ds_read_b64_tr_b16 a[116:117], v17 offset:28672\n" \
  "ds_read_b64_tr_b16 a[118:119], v17 offset:30720\n" \
  "ds_read_b64_tr_b16 a[120:121], v18 offset:28672\n" \
  "ds_read_b64_tr_b16 a[122:123], v18 offset:30720\n" \
  "ds_read_b64_tr_b16 a[124:125], v19 offset:28672\n" \
  "ds_read_b64_tr_b16 a[126:127], v19 offset:30720\n" \
  "s_waitcnt lgkmcnt(0)\n" \
  "s_add_u32 m0, s86, 16384\n" \
  "s_nop 0\n" \
  "buffer_load_dwordx4 v7, s[68:71], s90 offen lds\n" \
  "s_add_u32 s90, s90, 16384\n" \
  "s_add_u32 m0, s86, 17408\n" \
  "s_nop 0\n" \
  "buffer_load_dwordx4 v7, s[68:71], s90 offen lds\n" \
  "s_add_u32 s90, s90, 16384\n" \
  "s_add_u32 m0, s86, 18432\n" \
  "s_nop 0\n" \
  "buffer_load_dwordx4 v7, s[68:71], s90 offen lds\n" \
  "s_add_u32 s90, s90, 16384\n" \
  "s_add_u32 m0, s86, 19456\n" \
  "s_nop 0\n" \
  "buffer_load_dwordx4 v7, s[68:71], s90 offen lds\n" \
  "s_add_u32 s90, s90, 16384\n" \
  "s_add_u32 m0, s86, 20480\n" \
  "s_nop 0\n" \
  "buffer_load_dwordx4 v7, s[68:71], s90 offen lds\n" \
  "s_add_u32 s90, s90, 16384\n" \
  "s_add_u32 m0, s86, 21504\n" \
  "s_nop 0\n" \
  "buffer_load_dwordx4 v7, s[68:71], s90 offen lds\n" \
  "s_add_u32 s90, s90, 16384\n" \
  "s_add_u32 m0, s86, 22528\n" \
  "s_nop 0\n" \
  "buffer_load_dwordx4 v7, s[68:71], s90 offen lds\n" \
  "s_add_u32 s90, s90, 16384\n" \
  "s_add_u32 m0, s86, 23552\n" \
  "s_nop 0\n" \
  "buffer_load_dwordx4 v7, s[68:71], s90 offen lds\n" \
  "s_add_u32 s90, s90, 16384\n" \
  "s_add_u32 m0, s86, 24576\n" \
  "s_nop 0\n" \
  "buffer_load_dwordx4 v7, s[68:71], s90 offen lds\n" \
  "s_add_u32 s90, s90, 16384\n" \
  "s_add_u32 m0, s86, 25600\n" \
  "s_nop 0\n" \
  "buffer_load_dwordx4 v7, s[68:71], s90 offen lds\n" \
  "s_add_u32 s90, s90, 16384\n" \
  "s_add_u32 m0, s86, 26624\n" \
  "s_nop 0\n" \
  "buffer_load_dwordx4 v7, s[68:71], s90 offen lds\n" \
  "s_add_u32 s90, s90, 16384\n" \
  "s_add_u32 m0, s86, 27648\n" \
  "s_nop 0\n" \
  "buffer_load_dwordx4 v7, s[68:71], s90 offen lds\n" \
  "s_add_u32 s90, s90, 16384\n" \
  "s_add_u32 m0, s86, 28672\n" \
  "s_nop 0\n" \
  "buffer_load_dwordx4 v7, s[68:71], s90 offen lds\n" \
  "s_add_u32 s90, s90, 16384\n" \
  "s_add_u32 m0, s86, 29696\n" \
  "s_nop 0\n" \
  "buffer_load_dwordx4 v7, s[68:71], s90 offen lds\n" \
  "s_add_u32 s90, s90, 16384\n" \
  "s_add_u32 m0, s86, 30720\n" \
  "s_nop 0\n" \
  "buffer_load_dwordx4 v7, s[68:71], s90 offen lds\n" \
  "s_add_u32 s90, s90, 16384\n" \
  "s_add_u32 m0, s86, 31744\n" \
  "s_nop 0\n" \
  "buffer_load_dwordx4 v7, s[68:71], s90 offen lds\n" \
  "s_add_u32 s90, s90, 16384\n" \
  "s_waitcnt vmcnt(16)\n" \
  "ds_read_b64_tr_b16 a[128:129], v16 offset:0\n" \
  "ds_read_b64_tr_b16 a[130:131], v16 offset:2048\n" \
  "ds_read_b64_tr_b16 a[132:133], v17 offset:0\n" \
  "ds_read_b64_tr_b16 a[134:135], v17 offset:2048\n" \
  "ds_read_b64_tr_b16 a[136:137], v18 offset:0\n" \
  "ds_read_b64_tr_b16 a[138:139], v18 offset:2048\n" \
  "ds_read_b64_tr_b16 a[140:141], v19 offset:0\n" \
  "ds_read_b64_tr_b16 a[142:143], v19 offset:2048\n" \
  "ds_read_b64_tr_b16 a[144:145], v16 offset:4096\n" \
  "ds_read_b64_tr_b16 a[146:147], v16 offset:6144\n" \
  "ds_read_b64_tr_b16 a[148:149], v17 offset:4096\n" \
  "ds_read_b64_tr_b16 a[150:151], v17 offset:6144\n" \
  "ds_read_b64_tr_b16 a[152:153], v18 offset:4096\n" \
  "ds_read_b64_tr_b16 a[154:155], v18 offset:6144\n" \
  "ds_read_b64_tr_b16 a[156:157], v19 offset:4096\n" \
  "ds_read_b64_tr_b16 a[158:159], v19 offset:6144\n" \
  "ds_read_b64_tr_b16 a[160:161], v16 offset:8192\n" \
  "ds_read_b64_tr_b16 a[162:163], v16 offset:10240\n" \
  "ds_read_b64_tr_b16 a[164:165], v17 offset:8192\n" \
  "ds_read_b64_tr_b16 a[166:167], v17 offset:10240\n" \
  "ds_read_b64_tr_b16 a[168:169], v18 offset:8192\n" \
  "ds_read_b64_tr_b16 a[170:171], v18 offset:10240\n" \
  "ds_read_b64_tr_b16 a[172:173], v19 offset:8192\n" \
  "ds_read_b64_tr_b16 a[174:175], v19 offset:10240\n" \
  "ds_read_b64_tr_b16 a[176:177], v16 offset:12288\n" \
  "ds_read_b64_tr_b16 a[178:179], v16 offset:14336\n" \
  "ds_read_b64_tr_b16 a[180:181], v17 offset:12288\n" \
  "ds_read_b64_tr_b16 a[182:183], v17 offset:14336\n" \
  "ds_read_b64_tr_b16 a[184:185], v18 offset:12288\n" \
  "ds_read_b64_tr_b16 a[186:187], v18 offset:14336\n" \
  "ds_read_b64_tr_b16 a[188:189], v19 offset:12288\n" \
  "ds_read_b64_tr_b16 a[190:191], v19 offset:14336\n" \
  "s_waitcnt lgkmcnt(0)\n" \
  "s_add_u32 m0, s86, 0\n" \
  "s_nop 0\n" \
  "buffer_load_dwordx4 v7, s[68:71], s90 offen lds\n" \
  "s_add_u32 s90, s90, 16384\n" \
  "s_add_u32 m0, s86, 1024\n" \
  "s_nop 0\n" \
  "buffer_load_dwordx4 v7, s[68:71], s90 offen lds\n" \
  "s_add_u32 s90, s90, 16384\n" \
  "s_add_u32 m0, s86, 2048\n" \
  "s_nop 0\n" \
  "buffer_load_dwordx4 v7, s[68:71], s90 offen lds\n" \
  "s_add_u32 s90, s90, 16384\n" \
  "s_add_u32 m0, s86, 3072\n" \
  "s_nop 0\n" \
  "buffer_load_dwordx4 v7, s[68:71], s90 offen lds\n" \
  "s_add_u32 s90, s90, 16384\n" \
  "s_add_u32 m0, s86, 4096\n" \
  "s_nop 0\n" \
  "buffer_load_dwordx4 v7, s[68:71], s90 offen lds\n" \
  "s_add_u32 s90, s90, 16384\n" \
  "s_add_u32 m0, s86, 5120\n" \
  "s_nop 0\n" \
  "buffer_load_dwordx4 v7, s[68:71], s90 offen lds\n" \
  "s_add_u32 s90, s90, 16384\n" \
  "s_add_u32 m0, s86, 6144\n" \
  "s_nop 0\n" \
  "buffer_load_dwordx4 v7, s[68:71], s90 offen lds\n" \
  "s_add_u32 s90, s90, 16384\n" \
  "s_add_u32 m0, s86, 7168\n" \
  "s_nop 0\n" \
  "buffer_load_dwordx4 v7, s[68:71], s90 offen lds\n" \
  "s_add_u32 s90, s90, 16384\n" \
  "s_add_u32 m0, s86, 8192\n" \
  "s_nop 0\n" \
  "buffer_load_dwordx4 v7, s[68:71], s90 offen lds\n" \
  "s_add_u32 s90, s90, 16384\n" \
  "s_add_u32 m0, s86, 9216\n" \
  "s_nop 0\n" \
  "buffer_load_dwordx4 v7, s[68:71], s90 offen lds\n" \
  "s_add_u32 s90, s90, 16384\n" \
  "s_add_u32 m0, s86, 10240\n" \
  "s_nop 0\n" \
  "buffer_load_dwordx4 v7, s[68:71], s90 offen lds\n" \
  "s_add_u32 s90, s90, 16384\n" \
  "s_add_u32 m0, s86, 11264\n" \
  "s_nop 0\n" \
  "buffer_load_dwordx4 v7, s[68:71], s90 offen lds\n" \
  "s_add_u32 s90, s90, 16384\n" \
  "s_add_u32 m0, s86, 12288\n" \
  "s_nop 0\n" \
  "buffer_load_dwordx4 v7, s[68:71], s90 offen lds\n" \
  "s_add_u32 s90, s90, 16384\n" \
  "s_add_u32 m0, s86, 13312\n" \
  "s_nop 0\n" \
  "buffer_load_dwordx4 v7, s[68:71], s90 offen lds\n" \
  "s_add_u32 s90, s90, 16384\n" \
  "s_add_u32 m0, s86, 14336\n" \
  "s_nop 0\n" \
  "buffer_load_dwordx4 v7, s[68:71], s90 offen lds\n" \
  "s_add_u32 s90, s90, 16384\n" \
  "s_add_u32 m0, s86, 15360\n" \
  "s_nop 0\n" \
  "buffer_load_dwordx4 v7, s[68:71], s90 offen lds\n" \
  "s_add_u32 s90, s90, 16384\n" \
  "s_waitcnt vmcnt(16)\n" \
  "ds_read_b64_tr_b16 a[192:193], v16 offset:16384\n" \
  "ds_read_b64_tr_b16 a[194:195], v16 offset:18432\n" \
  "ds_read_b64_tr_b16 a[196:197], v17 offset:16384\n" \
  "ds_read_b64_tr_b16 a[198:199], v17 offset:18432\n" \
  "ds_read_b64_tr_b16 a[200:201], v18 offset:16384\n" \
  "ds_read_b64_tr_b16 a[202:203], v18 offset:18432\n" \
  "ds_read_b64_tr_b16 a[204:205], v19 offset:16384\n" \
  "ds_read_b64_tr_b16 a[206:207], v19 offset:18432\n" \
  "ds_read_b64_tr_b16 a[208:209], v16 offset:20480\n" \
  "ds_read_b64_tr_b16 a[210:211], v16 offset:22528\n" \
  "ds_read_b64_tr_b16 a[212:213], v17 offset:20480\n" \
  "ds_read_b64_tr_b16 a[214:215], v17 offset:22528\n" \
  "ds_read_b64_tr_b16 a[216:217], v18 offset:20480\n" \
  "ds_read_b64_tr_b16 a[218:219], v18 offset:22528\n" \
  "ds_read_b64_tr_b16 a[220:221], v19 offset:20480\n" \
  "ds_read_b64_tr_b16 a[222:223], v19 offset:22528\n" \
  "ds_read_b64_tr_b16 a[224:225], v16 offset:24576\n" \
  "ds_read_b64_tr_b16 a[226:227], v16 offset:26624\n" \
  "ds_read_b64_tr_b16 a[228:229], v17 offset:24576\n" \
  "ds_read_b64_tr_b16 a[230:231], v17 offset:26624\n" \
  "ds_read_b64_tr_b16 a[232:233], v18 offset:24576\n" \
  "ds_read_b64_tr_b16 a[234:235], v18 offset:26624\n" \
  "ds_read_b64_tr_b16 a[236:237], v19 offset:24576\n" \
  "ds_read_b64_tr_b16 a[238:239], v19 offset:26624\n" \
  "ds_read_b64_tr_b16 a[240:241], v16 offset:28672\n" \
  "ds_read_b64_tr_b16 a[242:243], v16 offset:30720\n" \
  "ds_read_b64_tr_b16 a[244:245], v17 offset:28672\n" \
  "ds_read_b64_tr_b16 a[246:247], v17 offset:30720\n" \
  "ds_read_b64_tr_b16 a[248:249], v18 offset:28672\n" \
  "ds_read_b64_tr_b16 a[250:251], v18 offset:30720\n" \
  "ds_read_b64_tr_b16 a[252:253], v19 offset:28672\n" \
  "ds_read_b64_tr_b16 a[254:255], v19 offset:30720\n" \
  "s_waitcnt lgkmcnt(0)\n" \
  "s_add_u32 m0, s86, 16384\n" \
  "s_nop 0\n" \
  "buffer_load_dwordx4 v7, s[68:71], s90 offen lds\n" \
  "s_add_u32 s90, s90, 16384\n" \
  "s_add_u32 m0, s86, 17408\n" \
  "s_nop 0\n" \
  "buffer_load_dwordx4 v7, s[68:71], s90 offen lds\n" \
  "s_add_u32 s90, s90, 16384\n" \
  "s_add_u32 m0, s86, 18432\n" \
  "s_nop 0\n" \
  "buffer_load_dwordx4 v7, s[68:71], s90 offen lds\n" \
  "s_add_u32 s90, s90, 16384\n" \
  "s_add_u32 m0, s86, 19456\n" \
  "s_nop 0\n" \
  "buffer_load_dwordx4 v7, s[68:71], s90 offen lds\n" \
  "s_add_u32 s90, s90, 16384\n" \
  "s_add_u32 m0, s86, 20480\n" \
  "s_nop 0\n" \
  "buffer_load_dwordx4 v7, s[68:71], s90 offen lds\n" \
  "s_add_u32 s90, s90, 16384\n" \
  "s_add_u32 m0, s86, 21504\n" \
  "s_nop 0\n" \
  "buffer_load_dwordx4 v7, s[68:71], s90 offen lds\n" \
  "s_add_u32 s90, s90, 16384\n" \
  "s_add_u32 m0, s86, 22528\n" \
  "s_nop 0\n" \
  "buffer_load_dwordx4 v7, s[68:71], s90 offen lds\n" \
  "s_add_u32 s90, s90, 16384\n" \
  "s_add_u32 m0, s86, 23552\n" \
  "s_nop 0\n" \
  "buffer_load_dwordx4 v7, s[68:71], s90 offen lds\n" \
  "s_add_u32 s90, s90, 16384\n" \
  "s_add_u32 m0, s86, 24576\n" \
  "s_nop 0\n" \
  "buffer_load_dwordx4 v7, s[68:71], s90 offen lds\n" \
  "s_add_u32 s90, s90, 16384\n" \
  "s_add_u32 m0, s86, 25600\n" \
  "s_nop 0\n" \
  "buffer_load_dwordx4 v7, s[68:71], s90 offen lds\n" \
  "s_add_u32 s90, s90, 16384\n" \
  "s_add_u32 m0, s86, 26624\n" \
  "s_nop 0\n" \
  "buffer_load_dwordx4 v7, s[68:71], s90 offen lds\n" \
  "s_add_u32 s90, s90, 16384\n" \
  "s_add_u32 m0, s86, 27648\n" \
  "s_nop 0\n" \
  "buffer_load_dwordx4 v7, s[68:71], s90 offen lds\n" \
  "s_add_u32 s90, s90, 16384\n" \
  "s_add_u32 m0, s86, 28672\n" \
  "s_nop 0\n" \
  "buffer_load_dwordx4 v7, s[68:71], s90 offen lds\n" \
  "s_add_u32 s90, s90, 16384\n" \
  "s_add_u32 m0, s86, 29696\n" \
  "s_nop 0\n" \
  "buffer_load_dwordx4 v7, s[68:71], s90 offen lds\n" \
  "s_add_u32 s90, s90, 16384\n" \
  "s_add_u32 m0, s86, 30720\n" \
  "s_nop 0\n" \
  "buffer_load_dwordx4 v7, s[68:71], s90 offen lds\n" \
  "s_add_u32 s90, s90, 16384\n" \
  "s_add_u32 m0, s86, 31744\n" \
  "s_nop 0\n" \
  "buffer_load_dwordx4 v7, s[68:71], s90 offen lds\n" \
  "s_add_u32 s90, s90, 16384\n" \
  "s_mov_b32 s92, 1\n" \
  "v_xor_b32 v7, 64, v12\n" \
  "LP0_%=:\n" \
  "buffer_load_dword v9, v8, s[80:83], 0 offen sc1\n" \
  "s_waitcnt vmcnt(0)\n" \
  "v_cmp_gt_u32 vcc, s92, v9\n" \
  "s_cbranch_vccz LP1_%=\n" \
  "s_sleep 8\n" \
  "s_add_u32 s91, s91, 1\n" \
  "s_cmp_lt_u32 s91, " STR(SPIN_CAP) "\n" \
  "s_cbranch_scc1 LP0_%=\n" \
  "v_mov_b32 v5, " STR(CW_DEAD) "*4\n" \
  "v_mov_b32 v15, 1\n" \
  "buffer_store_dword v15, v5, s[80:83], 0 offen sc1\n" \
  "s_branch LP1_%=\n" \
  "LP1_%=:\n" \
  "s_add_u32 s84, s87, 0\n" \
  "buffer_load_dwordx4 v[64:67], v6, s[72:75], s84 offen sc1\n" \
  "s_add_u32 s84, s87, 1024\n" \
  "buffer_load_dwordx4 v[68:71], v6, s[72:75], s84 offen sc1\n" \
  "s_add_u32 s84, s87, 2048\n" \
  "buffer_load_dwordx4 v[72:75], v6, s[72:75], s84 offen sc1\n" \
  "s_add_u32 s84, s87, 49152\n" \
  "buffer_load_dwordx4 v[76:79], v6, s[72:75], s84 offen sc1\n" \
  "s_add_u32 s84, s87, 3072\n" \
  "buffer_load_dwordx4 v[80:83], v6, s[72:75], s84 offen sc1\n" \
  "s_add_u32 s84, s87, 4096\n" \
  "buffer_load_dwordx4 v[84:87], v6, s[72:75], s84 offen sc1\n" \
  "s_add_u32 s84, s87, 5120\n" \
  "buffer_load_dwordx4 v[88:91], v6, s[72:75], s84 offen sc1\n" \
  "s_add_u32 s84, s87, 50176\n" \
  "buffer_load_dwordx4 v[92:95], v6, s[72:75], s84 offen sc1\n" \
  "s_add_u32 s84, s87, 6144\n" \
  "buffer_load_dwordx4 v[96:99], v6, s[72:75], s84 offen sc1\n" \
  "s_add_u32 s84, s87, 7168\n" \
  "buffer_load_dwordx4 v[100:103], v6, s[72:75], s84 offen sc1\n" \
  "s_add_u32 s84, s87, 16384\n" \
  "buffer_load_dwordx4 v[104:107], v6, s[72:75], s84 offen sc1\n" \
  "s_add_u32 s84, s87, 51200\n" \
  "buffer_load_dwordx4 v[108:111], v6, s[72:75], s84 offen sc1\n" \
  "s_add_u32 s84, s87, 17408\n" \
  "buffer_load_dwordx4 v[112:115], v6, s[72:75], s84 offen sc1\n" \
  "s_add_u32 s84, s87, 18432\n" \
  "buffer_load_dwordx4 v[116:119], v6, s[72:75], s84 offen sc1\n" \
  "s_add_u32 s84, s87, 19456\n" \
  "buffer_load_dwordx4 v[120:123], v6, s[72:75], s84 offen sc1\n" \
  "s_add_u32 s84, s87, 52224\n" \
  "buffer_load_dwordx4 v[124:127], v6, s[72:75], s84 offen sc1\n" \
  "v_or_b32 v10, 0x80000000, v10\n" \
  "s_sub_u32 s88, s88, 131072\n" \
  ".p2align 6\n" \
  "LX_%=:\n" \
  "s_waitcnt vmcnt(15)\n" \
  "v_mfma_f32_16x16x32_bf16 v[20:23], v[128:131], v[64:67], 0\n" \
  "v_mfma_f32_16x16x32_bf16 v[24:27], v[132:135], v[64:67], 0\n" \
  "v_mfma_f32_16x16x32_bf16 v[28:31], v[136:139], v[64:67], 0\n" \
  "v_mfma_f32_16x16x32_bf16 v[32:35], v[140:143], v[64:67], 0\n" \
  "s_add_u32 s84, s87, 20480\n" \
  "buffer_load_dwordx4 v[64:67], v6, s[72:75], s84 offen sc1\n" \
  "s_waitcnt vmcnt(15)\n" \
  "v_mfma_f32_16x16x32_bf16 v[20:23], v[144:147], v[68:71], v[20:23]\n" \
  "v_mfma_f32_16x16x32_bf16 v[24:27], v[148:151], v[68:71], v[24:27]\n" \
  "v_mfma_f32_16x16x32_bf16 v[28:31], v[152:155], v[68:71], v[28:31]\n" \
  "v_mfma_f32_16x16x32_bf16 v[32:35], v[156:159], v[68:71], v[32:35]\n" \
  "s_add_u32 s84, s87, 21504\n" \
  "buffer_load_dwordx4 v[68:71], v6, s[72:75], s84 offen sc1\n" \
  "s_waitcnt vmcnt(15)\n" \
  "v_mfma_f32_16x16x32_bf16 v[20:23], v[160:163], v[72:75], v[20:23]\n" \
  "ds_read_b64_tr_b16 v[40:41], v16 offset:0\n" \
  "ds_read_b64_tr_b16 v[42:43], v16 offset:2048\n" \
  "v_mfma_f32_16x16x32_bf16 v[24:27], v[164:167], v[72:75], v[24:27]\n" \
  "ds_read_b64_tr_b16 v[44:45], v17 offset:0\n" \
  "ds_read_b64_tr_b16 v[46:47], v17 offset:2048\n" \
  "v_mfma_f32_16x16x32_bf16 v[28:31], v[168:171], v[72:75], v[28:31]\n" \
  "ds_read_b64_tr_b16 v[48:49], v18 offset:0\n" \
  "ds_read_b64_tr_b16 v[50:51], v18 offset:2048\n" \
  "v_mfma_f32_16x16x32_bf16 v[32:35], v[172:175], v[72:75], v[32:35]\n" \
  "ds_read_b64_tr_b16 v[52:53], v19 offset:0\n" \
  "ds_read_b64_tr_b16 v[54:55], v19 offset:2048\n" \
  "s_add_u32 s84, s87, 22528\n" \
  "buffer_load_dwordx4 v[72:75], v6, s[72:75], s84 offen sc1\n" \
  "s_waitcnt vmcnt(15)\n" \
  "s_waitcnt lgkmcnt(6)\n" \
  "v_mfma_f32_16x16x32_bf16 v[20:23], v[40:43], v[76:79], v[20:23]\n" \
  "s_waitcnt lgkmcnt(4)\n" \
  "v_mfma_f32_16x16x32_bf16 v[24:27], v[44:47], v[76:79], v[24:27]\n" \
  "s_waitcnt lgkmcnt(2)\n" \
  "v_mfma_f32_16x16x32_bf16 v[28:31], v[48:51], v[76:79], v[28:31]\n" \
  "s_waitcnt lgkmcnt(0)\n" \
  "v_mfma_f32_16x16x32_bf16 v[32:35], v[52:55], v[76:79], v[32:35]\n" \
  "s_add_u32 s84, s87, 53248\n" \
  "buffer_load_dwordx4 v[76:79], v6, s[72:75], s84 offen sc1\n" \
  "s_waitcnt lgkmcnt(0)\n" \
  "s_barrier\n" \
  "ds_read_b128 v[52:55], v11 offset:0\n" \
  "ds_read_b128 v[56:59], v11 offset:4096\n" \
  "ds_read_b128 v[60:63], v11 offset:8192\n" \
  "ds_read_b128 v[36:39], v11 offset:12288\n" \
  "s_waitcnt vmcnt(15)\n" \
  "v_mfma_f32_16x16x32_bf16 v[20:23], v[176:179], v[80:83], v[20:23]\n" \
  "v_mfma_f32_16x16x32_bf16 v[24:27], v[180:183], v[80:83], v[24:27]\n" \
  "v_mfma_f32_16x16x32_bf16 v[28:31], v[184:187], v[80:83], v[28:31]\n" \
  "v_mfma_f32_16x16x32_bf16 v[32:35], v[188:191], v[80:83], v[32:35]\n" \
  "s_add_u32 s84, s87, 23552\n" \
  "buffer_load_dwordx4 v[80:83], v6, s[72:75], s84 offen sc1\n" \
  "s_waitcnt lgkmcnt(0)\n" \
  "s_waitcnt vmcnt(15)\n" \
  "v_mfma_f32_16x16x32_bf16 v[20:23], v[192:195], v[84:87], v[20:23]\n" \
  "v_add_f32 v36, v36, v52\n" \
  "v_add_f32 v37, v37, v53\n" \
  "v_add_f32 v38, v38, v54\n" \
  "v_add_f32 v39, v39, v55\n" \
  "v_mfma_f32_16x16x32_bf16 v[24:27], v[196:199], v[84:87], v[24:27]\n" \
  "v_add_f32 v56, v56, v60\n" \
  "v_add_f32 v57, v57, v61\n" \
  "v_add_f32 v58, v58, v62\n" \
  "v_add_f32 v59, v59, v63\n" \
  "v_mfma_f32_16x16x32_bf16 v[28:31], v[200:203], v[84:87], v[28:31]\n" \
  "v_add_f32 v36, v36, v56\n" \
  "v_add_f32 v37, v37, v57\n" \
  "v_add_f32 v38, v38, v58\n" \
  "v_add_f32 v39, v39, v59\n" \
  "v_mfma_f32_16x16x32_bf16 v[32:35], v[204:207], v[84:87], v[32:35]\n" \
  "v_cvt_pk_bf16_f32 v36, v36, v37\n" \
  "v_cvt_pk_bf16_f32 v37, v38, v39\n" \
  "buffer_store_dwordx2 v[36:37], v10, s[76:79], s88 offen\n" \
  "s_add_u32 s88, s88, 131072\n" \
  "v_and_b32 v10, 0x7fffffff, v10\n" \
  "s_add_u32 s84, s87, 32768\n" \
  "buffer_load_dwordx4 v[84:87], v6, s[72:75], s84 offen sc1\n" \
  "s_waitcnt vmcnt(16)\n" \
  "v_mfma_f32_16x16x32_bf16 v[20:23], v[208:211], v[88:91], v[20:23]\n" \
  "ds_read_b64_tr_b16 v[40:41], v16 offset:4096\n" \
  "ds_read_b64_tr_b16 v[42:43], v16 offset:6144\n" \
  "v_mfma_f32_16x16x32_bf16 v[24:27], v[212:215], v[88:91], v[24:27]\n" \
  "ds_read_b64_tr_b16 v[44:45], v17 offset:4096\n" \
  "ds_read_b64_tr_b16 v[46:47], v17 offset:6144\n" \
  "v_mfma_f32_16x16x32_bf16 v[28:31], v[216:219], v[88:91], v[28:31]\n" \
  "ds_read_b64_tr_b16 v[48:49], v18 offset:4096\n" \
  "ds_read_b64_tr_b16 v[50:51], v18 offset:6144\n" \
  "v_mfma_f32_16x16x32_bf16 v[32:35], v[220:223], v[88:91], v[32:35]\n" \
  "ds_read_b64_tr_b16 v[52:53], v19 offset:4096\n" \
  "ds_read_b64_tr_b16 v[54:55], v19 offset:6144\n" \
  "s_add_u32 s84, s87, 33792\n" \
  "buffer_load_dwordx4 v[88:91], v6, s[72:75], s84 offen sc1\n" \
  "s_waitcnt vmcnt(16)\n" \
  "s_waitcnt lgkmcnt(6)\n" \
  "v_mfma_f32_16x16x32_bf16 v[20:23], v[40:43], v[92:95], v[20:23]\n" \
  "s_waitcnt lgkmcnt(4)\n" \
  "v_mfma_f32_16x16x32_bf16 v[24:27], v[44:47], v[92:95], v[24:27]\n" \
  "s_waitcnt lgkmcnt(2)\n" \
  "v_mfma_f32_16x16x32_bf16 v[28:31], v[48:51], v[92:95], v[28:31]\n" \
  "s_waitcnt lgkmcnt(0)\n" \
  "v_mfma_f32_16x16x32_bf16 v[32:35], v[52:55], v[92:95], v[32:35]\n" \
  "s_add_u32 s84, s87, 54272\n" \
  "buffer_load_dwordx4 v[92:95], v6, s[72:75], s84 offen sc1\n" \
  "s_waitcnt vmcnt(16)\n" \
  "v_mfma_f32_16x16x32_bf16 v[20:23], v[224:227], v[96:99], v[20:23]\n" \
  "buffer_load_dword v9, v8, s[80:83], 0 offen sc1\n" \
  "v_mfma_f32_16x16x32_bf16 v[24:27], v[228:231], v[96:99], v[24:27]\n" \
  "v_xor_b32 v11, 0x4000, v11\n" \
  "v_xor_b32 v7, 0x4000, v7\n" \
  "v_mfma_f32_16x16x32_bf16 v[28:31], v[232:235], v[96:99], v[28:31]\n" \
  "v_xor_b32 v12, 0x4000, v12\n" \
  "v_xor_b32 v13, 0x4000, v13\n" \
  "v_xor_b32 v14, 0x4000, v14\n" \
  "v_mfma_f32_16x16x32_bf16 v[32:35], v[236:239], v[96:99], v[32:35]\n" \
  "s_add_u32 s84, s87, 34816\n" \
  "buffer_load_dwordx4 v[96:99], v6, s[72:75], s84 offen sc1\n" \
  "s_waitcnt vmcnt(17)\n" \
  "v_mfma_f32_16x16x32_bf16 v[20:23], v[240:243], v[100:103], v[20:23]\n" \
  "v_mfma_f32_16x16x32_bf16 v[24:27], v[244:247], v[100:103], v[24:27]\n" \
  "v_mfma_f32_16x16x32_bf16 v[28:31], v[248:251], v[100:103], v[28:31]\n" \
  "v_mfma_f32_16x16x32_bf16 v[32:35], v[252:255], v[100:103], v[32:35]\n" \
  "s_add_u32 s84, s87, 35840\n" \
  "buffer_load_dwordx4 v[100:103], v6, s[72:75], s84 offen sc1\n" \
  "s_waitcnt vmcnt(17)\n" \
  "v_mfma_f32_16x16x32_bf16 v[20:23], a[0:3], v[104:107], v[20:23]\n" \
  "ds_read_b64_tr_b16 v[40:41], v16 offset:8192\n" \
  "ds_read_b64_tr_b16 v[42:43], v16 offset:10240\n" \
  "v_mfma_f32_16x16x32_bf16 v[24:27], a[4:7], v[104:107], v[24:27]\n" \
  "ds_read_b64_tr_b16 v[44:45], v17 offset:8192\n" \
  "ds_read_b64_tr_b16 v[46:47], v17 offset:10240\n" \
  "v_mfma_f32_16x16x32_bf16 v[28:31], a[8:11], v[104:107], v[28:31]\n" \
  "ds_read_b64_tr_b16 v[48:49], v18 offset:8192\n" \
  "ds_read_b64_tr_b16 v[50:51], v18 offset:10240\n" \
  "v_mfma_f32_16x16x32_bf16 v[32:35], a[12:15], v[104:107], v[32:35]\n" \
  "ds_read_b64_tr_b16 v[52:53], v19 offset:8192\n" \
  "ds_read_b64_tr_b16 v[54:55], v19 offset:10240\n" \
  "s_add_u32 s84, s87, 36864\n" \
  "buffer_load_dwordx4 v[104:107], v6, s[72:75], s84 offen sc1\n" \
  "s_waitcnt vmcnt(17)\n" \
  "s_waitcnt lgkmcnt(6)\n" \
  "v_mfma_f32_16x16x32_bf16 v[20:23], v[40:43], v[108:111], v[20:23]\n" \
  "s_waitcnt lgkmcnt(4)\n" \
  "v_mfma_f32_16x16x32_bf16 v[24:27], v[44:47], v[108:111], v[24:27]\n" \
  "s_waitcnt lgkmcnt(2)\n" \
  "v_mfma_f32_16x16x32_bf16 v[28:31], v[48:51], v[108:111], v[28:31]\n" \
  "s_waitcnt lgkmcnt(0)\n" \
  "v_mfma_f32_16x16x32_bf16 v[32:35], v[52:55], v[108:111], v[32:35]\n" \
  "s_add_u32 s84, s87, 55296\n" \
  "buffer_load_dwordx4 v[108:111], v6, s[72:75], s84 offen sc1\n" \
  "s_waitcnt vmcnt(17)\n" \
  "v_mfma_f32_16x16x32_bf16 v[20:23], a[16:19], v[112:115], v[20:23]\n" \
  "v_mfma_f32_16x16x32_bf16 v[24:27], a[20:23], v[112:115], v[24:27]\n" \
  "v_mfma_f32_16x16x32_bf16 v[28:31], a[24:27], v[112:115], v[28:31]\n" \
  "v_mfma_f32_16x16x32_bf16 v[32:35], a[28:31], v[112:115], v[32:35]\n" \
  "s_add_u32 s84, s87, 37888\n" \
  "buffer_load_dwordx4 v[112:115], v6, s[72:75], s84 offen sc1\n" \
  "s_waitcnt vmcnt(17)\n" \
  "v_mfma_f32_16x16x32_bf16 v[20:23], a[32:35], v[116:119], v[20:23]\n" \
  "v_mfma_f32_16x16x32_bf16 v[24:27], a[36:39], v[116:119], v[24:27]\n" \
  "v_mfma_f32_16x16x32_bf16 v[28:31], a[40:43], v[116:119], v[28:31]\n" \
  "v_mfma_f32_16x16x32_bf16 v[32:35], a[44:47], v[116:119], v[32:35]\n" \
  "s_add_u32 s84, s87, 38912\n" \
  "buffer_load_dwordx4 v[116:119], v6, s[72:75], s84 offen sc1\n" \
  "s_waitcnt vmcnt(17)\n" \
  "v_mfma_f32_16x16x32_bf16 v[20:23], a[48:51], v[120:123], v[20:23]\n" \
  "ds_read_b64_tr_b16 v[40:41], v16 offset:12288\n" \
  "ds_read_b64_tr_b16 v[42:43], v16 offset:14336\n" \
  "v_mfma_f32_16x16x32_bf16 v[24:27], a[52:55], v[120:123], v[24:27]\n" \
  "ds_read_b64_tr_b16 v[44:45], v17 offset:12288\n" \
  "ds_read_b64_tr_b16 v[46:47], v17 offset:14336\n" \
  "v_mfma_f32_16x16x32_bf16 v[28:31], a[56:59], v[120:123], v[28:31]\n" \
  "ds_read_b64_tr_b16 v[48:49], v18 offset:12288\n" \
  "ds_read_b64_tr_b16 v[50:51], v18 offset:14336\n" \
  "v_mfma_f32_16x16x32_bf16 v[32:35], a[60:63], v[120:123], v[32:35]\n" \
  "ds_read_b64_tr_b16 v[52:53], v19 offset:12288\n" \
  "ds_read_b64_tr_b16 v[54:55], v19 offset:14336\n" \
  "s_add_u32 s84, s87, 39936\n" \
  "buffer_load_dwordx4 v[120:123], v6, s[72:75], s84 offen sc1\n" \
  "s_waitcnt vmcnt(17)\n" \
  "s_waitcnt lgkmcnt(6)\n" \
  "v_mfma_f32_16x16x32_bf16 v[20:23], v[40:43], v[124:127], v[20:23]\n" \
  "s_waitcnt lgkmcnt(4)\n" \
  "v_mfma_f32_16x16x32_bf16 v[24:27], v[44:47], v[124:127], v[24:27]\n" \
  "s_waitcnt lgkmcnt(2)\n" \
  "v_mfma_f32_16x16x32_bf16 v[28:31], v[48:51], v[124:127], v[28:31]\n" \
  "s_waitcnt lgkmcnt(0)\n" \
  "v_mfma_f32_16x16x32_bf16 v[32:35], v[52:55], v[124:127], v[32:35]\n" \
  "s_add_u32 s84, s87, 56320\n" \
  "buffer_load_dwordx4 v[124:127], v6, s[72:75], s84 offen sc1\n" \
  "s_waitcnt vmcnt(8)\n" \
  "s_add_u32 s92, s89, 2\n" \
  "s_min_u32 s92, s92, 64\n" \
  "v_cmp_gt_u32 vcc, s92, v9\n" \
  "s_sub_u32 s85, s92, 1\n" \
  "s_lshr_b32 s84, s85, 2\n" \
  "s_xnor_b32 s84, s84, %7\n" \
  "s_and_b32 s84, s84, 1\n" \
  "s_lshl_b32 s84, s84, 13\n" \
  "s_lshl_b32 s87, s85, 19\n" \
  "s_or_b32 s87, s87, s84\n" \
  "s_add_u32 s87, s87, s96\n" \
  "s_cbranch_vccnz LP2_%=\n" \
  "LP3_%=:\n" \
  "s_waitcnt vmcnt(17)\n" \
  "v_mfma_f32_16x16x32_bf16 v[20:23], a[64:67], v[64:67], v[20:23]\n" \
  "v_mfma_f32_16x16x32_bf16 v[24:27], a[68:71], v[64:67], v[24:27]\n" \
  "v_mfma_f32_16x16x32_bf16 v[28:31], a[72:75], v[64:67], v[28:31]\n" \
  "v_mfma_f32_16x16x32_bf16 v[32:35], a[76:79], v[64:67], v[32:35]\n" \
  "s_add_u32 s84, s87, 0\n" \
  "buffer_load_dwordx4 v[64:67], v6, s[72:75], s84 offen sc1\n" \
  "s_waitcnt vmcnt(17)\n" \
  "v_mfma_f32_16x16x32_bf16 v[20:23], a[80:83], v[68:71], v[20:23]\n" \
  "v_mfma_f32_16x16x32_bf16 v[24:27], a[84:87], v[68:71], v[24:27]\n" \
  "v_mfma_f32_16x16x32_bf16 v[28:31], a[88:91], v[68:71], v[28:31]\n" \
  "v_mfma_f32_16x16x32_bf16 v[32:35], a[92:95], v[68:71], v[32:35]\n" \
  "s_add_u32 s84, s87, 1024\n" \
  "buffer_load_dwordx4 v[68:71], v6, s[72:75], s84 offen sc1\n" \
  "s_waitcnt vmcnt(17)\n" \
  "v_mfma_f32_16x16x32_bf16 v[20:23], a[96:99], v[72:75], v[20:23]\n" \
  "ds_read_b64_tr_b16 v[40:41], v16 offset:16384\n" \
  "ds_read_b64_tr_b16 v[42:43], v16 offset:18432\n" \
  "v_mfma_f32_16x16x32_bf16 v[24:27], a[100:103], v[72:75], v[24:27]\n" \
  "ds_read_b64_tr_b16 v[44:45], v17 offset:16384\n" \
  "ds_read_b64_tr_b16 v[46:47], v17 offset:18432\n" \
  "v_mfma_f32_16x16x32_bf16 v[28:31], a[104:107], v[72:75], v[28:31]\n" \
  "ds_read_b64_tr_b16 v[48:49], v18 offset:16384\n" \
  "ds_read_b64_tr_b16 v[50:51], v18 offset:18432\n" \
  "v_mfma_f32_16x16x32_bf16 v[32:35], a[108:111], v[72:75], v[32:35]\n" \
  "ds_read_b64_tr_b16 v[52:53], v19 offset:16384\n" \
  "ds_read_b64_tr_b16 v[54:55], v19 offset:18432\n" \
  "s_add_u32 s84, s87, 2048\n" \
  "buffer_load_dwordx4 v[72:75], v6, s[72:75], s84 offen sc1\n" \
  "s_waitcnt vmcnt(17)\n" \
  "s_waitcnt lgkmcnt(6)\n" \
  "v_mfma_f32_16x16x32_bf16 v[20:23], v[40:43], v[76:79], v[20:23]\n" \
  "s_waitcnt lgkmcnt(4)\n" \
  "v_mfma_f32_16x16x32_bf16 v[24:27], v[44:47], v[76:79], v[24:27]\n" \
  "s_waitcnt lgkmcnt(2)\n" \
  "v_mfma_f32_16x16x32_bf16 v[28:31], v[48:51], v[76:79], v[28:31]\n" \
  "s_waitcnt lgkmcnt(0)\n" \
  "v_mfma_f32_16x16x32_bf16 v[32:35], v[52:55], v[76:79], v[32:35]\n" \
  "s_add_u32 s84, s87, 49152\n" \
  "buffer_load_dwordx4 v[76:79], v6, s[72:75], s84 offen sc1\n" \
  "s_waitcnt vmcnt(17)\n" \
  "v_mfma_f32_16x16x32_bf16 v[20:23], a[112:115], v[80:83], v[20:23]\n" \
  "v_mfma_f32_16x16x32_bf16 v[24:27], a[116:119], v[80:83], v[24:27]\n" \
  "v_mfma_f32_16x16x32_bf16 v[28:31], a[120:123], v[80:83], v[28:31]\n" \
  "v_mfma_f32_16x16x32_bf16 v[32:35], a[124:127], v[80:83], v[32:35]\n" \
  "s_add_u32 s84, s87, 3072\n" \
  "buffer_load_dwordx4 v[80:83], v6, s[72:75], s84 offen sc1\n" \
  "s_waitcnt vmcnt(16)\n" \
  "v_mfma_f32_16x16x32_bf16 v[20:23], a[128:131], v[84:87], v[20:23]\n" \
  "v_mfma_f32_16x16x32_bf16 v[24:27], a[132:135], v[84:87], v[24:27]\n" \
  "v_mfma_f32_16x16x32_bf16 v[28:31], a[136:139], v[84:87], v[28:31]\n" \
  "v_mfma_f32_16x16x32_bf16 v[32:35], a[140:143], v[84:87], v[32:35]\n" \
  "s_add_u32 s84, s87, 4096\n" \
  "buffer_load_dwordx4 v[84:87], v6, s[72:75], s84 offen sc1\n" \
  "s_waitcnt vmcnt(16)\n" \
  "v_mfma_f32_16x16x32_bf16 v[20:23], a[144:147], v[88:91], v[20:23]\n" \
  "ds_read_b64_tr_b16 v[40:41], v16 offset:20480\n" \
  "ds_read_b64_tr_b16 v[42:43], v16 offset:22528\n" \
  "v_mfma_f32_16x16x32_bf16 v[24:27], a[148:151], v[88:91], v[24:27]\n" \
  "ds_read_b64_tr_b16 v[44:45], v17 offset:20480\n" \
  "ds_read_b64_tr_b16 v[46:47], v17 offset:22528\n" \
  "v_mfma_f32_16x16x32_bf16 v[28:31], a[152:155], v[88:91], v[28:31]\n" \
  "ds_read_b64_tr_b16 v[48:49], v18 offset:20480\n" \
  "ds_read_b64_tr_b16 v[50:51], v18 offset:22528\n" \
  "v_mfma_f32_16x16x32_bf16 v[32:35], a[156:159], v[88:91], v[32:35]\n" \
  "ds_read_b64_tr_b16 v[52:53], v19 offset:20480\n" \
  "ds_read_b64_tr_b16 v[54:55], v19 offset:22528\n" \
  "s_add_u32 s84, s87, 5120\n" \
  "buffer_load_dwordx4 v[88:91], v6, s[72:75], s84 offen sc1\n" \
  "s_waitcnt vmcnt(16)\n" \
  "s_waitcnt lgkmcnt(6)\n" \
  "v_mfma_f32_16x16x32_bf16 v[20:23], v[40:43], v[92:95], v[20:23]\n" \
  "s_waitcnt lgkmcnt(4)\n" \
  "v_mfma_f32_16x16x32_bf16 v[24:27], v[44:47], v[92:95], v[24:27]\n" \
  "s_waitcnt lgkmcnt(2)\n" \
  "v_mfma_f32_16x16x32_bf16 v[28:31], v[48:51], v[92:95], v[28:31]\n" \
  "s_waitcnt lgkmcnt(0)\n" \
  "v_mfma_f32_16x16x32_bf16 v[32:35], v[52:55], v[92:95], v[32:35]\n" \
  "s_add_u32 s84, s87, 50176\n" \
  "buffer_load_dwordx4 v[92:95], v6, s[72:75], s84 offen sc1\n" \
  "s_waitcnt vmcnt(15)\n" \
  "v_mfma_f32_16x16x32_bf16 v[20:23], a[160:163], v[96:99], v[20:23]\n" \
  "v_mfma_f32_16x16x32_bf16 v[24:27], a[164:167], v[96:99], v[24:27]\n" \
  "v_mfma_f32_16x16x32_bf16 v[28:31], a[168:171], v[96:99], v[28:31]\n" \
  "v_mfma_f32_16x16x32_bf16 v[32:35], a[172:175], v[96:99], v[32:35]\n" \
  "s_add_u32 s84, s87, 6144\n" \
  "buffer_load_dwordx4 v[96:99], v6, s[72:75], s84 offen sc1\n" \
  "s_waitcnt vmcnt(15)\n" \
  "v_mfma_f32_16x16x32_bf16 v[20:23], a[176:179], v[100:103], v[20:23]\n" \
  "v_mfma_f32_16x16x32_bf16 v[24:27], a[180:183], v[100:103], v[24:27]\n" \
  "v_mfma_f32_16x16x32_bf16 v[28:31], a[184:187], v[100:103], v[28:31]\n" \
  "v_mfma_f32_16x16x32_bf16 v[32:35], a[188:191], v[100:103], v[32:35]\n" \
  "s_add_u32 s84, s87, 7168\n" \
  "buffer_load_dwordx4 v[100:103], v6, s[72:75], s84 offen sc1\n" \
  "s_waitcnt vmcnt(15)\n" \
  "v_mfma_f32_16x16x32_bf16 v[20:23], a[192:195], v[104:107], v[20:23]\n" \
  "ds_read_b64_tr_b16 v[40:41], v16 offset:24576\n" \
  "ds_read_b64_tr_b16 v[42:43], v16 offset:26624\n" \
  "v_mfma_f32_16x16x32_bf16 v[24:27], a[196:199], v[104:107], v[24:27]\n" \
  "ds_read_b64_tr_b16 v[44:45], v17 offset:24576\n" \
  "ds_read_b64_tr_b16 v[46:47], v17 offset:26624\n" \
  "v_mfma_f32_16x16x32_bf16 v[28:31], a[200:203], v[104:107], v[28:31]\n" \
  "ds_read_b64_tr_b16 v[48:49], v18 offset:24576\n" \
  "ds_read_b64_tr_b16 v[50:51], v18 offset:26624\n" \
  "v_mfma_f32_16x16x32_bf16 v[32:35], a[204:207], v[104:107], v[32:35]\n" \
  "ds_read_b64_tr_b16 v[52:53], v19 offset:24576\n" \
  "ds_read_b64_tr_b16 v[54:55], v19 offset:26624\n" \
  "s_add_u32 s84, s87, 16384\n" \
  "buffer_load_dwordx4 v[104:107], v6, s[72:75], s84 offen sc1\n" \
  "s_waitcnt vmcnt(15)\n" \
  "s_waitcnt lgkmcnt(6)\n" \
  "v_mfma_f32_16x16x32_bf16 v[20:23], v[40:43], v[108:111], v[20:23]\n" \
  "s_waitcnt lgkmcnt(4)\n" \
  "v_mfma_f32_16x16x32_bf16 v[24:27], v[44:47], v[108:111], v[24:27]\n" \
  "s_waitcnt lgkmcnt(2)\n" \
  "v_mfma_f32_16x16x32_bf16 v[28:31], v[48:51], v[108:111], v[28:31]\n" \
  "s_waitcnt lgkmcnt(0)\n" \
  "v_mfma_f32_16x16x32_bf16 v[32:35], v[52:55], v[108:111], v[32:35]\n" \
  "s_add_u32 s84, s87, 51200\n" \
  "buffer_load_dwordx4 v[108:111], v6, s[72:75], s84 offen sc1\n" \
  "s_waitcnt vmcnt(15)\n" \
  "v_mfma_f32_16x16x32_bf16 v[20:23], a[208:211], v[112:115], v[20:23]\n" \
  "v_mfma_f32_16x16x32_bf16 v[24:27], a[212:215], v[112:115], v[24:27]\n" \
  "v_mfma_f32_16x16x32_bf16 v[28:31], a[216:219], v[112:115], v[28:31]\n" \
  "v_mfma_f32_16x16x32_bf16 v[32:35], a[220:223], v[112:115], v[32:35]\n" \
  "s_add_u32 s84, s87, 17408\n" \
  "buffer_load_dwordx4 v[112:115], v6, s[72:75], s84 offen sc1\n" \
  "s_waitcnt vmcnt(15)\n" \
  "v_mfma_f32_16x16x32_bf16 v[20:23], a[224:227], v[116:119], v[20:23]\n" \
  "v_mfma_f32_16x16x32_bf16 v[24:27], a[228:231], v[116:119], v[24:27]\n" \
  "v_mfma_f32_16x16x32_bf16 v[28:31], a[232:235], v[116:119], v[28:31]\n" \
  "v_mfma_f32_16x16x32_bf16 v[32:35], a[236:239], v[116:119], v[32:35]\n" \
  "s_add_u32 s84, s87, 18432\n" \
  "buffer_load_dwordx4 v[116:119], v6, s[72:75], s84 offen sc1\n" \
  "s_waitcnt vmcnt(15)\n" \
  "v_mfma_f32_16x16x32_bf16 v[20:23], a[240:243], v[120:123], v[20:23]\n" \
  "ds_read_b64_tr_b16 v[40:41], v16 offset:28672\n" \
  "ds_read_b64_tr_b16 v[42:43], v16 offset:30720\n" \
  "v_mfma_f32_16x16x32_bf16 v[24:27], a[244:247], v[120:123], v[24:27]\n" \
  "ds_read_b64_tr_b16 v[44:45], v17 offset:28672\n" \
  "ds_read_b64_tr_b16 v[46:47], v17 offset:30720\n" \
  "v_mfma_f32_16x16x32_bf16 v[28:31], a[248:251], v[120:123], v[28:31]\n" \
  "ds_read_b64_tr_b16 v[48:49], v18 offset:28672\n" \
  "ds_read_b64_tr_b16 v[50:51], v18 offset:30720\n" \
  "v_mfma_f32_16x16x32_bf16 v[32:35], a[252:255], v[120:123], v[32:35]\n" \
  "ds_read_b64_tr_b16 v[52:53], v19 offset:28672\n" \
  "ds_read_b64_tr_b16 v[54:55], v19 offset:30720\n" \
  "s_add_u32 s84, s87, 19456\n" \
  "buffer_load_dwordx4 v[120:123], v6, s[72:75], s84 offen sc1\n" \
  "s_waitcnt vmcnt(15)\n" \
  "s_waitcnt lgkmcnt(6)\n" \
  "v_mfma_f32_16x16x32_bf16 v[20:23], v[40:43], v[124:127], v[20:23]\n" \
  "s_waitcnt lgkmcnt(4)\n" \
  "v_mfma_f32_16x16x32_bf16 v[24:27], v[44:47], v[124:127], v[24:27]\n" \
  "s_waitcnt lgkmcnt(2)\n" \
  "v_mfma_f32_16x16x32_bf16 v[28:31], v[48:51], v[124:127], v[28:31]\n" \
  "s_waitcnt lgkmcnt(0)\n" \
  "v_mfma_f32_16x16x32_bf16 v[32:35], v[52:55], v[124:127], v[32:35]\n" \
  "s_add_u32 s84, s87, 52224\n" \
  "buffer_load_dwordx4 v[124:127], v6, s[72:75], s84 offen sc1\n" \
  "s_add_u32 s89, s89, 1\n" \
  "s_cmp_eq_u32 s89, 64\n" \
  "ds_write_b128 v7, v[20:23]\n" \
  "ds_write_b128 v12, v[24:27]\n" \
  "ds_write_b128 v13, v[28:31]\n" \
  "s_nop 0\n" \
  "ds_write_b128 v14, v[32:35]\n" \
  "s_cbranch_scc0 LX_%=\n" \
  "s_waitcnt lgkmcnt(0)\n" \
  "s_barrier\n" \
  "ds_read_b128 v[52:55], v11 offset:0\n" \
  "ds_read_b128 v[56:59], v11 offset:4096\n" \
  "ds_read_b128 v[60:63], v11 offset:8192\n" \
  "ds_read_b128 v[36:39], v11 offset:12288\n" \
  "s_waitcnt lgkmcnt(0)\n" \
  "v_add_f32 v36, v36, v52\n" \
  "v_add_f32 v37, v37, v53\n" \
  "v_add_f32 v38, v38, v54\n" \
  "v_add_f32 v39, v39, v55\n" \
  "v_add_f32 v56, v56, v60\n" \
  "v_add_f32 v57, v57, v61\n" \
  "v_add_f32 v58, v58, v62\n" \
  "v_add_f32 v59, v59, v63\n" \
  "v_add_f32 v36, v36, v56\n" \
  "v_add_f32 v37, v37, v57\n" \
  "v_add_f32 v38, v38, v58\n" \
  "v_add_f32 v39, v39, v59\n" \
  "v_cvt_pk_bf16_f32 v36, v36, v37\n" \
  "v_cvt_pk_bf16_f32 v37, v38, v39\n" \
  "buffer_store_dwordx2 v[36:37], v10, s[76:79], s88 offen\n" \
  "s_add_u32 s88, s88, 131072\n" \
  "v_and_b32 v10, 0x7fffffff, v10\n" \
  "s_waitcnt vmcnt(0)\n" \
  "s_branch LE_%=\n" \
  "LP2_%=:\n" \
  "buffer_load_dword v9, v8, s[80:83], 0 offen sc1\n" \
  "s_waitcnt vmcnt(0)\n" \
  "v_cmp_gt_u32 vcc, s92, v9\n" \
  "s_cbranch_vccz LP3_%=\n" \
  "s_sleep 2\n" \
  "s_add_u32 s91, s91, 1\n" \
  "s_cmp_lt_u32 s91, " STR(SPIN_CAP) "\n" \
  "s_cbranch_scc1 LP2_%=\n" \
  "v_mov_b32 v5, " STR(CW_DEAD) "*4\n" \
  "v_mov_b32 v15, 1\n" \
  "buffer_store_dword v15, v5, s[80:83], 0 offen sc1\n" \
  "s_branch LP3_%=\n" \
  "LE_%=:\n" \
  ""

#define XA_ASM \
  "s_mov_b64 s[68:69], %0\n" \
  "s_and_b32 s69, s69, 0xffff\n" \
  "s_mov_b32 s70, " STR(EX_BYTES32) "\n" \
  "s_mov_b32 s71, 0x00020000\n" \
  "s_mov_b64 s[72:73], %1\n" \
  "s_and_b32 s73, s73, 0xffff\n" \
  "s_mov_b32 s74, 4*" STR(QKV_SZ32) "\n" \
  "s_mov_b32 s75, 0x00020000\n" \
  "s_mov_b64 s[76:77], %2\n" \
  "s_and_b32 s77, s77, 0xffff\n" \
  "s_mov_b32 s78, " STR(CTRL_BYTES) "\n" \
  "s_mov_b32 s79, 0x00020000\n" \
  "v_mbcnt_lo_u32_b32 v4, -1, 0\n" \
  "v_mbcnt_hi_u32_b32 v4, -1, v4\n" \
  "v_and_b32 v116, 15, v4\n" \
  "v_lshrrev_b32 v117, 4, v4\n" \
  "v_lshlrev_b32 v7, 4, v4\n" \
  "v_add_u32 v8, 4096, v7\n" \
  "v_add_u32 v17, 0x40000, v7\n" \
  "s_and_b32 s80, %3, 1\n" \
  "s_lshl_b32 s80, s80, 11\n" \
  "s_add_u32 s80, s80, 32768\n" \
  "v_add_u32 v18, s80, v7\n" \
  "v_add_u32 v11, %5, v7\n" \
  "v_lshrrev_b32 v5, 2, v116\n" \
  "v_and_b32 v5, 1, v5\n" \
  "v_lshl_add_u32 v5, v117, 2, v5\n" \
  "v_and_b32 v6, 2, v4\n" \
  "v_add_lshl_u32 v5, v5, v6, 4\n" \
  "v_and_b32 v6, 1, v4\n" \
  "v_lshl_add_u32 v5, v6, 3, v5\n" \
  "v_add_u32 v12, %5, v5\n" \
  "s_lshl_b32 s80, %3, 7\n" \
  "s_add_u32 s81, s80, " STR(CW_XPROG) "*4\n" \
  "v_lshl_add_u32 v15, v4, 24, s81\n" \
  "s_add_u32 s81, s80, " STR(CW_PROG) "*4\n" \
  "v_mov_b32 v13, s81\n" \
  "s_lshr_b32 s80, %3, 1\n" \
  "s_lshl_b32 s80, s80, 9\n" \
  "s_add_u32 s80, s80, " STR(CW_GPROG) "*4\n" \
  "v_writelane_b32 v13, s80, 1\n" \
  "s_add_u32 s80, s80, 128\n" \
  "v_writelane_b32 v13, s80, 2\n" \
  "s_add_u32 s80, s80, 128\n" \
  "v_writelane_b32 v13, s80, 3\n" \
  "s_add_u32 s80, s80, 128\n" \
  "v_writelane_b32 v13, s80, 4\n" \
  "v_mov_b32 v16, 0\n" \
  "v_mov_b32 v6, 0xff800000\n" \
  "v_lshlrev_b32 v5, 2, v117\n" \
  "v_cmp_gt_u32 vcc, v5, v116\n" \
  "v_mov_b32 v24, 0x3f803f80\n" \
  "v_add_u32 v5, 1, v5\n" \
  "v_cndmask_b32 v20, 0, v6, vcc\n" \
  "v_cmp_gt_u32 vcc, v5, v116\n" \
  "v_mov_b32 v25, 0x3f803f80\n" \
  "v_add_u32 v5, 1, v5\n" \
  "v_cndmask_b32 v21, 0, v6, vcc\n" \
  "v_cmp_gt_u32 vcc, v5, v116\n" \
  "v_mov_b32 v26, 0x3f803f80\n" \
  "v_add_u32 v5, 1, v5\n" \
  "v_cndmask_b32 v22, 0, v6, vcc\n" \
  "v_cmp_gt_u32 vcc, v5, v116\n" \
  "v_mov_b32 v27, 0x3f803f80\n" \
  "v_add_u32 v5, 1, v5\n" \
  "v_cndmask_b32 v23, 0, v6, vcc\n" \
  "v_mov_b64 v[130:131], 0\n" \
  "v_mov_b32 v156, 0\n" \
  "v_mov_b32 v158, 0\n" \
  "s_mov_b32 s89, 0x3e0293ee\n" \
  "s_mov_b32 s88, 0\n" \
  "s_mov_b32 s82, 0\n" \
  "s_mov_b32 s90, 0\n" \
  "s_lshr_b32 s80, s82, 2\n" \
  "s_xnor_b32 s80, s80, %4\n" \
  "s_and_b32 s80, s80, 1\n" \
  "s_lshl_b32 s80, s80, 13\n" \
  "s_lshl_b32 s83, s82, 19\n" \
  "s_or_b32 s83, s83, s80\n" \
  "s_lshl_b32 s80, %3, 14\n" \
  "s_add_u32 s83, s83, s80\n" \
  "s_add_u32 s84, s83, " STR(QKV_SZ32) "\n" \
  "s_add_u32 s85, s84, " STR(QKV_SZ32) "\n" \
  "s_mov_b32 s81, 1\n" \
  "LP0_%=:\n" \
  "buffer_load_dword v14, v13, s[76:79], 0 offen sc1\n" \
  "s_waitcnt vmcnt(0)\n" \
  "v_readlane_b32 s91, v14, 0\n" \
  "v_readlane_b32 s92, v14, 1\n" \
  "v_readlane_b32 s80, v14, 2\n" \
  "s_min_u32 s92, s92, s80\n" \
  "v_readlane_b32 s80, v14, 3\n" \
  "s_min_u32 s92, s92, s80\n" \
  "v_readlane_b32 s80, v14, 4\n" \
  "s_min_u32 s92, s92, s80\n" \
  "s_cmp_ge_u32 s92, s81\n" \
  "s_cbranch_scc1 LP1_%=\n" \
  "s_sleep 16\n" \
  "s_add_u32 s88, s88, 1\n" \
  "s_cmp_lt_u32 s88, " STR(SPIN_CAP) "\n" \
  "s_cbranch_scc1 LP0_%=\n" \
  "v_mov_b32 v5, " STR(CW_DEAD) "*4\n" \
  "v_mov_b32 v6, 1\n" \
  "buffer_store_dword v6, v5, s[76:79], 0 offen sc1\n" \
  "s_branch LP1_%=\n" \
  "LP1_%=:\n" \
  "buffer_load_dwordx4 a[64:67], v7, s[72:75], s84 offen offset:0 sc1\n" \
  "buffer_load_dwordx4 a[68:71], v7, s[72:75], s84 offen offset:1024 sc1\n" \
  "buffer_load_dwordx4 a[72:75], v7, s[72:75], s84 offen offset:2048 sc1\n" \
  "buffer_load_dwordx4 a[76:79], v7, s[72:75], s84 offen offset:3072 sc1\n" \
  "buffer_load_dwordx4 a[80:83], v8, s[72:75], s84 offen offset:0 sc1\n" \
  "buffer_load_dwordx4 a[84:87], v8, s[72:75], s84 offen offset:1024 sc1\n" \
  "buffer_load_dwordx4 a[88:91], v8, s[72:75], s84 offen offset:2048 sc1\n" \
  "buffer_load_dwordx4 a[92:95], v8, s[72:75], s84 offen offset:3072 sc1\n" \
  "buffer_load_dwordx4 a[0:3], v7, s[72:75], s83 offen offset:0 sc1\n" \
  "buffer_load_dwordx4 a[4:7], v7, s[72:75], s83 offen offset:1024 sc1\n" \
  "buffer_load_dwordx4 a[8:11], v7, s[72:75], s83 offen offset:2048 sc1\n" \
  "buffer_load_dwordx4 a[12:15], v7, s[72:75], s83 offen offset:3072 sc1\n" \
  "buffer_load_dwordx4 a[16:19], v8, s[72:75], s83 offen offset:0 sc1\n" \
  "buffer_load_dwordx4 a[20:23], v8, s[72:75], s83 offen offset:1024 sc1\n" \
  "buffer_load_dwordx4 a[24:27], v8, s[72:75], s83 offen offset:2048 sc1\n" \
  "buffer_load_dwordx4 a[28:31], v8, s[72:75], s83 offen offset:3072 sc1\n" \
  "buffer_load_dwordx4 a[96:99], v7, s[72:75], s85 offen offset:0 sc1\n" \
  "buffer_load_dwordx4 a[100:103], v7, s[72:75], s85 offen offset:1024 sc1\n" \
  "buffer_load_dwordx4 a[104:107], v7, s[72:75], s85 offen offset:2048 sc1\n" \
  "buffer_load_dwordx4 a[108:111], v7, s[72:75], s85 offen offset:3072 sc1\n" \
  "buffer_load_dwordx4 a[112:115], v8, s[72:75], s85 offen offset:0 sc1\n" \
  "buffer_load_dwordx4 a[116:119], v8, s[72:75], s85 offen offset:1024 sc1\n" \
  "buffer_load_dwordx4 a[120:123], v8, s[72:75], s85 offen offset:2048 sc1\n" \
  "buffer_load_dwordx4 a[124:127], v8, s[72:75], s85 offen offset:3072 sc1\n" \
  "s_lshr_b32 s80, s82, 2\n" \
  "s_add_u32 s80, s80, " STR(EXP_B) "\n" \
  "v_readlane_b32 s80, %6, s80\n" \
  "s_lshl_b32 s87, s82, 19\n" \
  "s_add_u32 s87, s87, " STR(EXP_B) "<<21\n" \
  "s_xor_b32 s87, s87, s80\n" \
  "s_lshr_b32 s80, %3, 3\n" \
  "s_xor_b32 s80, s80, s82\n" \
  "s_and_b32 s80, s80, 3\n" \
  "s_mul_i32 s80, s80, 0x4800\n" \
  "s_and_b32 s80, s80, 0x5000\n" \
  "s_xor_b32 s87, s87, s80\n" \
  "s_and_b32 s80, %3, 7\n" \
  "s_lshl_b32 s80, s80, 16\n" \
  "s_or_b32 s87, s87, s80\n" \
  "s_waitcnt vmcnt(0)\n" \
  ".p2align 6\n" \
  "LA_%=:\n" \
  "s_waitcnt vmcnt(24)\n" \
  "s_add_u32 s81, s82, 2\n" \
  "s_min_u32 s81, s81, 64\n" \
  "s_sub_u32 s84, s81, 1\n" \
  "v_mfma_f32_16x16x32_bf16 v[116:119], a[64:67], a[0:3], v[20:23]\n" \
  "s_lshr_b32 s80, s84, 2\n" \
  "s_xnor_b32 s80, s80, %4\n" \
  "v_mfma_f32_16x16x32_bf16 v[120:123], a[64:67], a[16:19], 0\n" \
  "s_and_b32 s80, s80, 1\n" \
  "s_lshl_b32 s80, s80, 13\n" \
  "v_mfma_f32_16x16x32_bf16 v[124:127], a[80:83], a[16:19], v[20:23]\n" \
  "s_lshl_b32 s83, s84, 19\n" \
  "s_or_b32 s83, s83, s80\n" \
  "v_mfma_f32_16x16x32_bf16 v[116:119], a[68:71], a[4:7], v[116:119]\n" \
  "s_lshl_b32 s80, %3, 14\n" \
  "s_add_u32 s83, s83, s80\n" \
  "v_mfma_f32_16x16x32_bf16 v[120:123], a[68:71], a[20:23], v[120:123]\n" \
  "s_add_u32 s84, s83, " STR(QKV_SZ32) "\n" \
  "s_add_u32 s85, s84, " STR(QKV_SZ32) "\n" \
  "v_mfma_f32_16x16x32_bf16 v[124:127], a[84:87], a[20:23], v[124:127]\n" \
  "s_lshr_b32 s80, s82, 2\n" \
  "s_xnor_b32 s80, s80, %4\n" \
  "v_mfma_f32_16x16x32_bf16 v[116:119], a[72:75], a[8:11], v[116:119]\n" \
  "s_and_b32 s80, s80, 1\n" \
  "s_lshl_b32 s80, s80, 13\n" \
  "v_mfma_f32_16x16x32_bf16 v[120:123], a[72:75], a[24:27], v[120:123]\n" \
  "s_lshl_b32 s86, s82, 19\n" \
  "s_or_b32 s86, s86, s80\n" \
  "v_mfma_f32_16x16x32_bf16 v[124:127], a[88:91], a[24:27], v[124:127]\n" \
  "s_lshr_b32 s80, %3, 1\n" \
  "s_lshl_b32 s80, s80, 14\n" \
  "v_mfma_f32_16x16x32_bf16 v[116:119], a[76:79], a[12:15], v[116:119]\n" \
  "s_add_u32 s86, s86, s80\n" \
  "s_and_b32 s80, %3, 1\n" \
  "v_mfma_f32_16x16x32_bf16 v[120:123], a[76:79], a[28:31], v[120:123]\n" \
  "s_lshl_b32 s80, s80, 12\n" \
  "s_add_u32 s86, s86, s80\n" \
  "v_mfma_f32_16x16x32_bf16 v[124:127], a[92:95], a[28:31], v[124:127]\n" \
  "s_add_u32 s86, s86, 3*" STR(QKV_SZ32) "\n" \
  "s_cmp_lt_u32 s92, s81\n" \
  "s_cbranch_scc1 LG0_%=\n" \
  "LH0_%=:\n" \
  "buffer_load_dwordx4 a[64:67], v7, s[72:75], s84 offen offset:0 sc1\n" \
  "buffer_load_dwordx4 a[68:71], v7, s[72:75], s84 offen offset:1024 sc1\n" \
  "buffer_load_dwordx4 a[72:75], v7, s[72:75], s84 offen offset:2048 sc1\n" \
  "buffer_load_dwordx4 a[76:79], v7, s[72:75], s84 offen offset:3072 sc1\n" \
  "buffer_load_dwordx4 a[80:83], v8, s[72:75], s84 offen offset:0 sc1\n" \
  "buffer_load_dwordx4 a[84:87], v8, s[72:75], s84 offen offset:1024 sc1\n" \
  "v_max3_f32 v144, v116, v117, v118\n" \
  "v_max3_f32 v145, v120, v121, v122\n" \
  "v_max_f32 v144, v144, v119\n" \
  "v_max3_f32 v145, v145, v123, v124\n" \
  "v_max3_f32 v145, v145, v125, v126\n" \
  "v_max_f32 v145, v145, v127\n" \
  "v_mov_b32 v5, v144\n" \
  "v_mov_b32 v6, v145\n" \
  "s_nop 0\n" \
  "v_permlane16_swap_b32 v144, v5\n" \
  "v_permlane16_swap_b32 v145, v6\n" \
  "v_max_f32 v144, v144, v5\n" \
  "v_max_f32 v145, v145, v6\n" \
  "v_mov_b32 v5, v144\n" \
  "v_mov_b32 v6, v145\n" \
  "s_nop 0\n" \
  "v_permlane32_swap_b32 v144, v5\n" \
  "v_permlane32_swap_b32 v145, v6\n" \
  "v_max_f32 v144, v144, v5\n" \
  "v_max_f32 v145, v145, v6\n" \
  "v_mul_f32 v144, s89, v144\n" \
  "v_mul_f32 v145, s89, v145\n" \
  "v_fma_f32 v116, v116, s89, -v144\n" \
  "v_exp_f32 v116, v116\n" \
  "buffer_load_dwordx4 a[88:91], v8, s[72:75], s84 offen offset:2048 sc1\n" \
  "v_fma_f32 v117, v117, s89, -v144\n" \
  "v_exp_f32 v117, v117\n" \
  "buffer_load_dwordx4 a[92:95], v8, s[72:75], s84 offen offset:3072 sc1\n" \
  "v_fma_f32 v118, v118, s89, -v144\n" \
  "v_exp_f32 v118, v118\n" \
  "buffer_load_dwordx4 a[32:35], v7, s[72:75], s83 offen offset:0 sc1\n" \
  "v_fma_f32 v119, v119, s89, -v144\n" \
  "v_exp_f32 v119, v119\n" \
  "buffer_load_dwordx4 a[36:39], v7, s[72:75], s83 offen offset:1024 sc1\n" \
  "v_fma_f32 v120, v120, s89, -v145\n" \
  "v_exp_f32 v120, v120\n" \
  "buffer_load_dwordx4 a[40:43], v7, s[72:75], s83 offen offset:2048 sc1\n" \
  "v_fma_f32 v121, v121, s89, -v145\n" \
  "v_exp_f32 v121, v121\n" \
  "buffer_load_dwordx4 a[44:47], v7, s[72:75], s83 offen offset:3072 sc1\n" \
  "v_fma_f32 v122, v122, s89, -v145\n" \
  "v_exp_f32 v122, v122\n" \
  "buffer_load_dwordx4 a[48:51], v8, s[72:75], s83 offen offset:0 sc1\n" \
  "v_fma_f32 v123, v123, s89, -v145\n" \
  "v_exp_f32 v123, v123\n" \
  "buffer_load_dwordx4 a[52:55], v8, s[72:75], s83 offen offset:1024 sc1\n" \
  "v_fma_f32 v124, v124, s89, -v145\n" \
  "v_exp_f32 v124, v124\n" \
  "buffer_load_dwordx4 a[56:59], v8, s[72:75], s83 offen offset:2048 sc1\n" \
  "v_fma_f32 v125, v125, s89, -v145\n" \
  "v_exp_f32 v125, v125\n" \
  "buffer_load_dwordx4 a[60:63], v8, s[72:75], s83 offen offset:3072 sc1\n" \
  "v_fma_f32 v126, v126, s89, -v145\n" \
  "v_exp_f32 v126, v126\n" \
  "v_fma_f32 v127, v127, s89, -v145\n" \
  "v_exp_f32 v127, v127\n" \
  "s_nop 0\n" \
  "v_cvt_pk_bf16_f32 v128, v116, v117\n" \
  "v_cvt_pk_bf16_f32 v129, v118, v119\n" \
  "v_cvt_pk_bf16_f32 v132, v120, v121\n" \
  "v_cvt_pk_bf16_f32 v133, v122, v123\n" \
  "v_cvt_pk_bf16_f32 v134, v124, v125\n" \
  "v_cvt_pk_bf16_f32 v135, v126, v127\n" \
  "s_waitcnt vmcnt(22)\n" \
  "buffer_store_dword v16, v15, s[76:79], 0 offen sc1\n" \
  "v_mfma_f32_16x16x32_bf16 v[136:139], v[24:27], v[128:131], 0\n" \
  "v_mfma_f32_16x16x32_bf16 v[140:143], v[24:27], v[132:135], 0\n" \
  "v_add_u32 v16, 1, v16\n" \
  "v_mfma_f32_16x16x32_bf16 v[52:55], a[96:99], v[128:131], 0\n" \
  "v_mfma_f32_16x16x32_bf16 v[84:87], a[96:99], v[132:135], 0\n" \
  "buffer_load_dwordx4 a[96:99], v7, s[72:75], s85 offen offset:0 sc1\n" \
  "v_mfma_f32_16x16x32_bf16 v[56:59], a[100:103], v[128:131], 0\n" \
  "v_mfma_f32_16x16x32_bf16 v[88:91], a[100:103], v[132:135], 0\n" \
  "buffer_load_dwordx4 a[100:103], v7, s[72:75], s85 offen offset:1024 sc1\n" \
  "v_mfma_f32_16x16x32_bf16 v[60:63], a[104:107], v[128:131], 0\n" \
  "v_mfma_f32_16x16x32_bf16 v[92:95], a[104:107], v[132:135], 0\n" \
  "buffer_load_dwordx4 a[104:107], v7, s[72:75], s85 offen offset:2048 sc1\n" \
  "v_mfma_f32_16x16x32_bf16 v[64:67], a[108:111], v[128:131], 0\n" \
  "v_mfma_f32_16x16x32_bf16 v[96:99], a[108:111], v[132:135], 0\n" \
  "buffer_load_dwordx4 a[108:111], v7, s[72:75], s85 offen offset:3072 sc1\n" \
  "v_mfma_f32_16x16x32_bf16 v[68:71], a[112:115], v[128:131], 0\n" \
  "v_mfma_f32_16x16x32_bf16 v[100:103], a[112:115], v[132:135], 0\n" \
  "buffer_load_dwordx4 a[112:115], v8, s[72:75], s85 offen offset:0 sc1\n" \
  "v_mfma_f32_16x16x32_bf16 v[72:75], a[116:119], v[128:131], 0\n" \
  "v_mfma_f32_16x16x32_bf16 v[104:107], a[116:119], v[132:135], 0\n" \
  "buffer_load_dwordx4 a[116:119], v8, s[72:75], s85 offen offset:1024 sc1\n" \
  "v_mfma_f32_16x16x32_bf16 v[76:79], a[120:123], v[128:131], 0\n" \
  "v_mfma_f32_16x16x32_bf16 v[108:111], a[120:123], v[132:135], 0\n" \
  "buffer_load_dwordx4 a[120:123], v8, s[72:75], s85 offen offset:2048 sc1\n" \
  "v_mfma_f32_16x16x32_bf16 v[80:83], a[124:127], v[128:131], 0\n" \
  "v_mfma_f32_16x16x32_bf16 v[112:115], a[124:127], v[132:135], 0\n" \
  "buffer_load_dwordx4 a[124:127], v8, s[72:75], s85 offen offset:3072 sc1\n" \
  "buffer_load_dword v14, v13, s[76:79], 0 offen sc1\n" \
  "s_cmp_eq_u32 s90, 0\n" \
  "s_cbranch_scc1 LW0_%=\n" \
  "s_waitcnt vmcnt(26)\n" \
  "LM0_%=:\n" \
  "ds_write_b128 v11, v[44:47]\n" \
  "ds_write_b128 v11, v[48:51] offset:1024\n" \
  "v_mfma_f32_16x16x32_bf16 v[148:151], v[28:31], a[0:3], 0\n" \
  "v_max3_u32 v5, v28, v29, v30\n" \
  "v_max3_u32 v5, v5, v31, v32\n" \
  "v_mfma_f32_16x16x32_bf16 v[152:155], v[28:31], a[16:19], 0\n" \
  "v_max3_u32 v5, v5, v33, v34\n" \
  "v_max3_u32 v5, v5, v35, v36\n" \
  "v_mfma_f32_16x16x32_bf16 v[148:151], v[32:35], a[4:7], v[148:151]\n" \
  "v_max3_u32 v5, v5, v37, v38\n" \
  "v_max3_u32 v5, v5, v39, v40\n" \
  "v_mfma_f32_16x16x32_bf16 v[152:155], v[32:35], a[20:23], v[152:155]\n" \
  "v_max3_u32 v5, v5, v41, v42\n" \
  "v_max3_u32 v5, v5, v43, v44\n" \
  "v_mfma_f32_16x16x32_bf16 v[148:151], v[36:39], a[8:11], v[148:151]\n" \
  "v_max3_u32 v5, v5, v45, v46\n" \
  "v_max3_u32 v5, v5, v47, v48\n" \
  "v_mfma_f32_16x16x32_bf16 v[152:155], v[36:39], a[24:27], v[152:155]\n" \
  "v_max3_u32 v5, v5, v49, v50\n" \
  "v_max_u32 v5, v5, v51\n" \
  "v_mfma_f32_16x16x32_bf16 v[148:151], v[40:43], a[12:15], v[148:151]\n" \
  "v_cmp_eq_u32 vcc, -1, v5\n" \
  "v_mfma_f32_16x16x32_bf16 v[152:155], v[40:43], a[28:31], v[152:155]\n" \
  "ds_read_b64_tr_b16 a[128:129], v12 offset:0\n" \
  "ds_read_b64_tr_b16 a[130:131], v12 offset:256\n" \
  "ds_read_b64_tr_b16 a[132:133], v12 offset:512\n" \
  "ds_read_b64_tr_b16 a[134:135], v12 offset:768\n" \
  "ds_read_b64_tr_b16 a[136:137], v12 offset:1024\n" \
  "ds_read_b64_tr_b16 a[138:139], v12 offset:1280\n" \
  "ds_read_b64_tr_b16 a[140:141], v12 offset:1536\n" \
  "ds_read_b64_tr_b16 a[142:143], v12 offset:1792\n" \
  "s_cbranch_vccnz LR0_%=\n" \
  "v_max_f32 v146, v150, v151\n" \
  "v_max_f32 v147, v154, v155\n" \
  "v_mov_b32 v5, v146\n" \
  "v_mov_b32 v6, v147\n" \
  "s_nop 0\n" \
  "v_permlane16_swap_b32 v146, v5\n" \
  "v_permlane16_swap_b32 v147, v6\n" \
  "v_max_f32 v146, v146, v5\n" \
  "v_max_f32 v147, v147, v6\n" \
  "v_mov_b32 v5, v146\n" \
  "v_mov_b32 v6, v147\n" \
  "s_nop 0\n" \
  "v_permlane32_swap_b32 v146, v5\n" \
  "v_permlane32_swap_b32 v147, v6\n" \
  "v_max_f32 v146, v146, v5\n" \
  "v_max_f32 v147, v147, v6\n" \
  "v_mul_f32 v146, s89, v146\n" \
  "v_mul_f32 v147, s89, v147\n" \
  "v_max_f32 v146, v146, v144\n" \
  "v_max_f32 v147, v147, v145\n" \
  "v_sub_f32 v160, v144, v146\n" \
  "v_sub_f32 v162, v145, v147\n" \
  "v_fma_f32 v150, v150, s89, -v146\n" \
  "v_fma_f32 v151, v151, s89, -v146\n" \
  "v_fma_f32 v154, v154, s89, -v147\n" \
  "v_fma_f32 v155, v155, s89, -v147\n" \
  "v_exp_f32 v150, v150\n" \
  "v_exp_f32 v151, v151\n" \
  "v_exp_f32 v154, v154\n" \
  "v_exp_f32 v155, v155\n" \
  "v_exp_f32 v160, v160\n" \
  "v_exp_f32 v162, v162\n" \
  "v_add_f32 v146, v150, v151\n" \
  "v_add_f32 v147, v154, v155\n" \
  "v_mov_b32 v5, v146\n" \
  "v_mov_b32 v6, v147\n" \
  "s_nop 0\n" \
  "v_permlane16_swap_b32 v146, v5\n" \
  "v_permlane16_swap_b32 v147, v6\n" \
  "v_add_f32 v146, v146, v5\n" \
  "v_add_f32 v147, v147, v6\n" \
  "v_mov_b32 v5, v146\n" \
  "v_mov_b32 v6, v147\n" \
  "s_nop 0\n" \
  "v_permlane32_swap_b32 v146, v5\n" \
  "v_permlane32_swap_b32 v147, v6\n" \
  "v_add_f32 v146, v146, v5\n" \
  "v_add_f32 v147, v147, v6\n" \
  "v_fma_f32 v146, v160, v136, v146\n" \
  "v_fma_f32 v147, v162, v140, v147\n" \
  "v_rcp_f32 v146, v146\n" \
  "v_rcp_f32 v147, v147\n" \
  "v_mul_f32 v160, v160, v146\n" \
  "v_mul_f32 v162, v162, v147\n" \
  "v_mul_f32 v150, v150, v146\n" \
  "v_mul_f32 v151, v151, v146\n" \
  "v_mul_f32 v154, v154, v147\n" \
  "v_mul_f32 v155, v155, v147\n" \
  "v_cvt_pk_bf16_f32 v157, v150, v151\n" \
  "v_cvt_pk_bf16_f32 v159, v154, v155\n" \
  "v_pk_mul_f32 v[52:53], v[52:53], v[160:161] op_sel_hi:[1,0]\n" \
  "v_pk_mul_f32 v[54:55], v[54:55], v[160:161] op_sel_hi:[1,0]\n" \
  "v_pk_mul_f32 v[56:57], v[56:57], v[160:161] op_sel_hi:[1,0]\n" \
  "v_pk_mul_f32 v[58:59], v[58:59], v[160:161] op_sel_hi:[1,0]\n" \
  "v_pk_mul_f32 v[60:61], v[60:61], v[160:161] op_sel_hi:[1,0]\n" \
  "v_pk_mul_f32 v[62:63], v[62:63], v[160:161] op_sel_hi:[1,0]\n" \
  "v_pk_mul_f32 v[64:65], v[64:65], v[160:161] op_sel_hi:[1,0]\n" \
  "v_pk_mul_f32 v[66:67], v[66:67], v[160:161] op_sel_hi:[1,0]\n" \
  "v_pk_mul_f32 v[68:69], v[68:69], v[160:161] op_sel_hi:[1,0]\n" \
  "v_pk_mul_f32 v[70:71], v[70:71], v[160:161] op_sel_hi:[1,0]\n" \
  "v_pk_mul_f32 v[72:73], v[72:73], v[160:161] op_sel_hi:[1,0]\n" \
  "v_pk_mul_f32 v[74:75], v[74:75], v[160:161] op_sel_hi:[1,0]\n" \
  "v_pk_mul_f32 v[76:77], v[76:77], v[160:161] op_sel_hi:[1,0]\n" \
  "v_pk_mul_f32 v[78:79], v[78:79], v[160:161] op_sel_hi:[1,0]\n" \
  "v_pk_mul_f32 v[80:81], v[80:81], v[160:161] op_sel_hi:[1,0]\n" \
  "v_pk_mul_f32 v[82:83], v[82:83], v[160:161] op_sel_hi:[1,0]\n" \
  "v_pk_mul_f32 v[84:85], v[84:85], v[162:163] op_sel_hi:[1,0]\n" \
  "v_pk_mul_f32 v[86:87], v[86:87], v[162:163] op_sel_hi:[1,0]\n" \
  "v_pk_mul_f32 v[88:89], v[88:89], v[162:163] op_sel_hi:[1,0]\n" \
  "v_pk_mul_f32 v[90:91], v[90:91], v[162:163] op_sel_hi:[1,0]\n" \
  "v_pk_mul_f32 v[92:93], v[92:93], v[162:163] op_sel_hi:[1,0]\n" \
  "v_pk_mul_f32 v[94:95], v[94:95], v[162:163] op_sel_hi:[1,0]\n" \
  "v_pk_mul_f32 v[96:97], v[96:97], v[162:163] op_sel_hi:[1,0]\n" \
  "v_pk_mul_f32 v[98:99], v[98:99], v[162:163] op_sel_hi:[1,0]\n" \
  "v_pk_mul_f32 v[100:101], v[100:101], v[162:163] op_sel_hi:[1,0]\n" \
  "v_pk_mul_f32 v[102:103], v[102:103], v[162:163] op_sel_hi:[1,0]\n" \
  "v_pk_mul_f32 v[104:105], v[104:105], v[162:163] op_sel_hi:[1,0]\n" \
  "v_pk_mul_f32 v[106:107], v[106:107], v[162:163] op_sel_hi:[1,0]\n" \
  "v_pk_mul_f32 v[108:109], v[108:109], v[162:163] op_sel_hi:[1,0]\n" \
  "v_pk_mul_f32 v[110:111], v[110:111], v[162:163] op_sel_hi:[1,0]\n" \
  "v_pk_mul_f32 v[112:113], v[112:113], v[162:163] op_sel_hi:[1,0]\n" \
  "v_pk_mul_f32 v[114:115], v[114:115], v[162:163] op_sel_hi:[1,0]\n" \
  "s_waitcnt lgkmcnt(0)\n" \
  "v_mfma_f32_16x16x16_bf16 v[52:55], a[128:129], v[156:157], v[52:55]\n" \
  "v_mfma_f32_16x16x16_bf16 v[56:59], a[130:131], v[156:157], v[56:59]\n" \
  "v_mfma_f32_16x16x16_bf16 v[60:63], a[132:133], v[156:157], v[60:63]\n" \
  "v_mfma_f32_16x16x16_bf16 v[64:67], a[134:135], v[156:157], v[64:67]\n" \
  "v_mfma_f32_16x16x16_bf16 v[68:71], a[136:137], v[156:157], v[68:71]\n" \
  "v_cvt_pk_bf16_f32 v164, v52, v53\n" \
  "v_cvt_pk_bf16_f32 v165, v54, v55\n" \
  "v_mfma_f32_16x16x16_bf16 v[72:75], a[138:139], v[156:157], v[72:75]\n" \
  "v_cvt_pk_bf16_f32 v166, v56, v57\n" \
  "v_cvt_pk_bf16_f32 v167, v58, v59\n" \
  "buffer_store_dwordx4 v[164:167], v7, s[72:75], s86 offen offset:0 sc1\n" \
  "v_mfma_f32_16x16x16_bf16 v[76:79], a[140:141], v[156:157], v[76:79]\n" \
  "v_cvt_pk_bf16_f32 v168, v60, v61\n" \
  "v_cvt_pk_bf16_f32 v169, v62, v63\n" \
  "v_mfma_f32_16x16x16_bf16 v[80:83], a[142:143], v[156:157], v[80:83]\n" \
  "v_cvt_pk_bf16_f32 v170, v64, v65\n" \
  "v_cvt_pk_bf16_f32 v171, v66, v67\n" \
  "buffer_store_dwordx4 v[168:171], v7, s[72:75], s86 offen offset:1024 sc1\n" \
  "v_mfma_f32_16x16x16_bf16 v[84:87], a[128:129], v[158:159], v[84:87]\n" \
  "v_cvt_pk_bf16_f32 v172, v68, v69\n" \
  "v_cvt_pk_bf16_f32 v173, v70, v71\n" \
  "v_mfma_f32_16x16x16_bf16 v[88:91], a[130:131], v[158:159], v[88:91]\n" \
  "v_cvt_pk_bf16_f32 v174, v72, v73\n" \
  "v_cvt_pk_bf16_f32 v175, v74, v75\n" \
  "buffer_store_dwordx4 v[172:175], v7, s[72:75], s86 offen offset:2048 sc1\n" \
  "v_mfma_f32_16x16x16_bf16 v[92:95], a[132:133], v[158:159], v[92:95]\n" \
  "v_cvt_pk_bf16_f32 v176, v76, v77\n" \
  "v_cvt_pk_bf16_f32 v177, v78, v79\n" \
  "v_mfma_f32_16x16x16_bf16 v[96:99], a[134:135], v[158:159], v[96:99]\n" \
  "v_cvt_pk_bf16_f32 v178, v80, v81\n" \
  "v_cvt_pk_bf16_f32 v179, v82, v83\n" \
  "buffer_store_dwordx4 v[176:179], v7, s[72:75], s86 offen offset:3072 sc1\n" \
  "v_mfma_f32_16x16x16_bf16 v[100:103], a[136:137], v[158:159], v[100:103]\n" \
  "v_cvt_pk_bf16_f32 v180, v84, v85\n" \
  "v_cvt_pk_bf16_f32 v181, v86, v87\n" \
  "v_mfma_f32_16x16x16_bf16 v[104:107], a[138:139], v[158:159], v[104:107]\n" \
  "v_cvt_pk_bf16_f32 v182, v88, v89\n" \
  "v_cvt_pk_bf16_f32 v183, v90, v91\n" \
  "buffer_store_dwordx4 v[180:183], v17, s[72:75], s86 offen offset:0 sc1\n" \
  "v_mfma_f32_16x16x16_bf16 v[108:111], a[140:141], v[158:159], v[108:111]\n" \
  "v_cvt_pk_bf16_f32 v184, v92, v93\n" \
  "v_cvt_pk_bf16_f32 v185, v94, v95\n" \
  "v_mfma_f32_16x16x16_bf16 v[112:115], a[142:143], v[158:159], v[112:115]\n" \
  "v_cvt_pk_bf16_f32 v186, v96, v97\n" \
  "v_cvt_pk_bf16_f32 v187, v98, v99\n" \
  "buffer_store_dwordx4 v[184:187], v17, s[72:75], s86 offen offset:1024 sc1\n" \
  "v_cvt_pk_bf16_f32 v188, v100, v101\n" \
  "v_cvt_pk_bf16_f32 v189, v102, v103\n" \
  "v_cvt_pk_bf16_f32 v190, v104, v105\n" \
  "v_cvt_pk_bf16_f32 v191, v106, v107\n" \
  "buffer_store_dwordx4 v[188:191], v17, s[72:75], s86 offen offset:2048 sc1\n" \
  "s_nop 1\n" \
  "v_cvt_pk_bf16_f32 v192, v108, v109\n" \
  "v_cvt_pk_bf16_f32 v193, v110, v111\n" \
  "v_cvt_pk_bf16_f32 v194, v112, v113\n" \
  "v_cvt_pk_bf16_f32 v195, v114, v115\n" \
  "buffer_store_dwordx4 v[192:195], v17, s[72:75], s86 offen offset:3072 sc1\n" \
  "s_add_u32 s82, s82, 1\n" \
  "s_cmp_eq_u32 s82, 64\n" \
  "s_cbranch_scc1 LZ_%=\n" \
  "s_lshr_b32 s80, s82, 2\n" \
  "s_add_u32 s80, s80, " STR(EXP_B) "\n" \
  "v_readlane_b32 s80, %6, s80\n" \
  "s_lshl_b32 s87, s82, 19\n" \
  "s_add_u32 s87, s87, " STR(EXP_B) "<<21\n" \
  "s_xor_b32 s87, s87, s80\n" \
  "s_lshr_b32 s80, %3, 3\n" \
  "s_xor_b32 s80, s80, s82\n" \
  "s_and_b32 s80, s80, 3\n" \
  "s_mul_i32 s80, s80, 0x4800\n" \
  "s_and_b32 s80, s80, 0x5000\n" \
  "s_xor_b32 s87, s87, s80\n" \
  "s_and_b32 s80, %3, 7\n" \
  "s_lshl_b32 s80, s80, 16\n" \
  "s_or_b32 s87, s87, s80\n" \
  "s_waitcnt vmcnt(8)\n" \
  "v_readlane_b32 s91, v14, 0\n" \
  "v_readlane_b32 s92, v14, 1\n" \
  "v_readlane_b32 s80, v14, 2\n" \
  "s_min_u32 s92, s92, s80\n" \
  "v_readlane_b32 s80, v14, 3\n" \
  "s_min_u32 s92, s92, s80\n" \
  "v_readlane_b32 s80, v14, 4\n" \
  "s_min_u32 s92, s92, s80\n" \
  "s_cmp_gt_u32 s91, s82\n" \
  "s_cselect_b32 s90, 1, 0\n" \
  "s_cselect_b32 s80, 0, 0x80000000\n" \
  "v_or_b32 v9, s80, v7\n" \
  "v_or_b32 v10, s80, v18\n" \
  "buffer_load_dwordx4 v[28:31], v9, s[68:71], s87 offen offset:0 sc1\n" \
  "buffer_load_dwordx4 v[32:35], v9, s[68:71], s87 offen offset:1024 sc1\n" \
  "buffer_load_dwordx4 v[36:39], v9, s[68:71], s87 offen offset:2048 sc1\n" \
  "buffer_load_dwordx4 v[40:43], v9, s[68:71], s87 offen offset:3072 sc1\n" \
  "buffer_load_dwordx4 v[44:47], v10, s[68:71], s87 offen offset:0 sc1\n" \
  "buffer_load_dwordx4 v[48:51], v10, s[68:71], s87 offen offset:1024 sc1\n" \
  "s_waitcnt vmcnt(24)\n" \
  "s_add_u32 s81, s82, 2\n" \
  "s_min_u32 s81, s81, 64\n" \
  "s_sub_u32 s84, s81, 1\n" \
  "v_mfma_f32_16x16x32_bf16 v[116:119], a[64:67], a[32:35], v[20:23]\n" \
  "s_lshr_b32 s80, s84, 2\n" \
  "s_xnor_b32 s80, s80, %4\n" \
  "v_mfma_f32_16x16x32_bf16 v[120:123], a[64:67], a[48:51], 0\n" \
  "s_and_b32 s80, s80, 1\n" \
  "s_lshl_b32 s80, s80, 13\n" \
  "v_mfma_f32_16x16x32_bf16 v[124:127], a[80:83], a[48:51], v[20:23]\n" \
  "s_lshl_b32 s83, s84, 19\n" \
  "s_or_b32 s83, s83, s80\n" \
  "v_mfma_f32_16x16x32_bf16 v[116:119], a[68:71], a[36:39], v[116:119]\n" \
  "s_lshl_b32 s80, %3, 14\n" \
  "s_add_u32 s83, s83, s80\n" \
  "v_mfma_f32_16x16x32_bf16 v[120:123], a[68:71], a[52:55], v[120:123]\n" \
  "s_add_u32 s84, s83, " STR(QKV_SZ32) "\n" \
  "s_add_u32 s85, s84, " STR(QKV_SZ32) "\n" \
  "v_mfma_f32_16x16x32_bf16 v[124:127], a[84:87], a[52:55], v[124:127]\n" \
  "s_lshr_b32 s80, s82, 2\n" \
  "s_xnor_b32 s80, s80, %4\n" \
  "v_mfma_f32_16x16x32_bf16 v[116:119], a[72:75], a[40:43], v[116:119]\n" \
  "s_and_b32 s80, s80, 1\n" \
  "s_lshl_b32 s80, s80, 13\n" \
  "v_mfma_f32_16x16x32_bf16 v[120:123], a[72:75], a[56:59], v[120:123]\n" \
  "s_lshl_b32 s86, s82, 19\n" \
  "s_or_b32 s86, s86, s80\n" \
  "v_mfma_f32_16x16x32_bf16 v[124:127], a[88:91], a[56:59], v[124:127]\n" \
  "s_lshr_b32 s80, %3, 1\n" \
  "s_lshl_b32 s80, s80, 14\n" \
  "v_mfma_f32_16x16x32_bf16 v[116:119], a[76:79], a[44:47], v[116:119]\n" \
  "s_add_u32 s86, s86, s80\n" \
  "s_and_b32 s80, %3, 1\n" \
  "v_mfma_f32_16x16x32_bf16 v[120:123], a[76:79], a[60:63], v[120:123]\n" \
  "s_lshl_b32 s80, s80, 12\n" \
  "s_add_u32 s86, s86, s80\n" \
  "v_mfma_f32_16x16x32_bf16 v[124:127], a[92:95], a[60:63], v[124:127]\n" \
  "s_add_u32 s86, s86, 3*" STR(QKV_SZ32) "\n" \
  "s_cmp_lt_u32 s92, s81\n" \
  "s_cbranch_scc1 LG1_%=\n" \
  "LH1_%=:\n" \
  "buffer_load_dwordx4 a[64:67], v7, s[72:75], s84 offen offset:0 sc1\n" \
  "buffer_load_dwordx4 a[68:71], v7, s[72:75], s84 offen offset:1024 sc1\n" \
  "buffer_load_dwordx4 a[72:75], v7, s[72:75], s84 offen offset:2048 sc1\n" \
  "buffer_load_dwordx4 a[76:79], v7, s[72:75], s84 offen offset:3072 sc1\n" \
  "buffer_load_dwordx4 a[80:83], v8, s[72:75], s84 offen offset:0 sc1\n" \
  "buffer_load_dwordx4 a[84:87], v8, s[72:75], s84 offen offset:1024 sc1\n" \
  "v_max3_f32 v144, v116, v117, v118\n" \
  "v_max3_f32 v145, v120, v121, v122\n" \
  "v_max_f32 v144, v144, v119\n" \
  "v_max3_f32 v145, v145, v123, v124\n" \
  "v_max3_f32 v145, v145, v125, v126\n" \
  "v_max_f32 v145, v145, v127\n" \
  "v_mov_b32 v5, v144\n" \
  "v_mov_b32 v6, v145\n" \
  "s_nop 0\n" \
  "v_permlane16_swap_b32 v144, v5\n" \
  "v_permlane16_swap_b32 v145, v6\n" \
  "v_max_f32 v144, v144, v5\n" \
  "v_max_f32 v145, v145, v6\n" \
  "v_mov_b32 v5, v144\n" \
  "v_mov_b32 v6, v145\n" \
  "s_nop 0\n" \
  "v_permlane32_swap_b32 v144, v5\n" \
  "v_permlane32_swap_b32 v145, v6\n" \
  "v_max_f32 v144, v144, v5\n" \
  "v_max_f32 v145, v145, v6\n" \
  "v_mul_f32 v144, s89, v144\n" \
  "v_mul_f32 v145, s89, v145\n" \
  "v_fma_f32 v116, v116, s89, -v144\n" \
  "v_exp_f32 v116, v116\n" \
  "buffer_load_dwordx4 a[88:91], v8, s[72:75], s84 offen offset:2048 sc1\n" \
  "v_fma_f32 v117, v117, s89, -v144\n" \
  "v_exp_f32 v117, v117\n" \
  "buffer_load_dwordx4 a[92:95], v8, s[72:75], s84 offen offset:3072 sc1\n" \
  "v_fma_f32 v118, v118, s89, -v144\n" \
  "v_exp_f32 v118, v118\n" \
  "buffer_load_dwordx4 a[0:3], v7, s[72:75], s83 offen offset:0 sc1\n" \
  "v_fma_f32 v119, v119, s89, -v144\n" \
  "v_exp_f32 v119, v119\n" \
  "buffer_load_dwordx4 a[4:7], v7, s[72:75], s83 offen offset:1024 sc1\n" \
  "v_fma_f32 v120, v120, s89, -v145\n" \
  "v_exp_f32 v120, v120\n" \
  "buffer_load_dwordx4 a[8:11], v7, s[72:75], s83 offen offset:2048 sc1\n" \
  "v_fma_f32 v121, v121, s89, -v145\n" \
  "v_exp_f32 v121, v121\n" \
  "buffer_load_dwordx4 a[12:15], v7, s[72:75], s83 offen offset:3072 sc1\n" \
  "v_fma_f32 v122, v122, s89, -v145\n" \
  "v_exp_f32 v122, v122\n" \
  "buffer_load_dwordx4 a[16:19], v8, s[72:75], s83 offen offset:0 sc1\n" \
  "v_fma_f32 v123, v123, s89, -v145\n" \
  "v_exp_f32 v123, v123\n" \
  "buffer_load_dwordx4 a[20:23], v8, s[72:75], s83 offen offset:1024 sc1\n" \
  "v_fma_f32 v124, v124, s89, -v145\n" \
  "v_exp_f32 v124, v124\n" \
  "buffer_load_dwordx4 a[24:27], v8, s[72:75], s83 offen offset:2048 sc1\n" \
  "v_fma_f32 v125, v125, s89, -v145\n" \
  "v_exp_f32 v125, v125\n" \
  "buffer_load_dwordx4 a[28:31], v8, s[72:75], s83 offen offset:3072 sc1\n" \
  "v_fma_f32 v126, v126, s89, -v145\n" \
  "v_exp_f32 v126, v126\n" \
  "v_fma_f32 v127, v127, s89, -v145\n" \
  "v_exp_f32 v127, v127\n" \
  "s_nop 0\n" \
  "v_cvt_pk_bf16_f32 v128, v116, v117\n" \
  "v_cvt_pk_bf16_f32 v129, v118, v119\n" \
  "v_cvt_pk_bf16_f32 v132, v120, v121\n" \
  "v_cvt_pk_bf16_f32 v133, v122, v123\n" \
  "v_cvt_pk_bf16_f32 v134, v124, v125\n" \
  "v_cvt_pk_bf16_f32 v135, v126, v127\n" \
  "s_waitcnt vmcnt(22)\n" \
  "buffer_store_dword v16, v15, s[76:79], 0 offen sc1\n" \
  "v_mfma_f32_16x16x32_bf16 v[136:139], v[24:27], v[128:131], 0\n" \
  "v_mfma_f32_16x16x32_bf16 v[140:143], v[24:27], v[132:135], 0\n" \
  "v_add_u32 v16, 1, v16\n" \
  "v_mfma_f32_16x16x32_bf16 v[52:55], a[96:99], v[128:131], 0\n" \
  "v_mfma_f32_16x16x32_bf16 v[84:87], a[96:99], v[132:135], 0\n" \
  "buffer_load_dwordx4 a[96:99], v7, s[72:75], s85 offen offset:0 sc1\n" \
  "v_mfma_f32_16x16x32_bf16 v[56:59], a[100:103], v[128:131], 0\n" \
  "v_mfma_f32_16x16x32_bf16 v[88:91], a[100:103], v[132:135], 0\n" \
  "buffer_load_dwordx4 a[100:103], v7, s[72:75], s85 offen offset:1024 sc1\n" \
  "v_mfma_f32_16x16x32_bf16 v[60:63], a[104:107], v[128:131], 0\n" \
  "v_mfma_f32_16x16x32_bf16 v[92:95], a[104:107], v[132:135], 0\n" \
  "buffer_load_dwordx4 a[104:107], v7, s[72:75], s85 offen offset:2048 sc1\n" \
  "v_mfma_f32_16x16x32_bf16 v[64:67], a[108:111], v[128:131], 0\n" \
  "v_mfma_f32_16x16x32_bf16 v[96:99], a[108:111], v[132:135], 0\n" \
  "buffer_load_dwordx4 a[108:111], v7, s[72:75], s85 offen offset:3072 sc1\n" \
  "v_mfma_f32_16x16x32_bf16 v[68:71], a[112:115], v[128:131], 0\n" \
  "v_mfma_f32_16x16x32_bf16 v[100:103], a[112:115], v[132:135], 0\n" \
  "buffer_load_dwordx4 a[112:115], v8, s[72:75], s85 offen offset:0 sc1\n" \
  "v_mfma_f32_16x16x32_bf16 v[72:75], a[116:119], v[128:131], 0\n" \
  "v_mfma_f32_16x16x32_bf16 v[104:107], a[116:119], v[132:135], 0\n" \
  "buffer_load_dwordx4 a[116:119], v8, s[72:75], s85 offen offset:1024 sc1\n" \
  "v_mfma_f32_16x16x32_bf16 v[76:79], a[120:123], v[128:131], 0\n" \
  "v_mfma_f32_16x16x32_bf16 v[108:111], a[120:123], v[132:135], 0\n" \
  "buffer_load_dwordx4 a[120:123], v8, s[72:75], s85 offen offset:2048 sc1\n" \
  "v_mfma_f32_16x16x32_bf16 v[80:83], a[124:127], v[128:131], 0\n" \
  "v_mfma_f32_16x16x32_bf16 v[112:115], a[124:127], v[132:135], 0\n" \
  "buffer_load_dwordx4 a[124:127], v8, s[72:75], s85 offen offset:3072 sc1\n" \
  "buffer_load_dword v14, v13, s[76:79], 0 offen sc1\n" \
  "s_cmp_eq_u32 s90, 0\n" \
  "s_cbranch_scc1 LW1_%=\n" \
  "s_waitcnt vmcnt(26)\n" \
  "LM1_%=:\n" \
  "ds_write_b128 v11, v[44:47]\n" \
  "ds_write_b128 v11, v[48:51] offset:1024\n" \
  "v_mfma_f32_16x16x32_bf16 v[148:151], v[28:31], a[32:35], 0\n" \
  "v_max3_u32 v5, v28, v29, v30\n" \
  "v_max3_u32 v5, v5, v31, v32\n" \
  "v_mfma_f32_16x16x32_bf16 v[152:155], v[28:31], a[48:51], 0\n" \
  "v_max3_u32 v5, v5, v33, v34\n" \
  "v_max3_u32 v5, v5, v35, v36\n" \
  "v_mfma_f32_16x16x32_bf16 v[148:151], v[32:35], a[36:39], v[148:151]\n" \
  "v_max3_u32 v5, v5, v37, v38\n" \
  "v_max3_u32 v5, v5, v39, v40\n" \
  "v_mfma_f32_16x16x32_bf16 v[152:155], v[32:35], a[52:55], v[152:155]\n" \
  "v_max3_u32 v5, v5, v41, v42\n" \
  "v_max3_u32 v5, v5, v43, v44\n" \
  "v_mfma_f32_16x16x32_bf16 v[148:151], v[36:39], a[40:43], v[148:151]\n" \
  "v_max3_u32 v5, v5, v45, v46\n" \
  "v_max3_u32 v5, v5, v47, v48\n" \
  "v_mfma_f32_16x16x32_bf16 v[152:155], v[36:39], a[56:59], v[152:155]\n" \
  "v_max3_u32 v5, v5, v49, v50\n" \
  "v_max_u32 v5, v5, v51\n" \
  "v_mfma_f32_16x16x32_bf16 v[148:151], v[40:43], a[44:47], v[148:151]\n" \
  "v_cmp_eq_u32 vcc, -1, v5\n" \
  "v_mfma_f32_16x16x32_bf16 v[152:155], v[40:43], a[60:63], v[152:155]\n" \
  "ds_read_b64_tr_b16 a[128:129], v12 offset:0\n" \
  "ds_read_b64_tr_b16 a[130:131], v12 offset:256\n" \
  "ds_read_b64_tr_b16 a[132:133], v12 offset:512\n" \
  "ds_read_b64_tr_b16 a[134:135], v12 offset:768\n" \
  "ds_read_b64_tr_b16 a[136:137], v12 offset:1024\n" \
  "ds_read_b64_tr_b16 a[138:139], v12 offset:1280\n" \
  "ds_read_b64_tr_b16 a[140:141], v12 offset:1536\n" \
  "ds_read_b64_tr_b16 a[142:143], v12 offset:1792\n" \
  "s_cbranch_vccnz LR1_%=\n" \
  "v_max_f32 v146, v150, v151\n" \
  "v_max_f32 v147, v154, v155\n" \
  "v_mov_b32 v5, v146\n" \
  "v_mov_b32 v6, v147\n" \
  "s_nop 0\n" \
  "v_permlane16_swap_b32 v146, v5\n" \
  "v_permlane16_swap_b32 v147, v6\n" \
  "v_max_f32 v146, v146, v5\n" \
  "v_max_f32 v147, v147, v6\n" \
  "v_mov_b32 v5, v146\n" \
  "v_mov_b32 v6, v147\n" \
  "s_nop 0\n" \
  "v_permlane32_swap_b32 v146, v5\n" \
  "v_permlane32_swap_b32 v147, v6\n" \
  "v_max_f32 v146, v146, v5\n" \
  "v_max_f32 v147, v147, v6\n" \
  "v_mul_f32 v146, s89, v146\n" \
  "v_mul_f32 v147, s89, v147\n" \
  "v_max_f32 v146, v146, v144\n" \
  "v_max_f32 v147, v147, v145\n" \
  "v_sub_f32 v160, v144, v146\n" \
  "v_sub_f32 v162, v145, v147\n" \
  "v_fma_f32 v150, v150, s89, -v146\n" \
  "v_fma_f32 v151, v151, s89, -v146\n" \
  "v_fma_f32 v154, v154, s89, -v147\n" \
  "v_fma_f32 v155, v155, s89, -v147\n" \
  "v_exp_f32 v150, v150\n" \
  "v_exp_f32 v151, v151\n" \
  "v_exp_f32 v154, v154\n" \
  "v_exp_f32 v155, v155\n" \
  "v_exp_f32 v160, v160\n" \
  "v_exp_f32 v162, v162\n" \
  "v_add_f32 v146, v150, v151\n" \
  "v_add_f32 v147, v154, v155\n" \
  "v_mov_b32 v5, v146\n" \
  "v_mov_b32 v6, v147\n" \
  "s_nop 0\n" \
  "v_permlane16_swap_b32 v146, v5\n" \
  "v_permlane16_swap_b32 v147, v6\n" \
  "v_add_f32 v146, v146, v5\n" \
  "v_add_f32 v147, v147, v6\n" \
  "v_mov_b32 v5, v146\n" \
  "v_mov_b32 v6, v147\n" \
  "s_nop 0\n" \
  "v_permlane32_swap_b32 v146, v5\n" \
  "v_permlane32_swap_b32 v147, v6\n" \
  "v_add_f32 v146, v146, v5\n" \
  "v_add_f32 v147, v147, v6\n" \
  "v_fma_f32 v146, v160, v136, v146\n" \
  "v_fma_f32 v147, v162, v140, v147\n" \
  "v_rcp_f32 v146, v146\n" \
  "v_rcp_f32 v147, v147\n" \
  "v_mul_f32 v160, v160, v146\n" \
  "v_mul_f32 v162, v162, v147\n" \
  "v_mul_f32 v150, v150, v146\n" \
  "v_mul_f32 v151, v151, v146\n" \
  "v_mul_f32 v154, v154, v147\n" \
  "v_mul_f32 v155, v155, v147\n" \
  "v_cvt_pk_bf16_f32 v157, v150, v151\n" \
  "v_cvt_pk_bf16_f32 v159, v154, v155\n" \
  "v_pk_mul_f32 v[52:53], v[52:53], v[160:161] op_sel_hi:[1,0]\n" \
  "v_pk_mul_f32 v[54:55], v[54:55], v[160:161] op_sel_hi:[1,0]\n" \
  "v_pk_mul_f32 v[56:57], v[56:57], v[160:161] op_sel_hi:[1,0]\n" \
  "v_pk_mul_f32 v[58:59], v[58:59], v[160:161] op_sel_hi:[1,0]\n" \
  "v_pk_mul_f32 v[60:61], v[60:61], v[160:161] op_sel_hi:[1,0]\n" \
  "v_pk_mul_f32 v[62:63], v[62:63], v[160:161] op_sel_hi:[1,0]\n" \
  "v_pk_mul_f32 v[64:65], v[64:65], v[160:161] op_sel_hi:[1,0]\n" \
  "v_pk_mul_f32 v[66:67], v[66:67], v[160:161] op_sel_hi:[1,0]\n" \
  "v_pk_mul_f32 v[68:69], v[68:69], v[160:161] op_sel_hi:[1,0]\n" \
  "v_pk_mul_f32 v[70:71], v[70:71], v[160:161] op_sel_hi:[1,0]\n" \
  "v_pk_mul_f32 v[72:73], v[72:73], v[160:161] op_sel_hi:[1,0]\n" \
  "v_pk_mul_f32 v[74:75], v[74:75], v[160:161] op_sel_hi:[1,0]\n" \
  "v_pk_mul_f32 v[76:77], v[76:77], v[160:161] op_sel_hi:[1,0]\n" \
  "v_pk_mul_f32 v[78:79], v[78:79], v[160:161] op_sel_hi:[1,0]\n" \
  "v_pk_mul_f32 v[80:81], v[80:81], v[160:161] op_sel_hi:[1,0]\n" \
  "v_pk_mul_f32 v[82:83], v[82:83], v[160:161] op_sel_hi:[1,0]\n" \
  "v_pk_mul_f32 v[84:85], v[84:85], v[162:163] op_sel_hi:[1,0]\n" \
  "v_pk_mul_f32 v[86:87], v[86:87], v[162:163] op_sel_hi:[1,0]\n" \
  "v_pk_mul_f32 v[88:89], v[88:89], v[162:163] op_sel_hi:[1,0]\n" \
  "v_pk_mul_f32 v[90:91], v[90:91], v[162:163] op_sel_hi:[1,0]\n" \
  "v_pk_mul_f32 v[92:93], v[92:93], v[162:163] op_sel_hi:[1,0]\n" \
  "v_pk_mul_f32 v[94:95], v[94:95], v[162:163] op_sel_hi:[1,0]\n" \
  "v_pk_mul_f32 v[96:97], v[96:97], v[162:163] op_sel_hi:[1,0]\n" \
  "v_pk_mul_f32 v[98:99], v[98:99], v[162:163] op_sel_hi:[1,0]\n" \
  "v_pk_mul_f32 v[100:101], v[100:101], v[162:163] op_sel_hi:[1,0]\n" \
  "v_pk_mul_f32 v[102:103], v[102:103], v[162:163] op_sel_hi:[1,0]\n" \
  "v_pk_mul_f32 v[104:105], v[104:105], v[162:163] op_sel_hi:[1,0]\n" \
  "v_pk_mul_f32 v[106:107], v[106:107], v[162:163] op_sel_hi:[1,0]\n" \
  "v_pk_mul_f32 v[108:109], v[108:109], v[162:163] op_sel_hi:[1,0]\n" \
  "v_pk_mul_f32 v[110:111], v[110:111], v[162:163] op_sel_hi:[1,0]\n" \
  "v_pk_mul_f32 v[112:113], v[112:113], v[162:163] op_sel_hi:[1,0]\n" \
  "v_pk_mul_f32 v[114:115], v[114:115], v[162:163] op_sel_hi:[1,0]\n" \
  "s_waitcnt lgkmcnt(0)\n" \
  "v_mfma_f32_16x16x16_bf16 v[52:55], a[128:129], v[156:157], v[52:55]\n" \
  "v_mfma_f32_16x16x16_bf16 v[56:59], a[130:131], v[156:157], v[56:59]\n" \
  "v_mfma_f32_16x16x16_bf16 v[60:63], a[132:133], v[156:157], v[60:63]\n" \
  "v_mfma_f32_16x16x16_bf16 v[64:67], a[134:135], v[156:157], v[64:67]\n" \
  "v_mfma_f32_16x16x16_bf16 v[68:71], a[136:137], v[156:157], v[68:71]\n" \
  "v_cvt_pk_bf16_f32 v164, v52, v53\n" \
  "v_cvt_pk_bf16_f32 v165, v54, v55\n" \
  "v_mfma_f32_16x16x16_bf16 v[72:75], a[138:139], v[156:157], v[72:75]\n" \
  "v_cvt_pk_bf16_f32 v166, v56, v57\n" \
  "v_cvt_pk_bf16_f32 v167, v58, v59\n" \
  "buffer_store_dwordx4 v[164:167], v7, s[72:75], s86 offen offset:0 sc1\n" \
  "v_mfma_f32_16x16x16_bf16 v[76:79], a[140:141], v[156:157], v[76:79]\n" \
  "v_cvt_pk_bf16_f32 v168, v60, v61\n" \
  "v_cvt_pk_bf16_f32 v169, v62, v63\n" \
  "v_mfma_f32_16x16x16_bf16 v[80:83], a[142:143], v[156:157], v[80:83]\n" \
  "v_cvt_pk_bf16_f32 v170, v64, v65\n" \
  "v_cvt_pk_bf16_f32 v171, v66, v67\n" \
  "buffer_store_dwordx4 v[168:171], v7, s[72:75], s86 offen offset:1024 sc1\n" \
  "v_mfma_f32_16x16x16_bf16 v[84:87], a[128:129], v[158:159], v[84:87]\n" \
  "v_cvt_pk_bf16_f32 v172, v68, v69\n" \
  "v_cvt_pk_bf16_f32 v173, v70, v71\n" \
  "v_mfma_f32_16x16x16_bf16 v[88:91], a[130:131], v[158:159], v[88:91]\n" \
  "v_cvt_pk_bf16_f32 v174, v72, v73\n" \
  "v_cvt_pk_bf16_f32 v175, v74, v75\n" \
  "buffer_store_dwordx4 v[172:175], v7, s[72:75], s86 offen offset:2048 sc1\n" \
  "v_mfma_f32_16x16x16_bf16 v[92:95], a[132:133], v[158:159], v[92:95]\n" \
  "v_cvt_pk_bf16_f32 v176, v76, v77\n" \
  "v_cvt_pk_bf16_f32 v177, v78, v79\n" \
  "v_mfma_f32_16x16x16_bf16 v[96:99], a[134:135], v[158:159], v[96:99]\n" \
  "v_cvt_pk_bf16_f32 v178, v80, v81\n" \
  "v_cvt_pk_bf16_f32 v179, v82, v83\n" \
  "buffer_store_dwordx4 v[176:179], v7, s[72:75], s86 offen offset:3072 sc1\n" \
  "v_mfma_f32_16x16x16_bf16 v[100:103], a[136:137], v[158:159], v[100:103]\n" \
  "v_cvt_pk_bf16_f32 v180, v84, v85\n" \
  "v_cvt_pk_bf16_f32 v181, v86, v87\n" \
  "v_mfma_f32_16x16x16_bf16 v[104:107], a[138:139], v[158:159], v[104:107]\n" \
  "v_cvt_pk_bf16_f32 v182, v88, v89\n" \
  "v_cvt_pk_bf16_f32 v183, v90, v91\n" \
  "buffer_store_dwordx4 v[180:183], v17, s[72:75], s86 offen offset:0 sc1\n" \
  "v_mfma_f32_16x16x16_bf16 v[108:111], a[140:141], v[158:159], v[108:111]\n" \
  "v_cvt_pk_bf16_f32 v184, v92, v93\n" \
  "v_cvt_pk_bf16_f32 v185, v94, v95\n" \
  "v_mfma_f32_16x16x16_bf16 v[112:115], a[142:143], v[158:159], v[112:115]\n" \
  "v_cvt_pk_bf16_f32 v186, v96, v97\n" \
  "v_cvt_pk_bf16_f32 v187, v98, v99\n" \
  "buffer_store_dwordx4 v[184:187], v17, s[72:75], s86 offen offset:1024 sc1\n" \
  "v_cvt_pk_bf16_f32 v188, v100, v101\n" \
  "v_cvt_pk_bf16_f32 v189, v102, v103\n" \
  "v_cvt_pk_bf16_f32 v190, v104, v105\n" \
  "v_cvt_pk_bf16_f32 v191, v106, v107\n" \
  "buffer_store_dwordx4 v[188:191], v17, s[72:75], s86 offen offset:2048 sc1\n" \
  "s_nop 1\n" \
  "v_cvt_pk_bf16_f32 v192, v108, v109\n" \
  "v_cvt_pk_bf16_f32 v193, v110, v111\n" \
  "v_cvt_pk_bf16_f32 v194, v112, v113\n" \
  "v_cvt_pk_bf16_f32 v195, v114, v115\n" \
  "buffer_store_dwordx4 v[192:195], v17, s[72:75], s86 offen offset:3072 sc1\n" \
  "s_add_u32 s82, s82, 1\n" \
  "s_cmp_eq_u32 s82, 64\n" \
  "s_cbranch_scc1 LZ_%=\n" \
  "s_lshr_b32 s80, s82, 2\n" \
  "s_add_u32 s80, s80, " STR(EXP_B) "\n" \
  "v_readlane_b32 s80, %6, s80\n" \
  "s_lshl_b32 s87, s82, 19\n" \
  "s_add_u32 s87, s87, " STR(EXP_B) "<<21\n" \
  "s_xor_b32 s87, s87, s80\n" \
  "s_lshr_b32 s80, %3, 3\n" \
  "s_xor_b32 s80, s80, s82\n" \
  "s_and_b32 s80, s80, 3\n" \
  "s_mul_i32 s80, s80, 0x4800\n" \
  "s_and_b32 s80, s80, 0x5000\n" \
  "s_xor_b32 s87, s87, s80\n" \
  "s_and_b32 s80, %3, 7\n" \
  "s_lshl_b32 s80, s80, 16\n" \
  "s_or_b32 s87, s87, s80\n" \
  "s_waitcnt vmcnt(8)\n" \
  "v_readlane_b32 s91, v14, 0\n" \
  "v_readlane_b32 s92, v14, 1\n" \
  "v_readlane_b32 s80, v14, 2\n" \
  "s_min_u32 s92, s92, s80\n" \
  "v_readlane_b32 s80, v14, 3\n" \
  "s_min_u32 s92, s92, s80\n" \
  "v_readlane_b32 s80, v14, 4\n" \
  "s_min_u32 s92, s92, s80\n" \
  "s_cmp_gt_u32 s91, s82\n" \
  "s_cselect_b32 s90, 1, 0\n" \
  "s_cselect_b32 s80, 0, 0x80000000\n" \
  "v_or_b32 v9, s80, v7\n" \
  "v_or_b32 v10, s80, v18\n" \
  "buffer_load_dwordx4 v[28:31], v9, s[68:71], s87 offen offset:0 sc1\n" \
  "buffer_load_dwordx4 v[32:35], v9, s[68:71], s87 offen offset:1024 sc1\n" \
  "buffer_load_dwordx4 v[36:39], v9, s[68:71], s87 offen offset:2048 sc1\n" \
  "buffer_load_dwordx4 v[40:43], v9, s[68:71], s87 offen offset:3072 sc1\n" \
  "buffer_load_dwordx4 v[44:47], v10, s[68:71], s87 offen offset:0 sc1\n" \
  "buffer_load_dwordx4 v[48:51], v10, s[68:71], s87 offen offset:1024 sc1\n" \
  "s_branch LA_%=\n" \
  "LG0_%=:\n" \
  "buffer_load_dword v14, v13, s[76:79], 0 offen sc1\n" \
  "s_waitcnt vmcnt(0)\n" \
  "v_readlane_b32 s91, v14, 0\n" \
  "v_readlane_b32 s92, v14, 1\n" \
  "v_readlane_b32 s80, v14, 2\n" \
  "s_min_u32 s92, s92, s80\n" \
  "v_readlane_b32 s80, v14, 3\n" \
  "s_min_u32 s92, s92, s80\n" \
  "v_readlane_b32 s80, v14, 4\n" \
  "s_min_u32 s92, s92, s80\n" \
  "s_cmp_ge_u32 s92, s81\n" \
  "s_cbranch_scc1 LH0_%=\n" \
  "s_sleep 4\n" \
  "s_add_u32 s88, s88, 1\n" \
  "s_cmp_lt_u32 s88, " STR(SPIN_CAP) "\n" \
  "s_cbranch_scc1 LG0_%=\n" \
  "v_mov_b32 v5, " STR(CW_DEAD) "*4\n" \
  "v_mov_b32 v6, 1\n" \
  "buffer_store_dword v6, v5, s[76:79], 0 offen sc1\n" \
  "s_branch LH0_%=\n" \
  "LW0_%=:\n" \
  "s_add_u32 s81, s82, 1\n" \
  "LQ0_%=:\n" \
  "buffer_load_dword v14, v13, s[76:79], 0 offen sc1\n" \
  "s_waitcnt vmcnt(0)\n" \
  "v_readlane_b32 s91, v14, 0\n" \
  "v_readlane_b32 s92, v14, 1\n" \
  "v_readlane_b32 s80, v14, 2\n" \
  "s_min_u32 s92, s92, s80\n" \
  "v_readlane_b32 s80, v14, 3\n" \
  "s_min_u32 s92, s92, s80\n" \
  "v_readlane_b32 s80, v14, 4\n" \
  "s_min_u32 s92, s92, s80\n" \
  "s_cmp_ge_u32 s91, s81\n" \
  "s_cbranch_scc1 LR0_%=\n" \
  "s_sleep 2\n" \
  "s_add_u32 s88, s88, 1\n" \
  "s_cmp_lt_u32 s88, " STR(SPIN_CAP) "\n" \
  "s_cbranch_scc1 LQ0_%=\n" \
  "v_mov_b32 v5, " STR(CW_DEAD) "*4\n" \
  "v_mov_b32 v6, 1\n" \
  "buffer_store_dword v6, v5, s[76:79], 0 offen sc1\n" \
  "s_branch LR0_%=\n" \
  "LR0_%=:\n" \
  "buffer_load_dwordx4 v[28:31], v7, s[68:71], s87 offen offset:0 sc1\n" \
  "buffer_load_dwordx4 v[32:35], v7, s[68:71], s87 offen offset:1024 sc1\n" \
  "buffer_load_dwordx4 v[36:39], v7, s[68:71], s87 offen offset:2048 sc1\n" \
  "buffer_load_dwordx4 v[40:43], v7, s[68:71], s87 offen offset:3072 sc1\n" \
  "buffer_load_dwordx4 v[44:47], v18, s[68:71], s87 offen offset:0 sc1\n" \
  "buffer_load_dwordx4 v[48:51], v18, s[68:71], s87 offen offset:1024 sc1\n" \
  "s_waitcnt vmcnt(0)\n" \
  "v_max3_u32 v5, v28, v29, v30\n" \
  "v_max3_u32 v5, v5, v31, v32\n" \
  "v_max3_u32 v5, v5, v33, v34\n" \
  "v_max3_u32 v5, v5, v35, v36\n" \
  "v_max3_u32 v5, v5, v37, v38\n" \
  "v_max3_u32 v5, v5, v39, v40\n" \
  "v_max3_u32 v5, v5, v41, v42\n" \
  "v_max3_u32 v5, v5, v43, v44\n" \
  "v_max3_u32 v5, v5, v45, v46\n" \
  "v_max3_u32 v5, v5, v47, v48\n" \
  "v_max3_u32 v5, v5, v49, v50\n" \
  "v_max_u32 v5, v5, v51\n" \
  "v_cmp_eq_u32 vcc, -1, v5\n" \
  "s_cbranch_vccz LU0_%=\n" \
  "s_sleep 1\n" \
  "s_add_u32 s88, s88, 1\n" \
  "s_cmp_lt_u32 s88, " STR(SPIN_CAP) "\n" \
  "s_cbranch_scc1 LR0_%=\n" \
  "v_mov_b32 v5, " STR(CW_DEAD) "*4\n" \
  "v_mov_b32 v6, 1\n" \
  "buffer_store_dword v6, v5, s[76:79], 0 offen sc1\n" \
  "s_branch LV0_%=\n" \
  "LV0_%=:\n" \
  "v_mov_b64 v[28:29], 0\n" \
  "v_mov_b64 v[30:31], 0\n" \
  "v_mov_b64 v[32:33], 0\n" \
  "v_mov_b64 v[34:35], 0\n" \
  "v_mov_b64 v[36:37], 0\n" \
  "v_mov_b64 v[38:39], 0\n" \
  "v_mov_b64 v[40:41], 0\n" \
  "v_mov_b64 v[42:43], 0\n" \
  "v_mov_b64 v[44:45], 0\n" \
  "v_mov_b64 v[46:47], 0\n" \
  "v_mov_b64 v[48:49], 0\n" \
  "v_mov_b64 v[50:51], 0\n" \
  "LU0_%=:\n" \
  "s_nop 1\n" \
  "s_branch LM0_%=\n" \
  "LG1_%=:\n" \
  "buffer_load_dword v14, v13, s[76:79], 0 offen sc1\n" \
  "s_waitcnt vmcnt(0)\n" \
  "v_readlane_b32 s91, v14, 0\n" \
  "v_readlane_b32 s92, v14, 1\n" \
  "v_readlane_b32 s80, v14, 2\n" \
  "s_min_u32 s92, s92, s80\n" \
  "v_readlane_b32 s80, v14, 3\n" \
  "s_min_u32 s92, s92, s80\n" \
  "v_readlane_b32 s80, v14, 4\n" \
  "s_min_u32 s92, s92, s80\n" \
  "s_cmp_ge_u32 s92, s81\n" \
  "s_cbranch_scc1 LH1_%=\n" \
  "s_sleep 4\n" \
  "s_add_u32 s88, s88, 1\n" \
  "s_cmp_lt_u32 s88, " STR(SPIN_CAP) "\n" \
  "s_cbranch_scc1 LG1_%=\n" \
  "v_mov_b32 v5, " STR(CW_DEAD) "*4\n" \
  "v_mov_b32 v6, 1\n" \
  "buffer_store_dword v6, v5, s[76:79], 0 offen sc1\n" \
  "s_branch LH1_%=\n" \
  "LW1_%=:\n" \
  "s_add_u32 s81, s82, 1\n" \
  "LQ1_%=:\n" \
  "buffer_load_dword v14, v13, s[76:79], 0 offen sc1\n" \
  "s_waitcnt vmcnt(0)\n" \
  "v_readlane_b32 s91, v14, 0\n" \
  "v_readlane_b32 s92, v14, 1\n" \
  "v_readlane_b32 s80, v14, 2\n" \
  "s_min_u32 s92, s92, s80\n" \
  "v_readlane_b32 s80, v14, 3\n" \
  "s_min_u32 s92, s92, s80\n" \
  "v_readlane_b32 s80, v14, 4\n" \
  "s_min_u32 s92, s92, s80\n" \
  "s_cmp_ge_u32 s91, s81\n" \
  "s_cbranch_scc1 LR1_%=\n" \
  "s_sleep 2\n" \
  "s_add_u32 s88, s88, 1\n" \
  "s_cmp_lt_u32 s88, " STR(SPIN_CAP) "\n" \
  "s_cbranch_scc1 LQ1_%=\n" \
  "v_mov_b32 v5, " STR(CW_DEAD) "*4\n" \
  "v_mov_b32 v6, 1\n" \
  "buffer_store_dword v6, v5, s[76:79], 0 offen sc1\n" \
  "s_branch LR1_%=\n" \
  "LR1_%=:\n" \
  "buffer_load_dwordx4 v[28:31], v7, s[68:71], s87 offen offset:0 sc1\n" \
  "buffer_load_dwordx4 v[32:35], v7, s[68:71], s87 offen offset:1024 sc1\n" \
  "buffer_load_dwordx4 v[36:39], v7, s[68:71], s87 offen offset:2048 sc1\n" \
  "buffer_load_dwordx4 v[40:43], v7, s[68:71], s87 offen offset:3072 sc1\n" \
  "buffer_load_dwordx4 v[44:47], v18, s[68:71], s87 offen offset:0 sc1\n" \
  "buffer_load_dwordx4 v[48:51], v18, s[68:71], s87 offen offset:1024 sc1\n" \
  "s_waitcnt vmcnt(0)\n" \
  "v_max3_u32 v5, v28, v29, v30\n" \
  "v_max3_u32 v5, v5, v31, v32\n" \
  "v_max3_u32 v5, v5, v33, v34\n" \
  "v_max3_u32 v5, v5, v35, v36\n" \
  "v_max3_u32 v5, v5, v37, v38\n" \
  "v_max3_u32 v5, v5, v39, v40\n" \
  "v_max3_u32 v5, v5, v41, v42\n" \
  "v_max3_u32 v5, v5, v43, v44\n" \
  "v_max3_u32 v5, v5, v45, v46\n" \
  "v_max3_u32 v5, v5, v47, v48\n" \
  "v_max3_u32 v5, v5, v49, v50\n" \
  "v_max_u32 v5, v5, v51\n" \
  "v_cmp_eq_u32 vcc, -1, v5\n" \
  "s_cbranch_vccz LU1_%=\n" \
  "s_sleep 1\n" \
  "s_add_u32 s88, s88, 1\n" \
  "s_cmp_lt_u32 s88, " STR(SPIN_CAP) "\n" \
  "s_cbranch_scc1 LR1_%=\n" \
  "v_mov_b32 v5, " STR(CW_DEAD) "*4\n" \
  "v_mov_b32 v6, 1\n" \
  "buffer_store_dword v6, v5, s[76:79], 0 offen sc1\n" \
  "s_branch LV1_%=\n" \
  "LV1_%=:\n" \
  "v_mov_b64 v[28:29], 0\n" \
  "v_mov_b64 v[30:31], 0\n" \
  "v_mov_b64 v[32:33], 0\n" \
  "v_mov_b64 v[34:35], 0\n" \
  "v_mov_b64 v[36:37], 0\n" \
  "v_mov_b64 v[38:39], 0\n" \
  "v_mov_b64 v[40:41], 0\n" \
  "v_mov_b64 v[42:43], 0\n" \
  "v_mov_b64 v[44:45], 0\n" \
  "v_mov_b64 v[46:47], 0\n" \
  "v_mov_b64 v[48:49], 0\n" \
  "v_mov_b64 v[50:51], 0\n" \
  "LU1_%=:\n" \
  "s_nop 1\n" \
  "s_branch LM1_%=\n" \
  "LZ_%=:\n" \
  "s_waitcnt vmcnt(0)\n" \
  "buffer_store_dword v16, v15, s[76:79], 0 offen sc1\n" \
  "s_waitcnt vmcnt(0)\n" \
  ""

#define CONV_ASM \
  "s_waitcnt lgkmcnt(0)\n" \
  "ds_read_b64 a[0:1], %2 offset:32\n" \
  "ds_read_b64 a[2:3], %2 offset:40\n" \
  "ds_read_b128 a[4:7], %3 offset:5040\n" \
  "ds_read_b128 a[8:11], %3 offset:2480\n" \
  "ds_read_b64 a[12:13], %2 offset:2080\n" \
  "ds_read_b64 a[14:15], %2 offset:2088\n" \
  "ds_read_b64 a[16:17], %2 offset:0\n" \
  "ds_read_b64 a[18:19], %2 offset:8\n" \
  "ds_read_b128 a[20:23], %3 offset:5072\n" \
  "ds_read_b128 a[24:27], %3 offset:2512\n" \
  "ds_read_b64 a[28:29], %2 offset:2048\n" \
  "ds_read_b64 a[30:31], %2 offset:2056\n" \
  "s_waitcnt lgkmcnt(6)\n" \
  "v_mfma_f32_32x32x16_bf16 %1, a[0:3], a[4:7], 0\n" \
  "ds_read_b64 a[32:33], %2 offset:96\n" \
  "ds_read_b64 a[34:35], %2 offset:104\n" \
  "v_mfma_f32_32x32x16_bf16 %0, a[0:3], a[8:11], 0\n" \
  "ds_read_b128 a[36:39], %3 offset:4960\n" \
  "ds_read_b128 a[40:43], %3 offset:2400\n" \
  "v_mfma_f32_32x32x16_bf16 %1, a[12:15], a[8:11], %1\n" \
  "ds_read_b64 a[44:45], %2 offset:2144\n" \
  "ds_read_b64 a[46:47], %2 offset:2152\n" \
  "s_waitcnt lgkmcnt(6)\n" \
  "v_mfma_f32_32x32x16_bf16 %1, a[16:19], a[20:23], %1\n" \
  "ds_read_b64 a[0:1], %2 offset:64\n" \
  "ds_read_b64 a[2:3], %2 offset:72\n" \
  "v_mfma_f32_32x32x16_bf16 %0, a[16:19], a[24:27], %0\n" \
  "ds_read_b128 a[4:7], %3 offset:4992\n" \
  "ds_read_b128 a[8:11], %3 offset:2432\n" \
  "v_mfma_f32_32x32x16_bf16 %1, a[28:31], a[24:27], %1\n" \
  "ds_read_b64 a[12:13], %2 offset:2112\n" \
  "ds_read_b64 a[14:15], %2 offset:2120\n" \
  "s_waitcnt lgkmcnt(6)\n" \
  "v_mfma_f32_32x32x16_bf16 %1, a[32:35], a[36:39], %1\n" \
  "ds_read_b64 a[16:17], %2 offset:160\n" \
  "ds_read_b64 a[18:19], %2 offset:168\n" \
  "v_mfma_f32_32x32x16_bf16 %0, a[32:35], a[40:43], %0\n" \
  "ds_read_b128 a[20:23], %3 offset:4880\n" \
  "ds_read_b128 a[24:27], %3 offset:2320\n" \
  "v_mfma_f32_32x32x16_bf16 %1, a[44:47], a[40:43], %1\n" \
  "ds_read_b64 a[28:29], %2 offset:2208\n" \
  "ds_read_b64 a[30:31], %2 offset:2216\n" \
  "s_waitcnt lgkmcnt(6)\n" \
  "v_mfma_f32_32x32x16_bf16 %1, a[0:3], a[4:7], %1\n" \
  "ds_read_b64 a[32:33], %2 offset:128\n" \
  "ds_read_b64 a[34:35], %2 offset:136\n" \
  "v_mfma_f32_32x32x16_bf16 %0, a[0:3], a[8:11], %0\n" \
  "ds_read_b128 a[36:39], %3 offset:4912\n" \
  "ds_read_b128 a[40:43], %3 offset:2352\n" \
  "v_mfma_f32_32x32x16_bf16 %1, a[12:15], a[8:11], %1\n" \
  "ds_read_b64 a[44:45], %2 offset:2176\n" \
  "ds_read_b64 a[46:47], %2 offset:2184\n" \
  "s_waitcnt lgkmcnt(6)\n" \
  "v_mfma_f32_32x32x16_bf16 %1, a[16:19], a[20:23], %1\n" \
  "ds_read_b64 a[0:1], %2 offset:224\n" \
  "ds_read_b64 a[2:3], %2 offset:232\n" \
  "v_mfma_f32_32x32x16_bf16 %0, a[16:19], a[24:27], %0\n" \
  "ds_read_b128 a[4:7], %3 offset:4800\n" \
  "ds_read_b128 a[8:11], %3 offset:2240\n" \
  "v_mfma_f32_32x32x16_bf16 %1, a[28:31], a[24:27], %1\n" \
  "ds_read_b64 a[12:13], %2 offset:2272\n" \
  "ds_read_b64 a[14:15], %2 offset:2280\n" \
  "s_waitcnt lgkmcnt(6)\n" \
  "v_mfma_f32_32x32x16_bf16 %1, a[32:35], a[36:39], %1\n" \
  "ds_read_b64 a[16:17], %2 offset:192\n" \
  "ds_read_b64 a[18:19], %2 offset:200\n" \
  "v_mfma_f32_32x32x16_bf16 %0, a[32:35], a[40:43], %0\n" \
  "ds_read_b128 a[20:23], %3 offset:4832\n" \
  "ds_read_b128 a[24:27], %3 offset:2272\n" \
  "v_mfma_f32_32x32x16_bf16 %1, a[44:47], a[40:43], %1\n" \
  "ds_read_b64 a[28:29], %2 offset:2240\n" \
  "ds_read_b64 a[30:31], %2 offset:2248\n" \
  "s_waitcnt lgkmcnt(6)\n" \
  "v_mfma_f32_32x32x16_bf16 %1, a[0:3], a[4:7], %1\n" \
  "ds_read_b64 a[32:33], %2 offset:288\n" \
  "ds_read_b64 a[34:35], %2 offset:296\n" \
  "v_mfma_f32_32x32x16_bf16 %0, a[0:3], a[8:11], %0\n" \
  "ds_read_b128 a[36:39], %3 offset:4720\n" \
  "ds_read_b128 a[40:43], %3 offset:2160\n" \
  "v_mfma_f32_32x32x16_bf16 %1, a[12:15], a[8:11], %1\n" \
  "ds_read_b64 a[44:45], %2 offset:2336\n" \
  "ds_read_b64 a[46:47], %2 offset:2344\n" \
  "s_waitcnt lgkmcnt(6)\n" \
  "v_mfma_f32_32x32x16_bf16 %1, a[16:19], a[20:23], %1\n" \
  "ds_read_b64 a[0:1], %2 offset:256\n" \
  "ds_read_b64 a[2:3], %2 offset:264\n" \
  "v_mfma_f32_32x32x16_bf16 %0, a[16:19], a[24:27], %0\n" \
  "ds_read_b128 a[4:7], %3 offset:4752\n" \
  "ds_read_b128 a[8:11], %3 offset:2192\n" \
  "v_mfma_f32_32x32x16_bf16 %1, a[28:31], a[24:27], %1\n" \
  "ds_read_b64 a[12:13], %2 offset:2304\n" \
  "ds_read_b64 a[14:15], %2 offset:2312\n" \
  "s_waitcnt lgkmcnt(6)\n" \
  "v_mfma_f32_32x32x16_bf16 %1, a[32:35], a[36:39], %1\n" \
  "ds_read_b64 a[16:17], %2 offset:352\n" \
  "ds_read_b64 a[18:19], %2 offset:360\n" \
  "v_mfma_f32_32x32x16_bf16 %0, a[32:35], a[40:43], %0\n" \
  "ds_read_b128 a[20:23], %3 offset:4640\n" \
  "ds_read_b128 a[24:27], %3 offset:2080\n" \
  "v_mfma_f32_32x32x16_bf16 %1, a[44:47], a[40:43], %1\n" \
  "ds_read_b64 a[28:29], %2 offset:2400\n" \
  "ds_read_b64 a[30:31], %2 offset:2408\n" \
  "s_waitcnt lgkmcnt(6)\n" \
  "v_mfma_f32_32x32x16_bf16 %1, a[0:3], a[4:7], %1\n" \
  "ds_read_b64 a[32:33], %2 offset:320\n" \
  "ds_read_b64 a[34:35], %2 offset:328\n" \
  "v_mfma_f32_32x32x16_bf16 %0, a[0:3], a[8:11], %0\n" \
  "ds_read_b128 a[36:39], %3 offset:4672\n" \
  "ds_read_b128 a[40:43], %3 offset:2112\n" \
  "v_mfma_f32_32x32x16_bf16 %1, a[12:15], a[8:11], %1\n" \
  "ds_read_b64 a[44:45], %2 offset:2368\n" \
  "ds_read_b64 a[46:47], %2 offset:2376\n" \
  "s_waitcnt lgkmcnt(6)\n" \
  "v_mfma_f32_32x32x16_bf16 %1, a[16:19], a[20:23], %1\n" \
  "ds_read_b64 a[0:1], %2 offset:416\n" \
  "ds_read_b64 a[2:3], %2 offset:424\n" \
  "v_mfma_f32_32x32x16_bf16 %0, a[16:19], a[24:27], %0\n" \
  "ds_read_b128 a[4:7], %3 offset:4560\n" \
  "ds_read_b128 a[8:11], %3 offset:2000\n" \
  "v_mfma_f32_32x32x16_bf16 %1, a[28:31], a[24:27], %1\n" \
  "ds_read_b64 a[12:13], %2 offset:2464\n" \
  "ds_read_b64 a[14:15], %2 offset:2472\n" \
  "s_waitcnt lgkmcnt(6)\n" \
  "v_mfma_f32_32x32x16_bf16 %1, a[32:35], a[36:39], %1\n" \
  "ds_read_b64 a[16:17], %2 offset:384\n" \
  "ds_read_b64 a[18:19], %2 offset:392\n" \
  "v_mfma_f32_32x32x16_bf16 %0, a[32:35], a[40:43], %0\n" \
  "ds_read_b128 a[20:23], %3 offset:4592\n" \
  "ds_read_b128 a[24:27], %3 offset:2032\n" \
  "v_mfma_f32_32x32x16_bf16 %1, a[44:47], a[40:43], %1\n" \
  "ds_read_b64 a[28:29], %2 offset:2432\n" \
  "ds_read_b64 a[30:31], %2 offset:2440\n" \
  "s_waitcnt lgkmcnt(6)\n" \
  "v_mfma_f32_32x32x16_bf16 %1, a[0:3], a[4:7], %1\n" \
  "ds_read_b64 a[32:33], %2 offset:480\n" \
  "ds_read_b64 a[34:35], %2 offset:488\n" \
  "v_mfma_f32_32x32x16_bf16 %0, a[0:3], a[8:11], %0\n" \
  "ds_read_b128 a[36:39], %3 offset:4480\n" \
  "ds_read_b128 a[40:43], %3 offset:1920\n" \
  "v_mfma_f32_32x32x16_bf16 %1, a[12:15], a[8:11], %1\n" \
  "ds_read_b64 a[44:45], %2 offset:2528\n" \
  "ds_read_b64 a[46:47], %2 offset:2536\n" \
  "s_waitcnt lgkmcnt(6)\n" \
  "v_mfma_f32_32x32x16_bf16 %1, a[16:19], a[20:23], %1\n" \
  "ds_read_b64 a[0:1], %2 offset:448\n" \
  "ds_read_b64 a[2:3], %2 offset:456\n" \
  "v_mfma_f32_32x32x16_bf16 %0, a[16:19], a[24:27], %0\n" \
  "ds_read_b128 a[4:7], %3 offset:4512\n" \
  "ds_read_b128 a[8:11], %3 offset:1952\n" \
  "v_mfma_f32_32x32x16_bf16 %1, a[28:31], a[24:27], %1\n" \
  "ds_read_b64 a[12:13], %2 offset:2496\n" \
  "ds_read_b64 a[14:15], %2 offset:2504\n" \
  "s_waitcnt lgkmcnt(6)\n" \
  "v_mfma_f32_32x32x16_bf16 %1, a[32:35], a[36:39], %1\n" \
  "ds_read_b64 a[16:17], %2 offset:544\n" \
  "ds_read_b64 a[18:19], %2 offset:552\n" \
  "v_mfma_f32_32x32x16_bf16 %0, a[32:35], a[40:43], %0\n" \
  "ds_read_b128 a[20:23], %3 offset:4400\n" \
  "ds_read_b128 a[24:27], %3 offset:1840\n" \
  "v_mfma_f32_32x32x16_bf16 %1, a[44:47], a[40:43], %1\n" \
  "ds_read_b64 a[28:29], %2 offset:2592\n" \
  "ds_read_b64 a[30:31], %2 offset:2600\n" \
  "s_waitcnt lgkmcnt(6)\n" \
  "v_mfma_f32_32x32x16_bf16 %1, a[0:3], a[4:7], %1\n" \
  "ds_read_b64 a[32:33], %2 offset:512\n" \
  "ds_read_b64 a[34:35], %2 offset:520\n" \
  "v_mfma_f32_32x32x16_bf16 %0, a[0:3], a[8:11], %0\n" \
  "ds_read_b128 a[36:39], %3 offset:4432\n" \
  "ds_read_b128 a[40:43], %3 offset:1872\n" \
  "v_mfma_f32_32x32x16_bf16 %1, a[12:15], a[8:11], %1\n" \
  "ds_read_b64 a[44:45], %2 offset:2560\n" \
  "ds_read_b64 a[46:47], %2 offset:2568\n" \
  "s_waitcnt lgkmcnt(6)\n" \
  "v_mfma_f32_32x32x16_bf16 %1, a[16:19], a[20:23], %1\n" \
  "ds_read_b64 a[0:1], %2 offset:608\n" \
  "ds_read_b64 a[2:3], %2 offset:616\n" \
  "v_mfma_f32_32x32x16_bf16 %0, a[16:19], a[24:27], %0\n" \
  "ds_read_b128 a[4:7], %3 offset:4320\n" \
  "ds_read_b128 a[8:11], %3 offset:1760\n" \
  "v_mfma_f32_32x32x16_bf16 %1, a[28:31], a[24:27], %1\n" \
  "ds_read_b64 a[12:13], %2 offset:2656\n" \
  "ds_read_b64 a[14:15], %2 offset:2664\n" \
  "s_waitcnt lgkmcnt(6)\n" \
  "v_mfma_f32_32x32x16_bf16 %1, a[32:35], a[36:39], %1\n" \
  "ds_read_b64 a[16:17], %2 offset:576\n" \
  "ds_read_b64 a[18:19], %2 offset:584\n" \
  "v_mfma_f32_32x32x16_bf16 %0, a[32:35], a[40:43], %0\n" \
  "ds_read_b128 a[20:23], %3 offset:4352\n" \
  "ds_read_b128 a[24:27], %3 offset:1792\n" \
  "v_mfma_f32_32x32x16_bf16 %1, a[44:47], a[40:43], %1\n" \
  "ds_read_b64 a[28:29], %2 offset:2624\n" \
  "ds_read_b64 a[30:31], %2 offset:2632\n" \
  "s_waitcnt lgkmcnt(6)\n" \
  "v_mfma_f32_32x32x16_bf16 %1, a[0:3], a[4:7], %1\n" \
  "ds_read_b64 a[32:33], %2 offset:672\n" \
  "ds_read_b64 a[34:35], %2 offset:680\n" \
  "v_mfma_f32_32x32x16_bf16 %0, a[0:3], a[8:11], %0\n" \
  "ds_read_b128 a[36:39], %3 offset:4240\n" \
  "ds_read_b128 a[40:43], %3 offset:1680\n" \
  "v_mfma_f32_32x32x16_bf16 %1, a[12:15], a[8:11], %1\n" \
  "ds_read_b64 a[44:45], %2 offset:2720\n" \
  "ds_read_b64 a[46:47], %2 offset:2728\n" \
  "s_waitcnt lgkmcnt(6)\n" \
  "v_mfma_f32_32x32x16_bf16 %1, a[16:19], a[20:23], %1\n" \
  "ds_read_b64 a[0:1], %2 offset:640\n" \
  "ds_read_b64 a[2:3], %2 offset:648\n" \
  "v_mfma_f32_32x32x16_bf16 %0, a[16:19], a[24:27], %0\n" \
  "ds_read_b128 a[4:7], %3 offset:4272\n" \
  "ds_read_b128 a[8:11], %3 offset:1712\n" \
  "v_mfma_f32_32x32x16_bf16 %1, a[28:31], a[24:27], %1\n" \
  "ds_read_b64 a[12:13], %2 offset:2688\n" \
  "ds_read_b64 a[14:15], %2 offset:2696\n" \
  "s_waitcnt lgkmcnt(6)\n" \
  "v_mfma_f32_32x32x16_bf16 %1, a[32:35], a[36:39], %1\n" \
  "ds_read_b64 a[16:17], %2 offset:736\n" \
  "ds_read_b64 a[18:19], %2 offset:744\n" \
  "v_mfma_f32_32x32x16_bf16 %0, a[32:35], a[40:43], %0\n" \
  "ds_read_b128 a[20:23], %3 offset:4160\n" \
  "ds_read_b128 a[24:27], %3 offset:1600\n" \
  "v_mfma_f32_32x32x16_bf16 %1, a[44:47], a[40:43], %1\n" \
  "ds_read_b64 a[28:29], %2 offset:2784\n" \
  "ds_read_b64 a[30:31], %2 offset:2792\n" \
  "s_waitcnt lgkmcnt(6)\n" \
  "v_mfma_f32_32x32x16_bf16 %1, a[0:3], a[4:7], %1\n" \
  "ds_read_b64 a[32:33], %2 offset:704\n" \
  "ds_read_b64 a[34:35], %2 offset:712\n" \
  "v_mfma_f32_32x32x16_bf16 %0, a[0:3], a[8:11], %0\n" \
  "ds_read_b128 a[36:39], %3 offset:4192\n" \
  "ds_read_b128 a[40:43], %3 offset:1632\n" \
  "v_mfma_f32_32x32x16_bf16 %1, a[12:15], a[8:11], %1\n" \
  "ds_read_b64 a[44:45], %2 offset:2752\n" \
  "ds_read_b64 a[46:47], %2 offset:2760\n" \
  "s_waitcnt lgkmcnt(6)\n" \
  "v_mfma_f32_32x32x16_bf16 %1, a[16:19], a[20:23], %1\n" \
  "ds_read_b64 a[0:1], %2 offset:800\n" \
  "ds_read_b64 a[2:3], %2 offset:808\n" \
  "v_mfma_f32_32x32x16_bf16 %0, a[16:19], a[24:27], %0\n" \
  "ds_read_b128 a[4:7], %3 offset:4080\n" \
  "ds_read_b128 a[8:11], %3 offset:1520\n" \
  "v_mfma_f32_32x32x16_bf16 %1, a[28:31], a[24:27], %1\n" \
  "ds_read_b64 a[12:13], %2 offset:2848\n" \
  "ds_read_b64 a[14:15], %2 offset:2856\n" \
  "s_waitcnt lgkmcnt(6)\n" \
  "v_mfma_f32_32x32x16_bf16 %1, a[32:35], a[36:39], %1\n" \
  "ds_read_b64 a[16:17], %2 offset:768\n" \
  "ds_read_b64 a[18:19], %2 offset:776\n" \
  "v_mfma_f32_32x32x16_bf16 %0, a[32:35], a[40:43], %0\n" \
  "ds_read_b128 a[20:23], %3 offset:4112\n" \
  "ds_read_b128 a[24:27], %3 offset:1552\n" \
  "v_mfma_f32_32x32x16_bf16 %1, a[44:47], a[40:43], %1\n" \
  "ds_read_b64 a[28:29], %2 offset:2816\n" \
  "ds_read_b64 a[30:31], %2 offset:2824\n" \
  "s_waitcnt lgkmcnt(6)\n" \
  "v_mfma_f32_32x32x16_bf16 %1, a[0:3], a[4:7], %1\n" \
  "ds_read_b64 a[32:33], %2 offset:864\n" \
  "ds_read_b64 a[34:35], %2 offset:872\n" \
  "v_mfma_f32_32x32x16_bf16 %0, a[0:3], a[8:11], %0\n" \
  "ds_read_b128 a[36:39], %3 offset:4000\n" \
  "ds_read_b128 a[40:43], %3 offset:1440\n" \
  "v_mfma_f32_32x32x16_bf16 %1, a[12:15], a[8:11], %1\n" \
  "ds_read_b64 a[44:45], %2 offset:2912\n" \
  "ds_read_b64 a[46:47], %2 offset:2920\n" \
  "s_waitcnt lgkmcnt(6)\n" \
  "v_mfma_f32_32x32x16_bf16 %1, a[16:19], a[20:23], %1\n" \
  "ds_read_b64 a[0:1], %2 offset:832\n" \
  "ds_read_b64 a[2:3], %2 offset:840\n" \
  "v_mfma_f32_32x32x16_bf16 %0, a[16:19], a[24:27], %0\n" \
  "ds_read_b128 a[4:7], %3 offset:4032\n" \
  "ds_read_b128 a[8:11], %3 offset:1472\n" \
  "v_mfma_f32_32x32x16_bf16 %1, a[28:31], a[24:27], %1\n" \
  "ds_read_b64 a[12:13], %2 offset:2880\n" \
  "ds_read_b64 a[14:15], %2 offset:2888\n" \
  "s_waitcnt lgkmcnt(6)\n" \
  "v_mfma_f32_32x32x16_bf16 %1, a[32:35], a[36:39], %1\n" \
  "ds_read_b64 a[16:17], %2 offset:928\n" \
  "ds_read_b64 a[18:19], %2 offset:936\n" \
  "v_mfma_f32_32x32x16_bf16 %0, a[32:35], a[40:43], %0\n" \
  "ds_read_b128 a[20:23], %3 offset:3920\n" \
  "ds_read_b128 a[24:27], %3 offset:1360\n" \
  "v_mfma_f32_32x32x16_bf16 %1, a[44:47], a[40:43], %1\n" \
  "ds_read_b64 a[28:29], %2 offset:2976\n" \
  "ds_read_b64 a[30:31], %2 offset:2984\n" \
  "s_waitcnt lgkmcnt(6)\n" \
  "v_mfma_f32_32x32x16_bf16 %1, a[0:3], a[4:7], %1\n" \
  "ds_read_b64 a[32:33], %2 offset:896\n" \
  "ds_read_b64 a[34:35], %2 offset:904\n" \
  "v_mfma_f32_32x32x16_bf16 %0, a[0:3], a[8:11], %0\n" \
  "ds_read_b128 a[36:39], %3 offset:3952\n" \
  "ds_read_b128 a[40:43], %3 offset:1392\n" \
  "v_mfma_f32_32x32x16_bf16 %1, a[12:15], a[8:11], %1\n" \
  "ds_read_b64 a[44:45], %2 offset:2944\n" \
  "ds_read_b64 a[46:47], %2 offset:2952\n" \
  "s_waitcnt lgkmcnt(6)\n" \
  "v_mfma_f32_32x32x16_bf16 %1, a[16:19], a[20:23], %1\n" \
  "ds_read_b64 a[0:1], %2 offset:992\n" \
  "ds_read_b64 a[2:3], %2 offset:1000\n" \
  "v_mfma_f32_32x32x16_bf16 %0, a[16:19], a[24:27], %0\n" \
  "ds_read_b128 a[4:7], %3 offset:3840\n" \
  "ds_read_b128 a[8:11], %3 offset:1280\n" \
  "v_mfma_f32_32x32x16_bf16 %1, a[28:31], a[24:27], %1\n" \
  "ds_read_b64 a[12:13], %2 offset:3040\n" \
  "ds_read_b64 a[14:15], %2 offset:3048\n" \
  "s_waitcnt lgkmcnt(6)\n" \
  "v_mfma_f32_32x32x16_bf16 %1, a[32:35], a[36:39], %1\n" \
  "ds_read_b64 a[16:17], %2 offset:960\n" \
  "ds_read_b64 a[18:19], %2 offset:968\n" \
  "v_mfma_f32_32x32x16_bf16 %0, a[32:35], a[40:43], %0\n" \
  "ds_read_b128 a[20:23], %3 offset:3872\n" \
  "ds_read_b128 a[24:27], %3 offset:1312\n" \
  "v_mfma_f32_32x32x16_bf16 %1, a[44:47], a[40:43], %1\n" \
  "ds_read_b64 a[28:29], %2 offset:3008\n" \
  "ds_read_b64 a[30:31], %2 offset:3016\n" \
  "s_waitcnt lgkmcnt(6)\n" \
  "v_mfma_f32_32x32x16_bf16 %1, a[0:3], a[4:7], %1\n" \
  "ds_read_b64 a[32:33], %2 offset:1056\n" \
  "ds_read_b64 a[34:35], %2 offset:1064\n" \
  "v_mfma_f32_32x32x16_bf16 %0, a[0:3], a[8:11], %0\n" \
  "ds_read_b128 a[36:39], %3 offset:3760\n" \
  "ds_read_b128 a[40:43], %3 offset:1200\n" \
  "v_mfma_f32_32x32x16_bf16 %1, a[12:15], a[8:11], %1\n" \
  "ds_read_b64 a[44:45], %2 offset:3104\n" \
  "ds_read_b64 a[46:47], %2 offset:3112\n" \
  "s_waitcnt lgkmcnt(6)\n" \
  "v_mfma_f32_32x32x16_bf16 %1, a[16:19], a[20:23], %1\n" \
  "ds_read_b64 a[0:1], %2 offset:1024\n" \
  "ds_read_b64 a[2:3], %2 offset:1032\n" \
  "v_mfma_f32_32x32x16_bf16 %0, a[16:19], a[24:27], %0\n" \
  "ds_read_b128 a[4:7], %3 offset:3792\n" \
  "ds_read_b128 a[8:11], %3 offset:1232\n" \
  "v_mfma_f32_32x32x16_bf16 %1, a[28:31], a[24:27], %1\n" \
  "ds_read_b64 a[12:13], %2 offset:3072\n" \
  "ds_read_b64 a[14:15], %2 offset:3080\n" \
  "s_waitcnt lgkmcnt(6)\n" \
  "v_mfma_f32_32x32x16_bf16 %1, a[32:35], a[36:39], %1\n" \
  "ds_read_b64 a[16:17], %2 offset:1120\n" \
  "ds_read_b64 a[18:19], %2 offset:1128\n" \
  "v_mfma_f32_32x32x16_bf16 %0, a[32:35], a[40:43], %0\n" \
  "ds_read_b128 a[20:23], %3 offset:3680\n" \
  "ds_read_b128 a[24:27], %3 offset:1120\n" \
  "v_mfma_f32_32x32x16_bf16 %1, a[44:47], a[40:43], %1\n" \
  "ds_read_b64 a[28:29], %2 offset:3168\n" \
  "ds_read_b64 a[30:31], %2 offset:3176\n" \
  "s_waitcnt lgkmcnt(6)\n" \
  "v_mfma_f32_32x32x16_bf16 %1, a[0:3], a[4:7], %1\n" \
  "ds_read_b64 a[32:33], %2 offset:1088\n" \
  "ds_read_b64 a[34:35], %2 offset:1096\n" \
  "v_mfma_f32_32x32x16_bf16 %0, a[0:3], a[8:11], %0\n" \
  "ds_read_b128 a[36:39], %3 offset:3712\n" \
  "ds_read_b128 a[40:43], %3 offset:1152\n" \
  "v_mfma_f32_32x32x16_bf16 %1, a[12:15], a[8:11], %1\n" \
  "ds_read_b64 a[44:45], %2 offset:3136\n" \
  "ds_read_b64 a[46:47], %2 offset:3144\n" \
  "s_waitcnt lgkmcnt(6)\n" \
  "v_mfma_f32_32x32x16_bf16 %1, a[16:19], a[20:23], %1\n" \
  "ds_read_b64 a[0:1], %2 offset:1184\n" \
  "ds_read_b64 a[2:3], %2 offset:1192\n" \
  "v_mfma_f32_32x32x16_bf16 %0, a[16:19], a[24:27], %0\n" \
  "ds_read_b128 a[4:7], %3 offset:3600\n" \
  "ds_read_b128 a[8:11], %3 offset:1040\n" \
  "v_mfma_f32_32x32x16_bf16 %1, a[28:31], a[24:27], %1\n" \
  "ds_read_b64 a[12:13], %2 offset:3232\n" \
  "ds_read_b64 a[14:15], %2 offset:3240\n" \
  "s_waitcnt lgkmcnt(6)\n" \
  "v_mfma_f32_32x32x16_bf16 %1, a[32:35], a[36:39], %1\n" \
  "ds_read_b64 a[16:17], %2 offset:1152\n" \
  "ds_read_b64 a[18:19], %2 offset:1160\n" \
  "v_mfma_f32_32x32x16_bf16 %0, a[32:35], a[40:43], %0\n" \
  "ds_read_b128 a[20:23], %3 offset:3632\n" \
  "ds_read_b128 a[24:27], %3 offset:1072\n" \
  "v_mfma_f32_32x32x16_bf16 %1, a[44:47], a[40:43], %1\n" \
  "ds_read_b64 a[28:29], %2 offset:3200\n" \
  "ds_read_b64 a[30:31], %2 offset:3208\n" \
  "s_waitcnt lgkmcnt(6)\n" \
  "v_mfma_f32_32x32x16_bf16 %1, a[0:3], a[4:7], %1\n" \
  "ds_read_b64 a[32:33], %2 offset:1248\n" \
  "ds_read_b64 a[34:35], %2 offset:1256\n" \
  "v_mfma_f32_32x32x16_bf16 %0, a[0:3], a[8:11], %0\n" \
  "ds_read_b128 a[36:39], %3 offset:3520\n" \
  "ds_read_b128 a[40:43], %3 offset:960\n" \
  "v_mfma_f32_32x32x16_bf16 %1, a[12:15], a[8:11], %1\n" \
  "ds_read_b64 a[44:45], %2 offset:3296\n" \
  "ds_read_b64 a[46:47], %2 offset:3304\n" \
  "s_waitcnt lgkmcnt(6)\n" \
  "v_mfma_f32_32x32x16_bf16 %1, a[16:19], a[20:23], %1\n" \
  "ds_read_b64 a[0:1], %2 offset:1216\n" \
  "ds_read_b64 a[2:3], %2 offset:1224\n" \
  "v_mfma_f32_32x32x16_bf16 %0, a[16:19], a[24:27], %0\n" \
  "ds_read_b128 a[4:7], %3 offset:3552\n" \
  "ds_read_b128 a[8:11], %3 offset:992\n" \
  "v_mfma_f32_32x32x16_bf16 %1, a[28:31], a[24:27], %1\n" \
  "ds_read_b64 a[12:13], %2 offset:3264\n" \
  "ds_read_b64 a[14:15], %2 offset:3272\n" \
  "s_waitcnt lgkmcnt(6)\n" \
  "v_mfma_f32_32x32x16_bf16 %1, a[32:35], a[36:39], %1\n" \
  "ds_read_b64 a[16:17], %2 offset:1312\n" \
  "ds_read_b64 a[18:19], %2 offset:1320\n" \
  "v_mfma_f32_32x32x16_bf16 %0, a[32:35], a[40:43], %0\n" \
  "ds_read_b128 a[20:23], %3 offset:3440\n" \
  "ds_read_b128 a[24:27], %3 offset:880\n" \
  "v_mfma_f32_32x32x16_bf16 %1, a[44:47], a[40:43], %1\n" \
  "ds_read_b64 a[28:29], %2 offset:3360\n" \
  "ds_read_b64 a[30:31], %2 offset:3368\n" \
  "s_waitcnt lgkmcnt(6)\n" \
  "v_mfma_f32_32x32x16_bf16 %1, a[0:3], a[4:7], %1\n" \
  "ds_read_b64 a[32:33], %2 offset:1280\n" \
  "ds_read_b64 a[34:35], %2 offset:1288\n" \
  "v_mfma_f32_32x32x16_bf16 %0, a[0:3], a[8:11], %0\n" \
  "ds_read_b128 a[36:39], %3 offset:3472\n" \
  "ds_read_b128 a[40:43], %3 offset:912\n" \
  "v_mfma_f32_32x32x16_bf16 %1, a[12:15], a[8:11], %1\n" \
  "ds_read_b64 a[44:45], %2 offset:3328\n" \
  "ds_read_b64 a[46:47], %2 offset:3336\n" \
  "s_waitcnt lgkmcnt(6)\n" \
  "v_mfma_f32_32x32x16_bf16 %1, a[16:19], a[20:23], %1\n" \
  "ds_read_b64 a[0:1], %2 offset:1376\n" \
  "ds_read_b64 a[2:3], %2 offset:1384\n" \
  "v_mfma_f32_32x32x16_bf16 %0, a[16:19], a[24:27], %0\n" \
  "ds_read_b128 a[4:7], %3 offset:3360\n" \
  "ds_read_b128 a[8:11], %3 offset:800\n" \
  "v_mfma_f32_32x32x16_bf16 %1, a[28:31], a[24:27], %1\n" \
  "ds_read_b64 a[12:13], %2 offset:3424\n" \
  "ds_read_b64 a[14:15], %2 offset:3432\n" \
  "s_waitcnt lgkmcnt(6)\n" \
  "v_mfma_f32_32x32x16_bf16 %1, a[32:35], a[36:39], %1\n" \
  "ds_read_b64 a[16:17], %2 offset:1344\n" \
  "ds_read_b64 a[18:19], %2 offset:1352\n" \
  "v_mfma_f32_32x32x16_bf16 %0, a[32:35], a[40:43], %0\n" \
  "ds_read_b128 a[20:23], %3 offset:3392\n" \
  "ds_read_b128 a[24:27], %3 offset:832\n" \
  "v_mfma_f32_32x32x16_bf16 %1, a[44:47], a[40:43], %1\n" \
  "ds_read_b64 a[28:29], %2 offset:3392\n" \
  "ds_read_b64 a[30:31], %2 offset:3400\n" \
  "s_waitcnt lgkmcnt(6)\n" \
  "v_mfma_f32_32x32x16_bf16 %1, a[0:3], a[4:7], %1\n" \
  "ds_read_b64 a[32:33], %2 offset:1440\n" \
  "ds_read_b64 a[34:35], %2 offset:1448\n" \
  "v_mfma_f32_32x32x16_bf16 %0, a[0:3], a[8:11], %0\n" \
  "ds_read_b128 a[36:39], %3 offset:3280\n" \
  "ds_read_b128 a[40:43], %3 offset:720\n" \
  "v_mfma_f32_32x32x16_bf16 %1, a[12:15], a[8:11], %1\n" \
  "ds_read_b64 a[44:45], %2 offset:3488\n" \
  "ds_read_b64 a[46:47], %2 offset:3496\n" \
  "s_waitcnt lgkmcnt(6)\n" \
  "v_mfma_f32_32x32x16_bf16 %1, a[16:19], a[20:23], %1\n" \
  "ds_read_b64 a[0:1], %2 offset:1408\n" \
  "ds_read_b64 a[2:3], %2 offset:1416\n" \
  "v_mfma_f32_32x32x16_bf16 %0, a[16:19], a[24:27], %0\n" \
  "ds_read_b128 a[4:7], %3 offset:3312\n" \
  "ds_read_b128 a[8:11], %3 offset:752\n" \
  "v_mfma_f32_32x32x16_bf16 %1, a[28:31], a[24:27], %1\n" \
  "ds_read_b64 a[12:13], %2 offset:3456\n" \
  "ds_read_b64 a[14:15], %2 offset:3464\n" \
  "s_waitcnt lgkmcnt(6)\n" \
  "v_mfma_f32_32x32x16_bf16 %1, a[32:35], a[36:39], %1\n" \
  "ds_read_b64 a[16:17], %2 offset:1504\n" \
  "ds_read_b64 a[18:19], %2 offset:1512\n" \
  "v_mfma_f32_32x32x16_bf16 %0, a[32:35], a[40:43], %0\n" \
  "ds_read_b128 a[20:23], %3 offset:3200\n" \
  "ds_read_b128 a[24:27], %3 offset:640\n" \
  "v_mfma_f32_32x32x16_bf16 %1, a[44:47], a[40:43], %1\n" \
  "ds_read_b64 a[28:29], %2 offset:3552\n" \
  "ds_read_b64 a[30:31], %2 offset:3560\n" \
  "s_waitcnt lgkmcnt(6)\n" \
  "v_mfma_f32_32x32x16_bf16 %1, a[0:3], a[4:7], %1\n" \
  "ds_read_b64 a[32:33], %2 offset:1472\n" \
  "ds_read_b64 a[34:35], %2 offset:1480\n" \
  "v_mfma_f32_32x32x16_bf16 %0, a[0:3], a[8:11], %0\n" \
  "ds_read_b128 a[36:39], %3 offset:3232\n" \
  "ds_read_b128 a[40:43], %3 offset:672\n" \
  "v_mfma_f32_32x32x16_bf16 %1, a[12:15], a[8:11], %1\n" \
  "ds_read_b64 a[44:45], %2 offset:3520\n" \
  "ds_read_b64 a[46:47], %2 offset:3528\n" \
  "s_waitcnt lgkmcnt(6)\n" \
  "v_mfma_f32_32x32x16_bf16 %1, a[16:19], a[20:23], %1\n" \
  "ds_read_b64 a[0:1], %2 offset:1568\n" \
  "ds_read_b64 a[2:3], %2 offset:1576\n" \
  "v_mfma_f32_32x32x16_bf16 %0, a[16:19], a[24:27], %0\n" \
  "ds_read_b128 a[4:7], %3 offset:3120\n" \
  "ds_read_b128 a[8:11], %3 offset:560\n" \
  "v_mfma_f32_32x32x16_bf16 %1, a[28:31], a[24:27], %1\n" \
  "ds_read_b64 a[12:13], %2 offset:3616\n" \
  "ds_read_b64 a[14:15], %2 offset:3624\n" \
  "s_waitcnt lgkmcnt(6)\n" \
  "v_mfma_f32_32x32x16_bf16 %1, a[32:35], a[36:39], %1\n" \
  "ds_read_b64 a[16:17], %2 offset:1536\n" \
  "ds_read_b64 a[18:19], %2 offset:1544\n" \
  "v_mfma_f32_32x32x16_bf16 %0, a[32:35], a[40:43], %0\n" \
  "ds_read_b128 a[20:23], %3 offset:3152\n" \
  "ds_read_b128 a[24:27], %3 offset:592\n" \
  "v_mfma_f32_32x32x16_bf16 %1, a[44:47], a[40:43], %1\n" \
  "ds_read_b64 a[28:29], %2 offset:3584\n" \
  "ds_read_b64 a[30:31], %2 offset:3592\n" \
  "s_waitcnt lgkmcnt(6)\n" \
  "v_mfma_f32_32x32x16_bf16 %1, a[0:3], a[4:7], %1\n" \
  "ds_read_b64 a[32:33], %2 offset:1632\n" \
  "ds_read_b64 a[34:35], %2 offset:1640\n" \
  "v_mfma_f32_32x32x16_bf16 %0, a[0:3], a[8:11], %0\n" \
  "ds_read_b128 a[36:39], %3 offset:3040\n" \
  "ds_read_b128 a[40:43], %3 offset:480\n" \
  "v_mfma_f32_32x32x16_bf16 %1, a[12:15], a[8:11], %1\n" \
  "ds_read_b64 a[44:45], %2 offset:3680\n" \
  "ds_read_b64 a[46:47], %2 offset:3688\n" \
  "s_waitcnt lgkmcnt(6)\n" \
  "v_mfma_f32_32x32x16_bf16 %1, a[16:19], a[20:23], %1\n" \
  "ds_read_b64 a[0:1], %2 offset:1600\n" \
  "ds_read_b64 a[2:3], %2 offset:1608\n" \
  "v_mfma_f32_32x32x16_bf16 %0, a[16:19], a[24:27], %0\n" \
  "ds_read_b128 a[4:7], %3 offset:3072\n" \
  "ds_read_b128 a[8:11], %3 offset:512\n" \
  "v_mfma_f32_32x32x16_bf16 %1, a[28:31], a[24:27], %1\n" \
  "ds_read_b64 a[12:13], %2 offset:3648\n" \
  "ds_read_b64 a[14:15], %2 offset:3656\n" \
  "s_waitcnt lgkmcnt(6)\n" \
  "v_mfma_f32_32x32x16_bf16 %1, a[32:35], a[36:39], %1\n" \
  "ds_read_b64 a[16:17], %2 offset:1696\n" \
  "ds_read_b64 a[18:19], %2 offset:1704\n" \
  "v_mfma_f32_32x32x16_bf16 %0, a[32:35], a[40:43], %0\n" \
  "ds_read_b128 a[20:23], %3 offset:2960\n" \
  "ds_read_b128 a[24:27], %3 offset:400\n" \
  "v_mfma_f32_32x32x16_bf16 %1, a[44:47], a[40:43], %1\n" \
  "ds_read_b64 a[28:29], %2 offset:3744\n" \
  "ds_read_b64 a[30:31], %2 offset:3752\n" \
  "s_waitcnt lgkmcnt(6)\n" \
  "v_mfma_f32_32x32x16_bf16 %1, a[0:3], a[4:7], %1\n" \
  "ds_read_b64 a[32:33], %2 offset:1664\n" \
  "ds_read_b64 a[34:35], %2 offset:1672\n" \
  "v_mfma_f32_32x32x16_bf16 %0, a[0:3], a[8:11], %0\n" \
  "ds_read_b128 a[36:39], %3 offset:2992\n" \
  "ds_read_b128 a[40:43], %3 offset:432\n" \
  "v_mfma_f32_32x32x16_bf16 %1, a[12:15], a[8:11], %1\n" \
  "ds_read_b64 a[44:45], %2 offset:3712\n" \
  "ds_read_b64 a[46:47], %2 offset:3720\n" \
  "s_waitcnt lgkmcnt(6)\n" \
  "v_mfma_f32_32x32x16_bf16 %1, a[16:19], a[20:23], %1\n" \
  "ds_read_b64 a[0:1], %2 offset:1760\n" \
  "ds_read_b64 a[2:3], %2 offset:1768\n" \
  "v_mfma_f32_32x32x16_bf16 %0, a[16:19], a[24:27], %0\n" \
  "ds_read_b128 a[4:7], %3 offset:2880\n" \
  "ds_read_b128 a[8:11], %3 offset:320\n" \
  "v_mfma_f32_32x32x16_bf16 %1, a[28:31], a[24:27], %1\n" \
  "ds_read_b64 a[12:13], %2 offset:3808\n" \
  "ds_read_b64 a[14:15], %2 offset:3816\n" \
  "s_waitcnt lgkmcnt(6)\n" \
  "v_mfma_f32_32x32x16_bf16 %1, a[32:35], a[36:39], %1\n" \
  "ds_read_b64 a[16:17], %2 offset:1728\n" \
  "ds_read_b64 a[18:19], %2 offset:1736\n" \
  "v_mfma_f32_32x32x16_bf16 %0, a[32:35], a[40:43], %0\n" \
  "ds_read_b128 a[20:23], %3 offset:2912\n" \
  "ds_read_b128 a[24:27], %3 offset:352\n" \
  "v_mfma_f32_32x32x16_bf16 %1, a[44:47], a[40:43], %1\n" \
  "ds_read_b64 a[28:29], %2 offset:3776\n" \
  "ds_read_b64 a[30:31], %2 offset:3784\n" \
  "s_waitcnt lgkmcnt(6)\n" \
  "v_mfma_f32_32x32x16_bf16 %1, a[0:3], a[4:7], %1\n" \
  "ds_read_b64 a[32:33], %2 offset:1824\n" \
  "ds_read_b64 a[34:35], %2 offset:1832\n" \
  "v_mfma_f32_32x32x16_bf16 %0, a[0:3], a[8:11], %0\n" \
  "ds_read_b128 a[36:39], %3 offset:2800\n" \
  "ds_read_b128 a[40:43], %3 offset:240\n" \
  "v_mfma_f32_32x32x16_bf16 %1, a[12:15], a[8:11], %1\n" \
  "ds_read_b64 a[44:45], %2 offset:3872\n" \
  "ds_read_b64 a[46:47], %2 offset:3880\n" \
  "s_waitcnt lgkmcnt(6)\n" \
  "v_mfma_f32_32x32x16_bf16 %1, a[16:19], a[20:23], %1\n" \
  "ds_read_b64 a[0:1], %2 offset:1792\n" \
  "ds_read_b64 a[2:3], %2 offset:1800\n" \
  "v_mfma_f32_32x32x16_bf16 %0, a[16:19], a[24:27], %0\n" \
  "ds_read_b128 a[4:7], %3 offset:2832\n" \
  "ds_read_b128 a[8:11], %3 offset:272\n" \
  "v_mfma_f32_32x32x16_bf16 %1, a[28:31], a[24:27], %1\n" \
  "ds_read_b64 a[12:13], %2 offset:3840\n" \
  "ds_read_b64 a[14:15], %2 offset:3848\n" \
  "s_waitcnt lgkmcnt(6)\n" \
  "v_mfma_f32_32x32x16_bf16 %1, a[32:35], a[36:39], %1\n" \
  "ds_read_b64 a[16:17], %2 offset:1888\n" \
  "ds_read_b64 a[18:19], %2 offset:1896\n" \
  "v_mfma_f32_32x32x16_bf16 %0, a[32:35], a[40:43], %0\n" \
  "ds_read_b128 a[20:23], %3 offset:2720\n" \
  "ds_read_b128 a[24:27], %3 offset:160\n" \
  "v_mfma_f32_32x32x16_bf16 %1, a[44:47], a[40:43], %1\n" \
  "ds_read_b64 a[28:29], %2 offset:3936\n" \
  "ds_read_b64 a[30:31], %2 offset:3944\n" \
  "s_waitcnt lgkmcnt(6)\n" \
  "v_mfma_f32_32x32x16_bf16 %1, a[0:3], a[4:7], %1\n" \
  "ds_read_b64 a[32:33], %2 offset:1856\n" \
  "ds_read_b64 a[34:35], %2 offset:1864\n" \
  "v_mfma_f32_32x32x16_bf16 %0, a[0:3], a[8:11], %0\n" \
  "ds_read_b128 a[36:39], %3 offset:2752\n" \
  "ds_read_b128 a[40:43], %3 offset:192\n" \
  "v_mfma_f32_32x32x16_bf16 %1, a[12:15], a[8:11], %1\n" \
  "ds_read_b64 a[44:45], %2 offset:3904\n" \
  "ds_read_b64 a[46:47], %2 offset:3912\n" \
  "s_waitcnt lgkmcnt(6)\n" \
  "v_mfma_f32_32x32x16_bf16 %1, a[16:19], a[20:23], %1\n" \
  "ds_read_b64 a[0:1], %2 offset:1952\n" \
  "ds_read_b64 a[2:3], %2 offset:1960\n" \
  "v_mfma_f32_32x32x16_bf16 %0, a[16:19], a[24:27], %0\n" \
  "ds_read_b128 a[4:7], %3 offset:2640\n" \
  "ds_read_b128 a[8:11], %3 offset:80\n" \
  "v_mfma_f32_32x32x16_bf16 %1, a[28:31], a[24:27], %1\n" \
  "ds_read_b64 a[12:13], %2 offset:4000\n" \
  "ds_read_b64 a[14:15], %2 offset:4008\n" \
  "s_waitcnt lgkmcnt(6)\n" \
  "v_mfma_f32_32x32x16_bf16 %1, a[32:35], a[36:39], %1\n" \
  "ds_read_b64 a[16:17], %2 offset:1920\n" \
  "ds_read_b64 a[18:19], %2 offset:1928\n" \
  "v_mfma_f32_32x32x16_bf16 %0, a[32:35], a[40:43], %0\n" \
  "ds_read_b128 a[20:23], %3 offset:2672\n" \
  "ds_read_b128 a[24:27], %3 offset:112\n" \
  "v_mfma_f32_32x32x16_bf16 %1, a[44:47], a[40:43], %1\n" \
  "ds_read_b64 a[28:29], %2 offset:3968\n" \
  "ds_read_b64 a[30:31], %2 offset:3976\n" \
  "s_waitcnt lgkmcnt(6)\n" \
  "v_mfma_f32_32x32x16_bf16 %1, a[0:3], a[4:7], %1\n" \
  "ds_read_b64 a[32:33], %2 offset:2016\n" \
  "ds_read_b64 a[34:35], %2 offset:2024\n" \
  "v_mfma_f32_32x32x16_bf16 %0, a[0:3], a[8:11], %0\n" \
  "ds_read_b128 a[36:39], %3 offset:2560\n" \
  "ds_read_b128 a[40:43], %3 offset:0\n" \
  "v_mfma_f32_32x32x16_bf16 %1, a[12:15], a[8:11], %1\n" \
  "ds_read_b64 a[44:45], %2 offset:4064\n" \
  "ds_read_b64 a[46:47], %2 offset:4072\n" \
  "s_waitcnt lgkmcnt(6)\n" \
  "v_mfma_f32_32x32x16_bf16 %1, a[16:19], a[20:23], %1\n" \
  "ds_read_b64 a[0:1], %2 offset:1984\n" \
  "ds_read_b64 a[2:3], %2 offset:1992\n" \
  "v_mfma_f32_32x32x16_bf16 %0, a[16:19], a[24:27], %0\n" \
  "ds_read_b128 a[4:7], %3 offset:2592\n" \
  "ds_read_b128 a[8:11], %3 offset:32\n" \
  "v_mfma_f32_32x32x16_bf16 %1, a[28:31], a[24:27], %1\n" \
  "ds_read_b64 a[12:13], %2 offset:4032\n" \
  "ds_read_b64 a[14:15], %2 offset:4040\n" \
  "s_waitcnt lgkmcnt(6)\n" \
  "v_mfma_f32_32x32x16_bf16 %1, a[32:35], a[36:39], %1\n" \
  "v_mfma_f32_32x32x16_bf16 %0, a[32:35], a[40:43], %0\n" \
  "v_mfma_f32_32x32x16_bf16 %1, a[44:47], a[40:43], %1\n" \
  "s_waitcnt lgkmcnt(0)\n" \
  "v_mfma_f32_32x32x16_bf16 %1, a[0:3], a[4:7], %1\n" \
  "v_mfma_f32_32x32x16_bf16 %0, a[0:3], a[8:11], %0\n" \
  "v_mfma_f32_32x32x16_bf16 %1, a[12:15], a[8:11], %1\n" \
  "s_nop 15\n" \
  ""

#define CONV_CLOBBERS "a0", "a1", "a2", "a3", "a4", "a5", "a6", "a7", "a8", "a9", "a10", "a11", "a12", "a13", "a14", "a15", "a16", "a17", "a18", "a19", "a20", "a21", "a22", "a23", "a24", "a25", "a26", "a27", "a28", "a29", "a30", "a31", "a32", "a33", "a34", "a35", "a36", "a37", "a38", "a39", "a40", "a41", "a42", "a43", "a44", "a45", "a46", "a47", "memory"
#define REC_CLOBBERS \
  "v0", "v1", "v2", "v3", "v4", "v5", "v6", "v7", "v8", "v9", "v10", "v11", "v12", "v13", "v14", "v15", "v16", "v17", "v18", "v19", "v20", "v21", "v22",\
  "v23", "v25", "v26", "v27", "v28", "v29", "v30", "v31", "v32", "v33", "v34", "v35", "v36", "v37", "v38", "v39", "v40", "v41", "v42", "v43",\
  "v44", "v45", "v46", "v47", "v48", "v49", "v50", "v51", "v52", "v53", "v54", "v55", "v56", "v57", "v58", "v59", "v60", "v61", "v62", "v63", "v64",\
  "v65", "v66", "v67", "v68", "v69", "v70", "v71", "v72", "v73", "v74", "v75", "v76", "v77", "v78", "v79", "v80", "v81", "v82", "v83", "v84", "v85",\
  "v86", "v87", "v88", "v89", "v90", "v91", "v92", "v93", "v94", "v95", "v96", "v97", "v98", "v99", "v100", "v101", "v102", "v103", "v104", "v105",\
  "v106", "v107", "v108", "v109", "v110", "v111", "v112", "v113", "v114", "v115", "v116", "v117", "v118", "v119", "v120", "v121", "v122", "v123",\
  "v124", "v125", "v126", "v127", "v128", "v129", "v130", "v131", "v132", "v133", "v134", "v135", "v136", "v137", "v138", "v139", "v140", "v141",\
  "v142", "v143", "v144", "v145", "v146", "v147", "v148", "v149", "v150", "v151", "v152", "v153", "v154", "v155", "v156", "v157", "v158", "v159",\
  "v160", "v161", "v162", "v163", "v164", "v165", "v166", "v167", "v168", "v169", "v170", "v171", "v172", "v173", "v174", "v175", "v176", "v177",\
  "v178", "v179", "v180", "v181", "v182", "v183", "v184", "v185", "v186", "v187", "v188", "v189", "v190", "v191", "v192", "v193", "v194", "v195",\
  "v196", "v197", "v198", "v199", "v200", "v201", "v202", "v203", "v204", "v205", "v206", "v207", "v208", "v209", "v210", "v211", "v212", "v213",\
  "v214", "v215", "v216", "v217", "v218", "v219", "v220", "v221", "v222", "v223", "v224", "v225", "v226", "v227", "v228", "v229", "v230", "v231",\
  "v232", "v233", "v234", "v235", "v236", "v237", "v238", "v239", "v240", "v241", "v242", "v243", "v244", "v245", "v246", "v247", "v248", "v249",\
  "v250", "v251", "v252", "v253", "v254", "v255", "a0", "a1", "a2", "a3", "a4", "a5", "a6", "a7", "a8", "a9", "a10", "a11", "a12", "a13", "a14", "a15",\
  "a16", "a17", "a18", "a19", "a20", "a21", "a22", "a23", "a24", "a25", "a26", "a27", "a28", "a29", "a30", "a31", "a32", "a33", "a34", "a35", "a36",\
  "a37", "a38", "a39", "a40", "a41", "a42", "a43", "a44", "a45", "a46", "a47", "a48", "a49", "a50", "a51", "a52", "a53", "a54", "a55", "a56", "a57",\
  "a58", "a59", "a60", "a61", "a62", "a63", "a64", "a65", "a66", "a67", "a68", "a69", "a70", "a71", "a72", "a73", "a74", "a75", "a76", "a77", "a78",\
  "a79", "a80", "a81", "a82", "a83", "a84", "a85", "a86", "a87", "a88", "a89", "a90", "a91", "a92", "a93", "a94", "a95", "a96", "a97", "a98", "a99",\
  "a100", "a101", "a102", "a103", "a104", "a105", "a106", "a107", "a108", "a109", "a110", "a111", "a112", "a113", "a114", "a115", "a116", "a117",\
  "a118", "a119", "a120", "a121", "a122", "a123", "a124", "a125", "a126", "a127", "a128", "a129", "a130", "a131", "a132", "a133", "a134", "a135",\
  "a136", "a137", "a138", "a139", "a140", "a141", "a142", "a143", "a144", "a145", "a146", "a147", "a148", "a149", "a150", "a151", "a152", "a153",\
  "a154", "a155", "a156", "a157", "a158", "a159", "a160", "a161", "a162", "a163", "a164", "a165", "a166", "a167", "a168", "a169", "a170", "a171",\
  "a172", "a173", "a174", "a175", "a176", "a177", "a178", "a179", "a180", "a181", "a182", "a183", "a184", "a185", "a186", "a187", "a188", "a189",\
  "a190", "a191", "a192", "a193", "a194", "a195", "a196", "a197", "a198", "a199", "a200", "a201", "a202", "a203", "a204", "a205", "a206", "a207",\
  "a208", "a209", "a210", "a211", "a212", "a213", "a214", "a215", "a216", "a217", "a218", "a219", "a220", "a221", "a222", "a223", "a224", "a225",\
  "a226", "a227", "a228", "a229", "a230", "a231", "a232", "a233", "a234", "a235", "a236", "a237", "a238", "a239", "a240", "a241", "a242", "a243",\
  "a244", "a245", "a246", "a247", "a248", "a249", "a250", "a251", "a252", "a253", "a254", "a255", "s30", "s31", "s34", "s35", "s36", "s37", "s38", "s39", "s40", "s41", "s42",\
  "s43", "s44", "s45", "s46", "s47", "s48", "s49", "s50", "s51", "s52", "s53", "s54", "s55", "s56", "s57", "s58", "s59", "s60", "s61", "s62", "s63",\
  "s64", "s65", "s66", "s67", "s68", "s69", "s70", "s71", "s72", "s73", "s74", "s75", "s76", "s77", "s78", "s79", "s80", "s81", "s82", "s83", "s84",\
  "s85", "s86", "s87", "s88", "s89", "s90", "s91", "s92", "s93", "s94", "s95", "s96", "s97", "s98", "s99", "vcc", "scc", "memory"
__device__ __forceinline__ void role_rec(const Args& a, char LAS* smem, const int rk, const u32 pol, const u32 code) {
  const int cu = rk;
  const int tid = threadIdx.x, w = __builtin_amdgcn_readfirstlane(tid >> 6);
  const int head = cu >> 2, j = cu & 3;
  const u16* pq = a.Wq + head * DH + 64 * (j >> 1);
  const u16* pk = a.Wk + head * DH + 64 * (j >> 1);
  const u16* pv = a.Wv + head * DH + 64 * ((j >> 1) ^ 1);
  const u16* po = a.Wo + (size_t)head * DH * DM + 256 * j;
  asm volatile(REC_ASM
               :: "s"(pq), "s"(pk), "s"(pv), "s"(po), "s"(a.ex), "s"(a.ctrl), "s"(cu), "s"(w), "s"(pol), "s"((u32)(uintptr_t)smem), "s"(rk), "s"(a.mem0 + (size_t)(4 * (cu & 1)) * DM + 16 * (cu >> 1)), "{v24}"(code)
               : REC_CLOBBERS);
  __builtin_unreachable();
}
#define S_ROW 4160
#define S_WSZ (4 * S_ROW + 96 * 80 + 2048 + 64)
#define T_LD 132
__device__ __forceinline__ u32 offT(u32 pol, u32 ch) { return (ch >> 1) * 16384 + ((~(pol >> (ch >> 8)) & 1u) << 13) + (ch & 1) * 4096; }
__device__ __forceinline__ void wait_all(u32* ctrl, int word, int n, int slp) {
  const int lane = threadIdx.x & 63;
  for (int spins = 0;; ++spins) {
    asm volatile("" ::: "memory");
    const u32 v = __hip_atomic_load(ctrl + word + 32 * min(lane, n - 1), RLX_AGENT);
    if (__builtin_amdgcn_ballot_w64(v == 0) == 0) break;
    if (spins > SPIN_CAP) { set_dead(ctrl); break; }
    if ((spins & 63) == 63 && __builtin_amdgcn_readfirstlane((int)is_dead(ctrl))) break;
    sleepn(slp);
  }
  asm volatile("" ::: "memory");
}
__device__ __forceinline__ void ssm_in(const Args& a, char LAS* smem, const int rb, const u32 pol) {
  const int tid = threadIdx.x;
  const __amdgpu_buffer_rsrc_t rt = __builtin_amdgcn_make_buffer_rsrc(a.ex + WS_UT_OFF, 0, 8 << 20, 0x00020000);
#pragma unroll
  for (int q = 0; q < 4; ++q) {
    u32x4 x[8];
#pragma unroll
    for (int i = 0; i < 4; ++i) {
      const u16* p = a.u + (size_t)(64 * rb + 2 * (8 * q + 2 * i + (tid >> 7))) * SSM_D + (tid & 127) * 8;
      x[2 * i] = *(const u32x4*)p; x[2 * i + 1] = *(const u32x4*)(p + SSM_D);
    }
#pragma unroll
    for (int i = 0; i < 4; ++i)
#pragma unroll
      for (int e = 0; e < 4; ++e) {
        char LAS* o = smem + ((tid & 127) + 256 * e) * T_LD + (8 * q + 2 * i + (tid >> 7)) * 4;
        *(LAS u32*)o = __builtin_amdgcn_perm(x[2 * i + 1][e], x[2 * i][e], 0x05040100u);
        *(LAS u32*)(o + 128 * T_LD) = __builtin_amdgcn_perm(x[2 * i + 1][e], x[2 * i][e], 0x07060302u);
      }
  }
  __syncthreads();
#pragma unroll 4
  for (int i = 0; i < 32; ++i) {
    const int sl = 32 * i + (tid >> 3), ch = (sl & 127) * 8 + (sl >> 7);
    const LAS u32* p = (const LAS u32*)(smem + sl * T_LD + (tid & 7) * 16);
    __builtin_amdgcn_raw_buffer_store_b128((u32x4){p[0], p[1], p[2], p[3]}, rt, offT(pol, ch) + rb * 128 + (tid & 7) * 16, 0, SC1);
  }
  WAIT_VM0();
  __syncthreads();
  if (tid == 0) __hip_atomic_store(a.ctrl + CW_UIN + rb * 32, 1u, RLX_AGENT);
}
__device__ __forceinline__ void ssm_out(const Args& a, char LAS* smem, const int rb, const int hb, const u32 pol) {
  const int tid = threadIdx.x;
  const __amdgpu_buffer_rsrc_t rt = __builtin_amdgcn_make_buffer_rsrc(a.ex + WS_UT_OFF, 0, 8 << 20, 0x00020000);
  wait_all(a.ctrl, CW_YOUT + hb * 48 * 32, 48, 8);
  u32x4 x[16];
#pragma unroll
  for (int i = 0; i < 16; ++i) {
    const int sl = 32 * i + (tid >> 3), ch = (sl & 63) * 8 + (sl >> 6);
    x[i] = __builtin_amdgcn_raw_buffer_load_b128(rt, offT(pol, 512 * hb + ch) + rb * 128 + (tid & 7) * 16, 0, SC1);
  }
#pragma unroll
  for (int i = 0; i < 16; ++i) {
    LAS u32* p = (LAS u32*)(smem + (32 * i + (tid >> 3)) * T_LD + (tid & 7) * 16);
#pragma unroll
    for (int e = 0; e < 4; ++e) p[e] = x[i][e];
  }
  __syncthreads();
#pragma unroll 2
  for (int i = 0; i < 8; ++i) {
    const int rp = 4 * i + (tid >> 6);
    u32x4 v0, v1;
#pragma unroll
    for (int e = 0; e < 4; ++e) {
      const char LAS* o = smem + ((tid & 63) + 128 * e) * T_LD + rp * 4;
      const u32 x0 = *(const LAS u32*)o, x1 = *(const LAS u32*)(o + 64 * T_LD);
      v0[e] = __builtin_amdgcn_perm(x1, x0, 0x05040100u); v1[e] = __builtin_amdgcn_perm(x1, x0, 0x07060302u);
    }
    u16* p = a.out + (size_t)(64 * rb + 2 * rp) * OUTLD + DM + 512 * hb + (tid & 63) * 8;
    *(u32x4*)p = v0; *(u32x4*)(p + OUTLD) = v1;
  }
}
__device__ __forceinline__ void split2(float a, float b, u32& hi, u32& lo) {
  hi = pk2(a, b);
  lo = pk2(a - __uint_as_float(hi << 16), b - __uint_as_float(hi & 0xffff0000u));
}
__device__ __forceinline__ void cexp1(float ar, float ai, float l, float& xr, float& xi) {
  const float e = __builtin_amdgcn_exp2f(ar * l);
  const float rv = __builtin_amdgcn_fractf(ai * l);
  xr = e * __builtin_amdgcn_cosf(rv); xi = e * __builtin_amdgcn_sinf(rv);
}
__device__ __forceinline__ u32 lo_hi(u32 lo, u32 hi) { return __builtin_amdgcn_perm(hi, lo, 0x07060100u); }
__device__ __forceinline__ void role_ssm(const Args& a, char LAS* smem, const int cx, const int rank, const u32 pol) {
  const int tid = threadIdx.x, lane = tid & 63, w = __builtin_amdgcn_readfirstlane(tid >> 6);
  const int c = lane & 15, g = lane >> 4, b = lane & 31, h = lane >> 5;
  const __amdgpu_buffer_rsrc_t rt = __builtin_amdgcn_make_buffer_rsrc(a.ex + WS_UT_OFF, 0, 8 << 20, 0x00020000);
  char LAS* lu = smem + w * S_WSZ;
  char LAS* lw = lu + S_ROW;
  char LAS* lk = lw + 3 * S_ROW + 31 * 80;
  char LAS* lp = lk + 65 * 80;
  if (lane < 4) *(LAS u32x4*)(lu + lane * 16) = (u32x4){0, 0, 0, 0};
  for (int i = lane; i < 160; i += 64) *(LAS u32x4*)(lw + 3 * S_ROW + i * 16) = (u32x4){0, 0, 0, 0};
  float la[3];
  la[0] = (float)c;
  la[1] = c < 8 ? (float)(256 + 2 * c) : (float)(512 + 256 * ((c - 8) >> 2) + 4 * ((c - 8) & 3));
  la[2] = c < 8 ? (float)(1024 + 256 * (c >> 1) + 8 * (c & 1)) : (c == 8 ? 2048.f : 0.f);
  const float ly = (float)(16 * c);
  u32x4 un[4];
  auto fetch_u = [&](int d) {
#pragma unroll
    for (int i = 0; i < 4; ++i) un[i] = __builtin_amdgcn_raw_buffer_load_b128(rt, lane * 16 + i * 1024, offT(pol, d), SC1);
  };
  u32 pr[6]; u16 pdt, pds;
  auto fetch_p = [&](int d) {
    const size_t o = (size_t)d * SSM_N + 2 * lane;
    pr[0] = *(const u32*)(a.A_re + o); pr[1] = *(const u32*)(a.A_im + o); pr[2] = *(const u32*)(a.B_re + o);
    pr[3] = *(const u32*)(a.B_im + o); pr[4] = *(const u32*)(a.C_re + o); pr[5] = *(const u32*)(a.C_im + o);
    pdt = a.log_dt[d]; pds = a.Dskip[d];
  };
#define SST(i)
  const int first = cx * 256 + rank * 3 + w, last = cx * 256 + 256;
  fetch_p(first);
  {
    for (int d = first; d < last; d += 24) {
      const int nxt = d + 24 < last ? d + 24 : d;
      SST(0);
      const float dt = rbf(expf(bf2f(pdt))), ds = bf2f(pds);
#pragma unroll
      for (int i = 0; i < 2; ++i) {
        auto el = [&](int k) { return __uint_as_float(i ? (pr[k] & 0xffff0000u) : (pr[k] << 16)); };
        const float br = dt * el(2), bi = dt * el(3), cr = el(4), ci = el(5);
        *(LAS f32x4*)(lp + (2 * lane + i) * 16) = (f32x4){dt * el(0) * 1.4426950408889634f, dt * el(1) * 0.15915494309189535f, cr * br - ci * bi, cr * bi + ci * br};
      }
      fetch_p(nxt);
      SST(1);
      f32x4 ka[3];
#pragma unroll
      for (int t = 0; t < 3; ++t) ka[t] = (f32x4){0, 0, 0, 0};
      f32x4 pn[4];
#pragma unroll
      for (int i = 0; i < 4; ++i) pn[i] = *(const LAS f32x4*)(lp + (4 * g + i) * 16);
      for (int k = 0; k < 8; ++k) {
        f32x4 p[4];
#pragma unroll
        for (int i = 0; i < 4; ++i) { p[i] = pn[i]; pn[i] = *(const LAS f32x4*)(lp + (16 * ((k + 1) & 7) + 4 * g + i) * 16); }
        u32x4 yh, yl, xh[3], xl[3];
#pragma unroll
        for (int i = 0; i < 4; ++i) {
          float er, ei; u32 hi, lo;
          cexp1(p[i][0], p[i][1], ly, er, ei);
          split2(p[i][2] * er - p[i][3] * ei, -(p[i][2] * ei + p[i][3] * er), hi, lo); yh[i] = hi; yl[i] = lo;
#pragma unroll
          for (int t = 0; t < 3; ++t) { cexp1(p[i][0], p[i][1], la[t], er, ei); split2(er, ei, hi, lo); xh[t][i] = hi; xl[t][i] = lo; }
        }
        __builtin_amdgcn_sched_barrier(0);
#pragma unroll
        for (int t = 0; t < 3; ++t) ka[t] = MFMA32(asb(xh[t]), asb(yh), ka[t]);
#pragma unroll
        for (int t = 0; t < 3; ++t) ka[t] = MFMA32(asb(xh[t]), asb(yl), ka[t]);
#pragma unroll
        for (int t = 0; t < 3; ++t) ka[t] = MFMA32(asb(xl[t]), asb(yh), ka[t]);
        __builtin_amdgcn_sched_barrier(0);
      }
      SST(2);
      {
        const int l = 16 * c + 4 * g;
        *(LAS u32x2*)(lk + (1 + (l >> 5)) * 80 + ((l & 31) ^ 4) * 2) = (u32x2){pk2(ka[0][3], ka[0][2]), pk2(ka[0][1], ka[0][0])};
        const int j1 = g < 2 ? 8 * c + 4 * g : 128 + 64 * (g - 2) + 4 * c;
        *(LAS u32x4*)(lp + j1 * 4) = (u32x4){pk2(ka[1][0], ka[1][0]), pk2(ka[1][1], ka[1][1]), pk2(ka[1][2], ka[1][2]), pk2(ka[1][3], ka[1][3])};
        if (g < 2) {
          *(LAS u32x2*)(lp + (256 + 64 * g + 2 * c) * 4) = (u32x2){pk2(ka[2][0], ka[2][0]), pk2(ka[2][1], ka[2][1])};
          *(LAS u32x2*)(lp + (256 + 64 * g + 32 + 2 * c) * 4) = (u32x2){pk2(ka[2][2], ka[2][2]), pk2(ka[2][3], ka[2][3])};
        } else if (lane == 32) *(LAS u32*)(lp + 384 * 4) = pk2(ka[2][0], ka[2][0]);
        if (lane < 32) {
          const u32x4 v = *(const LAS u32x4*)(lp + lane * 16);
          const u32 v4 = *(const LAS u32*)(lp + lane * 16 + 16);
          *(LAS u32x4*)(lk + (9 + (lane >> 2)) * 80 + (lane & 3) * 16) = (u32x4){lo_hi(v4, v[3]), v[2], lo_hi(v[2], v[1]), v[0]};
        }
        {
          const u32x2 v = *(const LAS u32x2*)(lp + 512 + lane * 8);
          const u32 v2 = *(const LAS u32*)(lp + 512 + lane * 8 + 8);
          *(LAS u32x4*)(lk + (17 + (lane >> 2)) * 80 + (lane & 3) * 16) = (u32x4){v2, v[1], lo_hi(v[1], v[0]), v[0]};
        }
        {
          const u32x2 v = *(const LAS u32x2*)(lp + 1024 + lane * 8);
          const u32 v2 = *(const LAS u32*)(lp + 1024 + lane * 8 + 8);
          char LAS* o = lk + (33 + (lane >> 1)) * 80 + (lane & 1) * 32;
          *(LAS u32x4*)o = (u32x4){v[1], lo_hi(v[1], v[0]), v[0], v[0]};
          *(LAS u32x4*)(o + 16) = (u32x4){v2, v2, v[1], v[1]};
        }
      }
      if (d == first) { wait_all(a.ctrl, CW_UIN, 32, 8); fetch_u(d); }
#pragma unroll
      for (int i = 0; i < 4; ++i) *(LAS u32x4*)(lu + 64 + lane * 16 + i * 1024) = un[i];
      if (nxt != d) fetch_u(nxt);
#pragma unroll
      for (int i = 0; i < 5; ++i) {
        const int q = lane + 64 * i;
        if (i < 4 || lane < 4) {
          const u32x4 x = *(const LAS u32x4*)(lu + q * 16);
          u32x2 y = *(const LAS u32x2*)(lu + q * 16 + 16);
          *(LAS u32x4*)(lw + q * 16) = (u32x4){__builtin_amdgcn_alignbit(x[1], x[0], 16), __builtin_amdgcn_alignbit(x[2], x[1], 16), __builtin_amdgcn_alignbit(x[3], x[2], 16), __builtin_amdgcn_alignbit(y[0], x[3], 16)};
          *(LAS u32x4*)(lw + S_ROW + q * 16) = (u32x4){x[1], x[2], x[3], y[0]};
          *(LAS u32x4*)(lw + 2 * S_ROW + q * 16) = (u32x4){__builtin_amdgcn_alignbit(x[2], x[1], 16), __builtin_amdgcn_alignbit(x[3], x[2], 16), __builtin_amdgcn_alignbit(y[0], x[3], 16), __builtin_amdgcn_alignbit(y[1], y[0], 16)};
        }
      }
      SST(3);
      const int p0 = 32 + b - 8 * h - 7, cp = p0 & 3;
      const char LAS* ra = lu + cp * S_ROW + (p0 - cp) * 2 - 32;
      const char LAS* rb = lk + 16 * h + 80 * (b - 30);
      f32x16 y0, y1;
      asm volatile(CONV_ASM : "=&v"(y0), "=&v"(y1) : "v"((u32)(uintptr_t)ra), "v"((u32)(uintptr_t)rb) : CONV_CLOBBERS);
      SST(4);
#pragma unroll
      for (int cg = 0; cg < 2; ++cg)
#pragma unroll
        for (int i4 = 0; i4 < 4; ++i4) {
          LAS u32x2* up = (LAS u32x2*)(lu + 64 + (32 * (32 * cg + b) + 8 * i4 + 4 * h) * 2);
          const u32x2 uv = *up;
          float v[4];
#pragma unroll
          for (int e = 0; e < 4; ++e) v[e] = cg ? y1[4 * i4 + e] : y0[4 * i4 + e];
          const u32 ya = pk2(v[0], v[1]), yb = pk2(v[2], v[3]);
          const u32 sa = pk2(__uint_as_float(uv[0] << 16) * ds, __uint_as_float(uv[0] & 0xffff0000u) * ds);
          const u32 sb = pk2(__uint_as_float(uv[1] << 16) * ds, __uint_as_float(uv[1] & 0xffff0000u) * ds);
          *up = (u32x2){pk2(__uint_as_float(ya << 16) + __uint_as_float(sa << 16), __uint_as_float(ya & 0xffff0000u) + __uint_as_float(sa & 0xffff0000u)),
                        pk2(__uint_as_float(yb << 16) + __uint_as_float(sb << 16), __uint_as_float(yb & 0xffff0000u) + __uint_as_float(sb & 0xffff0000u))};
        }
#pragma unroll
      for (int i = 0; i < 4; ++i) __builtin_amdgcn_raw_buffer_store_b128(*(const LAS u32x4*)(lu + 64 + lane * 16 + i * 1024), rt, lane * 16 + i * 1024, offT(pol, d), SC1);
      SST(5);
    }
    WAIT_VM0();
    if (lane == 0) __hip_atomic_store(a.ctrl + CW_YOUT + (cx * 24 + rank * 3 + w) * 32, 1u, RLX_AGENT);
  }
}
#define BULK_CLOBBERS \
  "v4", "v5", "v6", "v7", "v8", "v9", "v10", "v11", "v12", "v13", "v14", "v15", "v16", "v17", "v18", "v19", "v20", "v21", "v22", "v23", "v24", "v25",\
  "v26", "v27", "v28", "v29", "v30", "v31", "v32", "v33", "v34", "v35", "v36", "v37", "v38", "v39", "v40", "v41", "v42", "v43", "v44", "v45", "v46",\
  "v47", "v48", "v49", "v50", "v51", "v52", "v53", "v54", "v55", "v56", "v57", "v58", "v59", "v60", "v61", "v62", "v63", "v64", "v65", "v66", "v67",\
  "v68", "v69", "v70", "v71", "v72", "v73", "v74", "v75", "v76", "v77", "v78", "v79", "v80", "v81", "v82", "v83", "v84", "v85", "v86", "v87", "v88",\
  "v89", "v90", "v91", "v92", "v93", "v94", "v95", "v96", "v97", "v98", "v99", "v100", "v101", "v102", "v103", "v104", "v105", "v106", "v107", "v108",\
  "v109", "v110", "v111", "v112", "v113", "v114", "v115", "v116", "v117", "v118", "v119", "v120", "v121", "v122", "v123", "v124", "v125", "v126",\
  "v127", "v128", "v129", "v130", "v131", "v132", "v133", "v134", "v135", "v136", "v137", "v138", "v139", "v140", "v141", "v142", "v143", "v144",\
  "v145", "v146", "v147", "v148", "v149", "v150", "v151", "v152", "v153", "v154", "v155", "v156", "v157", "v158", "v159", "v160", "v161", "v162",\
  "v163", "v164", "v165", "v166", "v167", "v168", "v169", "v170", "v171", "v172", "v173", "v174", "v175", "v176", "v177", "v178", "v179", "v180",\
  "v181", "v182", "v183", "v184", "v185", "v186", "v187", "v188", "v189", "v190", "v191", "v192", "v193", "v194", "v195", "v196", "v197", "v198",\
  "v199", "v200", "v201", "v202", "v203", "v204", "v205", "v206", "v207", "v208", "v209", "v210", "v211", "v212", "v213", "v214", "v215", "v216",\
  "v217", "v218", "v219", "v220", "v221", "v222", "v223", "v224", "v225", "v226", "v227", "v228", "v229", "v230", "v231", "v232", "v233", "v234",\
  "v235", "v236", "v237", "v238", "v239", "v240", "v241", "v242", "v243", "v244", "v245", "v246", "v247", "v248", "v249", "v250", "v251", "v252",\
  "v253", "v254", "v255", "a0", "a1", "a2", "a3", "a4", "a5", "a6", "a7", "a8", "a9", "a10", "a11", "a12", "a13", "a14", "a15", "a16", "a17", "a18",\
  "a19", "a20", "a21", "a22", "a23", "a24", "a25", "a26", "a27", "a28", "a29", "a30", "a31", "a32", "a33", "a34", "a35", "a36", "a37", "a38", "a39",\
  "a40", "a41", "a42", "a43", "a44", "a45", "a46", "a47", "a48", "a49", "a50", "a51", "a52", "a53", "a54", "a55", "a56", "a57", "a58", "a59", "a60",\
  "a61", "a62", "a63", "a64", "a65", "a66", "a67", "a68", "a69", "a70", "a71", "a72", "a73", "a74", "a75", "a76", "a77", "a78", "a79", "a80", "a81",\
  "a82", "a83", "a84", "a85", "a86", "a87", "a88", "a89", "a90", "a91", "a92", "a93", "a94", "a95", "a96", "a97", "a98", "a99", "a100", "a101", "a102",\
  "a103", "a104", "a105", "a106", "a107", "a108", "a109", "a110", "a111", "a112", "a113", "a114", "a115", "a116", "a117", "a118", "a119", "a120",\
  "a121", "a122", "a123", "a124", "a125", "a126", "a127", "a128", "a129", "a130", "a131", "a132", "a133", "a134", "a135", "a136", "a137", "a138",\
  "a139", "a140", "a141", "a142", "a143", "a144", "a145", "a146", "a147", "a148", "a149", "a150", "a151", "a152", "a153", "a154", "a155", "a156",\
  "a157", "a158", "a159", "a160", "a161", "a162", "a163", "a164", "a165", "a166", "a167", "a168", "a169", "a170", "a171", "a172", "a173", "a174",\
  "a175", "a176", "a177", "a178", "a179", "a180", "a181", "a182", "a183", "a184", "a185", "a186", "a187", "a188", "a189", "a190", "a191", "a192",\
  "a193", "a194", "a195", "a196", "a197", "a198", "a199", "a200", "a201", "a202", "a203", "a204", "a205", "a206", "a207", "a208", "a209", "a210",\
  "a211", "a212", "a213", "a214", "a215", "a216", "a217", "a218", "a219", "a220", "a221", "a222", "a223", "a224", "a225", "a226", "a227", "a228",\
  "a229", "a230", "a231", "a232", "a233", "a234", "a235", "a236", "a237", "a238", "a239", "a240", "a241", "a242", "a243", "a244", "a245", "a246",\
  "a247", "a248", "a249", "a250", "a251", "a252", "a253", "a254", "a255", "s68", "s69", "s70", "s71", "s72", "s73", "s74", "s75", "s76", "s77", "s78",\
  "s79", "s80", "s81", "s82", "s83", "s84", "s85", "s86", "s87", "s88", "s89", "s90", "s91", "s92", "s93", "s94", "s95", "s96", "vcc", "scc", "memory"
#define GEMM_CLOBBERS \
  "v4", "v5", "v6", "v7", "v8", "v9", "v10", "v11", "v12", "v13", "v14", "v15", "v16", "v17", "v18", "v19", "v20", "v21", "v22", "v23", "v24", "v25",\
  "v26", "v27", "v28", "v29", "v30", "v31", "v32", "v33", "v34", "v35", "v36", "v37", "v38", "v39", "v40", "v41", "v42", "v43", "v44", "v45", "v46",\
  "v47", "v48", "v49", "v50", "v51", "v52", "v53", "v54", "v55", "v56", "v57", "v58", "v59", "v60", "v61", "v62", "v63", "v64", "v65", "v66", "v67",\
  "v68", "v69", "v70", "v71", "v72", "v73", "v74", "v75", "v76", "v77", "v78", "v79", "v80", "v81", "v82", "v83", "v84", "v85", "v86", "v87", "v88",\
  "v89", "v90", "v91", "v92", "v93", "v94", "v95", "v96", "v97", "v98", "v99", "v116", "v117", "v118", "v119", "v120", "v121", "v122", "v123", "v124",\
  "v125", "v126", "v127", "v128", "v129", "v130", "v131", "v132", "v133", "v134", "v135", "v136", "v137", "v138", "v139", "v140", "v141", "v142",\
  "v143", "v144", "v145", "v146", "v147", "v148", "v149", "v150", "v151", "v152", "v153", "v154", "v155", "v156", "v157", "v158", "v159", "v160",\
  "v161", "v162", "v163", "v164", "v165", "v166", "v167", "v168", "v169", "v170", "v171", "v172", "v173", "v174", "v175", "v176", "v177", "v178",\
  "v179", "v180", "v181", "v182", "v183", "v184", "v185", "v186", "v187", "v188", "v189", "v190", "v191", "v192", "v193", "v194", "v195", "v196",\
  "v197", "v198", "v199", "v200", "v201", "v202", "v203", "v204", "v205", "v206", "v207", "v208", "v209", "v210", "v211", "v212", "v213", "v214",\
  "v215", "v216", "v217", "v218", "v219", "v220", "v221", "v222", "v223", "v224", "v225", "v226", "v227", "v228", "v229", "v230", "v231", "v232",\
  "v233", "v234", "v235", "v236", "v237", "v238", "v239", "v240", "v241", "v242", "v243", "v244", "v245", "v246", "v247", "v248", "v249", "v250",\
  "v251", "v252", "v253", "v254", "v255", "a0", "a1", "a2", "a3", "a4", "a5", "a6", "a7", "a8", "a9", "a10", "a11", "a12", "a13", "a14", "a15", "a16",\
  "a17", "a18", "a19", "a20", "a21", "a22", "a23", "a24", "a25", "a26", "a27", "a28", "a29", "a30", "a31", "a32", "a33", "a34", "a35", "a36", "a37",\
  "a38", "a39", "a40", "a41", "a42", "a43", "a44", "a45", "a46", "a47", "a48", "a49", "a50", "a51", "a52", "a53", "a54", "a55", "a56", "a57", "a58",\
  "a59", "a60", "a61", "a62", "a63", "a64", "a65", "a66", "a67", "a68", "a69", "a70", "a71", "a72", "a73", "a74", "a75", "a76", "a77", "a78", "a79",\
  "a80", "a81", "a82", "a83", "a84", "a85", "a86", "a87", "a88", "a89", "a90", "a91", "a92", "a93", "a94", "a95", "a96", "a97", "a98", "a99", "a100",\
  "a101", "a102", "a103", "a104", "a105", "a106", "a107", "a108", "a109", "a110", "a111", "a112", "a113", "a114", "a115", "a116", "a117", "a118",\
  "a119", "a120", "a121", "a122", "a123", "a124", "a125", "a126", "a127", "a128", "a129", "a130", "a131", "a132", "a133", "a134", "a135", "a136",\
  "a137", "a138", "a139", "a140", "a141", "a142", "a143", "a144", "a145", "a146", "a147", "a148", "a149", "a150", "a151", "a152", "a153", "a154",\
  "a155", "a156", "a157", "a158", "a159", "a160", "a161", "a162", "a163", "a164", "a165", "a166", "a167", "a168", "a169", "a170", "a171", "a172",\
  "a173", "a174", "a175", "a176", "a177", "a178", "a179", "a180", "a181", "a182", "a183", "a184", "a185", "a186", "a187", "a188", "a189", "a190",\
  "a191", "a192", "a193", "a194", "a195", "a196", "a197", "a198", "a199", "a200", "a201", "a202", "a203", "a204", "a205", "a206", "a207", "a208",\
  "a209", "a210", "a211", "a212", "a213", "a214", "a215", "a216", "a217", "a218", "a219", "a220", "a221", "a222", "a223", "a224", "a225", "a226",\
  "a227", "a228", "a229", "a230", "a231", "a232", "a233", "a234", "a235", "a236", "a237", "a238", "a239", "a240", "a241", "a242", "a243", "a244",\
  "a245", "a246", "a247", "a248", "a249", "a250", "a251", "a252", "a253", "a254", "a255", "s68", "s69", "s70", "s71", "s72", "s73", "s74", "s75", "s76",\
  "s77", "s78", "s79", "s80", "s81", "s82", "s83", "s84", "s85", "s86", "s87", "s88", "s89", "s90", "s91", "s92", "s93", "s94", "s95", "s96", "vcc", "scc", "memory"
__device__ __forceinline__ void role_gemm(const Args& a, char LAS* smem, const int gi, const u32 pol) {
  const int w = __builtin_amdgcn_readfirstlane(threadIdx.x >> 6);
  asm volatile(GEMM_ASM
               :: "s"(a.Wq + 64 * gi), "s"(a.Wk + 64 * (gi ^ 1)), "s"(a.Wv + 64 * (gi ^ 2)), "s"(a.x), "s"(a.ex + WS_Q_OFF), "s"(a.ctrl), "s"(gi), "s"(w), "s"((u32)(uintptr_t)smem), "s"(pol)
               : GEMM_CLOBBERS);
  ssm_out(a, smem, gi & 31, gi >> 5, pol);
}
__device__ __forceinline__ void role_xo(const Args& a, char LAS* smem, const int xi, const u32 pol) {
  const int w = __builtin_amdgcn_readfirstlane(threadIdx.x >> 6);
  sleepn(XO_SLEEP);
  ssm_in(a, smem, xi, pol);
  asm volatile(XO_ASM
               :: "s"(a.Wo + 64 * (xi >> 1)), "s"(a.ex + WS_OX_OFF), "s"(a.out), "s"(a.ctrl), "s"(xi), "s"(w), "s"((u32)(uintptr_t)smem), "s"(pol)
               : BULK_CLOBBERS);
}
__device__ __forceinline__ void role_xa(const Args& a, char LAS* smem, const int head, const u32 pol, const u32 code) {
  const int lane = threadIdx.x & 63;
  sleepn(XPRE);
  {
    u32 acc = 0;
    for (int i = 0; i < 16; ++i) acc |= *(const u32*)((const char*)a.x + ((size_t)(i * 32 + head) * 64 + lane) * 128);
    const u16* pp[6] = {a.A_re, a.A_im, a.B_re, a.B_im, a.C_re, a.C_im};
#pragma unroll
    for (int i = 0; i < 6; ++i) acc |= *(const u32*)((const char*)pp[i] + ((size_t)head * 64 + lane) * 128);
    asm volatile("" :: "v"(acc));
  }
  asm volatile(XA_ASM
               :: "s"(a.ex), "s"(a.ex + WS_Q_OFF), "s"(a.ctrl), "s"(head), "s"(pol), "s"((u32)(uintptr_t)smem + 3 * S_WSZ), "v"(code)
               : BULK_CLOBBERS);
}
__global__ void __launch_bounds__(128) prep_kernel(u32* ctrl, char* ex) {
  __shared__ u32 fill[24576];
  const int b = blockIdx.x, tid = threadIdx.x, pg = b >> 3;
  const u32 xcc = (u32)__builtin_amdgcn_s_getreg(20 | (3 << 11)) & 7u;
  const __amdgpu_buffer_rsrc_t rs = __builtin_amdgcn_make_buffer_rsrc(ex, 0, (int)EX_BYTES, 0x00020000);
  const u32 bit = (pg & 4) ? 16384u : 4096u, flip = ((pg & 4) ? xcc : xcc >> 1) & 1u;
  const u32 o0 = ((u32)pg << 21) + (b & 7) * 32768 + ((pg >> 1) & 1) * 8192 + (pg & 15) * 256 + flip * bit + (tid < 8 ? tid * 16 : 0x80000000u), o1 = o0 ^ bit;
  u32 b0 = 0xffffu, b1 = 0xffffu;
  if (xcc < 4 && tid < 64) {
#pragma unroll 1
    for (int rep = 0; rep < NPROBE; ++rep) {
      u64 t0, t1, t2;
      asm volatile("s_memtime %0\n\tbuffer_store_dwordx4 %3, %4, %6, 0 offen sc1\n\ts_waitcnt vmcnt(0)\n\ts_memtime %1\n\tbuffer_store_dwordx4 %3, %5, %6, 0 offen sc1\n\t"
                   "s_waitcnt vmcnt(0)\n\ts_memtime %2\n\ts_waitcnt lgkmcnt(0)"
                   : "=&s"(t0), "=&s"(t1), "=&s"(t2) : "v"((u32x4){SENT, SENT, SENT, SENT}), "v"(o0), "v"(o1), "s"(rs) : "memory");
      b0 = min(b0, (u32)(t1 - t0)); b1 = min(b1, (u32)(t2 - t1));
    }
  }
  if (tid == 0) { fill[b] = xcc < 4 ? (min(b0, 4095u) | (min(b1, 4095u) << 16)) : 0u; ctrl[CW_POL + b] = *(volatile u32*)&fill[b]; }
  __syncthreads();
  const int i = b * 128 + tid;
  if (i < CTRL_BYTES / 4 && (i < CW_POL || i >= CW_POL + EX_PAGES * 8)) ctrl[i] = 0;
}

__global__ void __launch_bounds__(256, 1) mega_kernel(Args a) {
  __shared__ __attribute__((aligned(16))) char smem_raw[163840];
  char LAS* smem = (char LAS*)smem_raw;
  const int tid = threadIdx.x;
  const u32x4* pv = (const u32x4*)(a.ctrl + CW_POL + min(tid & 63, EX_PAGES - 1) * 8);
  const u32x4 pv0 = pv[0], pv1 = pv[1];
  if (tid == 0) {
    const u32 xcc = (u32)__builtin_amdgcn_s_getreg(20 | (3 << 11)) & 7u;
    int role = -1;
    for (int k = 0; k < 8 && role < 0; ++k) {
      const u32 cc = (xcc + k) & 7u;
      const u32 t = __hip_atomic_fetch_add(a.ctrl + CW_TICKET + cc * 32, 1u, RLX_AGENT);
      if (t < 32) role = (int)(cc * 32 + t);
    }
    *(LAS int*)smem = role;
  }
  __syncthreads();
  const int role = __builtin_amdgcn_readfirstlane(*(LAS int*)smem);
  __syncthreads();
  if (role < 0) return;
  u32 pol;
  const u32 code = vote_code(pv0, pv1, pol);
  if (role < 128) role_rec(a, smem, role, pol, code);
  else {
    const int cx = (role - 128) >> 5, t = role & 31;
    if (t < 8) role_xo(a, smem, ((cx >> 1) * 8 + t) * 2 + (cx & 1), pol);
    else if (t < 16) {
      if (__builtin_amdgcn_readfirstlane(tid >> 6) == 3) role_xa(a, smem, cx * 8 + t - 8, pol, code);
      else role_ssm(a, smem, cx, t - 8, pol);
    }
    else role_gemm(a, smem, (cx >> 1) * 32 + (t - 16) * 2 + (cx & 1), pol);
  }
}

extern "C" void kernel_launch(void* const* d_in, const int* in_sizes, int n_in, void* d_out, int out_size,
                              void* d_ws, size_t ws_size, hipStream_t stream) {
  Args a;
  a.x = (const u16*)d_in[0]; a.mem0 = (const u16*)d_in[1];
  a.Wq = (const u16*)d_in[2]; a.Wk = (const u16*)d_in[3]; a.Wv = (const u16*)d_in[4]; a.Wo = (const u16*)d_in[5];
  a.u = (const u16*)d_in[7]; a.A_re = (const u16*)d_in[8]; a.A_im = (const u16*)d_in[9];
  a.B_re = (const u16*)d_in[10]; a.B_im = (const u16*)d_in[11]; a.C_re = (const u16*)d_in[12]; a.C_im = (const u16*)d_in[13];
  a.log_dt = (const u16*)d_in[14]; a.Dskip = (const u16*)d_in[15];
  a.out = (u16*)d_out;
  char* ws = (char*)d_ws;
  a.ctrl = (u32*)ws;
  a.ex = (char*)(((uintptr_t)ws + CTRL_BYTES + (2u << 20) - 1) & ~(uintptr_t)((2u << 20) - 1));
  prep_kernel<<<EX_PAGES * 8, 128, 0, stream>>>(a.ctrl, a.ex);
  mega_kernel<<<256, 256, 0, stream>>>(a);
}
```

```cpp
#include <hip/hip_runtime.h>
#include <hip/hip_bf16.h>
#include <stdint.h>

#define S_LEN 2048
#define DM 1024
#define NH 32
#define DH 128
#define HD 4096
#define NSEG 64
#define SSM_L 2048
#define SSM_D 1024
#define SSM_N 128
#define OUTLD 2048

typedef unsigned short u16;
typedef unsigned int u32;
typedef unsigned long long u64;
typedef __attribute__((ext_vector_type(8))) short bf16x8;
typedef __attribute__((ext_vector_type(4))) short bf16x4;
typedef __attribute__((ext_vector_type(4))) float f32x4;
typedef __attribute__((ext_vector_type(16))) float f32x16;
typedef __attribute__((ext_vector_type(4))) unsigned int u32x4;
typedef __attribute__((ext_vector_type(2))) unsigned int u32x2;
typedef __attribute__((ext_vector_type(2))) float f32x2;
typedef __attribute__((ext_vector_type(2))) __bf16 bf2_t;
typedef short v4i16_t __attribute__((ext_vector_type(4)));
#define LAS __attribute__((address_space(3)))

#define SENT 0xFFFFFFFFu
#define RLX_AGENT __ATOMIC_RELAXED, __HIP_MEMORY_SCOPE_AGENT
#define SC1 16

__device__ __forceinline__ float bf2f(u16 v) { return __uint_as_float(((u32)v) << 16); }
__device__ __forceinline__ u32 pk2(float a, float b) {
  f32x2 f = {a, b};
  bf2_t h = __builtin_convertvector(f, bf2_t);
  return __builtin_bit_cast(u32, h);
}
__device__ __forceinline__ float rbf(float f) { return __uint_as_float(pk2(f, 0.f) << 16); }
__device__ __forceinline__ u32 nosent(u32 v) { return v < 0xFFFFFFFEu ? v : 0xFFFFFFFEu; }
__device__ __forceinline__ bool okv(u32x4 v) { return v[0] != SENT && v[1] != SENT && v[2] != SENT && v[3] != SENT; }
__device__ __forceinline__ bf16x8 asb(u32x4 v) { return __builtin_bit_cast(bf16x8, v); }
__device__ __forceinline__ void sleepn(int n) { for (int k = 0; k < n; ++k) __builtin_amdgcn_s_sleep(1); }
#define WAIT_VM0() asm volatile("s_waitcnt vmcnt(0)" ::: "memory")
#define BAR() do { asm volatile("s_waitcnt lgkmcnt(0)" ::: "memory"); __builtin_amdgcn_s_barrier(); asm volatile("" ::: "memory"); } while (0)

__device__ __forceinline__ float xmax16(float x) {
  auto r = __builtin_amdgcn_permlane16_swap(__float_as_uint(x), __float_as_uint(x), false, false);
  return fmaxf(__uint_as_float(r[0]), __uint_as_float(r[1]));
}
__device__ __forceinline__ float xmax32(float x) {
  auto r = __builtin_amdgcn_permlane32_swap(__float_as_uint(x), __float_as_uint(x), false, false);
  return fmaxf(__uint_as_float(r[0]), __uint_as_float(r[1]));
}
__device__ __forceinline__ float xadd16(float x) {
  auto r = __builtin_amdgcn_permlane16_swap(__float_as_uint(x), __float_as_uint(x), false, false);
  return __uint_as_float(r[0]) + __uint_as_float(r[1]);
}
__device__ __forceinline__ float xadd32(float x) {
  auto r = __builtin_amdgcn_permlane32_swap(__float_as_uint(x), __float_as_uint(x), false, false);
  return __uint_as_float(r[0]) + __uint_as_float(r[1]);
}
__device__ __forceinline__ u32 ror8(u32 x) { return (u32)__builtin_amdgcn_update_dpp(0, (int)x, 0x128, 0xf, 0xf, true); }
template <int N> __device__ __forceinline__ float rorf(float x) {
  return __uint_as_float((u32)__builtin_amdgcn_update_dpp(0, (int)__float_as_uint(x), 0x120 + N, 0xf, 0xf, true));
}
__device__ __forceinline__ float ror8f(float x) { return __uint_as_float(ror8(__float_as_uint(x))); }

#define MFMA32(a, b, c) __builtin_amdgcn_mfma_f32_16x16x32_bf16(a, b, c, 0, 0, 0)
#define MFMA16(a, b, c) __builtin_amdgcn_mfma_f32_16x16x16bf16_1k(a, b, c, 0, 0, 0)
#define MFMA_AV0(acc, wa, xb) asm volatile("v_mfma_f32_16x16x32_bf16 %0, %1, %2, 0" : "=&v"(acc) : "a"(wa), "v"(xb))
#define MFMA_AV(acc, wa, xb) asm volatile("v_mfma_f32_16x16x32_bf16 %0, %1, %2, %0" : "+v"(acc) : "a"(wa), "v"(xb))
#define MFMA_VA0(acc, xa, wb) asm volatile("v_mfma_f32_16x16x32_bf16 %0, %1, %2, 0" : "=&v"(acc) : "v"(xa), "a"(wb))
#define MFMA_VA(acc, xa, wb) asm volatile("v_mfma_f32_16x16x32_bf16 %0, %1, %2, %0" : "+v"(acc) : "v"(xa), "a"(wb))
#define MFMA_VV0(acc, xa, xb) asm volatile("v_mfma_f32_16x16x32_bf16 %0, %1, %2, 0" : "=&v"(acc) : "v"(xa), "v"(xb))
#define MFMA16_VV(acc, xa, xb) asm volatile("v_mfma_f32_16x16x16_bf16 %0, %1, %2, %0" : "+v"(acc) : "v"(xa), "v"(xb))
#define MFMA_ACC(acc, xa, xb) asm volatile("v_mfma_f32_16x16x32_bf16 %0, %1, %2, %0" : "+a"(acc) : "v"(xa), "v"(xb))
#define NOP_IN() asm volatile("s_nop 1" ::: "memory")
#define PIN(x) asm volatile("" : "+v"(x))
#define PINA(x) asm volatile("" : "+a"(x))

__device__ __forceinline__ bf16x4 trread(const char LAS* p) {
  return __builtin_amdgcn_ds_read_tr16_b64_v4i16((LAS v4i16_t*)p);
}
__device__ __forceinline__ bf16x8 cat8(bf16x4 a, bf16x4 b) {
  return __builtin_shufflevector(a, b, 0, 1, 2, 3, 4, 5, 6, 7);
}

#define CW_READY 0
#define CW_CLAIM 12288
#define CW_MEAS 20480
#define CW_INIT 288
#define CW_DEAD 320
#define CW_PROG 384
#define CW_XPROG 1408
#define CW_GPROG 3456
#define CW_UIN 2432
#define CW_YOUT 8192
#define CTRL_BYTES 114688
#define EX_PAGES 20
#define EX_BYTES ((u64)EX_PAGES << 21)
#define EXP_C 0
#define EXP_A 1
#define EXP_B 2
#define EXP_DBG 19
#define QKV_SZ ((u64)S_LEN * HD * 4)
#define WS_Q_OFF (EX_BYTES)
#define WS_K_OFF (WS_Q_OFF + QKV_SZ)
#define WS_VT_OFF (WS_K_OFF + QKV_SZ)
#define WS_OX_OFF (WS_VT_OFF + QKV_SZ)
#define WS_UT_OFF (WS_OX_OFF + QKV_SZ)
#define EX_BYTES32 41943040
#define QKV_SZ32 33554432
#define WS_K_OFF32 75497472
static_assert(EX_BYTES32 == EX_BYTES && QKV_SZ32 == QKV_SZ && WS_K_OFF32 == WS_K_OFF, "layout");

#define SPIN_CAP 300000
#define NPROBE 3
#define F_B 1460
#define F_C 3340
#define F_P 4220
#ifndef M_C
#define M_C 150
#endif
#ifndef M_A
#define M_A 150
#endif
#define T_Q 1300
#define T_TOL 100
#define D_K 15
#define DECAY 0.996
#define RI0 1.11
#define XPRE 300
#define O_N 0
#define T_SNAP 300
#define E_0 0
#define E_UP 64
#define E_DOWN 2
#define XO_SLEEP2 0
#define OAUX 16
#define T_LATE 0
#define O_A 110
#define O_B 130
#define O_C 40
#define GAP_B 4
#define GAP_V 0
#define GAP_0 2
#define XO_SLEEP 230
#define D_PF (-200)
#define STR_(x) #x
#define STR(x) STR_(x)

struct Args {
  const u16 *x, *mem0, *Wq, *Wk, *Wv, *Wo;
  const u16 *u, *A_re, *A_im, *B_re, *B_im, *C_re, *C_im, *log_dt, *Dskip;
  u16* out;
  u32* ctrl;
  char* ex;
};

typedef const __attribute__((address_space(4))) Args* ArgP;
__device__ __forceinline__ Args args() {
  ArgP p = (ArgP)__builtin_amdgcn_kernarg_segment_ptr();
  asm volatile("" : "+s"(p));
  Args a;
  a.x = p->x; a.mem0 = p->mem0; a.Wq = p->Wq; a.Wk = p->Wk; a.Wv = p->Wv; a.Wo = p->Wo; a.u = p->u; a.A_re = p->A_re; a.A_im = p->A_im;
  a.B_re = p->B_re; a.B_im = p->B_im; a.C_re = p->C_re; a.C_im = p->C_im; a.log_dt = p->log_dt; a.Dskip = p->Dskip; a.out = p->out; a.ctrl = p->ctrl; a.ex = p->ex;
  return a;
}
__device__ __forceinline__ bool is_dead(u32* ctrl) { return __hip_atomic_load(ctrl + CW_DEAD, RLX_AGENT) != 0; }
__device__ __forceinline__ void set_dead(u32* ctrl) { __hip_atomic_store(ctrl + CW_DEAD, 1u, RLX_AGENT); }
__device__ __forceinline__ void wait_ge(u32* ctrl, int word, u32 want, int slp) {
  for (int spins = 0;; ++spins) {
    asm volatile("" ::: "memory");
    u32 v = __builtin_amdgcn_readfirstlane(__hip_atomic_load(ctrl + word, RLX_AGENT));
    if (v >= want) break;
    if (spins > SPIN_CAP) { set_dead(ctrl); break; }
    if ((spins & 63) == 63 && __builtin_amdgcn_readfirstlane((int)is_dead(ctrl))) break;
    sleepn(slp);
  }
}
__device__ __forceinline__ u32 wait_ready(u32* ctrl, u64 tag, int grp) {
  const __amdgpu_buffer_rsrc_t rc = __builtin_amdgcn_make_buffer_rsrc(ctrl, 0, CTRL_BYTES, 0x00020000);
  u32 code = 0;
  for (int spins = 0; spins < SPIN_CAP; ++spins) {
    asm volatile("" ::: "memory");
    const u32x4 v = __builtin_amdgcn_raw_buffer_load_b128(rc, 0, grp * 128, SC1);
    code = __builtin_amdgcn_readfirstlane(v[2] ^ (u32)tag);
    const u32 bad = (v[0] ^ (u32)tag) | (v[1] ^ (u32)(tag >> 32)) | (v[3] ^ (u32)(tag >> 32) ^ code);
    if (__builtin_amdgcn_readfirstlane(bad) == 0) break;
    sleepn(16);
  }
  asm volatile("" ::: "memory");
  return code & 0x7000u;
}
__device__ __forceinline__ u32 pol_of(u32 code) { return (code & 0x2000u) ? 0x55555555u : 0xAAAAAAAAu; }
__device__ __forceinline__ void make_ready(u32* ctrl, u64 tag) {
  const int lane = threadIdx.x & 63;
  const __amdgpu_buffer_rsrc_t rc = __builtin_amdgcn_make_buffer_rsrc(ctrl, 0, CTRL_BYTES, 0x00020000);
#pragma unroll
  for (int i = 0; i < 5; ++i) __builtin_amdgcn_raw_buffer_store_b32(0u, rc, (i < 3 ? min(8 + i * 64 + lane, 172) : 64 + i * 64 + lane) * 128, 0, SC1);
  u32 tm[3];
  for (int spins = 0; spins < SPIN_CAP; ++spins) {
    asm volatile("" ::: "memory");
    u32 bad = 0;
#pragma unroll
    for (int i = 0; i < 4; ++i) {
      const u32x4 v = __builtin_amdgcn_raw_buffer_load_b128(rc, (i * 64 + lane) * 128, CW_MEAS * 4, SC1);
      bad |= (v[0] ^ (u32)tag) | (v[1] ^ (u32)(tag >> 32));
      if (i < 3) tm[i] = v[2];
    }
    if (__builtin_amdgcn_ballot_w64(bad != 0) == 0) break;
    sleepn(4);
  }
  WAIT_VM0();
  const int k = lane >> 3, same = ((k >> 1) ^ k ^ 1) & 1;
  int s = 0, d = 0;
#pragma unroll
  for (int i = 0; i < 3; ++i) {
    const int x0 = tm[i] & 0xfff, x1 = (tm[i] >> 16) & 0xfff;
    s += same ? x0 + x1 : -(x0 + x1);
    d += (x1 < x0) - (x1 > x0);
  }
#pragma unroll
  for (int m = 1; m < 64; m *= 2) s += __shfl_xor(s, m);
  if (same != (s < 0 ? 1 : 0)) d = 0;
#pragma unroll
  for (int m = 1; m < 32; m *= 2) d += __shfl_xor(d, m);
  const u32 code = (__shfl(d, 0) > 0 ? 0x1000u : 0u) | (__shfl(d, 32) > 0 ? 0x4000u : 0u) | (s < 0 ? 0u : 0x2000u);
  __builtin_amdgcn_raw_buffer_store_b128((u32x4){(u32)tag, (u32)(tag >> 32), (u32)tag ^ code, (u32)(tag >> 32) ^ code}, rc, lane * 16, 0, SC1);
}
__device__ __forceinline__ u32 max4(u32x4 v) { return max(max(v[0], v[1]), max(v[2], v[3])); }
#define POLL_FAIL(spins) ((++(spins) > SPIN_CAP) ? (set_dead(ctrl), true) : (((spins) & 255) == 255 && __builtin_amdgcn_readfirstlane((int)is_dead(ctrl))))
#define REC_ASM \
  "s_mov_b64 exec, -1\n" \
  "v_mbcnt_lo_u32_b32 v24, -1, 0\n" \
  "v_mbcnt_hi_u32_b32 v24, -1, v24\n" \
  "s_lshr_b32 s70, %6, 2\n" \
  "s_and_b32 s71, %6, 3\n" \
  "v_lshlrev_b32 v1, 4, v24\n" \
  "v_and_b32 v27, 7, v24\n" \
  "v_lshrrev_b32 v28, 3, v24\n" \
  "v_lshlrev_b32 v34, 11, v28\n" \
  "v_lshl_add_u32 v34, v27, 4, v34\n" \
  "v_lshlrev_b32 v32, 13, v28\n" \
  "v_lshl_add_u32 v32, v27, 4, v32\n" \
  "v_and_b32 v27, 32, v24\n" \
  "v_xor_b32 v32, v32, v27\n" \
  "v_xor_b32 v33, 64, v32\n" \
  "s_mul_i32 s88, %7, 40960\n" \
  "s_add_u32 s88, s88, 0\n" \
  "s_lshl_b32 s60, %7, 21\n" \
  "s_mov_b64 s[72:73], %0\n" \
  "s_and_b32 s73, s73, 0xffff\n" \
  "s_mov_b32 s74, 8388608\n" \
  "s_mov_b32 s75, 0x00020000\n" \
  "s_mov_b64 s[76:77], %1\n" \
  "s_and_b32 s77, s77, 0xffff\n" \
  "s_mov_b32 s78, 8388608\n" \
  "s_mov_b32 s79, 0x00020000\n" \
  "s_mov_b64 s[80:81], %2\n" \
  "s_and_b32 s81, s81, 0xffff\n" \
  "s_mov_b32 s82, 8388608\n" \
  "s_mov_b32 s83, 0x00020000\n" \
  "s_mov_b64 s[84:85], %3\n" \
  "s_and_b32 s85, s85, 0xffff\n" \
  "s_mov_b32 s86, 262144\n" \
  "s_mov_b32 s87, 0x00020000\n" \
  "s_mov_b64 s[90:91], %9\n" \
  "v_and_b32 v27, 1, v24\n" \
  "v_lshlrev_b32 v27, 11, v27\n" \
  "v_and_b32 v28, 4, v24\n" \
  "v_lshl_add_u32 v27, v28, 10, v27\n" \
  "v_and_b32 v28, 2, v24\n" \
  "v_lshl_add_u32 v27, v28, 3, v27\n" \
  "global_load_dwordx4 v[64:67], v27, s[90:91]\n" \
  "s_lshr_b32 s59, %6, 1\n" \
  "s_add_u32 s59, s59, %7\n" \
  "s_add_u32 s59, s59, 0\n" \
  "s_and_b32 s59, s59, 3\n" \
  "s_lshl_b32 s59, s59, 19\n" \
  "s_lshl_b32 s60, %7, 21\n" \
  "s_add_u32 s59, s59, s60\n" \
  "s_add_u32 s59, s59, 0\n" \
  "s_add_u32 m0, s88, 0\n" \
  "s_nop 0\n" \
  "buffer_load_dwordx4 v32, s[72:75], s59 offen lds\n" \
  "s_add_u32 m0, s88, 4096\n" \
  "s_nop 0\n" \
  "buffer_load_dwordx4 v32, s[76:79], s59 offen lds\n" \
  "s_add_u32 s59, s59, 65536\n" \
  "s_add_u32 m0, s88, 1024\n" \
  "s_nop 0\n" \
  "buffer_load_dwordx4 v33, s[72:75], s59 offen lds\n" \
  "s_add_u32 m0, s88, 5120\n" \
  "s_nop 0\n" \
  "buffer_load_dwordx4 v33, s[76:79], s59 offen lds\n" \
  "s_add_u32 s59, s59, 65536\n" \
  "s_add_u32 m0, s88, 2048\n" \
  "s_nop 0\n" \
  "buffer_load_dwordx4 v32, s[72:75], s59 offen lds\n" \
  "s_add_u32 m0, s88, 6144\n" \
  "s_nop 0\n" \
  "buffer_load_dwordx4 v32, s[76:79], s59 offen lds\n" \
  "s_add_u32 s59, s59, 65536\n" \
  "s_add_u32 m0, s88, 3072\n" \
  "s_nop 0\n" \
  "buffer_load_dwordx4 v33, s[72:75], s59 offen lds\n" \
  "s_add_u32 m0, s88, 7168\n" \
  "s_nop 0\n" \
  "buffer_load_dwordx4 v33, s[76:79], s59 offen lds\n" \
  "s_lshr_b32 s59, %6, 1\n" \
  "s_add_u32 s59, s59, %7\n" \
  "s_add_u32 s59, s59, 0\n" \
  "s_and_b32 s59, s59, 3\n" \
  "s_lshl_b32 s59, s59, 19\n" \
  "s_lshl_b32 s60, %7, 21\n" \
  "s_add_u32 s59, s59, s60\n" \
  "s_add_u32 s59, s59, 262144\n" \
  "s_add_u32 m0, s88, 8192\n" \
  "s_nop 0\n" \
  "buffer_load_dwordx4 v32, s[72:75], s59 offen lds\n" \
  "s_add_u32 m0, s88, 12288\n" \
  "s_nop 0\n" \
  "buffer_load_dwordx4 v32, s[76:79], s59 offen lds\n" \
  "s_add_u32 s59, s59, 65536\n" \
  "s_add_u32 m0, s88, 9216\n" \
  "s_nop 0\n" \
  "buffer_load_dwordx4 v33, s[72:75], s59 offen lds\n" \
  "s_add_u32 m0, s88, 13312\n" \
  "s_nop 0\n" \
  "buffer_load_dwordx4 v33, s[76:79], s59 offen lds\n" \
  "s_add_u32 s59, s59, 65536\n" \
  "s_add_u32 m0, s88, 10240\n" \
  "s_nop 0\n" \
  "buffer_load_dwordx4 v32, s[72:75], s59 offen lds\n" \
  "s_add_u32 m0, s88, 14336\n" \
  "s_nop 0\n" \
  "buffer_load_dwordx4 v32, s[76:79], s59 offen lds\n" \
  "s_add_u32 s59, s59, 65536\n" \
  "s_add_u32 m0, s88, 11264\n" \
  "s_nop 0\n" \
  "buffer_load_dwordx4 v33, s[72:75], s59 offen lds\n" \
  "s_add_u32 m0, s88, 15360\n" \
  "s_nop 0\n" \
  "buffer_load_dwordx4 v33, s[76:79], s59 offen lds\n" \
  "s_lshr_b32 s59, %6, 1\n" \
  "s_add_u32 s59, s59, %7\n" \
  "s_add_u32 s59, s59, 1\n" \
  "s_and_b32 s59, s59, 3\n" \
  "s_lshl_b32 s59, s59, 19\n" \
  "s_lshl_b32 s60, %7, 21\n" \
  "s_add_u32 s59, s59, s60\n" \
  "s_add_u32 s59, s59, 0\n" \
  "s_add_u32 m0, s88, 16384\n" \
  "s_nop 0\n" \
  "buffer_load_dwordx4 v32, s[72:75], s59 offen lds\n" \
  "s_add_u32 m0, s88, 20480\n" \
  "s_nop 0\n" \
  "buffer_load_dwordx4 v32, s[76:79], s59 offen lds\n" \
  "s_add_u32 s59, s59, 65536\n" \
  "s_add_u32 m0, s88, 17408\n" \
  "s_nop 0\n" \
  "buffer_load_dwordx4 v33, s[72:75], s59 offen lds\n" \
  "s_add_u32 m0, s88, 21504\n" \
  "s_nop 0\n" \
  "buffer_load_dwordx4 v33, s[76:79], s59 offen lds\n" \
  "s_add_u32 s59, s59, 65536\n" \
  "s_add_u32 m0, s88, 18432\n" \
  "s_nop 0\n" \
  "buffer_load_dwordx4 v32, s[72:75], s59 offen lds\n" \
  "s_add_u32 m0, s88, 22528\n" \
  "s_nop 0\n" \
  "buffer_load_dwordx4 v32, s[76:79], s59 offen lds\n" \
  "s_add_u32 s59, s59, 65536\n" \
  "s_add_u32 m0, s88, 19456\n" \
  "s_nop 0\n" \
  "buffer_load_dwordx4 v33, s[72:75], s59 offen lds\n" \
  "s_add_u32 m0, s88, 23552\n" \
  "s_nop 0\n" \
  "buffer_load_dwordx4 v33, s[76:79], s59 offen lds\n" \
  "v_and_b32 v36, 15, v24\n" \
  "v_lshrrev_b32 v37, 4, v24\n" \
  "v_lshrrev_b32 v38, 2, v36\n" \
  "v_and_b32 v39, 3, v24\n" \
  "v_lshrrev_b32 v46, 1, v37\n" \
  "v_lshlrev_b32 v46, 4, v46\n" \
  "v_and_b32 v27, 1, v37\n" \
  "v_lshl_add_u32 v46, v27, 2, v46\n" \
  "v_and_b32 v28, 1, v38\n" \
  "v_add_u32 v46, v46, v28\n" \
  "v_lshrrev_b32 v28, 1, v38\n" \
  "v_lshl_add_u32 v46, v28, 3, v46\n" \
  "v_lshl_add_u32 v27, v28, 1, v27\n" \
  "v_lshlrev_b32 v27, 5, v27\n" \
  "s_and_b32 s59, s71, 1\n" \
  "s_lshl_b32 s59, s59, 6\n" \
  "v_xor_b32 v27, s59, v27\n" \
  "v_lshl_add_u32 v27, v46, 7, v27\n" \
  "v_lshl_add_u32 v28, v39, 3, v27\n" \
  "v_add_u32 v42, s88, v28\n" \
  "v_xor_b32 v28, 32, v28\n" \
  "v_add_u32 v43, s88, v28\n" \
  "v_and_b32 v28, 1, v39\n" \
  "v_lshl_add_u32 v27, v28, 3, v27\n" \
  "v_lshrrev_b32 v28, 1, v39\n" \
  "v_lshl_add_u32 v27, v28, 12, v27\n" \
  "s_add_u32 s59, %7, 0\n" \
  "s_and_b32 s59, s59, 3\n" \
  "s_lshl_b32 s59, s59, 4\n" \
  "v_xor_b32 v28, s59, v27\n" \
  "v_add_u32 v52, s88, v28\n" \
  "s_add_u32 s59, %7, 1\n" \
  "s_and_b32 s59, s59, 3\n" \
  "s_lshl_b32 s59, s59, 4\n" \
  "v_xor_b32 v28, s59, v27\n" \
  "v_add_u32 v53, s88, v28\n" \
  "s_add_u32 s59, %7, 2\n" \
  "s_and_b32 s59, s59, 3\n" \
  "s_lshl_b32 s59, s59, 4\n" \
  "v_xor_b32 v28, s59, v27\n" \
  "v_add_u32 v54, s88, v28\n" \
  "s_add_u32 s59, %7, 3\n" \
  "s_and_b32 s59, s59, 3\n" \
  "s_lshl_b32 s59, s59, 4\n" \
  "v_xor_b32 v28, s59, v27\n" \
  "v_add_u32 v55, s88, v28\n" \
  "v_lshlrev_b32 v49, 7, v46\n" \
  "v_lshl_add_u32 v49, v39, 5, v49\n" \
  "v_add_u32 v49, s88, v49\n" \
  "s_mov_b64 s[44:45], %5\n" \
  "s_and_b32 s45, s45, 0xffff\n" \
  "s_mov_b32 s46, " STR(CTRL_BYTES) "\n" \
  "s_mov_b32 s47, 0x00020000\n" \
  "s_mov_b64 s[36:37], %4\n" \
  "s_and_b32 s37, s37, 0xffff\n" \
  "s_mov_b32 s38, " STR(EX_BYTES32) "\n" \
  "s_mov_b32 s39, 0x00020000\n" \
  "s_mov_b64 s[40:41], %4\n" \
  "s_add_u32 s40, s40, " STR(WS_K_OFF32) "\n" \
  "s_addc_u32 s41, s41, 0\n" \
  "s_and_b32 s41, s41, 0xffff\n" \
  "s_mov_b32 s42, 3*" STR(QKV_SZ32) "\n" \
  "s_mov_b32 s43, 0x00020000\n" \
  "v_lshrrev_b32 v50, 5, v24\n" \
  "v_and_b32 v51, 31, v24\n" \
  "v_mov_b32 v2, 0x4444\n" \
  "v_mov_b32 v27, 0xeeee\n" \
  "v_cmp_lt_u32 vcc, 1, v39\n" \
  "s_mul_i32 s59, %7, 1280\n" \
  "s_add_u32 s59, s59, 0\n" \
  "v_cndmask_b32 v2, v2, v27, vcc\n" \
  "v_cndmask_b32 v29, 0, -1, vcc\n" \
  "v_cndmask_b32 v4, -1, 0, vcc\n" \
  "v_mul_u32_u24 v27, 80, v37\n" \
  "v_add_lshl_u32 v27, v27, v36, 2\n" \
  "v_add_u32 v5, s59, v27\n" \
  "v_lshlrev_b32 v27, 3, v24\n" \
  "s_add_u32 s60, 0, 8192\n" \
  "v_add_u32 v27, s60, v27\n" \
  "s_mul_i32 s59, %7, 1536\n" \
  "v_add_u32 v6, s59, v27\n" \
  "s_add_u32 s59, %7, 1\n" \
  "s_and_b32 s59, s59, 3\n" \
  "s_mul_i32 s59, s59, 1536\n" \
  "v_add_u32 v3, s59, v27\n" \
  "s_add_u32 s59, %7, 2\n" \
  "s_and_b32 s59, s59, 3\n" \
  "s_mul_i32 s59, s59, 1536\n" \
  "v_add_u32 v244, s59, v27\n" \
  "s_add_u32 s59, %7, 3\n" \
  "s_and_b32 s59, s59, 3\n" \
  "s_mul_i32 s59, s59, 1536\n" \
  "v_add_u32 v245, s59, v27\n" \
  "v_and_b32 v27, 1, v37\n" \
  "v_lshlrev_b32 v27, 1, v27\n" \
  "v_and_b32 v28, 1, v38\n" \
  "v_add_u32 v27, v27, v28\n" \
  "v_mul_u32_u24 v27, 160, v27\n" \
  "v_lshrrev_b32 v28, 1, v38\n" \
  "v_lshl_add_u32 v27, v28, 5, v27\n" \
  "v_lshl_add_u32 v27, v39, 3, v27\n" \
  "s_and_b32 s59, %7, 1\n" \
  "s_mul_i32 s59, s59, 640\n" \
  "s_lshr_b32 s60, %7, 1\n" \
  "s_lshl_b32 s60, s60, 6\n" \
  "s_add_u32 s59, s59, s60\n" \
  "s_add_u32 s59, s59, 0\n" \
  "v_add_u32 v7, s59, v27\n" \
  "s_and_b32 s94, s70, 1\n" \
  "s_lshl_b32 s94, s94, 11\n" \
  "s_add_u32 s94, s94, 32768\n" \
  "v_add_u32 v17, s94, v1\n" \
  "s_lshl_b32 s59, s71, 2\n" \
  "s_add_u32 s59, s59, %7\n" \
  "s_lshl_b32 s59, s59, 8\n" \
  "v_and_b32 v8, 0x38, v24\n" \
  "v_lshlrev_b32 v8, 2, v8\n" \
  "v_and_b32 v27, 1, v24\n" \
  "v_lshl_add_u32 v8, v27, 4, v8\n" \
  "v_and_b32 v27, 6, v24\n" \
  "v_lshl_add_u32 v8, v27, 1, v8\n" \
  "v_add_u32 v8, s59, v8\n" \
  "v_and_b32 v27, 7, v24\n" \
  "v_lshlrev_b32 v27, 4, v27\n" \
  "v_lshrrev_b32 v28, 3, v24\n" \
  "v_add_u32 v15, s59, v1\n" \
  "s_xor_b32 s59, s71, 2\n" \
  "s_lshl_b32 s59, s59, 2\n" \
  "s_add_u32 s59, s59, %7\n" \
  "s_lshl_b32 s59, s59, 7\n" \
  "s_add_u32 s59, s59, s94\n" \
  "v_lshlrev_b32 v9, 2, v51\n" \
  "v_add_u32 v9, s59, v9\n" \
  "v_lshl_add_u32 v9, v50, 31, v9\n" \
  "v_add_u32 v25, s59, v27\n" \
  "v_cmp_eq_u32 vcc, 2, v28\n" \
  "s_and_b32 s60, %6, 31\n" \
  "s_lshl_b32 s60, s60, 2\n" \
  "s_or_b32 s60, s60, %7\n" \
  "v_cndmask_b32 v15, v15, v25, vcc\n" \
  "s_cmp_eq_u32 s60, 1\n" \
  "s_cselect_b32 s59, 0, 0x80000000\n" \
  "s_or_b32 s59, s59, 32768+2048\n" \
  "v_mov_b32 v25, 0x80000000\n" \
  "v_cmp_eq_u32 vcc, 3, v28\n" \
  "v_lshl_add_u32 v14, v24, 3, s59\n" \
  "s_nop 0\n" \
  "v_cndmask_b32 v15, v15, v25, vcc\n" \
  "v_lshl_add_u32 v25, v51, 4, s59\n" \
  "v_cmp_eq_u32 vcc, 1, v50\n" \
  "s_lshr_b32 s59, %6, 5\n" \
  "s_and_b32 s93, s59, 1\n" \
  "s_lshl_b32 s93, s93, 14\n" \
  "s_lshr_b32 s60, s59, 1\n" \
  "s_lshl_b32 s60, s60, 12\n" \
  "s_or_b32 s93, s93, s60\n" \
  "s_lshl_b32 s59, s59, 7\n" \
  "s_add_u32 s59, s59, 32768+2048\n" \
  "s_or_b32 s59, s59, s93\n" \
  "v_cndmask_b32 v15, v15, v25, vcc\n" \
  "v_and_b32 v25, 1, v39\n" \
  "v_lshlrev_b32 v25, 14, v25\n" \
  "v_lshrrev_b32 v13, 1, v39\n" \
  "v_lshl_or_b32 v13, v13, 12, v25\n" \
  "v_xor_b32 v13, s59, v13\n" \
  "s_xor_b32 s59, %7, 1\n" \
  "s_lshl_b32 s59, s59, 29\n" \
  "v_or_b32 v13, s59, v13\n" \
  "s_lshl_b32 s59, %7, 18\n" \
  "v_lshl_add_u32 v10, v28, 15, v27\n" \
  "v_add_u32 v10, s59, v10\n" \
  "s_lshl_b32 s60, %7, 4\n" \
  "s_add_u32 s60, s60, s70\n" \
  "v_lshl_add_u32 v25, v28, 1, s60\n" \
  "v_and_b32 v25, 31, v25\n" \
  "v_lshl_add_u32 v10, v25, 7, v10\n" \
  "s_and_b32 s59, %6, 31\n" \
  "s_lshl_b32 s59, s59, 15\n" \
  "s_lshl_b32 s60, %7, 10\n" \
  "s_add_u32 s59, s59, s60\n" \
  "v_add_u32 v11, s59, v1\n" \
  "s_mov_b32 s49, (" STR(EXP_A) "&1)<<13\n" \
  "s_mov_b32 s51, (" STR(EXP_C) "&1)<<13\n" \
  "s_lshr_b32 s59, %6, 5\n" \
  "s_lshl_b32 s59, s59, 15\n" \
  "s_and_b32 s60, %6, 31\n" \
  "s_lshl_b32 s60, s60, 7\n" \
  "s_add_u32 s59, s59, s60\n" \
  "s_or_b32 s49, s49, " STR(EXP_A) "<<21\n" \
  "s_or_b32 s59, s59, s49\n" \
  "v_lshrrev_b32 v25, 3, v36\n" \
  "v_lshlrev_b32 v25, 14, v25\n" \
  "v_lshl_or_b32 v25, v50, 12, v25\n" \
  "v_xor_b32 v12, s59, v25\n" \
  "v_add_u32 v12, v12, v27\n" \
  "v_lshl_add_u32 v12, v37, 31, v12\n" \
  "v_and_b32 v27, 1, v37\n" \
  "v_lshlrev_b32 v27, 5, v27\n" \
  "v_xor_b32 v27, v1, v27\n" \
  "s_add_u32 s59, 0, 32768\n" \
  "v_add_u32 v19, s59, v27\n" \
  "s_lshl_b32 s60, %7, 12\n" \
  "v_add_u32 v18, s60, v19\n" \
  "v_and_b32 v20, 15, v46\n" \
  "v_lshlrev_b32 v20, 4, v20\n" \
  "v_lshl_add_u32 v20, v50, 10, v20\n" \
  "v_lshl_add_u32 v20, v39, 8, v20\n" \
  "v_and_b32 v27, 1, v39\n" \
  "v_lshlrev_b32 v27, 5, v27\n" \
  "v_xor_b32 v20, v20, v27\n" \
  "v_xor_b32 v21, 32, v20\n" \
  "s_add_u32 s59, s59, 8192\n" \
  "v_add_u32 v20, s59, v20\n" \
  "v_add_u32 v21, s59, v21\n" \
  "v_and_b32 v22, 1, v24\n" \
  "v_lshlrev_b32 v22, 7, v22\n" \
  "s_lshr_b32 s59, %7, 1\n" \
  "s_xor_b32 s59, s59, s70\n" \
  "s_lshl_b32 s59, s59, 8\n" \
  "s_add_u32 s59, s59, " STR(CW_GPROG) "*4\n" \
  "v_add_u32 v22, s59, v22\n" \
  "s_lshr_b32 s59, %6, 1\n" \
  "s_add_u32 s59, s59, %7\n" \
  "s_add_u32 s59, s59, 0\n" \
  "s_and_b32 s59, s59, 3\n" \
  "s_lshl_b32 s59, s59, 10\n" \
  "v_add_u32 v80, s59, v1\n" \
  "v_add_u32 v84, s59, v19\n" \
  "v_mov_b32 v88, -1\n" \
  "s_lshr_b32 s59, %6, 1\n" \
  "s_add_u32 s59, s59, %7\n" \
  "s_add_u32 s59, s59, 1\n" \
  "s_and_b32 s59, s59, 3\n" \
  "s_lshl_b32 s59, s59, 10\n" \
  "v_add_u32 v81, s59, v1\n" \
  "v_add_u32 v85, s59, v19\n" \
  "v_mov_b32 v89, -1\n" \
  "s_lshr_b32 s59, %6, 1\n" \
  "s_add_u32 s59, s59, %7\n" \
  "s_add_u32 s59, s59, 2\n" \
  "s_and_b32 s59, s59, 3\n" \
  "s_lshl_b32 s59, s59, 10\n" \
  "v_add_u32 v82, s59, v1\n" \
  "v_add_u32 v86, s59, v19\n" \
  "v_mov_b32 v90, -1\n" \
  "s_lshr_b32 s59, %6, 1\n" \
  "s_add_u32 s59, s59, %7\n" \
  "s_add_u32 s59, s59, 3\n" \
  "s_and_b32 s59, s59, 3\n" \
  "s_lshl_b32 s59, s59, 10\n" \
  "v_add_u32 v83, s59, v1\n" \
  "v_add_u32 v87, s59, v19\n" \
  "v_mov_b32 v91, -1\n" \
  "v_mov_b32 v248, 0\n" \
  "v_mov_b32 v27, 0x3f80\n" \
  "v_and_b32 v25, 7, v36\n" \
  "v_cmp_eq_u32 vcc, 0, v25\n" \
  "v_mov_b32 v28, 1.0\n" \
  "s_nop 0\n" \
  "v_cndmask_b32 v248, v248, v27, vcc\n" \
  "v_cmp_eq_u32 vcc, 1, v25\n" \
  "s_nop 1\n" \
  "v_cndmask_b32 v248, v248, v28, vcc\n" \
  "v_mov_b32 v249, 0\n" \
  "v_mov_b32 v27, 0x3f80\n" \
  "v_and_b32 v25, 7, v36\n" \
  "v_cmp_eq_u32 vcc, 2, v25\n" \
  "v_mov_b32 v28, 1.0\n" \
  "s_nop 0\n" \
  "v_cndmask_b32 v249, v249, v27, vcc\n" \
  "v_cmp_eq_u32 vcc, 3, v25\n" \
  "s_nop 1\n" \
  "v_cndmask_b32 v249, v249, v28, vcc\n" \
  "v_mov_b32 v250, 0\n" \
  "v_mov_b32 v27, 0x3f80\n" \
  "v_and_b32 v25, 7, v36\n" \
  "v_cmp_eq_u32 vcc, 4, v25\n" \
  "v_mov_b32 v28, 1.0\n" \
  "s_nop 0\n" \
  "v_cndmask_b32 v250, v250, v27, vcc\n" \
  "v_cmp_eq_u32 vcc, 5, v25\n" \
  "s_nop 1\n" \
  "v_cndmask_b32 v250, v250, v28, vcc\n" \
  "v_mov_b32 v251, 0\n" \
  "v_mov_b32 v27, 0x3f80\n" \
  "v_and_b32 v25, 7, v36\n" \
  "v_cmp_eq_u32 vcc, 6, v25\n" \
  "v_mov_b32 v28, 1.0\n" \
  "s_nop 0\n" \
  "v_cndmask_b32 v251, v251, v27, vcc\n" \
  "v_cmp_eq_u32 vcc, 7, v25\n" \
  "s_nop 1\n" \
  "v_cndmask_b32 v251, v251, v28, vcc\n" \
  "v_and_b32 v27, 1, v24\n" \
  "v_cmp_eq_u32 vcc, 1, v27\n" \
  "v_mov_b32 v246, 0x05040100\n" \
  "v_mov_b32 v28, 0x03020706\n" \
  "v_cndmask_b32 v246, v246, v28, vcc\n" \
  "v_mov_b32 v252, 0x3f803f80\n" \
  "v_mov_b32 v253, 0x3f803f80\n" \
  "v_mov_b32 v254, 0x3f803f80\n" \
  "v_mov_b32 v255, 0\n" \
  "v_mov_b32 v30, 0\n" \
  "s_mov_b32 s92, 0x3e0293ee\n" \
  "s_mov_b32 s58, 0\n" \
  "s_mov_b32 s29, " STR(E_0) "\n" \
  "s_mov_b32 s48, 0\n" \
  "s_xor_b32 s49, s49, s93\n" \
  "s_lshl_b32 s59, %7, 15\n" \
  "s_add_u32 s49, s49, s59\n" \
  "s_mov_b32 s53, 0\n" \
  "s_or_b32 s51, s51, " STR(EXP_C) "<<21\n" \
  "s_xor_b32 s52, s51, s93\n" \
  "s_and_b32 s59, %6, 16\n" \
  "s_lshl_b32 s59, s59, 10\n" \
  "s_xor_b32 s59, s59, 1052672\n" \
  "s_xor_b32 s52, s52, s59\n" \
  "s_lshr_b32 s59, %7, 1\n" \
  "s_xor_b32 s59, s59, s71\n" \
  "s_and_b32 s59, s59, 1\n" \
  "s_lshl_b32 s59, s59, 14\n" \
  "s_xor_b32 s51, s51, s59\n" \
  "s_lshr_b32 s59, s71, 1\n" \
  "s_lshl_b32 s59, s59, 12\n" \
  "s_xor_b32 s51, s51, s59\n" \
  "s_xor_b32 s51, s51, 1052672\n" \
  "s_and_b32 s59, s70, 7\n" \
  "s_lshl_b32 s59, s59, 16\n" \
  "s_or_b32 s93, s93, s59\n" \
  "s_lshr_b32 s59, %7, 1\n" \
  "s_mul_i32 s59, s59, " STR(QKV_SZ32) "\n" \
  "s_lshl_b32 s60, s70, 14\n" \
  "s_add_u32 s59, s59, s60\n" \
  "s_and_b32 s60, %7, 1\n" \
  "s_lshl_b32 s60, s60, 12\n" \
  "s_add_u32 s59, s59, s60\n" \
  "v_add_u32 v247, s59, v1\n" \
  "s_lshr_b32 s61, %6, 5\n" \
  "s_lshl_b32 s61, s61, 7\n" \
  "s_mov_b64 s[90:91], %8\n" \
  "s_lshr_b32 s59, %6, 1\n" \
  "s_add_u32 s59, s59, %7\n" \
  "s_add_u32 s59, s59, 1\n" \
  "s_and_b32 s59, s59, 3\n" \
  "s_lshl_b32 s59, s59, 19\n" \
  "s_lshl_b32 s60, %7, 21\n" \
  "s_add_u32 s59, s59, s60\n" \
  "s_add_u32 s59, s59, 262144\n" \
  "s_add_u32 m0, s88, 24576\n" \
  "s_nop 0\n" \
  "buffer_load_dwordx4 v32, s[72:75], s59 offen lds\n" \
  "s_add_u32 m0, s88, 28672\n" \
  "s_nop 0\n" \
  "buffer_load_dwordx4 v32, s[76:79], s59 offen lds\n" \
  "s_add_u32 s59, s59, 65536\n" \
  "s_add_u32 m0, s88, 25600\n" \
  "s_nop 0\n" \
  "buffer_load_dwordx4 v33, s[72:75], s59 offen lds\n" \
  "s_add_u32 m0, s88, 29696\n" \
  "s_nop 0\n" \
  "buffer_load_dwordx4 v33, s[76:79], s59 offen lds\n" \
  "s_add_u32 s59, s59, 65536\n" \
  "s_add_u32 m0, s88, 26624\n" \
  "s_nop 0\n" \
  "buffer_load_dwordx4 v32, s[72:75], s59 offen lds\n" \
  "s_add_u32 m0, s88, 30720\n" \
  "s_nop 0\n" \
  "buffer_load_dwordx4 v32, s[76:79], s59 offen lds\n" \
  "s_add_u32 s59, s59, 65536\n" \
  "s_add_u32 m0, s88, 27648\n" \
  "s_nop 0\n" \
  "buffer_load_dwordx4 v33, s[72:75], s59 offen lds\n" \
  "s_add_u32 m0, s88, 31744\n" \
  "s_nop 0\n" \
  "buffer_load_dwordx4 v33, s[76:79], s59 offen lds\n" \
  "s_lshr_b32 s59, %6, 1\n" \
  "s_add_u32 s59, s59, %7\n" \
  "s_add_u32 s59, s59, 2\n" \
  "s_and_b32 s59, s59, 3\n" \
  "s_lshl_b32 s59, s59, 19\n" \
  "s_lshl_b32 s60, %7, 21\n" \
  "s_add_u32 s59, s59, s60\n" \
  "s_add_u32 s59, s59, 0\n" \
  "s_add_u32 m0, s88, 32768\n" \
  "s_nop 0\n" \
  "buffer_load_dwordx4 v32, s[72:75], s59 offen lds\n" \
  "s_add_u32 m0, s88, 36864\n" \
  "s_nop 0\n" \
  "buffer_load_dwordx4 v32, s[76:79], s59 offen lds\n" \
  "s_add_u32 s59, s59, 65536\n" \
  "s_add_u32 m0, s88, 33792\n" \
  "s_nop 0\n" \
  "buffer_load_dwordx4 v33, s[72:75], s59 offen lds\n" \
  "s_add_u32 m0, s88, 37888\n" \
  "s_nop 0\n" \
  "buffer_load_dwordx4 v33, s[76:79], s59 offen lds\n" \
  "s_add_u32 s59, s59, 65536\n" \
  "s_add_u32 m0, s88, 34816\n" \
  "s_nop 0\n" \
  "buffer_load_dwordx4 v32, s[72:75], s59 offen lds\n" \
  "s_add_u32 m0, s88, 38912\n" \
  "s_nop 0\n" \
  "buffer_load_dwordx4 v32, s[76:79], s59 offen lds\n" \
  "s_add_u32 s59, s59, 65536\n" \
  "s_add_u32 m0, s88, 35840\n" \
  "s_nop 0\n" \
  "buffer_load_dwordx4 v33, s[72:75], s59 offen lds\n" \
  "s_add_u32 m0, s88, 39936\n" \
  "s_nop 0\n" \
  "buffer_load_dwordx4 v33, s[76:79], s59 offen lds\n" \
  "s_waitcnt vmcnt(32)\n" \
  "ds_read_b64_tr_b16 a[0:1], v52 offset:0\n" \
  "ds_read_b64_tr_b16 a[2:3], v52 offset:256\n" \
  "ds_read_b64_tr_b16 a[8:9], v53 offset:0\n" \
  "ds_read_b64_tr_b16 a[10:11], v53 offset:256\n" \
  "ds_read_b64_tr_b16 a[16:17], v54 offset:0\n" \
  "ds_read_b64_tr_b16 a[18:19], v54 offset:256\n" \
  "ds_read_b64_tr_b16 a[24:25], v55 offset:0\n" \
  "ds_read_b64_tr_b16 a[26:27], v55 offset:256\n" \
  "s_waitcnt lgkmcnt(0)\n" \
  "s_lshr_b32 s59, %6, 1\n" \
  "s_add_u32 s59, s59, %7\n" \
  "s_add_u32 s59, s59, 2\n" \
  "s_and_b32 s59, s59, 3\n" \
  "s_lshl_b32 s59, s59, 19\n" \
  "s_lshl_b32 s60, %7, 21\n" \
  "s_add_u32 s59, s59, s60\n" \
  "s_add_u32 s59, s59, 262144\n" \
  "s_add_u32 m0, s88, 0\n" \
  "s_nop 0\n" \
  "buffer_load_dwordx4 v32, s[72:75], s59 offen lds\n" \
  "s_add_u32 m0, s88, 4096\n" \
  "s_nop 0\n" \
  "buffer_load_dwordx4 v32, s[76:79], s59 offen lds\n" \
  "s_add_u32 s59, s59, 65536\n" \
  "s_add_u32 m0, s88, 1024\n" \
  "s_nop 0\n" \
  "buffer_load_dwordx4 v33, s[72:75], s59 offen lds\n" \
  "s_add_u32 m0, s88, 5120\n" \
  "s_nop 0\n" \
  "buffer_load_dwordx4 v33, s[76:79], s59 offen lds\n" \
  "s_add_u32 s59, s59, 65536\n" \
  "s_add_u32 m0, s88, 2048\n" \
  "s_nop 0\n" \
  "buffer_load_dwordx4 v32, s[72:75], s59 offen lds\n" \
  "s_add_u32 m0, s88, 6144\n" \
  "s_nop 0\n" \
  "buffer_load_dwordx4 v32, s[76:79], s59 offen lds\n" \
  "s_add_u32 s59, s59, 65536\n" \
  "s_add_u32 m0, s88, 3072\n" \
  "s_nop 0\n" \
  "buffer_load_dwordx4 v33, s[72:75], s59 offen lds\n" \
  "s_add_u32 m0, s88, 7168\n" \
  "s_nop 0\n" \
  "buffer_load_dwordx4 v33, s[76:79], s59 offen lds\n" \
  "s_waitcnt vmcnt(32)\n" \
  "ds_read_b64_tr_b16 a[4:5], v52 offset:8192\n" \
  "ds_read_b64_tr_b16 a[6:7], v52 offset:8448\n" \
  "ds_read_b64_tr_b16 a[12:13], v53 offset:8192\n" \
  "ds_read_b64_tr_b16 a[14:15], v53 offset:8448\n" \
  "ds_read_b64_tr_b16 a[20:21], v54 offset:8192\n" \
  "ds_read_b64_tr_b16 a[22:23], v54 offset:8448\n" \
  "ds_read_b64_tr_b16 a[28:29], v55 offset:8192\n" \
  "ds_read_b64_tr_b16 a[30:31], v55 offset:8448\n" \
  "buffer_load_dwordx4 v[68:71], off, s[44:47], s61 sc1\n" \
  "s_waitcnt lgkmcnt(0)\n" \
  "s_lshr_b32 s59, %6, 1\n" \
  "s_add_u32 s59, s59, %7\n" \
  "s_add_u32 s59, s59, 3\n" \
  "s_and_b32 s59, s59, 3\n" \
  "s_lshl_b32 s59, s59, 19\n" \
  "s_lshl_b32 s60, %7, 21\n" \
  "s_add_u32 s59, s59, s60\n" \
  "s_add_u32 s59, s59, 0\n" \
  "s_add_u32 m0, s88, 8192\n" \
  "s_nop 0\n" \
  "buffer_load_dwordx4 v32, s[72:75], s59 offen lds\n" \
  "s_add_u32 m0, s88, 12288\n" \
  "s_nop 0\n" \
  "buffer_load_dwordx4 v32, s[76:79], s59 offen lds\n" \
  "s_add_u32 s59, s59, 65536\n" \
  "s_add_u32 m0, s88, 9216\n" \
  "s_nop 0\n" \
  "buffer_load_dwordx4 v33, s[72:75], s59 offen lds\n" \
  "s_add_u32 m0, s88, 13312\n" \
  "s_nop 0\n" \
  "buffer_load_dwordx4 v33, s[76:79], s59 offen lds\n" \
  "s_add_u32 s59, s59, 65536\n" \
  "s_add_u32 m0, s88, 10240\n" \
  "s_nop 0\n" \
  "buffer_load_dwordx4 v32, s[72:75], s59 offen lds\n" \
  "s_add_u32 m0, s88, 14336\n" \
  "s_nop 0\n" \
  "buffer_load_dwordx4 v32, s[76:79], s59 offen lds\n" \
  "s_add_u32 s59, s59, 65536\n" \
  "s_add_u32 m0, s88, 11264\n" \
  "s_nop 0\n" \
  "buffer_load_dwordx4 v33, s[72:75], s59 offen lds\n" \
  "s_add_u32 m0, s88, 15360\n" \
  "s_nop 0\n" \
  "buffer_load_dwordx4 v33, s[76:79], s59 offen lds\n" \
  "s_waitcnt vmcnt(33)\n" \
  "ds_read_b64_tr_b16 a[32:33], v52 offset:16384\n" \
  "ds_read_b64_tr_b16 a[34:35], v52 offset:16640\n" \
  "ds_read_b64_tr_b16 a[40:41], v53 offset:16384\n" \
  "ds_read_b64_tr_b16 a[42:43], v53 offset:16640\n" \
  "ds_read_b64_tr_b16 a[48:49], v54 offset:16384\n" \
  "ds_read_b64_tr_b16 a[50:51], v54 offset:16640\n" \
  "ds_read_b64_tr_b16 a[56:57], v55 offset:16384\n" \
  "ds_read_b64_tr_b16 a[58:59], v55 offset:16640\n" \
  "s_waitcnt lgkmcnt(0)\n" \
  "s_lshr_b32 s59, %6, 1\n" \
  "s_add_u32 s59, s59, %7\n" \
  "s_add_u32 s59, s59, 3\n" \
  "s_and_b32 s59, s59, 3\n" \
  "s_lshl_b32 s59, s59, 19\n" \
  "s_lshl_b32 s60, %7, 21\n" \
  "s_add_u32 s59, s59, s60\n" \
  "s_add_u32 s59, s59, 262144\n" \
  "s_add_u32 m0, s88, 16384\n" \
  "s_nop 0\n" \
  "buffer_load_dwordx4 v32, s[72:75], s59 offen lds\n" \
  "s_add_u32 m0, s88, 20480\n" \
  "s_nop 0\n" \
  "buffer_load_dwordx4 v32, s[76:79], s59 offen lds\n" \
  "s_add_u32 s59, s59, 65536\n" \
  "s_add_u32 m0, s88, 17408\n" \
  "s_nop 0\n" \
  "buffer_load_dwordx4 v33, s[72:75], s59 offen lds\n" \
  "s_add_u32 m0, s88, 21504\n" \
  "s_nop 0\n" \
  "buffer_load_dwordx4 v33, s[76:79], s59 offen lds\n" \
  "s_add_u32 s59, s59, 65536\n" \
  "s_add_u32 m0, s88, 18432\n" \
  "s_nop 0\n" \
  "buffer_load_dwordx4 v32, s[72:75], s59 offen lds\n" \
  "s_add_u32 m0, s88, 22528\n" \
  "s_nop 0\n" \
  "buffer_load_dwordx4 v32, s[76:79], s59 offen lds\n" \
  "s_add_u32 s59, s59, 65536\n" \
  "s_add_u32 m0, s88, 19456\n" \
  "s_nop 0\n" \
  "buffer_load_dwordx4 v33, s[72:75], s59 offen lds\n" \
  "s_add_u32 m0, s88, 23552\n" \
  "s_nop 0\n" \
  "buffer_load_dwordx4 v33, s[76:79], s59 offen lds\n" \
  "s_waitcnt vmcnt(33)\n" \
  "ds_read_b64_tr_b16 a[36:37], v52 offset:24576\n" \
  "ds_read_b64_tr_b16 a[38:39], v52 offset:24832\n" \
  "ds_read_b64_tr_b16 a[44:45], v53 offset:24576\n" \
  "ds_read_b64_tr_b16 a[46:47], v53 offset:24832\n" \
  "ds_read_b64_tr_b16 a[52:53], v54 offset:24576\n" \
  "ds_read_b64_tr_b16 a[54:55], v54 offset:24832\n" \
  "ds_read_b64_tr_b16 a[60:61], v55 offset:24576\n" \
  "ds_read_b64_tr_b16 a[62:63], v55 offset:24832\n" \
  "s_waitcnt lgkmcnt(0)\n" \
  "s_lshr_b32 s59, %6, 1\n" \
  "s_add_u32 s59, s59, %7\n" \
  "s_add_u32 s59, s59, 0\n" \
  "s_and_b32 s59, s59, 3\n" \
  "s_lshl_b32 s59, s59, 19\n" \
  "s_lshl_b32 s60, %7, 21\n" \
  "s_add_u32 s59, s59, s60\n" \
  "s_add_u32 s59, s59, 0\n" \
  "s_add_u32 m0, s88, 24576\n" \
  "s_nop 0\n" \
  "buffer_load_dwordx4 v32, s[80:83], s59 offen lds\n" \
  "s_add_u32 s59, s59, 65536\n" \
  "s_add_u32 m0, s88, 25600\n" \
  "s_nop 0\n" \
  "buffer_load_dwordx4 v33, s[80:83], s59 offen lds\n" \
  "s_add_u32 s59, s59, 65536\n" \
  "s_add_u32 m0, s88, 26624\n" \
  "s_nop 0\n" \
  "buffer_load_dwordx4 v32, s[80:83], s59 offen lds\n" \
  "s_add_u32 s59, s59, 65536\n" \
  "s_add_u32 m0, s88, 27648\n" \
  "s_nop 0\n" \
  "buffer_load_dwordx4 v33, s[80:83], s59 offen lds\n" \
  "s_add_u32 s59, s59, 65536\n" \
  "s_add_u32 m0, s88, 28672\n" \
  "s_nop 0\n" \
  "buffer_load_dwordx4 v32, s[80:83], s59 offen lds\n" \
  "s_add_u32 s59, s59, 65536\n" \
  "s_add_u32 m0, s88, 29696\n" \
  "s_nop 0\n" \
  "buffer_load_dwordx4 v33, s[80:83], s59 offen lds\n" \
  "s_add_u32 s59, s59, 65536\n" \
  "s_add_u32 m0, s88, 30720\n" \
  "s_nop 0\n" \
  "buffer_load_dwordx4 v32, s[80:83], s59 offen lds\n" \
  "s_add_u32 s59, s59, 65536\n" \
  "s_add_u32 m0, s88, 31744\n" \
  "s_nop 0\n" \
  "buffer_load_dwordx4 v33, s[80:83], s59 offen lds\n" \
  "s_waitcnt vmcnt(33)\n" \
  "ds_read_b64_tr_b16 a[64:65], v52 offset:32768\n" \
  "ds_read_b64_tr_b16 a[66:67], v52 offset:33024\n" \
  "ds_read_b64_tr_b16 a[72:73], v53 offset:32768\n" \
  "ds_read_b64_tr_b16 a[74:75], v53 offset:33024\n" \
  "ds_read_b64_tr_b16 a[80:81], v54 offset:32768\n" \
  "ds_read_b64_tr_b16 a[82:83], v54 offset:33024\n" \
  "ds_read_b64_tr_b16 a[88:89], v55 offset:32768\n" \
  "ds_read_b64_tr_b16 a[90:91], v55 offset:33024\n" \
  "s_waitcnt lgkmcnt(0)\n" \
  "s_lshr_b32 s59, %6, 1\n" \
  "s_add_u32 s59, s59, %7\n" \
  "s_add_u32 s59, s59, 1\n" \
  "s_and_b32 s59, s59, 3\n" \
  "s_lshl_b32 s59, s59, 19\n" \
  "s_lshl_b32 s60, %7, 21\n" \
  "s_add_u32 s59, s59, s60\n" \
  "s_add_u32 s59, s59, 0\n" \
  "s_add_u32 m0, s88, 32768\n" \
  "s_nop 0\n" \
  "buffer_load_dwordx4 v32, s[80:83], s59 offen lds\n" \
  "s_add_u32 s59, s59, 65536\n" \
  "s_add_u32 m0, s88, 33792\n" \
  "s_nop 0\n" \
  "buffer_load_dwordx4 v33, s[80:83], s59 offen lds\n" \
  "s_add_u32 s59, s59, 65536\n" \
  "s_add_u32 m0, s88, 34816\n" \
  "s_nop 0\n" \
  "buffer_load_dwordx4 v32, s[80:83], s59 offen lds\n" \
  "s_add_u32 s59, s59, 65536\n" \
  "s_add_u32 m0, s88, 35840\n" \
  "s_nop 0\n" \
  "buffer_load_dwordx4 v33, s[80:83], s59 offen lds\n" \
  "s_add_u32 s59, s59, 65536\n" \
  "s_add_u32 m0, s88, 36864\n" \
  "s_nop 0\n" \
  "buffer_load_dwordx4 v32, s[80:83], s59 offen lds\n" \
  "s_add_u32 s59, s59, 65536\n" \
  "s_add_u32 m0, s88, 37888\n" \
  "s_nop 0\n" \
  "buffer_load_dwordx4 v33, s[80:83], s59 offen lds\n" \
  "s_add_u32 s59, s59, 65536\n" \
  "s_add_u32 m0, s88, 38912\n" \
  "s_nop 0\n" \
  "buffer_load_dwordx4 v32, s[80:83], s59 offen lds\n" \
  "s_add_u32 s59, s59, 65536\n" \
  "s_add_u32 m0, s88, 39936\n" \
  "s_nop 0\n" \
  "buffer_load_dwordx4 v33, s[80:83], s59 offen lds\n" \
  "s_waitcnt vmcnt(33)\n" \
  "ds_read_b64_tr_b16 a[68:69], v52 offset:0\n" \
  "ds_read_b64_tr_b16 a[70:71], v52 offset:256\n" \
  "ds_read_b64_tr_b16 a[76:77], v53 offset:0\n" \
  "ds_read_b64_tr_b16 a[78:79], v53 offset:256\n" \
  "ds_read_b64_tr_b16 a[84:85], v54 offset:0\n" \
  "ds_read_b64_tr_b16 a[86:87], v54 offset:256\n" \
  "ds_read_b64_tr_b16 a[92:93], v55 offset:0\n" \
  "ds_read_b64_tr_b16 a[94:95], v55 offset:256\n" \
  "s_waitcnt lgkmcnt(0)\n" \
  "s_lshr_b32 s59, %6, 1\n" \
  "s_add_u32 s59, s59, %7\n" \
  "s_add_u32 s59, s59, 2\n" \
  "s_and_b32 s59, s59, 3\n" \
  "s_lshl_b32 s59, s59, 19\n" \
  "s_lshl_b32 s60, %7, 21\n" \
  "s_add_u32 s59, s59, s60\n" \
  "s_add_u32 s59, s59, 0\n" \
  "s_add_u32 m0, s88, 0\n" \
  "s_nop 0\n" \
  "buffer_load_dwordx4 v32, s[80:83], s59 offen lds\n" \
  "s_add_u32 s59, s59, 65536\n" \
  "s_add_u32 m0, s88, 1024\n" \
  "s_nop 0\n" \
  "buffer_load_dwordx4 v33, s[80:83], s59 offen lds\n" \
  "s_add_u32 s59, s59, 65536\n" \
  "s_add_u32 m0, s88, 2048\n" \
  "s_nop 0\n" \
  "buffer_load_dwordx4 v32, s[80:83], s59 offen lds\n" \
  "s_add_u32 s59, s59, 65536\n" \
  "s_add_u32 m0, s88, 3072\n" \
  "s_nop 0\n" \
  "buffer_load_dwordx4 v33, s[80:83], s59 offen lds\n" \
  "s_add_u32 s59, s59, 65536\n" \
  "s_add_u32 m0, s88, 4096\n" \
  "s_nop 0\n" \
  "buffer_load_dwordx4 v32, s[80:83], s59 offen lds\n" \
  "s_add_u32 s59, s59, 65536\n" \
  "s_add_u32 m0, s88, 5120\n" \
  "s_nop 0\n" \
  "buffer_load_dwordx4 v33, s[80:83], s59 offen lds\n" \
  "s_add_u32 s59, s59, 65536\n" \
  "s_add_u32 m0, s88, 6144\n" \
  "s_nop 0\n" \
  "buffer_load_dwordx4 v32, s[80:83], s59 offen lds\n" \
  "s_add_u32 s59, s59, 65536\n" \
  "s_add_u32 m0, s88, 7168\n" \
  "s_nop 0\n" \
  "buffer_load_dwordx4 v33, s[80:83], s59 offen lds\n" \
  "s_waitcnt vmcnt(32)\n" \
  "ds_read_b64_tr_b16 a[96:97], v52 offset:8192\n" \
  "ds_read_b64_tr_b16 a[98:99], v52 offset:8448\n" \
  "ds_read_b64_tr_b16 a[104:105], v53 offset:8192\n" \
  "ds_read_b64_tr_b16 a[106:107], v53 offset:8448\n" \
  "ds_read_b64_tr_b16 a[112:113], v54 offset:8192\n" \
  "ds_read_b64_tr_b16 a[114:115], v54 offset:8448\n" \
  "ds_read_b64_tr_b16 a[120:121], v55 offset:8192\n" \
  "ds_read_b64_tr_b16 a[122:123], v55 offset:8448\n" \
  "LR1_%=:\n" \
  "v_readfirstlane_b32 s59, v68\n" \
  "v_readfirstlane_b32 s60, v69\n" \
  "v_readfirstlane_b32 s94, v70\n" \
  "v_readfirstlane_b32 s28, v71\n" \
  "s_xor_b32 s59, s59, s90\n" \
  "s_xor_b32 s60, s60, s91\n" \
  "s_xor_b32 s94, s94, s90\n" \
  "s_xor_b32 s28, s28, s91\n" \
  "s_or_b32 s59, s59, s60\n" \
  "s_xor_b32 s28, s28, s94\n" \
  "s_or_b32 s59, s59, s28\n" \
  "s_cbranch_scc1 LR2_%=\n" \
  "LR3_%=:\n" \
  "s_and_b32 s94, s94, 0x7000\n" \
  "s_lshr_b32 s28, s94, 13\n" \
  "s_xor_b32 s49, s49, s94\n" \
  "s_xor_b32 s51, s51, s94\n" \
  "s_xor_b32 s52, s52, s94\n" \
  "s_xor_b32 s93, s93, s94\n" \
  "v_xor_b32 v12, s94, v12\n" \
  "s_lshr_b32 s59, s48, 2\n" \
  "s_xnor_b32 s59, s59, s28\n" \
  "s_and_b32 s59, s59, 1\n" \
  "s_lshl_b32 s59, s59, 13\n" \
  "s_lshl_b32 s55, s48, 19\n" \
  "s_or_b32 s55, s55, s59\n" \
  "s_mov_b32 s60, 0\n" \
  "s_lshl_b32 s50, s60, 19\n" \
  "s_add_u32 s50, s50, " STR(EXP_B) "<<21\n" \
  "s_xor_b32 s50, s50, s93\n" \
  "s_and_b32 s59, s60, 7\n" \
  "s_mul_i32 s59, s59, 0x4800\n" \
  "s_and_b32 s59, s59, 0x7000\n" \
  "s_xor_b32 s50, s50, s59\n" \
  "buffer_store_dwordx4 v[88:91], v15, s[36:39], s50 offen sc1\n" \
  "s_mov_b32 s60, 1\n" \
  "s_lshl_b32 s50, s60, 19\n" \
  "s_add_u32 s50, s50, " STR(EXP_B) "<<21\n" \
  "s_xor_b32 s50, s50, s93\n" \
  "s_and_b32 s59, s60, 7\n" \
  "s_mul_i32 s59, s59, 0x4800\n" \
  "s_and_b32 s59, s59, 0x7000\n" \
  "s_xor_b32 s50, s50, s59\n" \
  "buffer_store_dwordx4 v[88:91], v15, s[36:39], s50 offen sc1\n" \
  "s_mov_b32 s60, 2\n" \
  "s_lshl_b32 s50, s60, 19\n" \
  "s_add_u32 s50, s50, " STR(EXP_B) "<<21\n" \
  "s_xor_b32 s50, s50, s93\n" \
  "s_and_b32 s59, s60, 7\n" \
  "s_mul_i32 s59, s59, 0x4800\n" \
  "s_and_b32 s59, s59, 0x7000\n" \
  "s_xor_b32 s50, s50, s59\n" \
  "buffer_store_dwordx4 v[88:91], v15, s[36:39], s50 offen sc1\n" \
  "s_mov_b32 s60, 3\n" \
  "s_lshl_b32 s50, s60, 19\n" \
  "s_add_u32 s50, s50, " STR(EXP_B) "<<21\n" \
  "s_xor_b32 s50, s50, s93\n" \
  "s_and_b32 s59, s60, 7\n" \
  "s_mul_i32 s59, s59, 0x4800\n" \
  "s_and_b32 s59, s59, 0x7000\n" \
  "s_xor_b32 s50, s50, s59\n" \
  "buffer_store_dwordx4 v[88:91], v15, s[36:39], s50 offen sc1\n" \
  "s_xor_b32 s59, s52, 1052672\n" \
  "buffer_store_dwordx4 v[88:91], v11, s[36:39], s59 offen sc1\n" \
  "buffer_store_dwordx4 v[88:91], v11, s[36:39], s52 offen sc1\n" \
  "s_max_u32 s59, %7, 1\n" \
  "s_lshl_b32 s59, s59, 17\n" \
  "buffer_store_dwordx4 v[88:91], v12, s[36:39], s59 offen sc1\n" \
  "v_min_u32 v64, -2, v64\n" \
  "v_min_u32 v65, -2, v65\n" \
  "v_min_u32 v66, -2, v66\n" \
  "v_min_u32 v67, -2, v67\n" \
  "s_lshl_b32 s59, %7, 29\n" \
  "v_or_b32 v27, s59, v12\n" \
  "buffer_store_dwordx4 v[64:67], v27, s[36:39], 0 offen sc1\n" \
  "s_waitcnt lgkmcnt(0)\n" \
  "s_lshr_b32 s59, %6, 1\n" \
  "s_add_u32 s59, s59, %7\n" \
  "s_add_u32 s59, s59, 3\n" \
  "s_and_b32 s59, s59, 3\n" \
  "s_lshl_b32 s59, s59, 19\n" \
  "s_lshl_b32 s60, %7, 21\n" \
  "s_add_u32 s59, s59, s60\n" \
  "s_add_u32 s59, s59, 0\n" \
  "s_add_u32 m0, s88, 8192\n" \
  "s_nop 0\n" \
  "buffer_load_dwordx4 v32, s[80:83], s59 offen lds\n" \
  "s_add_u32 s59, s59, 65536\n" \
  "s_add_u32 m0, s88, 9216\n" \
  "s_nop 0\n" \
  "buffer_load_dwordx4 v33, s[80:83], s59 offen lds\n" \
  "s_add_u32 s59, s59, 65536\n" \
  "s_add_u32 m0, s88, 10240\n" \
  "s_nop 0\n" \
  "buffer_load_dwordx4 v32, s[80:83], s59 offen lds\n" \
  "s_add_u32 s59, s59, 65536\n" \
  "s_add_u32 m0, s88, 11264\n" \
  "s_nop 0\n" \
  "buffer_load_dwordx4 v33, s[80:83], s59 offen lds\n" \
  "s_add_u32 s59, s59, 65536\n" \
  "s_add_u32 m0, s88, 12288\n" \
  "s_nop 0\n" \
  "buffer_load_dwordx4 v32, s[80:83], s59 offen lds\n" \
  "s_add_u32 s59, s59, 65536\n" \
  "s_add_u32 m0, s88, 13312\n" \
  "s_nop 0\n" \
  "buffer_load_dwordx4 v33, s[80:83], s59 offen lds\n" \
  "s_add_u32 s59, s59, 65536\n" \
  "s_add_u32 m0, s88, 14336\n" \
  "s_nop 0\n" \
  "buffer_load_dwordx4 v32, s[80:83], s59 offen lds\n" \
  "s_add_u32 s59, s59, 65536\n" \
  "s_add_u32 m0, s88, 15360\n" \
  "s_nop 0\n" \
  "buffer_load_dwordx4 v33, s[80:83], s59 offen lds\n" \
  "s_waitcnt vmcnt(40)\n" \
  "ds_read_b64_tr_b16 a[100:101], v52 offset:16384\n" \
  "ds_read_b64_tr_b16 a[102:103], v52 offset:16640\n" \
  "ds_read_b64_tr_b16 a[108:109], v53 offset:16384\n" \
  "ds_read_b64_tr_b16 a[110:111], v53 offset:16640\n" \
  "ds_read_b64_tr_b16 a[116:117], v54 offset:16384\n" \
  "ds_read_b64_tr_b16 a[118:119], v54 offset:16640\n" \
  "ds_read_b64_tr_b16 a[124:125], v55 offset:16384\n" \
  "ds_read_b64_tr_b16 a[126:127], v55 offset:16640\n" \
  "s_waitcnt lgkmcnt(0)\n" \
  "s_lshl_b32 s59, %7, 7\n" \
  "s_add_u32 s59, s59, 0\n" \
  "s_add_u32 m0, s88, 16384\n" \
  "s_nop 0\n" \
  "buffer_load_dwordx4 v34, s[84:87], s59 offen lds\n" \
  "s_add_u32 s59, s59, 16384\n" \
  "s_add_u32 m0, s88, 17408\n" \
  "s_nop 0\n" \
  "buffer_load_dwordx4 v34, s[84:87], s59 offen lds\n" \
  "s_add_u32 s59, s59, 16384\n" \
  "s_add_u32 m0, s88, 18432\n" \
  "s_nop 0\n" \
  "buffer_load_dwordx4 v34, s[84:87], s59 offen lds\n" \
  "s_add_u32 s59, s59, 16384\n" \
  "s_add_u32 m0, s88, 19456\n" \
  "s_nop 0\n" \
  "buffer_load_dwordx4 v34, s[84:87], s59 offen lds\n" \
  "s_add_u32 s59, s59, 16384\n" \
  "s_add_u32 m0, s88, 20480\n" \
  "s_nop 0\n" \
  "buffer_load_dwordx4 v34, s[84:87], s59 offen lds\n" \
  "s_add_u32 s59, s59, 16384\n" \
  "s_add_u32 m0, s88, 21504\n" \
  "s_nop 0\n" \
  "buffer_load_dwordx4 v34, s[84:87], s59 offen lds\n" \
  "s_add_u32 s59, s59, 16384\n" \
  "s_add_u32 m0, s88, 22528\n" \
  "s_nop 0\n" \
  "buffer_load_dwordx4 v34, s[84:87], s59 offen lds\n" \
  "s_add_u32 s59, s59, 16384\n" \
  "s_add_u32 m0, s88, 23552\n" \
  "s_nop 0\n" \
  "buffer_load_dwordx4 v34, s[84:87], s59 offen lds\n" \
  "s_waitcnt vmcnt(40)\n" \
  "ds_read_b64_tr_b16 a[128:129], v42 offset:24576\n" \
  "ds_read_b64_tr_b16 a[130:131], v42 offset:24832\n" \
  "ds_read_b64_tr_b16 a[132:133], v42 offset:28672\n" \
  "ds_read_b64_tr_b16 a[134:135], v42 offset:28928\n" \
  "ds_read_b64_tr_b16 a[136:137], v43 offset:24576\n" \
  "ds_read_b64_tr_b16 a[138:139], v43 offset:24832\n" \
  "ds_read_b64_tr_b16 a[140:141], v43 offset:28672\n" \
  "ds_read_b64_tr_b16 a[142:143], v43 offset:28928\n" \
  "s_waitcnt lgkmcnt(0)\n" \
  "s_lshl_b32 s59, %7, 7\n" \
  "s_add_u32 s59, s59, 131072\n" \
  "s_add_u32 m0, s88, 24576\n" \
  "s_nop 0\n" \
  "buffer_load_dwordx4 v34, s[84:87], s59 offen lds\n" \
  "s_add_u32 s59, s59, 16384\n" \
  "s_add_u32 m0, s88, 25600\n" \
  "s_nop 0\n" \
  "buffer_load_dwordx4 v34, s[84:87], s59 offen lds\n" \
  "s_add_u32 s59, s59, 16384\n" \
  "s_add_u32 m0, s88, 26624\n" \
  "s_nop 0\n" \
  "buffer_load_dwordx4 v34, s[84:87], s59 offen lds\n" \
  "s_add_u32 s59, s59, 16384\n" \
  "s_add_u32 m0, s88, 27648\n" \
  "s_nop 0\n" \
  "buffer_load_dwordx4 v34, s[84:87], s59 offen lds\n" \
  "s_add_u32 s59, s59, 16384\n" \
  "s_add_u32 m0, s88, 28672\n" \
  "s_nop 0\n" \
  "buffer_load_dwordx4 v34, s[84:87], s59 offen lds\n" \
  "s_add_u32 s59, s59, 16384\n" \
  "s_add_u32 m0, s88, 29696\n" \
  "s_nop 0\n" \
  "buffer_load_dwordx4 v34, s[84:87], s59 offen lds\n" \
  "s_add_u32 s59, s59, 16384\n" \
  "s_add_u32 m0, s88, 30720\n" \
  "s_nop 0\n" \
  "buffer_load_dwordx4 v34, s[84:87], s59 offen lds\n" \
  "s_add_u32 s59, s59, 16384\n" \
  "s_add_u32 m0, s88, 31744\n" \
  "s_nop 0\n" \
  "buffer_load_dwordx4 v34, s[84:87], s59 offen lds\n" \
  "s_memtime s[72:73]\n" \
  "s_memrealtime s[74:75]\n" \
  "s_nop 1\n" \
  "s_memrealtime s[76:77]\n" \
  "s_nop 1\n" \
  "s_memrealtime s[78:79]\n" \
  "s_nop 1\n" \
  "s_memrealtime s[80:81]\n" \
  "s_nop 1\n" \
  "s_memrealtime s[82:83]\n" \
  "s_nop 1\n" \
  "s_memrealtime s[84:85]\n" \
  "s_nop 1\n" \
  "s_memrealtime s[86:87]\n" \
  "s_nop 1\n" \
  "s_memrealtime s[88:89]\n" \
  "s_nop 1\n" \
  "s_memrealtime s[90:91]\n" \
  "s_mov_b32 s59, 0\n" \
  "s_waitcnt lgkmcnt(0)\n" \
  "s_cmp_eq_u32 s76, s74\n" \
  "s_addc_u32 s59, s59, 0\n" \
  "s_cmp_eq_u32 s78, s74\n" \
  "s_addc_u32 s59, s59, 0\n" \
  "s_cmp_eq_u32 s80, s74\n" \
  "s_addc_u32 s59, s59, 0\n" \
  "s_cmp_eq_u32 s82, s74\n" \
  "s_addc_u32 s59, s59, 0\n" \
  "s_cmp_eq_u32 s84, s74\n" \
  "s_addc_u32 s59, s59, 0\n" \
  "s_cmp_eq_u32 s86, s74\n" \
  "s_addc_u32 s59, s59, 0\n" \
  "s_cmp_eq_u32 s88, s74\n" \
  "s_addc_u32 s59, s59, 0\n" \
  "s_cmp_eq_u32 s90, s74\n" \
  "s_addc_u32 s59, s59, 0\n" \
  "s_mul_i32 s59, s59, 12\n" \
  "s_add_u32 s68, s72, s59\n" \
  "s_add_u32 s59, s74, 4\n" \
  "s_mul_i32 s69, s59, 24\n" \
  "s_mov_b32 s62, s69\n" \
  "s_mov_b32 s56, s68\n" \
  "s_waitcnt vmcnt(40)\n" \
  "ds_read_b64_tr_b16 a[144:145], v42 offset:32768\n" \
  "ds_read_b64_tr_b16 a[146:147], v42 offset:33024\n" \
  "ds_read_b64_tr_b16 a[148:149], v42 offset:36864\n" \
  "ds_read_b64_tr_b16 a[150:151], v42 offset:37120\n" \
  "ds_read_b64_tr_b16 a[152:153], v43 offset:32768\n" \
  "ds_read_b64_tr_b16 a[154:155], v43 offset:33024\n" \
  "ds_read_b64_tr_b16 a[156:157], v43 offset:36864\n" \
  "ds_read_b64_tr_b16 a[158:159], v43 offset:37120\n" \
  "s_waitcnt vmcnt(32)\n" \
  "ds_read_b64_tr_b16 a[160:161], v42 offset:0\n" \
  "ds_read_b64_tr_b16 a[162:163], v42 offset:256\n" \
  "ds_read_b64_tr_b16 a[164:165], v42 offset:4096\n" \
  "ds_read_b64_tr_b16 a[166:167], v42 offset:4352\n" \
  "ds_read_b64_tr_b16 a[168:169], v43 offset:0\n" \
  "ds_read_b64_tr_b16 a[170:171], v43 offset:256\n" \
  "ds_read_b64_tr_b16 a[172:173], v43 offset:4096\n" \
  "ds_read_b64_tr_b16 a[174:175], v43 offset:4352\n" \
  "s_waitcnt vmcnt(16)\n" \
  "ds_read_b64_tr_b16 a[176:177], v42 offset:8192\n" \
  "ds_read_b64_tr_b16 a[178:179], v42 offset:8448\n" \
  "ds_read_b64_tr_b16 a[180:181], v42 offset:12288\n" \
  "ds_read_b64_tr_b16 a[182:183], v42 offset:12544\n" \
  "ds_read_b64_tr_b16 a[184:185], v43 offset:8192\n" \
  "ds_read_b64_tr_b16 a[186:187], v43 offset:8448\n" \
  "ds_read_b64_tr_b16 a[188:189], v43 offset:12288\n" \
  "ds_read_b64_tr_b16 a[190:191], v43 offset:12544\n" \
  "s_barrier\n" \
  "v_mov_b32 v27, " STR(CW_INIT) "*4\n" \
  "v_mov_b32 v28, 1\n" \
  "v_or_b32 v25, %7, v24\n" \
  "v_lshl_add_u32 v27, v25, 24, v27\n" \
  "buffer_atomic_add v28, v27, s[44:47], 0 offen\n" \
  "s_waitcnt vmcnt(9)\n" \
  "ds_read_b64_tr_b16 a[192:193], v49 offset:16384\n" \
  "ds_read_b64_tr_b16 a[194:195], v49 offset:16640\n" \
  "ds_read_b64_tr_b16 a[196:197], v49 offset:20480\n" \
  "ds_read_b64_tr_b16 a[198:199], v49 offset:20736\n" \
  "ds_read_b64_tr_b16 a[208:209], v49 offset:16400\n" \
  "ds_read_b64_tr_b16 a[210:211], v49 offset:16656\n" \
  "ds_read_b64_tr_b16 a[212:213], v49 offset:20496\n" \
  "ds_read_b64_tr_b16 a[214:215], v49 offset:20752\n" \
  "ds_read_b64_tr_b16 a[224:225], v49 offset:16392\n" \
  "ds_read_b64_tr_b16 a[226:227], v49 offset:16648\n" \
  "ds_read_b64_tr_b16 a[228:229], v49 offset:20488\n" \
  "ds_read_b64_tr_b16 a[230:231], v49 offset:20744\n" \
  "ds_read_b64_tr_b16 a[240:241], v49 offset:16408\n" \
  "ds_read_b64_tr_b16 a[242:243], v49 offset:16664\n" \
  "ds_read_b64_tr_b16 a[244:245], v49 offset:20504\n" \
  "ds_read_b64_tr_b16 a[246:247], v49 offset:20760\n" \
  "s_waitcnt vmcnt(1)\n" \
  "ds_read_b64_tr_b16 a[200:201], v49 offset:24576\n" \
  "ds_read_b64_tr_b16 a[202:203], v49 offset:24832\n" \
  "ds_read_b64_tr_b16 a[204:205], v49 offset:28672\n" \
  "ds_read_b64_tr_b16 a[206:207], v49 offset:28928\n" \
  "ds_read_b64_tr_b16 a[216:217], v49 offset:24592\n" \
  "ds_read_b64_tr_b16 a[218:219], v49 offset:24848\n" \
  "ds_read_b64_tr_b16 a[220:221], v49 offset:28688\n" \
  "ds_read_b64_tr_b16 a[222:223], v49 offset:28944\n" \
  "ds_read_b64_tr_b16 a[232:233], v49 offset:24584\n" \
  "ds_read_b64_tr_b16 a[234:235], v49 offset:24840\n" \
  "ds_read_b64_tr_b16 a[236:237], v49 offset:28680\n" \
  "ds_read_b64_tr_b16 a[238:239], v49 offset:28936\n" \
  "ds_read_b64_tr_b16 a[248:249], v49 offset:24600\n" \
  "ds_read_b64_tr_b16 a[250:251], v49 offset:24856\n" \
  "ds_read_b64_tr_b16 a[252:253], v49 offset:28696\n" \
  "ds_read_b64_tr_b16 a[254:255], v49 offset:28952\n" \
  "s_waitcnt lgkmcnt(0)\n" \
  "s_barrier\n" \
  "LI2a_%=:\n" \
  "buffer_load_dword v23, v22, s[44:47], 0 offen sc1\n" \
  "s_waitcnt vmcnt(0)\n" \
  "v_cmp_gt_u32 vcc, 1, v23\n" \
  "s_cbranch_vccz LI2b_%=\n" \
  "s_sleep 8\n" \
  "s_add_u32 s58, s58, 1\n" \
  "s_cmp_lt_u32 s58, " STR(SPIN_CAP) "\n" \
  "s_cbranch_scc1 LI2a_%=\n" \
  "v_mov_b32 v27, " STR(CW_DEAD) "*4\n" \
  "v_mov_b32 v28, 1\n" \
  "buffer_store_dword v28, v27, s[44:47], 0 offen sc1\n" \
  "s_branch LI2b_%=\n" \
  "LI2b_%=:\n" \
  "buffer_load_dwordx4 v[200:203], v247, s[40:43], s55 offen offset:0 sc1\n" \
  "buffer_load_dwordx4 v[204:207], v247, s[40:43], s55 offen offset:1024 sc1\n" \
  "buffer_load_dwordx4 v[208:211], v247, s[40:43], s55 offen offset:2048 sc1\n" \
  "buffer_load_dwordx4 v[212:215], v247, s[40:43], s55 offen offset:3072 sc1\n" \
  "s_cmp_lg_u32 %7, 2\n" \
  "s_cbranch_scc1 LI6_%=\n" \
  "v_mov_b32 v25, " STR(CW_INIT) "*4\n" \
  "LI1a_%=:\n" \
  "buffer_load_dword v23, v25, s[44:47], 0 offen sc1\n" \
  "s_waitcnt vmcnt(0)\n" \
  "v_cmp_gt_u32 vcc, 128, v23\n" \
  "s_cbranch_vccz LI1b_%=\n" \
  "s_sleep 12\n" \
  "s_add_u32 s58, s58, 1\n" \
  "s_cmp_lt_u32 s58, " STR(SPIN_CAP) "\n" \
  "s_cbranch_scc1 LI1a_%=\n" \
  "v_mov_b32 v27, " STR(CW_DEAD) "*4\n" \
  "v_mov_b32 v28, 1\n" \
  "buffer_store_dword v28, v27, s[44:47], 0 offen sc1\n" \
  "s_branch LI1b_%=\n" \
  "LI1b_%=:\n" \
  "LI6_%=:\n" \
  "s_memtime s[72:73]\n" \
  "s_memrealtime s[74:75]\n" \
  "s_nop 1\n" \
  "s_memrealtime s[76:77]\n" \
  "s_nop 1\n" \
  "s_memrealtime s[78:79]\n" \
  "s_nop 1\n" \
  "s_memrealtime s[80:81]\n" \
  "s_nop 1\n" \
  "s_memrealtime s[82:83]\n" \
  "s_nop 1\n" \
  "s_memrealtime s[84:85]\n" \
  "s_nop 1\n" \
  "s_memrealtime s[86:87]\n" \
  "s_nop 1\n" \
  "s_memrealtime s[88:89]\n" \
  "s_nop 1\n" \
  "s_memrealtime s[90:91]\n" \
  "s_mov_b32 s59, 0\n" \
  "s_waitcnt lgkmcnt(0)\n" \
  "s_cmp_eq_u32 s76, s74\n" \
  "s_addc_u32 s59, s59, 0\n" \
  "s_cmp_eq_u32 s78, s74\n" \
  "s_addc_u32 s59, s59, 0\n" \
  "s_cmp_eq_u32 s80, s74\n" \
  "s_addc_u32 s59, s59, 0\n" \
  "s_cmp_eq_u32 s82, s74\n" \
  "s_addc_u32 s59, s59, 0\n" \
  "s_cmp_eq_u32 s84, s74\n" \
  "s_addc_u32 s59, s59, 0\n" \
  "s_cmp_eq_u32 s86, s74\n" \
  "s_addc_u32 s59, s59, 0\n" \
  "s_cmp_eq_u32 s88, s74\n" \
  "s_addc_u32 s59, s59, 0\n" \
  "s_cmp_eq_u32 s90, s74\n" \
  "s_addc_u32 s59, s59, 0\n" \
  "s_mul_i32 s59, s59, 12\n" \
  "s_add_u32 s68, s72, s59\n" \
  "s_add_u32 s59, s74, 4\n" \
  "s_mul_i32 s69, s59, 24\n" \
  "v_mov_b32 v95, " STR(RI0) "\n" \
  "s_sub_u32 s60, s69, s62\n" \
  "s_sub_u32 s59, s68, s56\n" \
  "s_mov_b32 s62, s69\n" \
  "s_mov_b32 s56, s68\n" \
  "v_cvt_f32_u32 v27, s59\n" \
  "v_cvt_f32_u32 v28, s60\n" \
  "v_rcp_f32 v26, v27\n" \
  "v_rcp_f32 v19, v28\n" \
  "v_mul_f32 v26, v26, v28\n" \
  "v_mul_f32 v95, " STR(DECAY) ", v95\n" \
  "v_mul_f32 v19, v19, v27\n" \
  "v_max_f32 v95, v95, v26\n" \
  "v_cvt_f32_u32 v28, " STR(F_P) "\n" \
  "v_mul_f32 v27, v95, v28\n" \
  "v_cvt_u32_f32 v27, v27\n" \
  "v_min_u32 v27, 0x7fff, v27\n" \
  "s_nop 0\n" \
  "v_readfirstlane_b32 s63, v27\n" \
  "s_add_u32 s59, s74, 4+63\n" \
  "s_and_b32 s59, s59, -64\n" \
  "s_mul_i32 s59, s59, 24\n" \
  "s_sub_u32 s59, s59, s69\n" \
  "v_cvt_f32_u32 v27, s59\n" \
  "v_mul_f32 v28, 0x44c00000, v19\n" \
  "v_mul_f32 v27, v27, v19\n" \
  "v_cvt_u32_f32 v28, v28\n" \
  "v_cvt_u32_f32 v27, v27\n" \
  "v_max_u32 v28, " STR(T_Q) ", v28\n" \
  "v_min_u32 v28, 2047, v28\n" \
  "v_readfirstlane_b32 s59, v27\n" \
  "v_readfirstlane_b32 s95, v28\n" \
  "v_subrev_u32 v28, " STR(T_Q) ", v28\n" \
  "v_lshlrev_b32 v28, 2, v28\n" \
  "v_mov_b32 v27, 0\n" \
  "s_add_u32 s59, s59, s68\n" \
  "s_cmp_lg_u32 %7, 1\n" \
  "s_cbranch_scc1 LI8_%=\n" \
  "ds_write_b32 v255, v28 offset:14344\n" \
  "ds_write_b32 v255, v27 offset:14356\n" \
  "v_mov_b32 v92, s59\n" \
  "v_mov_b32 v93, 0\n" \
  "ds_write_b64 v255, v[92:93] offset:14336\n" \
  "LI8_%=:\n" \
  "s_waitcnt lgkmcnt(0)\n" \
  "s_lshl_b32 s59, s70, 7\n" \
  "s_add_u32 s59, s59, " STR(CW_PROG) "*4\n" \
  "s_lshl_b32 s60, s71, 2\n" \
  "s_or_b32 s60, s60, %7\n" \
  "s_cmp_eq_u32 s60, 3\n" \
  "s_cselect_b32 s59, s59, 0x80000000\n" \
  "v_lshl_add_u32 v24, v24, 24, s59\n" \
  "v_mov_b64 v[48:49], 0\n" \
  "v_mov_b64 v[50:51], 0\n" \
  "v_mov_b64 v[52:53], 0\n" \
  "v_mov_b64 v[54:55], 0\n" \
  "v_mov_b64 v[56:57], 0\n" \
  "v_mov_b64 v[58:59], 0\n" \
  "v_mov_b64 v[60:61], 0\n" \
  "v_mov_b64 v[62:63], 0\n" \
  "v_mov_b64 v[64:65], 0\n" \
  "v_mov_b64 v[66:67], 0\n" \
  "v_mov_b64 v[68:69], 0\n" \
  "v_mov_b64 v[70:71], 0\n" \
  "s_waitcnt vmcnt(0)\n" \
  "ds_write_b128 v18, v[200:203] offset:0\n" \
  "ds_write_b128 v18, v[204:207] offset:1024\n" \
  "ds_write_b128 v18, v[208:211] offset:2048\n" \
  "ds_write_b128 v18, v[212:215] offset:3072\n" \
  "ds_write_b32 v255, v255 offset:14360\n" \
  "s_waitcnt lgkmcnt(0)\n" \
  "s_barrier\n" \
  "s_cmp_eq_u32 %7, 0\n" \
  "s_cbranch_scc1 LL_%=\n" \
  "ds_read_b64_tr_b16 v[136:137], v20 offset:0\n" \
  "ds_read_b64_tr_b16 v[138:139], v21 offset:0\n" \
  "ds_read_b64_tr_b16 v[152:153], v20 offset:8\n" \
  "ds_read_b64_tr_b16 v[154:155], v21 offset:8\n" \
  "ds_read_b64_tr_b16 v[140:141], v20 offset:2048\n" \
  "ds_read_b64_tr_b16 v[142:143], v21 offset:2048\n" \
  "ds_read_b64_tr_b16 v[156:157], v20 offset:2056\n" \
  "ds_read_b64_tr_b16 v[158:159], v21 offset:2056\n" \
  "ds_read_b64_tr_b16 v[144:145], v20 offset:4096\n" \
  "ds_read_b64_tr_b16 v[146:147], v21 offset:4096\n" \
  "ds_read_b64_tr_b16 v[160:161], v20 offset:4104\n" \
  "ds_read_b64_tr_b16 v[162:163], v21 offset:4104\n" \
  "ds_read_b64_tr_b16 v[148:149], v20 offset:6144\n" \
  "ds_read_b64_tr_b16 v[150:151], v21 offset:6144\n" \
  "ds_read_b64_tr_b16 v[164:165], v20 offset:6152\n" \
  "ds_read_b64_tr_b16 v[166:167], v21 offset:6152\n" \
  "ds_read_b128 v[104:107], v84 offset:0\n" \
  "ds_read_b128 v[108:111], v85 offset:0\n" \
  "ds_read_b128 v[112:115], v86 offset:0\n" \
  "ds_read_b128 v[116:119], v87 offset:0\n" \
  "ds_read_b128 v[120:123], v84 offset:4096\n" \
  "ds_read_b128 v[124:127], v85 offset:4096\n" \
  "ds_read_b128 v[128:131], v86 offset:4096\n" \
  "ds_read_b128 v[132:135], v87 offset:4096\n" \
  "s_waitcnt lgkmcnt(0)\n" \
  "s_branch LM_%=\n" \
  ".p2align 6\n" \
  "s_nop 1\n" \
  "s_nop 1\n" \
  "s_nop 1\n" \
  "s_nop 1\n" \
  "s_nop 1\n" \
  "s_nop 1\n" \
  "s_nop 1\n" \
  "s_nop 1\n" \
  "s_nop 1\n" \
  "s_nop 1\n" \
  "s_nop 1\n" \
  "s_nop 1\n" \
  "s_nop 1\n" \
  "s_nop 1\n" \
  "s_nop 1\n" \
  "s_nop 1\n" \
  "s_nop 1\n" \
  "s_nop 1\n" \
  "s_nop 1\n" \
  "s_nop 1\n" \
  "s_nop 1\n" \
  "s_nop 1\n" \
  "s_nop 1\n" \
  "s_nop 1\n" \
  "s_nop 1\n" \
  "s_nop 1\n" \
  "s_nop 1\n" \
  "s_nop 1\n" \
  "s_nop 1\n" \
  "s_nop 1\n" \
  "s_nop 1\n" \
  "s_nop 1\n" \
  "s_nop 1\n" \
  "s_nop 1\n" \
  "s_nop 1\n" \
  "s_nop 1\n" \
  "s_nop 1\n" \
  "s_nop 1\n" \
  "s_nop 1\n" \
  "s_nop 1\n" \
  "s_nop 1\n" \
  "s_nop 1\n" \
  "s_nop 1\n" \
  "s_nop 1\n" \
  "s_nop 1\n" \
  "s_nop 1\n" \
  "s_nop 1\n" \
  "s_nop 1\n" \
  "s_nop 1\n" \
  "s_nop 1\n" \
  "s_nop 1\n" \
  "s_nop 1\n" \
  "s_nop 1\n" \
  "s_nop 1\n" \
  "s_nop 1\n" \
  "s_nop 1\n" \
  "s_nop 1\n" \
  "s_nop 1\n" \
  "s_nop 1\n" \
  "s_nop 1\n" \
  "s_nop 1\n" \
  "s_nop 1\n" \
  "s_nop 1\n" \
  "s_nop 1\n" \
  "s_nop 1\n" \
  "s_nop 1\n" \
  "s_nop 1\n" \
  "s_nop 1\n" \
  "s_nop 1\n" \
  "s_nop 1\n" \
  "s_nop 1\n" \
  "s_nop 1\n" \
  "s_nop 1\n" \
  "s_nop 1\n" \
  "s_nop 1\n" \
  "s_nop 1\n" \
  "s_nop 1\n" \
  "s_nop 1\n" \
  "s_nop 1\n" \
  "s_nop 1\n" \
  "s_nop 1\n" \
  "s_nop 1\n" \
  "s_nop 1\n" \
  "s_nop 1\n" \
  "s_nop 1\n" \
  "s_nop 1\n" \
  "s_nop 1\n" \
  "s_nop 1\n" \
  "s_nop 1\n" \
  "s_nop 1\n" \
  "s_nop 1\n" \
  "s_nop 1\n" \
  "s_nop 1\n" \
  "s_nop 1\n" \
  "s_nop 1\n" \
  "s_nop 1\n" \
  "LB_a_%=:\n" \
  "LL_%=:\n" \
  "s_barrier\n" \
  "buffer_load_dwordx4 v[32:35], v80, s[36:39], s49 offen sc1\n" \
  "ds_read_b64_tr_b16 v[136:137], v20 offset:0\n" \
  "ds_read_b64_tr_b16 v[138:139], v21 offset:0\n" \
  "ds_read_b64_tr_b16 v[152:153], v20 offset:8\n" \
  "ds_read_b64_tr_b16 v[154:155], v21 offset:8\n" \
  "ds_read_b64_tr_b16 v[140:141], v20 offset:2048\n" \
  "ds_read_b64_tr_b16 v[142:143], v21 offset:2048\n" \
  "ds_read_b64_tr_b16 v[156:157], v20 offset:2056\n" \
  "buffer_load_dwordx4 v[36:39], v81, s[36:39], s49 offen sc1\n" \
  "ds_read_b64_tr_b16 v[158:159], v21 offset:2056\n" \
  "ds_read_b64_tr_b16 v[144:145], v20 offset:4096\n" \
  "ds_read_b64_tr_b16 v[146:147], v21 offset:4096\n" \
  "ds_read_b64_tr_b16 v[160:161], v20 offset:4104\n" \
  "ds_read_b64_tr_b16 v[162:163], v21 offset:4104\n" \
  "ds_read_b64_tr_b16 v[148:149], v20 offset:6144\n" \
  "ds_read_b64_tr_b16 v[150:151], v21 offset:6144\n" \
  "buffer_load_dwordx4 v[40:43], v82, s[36:39], s49 offen sc1\n" \
  "ds_read_b64_tr_b16 v[164:165], v20 offset:6152\n" \
  "ds_read_b64_tr_b16 v[166:167], v21 offset:6152\n" \
  "s_waitcnt lgkmcnt(12)\n" \
  "v_mfma_f32_16x16x32_bf16 v[184:187], v[136:139], a[192:195], 0\n" \
  "ds_read_b128 v[104:107], v84 offset:0\n" \
  "s_xor_b32 s51, s51, 1052672\n" \
  "s_add_u32 s53, s53, 1<<17\n" \
  "v_mfma_f32_16x16x32_bf16 v[188:191], v[152:155], a[192:195], 0\n" \
  "ds_read_b128 v[108:111], v85 offset:0\n" \
  "s_and_b32 s53, s53, 0x60000\n" \
  "s_lshl_b32 s50, s48, 19\n" \
  "v_mfma_f32_16x16x32_bf16 v[192:195], v[136:139], a[208:211], 0\n" \
  "ds_read_b128 v[112:115], v86 offset:0\n" \
  "s_add_u32 s50, s50, " STR(EXP_B) "<<21\n" \
  "s_xor_b32 s50, s50, s93\n" \
  "buffer_load_dwordx4 v[44:47], v83, s[36:39], s49 offen sc1\n" \
  "v_mfma_f32_16x16x32_bf16 v[196:199], v[152:155], a[208:211], 0\n" \
  "ds_read_b128 v[116:119], v87 offset:0\n" \
  "s_and_b32 s59, s48, 7\n" \
  "s_mul_i32 s59, s59, 0x4800\n" \
  "s_waitcnt lgkmcnt(12)\n" \
  "v_mfma_f32_16x16x32_bf16 v[184:187], v[140:143], a[196:199], v[184:187]\n" \
  "ds_read_b128 v[120:123], v84 offset:4096\n" \
  "s_and_b32 s59, s59, 0x7000\n" \
  "s_xor_b32 s50, s50, s59\n" \
  "v_mfma_f32_16x16x32_bf16 v[188:191], v[156:159], a[196:199], v[188:191]\n" \
  "ds_read_b128 v[124:127], v85 offset:4096\n" \
  "s_add_u32 s60, s48, 4\n" \
  "s_lshl_b32 s54, s60, 19\n" \
  "v_mfma_f32_16x16x32_bf16 v[192:195], v[140:143], a[212:215], v[192:195]\n" \
  "ds_read_b128 v[128:131], v86 offset:4096\n" \
  "s_add_u32 s54, s54, " STR(EXP_B) "<<21\n" \
  "s_xor_b32 s54, s54, s93\n" \
  "v_mfma_f32_16x16x32_bf16 v[196:199], v[156:159], a[212:215], v[196:199]\n" \
  "ds_read_b128 v[132:135], v87 offset:4096\n" \
  "s_and_b32 s59, s60, 7\n" \
  "s_mul_i32 s59, s59, 0x4800\n" \
  "s_waitcnt lgkmcnt(12)\n" \
  "v_mfma_f32_16x16x32_bf16 v[184:187], v[144:147], a[200:203], v[184:187]\n" \
  "s_and_b32 s59, s59, 0x7000\n" \
  "s_xor_b32 s54, s54, s59\n" \
  "v_mfma_f32_16x16x32_bf16 v[188:191], v[160:163], a[200:203], v[188:191]\n" \
  "s_xor_b32 s59, %7, 3\n" \
  "s_lshl_b32 s59, s59, 29\n" \
  "v_mfma_f32_16x16x32_bf16 v[192:195], v[144:147], a[216:219], v[192:195]\n" \
  "s_xor_b32 s60, s53, 0x40000\n" \
  "s_or_b32 s59, s59, s60\n" \
  "v_mfma_f32_16x16x32_bf16 v[196:199], v[160:163], a[216:219], v[196:199]\n" \
  "s_waitcnt lgkmcnt(8)\n" \
  "v_mfma_f32_16x16x32_bf16 v[184:187], v[148:151], a[204:207], v[184:187]\n" \
  "v_mfma_f32_16x16x32_bf16 v[188:191], v[164:167], a[204:207], v[188:191]\n" \
  "v_mfma_f32_16x16x32_bf16 v[192:195], v[148:151], a[220:223], v[192:195]\n" \
  "v_mfma_f32_16x16x32_bf16 v[196:199], v[164:167], a[220:223], v[196:199]\n" \
  "v_add_u32 v26, s59, v12\n" \
  "LS1_%=:\n" \
  "s_waitcnt vmcnt(3)\n" \
  "v_smfmac_f32_16x16x64_bf16 v[52:55], v[32:35], a[8:15], v2\n" \
  "buffer_store_dwordx2 v[236:237], v14, s[36:39], s50 offen sc1\n" \
  "v_smfmac_f32_16x16x64_bf16 v[56:59], v[32:35], a[16:23], v2\n" \
  "v_cvt_pk_bf16_f32 v168, v184, v185\n" \
  "v_smfmac_f32_16x16x64_bf16 v[60:63], v[32:35], a[24:31], v2\n" \
  "buffer_store_dwordx4 v[88:91], v11, s[36:39], s52 offen sc1\n" \
  "v_smfmac_f32_16x16x64_bf16 v[48:51], v[32:35], a[0:7], v2\n" \
  "v_cvt_pk_bf16_f32 v169, v186, v187\n" \
  "s_waitcnt vmcnt(4)\n" \
  "v_smfmac_f32_16x16x64_bf16 v[52:55], v[36:39], a[40:47], v2\n" \
  "buffer_store_dwordx4 v[88:91], v15, s[36:39], s54 offen sc1\n" \
  "v_smfmac_f32_16x16x64_bf16 v[56:59], v[36:39], a[48:55], v2\n" \
  "v_cvt_pk_bf16_f32 v170, v188, v189\n" \
  "v_smfmac_f32_16x16x64_bf16 v[60:63], v[36:39], a[56:63], v2\n" \
  "buffer_load_dword v23, v22, s[44:47], 0 offen sc1\n" \
  "v_smfmac_f32_16x16x64_bf16 v[48:51], v[36:39], a[32:39], v2\n" \
  "v_cvt_pk_bf16_f32 v171, v190, v191\n" \
  "s_waitcnt vmcnt(5)\n" \
  "v_smfmac_f32_16x16x64_bf16 v[52:55], v[40:43], a[72:79], v2\n" \
  "v_cvt_pk_bf16_f32 v172, v192, v193\n" \
  "v_smfmac_f32_16x16x64_bf16 v[56:59], v[40:43], a[80:87], v2\n" \
  "buffer_store_dwordx4 v[88:91], v26, s[36:39], 0 offen sc1\n" \
  "v_smfmac_f32_16x16x64_bf16 v[60:63], v[40:43], a[88:95], v2\n" \
  "v_cvt_pk_bf16_f32 v173, v194, v195\n" \
  "v_smfmac_f32_16x16x64_bf16 v[48:51], v[40:43], a[64:71], v2\n" \
  "v_cvt_pk_bf16_f32 v174, v196, v197\n" \
  "s_waitcnt vmcnt(5)\n" \
  "v_smfmac_f32_16x16x64_bf16 v[52:55], v[44:47], a[104:111], v2\n" \
  "v_cvt_pk_bf16_f32 v175, v198, v199\n" \
  "v_smfmac_f32_16x16x64_bf16 v[56:59], v[44:47], a[112:119], v2\n" \
  "v_smfmac_f32_16x16x64_bf16 v[60:63], v[44:47], a[120:127], v2\n" \
  "v_add_f32 v52, v52, v54\n" \
  "v_add_f32 v53, v53, v55\n" \
  "v_smfmac_f32_16x16x64_bf16 v[48:51], v[44:47], a[96:103], v2\n" \
  "v_add_f32 v56, v56, v58\n" \
  "v_add_f32 v57, v57, v59\n" \
  "ds_write_b64 v3, v[52:53] offset:0\n" \
  "v_add_f32 v60, v60, v62\n" \
  "v_add_f32 v61, v61, v63\n" \
  "v_smfmac_f32_16x16x64_bf16 v[64:67], v[32:35], a[128:135], v2\n" \
  "ds_write_b64 v244, v[56:57] offset:512\n" \
  "v_smfmac_f32_16x16x64_bf16 v[68:71], v[32:35], a[136:143], v2\n" \
  "ds_write_b64 v245, v[60:61] offset:1024\n" \
  "v_cmp_u_f32 vcc, v52, v53\n" \
  "v_smfmac_f32_16x16x64_bf16 v[64:67], v[36:39], a[144:151], v2\n" \
  "v_add_f32 v48, v48, v50\n" \
  "v_add_f32 v49, v49, v51\n" \
  "v_smfmac_f32_16x16x64_bf16 v[68:71], v[36:39], a[152:159], v2\n" \
  "v_smfmac_f32_16x16x64_bf16 v[64:67], v[40:43], a[160:167], v2\n" \
  "s_cbranch_vccnz LA_%=\n" \
  "LA4_%=:\n" \
  "s_waitcnt lgkmcnt(0)\n" \
  "s_barrier\n" \
  "ds_read_b64 v[72:73], v6 offset:0\n" \
  "ds_read_b64 v[74:75], v6 offset:512\n" \
  "ds_read_b64 v[76:77], v6 offset:1024\n" \
  "ds_read_b96 v[92:94], v255 offset:14336\n" \
  "v_smfmac_f32_16x16x64_bf16 v[68:71], v[40:43], a[168:175], v2\n" \
  "v_smfmac_f32_16x16x64_bf16 v[64:67], v[44:47], a[176:183], v2\n" \
  "s_waitcnt lgkmcnt(3)\n" \
  "v_add_f32 v48, v48, v72\n" \
  "v_add_f32 v49, v49, v73\n" \
  "s_waitcnt lgkmcnt(1)\n" \
  "v_add_f32 v74, v74, v76\n" \
  "v_add_f32 v75, v75, v77\n" \
  "v_add_f32 v48, v48, v74\n" \
  "v_add_f32 v49, v49, v75\n" \
  "v_cvt_pk_bf16_f32 v27, v48, v49\n" \
  "v_smfmac_f32_16x16x64_bf16 v[68:71], v[44:47], a[184:191], v2\n" \
  "v_mov_b32_dpp v28, v27 quad_perm:[1,0,3,2] row_mask:0xf bank_mask:0xf\n" \
  "v_perm_b32 v27, v28, v27, v246\n" \
  "buffer_store_dword v27, v8, s[36:39], s50 offen sc1\n" \
  "v_add_f32 v64, v64, v66\n" \
  "v_add_f32 v65, v65, v67\n" \
  "s_nop 2\n" \
  "v_add_f32 v68, v68, v70\n" \
  "v_add_f32 v69, v69, v71\n" \
  "ds_write2_b32 v5, v64, v65 offset0:0 offset1:40\n" \
  "ds_write2_b32 v5, v68, v69 offset0:16 offset1:56\n" \
  "v_mov_b64 v[184:185], 0\n" \
  "s_andn2_b32 s49, s49, 0x60000\n" \
  "s_or_b32 s49, s49, s53\n" \
  "s_xor_b32 s52, s52, 1052672\n" \
  "v_mov_b64 v[186:187], 0\n" \
  "s_getpc_b64 s[82:83]\n" \
  "LG_b_%=:\n" \
  "s_add_u32 s82, s82, LB_b_%=-LG_b_%=\n" \
  "s_addc_u32 s83, s83, 0\n" \
  "v_mov_b64 v[188:189], 0\n" \
  "s_waitcnt lgkmcnt(0)\n" \
  "s_barrier\n" \
  "s_memtime s[66:67]\n" \
  "ds_read_b64 v[72:73], v7 offset:0\n" \
  "ds_read_b64 v[74:75], v7 offset:1280\n" \
  "ds_read_b64 v[76:77], v7 offset:2560\n" \
  "ds_read_b64 v[78:79], v7 offset:3840\n" \
  "v_readfirstlane_b32 s64, v92\n" \
  "v_readfirstlane_b32 s59, v93\n" \
  "v_readfirstlane_b32 s60, v94\n" \
  "v_mov_b64 v[190:191], 0\n" \
  "s_add_u32 s64, s64, s59\n" \
  "s_add_u32 s61, s64, " STR(F_B) "-" STR(T_Q) "\n" \
  "s_add_u32 s61, s61, s95\n" \
  "s_cmp_ge_u32 s48, 63\n" \
  "s_cbranch_scc1 LD_%=\n" \
  "s_waitcnt lgkmcnt(0)\n" \
  "s_sub_i32 s61, s61, s66\n" \
  "v_pk_add_f32 v[72:73], v[72:73], v[74:75]\n" \
  "v_pk_add_f32 v[76:77], v[76:77], v[78:79]\n" \
  "v_pk_add_f32 v[72:73], v[72:73], v[76:77]\n" \
  "v_cvt_pk_bf16_f32 v27, v72, v73\n" \
  "buffer_store_dword v27, v9, s[36:39], s50 offen sc1\n" \
  "s_add_i32 s59, s61, " STR(T_SNAP) "\n" \
  "s_cmp_gt_u32 s59, 768+" STR(T_SNAP) "\n" \
  "s_cbranch_scc1 LW_b_%=\n" \
  "LV_b_%=:\n" \
  "s_max_i32 s61, s61, 0\n" \
  "s_lshr_b32 s61, s61, 3\n" \
  "s_lshl_b32 s61, s61, 2\n" \
  "s_sub_u32 s82, s82, s61\n" \
  "s_subb_u32 s83, s83, 0\n" \
  "s_setpc_b64 s[82:83]\n" \
  "s_nop 1\n" \
  "s_nop 1\n" \
  "s_nop 1\n" \
  "s_nop 1\n" \
  "s_nop 1\n" \
  "s_nop 1\n" \
  "s_nop 1\n" \
  "s_nop 1\n" \
  "s_nop 1\n" \
  "s_nop 1\n" \
  "s_nop 1\n" \
  "s_nop 1\n" \
  "s_nop 1\n" \
  "s_nop 1\n" \
  "s_nop 1\n" \
  "s_nop 1\n" \
  "s_nop 1\n" \
  "s_nop 1\n" \
  "s_nop 1\n" \
  "s_nop 1\n" \
  "s_nop 1\n" \
  "s_nop 1\n" \
  "s_nop 1\n" \
  "s_nop 1\n" \
  "s_nop 1\n" \
  "s_nop 1\n" \
  "s_nop 1\n" \
  "s_nop 1\n" \
  "s_nop 1\n" \
  "s_nop 1\n" \
  "s_nop 1\n" \
  "s_nop 1\n" \
  "s_nop 1\n" \
  "s_nop 1\n" \
  "s_nop 1\n" \
  "s_nop 1\n" \
  "s_nop 1\n" \
  "s_nop 1\n" \
  "s_nop 1\n" \
  "s_nop 1\n" \
  "s_nop 1\n" \
  "s_nop 1\n" \
  "s_nop 1\n" \
  "s_nop 1\n" \
  "s_nop 1\n" \
  "s_nop 1\n" \
  "s_nop 1\n" \
  "s_nop 1\n" \
  "s_nop 1\n" \
  "s_nop 1\n" \
  "s_nop 1\n" \
  "s_nop 1\n" \
  "s_nop 1\n" \
  "s_nop 1\n" \
  "s_nop 1\n" \
  "s_nop 1\n" \
  "s_nop 1\n" \
  "s_nop 1\n" \
  "s_nop 1\n" \
  "s_nop 1\n" \
  "s_nop 1\n" \
  "s_nop 1\n" \
  "s_nop 1\n" \
  "s_nop 1\n" \
  "s_nop 1\n" \
  "s_nop 1\n" \
  "s_nop 1\n" \
  "s_nop 1\n" \
  "s_nop 1\n" \
  "s_nop 1\n" \
  "s_nop 1\n" \
  "s_nop 1\n" \
  "s_nop 1\n" \
  "s_nop 1\n" \
  "s_nop 1\n" \
  "s_nop 1\n" \
  "s_nop 1\n" \
  "s_nop 1\n" \
  "s_nop 1\n" \
  "s_nop 1\n" \
  "s_nop 1\n" \
  "s_nop 1\n" \
  "s_nop 1\n" \
  "s_nop 1\n" \
  "s_nop 1\n" \
  "s_nop 1\n" \
  "s_nop 1\n" \
  "s_nop 1\n" \
  "s_nop 1\n" \
  "s_nop 1\n" \
  "s_nop 1\n" \
  "s_nop 1\n" \
  "s_nop 1\n" \
  "s_nop 1\n" \
  "s_nop 1\n" \
  "s_nop 1\n" \
  "LB_b_%=:\n" \
  "buffer_load_dwordx4 v[32:35], v80, s[36:39], s50 offen sc1\n" \
  "v_mfma_f32_16x16x32_bf16 v[200:203], v[136:139], a[224:227], 0\n" \
  "s_lshr_b32 s95, s60, 2\n" \
  "v_mfma_f32_16x16x32_bf16 v[204:207], v[152:155], a[224:227], 0\n" \
  "s_sub_u32 s96, s60, s95\n" \
  "v_mfma_f32_16x16x32_bf16 v[208:211], v[136:139], a[240:243], 0\n" \
  "s_add_u32 s95, s95, " STR(T_Q) "\n" \
  "buffer_load_dwordx4 v[36:39], v81, s[36:39], s50 offen sc1\n" \
  "v_mfma_f32_16x16x32_bf16 v[212:215], v[152:155], a[240:243], 0\n" \
  "s_add_u32 s96, s96, " STR(F_C) "\n" \
  "v_mfma_f32_16x16x32_bf16 v[200:203], v[140:143], a[228:231], v[200:203]\n" \
  "s_add_u32 s60, s48, 1\n" \
  "v_mfma_f32_16x16x32_bf16 v[204:207], v[156:159], a[228:231], v[204:207]\n" \
  "s_lshr_b32 s59, s60, 2\n" \
  "buffer_load_dwordx4 v[40:43], v82, s[36:39], s50 offen sc1\n" \
  "v_mfma_f32_16x16x32_bf16 v[208:211], v[140:143], a[244:247], v[208:211]\n" \
  "s_xnor_b32 s59, s59, s28\n" \
  "v_mfma_f32_16x16x32_bf16 v[212:215], v[156:159], a[244:247], v[212:215]\n" \
  "s_and_b32 s59, s59, 1\n" \
  "v_mfma_f32_16x16x32_bf16 v[200:203], v[144:147], a[232:235], v[200:203]\n" \
  "s_lshl_b32 s59, s59, 13\n" \
  "buffer_load_dwordx4 v[44:47], v83, s[36:39], s50 offen sc1\n" \
  "v_mfma_f32_16x16x32_bf16 v[204:207], v[160:163], a[232:235], v[204:207]\n" \
  "s_lshl_b32 s55, s60, 19\n" \
  "v_mfma_f32_16x16x32_bf16 v[208:211], v[144:147], a[248:251], v[208:211]\n" \
  "s_or_b32 s55, s55, s59\n" \
  "v_mfma_f32_16x16x32_bf16 v[212:215], v[160:163], a[248:251], v[212:215]\n" \
  "v_mov_b64 v[192:193], 0\n" \
  "s_nop " STR(GAP_V) "\n" \
  "buffer_load_dwordx4 v[96:99], v17, s[36:39], s50 offen offset:0 sc1\n" \
  "v_mfma_f32_16x16x32_bf16 v[200:203], v[148:151], a[236:239], v[200:203]\n" \
  "v_mov_b64 v[194:195], 0\n" \
  "v_mfma_f32_16x16x32_bf16 v[204:207], v[164:167], a[236:239], v[204:207]\n" \
  "v_mov_b64 v[196:197], 0\n" \
  "v_mfma_f32_16x16x32_bf16 v[208:211], v[148:151], a[252:255], v[208:211]\n" \
  "v_mov_b64 v[198:199], 0\n" \
  "buffer_load_dwordx4 v[100:103], v17, s[36:39], s50 offen offset:1024 sc1\n" \
  "v_mfma_f32_16x16x32_bf16 v[212:215], v[164:167], a[252:255], v[212:215]\n" \
  "s_andn2_b32 s59, s50, 0x70000\n" \
  "v_xor_b32 v28, s59, v13\n" \
  "buffer_load_dwordx2 v[92:93], v28, s[36:39], 0 offen sc1\n" \
  "s_waitcnt vmcnt(9)\n" \
  "v_cmp_ge_u32 vcc, s60, v23\n" \
  "s_nop 1\n" \
  "v_cvt_pk_bf16_f32 v176, v200, v201\n" \
  "v_cvt_pk_bf16_f32 v177, v202, v203\n" \
  "v_cvt_pk_bf16_f32 v178, v204, v205\n" \
  "v_cvt_pk_bf16_f32 v179, v206, v207\n" \
  "v_cvt_pk_bf16_f32 v180, v208, v209\n" \
  "v_cvt_pk_bf16_f32 v181, v210, v211\n" \
  "v_cvt_pk_bf16_f32 v182, v212, v213\n" \
  "v_cvt_pk_bf16_f32 v183, v214, v215\n" \
  "s_cmp_lg_u64 vcc, 0\n" \
  "s_cselect_b32 s54, 0x80000000, 0\n" \
  "v_or_b32 v16, s54, v247\n" \
  "s_mov_b32 vcc_lo, 0x33333333\n" \
  "s_mov_b32 vcc_hi, 0x33333333\n" \
  "LS2_%=:\n" \
  "s_waitcnt vmcnt(6)\n" \
  "v_mfma_f32_16x16x32_bf16 v[228:231], v[120:123], v[32:35], 0\n" \
  "v_mfma_f32_16x16x32_bf16 v[224:227], v[104:107], v[32:35], 0\n" \
  "v_mfma_f32_16x16x32_bf16 v[232:235], v[32:35], v[32:35], 0\n" \
  "buffer_load_dwordx4 v[200:203], v16, s[40:43], s55 offen offset:0 sc1\n" \
  "s_waitcnt vmcnt(6)\n" \
  "v_mfma_f32_16x16x32_bf16 v[228:231], v[124:127], v[36:39], v[228:231]\n" \
  "v_mfma_f32_16x16x32_bf16 v[224:227], v[108:111], v[36:39], v[224:227]\n" \
  "v_mfma_f32_16x16x32_bf16 v[232:235], v[36:39], v[36:39], v[232:235]\n" \
  "buffer_load_dwordx4 v[204:207], v16, s[40:43], s55 offen offset:1024 sc1\n" \
  "s_waitcnt vmcnt(6)\n" \
  "v_mfma_f32_16x16x32_bf16 v[228:231], v[128:131], v[40:43], v[228:231]\n" \
  "v_mfma_f32_16x16x32_bf16 v[224:227], v[112:115], v[40:43], v[224:227]\n" \
  "v_mfma_f32_16x16x32_bf16 v[232:235], v[40:43], v[40:43], v[232:235]\n" \
  "buffer_load_dwordx4 v[208:211], v16, s[40:43], s55 offen offset:2048 sc1\n" \
  "s_waitcnt vmcnt(6)\n" \
  "v_mfma_f32_16x16x32_bf16 v[228:231], v[132:135], v[44:47], v[228:231]\n" \
  "v_mfma_f32_16x16x32_bf16 v[224:227], v[116:119], v[44:47], v[224:227]\n" \
  "v_mfma_f32_16x16x32_bf16 v[232:235], v[44:47], v[44:47], v[232:235]\n" \
  "buffer_load_dwordx4 v[212:215], v16, s[40:43], s55 offen offset:3072 sc1\n" \
  "s_nop 2\n" \
  "v_cndmask_b32_dpp v224, v228, v224, vcc quad_perm:[2,3,0,1] row_mask:0xf bank_mask:0xf\n" \
  "v_cndmask_b32_dpp v225, v229, v225, vcc quad_perm:[2,3,0,1] row_mask:0xf bank_mask:0xf\n" \
  "v_cndmask_b32_dpp v226, v230, v226, vcc quad_perm:[2,3,0,1] row_mask:0xf bank_mask:0xf\n" \
  "v_cndmask_b32_dpp v227, v231, v227, vcc quad_perm:[2,3,0,1] row_mask:0xf bank_mask:0xf\n" \
  "v_cndmask_b32_dpp v234, v235, v234, vcc quad_perm:[2,3,0,1] row_mask:0xf bank_mask:0xf\n" \
  "v_cmp_u_f32 vcc, v228, v228\n" \
  "v_max3_f32 v25, v224, v225, v226\n" \
  "v_max3_f32 v25, v25, v227, v234\n" \
  "s_waitcnt vmcnt(6)\n" \
  "v_smfmac_f32_16x16x64_bf16 v[184:187], v[96:99], a[192:199], v2\n" \
  "v_max_f32_dpp v26, v25, v25 quad_perm:[2,3,0,1] row_mask:0xf bank_mask:0xf\n" \
  "v_max_f32_dpp v25, v25, v25 quad_perm:[2,3,0,1] row_mask:0xf bank_mask:0xf\n" \
  "v_smfmac_f32_16x16x64_bf16 v[188:191], v[96:99], a[208:215], v2\n" \
  "v_permlane16_swap_b32 v25, v26\n" \
  "v_max_f32 v27, v25, v26\n" \
  "v_max_f32 v25, v25, v26\n" \
  "v_smfmac_f32_16x16x64_bf16 v[192:195], v[96:99], a[224:231], v2\n" \
  "s_cbranch_vccnz LBE_%=\n" \
  "LBH_%=:\n" \
  "v_permlane32_swap_b32 v25, v27\n" \
  "v_max_f32 v25, v25, v27\n" \
  "v_mul_f32 v25, s92, v25\n" \
  "v_fma_f32 v224, v224, s92, -v25\n" \
  "v_smfmac_f32_16x16x64_bf16 v[196:199], v[96:99], a[240:247], v2\n" \
  "v_fma_f32 v225, v225, s92, -v25\n" \
  "v_exp_f32 v224, v224\n" \
  "v_exp_f32 v225, v225\n" \
  "s_waitcnt vmcnt(5)\n" \
  "v_smfmac_f32_16x16x64_bf16 v[184:187], v[100:103], a[200:207], v2\n" \
  "v_fma_f32 v226, v226, s92, -v25\n" \
  "v_smfmac_f32_16x16x64_bf16 v[188:191], v[100:103], a[216:223], v2\n" \
  "v_fma_f32 v227, v227, s92, -v25\n" \
  "v_smfmac_f32_16x16x64_bf16 v[192:195], v[100:103], a[232:239], v2\n" \
  "v_exp_f32 v226, v226\n" \
  "v_smfmac_f32_16x16x64_bf16 v[196:199], v[100:103], a[248:255], v2\n" \
  "v_exp_f32 v227, v227\n" \
  "v_cvt_pk_bf16_f32 v228, v224, v225\n" \
  "v_cvt_pk_bf16_f32 v229, v226, v227\n" \
  "v_and_b32 v236, v228, v4\n" \
  "v_and_b32 v237, v229, v4\n" \
  "v_and_b32_dpp v238, v228, v4 quad_perm:[2,3,0,1] row_mask:0xf bank_mask:0xf\n" \
  "v_and_b32_dpp v239, v229, v4 quad_perm:[2,3,0,1] row_mask:0xf bank_mask:0xf\n" \
  "v_fma_f32 v234, v234, s92, -v25\n" \
  "v_and_b32_dpp v240, v228, v29 quad_perm:[2,3,0,1] row_mask:0xf bank_mask:0xf\n" \
  "v_mfma_f32_16x16x32_bf16 v[48:51], v[168:171], v[236:239], 0\n" \
  "v_and_b32_dpp v241, v229, v29 quad_perm:[2,3,0,1] row_mask:0xf bank_mask:0xf\n" \
  "v_and_b32 v242, v228, v29\n" \
  "v_mfma_f32_16x16x32_bf16 v[52:55], v[176:179], v[236:239], 0\n" \
  "v_and_b32 v243, v229, v29\n" \
  "v_exp_f32 v234, v234\n" \
  "v_cvt_pk_bf16_f32 v216, v184, v185\n" \
  "v_mfma_f32_16x16x32_bf16 v[48:51], v[172:175], v[240:243], v[48:51]\n" \
  "v_mov_b32_dpp v25, v234 quad_perm:[0,1,0,1] row_mask:0xf bank_mask:0xf\n" \
  "v_mov_b32_dpp v26, v234 quad_perm:[2,3,2,3] row_mask:0xf bank_mask:0xf\n" \
  "v_mfma_f32_16x16x32_bf16 v[52:55], v[180:183], v[240:243], v[52:55]\n" \
  "v_cvt_pk_bf16_f32 v25, v25, v26\n" \
  "v_and_b32 v230, v25, v4\n" \
  "v_and_b32 v231, v25, v4\n" \
  "v_and_b32 v232, v25, v29\n" \
  "v_and_b32 v233, v25, v29\n" \
  "v_mfma_f32_16x16x32_bf16 v[56:59], v[252:255], v[228:231], 0\n" \
  "v_cvt_pk_bf16_f32 v217, v186, v187\n" \
  "v_cvt_pk_bf16_f32 v218, v188, v189\n" \
  "v_cvt_pk_bf16_f32 v219, v190, v191\n" \
  "v_cvt_pk_bf16_f32 v220, v192, v193\n" \
  "v_cvt_pk_bf16_f32 v221, v194, v195\n" \
  "v_mfma_f32_16x16x32_bf16 v[48:51], v[216:219], v[230:233], v[48:51]\n" \
  "v_cvt_pk_bf16_f32 v222, v196, v197\n" \
  "v_cvt_pk_bf16_f32 v223, v198, v199\n" \
  "v_add_f32_dpp v56, v56, v56 quad_perm:[2,3,0,1] row_mask:0xf bank_mask:0xf\n" \
  "v_rcp_f32 v56, v56\n" \
  "v_mfma_f32_16x16x32_bf16 v[52:55], v[220:223], v[230:233], v[52:55]\n" \
  "s_nop 2\n" \
  "v_cmp_u_f32 vcc, v48, v48\n" \
  "v_mul_f32 v48, v48, v56\n" \
  "v_mul_f32 v49, v49, v56\n" \
  "v_mul_f32 v50, v50, v56\n" \
  "v_mul_f32 v51, v51, v56\n" \
  "v_pk_mul_f32 v[52:53], v[52:53], v[56:57] op_sel_hi:[1,0]\n" \
  "v_pk_mul_f32 v[54:55], v[54:55], v[56:57] op_sel_hi:[1,0]\n" \
  "v_cvt_pk_bf16_f32 v72, v48, v49\n" \
  "v_cvt_pk_bf16_f32 v73, v50, v51\n" \
  "v_cvt_pk_bf16_f32 v74, v52, v53\n" \
  "v_cvt_pk_bf16_f32 v75, v54, v55\n" \
  "s_cbranch_vccnz LBF_%=\n" \
  "LBG_%=:\n" \
  "buffer_store_dwordx4 v[72:75], v10, s[36:39], s51 offen sc1\n" \
  "s_memtime s[66:67]\n" \
  "s_add_u32 s61, s64, s96\n" \
  "s_cmp_lg_u32 %7, 0\n" \
  "s_cbranch_scc1 LO_%=\n" \
  "s_getpc_b64 s[82:83]\n" \
  "LG_c_%=:\n" \
  "s_add_u32 s82, s82, LB_c_%=-LG_c_%=\n" \
  "s_addc_u32 s83, s83, 0\n" \
  "s_waitcnt lgkmcnt(0)\n" \
  "s_sub_i32 s61, s61, s66\n" \
  "s_add_i32 s59, s61, " STR(T_SNAP) "\n" \
  "s_cmp_gt_u32 s59, 768+" STR(T_SNAP) "\n" \
  "s_cbranch_scc1 LW_c_%=\n" \
  "LV_c_%=:\n" \
  "s_max_i32 s61, s61, " STR(M_C) "\n" \
  "s_lshr_b32 s61, s61, 3\n" \
  "s_lshl_b32 s61, s61, 2\n" \
  "s_sub_u32 s82, s82, s61\n" \
  "s_subb_u32 s83, s83, 0\n" \
  "s_setpc_b64 s[82:83]\n" \
  "s_nop 1\n" \
  "s_nop 1\n" \
  "s_nop 1\n" \
  "s_nop 1\n" \
  "s_nop 1\n" \
  "s_nop 1\n" \
  "s_nop 1\n" \
  "s_nop 1\n" \
  "s_nop 1\n" \
  "s_nop 1\n" \
  "s_nop 1\n" \
  "s_nop 1\n" \
  "s_nop 1\n" \
  "s_nop 1\n" \
  "s_nop 1\n" \
  "s_nop 1\n" \
  "s_nop 1\n" \
  "s_nop 1\n" \
  "s_nop 1\n" \
  "s_nop 1\n" \
  "s_nop 1\n" \
  "s_nop 1\n" \
  "s_nop 1\n" \
  "s_nop 1\n" \
  "s_nop 1\n" \
  "s_nop 1\n" \
  "s_nop 1\n" \
  "s_nop 1\n" \
  "s_nop 1\n" \
  "s_nop 1\n" \
  "s_nop 1\n" \
  "s_nop 1\n" \
  "s_nop 1\n" \
  "s_nop 1\n" \
  "s_nop 1\n" \
  "s_nop 1\n" \
  "s_nop 1\n" \
  "s_nop 1\n" \
  "s_nop 1\n" \
  "s_nop 1\n" \
  "s_nop 1\n" \
  "s_nop 1\n" \
  "s_nop 1\n" \
  "s_nop 1\n" \
  "s_nop 1\n" \
  "s_nop 1\n" \
  "s_nop 1\n" \
  "s_nop 1\n" \
  "s_nop 1\n" \
  "s_nop 1\n" \
  "s_nop 1\n" \
  "s_nop 1\n" \
  "s_nop 1\n" \
  "s_nop 1\n" \
  "s_nop 1\n" \
  "s_nop 1\n" \
  "s_nop 1\n" \
  "s_nop 1\n" \
  "s_nop 1\n" \
  "s_nop 1\n" \
  "s_nop 1\n" \
  "s_nop 1\n" \
  "s_nop 1\n" \
  "s_nop 1\n" \
  "s_nop 1\n" \
  "s_nop 1\n" \
  "s_nop 1\n" \
  "s_nop 1\n" \
  "s_nop 1\n" \
  "s_nop 1\n" \
  "s_nop 1\n" \
  "s_nop 1\n" \
  "s_nop 1\n" \
  "s_nop 1\n" \
  "s_nop 1\n" \
  "s_nop 1\n" \
  "s_nop 1\n" \
  "s_nop 1\n" \
  "s_nop 1\n" \
  "s_nop 1\n" \
  "s_nop 1\n" \
  "s_nop 1\n" \
  "s_nop 1\n" \
  "s_nop 1\n" \
  "s_nop 1\n" \
  "s_nop 1\n" \
  "s_nop 1\n" \
  "s_nop 1\n" \
  "s_nop 1\n" \
  "s_nop 1\n" \
  "s_nop 1\n" \
  "s_nop 1\n" \
  "s_nop 1\n" \
  "s_nop 1\n" \
  "s_nop 1\n" \
  "s_nop 1\n" \
  "LB_c_%=:\n" \
  "buffer_load_dwordx4 v[32:35], v11, s[36:39], s52 offen offset:0 sc1\n" \
  "buffer_load_dwordx4 v[36:39], v11, s[36:39], s52 offen offset:1024 sc1\n" \
  "buffer_load_dwordx4 v[40:43], v11, s[36:39], s52 offen offset:2048 sc1\n" \
  "buffer_load_dwordx4 v[44:47], v11, s[36:39], s52 offen offset:3072 sc1\n" \
  "v_mov_b64 v[48:49], 0\n" \
  "v_mov_b64 v[50:51], 0\n" \
  "v_mov_b64 v[52:53], 0\n" \
  "v_mov_b64 v[54:55], 0\n" \
  "v_mov_b64 v[56:57], 0\n" \
  "v_mov_b64 v[58:59], 0\n" \
  "v_mov_b64 v[60:61], 0\n" \
  "v_mov_b64 v[62:63], 0\n" \
  "v_mov_b64 v[64:65], 0\n" \
  "v_mov_b64 v[66:67], 0\n" \
  "v_mov_b64 v[68:69], 0\n" \
  "v_mov_b64 v[70:71], 0\n" \
  "s_add_u32 s48, s48, 1\n" \
  "s_cmp_lg_u32 s54, 0\n" \
  "s_cbranch_scc1 LS3_%=\n" \
  "s_waitcnt vmcnt(5)\n" \
  "ds_write_b128 v18, v[200:203] offset:0\n" \
  "ds_write_b128 v18, v[204:207] offset:1024\n" \
  "ds_write_b128 v18, v[208:211] offset:2048\n" \
  "ds_write_b128 v18, v[212:215] offset:3072\n" \
  "s_mov_b64 exec, 1\n" \
  "ds_sub_u32 v255, v88 offset:14360\n" \
  "s_mov_b64 exec, -1\n" \
  "LS3_%=:\n" \
  "s_nop " STR(D_K) "\n" \
  "ds_read_b64 v[92:93], v255 offset:14352\n" \
  "s_waitcnt lgkmcnt(0)\n" \
  "v_readfirstlane_b32 s62, v93\n" \
  "v_readfirstlane_b32 s63, v92\n" \
  "s_getpc_b64 s[82:83]\n" \
  "LG_a_%=:\n" \
  "s_sub_u32 s82, s82, LG_a_%=-LB_a_%=\n" \
  "s_subb_u32 s83, s83, 0\n" \
  "s_xor_b32 s62, s62, s48\n" \
  "s_or_b32 s62, s62, s54\n" \
  "s_sub_i32 s59, s63, s64\n" \
  "s_cmp_lt_i32 s59, " STR(F_P) "-300\n" \
  "s_cselect_b32 s62, 1, s62\n" \
  "s_movk_i32 s68, -(" STR(T_LATE) ")\n" \
  "s_cmp_eq_u32 s62, 0\n" \
  "s_cselect_b32 s68, s68, 0x7fffffff\n" \
  "s_cselect_b32 s63, s63, s64\n" \
  "v_mov_b32 v92, s63\n" \
  "v_mov_b32 v93, 0\n" \
  "ds_write_b64 v255, v[92:93] offset:14336\n" \
  "s_waitcnt vmcnt(3)\n" \
  "v_mfma_f32_16x16x32_bf16 v[224:227], v[248:251], v[32:35], 0\n" \
  "s_waitcnt vmcnt(2)\n" \
  "v_mfma_f32_16x16x32_bf16 v[224:227], v[248:251], v[36:39], v[224:227]\n" \
  "s_waitcnt vmcnt(1)\n" \
  "v_mfma_f32_16x16x32_bf16 v[224:227], v[248:251], v[40:43], v[224:227]\n" \
  "s_waitcnt vmcnt(0)\n" \
  "v_mfma_f32_16x16x32_bf16 v[224:227], v[248:251], v[44:47], v[224:227]\n" \
  "s_nop 6\n" \
  "s_memtime s[66:67]\n" \
  "v_cmp_u_f32 vcc, v224, v224\n" \
  "v_add_f32_dpp v224, v224, v224 row_ror:8 row_mask:0xf bank_mask:0xf\n" \
  "v_add_f32_dpp v225, v225, v225 row_ror:8 row_mask:0xf bank_mask:0xf\n" \
  "v_add_f32_dpp v226, v226, v226 row_ror:8 row_mask:0xf bank_mask:0xf\n" \
  "v_add_f32_dpp v227, v227, v227 row_ror:8 row_mask:0xf bank_mask:0xf\n" \
  "v_cvt_pk_bf16_f32 v228, v224, v225\n" \
  "v_cvt_pk_bf16_f32 v229, v226, v227\n" \
  "s_cbranch_vccnz LC_%=\n" \
  "LC2_%=:\n" \
  "v_permlane16_swap_b32 v228, v230\n" \
  "v_permlane16_swap_b32 v229, v231\n" \
  "buffer_store_dwordx4 v[228:231], v12, s[36:39], s53 offen sc1\n" \
  "s_waitcnt lgkmcnt(0)\n" \
  "s_sub_i32 s61, s63, s66\n" \
  "s_cmp_lt_i32 s61, s68\n" \
  "s_cbranch_scc1 LN_%=\n" \
  "LN2_%=:\n" \
  "s_cmp_gt_i32 s61, 768\n" \
  "s_cbranch_scc1 LW_a_%=\n" \
  "LV_a_%=:\n" \
  "s_max_i32 s61, s61, 0\n" \
  "s_lshr_b32 s61, s61, 3\n" \
  "s_lshl_b32 s61, s61, 2\n" \
  "s_sub_u32 s82, s82, s61\n" \
  "s_subb_u32 s83, s83, 0\n" \
  "s_setpc_b64 s[82:83]\n" \
  "LN_%=:\n" \
  "s_mov_b32 s68, s66\n" \
  "s_cmp_lg_u32 s54, 0\n" \
  "s_cbranch_scc1 LFa_%=\n" \
  "LFa2_%=:\n" \
  "LK4_%=:\n" \
  "ds_read_b64 v[92:93], v255 offset:14352\n" \
  "s_waitcnt lgkmcnt(0)\n" \
  "v_readfirstlane_b32 s62, v93\n" \
  "v_readfirstlane_b32 s63, v92\n" \
  "s_cmp_lg_u32 s62, s48\n" \
  "s_cbranch_scc1 LK4_%=\n" \
  "s_sub_i32 s59, s63, s64\n" \
  "s_sub_i32 s59, s59, " STR(F_P) "-300\n" \
  "s_sub_i32 s60, s63, s68\n" \
  "s_add_i32 s60, s60, " STR(T_LATE) "\n" \
  "s_min_i32 s59, s59, s60\n" \
  "s_sub_u32 s63, s63, s95\n" \
  "LN3_%=:\n" \
  "s_add_u32 s63, s63, s95\n" \
  "s_add_i32 s59, s59, s95\n" \
  "s_cmp_lt_i32 s59, s95\n" \
  "s_cbranch_scc1 LN3_%=\n" \
  "v_mov_b32 v92, s63\n" \
  "v_mov_b32 v93, 0\n" \
  "ds_write_b64 v255, v[92:93] offset:14336\n" \
  "s_memtime s[66:67]\n" \
  "s_waitcnt lgkmcnt(0)\n" \
  "s_sub_i32 s61, s63, s66\n" \
  "s_add_i32 s61, s61, " STR(O_N) "\n" \
  "s_branch LN2_%=\n" \
  "LO_%=:\n" \
  "s_cmp_eq_u32 %7, 1\n" \
  "s_waitcnt lgkmcnt(0)\n" \
  "s_cbranch_scc1 LK_%=\n" \
  "s_add_u32 s59, s48, 1\n" \
  "v_mov_b32 v28, s59\n" \
  "buffer_store_dword v28, v24, s[44:47], 0 offen sc1\n" \
  "v_mov_b64 v[48:49], 0\n" \
  "v_mov_b64 v[50:51], 0\n" \
  "v_mov_b64 v[52:53], 0\n" \
  "v_mov_b64 v[54:55], 0\n" \
  "v_mov_b64 v[56:57], 0\n" \
  "v_mov_b64 v[58:59], 0\n" \
  "v_mov_b64 v[60:61], 0\n" \
  "v_mov_b64 v[62:63], 0\n" \
  "v_mov_b64 v[64:65], 0\n" \
  "v_mov_b64 v[66:67], 0\n" \
  "v_mov_b64 v[68:69], 0\n" \
  "v_mov_b64 v[70:71], 0\n" \
  "s_add_u32 s48, s48, 1\n" \
  "s_cmp_lg_u32 s54, 0\n" \
  "s_cbranch_scc1 LFb_%=\n" \
  "s_waitcnt vmcnt(2)\n" \
  "ds_write_b128 v18, v[200:203] offset:0\n" \
  "ds_write_b128 v18, v[204:207] offset:1024\n" \
  "ds_write_b128 v18, v[208:211] offset:2048\n" \
  "ds_write_b128 v18, v[212:215] offset:3072\n" \
  "s_mov_b64 exec, 1\n" \
  "ds_sub_u32 v255, v88 offset:14360\n" \
  "s_mov_b64 exec, -1\n" \
  "LFb2_%=:\n" \
  "s_lshl_b32 s60, s48, 2\n" \
  "LHb_%=:\n" \
  "ds_read_b32 v27, v255 offset:14360\n" \
  "s_waitcnt lgkmcnt(0)\n" \
  "v_readfirstlane_b32 s59, v27\n" \
  "s_cmp_lt_u32 s59, s60\n" \
  "s_cbranch_scc1 LHb_%=\n" \
  "ds_read_b64_tr_b16 v[136:137], v20 offset:0\n" \
  "ds_read_b64_tr_b16 v[138:139], v21 offset:0\n" \
  "ds_read_b64_tr_b16 v[152:153], v20 offset:8\n" \
  "ds_read_b64_tr_b16 v[154:155], v21 offset:8\n" \
  "ds_read_b64_tr_b16 v[140:141], v20 offset:2048\n" \
  "ds_read_b64_tr_b16 v[142:143], v21 offset:2048\n" \
  "ds_read_b64_tr_b16 v[156:157], v20 offset:2056\n" \
  "ds_read_b64_tr_b16 v[158:159], v21 offset:2056\n" \
  "ds_read_b64_tr_b16 v[144:145], v20 offset:4096\n" \
  "ds_read_b64_tr_b16 v[146:147], v21 offset:4096\n" \
  "ds_read_b64_tr_b16 v[160:161], v20 offset:4104\n" \
  "ds_read_b64_tr_b16 v[162:163], v21 offset:4104\n" \
  "ds_read_b64_tr_b16 v[148:149], v20 offset:6144\n" \
  "ds_read_b64_tr_b16 v[150:151], v21 offset:6144\n" \
  "ds_read_b64_tr_b16 v[164:165], v20 offset:6152\n" \
  "ds_read_b64_tr_b16 v[166:167], v21 offset:6152\n" \
  "ds_read_b128 v[104:107], v84 offset:0\n" \
  "ds_read_b128 v[108:111], v85 offset:0\n" \
  "ds_read_b128 v[112:115], v86 offset:0\n" \
  "ds_read_b128 v[116:119], v87 offset:0\n" \
  "ds_read_b128 v[120:123], v84 offset:4096\n" \
  "ds_read_b128 v[124:127], v85 offset:4096\n" \
  "ds_read_b128 v[128:131], v86 offset:4096\n" \
  "ds_read_b128 v[132:135], v87 offset:4096\n" \
  "s_waitcnt lgkmcnt(0)\n" \
  "s_branch LM_%=\n" \
  "LK_%=:\n" \
  "s_mov_b32 s94, s63\n" \
  "s_memtime s[72:73]\n" \
  "s_memrealtime s[74:75]\n" \
  "s_nop 1\n" \
  "s_memrealtime s[76:77]\n" \
  "s_nop 1\n" \
  "s_memrealtime s[78:79]\n" \
  "s_nop 1\n" \
  "s_memrealtime s[80:81]\n" \
  "s_nop 1\n" \
  "s_memrealtime s[82:83]\n" \
  "s_nop 1\n" \
  "s_memrealtime s[84:85]\n" \
  "s_nop 1\n" \
  "s_memrealtime s[86:87]\n" \
  "s_nop 1\n" \
  "s_memrealtime s[88:89]\n" \
  "s_nop 1\n" \
  "s_memrealtime s[90:91]\n" \
  "s_mov_b32 s59, 0\n" \
  "s_waitcnt lgkmcnt(0)\n" \
  "s_cmp_eq_u32 s76, s74\n" \
  "s_addc_u32 s59, s59, 0\n" \
  "s_cmp_eq_u32 s78, s74\n" \
  "s_addc_u32 s59, s59, 0\n" \
  "s_cmp_eq_u32 s80, s74\n" \
  "s_addc_u32 s59, s59, 0\n" \
  "s_cmp_eq_u32 s82, s74\n" \
  "s_addc_u32 s59, s59, 0\n" \
  "s_cmp_eq_u32 s84, s74\n" \
  "s_addc_u32 s59, s59, 0\n" \
  "s_cmp_eq_u32 s86, s74\n" \
  "s_addc_u32 s59, s59, 0\n" \
  "s_cmp_eq_u32 s88, s74\n" \
  "s_addc_u32 s59, s59, 0\n" \
  "s_cmp_eq_u32 s90, s74\n" \
  "s_addc_u32 s59, s59, 0\n" \
  "s_mul_i32 s59, s59, 12\n" \
  "s_add_u32 s68, s72, s59\n" \
  "s_add_u32 s59, s74, 4\n" \
  "s_mul_i32 s69, s59, 24\n" \
  "s_sub_u32 s59, s68, s56\n" \
  "s_sub_u32 s60, s95, " STR(T_Q) "\n" \
  "s_lshl_b32 s60, s60, 2\n" \
  "s_add_u32 s60, s60, " STR(F_P) "+650\n" \
  "s_cmp_gt_u32 s59, s60\n" \
  "s_cselect_b32 s59, " STR(E_UP) ", -(" STR(E_DOWN) ")\n" \
  "s_cmp_gt_u32 s48, 4\n" \
  "s_cselect_b32 s59, s59, 0\n" \
  "s_add_i32 s29, s29, s59\n" \
  "s_max_i32 s29, s29, 0\n" \
  "s_min_i32 s29, s29, 1500\n" \
  "s_sub_u32 s60, s69, s62\n" \
  "s_sub_u32 s59, s68, s56\n" \
  "s_mov_b32 s62, s69\n" \
  "s_mov_b32 s56, s68\n" \
  "v_cvt_f32_u32 v27, s59\n" \
  "v_cvt_f32_u32 v28, s60\n" \
  "v_rcp_f32 v26, v27\n" \
  "v_rcp_f32 v19, v28\n" \
  "v_mul_f32 v26, v26, v28\n" \
  "v_mul_f32 v95, " STR(DECAY) ", v95\n" \
  "v_mul_f32 v19, v19, v27\n" \
  "v_max_f32 v95, v95, v26\n" \
  "v_cvt_f32_u32 v28, " STR(F_P) "\n" \
  "v_mul_f32 v27, v95, v28\n" \
  "v_cvt_u32_f32 v27, v27\n" \
  "v_min_u32 v27, 0x7fff, v27\n" \
  "s_nop 0\n" \
  "v_readfirstlane_b32 s63, v27\n" \
  "s_waitcnt vmcnt(5)\n" \
  "LK2_%=:\n" \
  "v_max_u32 v26, v92, v93\n" \
  "v_cmp_eq_u32 vcc, -1, v26\n" \
  "v_subrev_u32 v25, s62, v92\n" \
  "v_subrev_u32 v26, s94, v93\n" \
  "s_nop 0\n" \
  "v_max_i32_dpp v25, v25, v25 quad_perm:[1,0,3,2] row_mask:0xf bank_mask:0xf\n" \
  "v_max_i32_dpp v26, v26, v26 quad_perm:[1,0,3,2] row_mask:0xf bank_mask:0xf\n" \
  "s_cbranch_vccnz LK3_%=\n" \
  "v_max_i32_dpp v25, v25, v25 quad_perm:[2,3,0,1] row_mask:0xf bank_mask:0xf\n" \
  "v_max_i32_dpp v26, v26, v26 quad_perm:[2,3,0,1] row_mask:0xf bank_mask:0xf\n" \
  "LK5_%=:\n" \
  "v_cvt_f32_i32 v25, v25\n" \
  "s_add_u32 s59, s48, 1\n" \
  "v_mov_b32 v93, s59\n" \
  "v_mul_f32 v25, v25, v19\n" \
  "v_cvt_i32_f32 v25, v25\n" \
  "v_add_u32 v92, s68, v25\n" \
  "ds_write_b64 v255, v[92:93] offset:14352\n" \
  "v_max_i32 v26, 0, v26\n" \
  "v_min_i32 v26, 2000, v26\n" \
  "ds_write_b32 v255, v26 offset:14344\n" \
  "v_mov_b64 v[48:49], 0\n" \
  "v_mov_b64 v[50:51], 0\n" \
  "v_mov_b64 v[52:53], 0\n" \
  "v_mov_b64 v[54:55], 0\n" \
  "v_mov_b64 v[56:57], 0\n" \
  "v_mov_b64 v[58:59], 0\n" \
  "v_mov_b64 v[60:61], 0\n" \
  "v_mov_b64 v[62:63], 0\n" \
  "v_mov_b64 v[64:65], 0\n" \
  "v_mov_b64 v[66:67], 0\n" \
  "v_mov_b64 v[68:69], 0\n" \
  "v_mov_b64 v[70:71], 0\n" \
  "s_add_u32 s48, s48, 1\n" \
  "s_cmp_lg_u32 s54, 0\n" \
  "s_cbranch_scc1 LFc_%=\n" \
  "s_waitcnt vmcnt(1)\n" \
  "ds_write_b128 v18, v[200:203] offset:0\n" \
  "ds_write_b128 v18, v[204:207] offset:1024\n" \
  "ds_write_b128 v18, v[208:211] offset:2048\n" \
  "ds_write_b128 v18, v[212:215] offset:3072\n" \
  "s_mov_b64 exec, 1\n" \
  "ds_sub_u32 v255, v88 offset:14360\n" \
  "s_mov_b64 exec, -1\n" \
  "LFc2_%=:\n" \
  "s_lshl_b32 s60, s48, 2\n" \
  "LHc_%=:\n" \
  "ds_read_b32 v27, v255 offset:14360\n" \
  "s_waitcnt lgkmcnt(0)\n" \
  "v_readfirstlane_b32 s59, v27\n" \
  "s_cmp_lt_u32 s59, s60\n" \
  "s_cbranch_scc1 LHc_%=\n" \
  "ds_read_b64_tr_b16 v[136:137], v20 offset:0\n" \
  "ds_read_b64_tr_b16 v[138:139], v21 offset:0\n" \
  "ds_read_b64_tr_b16 v[152:153], v20 offset:8\n" \
  "ds_read_b64_tr_b16 v[154:155], v21 offset:8\n" \
  "ds_read_b64_tr_b16 v[140:141], v20 offset:2048\n" \
  "ds_read_b64_tr_b16 v[142:143], v21 offset:2048\n" \
  "ds_read_b64_tr_b16 v[156:157], v20 offset:2056\n" \
  "ds_read_b64_tr_b16 v[158:159], v21 offset:2056\n" \
  "ds_read_b64_tr_b16 v[144:145], v20 offset:4096\n" \
  "ds_read_b64_tr_b16 v[146:147], v21 offset:4096\n" \
  "ds_read_b64_tr_b16 v[160:161], v20 offset:4104\n" \
  "ds_read_b64_tr_b16 v[162:163], v21 offset:4104\n" \
  "ds_read_b64_tr_b16 v[148:149], v20 offset:6144\n" \
  "ds_read_b64_tr_b16 v[150:151], v21 offset:6144\n" \
  "ds_read_b64_tr_b16 v[164:165], v20 offset:6152\n" \
  "ds_read_b64_tr_b16 v[166:167], v21 offset:6152\n" \
  "ds_read_b128 v[104:107], v84 offset:0\n" \
  "ds_read_b128 v[108:111], v85 offset:0\n" \
  "ds_read_b128 v[112:115], v86 offset:0\n" \
  "ds_read_b128 v[116:119], v87 offset:0\n" \
  "ds_read_b128 v[120:123], v84 offset:4096\n" \
  "ds_read_b128 v[124:127], v85 offset:4096\n" \
  "ds_read_b128 v[128:131], v86 offset:4096\n" \
  "ds_read_b128 v[132:135], v87 offset:4096\n" \
  "s_waitcnt lgkmcnt(0)\n" \
  "s_branch LM_%=\n" \
  "LR2_%=:\n" \
  "s_sleep 16\n" \
  "buffer_load_dwordx4 v[68:71], off, s[44:47], s61 sc1\n" \
  "s_waitcnt vmcnt(0)\n" \
  "s_add_u32 s58, s58, 1\n" \
  "s_cmp_lt_u32 s58, " STR(SPIN_CAP) "\n" \
  "s_cbranch_scc1 LR1_%=\n" \
  "v_mov_b32 v27, " STR(CW_DEAD) "*4\n" \
  "v_mov_b32 v28, 1\n" \
  "buffer_store_dword v28, v27, s[44:47], 0 offen sc1\n" \
  "s_branch LR3_%=\n" \
  ".p2align 6\n" \
  "LM_%=:\n" \
  "s_barrier\n" \
  "buffer_load_dwordx4 v[32:35], v80, s[36:39], s49 offen sc1\n" \
  "ds_read_b32 v236, v255 offset:14336\n" \
  "v_mfma_f32_16x16x32_bf16 v[184:187], v[136:139], a[192:195], 0\n" \
  "s_xor_b32 s51, s51, 1052672\n" \
  "s_add_u32 s53, s53, 1<<17\n" \
  "v_mfma_f32_16x16x32_bf16 v[188:191], v[152:155], a[192:195], 0\n" \
  "s_and_b32 s53, s53, 0x60000\n" \
  "s_lshl_b32 s50, s48, 19\n" \
  "v_mfma_f32_16x16x32_bf16 v[192:195], v[136:139], a[208:211], 0\n" \
  "s_add_u32 s50, s50, " STR(EXP_B) "<<21\n" \
  "s_xor_b32 s50, s50, s93\n" \
  "buffer_load_dwordx4 v[36:39], v81, s[36:39], s49 offen sc1\n" \
  "v_mfma_f32_16x16x32_bf16 v[196:199], v[152:155], a[208:211], 0\n" \
  "s_and_b32 s59, s48, 7\n" \
  "s_mul_i32 s59, s59, 0x4800\n" \
  "v_mfma_f32_16x16x32_bf16 v[184:187], v[140:143], a[196:199], v[184:187]\n" \
  "s_and_b32 s59, s59, 0x7000\n" \
  "s_xor_b32 s50, s50, s59\n" \
  "v_mfma_f32_16x16x32_bf16 v[188:191], v[156:159], a[196:199], v[188:191]\n" \
  "s_add_u32 s60, s48, 4\n" \
  "s_lshl_b32 s54, s60, 19\n" \
  "buffer_load_dwordx4 v[40:43], v82, s[36:39], s49 offen sc1\n" \
  "v_mfma_f32_16x16x32_bf16 v[192:195], v[140:143], a[212:215], v[192:195]\n" \
  "s_add_u32 s54, s54, " STR(EXP_B) "<<21\n" \
  "s_xor_b32 s54, s54, s93\n" \
  "v_mfma_f32_16x16x32_bf16 v[196:199], v[156:159], a[212:215], v[196:199]\n" \
  "s_and_b32 s59, s60, 7\n" \
  "s_mul_i32 s59, s59, 0x4800\n" \
  "v_mfma_f32_16x16x32_bf16 v[184:187], v[144:147], a[200:203], v[184:187]\n" \
  "s_waitcnt lgkmcnt(0)\n" \
  "s_sub_u32 s61, " STR(F_P) ", s56\n" \
  "s_add_u32 s61, s61, s29\n" \
  "s_and_b32 s59, s59, 0x7000\n" \
  "s_xor_b32 s54, s54, s59\n" \
  "buffer_load_dwordx4 v[44:47], v83, s[36:39], s49 offen sc1\n" \
  "v_mfma_f32_16x16x32_bf16 v[188:191], v[160:163], a[200:203], v[188:191]\n" \
  "v_add_u32 v236, s61, v236\n" \
  "s_xor_b32 s59, %7, 3\n" \
  "s_lshl_b32 s59, s59, 29\n" \
  "v_mfma_f32_16x16x32_bf16 v[192:195], v[144:147], a[216:219], v[192:195]\n" \
  "v_cvt_f32_i32 v236, v236\n" \
  "s_xor_b32 s60, s53, 0x40000\n" \
  "s_or_b32 s59, s59, s60\n" \
  "v_mfma_f32_16x16x32_bf16 v[196:199], v[160:163], a[216:219], v[196:199]\n" \
  "s_add_u32 s61, s63, s29\n" \
  "v_mov_b32 v237, s61\n" \
  "v_mfma_f32_16x16x32_bf16 v[184:187], v[148:151], a[204:207], v[184:187]\n" \
  "v_mul_f32 v236, v236, v95\n" \
  "v_mfma_f32_16x16x32_bf16 v[188:191], v[164:167], a[204:207], v[188:191]\n" \
  "v_cvt_i32_f32 v236, v236\n" \
  "v_mfma_f32_16x16x32_bf16 v[192:195], v[148:151], a[220:223], v[192:195]\n" \
  "v_add_u32 v236, s62, v236\n" \
  "v_mfma_f32_16x16x32_bf16 v[196:199], v[164:167], a[220:223], v[196:199]\n" \
  "v_min_u32 v236, -2, v236\n" \
  "v_add_u32 v26, s59, v12\n" \
  "s_branch LS1_%=\n" \
  "LA_%=:\n" \
  "ds_read_b64 v[92:93], v255 offset:14336\n" \
  "v_max3_u32 v25, v32, v33, v34\n" \
  "v_max3_u32 v25, v25, v35, v36\n" \
  "v_max3_u32 v25, v25, v37, v38\n" \
  "v_max3_u32 v25, v25, v39, v40\n" \
  "v_max3_u32 v25, v25, v41, v42\n" \
  "v_max3_u32 v25, v25, v43, v44\n" \
  "v_max3_u32 v25, v25, v45, v46\n" \
  "v_max_u32 v25, v25, v47\n" \
  "v_cmp_eq_u32 vcc, -1, v25\n" \
  "s_cbranch_vccz LA4_%=\n" \
  "s_add_u32 s58, s58, 1\n" \
  "s_cmp_lt_u32 s58, " STR(SPIN_CAP) "\n" \
  "s_cbranch_scc1 LA1_%=\n" \
  "v_mov_b32 v27, " STR(CW_DEAD) "*4\n" \
  "v_mov_b32 v28, 1\n" \
  "buffer_store_dword v28, v27, s[44:47], 0 offen sc1\n" \
  "s_branch LA4_%=\n" \
  "LA1_%=:\n" \
  "s_waitcnt lgkmcnt(0)\n" \
  "v_readfirstlane_b32 s64, v92\n" \
  "s_memtime s[66:67]\n" \
  "s_waitcnt lgkmcnt(0)\n" \
  "s_sub_u32 s59, " STR(O_A) ", s66\n" \
  "LQ_ra_%=:\n" \
  "s_add_u32 s64, s64, s95\n" \
  "s_add_i32 s61, s64, s59\n" \
  "s_cmp_lt_i32 s61, -" STR(T_TOL) "\n" \
  "s_cbranch_scc1 LQ_ra_%=\n" \
  "s_max_i32 s61, s61, 0\n" \
  "s_min_i32 s61, s61, 4000\n" \
  "s_bitcmp0_b32 s61, 4\n" \
  "s_cbranch_scc1 LX_ra_%=\n" \
  "s_nop 8\n" \
  "LX_ra_%=:\n" \
  "s_lshr_b32 s61, s61, 5\n" \
  "s_cbranch_scc0 LB_ra_%=\n" \
  ".p2align 4\n" \
  "LY_ra_%=:\n" \
  "s_sub_u32 s61, s61, 1\n" \
  "s_cmp_lg_u32 s61, 0\n" \
  "s_cbranch_scc1 LY_ra_%=\n" \
  "LB_ra_%=:\n" \
  "buffer_load_dwordx4 v[32:35], v80, s[36:39], s49 offen sc1\n" \
  "s_nop 11\n" \
  "buffer_load_dwordx4 v[36:39], v81, s[36:39], s49 offen sc1\n" \
  "s_nop 11\n" \
  "buffer_load_dwordx4 v[40:43], v82, s[36:39], s49 offen sc1\n" \
  "s_nop 11\n" \
  "buffer_load_dwordx4 v[44:47], v83, s[36:39], s49 offen sc1\n" \
  "v_mov_b64 v[48:49], 0\n" \
  "v_mov_b64 v[50:51], 0\n" \
  "v_mov_b64 v[52:53], 0\n" \
  "v_mov_b64 v[54:55], 0\n" \
  "v_mov_b64 v[56:57], 0\n" \
  "v_mov_b64 v[58:59], 0\n" \
  "v_mov_b64 v[60:61], 0\n" \
  "v_mov_b64 v[62:63], 0\n" \
  "v_mov_b64 v[64:65], 0\n" \
  "v_mov_b64 v[66:67], 0\n" \
  "v_mov_b64 v[68:69], 0\n" \
  "v_mov_b64 v[70:71], 0\n" \
  "v_sub_u32 v27, s64, v92\n" \
  "ds_max_u32 v255, v27 offset:14340\n" \
  "s_branch LS1_%=\n" \
  "LW_b_%=:\n" \
  "s_cmp_ge_i32 s61, -(" STR(T_SNAP) ")\n" \
  "s_cbranch_scc1 LU_b_%=\n" \
  "s_add_u32 s64, s64, s95\n" \
  "s_add_i32 s61, s61, s95\n" \
  "s_sub_i32 s61, s61, 48\n" \
  "s_branch LW_b_%=\n" \
  "LU_b_%=:\n" \
  "s_min_i32 s61, s61, 4000\n" \
  "s_sub_i32 s61, s61, 40\n" \
  ".p2align 4\n" \
  "LY_b_%=:\n" \
  "s_sub_i32 s61, s61, 32\n" \
  "s_cmp_gt_i32 s61, 704\n" \
  "s_cbranch_scc1 LY_b_%=\n" \
  "s_branch LV_b_%=\n" \
  "LBE_%=:\n" \
  "v_max3_u32 v26, v32, v33, v34\n" \
  "v_max3_u32 v26, v26, v35, v36\n" \
  "v_max3_u32 v26, v26, v37, v38\n" \
  "v_max3_u32 v26, v26, v39, v40\n" \
  "v_max3_u32 v26, v26, v41, v42\n" \
  "v_max3_u32 v26, v26, v43, v44\n" \
  "v_max3_u32 v26, v26, v45, v46\n" \
  "v_max_u32 v26, v26, v47\n" \
  "v_cmp_eq_u32 vcc, -1, v26\n" \
  "s_cbranch_vccz LBH_%=\n" \
  "s_add_u32 s58, s58, 1\n" \
  "s_cmp_lt_u32 s58, " STR(SPIN_CAP) "\n" \
  "s_cbranch_scc1 LB1_%=\n" \
  "v_mov_b32 v27, " STR(CW_DEAD) "*4\n" \
  "v_mov_b32 v28, 1\n" \
  "buffer_store_dword v28, v27, s[44:47], 0 offen sc1\n" \
  "s_branch LBI_%=\n" \
  "LBI_%=:\n" \
  "v_mov_b32 v27, v25\n" \
  "s_nop 1\n" \
  "s_branch LBH_%=\n" \
  "LBF_%=:\n" \
  "v_max3_u32 v25, v32, v33, v34\n" \
  "v_max3_u32 v25, v25, v35, v36\n" \
  "v_max3_u32 v25, v25, v37, v38\n" \
  "v_max3_u32 v25, v25, v39, v40\n" \
  "v_max3_u32 v25, v25, v41, v42\n" \
  "v_max3_u32 v25, v25, v43, v44\n" \
  "v_max3_u32 v25, v25, v45, v46\n" \
  "v_max_u32 v25, v25, v47\n" \
  "v_max3_u32 v25, v25, v96, v97\n" \
  "v_max3_u32 v25, v25, v98, v99\n" \
  "v_max3_u32 v25, v25, v100, v101\n" \
  "v_max3_u32 v25, v25, v102, v103\n" \
  "v_cmp_eq_u32 vcc, -1, v25\n" \
  "s_cbranch_vccz LBG_%=\n" \
  "s_add_u32 s58, s58, 1\n" \
  "s_cmp_lt_u32 s58, " STR(SPIN_CAP) "\n" \
  "s_cbranch_scc1 LB1_%=\n" \
  "v_mov_b32 v27, " STR(CW_DEAD) "*4\n" \
  "v_mov_b32 v28, 1\n" \
  "buffer_store_dword v28, v27, s[44:47], 0 offen sc1\n" \
  "s_branch LBG_%=\n" \
  "LB1_%=:\n" \
  "s_waitcnt vmcnt(0)\n" \
  "v_mov_b64 v[184:185], 0\n" \
  "v_mov_b64 v[186:187], 0\n" \
  "v_mov_b64 v[188:189], 0\n" \
  "v_mov_b64 v[190:191], 0\n" \
  "v_mov_b64 v[192:193], 0\n" \
  "v_mov_b64 v[194:195], 0\n" \
  "v_mov_b64 v[196:197], 0\n" \
  "v_mov_b64 v[198:199], 0\n" \
  "s_add_u32 s60, s95, " STR(F_B) "+" STR(O_B) "-" STR(T_Q) "\n" \
  "s_memtime s[66:67]\n" \
  "s_waitcnt lgkmcnt(0)\n" \
  "s_sub_u32 s59, s60, s66\n" \
  "LQ_rb_%=:\n" \
  "s_add_u32 s64, s64, s95\n" \
  "s_add_i32 s61, s64, s59\n" \
  "s_cmp_lt_i32 s61, -" STR(T_TOL) "\n" \
  "s_cbranch_scc1 LQ_rb_%=\n" \
  "s_max_i32 s61, s61, 0\n" \
  "s_min_i32 s61, s61, 4000\n" \
  "s_bitcmp0_b32 s61, 4\n" \
  "s_cbranch_scc1 LX_rb_%=\n" \
  "s_nop 8\n" \
  "LX_rb_%=:\n" \
  "s_lshr_b32 s61, s61, 5\n" \
  "s_cbranch_scc0 LB_rb_%=\n" \
  ".p2align 4\n" \
  "LY_rb_%=:\n" \
  "s_sub_u32 s61, s61, 1\n" \
  "s_cmp_lg_u32 s61, 0\n" \
  "s_cbranch_scc1 LY_rb_%=\n" \
  "LB_rb_%=:\n" \
  "buffer_load_dwordx4 v[32:35], v80, s[36:39], s50 offen sc1\n" \
  "s_nop 11\n" \
  "buffer_load_dwordx4 v[36:39], v81, s[36:39], s50 offen sc1\n" \
  "s_nop 11\n" \
  "buffer_load_dwordx4 v[40:43], v82, s[36:39], s50 offen sc1\n" \
  "s_nop 11\n" \
  "buffer_load_dwordx4 v[44:47], v83, s[36:39], s50 offen sc1\n" \
  "s_nop 11\n" \
  "buffer_load_dwordx4 v[96:99], v17, s[36:39], s50 offen offset:0 sc1\n" \
  "s_nop 11\n" \
  "buffer_load_dwordx4 v[100:103], v17, s[36:39], s50 offen offset:1024 sc1\n" \
  "buffer_store_dword v27, v88, s[36:39], 0 offen sc1\n" \
  "s_mov_b32 vcc_lo, 0x33333333\n" \
  "s_mov_b32 vcc_hi, 0x33333333\n" \
  "s_branch LS2_%=\n" \
  "LW_c_%=:\n" \
  "s_cmp_ge_i32 s61, -(" STR(T_SNAP) ")\n" \
  "s_cbranch_scc1 LU_c_%=\n" \
  "s_add_u32 s64, s64, s95\n" \
  "s_add_i32 s61, s61, s95\n" \
  "s_sub_i32 s61, s61, 48\n" \
  "s_branch LW_c_%=\n" \
  "LU_c_%=:\n" \
  "s_min_i32 s61, s61, 4000\n" \
  "s_sub_i32 s61, s61, 40\n" \
  ".p2align 4\n" \
  "LY_c_%=:\n" \
  "s_sub_i32 s61, s61, 32\n" \
  "s_cmp_gt_i32 s61, 704\n" \
  "s_cbranch_scc1 LY_c_%=\n" \
  "s_branch LV_c_%=\n" \
  "LW_a_%=:\n" \
  "s_min_i32 s61, s61, 4000\n" \
  "s_sub_i32 s61, s61, 24\n" \
  ".p2align 4\n" \
  "LY_a_%=:\n" \
  "s_sub_i32 s61, s61, 32\n" \
  "s_cmp_gt_i32 s61, 704\n" \
  "s_cbranch_scc1 LY_a_%=\n" \
  "s_branch LV_a_%=\n" \
  "LFa_%=:\n" \
  "buffer_load_dword v23, v22, s[44:47], 0 offen sc1\n" \
  "s_waitcnt vmcnt(0)\n" \
  "v_cmp_ge_u32 vcc, s48, v23\n" \
  "s_cbranch_vccz LFa3_%=\n" \
  "s_sleep 2\n" \
  "s_add_u32 s58, s58, 1\n" \
  "s_cmp_lt_u32 s58, " STR(SPIN_CAP) "\n" \
  "s_cbranch_scc1 LFa_%=\n" \
  "v_mov_b32 v27, " STR(CW_DEAD) "*4\n" \
  "v_mov_b32 v28, 1\n" \
  "buffer_store_dword v28, v27, s[44:47], 0 offen sc1\n" \
  "s_branch LFa3_%=\n" \
  "LFa3_%=:\n" \
  "buffer_load_dwordx4 v[200:203], v247, s[40:43], s55 offen offset:0 sc1\n" \
  "buffer_load_dwordx4 v[204:207], v247, s[40:43], s55 offen offset:1024 sc1\n" \
  "buffer_load_dwordx4 v[208:211], v247, s[40:43], s55 offen offset:2048 sc1\n" \
  "buffer_load_dwordx4 v[212:215], v247, s[40:43], s55 offen offset:3072 sc1\n" \
  "s_waitcnt vmcnt(0)\n" \
  "ds_write_b128 v18, v[200:203] offset:0\n" \
  "ds_write_b128 v18, v[204:207] offset:1024\n" \
  "ds_write_b128 v18, v[208:211] offset:2048\n" \
  "ds_write_b128 v18, v[212:215] offset:3072\n" \
  "s_mov_b64 exec, 1\n" \
  "ds_sub_u32 v255, v88 offset:14360\n" \
  "s_mov_b64 exec, -1\n" \
  "s_branch LFa2_%=\n" \
  "LC_%=:\n" \
  "v_max3_u32 v25, v32, v33, v34\n" \
  "v_max3_u32 v25, v25, v35, v36\n" \
  "v_max3_u32 v25, v25, v37, v38\n" \
  "v_max3_u32 v25, v25, v39, v40\n" \
  "v_max3_u32 v25, v25, v41, v42\n" \
  "v_max3_u32 v25, v25, v43, v44\n" \
  "v_max3_u32 v25, v25, v45, v46\n" \
  "v_max_u32 v25, v25, v47\n" \
  "v_cmp_eq_u32 vcc, -1, v25\n" \
  "s_cbranch_vccz LC2_%=\n" \
  "s_add_u32 s58, s58, 1\n" \
  "s_cmp_lt_u32 s58, " STR(SPIN_CAP) "\n" \
  "s_cbranch_scc1 LC1_%=\n" \
  "v_mov_b32 v27, " STR(CW_DEAD) "*4\n" \
  "v_mov_b32 v28, 1\n" \
  "buffer_store_dword v28, v27, s[44:47], 0 offen sc1\n" \
  "s_branch LC2_%=\n" \
  "LC1_%=:\n" \
  "s_add_u32 s60, s96, " STR(O_C) "\n" \
  "s_memtime s[66:67]\n" \
  "s_waitcnt lgkmcnt(0)\n" \
  "s_sub_u32 s59, s60, s66\n" \
  "LQ_rc_%=:\n" \
  "s_add_u32 s64, s64, s95\n" \
  "s_add_i32 s61, s64, s59\n" \
  "s_cmp_lt_i32 s61, -" STR(T_TOL) "\n" \
  "s_cbranch_scc1 LQ_rc_%=\n" \
  "s_max_i32 s61, s61, 0\n" \
  "s_min_i32 s61, s61, 4000\n" \
  "s_bitcmp0_b32 s61, 4\n" \
  "s_cbranch_scc1 LX_rc_%=\n" \
  "s_nop 8\n" \
  "LX_rc_%=:\n" \
  "s_lshr_b32 s61, s61, 5\n" \
  "s_cbranch_scc0 LB_rc_%=\n" \
  ".p2align 4\n" \
  "LY_rc_%=:\n" \
  "s_sub_u32 s61, s61, 1\n" \
  "s_cmp_lg_u32 s61, 0\n" \
  "s_cbranch_scc1 LY_rc_%=\n" \
  "LB_rc_%=:\n" \
  "buffer_load_dwordx4 v[32:35], v11, s[36:39], s52 offen offset:0 sc1\n" \
  "buffer_load_dwordx4 v[36:39], v11, s[36:39], s52 offen offset:1024 sc1\n" \
  "buffer_load_dwordx4 v[40:43], v11, s[36:39], s52 offen offset:2048 sc1\n" \
  "buffer_load_dwordx4 v[44:47], v11, s[36:39], s52 offen offset:3072 sc1\n" \
  "s_branch LS3_%=\n" \
  "LFb_%=:\n" \
  "buffer_load_dword v23, v22, s[44:47], 0 offen sc1\n" \
  "s_waitcnt vmcnt(0)\n" \
  "v_cmp_ge_u32 vcc, s48, v23\n" \
  "s_cbranch_vccz LFb3_%=\n" \
  "s_sleep 2\n" \
  "s_add_u32 s58, s58, 1\n" \
  "s_cmp_lt_u32 s58, " STR(SPIN_CAP) "\n" \
  "s_cbranch_scc1 LFb_%=\n" \
  "v_mov_b32 v27, " STR(CW_DEAD) "*4\n" \
  "v_mov_b32 v28, 1\n" \
  "buffer_store_dword v28, v27, s[44:47], 0 offen sc1\n" \
  "s_branch LFb3_%=\n" \
  "LFb3_%=:\n" \
  "buffer_load_dwordx4 v[200:203], v247, s[40:43], s55 offen offset:0 sc1\n" \
  "buffer_load_dwordx4 v[204:207], v247, s[40:43], s55 offen offset:1024 sc1\n" \
  "buffer_load_dwordx4 v[208:211], v247, s[40:43], s55 offen offset:2048 sc1\n" \
  "buffer_load_dwordx4 v[212:215], v247, s[40:43], s55 offen offset:3072 sc1\n" \
  "s_waitcnt vmcnt(0)\n" \
  "ds_write_b128 v18, v[200:203] offset:0\n" \
  "ds_write_b128 v18, v[204:207] offset:1024\n" \
  "ds_write_b128 v18, v[208:211] offset:2048\n" \
  "ds_write_b128 v18, v[212:215] offset:3072\n" \
  "s_mov_b64 exec, 1\n" \
  "ds_sub_u32 v255, v88 offset:14360\n" \
  "s_mov_b64 exec, -1\n" \
  "s_branch LFb2_%=\n" \
  "LFc_%=:\n" \
  "buffer_load_dword v23, v22, s[44:47], 0 offen sc1\n" \
  "s_waitcnt vmcnt(0)\n" \
  "v_cmp_ge_u32 vcc, s48, v23\n" \
  "s_cbranch_vccz LFc3_%=\n" \
  "s_sleep 2\n" \
  "s_add_u32 s58, s58, 1\n" \
  "s_cmp_lt_u32 s58, " STR(SPIN_CAP) "\n" \
  "s_cbranch_scc1 LFc_%=\n" \
  "v_mov_b32 v27, " STR(CW_DEAD) "*4\n" \
  "v_mov_b32 v28, 1\n" \
  "buffer_store_dword v28, v27, s[44:47], 0 offen sc1\n" \
  "s_branch LFc3_%=\n" \
  "LFc3_%=:\n" \
  "buffer_load_dwordx4 v[200:203], v247, s[40:43], s55 offen offset:0 sc1\n" \
  "buffer_load_dwordx4 v[204:207], v247, s[40:43], s55 offen offset:1024 sc1\n" \
  "buffer_load_dwordx4 v[208:211], v247, s[40:43], s55 offen offset:2048 sc1\n" \
  "buffer_load_dwordx4 v[212:215], v247, s[40:43], s55 offen offset:3072 sc1\n" \
  "s_waitcnt vmcnt(0)\n" \
  "ds_write_b128 v18, v[200:203] offset:0\n" \
  "ds_write_b128 v18, v[204:207] offset:1024\n" \
  "ds_write_b128 v18, v[208:211] offset:2048\n" \
  "ds_write_b128 v18, v[212:215] offset:3072\n" \
  "s_mov_b64 exec, 1\n" \
  "ds_sub_u32 v255, v88 offset:14360\n" \
  "s_mov_b64 exec, -1\n" \
  "s_branch LFc2_%=\n" \
  "LK3_%=:\n" \
  "s_sleep 2\n" \
  "s_andn2_b32 s59, s50, 0x70000\n" \
  "v_xor_b32 v28, s59, v13\n" \
  "buffer_load_dwordx2 v[92:93], v28, s[36:39], 0 offen sc1\n" \
  "s_waitcnt vmcnt(0)\n" \
  "s_add_u32 s58, s58, 1\n" \
  "s_cmp_lt_u32 s58, " STR(SPIN_CAP) "\n" \
  "s_cbranch_scc1 LK2_%=\n" \
  "v_mov_b32 v27, " STR(CW_DEAD) "*4\n" \
  "v_mov_b32 v28, 1\n" \
  "buffer_store_dword v28, v27, s[44:47], 0 offen sc1\n" \
  "s_branch LK6_%=\n" \
  "LK6_%=:\n" \
  "v_mov_b32 v25, 0\n" \
  "s_branch LK5_%=\n" \
  "LD_%=:\n" \
  "s_waitcnt lgkmcnt(0)\n" \
  "v_pk_add_f32 v[72:73], v[72:73], v[74:75]\n" \
  "v_pk_add_f32 v[76:77], v[76:77], v[78:79]\n" \
  "v_pk_add_f32 v[72:73], v[72:73], v[76:77]\n" \
  "v_cvt_pk_bf16_f32 v27, v72, v73\n" \
  "buffer_store_dword v27, v9, s[36:39], s50 offen sc1\n" \
  "LE_%=:\n" \
  "s_add_u32 s59, s48, 1\n" \
  "v_mov_b32 v28, s59\n" \
  "buffer_store_dword v28, v24, s[44:47], 0 offen sc1\n" \
  "s_endpgm\n" \
  ""

#define GEMM_ASM \
  "v_mbcnt_lo_u32_b32 v4, -1, 0\n" \
  "v_mbcnt_hi_u32_b32 v4, -1, v4\n" \
  "v_and_b32 v116, 15, v4\n" \
  "v_lshrrev_b32 v117, 4, v4\n" \
  "s_lshl_b32 s86, %8, 13\n" \
  "s_add_u32 s86, s86, %9\n" \
  "v_lshrrev_b32 v5, 3, v4\n" \
  "v_and_b32 v6, 7, v4\n" \
  "v_lshlrev_b32 v8, 13, v5\n" \
  "v_lshl_add_u32 v8, v6, 4, v8\n" \
  "v_and_b32 v9, 1, v117\n" \
  "v_lshlrev_b32 v9, 5, v9\n" \
  "v_xor_b32 v8, v8, v9\n" \
  "v_xor_b32 v9, 64, v8\n" \
  "s_mov_b64 s[68:69], %2\n" \
  "s_and_b32 s69, s69, 0xffff\n" \
  "s_mov_b32 s70, 8388608\n" \
  "s_mov_b32 s71, 0x00020000\n" \
  "s_lshl_b32 s92, %8, 19\n" \
  "s_add_u32 s92, s92, 0\n" \
  "s_add_u32 m0, s86, 98304\n" \
  "s_nop 0\n" \
  "buffer_load_dwordx4 v8, s[68:71], s92 offen lds\n" \
  "s_add_u32 m0, s86, 99328\n" \
  "s_add_u32 s92, s92, 65536\n" \
  "buffer_load_dwordx4 v9, s[68:71], s92 offen lds\n" \
  "s_add_u32 m0, s86, 100352\n" \
  "s_add_u32 s92, s92, 65536\n" \
  "buffer_load_dwordx4 v8, s[68:71], s92 offen lds\n" \
  "s_add_u32 m0, s86, 101376\n" \
  "s_add_u32 s92, s92, 65536\n" \
  "buffer_load_dwordx4 v9, s[68:71], s92 offen lds\n" \
  "s_add_u32 m0, s86, 102400\n" \
  "s_add_u32 s92, s92, 65536\n" \
  "buffer_load_dwordx4 v8, s[68:71], s92 offen lds\n" \
  "s_add_u32 m0, s86, 103424\n" \
  "s_add_u32 s92, s92, 65536\n" \
  "buffer_load_dwordx4 v9, s[68:71], s92 offen lds\n" \
  "s_add_u32 m0, s86, 104448\n" \
  "s_add_u32 s92, s92, 65536\n" \
  "buffer_load_dwordx4 v8, s[68:71], s92 offen lds\n" \
  "s_add_u32 m0, s86, 105472\n" \
  "s_add_u32 s92, s92, 65536\n" \
  "buffer_load_dwordx4 v9, s[68:71], s92 offen lds\n" \
  "s_lshl_b32 s92, %8, 19\n" \
  "s_add_u32 s92, s92, 2097152\n" \
  "s_add_u32 m0, s86, 131072\n" \
  "s_nop 0\n" \
  "buffer_load_dwordx4 v8, s[68:71], s92 offen lds\n" \
  "s_add_u32 m0, s86, 132096\n" \
  "s_add_u32 s92, s92, 65536\n" \
  "buffer_load_dwordx4 v9, s[68:71], s92 offen lds\n" \
  "s_add_u32 m0, s86, 133120\n" \
  "s_add_u32 s92, s92, 65536\n" \
  "buffer_load_dwordx4 v8, s[68:71], s92 offen lds\n" \
  "s_add_u32 m0, s86, 134144\n" \
  "s_add_u32 s92, s92, 65536\n" \
  "buffer_load_dwordx4 v9, s[68:71], s92 offen lds\n" \
  "s_add_u32 m0, s86, 135168\n" \
  "s_add_u32 s92, s92, 65536\n" \
  "buffer_load_dwordx4 v8, s[68:71], s92 offen lds\n" \
  "s_add_u32 m0, s86, 136192\n" \
  "s_add_u32 s92, s92, 65536\n" \
  "buffer_load_dwordx4 v9, s[68:71], s92 offen lds\n" \
  "s_add_u32 m0, s86, 137216\n" \
  "s_add_u32 s92, s92, 65536\n" \
  "buffer_load_dwordx4 v8, s[68:71], s92 offen lds\n" \
  "s_add_u32 m0, s86, 138240\n" \
  "s_add_u32 s92, s92, 65536\n" \
  "buffer_load_dwordx4 v9, s[68:71], s92 offen lds\n" \
  "s_lshl_b32 s92, %8, 19\n" \
  "s_add_u32 s92, s92, 4194304\n" \
  "s_add_u32 m0, s86, 0\n" \
  "s_nop 0\n" \
  "buffer_load_dwordx4 v8, s[68:71], s92 offen lds\n" \
  "s_add_u32 m0, s86, 1024\n" \
  "s_add_u32 s92, s92, 65536\n" \
  "buffer_load_dwordx4 v9, s[68:71], s92 offen lds\n" \
  "s_add_u32 m0, s86, 2048\n" \
  "s_add_u32 s92, s92, 65536\n" \
  "buffer_load_dwordx4 v8, s[68:71], s92 offen lds\n" \
  "s_add_u32 m0, s86, 3072\n" \
  "s_add_u32 s92, s92, 65536\n" \
  "buffer_load_dwordx4 v9, s[68:71], s92 offen lds\n" \
  "s_add_u32 m0, s86, 4096\n" \
  "s_add_u32 s92, s92, 65536\n" \
  "buffer_load_dwordx4 v8, s[68:71], s92 offen lds\n" \
  "s_add_u32 m0, s86, 5120\n" \
  "s_add_u32 s92, s92, 65536\n" \
  "buffer_load_dwordx4 v9, s[68:71], s92 offen lds\n" \
  "s_add_u32 m0, s86, 6144\n" \
  "s_add_u32 s92, s92, 65536\n" \
  "buffer_load_dwordx4 v8, s[68:71], s92 offen lds\n" \
  "s_add_u32 m0, s86, 7168\n" \
  "s_add_u32 s92, s92, 65536\n" \
  "buffer_load_dwordx4 v9, s[68:71], s92 offen lds\n" \
  "s_lshl_b32 s92, %8, 19\n" \
  "s_add_u32 s92, s92, 6291456\n" \
  "s_add_u32 m0, s86, 32768\n" \
  "s_nop 0\n" \
  "buffer_load_dwordx4 v8, s[68:71], s92 offen lds\n" \
  "s_add_u32 m0, s86, 33792\n" \
  "s_add_u32 s92, s92, 65536\n" \
  "buffer_load_dwordx4 v9, s[68:71], s92 offen lds\n" \
  "s_add_u32 m0, s86, 34816\n" \
  "s_add_u32 s92, s92, 65536\n" \
  "buffer_load_dwordx4 v8, s[68:71], s92 offen lds\n" \
  "s_add_u32 m0, s86, 35840\n" \
  "s_add_u32 s92, s92, 65536\n" \
  "buffer_load_dwordx4 v9, s[68:71], s92 offen lds\n" \
  "s_add_u32 m0, s86, 36864\n" \
  "s_add_u32 s92, s92, 65536\n" \
  "buffer_load_dwordx4 v8, s[68:71], s92 offen lds\n" \
  "s_add_u32 m0, s86, 37888\n" \
  "s_add_u32 s92, s92, 65536\n" \
  "buffer_load_dwordx4 v9, s[68:71], s92 offen lds\n" \
  "s_add_u32 m0, s86, 38912\n" \
  "s_add_u32 s92, s92, 65536\n" \
  "buffer_load_dwordx4 v8, s[68:71], s92 offen lds\n" \
  "s_add_u32 m0, s86, 39936\n" \
  "s_add_u32 s92, s92, 65536\n" \
  "buffer_load_dwordx4 v9, s[68:71], s92 offen lds\n" \
  "s_mov_b64 s[72:73], %4\n" \
  "s_and_b32 s73, s73, 0xffff\n" \
  "s_mov_b32 s74, 4194304\n" \
  "s_mov_b32 s75, 0x00020000\n" \
  "s_mov_b64 s[76:77], %5\n" \
  "s_and_b32 s77, s77, 0xffff\n" \
  "s_mov_b32 s78, 3*" STR(QKV_SZ32) "\n" \
  "s_mov_b32 s79, 0x00020000\n" \
  "s_mov_b64 s[80:81], %6\n" \
  "s_and_b32 s81, s81, 0xffff\n" \
  "s_mov_b32 s82, " STR(CTRL_BYTES) "\n" \
  "s_mov_b32 s83, 0x00020000\n" \
  "v_xor_b32 v6, v6, v117\n" \
  "s_and_b32 s84, %8, 1\n" \
  "s_lshl_b32 s84, s84, 2\n" \
  "v_xor_b32 v6, s84, v6\n" \
  "v_lshlrev_b32 v7, 11, v5\n" \
  "v_lshl_add_u32 v7, v6, 4, v7\n" \
  "s_lshl_b32 s87, %8, 14\n" \
  "s_add_u32 s96, s87, 63*65536\n" \
  "v_lshrrev_b32 v5, 1, v116\n" \
  "v_xor_b32 v5, v5, v117\n" \
  "v_lshlrev_b32 v5, 4, v5\n" \
  "v_lshl_add_u32 v5, v116, 7, v5\n" \
  "v_add_u32 v10, %9, v5\n" \
  "v_xor_b32 v5, 64, v5\n" \
  "v_add_u32 v11, %9, v5\n" \
  "v_add_u32 v12, 0x10000, v10\n" \
  "v_add_u32 v13, 0x10000, v11\n" \
  "s_lshr_b32 s84, %7, 1\n" \
  "s_lshl_b32 s84, s84, 14\n" \
  "s_and_b32 s85, %7, 1\n" \
  "s_lshl_b32 s85, s85, 2\n" \
  "s_add_u32 s85, s85, %8\n" \
  "s_lshl_b32 s91, s85, 9\n" \
  "s_add_u32 s91, s91, s84\n" \
  "v_and_b32 v5, 1, v117\n" \
  "v_lshlrev_b32 v14, 12, v5\n" \
  "v_lshrrev_b32 v5, 1, v117\n" \
  "v_lshl_add_u32 v14, v5, 8, v14\n" \
  "v_lshl_add_u32 v14, v116, 4, v14\n" \
  "v_add_u32 v14, s91, v14\n" \
  "s_lshl_b32 s91, s85, 10\n" \
  "s_add_u32 s91, s91, s84\n" \
  "v_lshlrev_b32 v15, 4, v4\n" \
  "v_add_u32 v15, s91, v15\n" \
  "v_xor_b32 v18, 0x800, v14\n" \
  "v_xor_b32 v15, 0x4000, v15\n" \
  "s_lshl_b32 s84, %7, 7\n" \
  "s_add_u32 s84, s84, " STR(CW_GPROG) "*4\n" \
  "v_or_b32 v5, %8, v4\n" \
  "v_lshl_add_u32 v16, v5, 24, s84\n" \
  "v_mov_b32 v17, 0\n" \
  "v_or_b32 v14, 0x80000000, v14\n" \
  "v_or_b32 v15, 0x80000000, v15\n" \
  "v_or_b32 v18, 0x80000000, v18\n" \
  "v_lshrrev_b32 v5, 2, v116\n" \
  "v_lshl_add_u32 v6, v117, 3, v5\n" \
  "v_lshlrev_b32 v6, 7, v6\n" \
  "v_lshrrev_b32 v5, 1, v5\n" \
  "v_and_b32 v119, 1, v117\n" \
  "v_lshl_add_u32 v5, v119, 1, v5\n" \
  "v_xor_b32 v5, %8, v5\n" \
  "v_lshl_add_u32 v6, v5, 5, v6\n" \
  "v_and_b32 v5, 3, v4\n" \
  "v_lshl_add_u32 v6, v5, 3, v6\n" \
  "v_add_u32 v118, %9, v6\n" \
  "v_add_u32 v119, 0x10000, v118\n" \
  "v_add_u32 v120, 0x20000, v118\n" \
  "s_lshl_b32 s84, %8, 10\n" \
  "s_add_u32 s91, s84, %9\n" \
  "s_waitcnt vmcnt(24) lgkmcnt(0)\n" \
  "s_barrier\n" \
  "s_mov_b64 s[68:69], %3\n" \
  "s_and_b32 s69, s69, 0xffff\n" \
  "s_mov_b32 s70, 8388608\n" \
  "s_mov_b32 s71, 0x00020000\n" \
  "s_lshl_b32 s92, %8, 19\n" \
  "s_add_u32 s92, s92, 0\n" \
  "s_add_u32 m0, s86, 65536\n" \
  "s_nop 0\n" \
  "buffer_load_dwordx4 v8, s[68:71], s92 offen lds\n" \
  "s_add_u32 m0, s86, 66560\n" \
  "s_add_u32 s92, s92, 65536\n" \
  "buffer_load_dwordx4 v9, s[68:71], s92 offen lds\n" \
  "s_add_u32 m0, s86, 67584\n" \
  "s_add_u32 s92, s92, 65536\n" \
  "buffer_load_dwordx4 v8, s[68:71], s92 offen lds\n" \
  "s_add_u32 m0, s86, 68608\n" \
  "s_add_u32 s92, s92, 65536\n" \
  "buffer_load_dwordx4 v9, s[68:71], s92 offen lds\n" \
  "s_add_u32 m0, s86, 69632\n" \
  "s_add_u32 s92, s92, 65536\n" \
  "buffer_load_dwordx4 v8, s[68:71], s92 offen lds\n" \
  "s_add_u32 m0, s86, 70656\n" \
  "s_add_u32 s92, s92, 65536\n" \
  "buffer_load_dwordx4 v9, s[68:71], s92 offen lds\n" \
  "s_add_u32 m0, s86, 71680\n" \
  "s_add_u32 s92, s92, 65536\n" \
  "buffer_load_dwordx4 v8, s[68:71], s92 offen lds\n" \
  "s_add_u32 m0, s86, 72704\n" \
  "s_add_u32 s92, s92, 65536\n" \
  "buffer_load_dwordx4 v9, s[68:71], s92 offen lds\n" \
  "ds_read_b64_tr_b16 a[0:1], v119 offset:32768\n" \
  "ds_read_b64_tr_b16 a[2:3], v119 offset:33280\n" \
  "ds_read_b64_tr_b16 a[4:5], v119 offset:36864\n" \
  "ds_read_b64_tr_b16 a[6:7], v119 offset:37376\n" \
  "ds_read_b64_tr_b16 a[8:9], v119 offset:40960\n" \
  "ds_read_b64_tr_b16 a[10:11], v119 offset:41472\n" \
  "ds_read_b64_tr_b16 a[12:13], v119 offset:45056\n" \
  "ds_read_b64_tr_b16 a[14:15], v119 offset:45568\n" \
  "ds_read_b64_tr_b16 a[16:17], v119 offset:49152\n" \
  "ds_read_b64_tr_b16 a[18:19], v119 offset:49664\n" \
  "ds_read_b64_tr_b16 a[20:21], v119 offset:53248\n" \
  "ds_read_b64_tr_b16 a[22:23], v119 offset:53760\n" \
  "ds_read_b64_tr_b16 a[24:25], v119 offset:57344\n" \
  "ds_read_b64_tr_b16 a[26:27], v119 offset:57856\n" \
  "ds_read_b64_tr_b16 a[28:29], v119 offset:61440\n" \
  "ds_read_b64_tr_b16 a[30:31], v119 offset:61952\n" \
  "s_waitcnt vmcnt(24) lgkmcnt(0)\n" \
  "s_barrier\n" \
  "s_lshl_b32 s92, %8, 19\n" \
  "s_add_u32 s92, s92, 2097152\n" \
  "s_add_u32 m0, s86, 98304\n" \
  "s_nop 0\n" \
  "buffer_load_dwordx4 v8, s[68:71], s92 offen lds\n" \
  "s_add_u32 m0, s86, 99328\n" \
  "s_add_u32 s92, s92, 65536\n" \
  "buffer_load_dwordx4 v9, s[68:71], s92 offen lds\n" \
  "s_add_u32 m0, s86, 100352\n" \
  "s_add_u32 s92, s92, 65536\n" \
  "buffer_load_dwordx4 v8, s[68:71], s92 offen lds\n" \
  "s_add_u32 m0, s86, 101376\n" \
  "s_add_u32 s92, s92, 65536\n" \
  "buffer_load_dwordx4 v9, s[68:71], s92 offen lds\n" \
  "s_add_u32 m0, s86, 102400\n" \
  "s_add_u32 s92, s92, 65536\n" \
  "buffer_load_dwordx4 v8, s[68:71], s92 offen lds\n" \
  "s_add_u32 m0, s86, 103424\n" \
  "s_add_u32 s92, s92, 65536\n" \
  "buffer_load_dwordx4 v9, s[68:71], s92 offen lds\n" \
  "s_add_u32 m0, s86, 104448\n" \
  "s_add_u32 s92, s92, 65536\n" \
  "buffer_load_dwordx4 v8, s[68:71], s92 offen lds\n" \
  "s_add_u32 m0, s86, 105472\n" \
  "s_add_u32 s92, s92, 65536\n" \
  "buffer_load_dwordx4 v9, s[68:71], s92 offen lds\n" \
  "ds_read_b64_tr_b16 a[32:33], v120 offset:0\n" \
  "ds_read_b64_tr_b16 a[34:35], v120 offset:512\n" \
  "ds_read_b64_tr_b16 a[36:37], v120 offset:4096\n" \
  "ds_read_b64_tr_b16 a[38:39], v120 offset:4608\n" \
  "ds_read_b64_tr_b16 a[40:41], v120 offset:8192\n" \
  "ds_read_b64_tr_b16 a[42:43], v120 offset:8704\n" \
  "ds_read_b64_tr_b16 a[44:45], v120 offset:12288\n" \
  "ds_read_b64_tr_b16 a[46:47], v120 offset:12800\n" \
  "ds_read_b64_tr_b16 a[48:49], v120 offset:16384\n" \
  "ds_read_b64_tr_b16 a[50:51], v120 offset:16896\n" \
  "ds_read_b64_tr_b16 a[52:53], v120 offset:20480\n" \
  "ds_read_b64_tr_b16 a[54:55], v120 offset:20992\n" \
  "ds_read_b64_tr_b16 a[56:57], v120 offset:24576\n" \
  "ds_read_b64_tr_b16 a[58:59], v120 offset:25088\n" \
  "ds_read_b64_tr_b16 a[60:61], v120 offset:28672\n" \
  "ds_read_b64_tr_b16 a[62:63], v120 offset:29184\n" \
  "s_waitcnt vmcnt(24) lgkmcnt(0)\n" \
  "s_barrier\n" \
  "s_lshl_b32 s92, %8, 19\n" \
  "s_add_u32 s92, s92, 4194304\n" \
  "s_add_u32 m0, s86, 131072\n" \
  "s_nop 0\n" \
  "buffer_load_dwordx4 v8, s[68:71], s92 offen lds\n" \
  "s_add_u32 m0, s86, 132096\n" \
  "s_add_u32 s92, s92, 65536\n" \
  "buffer_load_dwordx4 v9, s[68:71], s92 offen lds\n" \
  "s_add_u32 m0, s86, 133120\n" \
  "s_add_u32 s92, s92, 65536\n" \
  "buffer_load_dwordx4 v8, s[68:71], s92 offen lds\n" \
  "s_add_u32 m0, s86, 134144\n" \
  "s_add_u32 s92, s92, 65536\n" \
  "buffer_load_dwordx4 v9, s[68:71], s92 offen lds\n" \
  "s_add_u32 m0, s86, 135168\n" \
  "s_add_u32 s92, s92, 65536\n" \
  "buffer_load_dwordx4 v8, s[68:71], s92 offen lds\n" \
  "s_add_u32 m0, s86, 136192\n" \
  "s_add_u32 s92, s92, 65536\n" \
  "buffer_load_dwordx4 v9, s[68:71], s92 offen lds\n" \
  "s_add_u32 m0, s86, 137216\n" \
  "s_add_u32 s92, s92, 65536\n" \
  "buffer_load_dwordx4 v8, s[68:71], s92 offen lds\n" \
  "s_add_u32 m0, s86, 138240\n" \
  "s_add_u32 s92, s92, 65536\n" \
  "buffer_load_dwordx4 v9, s[68:71], s92 offen lds\n" \
  "ds_read_b64_tr_b16 a[64:65], v118 offset:0\n" \
  "ds_read_b64_tr_b16 a[66:67], v118 offset:512\n" \
  "ds_read_b64_tr_b16 a[68:69], v118 offset:4096\n" \
  "ds_read_b64_tr_b16 a[70:71], v118 offset:4608\n" \
  "ds_read_b64_tr_b16 a[72:73], v118 offset:8192\n" \
  "ds_read_b64_tr_b16 a[74:75], v118 offset:8704\n" \
  "ds_read_b64_tr_b16 a[76:77], v118 offset:12288\n" \
  "ds_read_b64_tr_b16 a[78:79], v118 offset:12800\n" \
  "ds_read_b64_tr_b16 a[80:81], v118 offset:16384\n" \
  "ds_read_b64_tr_b16 a[82:83], v118 offset:16896\n" \
  "ds_read_b64_tr_b16 a[84:85], v118 offset:20480\n" \
  "ds_read_b64_tr_b16 a[86:87], v118 offset:20992\n" \
  "ds_read_b64_tr_b16 a[88:89], v118 offset:24576\n" \
  "ds_read_b64_tr_b16 a[90:91], v118 offset:25088\n" \
  "ds_read_b64_tr_b16 a[92:93], v118 offset:28672\n" \
  "ds_read_b64_tr_b16 a[94:95], v118 offset:29184\n" \
  "s_waitcnt vmcnt(24) lgkmcnt(0)\n" \
  "s_barrier\n" \
  "s_lshl_b32 s92, %8, 19\n" \
  "s_add_u32 s92, s92, 6291456\n" \
  "s_add_u32 m0, s86, 0\n" \
  "s_nop 0\n" \
  "buffer_load_dwordx4 v8, s[68:71], s92 offen lds\n" \
  "s_add_u32 m0, s86, 1024\n" \
  "s_add_u32 s92, s92, 65536\n" \
  "buffer_load_dwordx4 v9, s[68:71], s92 offen lds\n" \
  "s_add_u32 m0, s86, 2048\n" \
  "s_add_u32 s92, s92, 65536\n" \
  "buffer_load_dwordx4 v8, s[68:71], s92 offen lds\n" \
  "s_add_u32 m0, s86, 3072\n" \
  "s_add_u32 s92, s92, 65536\n" \
  "buffer_load_dwordx4 v9, s[68:71], s92 offen lds\n" \
  "s_add_u32 m0, s86, 4096\n" \
  "s_add_u32 s92, s92, 65536\n" \
  "buffer_load_dwordx4 v8, s[68:71], s92 offen lds\n" \
  "s_add_u32 m0, s86, 5120\n" \
  "s_add_u32 s92, s92, 65536\n" \
  "buffer_load_dwordx4 v9, s[68:71], s92 offen lds\n" \
  "s_add_u32 m0, s86, 6144\n" \
  "s_add_u32 s92, s92, 65536\n" \
  "buffer_load_dwordx4 v8, s[68:71], s92 offen lds\n" \
  "s_add_u32 m0, s86, 7168\n" \
  "s_add_u32 s92, s92, 65536\n" \
  "buffer_load_dwordx4 v9, s[68:71], s92 offen lds\n" \
  "ds_read_b64_tr_b16 a[96:97], v118 offset:32768\n" \
  "ds_read_b64_tr_b16 a[98:99], v118 offset:33280\n" \
  "ds_read_b64_tr_b16 a[100:101], v118 offset:36864\n" \
  "ds_read_b64_tr_b16 a[102:103], v118 offset:37376\n" \
  "ds_read_b64_tr_b16 a[104:105], v118 offset:40960\n" \
  "ds_read_b64_tr_b16 a[106:107], v118 offset:41472\n" \
  "ds_read_b64_tr_b16 a[108:109], v118 offset:45056\n" \
  "ds_read_b64_tr_b16 a[110:111], v118 offset:45568\n" \
  "ds_read_b64_tr_b16 a[112:113], v118 offset:49152\n" \
  "ds_read_b64_tr_b16 a[114:115], v118 offset:49664\n" \
  "ds_read_b64_tr_b16 a[116:117], v118 offset:53248\n" \
  "ds_read_b64_tr_b16 a[118:119], v118 offset:53760\n" \
  "ds_read_b64_tr_b16 a[120:121], v118 offset:57344\n" \
  "ds_read_b64_tr_b16 a[122:123], v118 offset:57856\n" \
  "ds_read_b64_tr_b16 a[124:125], v118 offset:61440\n" \
  "ds_read_b64_tr_b16 a[126:127], v118 offset:61952\n" \
  "s_waitcnt vmcnt(24) lgkmcnt(0)\n" \
  "s_barrier\n" \
  "s_mov_b64 s[68:69], %1\n" \
  "s_and_b32 s69, s69, 0xffff\n" \
  "s_mov_b32 s70, 8388608\n" \
  "s_mov_b32 s71, 0x00020000\n" \
  "s_lshl_b32 s92, %8, 19\n" \
  "s_add_u32 s92, s92, 0\n" \
  "s_add_u32 m0, s86, 32768\n" \
  "s_nop 0\n" \
  "buffer_load_dwordx4 v8, s[68:71], s92 offen lds\n" \
  "s_add_u32 m0, s86, 33792\n" \
  "s_add_u32 s92, s92, 65536\n" \
  "buffer_load_dwordx4 v9, s[68:71], s92 offen lds\n" \
  "s_add_u32 m0, s86, 34816\n" \
  "s_add_u32 s92, s92, 65536\n" \
  "buffer_load_dwordx4 v8, s[68:71], s92 offen lds\n" \
  "s_add_u32 m0, s86, 35840\n" \
  "s_add_u32 s92, s92, 65536\n" \
  "buffer_load_dwordx4 v9, s[68:71], s92 offen lds\n" \
  "s_add_u32 m0, s86, 36864\n" \
  "s_add_u32 s92, s92, 65536\n" \
  "buffer_load_dwordx4 v8, s[68:71], s92 offen lds\n" \
  "s_add_u32 m0, s86, 37888\n" \
  "s_add_u32 s92, s92, 65536\n" \
  "buffer_load_dwordx4 v9, s[68:71], s92 offen lds\n" \
  "s_add_u32 m0, s86, 38912\n" \
  "s_add_u32 s92, s92, 65536\n" \
  "buffer_load_dwordx4 v8, s[68:71], s92 offen lds\n" \
  "s_add_u32 m0, s86, 39936\n" \
  "s_add_u32 s92, s92, 65536\n" \
  "buffer_load_dwordx4 v9, s[68:71], s92 offen lds\n" \
  "ds_read_b64_tr_b16 a[128:129], v119 offset:0\n" \
  "ds_read_b64_tr_b16 a[130:131], v119 offset:512\n" \
  "ds_read_b64_tr_b16 a[132:133], v119 offset:4096\n" \
  "ds_read_b64_tr_b16 a[134:135], v119 offset:4608\n" \
  "ds_read_b64_tr_b16 a[136:137], v119 offset:8192\n" \
  "ds_read_b64_tr_b16 a[138:139], v119 offset:8704\n" \
  "ds_read_b64_tr_b16 a[140:141], v119 offset:12288\n" \
  "ds_read_b64_tr_b16 a[142:143], v119 offset:12800\n" \
  "ds_read_b64_tr_b16 a[144:145], v119 offset:16384\n" \
  "ds_read_b64_tr_b16 a[146:147], v119 offset:16896\n" \
  "ds_read_b64_tr_b16 a[148:149], v119 offset:20480\n" \
  "ds_read_b64_tr_b16 a[150:151], v119 offset:20992\n" \
  "ds_read_b64_tr_b16 a[152:153], v119 offset:24576\n" \
  "ds_read_b64_tr_b16 a[154:155], v119 offset:25088\n" \
  "ds_read_b64_tr_b16 a[156:157], v119 offset:28672\n" \
  "ds_read_b64_tr_b16 a[158:159], v119 offset:29184\n" \
  "s_waitcnt vmcnt(24) lgkmcnt(0)\n" \
  "s_barrier\n" \
  "s_lshl_b32 s92, %8, 19\n" \
  "s_add_u32 s92, s92, 2097152\n" \
  "s_add_u32 m0, s86, 65536\n" \
  "s_nop 0\n" \
  "buffer_load_dwordx4 v8, s[68:71], s92 offen lds\n" \
  "s_add_u32 m0, s86, 66560\n" \
  "s_add_u32 s92, s92, 65536\n" \
  "buffer_load_dwordx4 v9, s[68:71], s92 offen lds\n" \
  "s_add_u32 m0, s86, 67584\n" \
  "s_add_u32 s92, s92, 65536\n" \
  "buffer_load_dwordx4 v8, s[68:71], s92 offen lds\n" \
  "s_add_u32 m0, s86, 68608\n" \
  "s_add_u32 s92, s92, 65536\n" \
  "buffer_load_dwordx4 v9, s[68:71], s92 offen lds\n" \
  "s_add_u32 m0, s86, 69632\n" \
  "s_add_u32 s92, s92, 65536\n" \
  "buffer_load_dwordx4 v8, s[68:71], s92 offen lds\n" \
  "s_add_u32 m0, s86, 70656\n" \
  "s_add_u32 s92, s92, 65536\n" \
  "buffer_load_dwordx4 v9, s[68:71], s92 offen lds\n" \
  "s_add_u32 m0, s86, 71680\n" \
  "s_add_u32 s92, s92, 65536\n" \
  "buffer_load_dwordx4 v8, s[68:71], s92 offen lds\n" \
  "s_add_u32 m0, s86, 72704\n" \
  "s_add_u32 s92, s92, 65536\n" \
  "buffer_load_dwordx4 v9, s[68:71], s92 offen lds\n" \
  "ds_read_b64_tr_b16 a[160:161], v119 offset:32768\n" \
  "ds_read_b64_tr_b16 a[162:163], v119 offset:33280\n" \
  "ds_read_b64_tr_b16 a[164:165], v119 offset:36864\n" \
  "ds_read_b64_tr_b16 a[166:167], v119 offset:37376\n" \
  "ds_read_b64_tr_b16 a[168:169], v119 offset:40960\n" \
  "ds_read_b64_tr_b16 a[170:171], v119 offset:41472\n" \
  "ds_read_b64_tr_b16 a[172:173], v119 offset:45056\n" \
  "ds_read_b64_tr_b16 a[174:175], v119 offset:45568\n" \
  "ds_read_b64_tr_b16 a[176:177], v119 offset:49152\n" \
  "ds_read_b64_tr_b16 a[178:179], v119 offset:49664\n" \
  "ds_read_b64_tr_b16 a[180:181], v119 offset:53248\n" \
  "ds_read_b64_tr_b16 a[182:183], v119 offset:53760\n" \
  "ds_read_b64_tr_b16 a[184:185], v119 offset:57344\n" \
  "ds_read_b64_tr_b16 a[186:187], v119 offset:57856\n" \
  "ds_read_b64_tr_b16 a[188:189], v119 offset:61440\n" \
  "ds_read_b64_tr_b16 a[190:191], v119 offset:61952\n" \
  "s_waitcnt vmcnt(24) lgkmcnt(0)\n" \
  "s_barrier\n" \
  "s_lshl_b32 s92, %8, 19\n" \
  "s_add_u32 s92, s92, 4194304\n" \
  "s_add_u32 m0, s86, 98304\n" \
  "s_nop 0\n" \
  "buffer_load_dwordx4 v8, s[68:71], s92 offen lds\n" \
  "s_add_u32 m0, s86, 99328\n" \
  "s_add_u32 s92, s92, 65536\n" \
  "buffer_load_dwordx4 v9, s[68:71], s92 offen lds\n" \
  "s_add_u32 m0, s86, 100352\n" \
  "s_add_u32 s92, s92, 65536\n" \
  "buffer_load_dwordx4 v8, s[68:71], s92 offen lds\n" \
  "s_add_u32 m0, s86, 101376\n" \
  "s_add_u32 s92, s92, 65536\n" \
  "buffer_load_dwordx4 v9, s[68:71], s92 offen lds\n" \
  "s_add_u32 m0, s86, 102400\n" \
  "s_add_u32 s92, s92, 65536\n" \
  "buffer_load_dwordx4 v8, s[68:71], s92 offen lds\n" \
  "s_add_u32 m0, s86, 103424\n" \
  "s_add_u32 s92, s92, 65536\n" \
  "buffer_load_dwordx4 v9, s[68:71], s92 offen lds\n" \
  "s_add_u32 m0, s86, 104448\n" \
  "s_add_u32 s92, s92, 65536\n" \
  "buffer_load_dwordx4 v8, s[68:71], s92 offen lds\n" \
  "s_add_u32 m0, s86, 105472\n" \
  "s_add_u32 s92, s92, 65536\n" \
  "buffer_load_dwordx4 v9, s[68:71], s92 offen lds\n" \
  "ds_read_b64_tr_b16 a[192:193], v120 offset:0\n" \
  "ds_read_b64_tr_b16 a[194:195], v120 offset:512\n" \
  "ds_read_b64_tr_b16 a[196:197], v120 offset:4096\n" \
  "ds_read_b64_tr_b16 a[198:199], v120 offset:4608\n" \
  "ds_read_b64_tr_b16 a[200:201], v120 offset:8192\n" \
  "ds_read_b64_tr_b16 a[202:203], v120 offset:8704\n" \
  "ds_read_b64_tr_b16 a[204:205], v120 offset:12288\n" \
  "ds_read_b64_tr_b16 a[206:207], v120 offset:12800\n" \
  "ds_read_b64_tr_b16 a[208:209], v120 offset:16384\n" \
  "ds_read_b64_tr_b16 a[210:211], v120 offset:16896\n" \
  "ds_read_b64_tr_b16 a[212:213], v120 offset:20480\n" \
  "ds_read_b64_tr_b16 a[214:215], v120 offset:20992\n" \
  "ds_read_b64_tr_b16 a[216:217], v120 offset:24576\n" \
  "ds_read_b64_tr_b16 a[218:219], v120 offset:25088\n" \
  "ds_read_b64_tr_b16 a[220:221], v120 offset:28672\n" \
  "ds_read_b64_tr_b16 a[222:223], v120 offset:29184\n" \
  "s_waitcnt vmcnt(24) lgkmcnt(0)\n" \
  "s_barrier\n" \
  "s_lshl_b32 s84, %11, 7\n" \
  "buffer_load_dwordx4 v[20:23], off, s[80:83], s84 sc1\n" \
  "s_lshl_b32 s92, %8, 19\n" \
  "s_add_u32 s92, s92, 6291456\n" \
  "s_add_u32 m0, s86, 131072\n" \
  "s_nop 0\n" \
  "buffer_load_dwordx4 v8, s[68:71], s92 offen lds\n" \
  "s_add_u32 m0, s86, 132096\n" \
  "s_add_u32 s92, s92, 65536\n" \
  "buffer_load_dwordx4 v9, s[68:71], s92 offen lds\n" \
  "s_add_u32 m0, s86, 133120\n" \
  "s_add_u32 s92, s92, 65536\n" \
  "buffer_load_dwordx4 v8, s[68:71], s92 offen lds\n" \
  "s_add_u32 m0, s86, 134144\n" \
  "s_add_u32 s92, s92, 65536\n" \
  "buffer_load_dwordx4 v9, s[68:71], s92 offen lds\n" \
  "s_add_u32 m0, s86, 135168\n" \
  "s_add_u32 s92, s92, 65536\n" \
  "buffer_load_dwordx4 v8, s[68:71], s92 offen lds\n" \
  "s_add_u32 m0, s86, 136192\n" \
  "s_add_u32 s92, s92, 65536\n" \
  "buffer_load_dwordx4 v9, s[68:71], s92 offen lds\n" \
  "s_add_u32 m0, s86, 137216\n" \
  "s_add_u32 s92, s92, 65536\n" \
  "buffer_load_dwordx4 v8, s[68:71], s92 offen lds\n" \
  "s_add_u32 m0, s86, 138240\n" \
  "s_add_u32 s92, s92, 65536\n" \
  "buffer_load_dwordx4 v9, s[68:71], s92 offen lds\n" \
  "ds_read_b64_tr_b16 a[224:225], v118 offset:0\n" \
  "ds_read_b64_tr_b16 a[226:227], v118 offset:512\n" \
  "ds_read_b64_tr_b16 a[228:229], v118 offset:4096\n" \
  "ds_read_b64_tr_b16 a[230:231], v118 offset:4608\n" \
  "ds_read_b64_tr_b16 a[232:233], v118 offset:8192\n" \
  "ds_read_b64_tr_b16 a[234:235], v118 offset:8704\n" \
  "ds_read_b64_tr_b16 a[236:237], v118 offset:12288\n" \
  "ds_read_b64_tr_b16 a[238:239], v118 offset:12800\n" \
  "ds_read_b64_tr_b16 a[240:241], v118 offset:16384\n" \
  "ds_read_b64_tr_b16 a[242:243], v118 offset:16896\n" \
  "ds_read_b64_tr_b16 a[244:245], v118 offset:20480\n" \
  "ds_read_b64_tr_b16 a[246:247], v118 offset:20992\n" \
  "ds_read_b64_tr_b16 a[248:249], v118 offset:24576\n" \
  "ds_read_b64_tr_b16 a[250:251], v118 offset:25088\n" \
  "ds_read_b64_tr_b16 a[252:253], v118 offset:28672\n" \
  "ds_read_b64_tr_b16 a[254:255], v118 offset:29184\n" \
  "s_waitcnt vmcnt(25) lgkmcnt(0)\n" \
  "s_barrier\n" \
  "s_add_u32 m0, s91, 0\n" \
  "s_nop 0\n" \
  "buffer_load_dwordx4 v7, s[72:75], s87 offen lds\n" \
  "s_add_u32 s87, s87, 128\n" \
  "s_add_u32 m0, s91, 4096\n" \
  "s_nop 0\n" \
  "buffer_load_dwordx4 v7, s[72:75], s87 offen lds\n" \
  "s_add_u32 s87, s87, 128\n" \
  "s_add_u32 m0, s91, 8192\n" \
  "s_nop 0\n" \
  "buffer_load_dwordx4 v7, s[72:75], s87 offen lds\n" \
  "s_add_u32 s87, s87, 128\n" \
  "s_add_u32 m0, s91, 12288\n" \
  "s_nop 0\n" \
  "buffer_load_dwordx4 v7, s[72:75], s87 offen lds\n" \
  "s_add_u32 s87, s87, 128\n" \
  "s_add_u32 m0, s91, 16384\n" \
  "s_nop 0\n" \
  "buffer_load_dwordx4 v7, s[72:75], s87 offen lds\n" \
  "s_add_u32 s87, s87, 128\n" \
  "s_add_u32 m0, s91, 20480\n" \
  "s_nop 0\n" \
  "buffer_load_dwordx4 v7, s[72:75], s87 offen lds\n" \
  "s_add_u32 s87, s87, 128\n" \
  "s_add_u32 m0, s91, 24576\n" \
  "s_nop 0\n" \
  "buffer_load_dwordx4 v7, s[72:75], s87 offen lds\n" \
  "s_add_u32 s87, s87, 128\n" \
  "s_add_u32 m0, s91, 28672\n" \
  "s_nop 0\n" \
  "buffer_load_dwordx4 v7, s[72:75], s87 offen lds\n" \
  "s_add_u32 s87, s87, 128\n" \
  "ds_read_b64_tr_b16 v[128:129], v118 offset:32768\n" \
  "ds_read_b64_tr_b16 v[130:131], v118 offset:33280\n" \
  "ds_read_b64_tr_b16 v[132:133], v118 offset:36864\n" \
  "ds_read_b64_tr_b16 v[134:135], v118 offset:37376\n" \
  "ds_read_b64_tr_b16 v[136:137], v118 offset:40960\n" \
  "ds_read_b64_tr_b16 v[138:139], v118 offset:41472\n" \
  "ds_read_b64_tr_b16 v[140:141], v118 offset:45056\n" \
  "ds_read_b64_tr_b16 v[142:143], v118 offset:45568\n" \
  "ds_read_b64_tr_b16 v[144:145], v118 offset:49152\n" \
  "ds_read_b64_tr_b16 v[146:147], v118 offset:49664\n" \
  "ds_read_b64_tr_b16 v[148:149], v118 offset:53248\n" \
  "ds_read_b64_tr_b16 v[150:151], v118 offset:53760\n" \
  "ds_read_b64_tr_b16 v[152:153], v118 offset:57344\n" \
  "ds_read_b64_tr_b16 v[154:155], v118 offset:57856\n" \
  "ds_read_b64_tr_b16 v[156:157], v118 offset:61440\n" \
  "ds_read_b64_tr_b16 v[158:159], v118 offset:61952\n" \
  "s_waitcnt vmcnt(25) lgkmcnt(0)\n" \
  "s_barrier\n" \
  "s_add_u32 m0, s91, 32768\n" \
  "s_nop 0\n" \
  "buffer_load_dwordx4 v7, s[72:75], s87 offen lds\n" \
  "s_add_u32 s87, s87, 128\n" \
  "s_add_u32 m0, s91, 36864\n" \
  "s_nop 0\n" \
  "buffer_load_dwordx4 v7, s[72:75], s87 offen lds\n" \
  "s_add_u32 s87, s87, 128\n" \
  "s_add_u32 m0, s91, 40960\n" \
  "s_nop 0\n" \
  "buffer_load_dwordx4 v7, s[72:75], s87 offen lds\n" \
  "s_add_u32 s87, s87, 128\n" \
  "s_add_u32 m0, s91, 45056\n" \
  "s_nop 0\n" \
  "buffer_load_dwordx4 v7, s[72:75], s87 offen lds\n" \
  "s_add_u32 s87, s87, 128\n" \
  "s_add_u32 m0, s91, 49152\n" \
  "s_nop 0\n" \
  "buffer_load_dwordx4 v7, s[72:75], s87 offen lds\n" \
  "s_add_u32 s87, s87, 128\n" \
  "s_add_u32 m0, s91, 53248\n" \
  "s_nop 0\n" \
  "buffer_load_dwordx4 v7, s[72:75], s87 offen lds\n" \
  "s_add_u32 s87, s87, 128\n" \
  "s_add_u32 m0, s91, 57344\n" \
  "s_nop 0\n" \
  "buffer_load_dwordx4 v7, s[72:75], s87 offen lds\n" \
  "s_add_u32 s87, s87, 128\n" \
  "s_add_u32 m0, s91, 61440\n" \
  "s_nop 0\n" \
  "buffer_load_dwordx4 v7, s[72:75], s87 offen lds\n" \
  "s_add_u32 s87, s87, 63616\n" \
  "s_min_u32 s87, s87, s96\n" \
  "ds_read_b64_tr_b16 v[160:161], v119 offset:0\n" \
  "ds_read_b64_tr_b16 v[162:163], v119 offset:512\n" \
  "ds_read_b64_tr_b16 v[164:165], v119 offset:4096\n" \
  "ds_read_b64_tr_b16 v[166:167], v119 offset:4608\n" \
  "ds_read_b64_tr_b16 v[168:169], v119 offset:8192\n" \
  "ds_read_b64_tr_b16 v[170:171], v119 offset:8704\n" \
  "ds_read_b64_tr_b16 v[172:173], v119 offset:12288\n" \
  "ds_read_b64_tr_b16 v[174:175], v119 offset:12800\n" \
  "ds_read_b64_tr_b16 v[176:177], v119 offset:16384\n" \
  "ds_read_b64_tr_b16 v[178:179], v119 offset:16896\n" \
  "ds_read_b64_tr_b16 v[180:181], v119 offset:20480\n" \
  "ds_read_b64_tr_b16 v[182:183], v119 offset:20992\n" \
  "ds_read_b64_tr_b16 v[184:185], v119 offset:24576\n" \
  "ds_read_b64_tr_b16 v[186:187], v119 offset:25088\n" \
  "ds_read_b64_tr_b16 v[188:189], v119 offset:28672\n" \
  "ds_read_b64_tr_b16 v[190:191], v119 offset:29184\n" \
  "s_waitcnt vmcnt(25) lgkmcnt(0)\n" \
  "s_barrier\n" \
  "s_add_u32 m0, s91, 65536\n" \
  "s_nop 0\n" \
  "buffer_load_dwordx4 v7, s[72:75], s87 offen lds\n" \
  "s_add_u32 s87, s87, 128\n" \
  "s_add_u32 m0, s91, 69632\n" \
  "s_nop 0\n" \
  "buffer_load_dwordx4 v7, s[72:75], s87 offen lds\n" \
  "s_add_u32 s87, s87, 128\n" \
  "s_add_u32 m0, s91, 73728\n" \
  "s_nop 0\n" \
  "buffer_load_dwordx4 v7, s[72:75], s87 offen lds\n" \
  "s_add_u32 s87, s87, 128\n" \
  "s_add_u32 m0, s91, 77824\n" \
  "s_nop 0\n" \
  "buffer_load_dwordx4 v7, s[72:75], s87 offen lds\n" \
  "s_add_u32 s87, s87, 128\n" \
  "s_add_u32 m0, s91, 81920\n" \
  "s_nop 0\n" \
  "buffer_load_dwordx4 v7, s[72:75], s87 offen lds\n" \
  "s_add_u32 s87, s87, 128\n" \
  "s_add_u32 m0, s91, 86016\n" \
  "s_nop 0\n" \
  "buffer_load_dwordx4 v7, s[72:75], s87 offen lds\n" \
  "s_add_u32 s87, s87, 128\n" \
  "s_add_u32 m0, s91, 90112\n" \
  "s_nop 0\n" \
  "buffer_load_dwordx4 v7, s[72:75], s87 offen lds\n" \
  "s_add_u32 s87, s87, 128\n" \
  "s_add_u32 m0, s91, 94208\n" \
  "s_nop 0\n" \
  "buffer_load_dwordx4 v7, s[72:75], s87 offen lds\n" \
  "s_add_u32 s87, s87, 128\n" \
  "ds_read_b64_tr_b16 v[192:193], v119 offset:32768\n" \
  "ds_read_b64_tr_b16 v[194:195], v119 offset:33280\n" \
  "ds_read_b64_tr_b16 v[196:197], v119 offset:36864\n" \
  "ds_read_b64_tr_b16 v[198:199], v119 offset:37376\n" \
  "ds_read_b64_tr_b16 v[200:201], v119 offset:40960\n" \
  "ds_read_b64_tr_b16 v[202:203], v119 offset:41472\n" \
  "ds_read_b64_tr_b16 v[204:205], v119 offset:45056\n" \
  "ds_read_b64_tr_b16 v[206:207], v119 offset:45568\n" \
  "ds_read_b64_tr_b16 v[208:209], v119 offset:49152\n" \
  "ds_read_b64_tr_b16 v[210:211], v119 offset:49664\n" \
  "ds_read_b64_tr_b16 v[212:213], v119 offset:53248\n" \
  "ds_read_b64_tr_b16 v[214:215], v119 offset:53760\n" \
  "ds_read_b64_tr_b16 v[216:217], v119 offset:57344\n" \
  "ds_read_b64_tr_b16 v[218:219], v119 offset:57856\n" \
  "ds_read_b64_tr_b16 v[220:221], v119 offset:61440\n" \
  "ds_read_b64_tr_b16 v[222:223], v119 offset:61952\n" \
  "s_waitcnt vmcnt(24) lgkmcnt(0)\n" \
  "s_barrier\n" \
  "ds_read_b64_tr_b16 v[224:225], v120 offset:0\n" \
  "ds_read_b64_tr_b16 v[226:227], v120 offset:512\n" \
  "ds_read_b64_tr_b16 v[228:229], v120 offset:4096\n" \
  "ds_read_b64_tr_b16 v[230:231], v120 offset:4608\n" \
  "ds_read_b64_tr_b16 v[232:233], v120 offset:8192\n" \
  "ds_read_b64_tr_b16 v[234:235], v120 offset:8704\n" \
  "ds_read_b64_tr_b16 v[236:237], v120 offset:12288\n" \
  "ds_read_b64_tr_b16 v[238:239], v120 offset:12800\n" \
  "ds_read_b64_tr_b16 v[240:241], v120 offset:16384\n" \
  "ds_read_b64_tr_b16 v[242:243], v120 offset:16896\n" \
  "ds_read_b64_tr_b16 v[244:245], v120 offset:20480\n" \
  "ds_read_b64_tr_b16 v[246:247], v120 offset:20992\n" \
  "ds_read_b64_tr_b16 v[248:249], v120 offset:24576\n" \
  "ds_read_b64_tr_b16 v[250:251], v120 offset:25088\n" \
  "ds_read_b64_tr_b16 v[252:253], v120 offset:28672\n" \
  "ds_read_b64_tr_b16 v[254:255], v120 offset:29184\n" \
  "s_mov_b64 s[68:69], %10\n" \
  "s_mov_b32 s70, 0\n" \
  "LR1_%=:\n" \
  "v_readfirstlane_b32 s84, v20\n" \
  "v_readfirstlane_b32 s85, v21\n" \
  "v_readfirstlane_b32 %0, v22\n" \
  "v_readfirstlane_b32 s92, v23\n" \
  "s_xor_b32 s84, s84, s68\n" \
  "s_xor_b32 s85, s85, s69\n" \
  "s_xor_b32 %0, %0, s68\n" \
  "s_xor_b32 s92, s92, s69\n" \
  "s_or_b32 s84, s84, s85\n" \
  "s_xor_b32 s92, s92, %0\n" \
  "s_or_b32 s84, s84, s92\n" \
  "s_cbranch_scc0 LR3_%=\n" \
  "s_sleep 16\n" \
  "s_lshl_b32 s84, %11, 7\n" \
  "buffer_load_dwordx4 v[20:23], off, s[80:83], s84 sc1\n" \
  "s_waitcnt vmcnt(0)\n" \
  "s_add_u32 s70, s70, 1\n" \
  "s_cmp_lt_u32 s70, " STR(SPIN_CAP) "\n" \
  "s_cbranch_scc1 LR1_%=\n" \
  "LR3_%=:\n" \
  "s_lshr_b32 %0, %0, 13\n" \
  "s_waitcnt vmcnt(8) lgkmcnt(0)\n" \
  "s_barrier\n" \
  "ds_read_b128 v[68:71], v10 offset:0\n" \
  "ds_read_b128 v[72:75], v10 offset:2048\n" \
  "ds_read_b128 v[76:79], v11 offset:0\n" \
  "ds_read_b128 v[80:83], v11 offset:2048\n" \
  "ds_read_b128 v[84:87], v10 offset:4096\n" \
  "ds_read_b128 v[88:91], v10 offset:6144\n" \
  "s_mov_b32 s85, 0\n" \
  ".p2align 6\n" \
  "LG_%=:\n" \
  "s_cmp_eq_u32 s85, 64\n" \
  "s_cbranch_scc1 LX0_%=\n" \
  "s_waitcnt vmcnt(8)\n" \
  "s_barrier\n" \
  "s_waitcnt lgkmcnt(4)\n" \
  "v_mfma_f32_16x16x32_bf16 v[20:23], v[128:131], v[68:71], 0\n" \
  "ds_read_b128 v[92:95], v11 offset:4096\n" \
  "s_sub_u32 s89, s85, 1\n" \
  "s_and_b32 s89, s89, 63\n" \
  "v_mfma_f32_16x16x32_bf16 v[28:31], a[0:3], v[68:71], 0\n" \
  "ds_read_b128 v[96:99], v11 offset:6144\n" \
  "s_lshr_b32 s84, s89, 2\n" \
  "s_xnor_b32 s84, s84, %0\n" \
  "v_mfma_f32_16x16x32_bf16 v[36:39], v[68:71], a[128:131], 0\n" \
  "s_and_b32 s84, s84, 1\n" \
  "s_lshl_b32 s84, s84, 13\n" \
  "v_mfma_f32_16x16x32_bf16 v[24:27], v[128:131], v[72:75], 0\n" \
  "s_lshl_b32 s88, s89, 19\n" \
  "s_or_b32 s88, s88, s84\n" \
  "v_mfma_f32_16x16x32_bf16 v[32:35], a[0:3], v[72:75], 0\n" \
  "s_add_u32 s89, s88, " STR(QKV_SZ32) "\n" \
  "s_add_u32 s90, s89, " STR(QKV_SZ32) "\n" \
  "v_mfma_f32_16x16x32_bf16 v[40:43], v[72:75], a[128:131], 0\n" \
  "s_add_u32 s85, s85, 1\n" \
  "s_waitcnt lgkmcnt(4)\n" \
  "v_mfma_f32_16x16x32_bf16 v[20:23], v[132:135], v[76:79], v[20:23]\n" \
  "ds_read_b128 v[68:71], v10 offset:8192\n" \
  "v_cvt_pk_bf16_f32 v116, v44, v45\n" \
  "v_mfma_f32_16x16x32_bf16 v[28:31], a[4:7], v[76:79], v[28:31]\n" \
  "ds_read_b128 v[72:75], v10 offset:10240\n" \
  "v_cvt_pk_bf16_f32 v117, v46, v47\n" \
  "v_mfma_f32_16x16x32_bf16 v[36:39], v[76:79], a[132:135], v[36:39]\n" \
  "v_cvt_pk_bf16_f32 v118, v48, v49\n" \
  "v_mfma_f32_16x16x32_bf16 v[24:27], v[132:135], v[80:83], v[24:27]\n" \
  "v_cvt_pk_bf16_f32 v119, v50, v51\n" \
  "v_mfma_f32_16x16x32_bf16 v[32:35], a[4:7], v[80:83], v[32:35]\n" \
  "v_cvt_pk_bf16_f32 v120, v52, v53\n" \
  "v_mfma_f32_16x16x32_bf16 v[40:43], v[80:83], a[132:135], v[40:43]\n" \
  "v_cvt_pk_bf16_f32 v121, v54, v55\n" \
  "s_waitcnt lgkmcnt(4)\n" \
  "v_mfma_f32_16x16x32_bf16 v[20:23], v[136:139], v[84:87], v[20:23]\n" \
  "ds_read_b128 v[76:79], v11 offset:8192\n" \
  "v_cvt_pk_bf16_f32 v122, v56, v57\n" \
  "v_mfma_f32_16x16x32_bf16 v[28:31], a[8:11], v[84:87], v[28:31]\n" \
  "ds_read_b128 v[80:83], v11 offset:10240\n" \
  "v_cvt_pk_bf16_f32 v123, v58, v59\n" \
  "v_mfma_f32_16x16x32_bf16 v[36:39], v[84:87], a[136:139], v[36:39]\n" \
  "v_cvt_pk_bf16_f32 v124, v60, v61\n" \
  "v_mfma_f32_16x16x32_bf16 v[24:27], v[136:139], v[88:91], v[24:27]\n" \
  "v_cvt_pk_bf16_f32 v125, v62, v63\n" \
  "v_mfma_f32_16x16x32_bf16 v[32:35], a[8:11], v[88:91], v[32:35]\n" \
  "v_cvt_pk_bf16_f32 v126, v64, v65\n" \
  "v_mfma_f32_16x16x32_bf16 v[40:43], v[88:91], a[136:139], v[40:43]\n" \
  "v_cvt_pk_bf16_f32 v127, v66, v67\n" \
  "s_waitcnt lgkmcnt(4)\n" \
  "v_mfma_f32_16x16x32_bf16 v[20:23], v[140:143], v[92:95], v[20:23]\n" \
  "ds_read_b128 v[84:87], v10 offset:12288\n" \
  "v_permlane16_swap_b32 v116, v118\n" \
  "v_mfma_f32_16x16x32_bf16 v[28:31], a[12:15], v[92:95], v[28:31]\n" \
  "ds_read_b128 v[88:91], v10 offset:14336\n" \
  "v_permlane16_swap_b32 v117, v119\n" \
  "v_mfma_f32_16x16x32_bf16 v[36:39], v[92:95], a[140:143], v[36:39]\n" \
  "v_permlane16_swap_b32 v120, v122\n" \
  "v_mfma_f32_16x16x32_bf16 v[24:27], v[140:143], v[96:99], v[24:27]\n" \
  "v_permlane16_swap_b32 v121, v123\n" \
  "v_mfma_f32_16x16x32_bf16 v[32:35], a[12:15], v[96:99], v[32:35]\n" \
  "v_mfma_f32_16x16x32_bf16 v[40:43], v[96:99], a[140:143], v[40:43]\n" \
  "s_waitcnt lgkmcnt(4)\n" \
  "v_mfma_f32_16x16x32_bf16 v[20:23], v[144:147], v[68:71], v[20:23]\n" \
  "ds_read_b128 v[92:95], v11 offset:12288\n" \
  "buffer_store_dwordx4 v[116:119], v14, s[76:79], s88 offen sc1\n" \
  "v_mfma_f32_16x16x32_bf16 v[28:31], a[16:19], v[68:71], v[28:31]\n" \
  "ds_read_b128 v[96:99], v11 offset:14336\n" \
  "buffer_store_dwordx4 v[120:123], v18, s[76:79], s89 offen sc1\n" \
  "v_mfma_f32_16x16x32_bf16 v[36:39], v[68:71], a[144:147], v[36:39]\n" \
  "buffer_store_dwordx4 v[124:127], v15, s[76:79], s90 offen sc1\n" \
  "v_mfma_f32_16x16x32_bf16 v[24:27], v[144:147], v[72:75], v[24:27]\n" \
  "v_mfma_f32_16x16x32_bf16 v[32:35], a[16:19], v[72:75], v[32:35]\n" \
  "v_mfma_f32_16x16x32_bf16 v[40:43], v[72:75], a[144:147], v[40:43]\n" \
  "s_waitcnt lgkmcnt(4)\n" \
  "v_mfma_f32_16x16x32_bf16 v[20:23], v[148:151], v[76:79], v[20:23]\n" \
  "ds_read_b128 v[68:71], v10 offset:16384\n" \
  "v_mfma_f32_16x16x32_bf16 v[28:31], a[20:23], v[76:79], v[28:31]\n" \
  "ds_read_b128 v[72:75], v10 offset:18432\n" \
  "v_mfma_f32_16x16x32_bf16 v[36:39], v[76:79], a[148:151], v[36:39]\n" \
  "v_mfma_f32_16x16x32_bf16 v[24:27], v[148:151], v[80:83], v[24:27]\n" \
  "s_add_u32 m0, s91, 98304\n" \
  "s_nop 0\n" \
  "buffer_load_dwordx4 v7, s[72:75], s87 offen lds\n" \
  "s_add_u32 s87, s87, 128\n" \
  "v_mfma_f32_16x16x32_bf16 v[32:35], a[20:23], v[80:83], v[32:35]\n" \
  "v_mfma_f32_16x16x32_bf16 v[40:43], v[80:83], a[148:151], v[40:43]\n" \
  "s_waitcnt lgkmcnt(4)\n" \
  "v_mfma_f32_16x16x32_bf16 v[20:23], v[152:155], v[84:87], v[20:23]\n" \
  "ds_read_b128 v[76:79], v11 offset:16384\n" \
  "v_mfma_f32_16x16x32_bf16 v[28:31], a[24:27], v[84:87], v[28:31]\n" \
  "ds_read_b128 v[80:83], v11 offset:18432\n" \
  "v_mfma_f32_16x16x32_bf16 v[36:39], v[84:87], a[152:155], v[36:39]\n" \
  "v_mfma_f32_16x16x32_bf16 v[24:27], v[152:155], v[88:91], v[24:27]\n" \
  "s_add_u32 m0, s91, 102400\n" \
  "s_nop 0\n" \
  "buffer_load_dwordx4 v7, s[72:75], s87 offen lds\n" \
  "s_add_u32 s87, s87, 128\n" \
  "v_mfma_f32_16x16x32_bf16 v[32:35], a[24:27], v[88:91], v[32:35]\n" \
  "v_mfma_f32_16x16x32_bf16 v[40:43], v[88:91], a[152:155], v[40:43]\n" \
  "s_waitcnt lgkmcnt(4)\n" \
  "v_mfma_f32_16x16x32_bf16 v[20:23], v[156:159], v[92:95], v[20:23]\n" \
  "ds_read_b128 v[84:87], v10 offset:20480\n" \
  "v_mfma_f32_16x16x32_bf16 v[28:31], a[28:31], v[92:95], v[28:31]\n" \
  "ds_read_b128 v[88:91], v10 offset:22528\n" \
  "v_mfma_f32_16x16x32_bf16 v[36:39], v[92:95], a[156:159], v[36:39]\n" \
  "v_mfma_f32_16x16x32_bf16 v[24:27], v[156:159], v[96:99], v[24:27]\n" \
  "s_add_u32 m0, s91, 106496\n" \
  "s_nop 0\n" \
  "buffer_load_dwordx4 v7, s[72:75], s87 offen lds\n" \
  "s_add_u32 s87, s87, 128\n" \
  "v_mfma_f32_16x16x32_bf16 v[32:35], a[28:31], v[96:99], v[32:35]\n" \
  "v_mfma_f32_16x16x32_bf16 v[40:43], v[96:99], a[156:159], v[40:43]\n" \
  "s_waitcnt lgkmcnt(4)\n" \
  "v_mfma_f32_16x16x32_bf16 v[20:23], v[160:163], v[68:71], v[20:23]\n" \
  "ds_read_b128 v[92:95], v11 offset:20480\n" \
  "v_mfma_f32_16x16x32_bf16 v[28:31], a[32:35], v[68:71], v[28:31]\n" \
  "ds_read_b128 v[96:99], v11 offset:22528\n" \
  "v_mfma_f32_16x16x32_bf16 v[36:39], v[68:71], a[160:163], v[36:39]\n" \
  "v_mfma_f32_16x16x32_bf16 v[24:27], v[160:163], v[72:75], v[24:27]\n" \
  "s_add_u32 m0, s91, 110592\n" \
  "s_nop 0\n" \
  "buffer_load_dwordx4 v7, s[72:75], s87 offen lds\n" \
  "s_add_u32 s87, s87, 128\n" \
  "v_mfma_f32_16x16x32_bf16 v[32:35], a[32:35], v[72:75], v[32:35]\n" \
  "v_mfma_f32_16x16x32_bf16 v[40:43], v[72:75], a[160:163], v[40:43]\n" \
  "s_waitcnt lgkmcnt(4)\n" \
  "v_mfma_f32_16x16x32_bf16 v[20:23], v[164:167], v[76:79], v[20:23]\n" \
  "ds_read_b128 v[68:71], v10 offset:24576\n" \
  "v_mfma_f32_16x16x32_bf16 v[28:31], a[36:39], v[76:79], v[28:31]\n" \
  "ds_read_b128 v[72:75], v10 offset:26624\n" \
  "v_mfma_f32_16x16x32_bf16 v[36:39], v[76:79], a[164:167], v[36:39]\n" \
  "v_mfma_f32_16x16x32_bf16 v[24:27], v[164:167], v[80:83], v[24:27]\n" \
  "s_add_u32 m0, s91, 114688\n" \
  "s_nop 0\n" \
  "buffer_load_dwordx4 v7, s[72:75], s87 offen lds\n" \
  "s_add_u32 s87, s87, 128\n" \
  "v_mfma_f32_16x16x32_bf16 v[32:35], a[36:39], v[80:83], v[32:35]\n" \
  "v_mfma_f32_16x16x32_bf16 v[40:43], v[80:83], a[164:167], v[40:43]\n" \
  "s_waitcnt lgkmcnt(4)\n" \
  "v_mfma_f32_16x16x32_bf16 v[20:23], v[168:171], v[84:87], v[20:23]\n" \
  "ds_read_b128 v[76:79], v11 offset:24576\n" \
  "v_mfma_f32_16x16x32_bf16 v[28:31], a[40:43], v[84:87], v[28:31]\n" \
  "ds_read_b128 v[80:83], v11 offset:26624\n" \
  "v_mfma_f32_16x16x32_bf16 v[36:39], v[84:87], a[168:171], v[36:39]\n" \
  "v_mfma_f32_16x16x32_bf16 v[24:27], v[168:171], v[88:91], v[24:27]\n" \
  "s_add_u32 m0, s91, 118784\n" \
  "s_nop 0\n" \
  "buffer_load_dwordx4 v7, s[72:75], s87 offen lds\n" \
  "s_add_u32 s87, s87, 128\n" \
  "v_mfma_f32_16x16x32_bf16 v[32:35], a[40:43], v[88:91], v[32:35]\n" \
  "v_mfma_f32_16x16x32_bf16 v[40:43], v[88:91], a[168:171], v[40:43]\n" \
  "s_waitcnt lgkmcnt(4)\n" \
  "v_mfma_f32_16x16x32_bf16 v[20:23], v[172:175], v[92:95], v[20:23]\n" \
  "ds_read_b128 v[84:87], v10 offset:28672\n" \
  "v_mfma_f32_16x16x32_bf16 v[28:31], a[44:47], v[92:95], v[28:31]\n" \
  "ds_read_b128 v[88:91], v10 offset:30720\n" \
  "v_mfma_f32_16x16x32_bf16 v[36:39], v[92:95], a[172:175], v[36:39]\n" \
  "v_mfma_f32_16x16x32_bf16 v[24:27], v[172:175], v[96:99], v[24:27]\n" \
  "s_add_u32 m0, s91, 122880\n" \
  "s_nop 0\n" \
  "buffer_load_dwordx4 v7, s[72:75], s87 offen lds\n" \
  "s_add_u32 s87, s87, 128\n" \
  "v_mfma_f32_16x16x32_bf16 v[32:35], a[44:47], v[96:99], v[32:35]\n" \
  "v_mfma_f32_16x16x32_bf16 v[40:43], v[96:99], a[172:175], v[40:43]\n" \
  "s_waitcnt lgkmcnt(4)\n" \
  "v_mfma_f32_16x16x32_bf16 v[20:23], v[176:179], v[68:71], v[20:23]\n" \
  "ds_read_b128 v[92:95], v11 offset:28672\n" \
  "v_mfma_f32_16x16x32_bf16 v[28:31], a[48:51], v[68:71], v[28:31]\n" \
  "ds_read_b128 v[96:99], v11 offset:30720\n" \
  "v_mfma_f32_16x16x32_bf16 v[36:39], v[68:71], a[176:179], v[36:39]\n" \
  "v_mfma_f32_16x16x32_bf16 v[24:27], v[176:179], v[72:75], v[24:27]\n" \
  "s_add_u32 m0, s91, 126976\n" \
  "s_nop 0\n" \
  "buffer_load_dwordx4 v7, s[72:75], s87 offen lds\n" \
  "s_add_u32 s87, s87, 63616\n" \
  "s_min_u32 s87, s87, s96\n" \
  "v_mfma_f32_16x16x32_bf16 v[32:35], a[48:51], v[72:75], v[32:35]\n" \
  "v_mfma_f32_16x16x32_bf16 v[40:43], v[72:75], a[176:179], v[40:43]\n" \
  "s_waitcnt lgkmcnt(4)\n" \
  "v_mfma_f32_16x16x32_bf16 v[20:23], v[180:183], v[76:79], v[20:23]\n" \
  "ds_read_b128 v[68:71], v10 offset:32768\n" \
  "v_mfma_f32_16x16x32_bf16 v[28:31], a[52:55], v[76:79], v[28:31]\n" \
  "ds_read_b128 v[72:75], v10 offset:34816\n" \
  "v_mfma_f32_16x16x32_bf16 v[36:39], v[76:79], a[180:183], v[36:39]\n" \
  "v_mfma_f32_16x16x32_bf16 v[24:27], v[180:183], v[80:83], v[24:27]\n" \
  "v_mfma_f32_16x16x32_bf16 v[32:35], a[52:55], v[80:83], v[32:35]\n" \
  "v_mfma_f32_16x16x32_bf16 v[40:43], v[80:83], a[180:183], v[40:43]\n" \
  "s_waitcnt lgkmcnt(4)\n" \
  "v_mfma_f32_16x16x32_bf16 v[20:23], v[184:187], v[84:87], v[20:23]\n" \
  "ds_read_b128 v[76:79], v11 offset:32768\n" \
  "v_mfma_f32_16x16x32_bf16 v[28:31], a[56:59], v[84:87], v[28:31]\n" \
  "ds_read_b128 v[80:83], v11 offset:34816\n" \
  "v_mfma_f32_16x16x32_bf16 v[36:39], v[84:87], a[184:187], v[36:39]\n" \
  "v_mfma_f32_16x16x32_bf16 v[24:27], v[184:187], v[88:91], v[24:27]\n" \
  "v_mfma_f32_16x16x32_bf16 v[32:35], a[56:59], v[88:91], v[32:35]\n" \
  "v_mfma_f32_16x16x32_bf16 v[40:43], v[88:91], a[184:187], v[40:43]\n" \
  "s_waitcnt lgkmcnt(4)\n" \
  "v_mfma_f32_16x16x32_bf16 v[20:23], v[188:191], v[92:95], v[20:23]\n" \
  "ds_read_b128 v[84:87], v10 offset:36864\n" \
  "v_mfma_f32_16x16x32_bf16 v[28:31], a[60:63], v[92:95], v[28:31]\n" \
  "ds_read_b128 v[88:91], v10 offset:38912\n" \
  "v_mfma_f32_16x16x32_bf16 v[36:39], v[92:95], a[188:191], v[36:39]\n" \
  "v_mfma_f32_16x16x32_bf16 v[24:27], v[188:191], v[96:99], v[24:27]\n" \
  "v_mfma_f32_16x16x32_bf16 v[32:35], a[60:63], v[96:99], v[32:35]\n" \
  "v_mfma_f32_16x16x32_bf16 v[40:43], v[96:99], a[188:191], v[40:43]\n" \
  "s_waitcnt vmcnt(8)\n" \
  "s_barrier\n" \
  "s_waitcnt lgkmcnt(4)\n" \
  "v_mfma_f32_16x16x32_bf16 v[20:23], v[192:195], v[68:71], v[20:23]\n" \
  "ds_read_b128 v[92:95], v11 offset:36864\n" \
  "buffer_store_dword v17, v16, s[80:83], 0 offen sc1\n" \
  "v_mfma_f32_16x16x32_bf16 v[28:31], a[64:67], v[68:71], v[28:31]\n" \
  "ds_read_b128 v[96:99], v11 offset:38912\n" \
  "v_add_u32 v17, 1, v17\n" \
  "v_mfma_f32_16x16x32_bf16 v[36:39], v[68:71], a[192:195], v[36:39]\n" \
  "v_and_b32 v14, 0x7fffffff, v14\n" \
  "v_mfma_f32_16x16x32_bf16 v[24:27], v[192:195], v[72:75], v[24:27]\n" \
  "v_and_b32 v15, 0x7fffffff, v15\n" \
  "v_mfma_f32_16x16x32_bf16 v[32:35], a[64:67], v[72:75], v[32:35]\n" \
  "v_and_b32 v18, 0x7fffffff, v18\n" \
  "v_mfma_f32_16x16x32_bf16 v[40:43], v[72:75], a[192:195], v[40:43]\n" \
  "s_waitcnt lgkmcnt(4)\n" \
  "v_mfma_f32_16x16x32_bf16 v[20:23], v[196:199], v[76:79], v[20:23]\n" \
  "ds_read_b128 v[68:71], v10 offset:40960\n" \
  "v_mfma_f32_16x16x32_bf16 v[28:31], a[68:71], v[76:79], v[28:31]\n" \
  "ds_read_b128 v[72:75], v10 offset:43008\n" \
  "v_mfma_f32_16x16x32_bf16 v[36:39], v[76:79], a[196:199], v[36:39]\n" \
  "v_mfma_f32_16x16x32_bf16 v[24:27], v[196:199], v[80:83], v[24:27]\n" \
  "v_mfma_f32_16x16x32_bf16 v[32:35], a[68:71], v[80:83], v[32:35]\n" \
  "v_mfma_f32_16x16x32_bf16 v[40:43], v[80:83], a[196:199], v[40:43]\n" \
  "s_waitcnt lgkmcnt(4)\n" \
  "v_mfma_f32_16x16x32_bf16 v[20:23], v[200:203], v[84:87], v[20:23]\n" \
  "ds_read_b128 v[76:79], v11 offset:40960\n" \
  "v_mfma_f32_16x16x32_bf16 v[28:31], a[72:75], v[84:87], v[28:31]\n" \
  "ds_read_b128 v[80:83], v11 offset:43008\n" \
  "v_mfma_f32_16x16x32_bf16 v[36:39], v[84:87], a[200:203], v[36:39]\n" \
  "v_mfma_f32_16x16x32_bf16 v[24:27], v[200:203], v[88:91], v[24:27]\n" \
  "v_mfma_f32_16x16x32_bf16 v[32:35], a[72:75], v[88:91], v[32:35]\n" \
  "v_mfma_f32_16x16x32_bf16 v[40:43], v[88:91], a[200:203], v[40:43]\n" \
  "s_waitcnt lgkmcnt(4)\n" \
  "v_mfma_f32_16x16x32_bf16 v[20:23], v[204:207], v[92:95], v[20:23]\n" \
  "ds_read_b128 v[84:87], v10 offset:45056\n" \
  "v_mfma_f32_16x16x32_bf16 v[28:31], a[76:79], v[92:95], v[28:31]\n" \
  "ds_read_b128 v[88:91], v10 offset:47104\n" \
  "v_mfma_f32_16x16x32_bf16 v[36:39], v[92:95], a[204:207], v[36:39]\n" \
  "v_mfma_f32_16x16x32_bf16 v[24:27], v[204:207], v[96:99], v[24:27]\n" \
  "v_mfma_f32_16x16x32_bf16 v[32:35], a[76:79], v[96:99], v[32:35]\n" \
  "v_mfma_f32_16x16x32_bf16 v[40:43], v[96:99], a[204:207], v[40:43]\n" \
  "s_waitcnt lgkmcnt(4)\n" \
  "v_mfma_f32_16x16x32_bf16 v[20:23], v[208:211], v[68:71], v[20:23]\n" \
  "ds_read_b128 v[92:95], v11 offset:45056\n" \
  "v_mfma_f32_16x16x32_bf16 v[28:31], a[80:83], v[68:71], v[28:31]\n" \
  "ds_read_b128 v[96:99], v11 offset:47104\n" \
  "v_mfma_f32_16x16x32_bf16 v[36:39], v[68:71], a[208:211], v[36:39]\n" \
  "v_mfma_f32_16x16x32_bf16 v[24:27], v[208:211], v[72:75], v[24:27]\n" \
  "v_mfma_f32_16x16x32_bf16 v[32:35], a[80:83], v[72:75], v[32:35]\n" \
  "v_mfma_f32_16x16x32_bf16 v[40:43], v[72:75], a[208:211], v[40:43]\n" \
  "s_waitcnt lgkmcnt(4)\n" \
  "v_mfma_f32_16x16x32_bf16 v[20:23], v[212:215], v[76:79], v[20:23]\n" \
  "ds_read_b128 v[68:71], v10 offset:49152\n" \
  "v_mfma_f32_16x16x32_bf16 v[28:31], a[84:87], v[76:79], v[28:31]\n" \
  "ds_read_b128 v[72:75], v10 offset:51200\n" \
  "v_mfma_f32_16x16x32_bf16 v[36:39], v[76:79], a[212:215], v[36:39]\n" \
  "v_mfma_f32_16x16x32_bf16 v[24:27], v[212:215], v[80:83], v[24:27]\n" \
  "s_add_u32 m0, s91, 0\n" \
  "s_nop 0\n" \
  "buffer_load_dwordx4 v7, s[72:75], s87 offen lds\n" \
  "s_add_u32 s87, s87, 128\n" \
  "v_mfma_f32_16x16x32_bf16 v[32:35], a[84:87], v[80:83], v[32:35]\n" \
  "v_mfma_f32_16x16x32_bf16 v[40:43], v[80:83], a[212:215], v[40:43]\n" \
  "s_waitcnt lgkmcnt(4)\n" \
  "v_mfma_f32_16x16x32_bf16 v[20:23], v[216:219], v[84:87], v[20:23]\n" \
  "ds_read_b128 v[76:79], v11 offset:49152\n" \
  "v_mfma_f32_16x16x32_bf16 v[28:31], a[88:91], v[84:87], v[28:31]\n" \
  "ds_read_b128 v[80:83], v11 offset:51200\n" \
  "v_mfma_f32_16x16x32_bf16 v[36:39], v[84:87], a[216:219], v[36:39]\n" \
  "v_mfma_f32_16x16x32_bf16 v[24:27], v[216:219], v[88:91], v[24:27]\n" \
  "s_add_u32 m0, s91, 4096\n" \
  "s_nop 0\n" \
  "buffer_load_dwordx4 v7, s[72:75], s87 offen lds\n" \
  "s_add_u32 s87, s87, 128\n" \
  "v_mfma_f32_16x16x32_bf16 v[32:35], a[88:91], v[88:91], v[32:35]\n" \
  "v_mfma_f32_16x16x32_bf16 v[40:43], v[88:91], a[216:219], v[40:43]\n" \
  "s_waitcnt lgkmcnt(4)\n" \
  "v_mfma_f32_16x16x32_bf16 v[20:23], v[220:223], v[92:95], v[20:23]\n" \
  "ds_read_b128 v[84:87], v10 offset:53248\n" \
  "v_mfma_f32_16x16x32_bf16 v[28:31], a[92:95], v[92:95], v[28:31]\n" \
  "ds_read_b128 v[88:91], v10 offset:55296\n" \
  "v_mfma_f32_16x16x32_bf16 v[36:39], v[92:95], a[220:223], v[36:39]\n" \
  "v_mfma_f32_16x16x32_bf16 v[24:27], v[220:223], v[96:99], v[24:27]\n" \
  "s_add_u32 m0, s91, 8192\n" \
  "s_nop 0\n" \
  "buffer_load_dwordx4 v7, s[72:75], s87 offen lds\n" \
  "s_add_u32 s87, s87, 128\n" \
  "v_mfma_f32_16x16x32_bf16 v[32:35], a[92:95], v[96:99], v[32:35]\n" \
  "v_mfma_f32_16x16x32_bf16 v[40:43], v[96:99], a[220:223], v[40:43]\n" \
  "s_waitcnt lgkmcnt(4)\n" \
  "v_mfma_f32_16x16x32_bf16 v[20:23], v[224:227], v[68:71], v[20:23]\n" \
  "ds_read_b128 v[92:95], v11 offset:53248\n" \
  "v_mfma_f32_16x16x32_bf16 v[28:31], a[96:99], v[68:71], v[28:31]\n" \
  "ds_read_b128 v[96:99], v11 offset:55296\n" \
  "v_mfma_f32_16x16x32_bf16 v[36:39], v[68:71], a[224:227], v[36:39]\n" \
  "v_mfma_f32_16x16x32_bf16 v[24:27], v[224:227], v[72:75], v[24:27]\n" \
  "s_add_u32 m0, s91, 12288\n" \
  "s_nop 0\n" \
  "buffer_load_dwordx4 v7, s[72:75], s87 offen lds\n" \
  "s_add_u32 s87, s87, 128\n" \
  "v_mfma_f32_16x16x32_bf16 v[32:35], a[96:99], v[72:75], v[32:35]\n" \
  "v_mfma_f32_16x16x32_bf16 v[40:43], v[72:75], a[224:227], v[40:43]\n" \
  "s_waitcnt lgkmcnt(4)\n" \
  "v_mfma_f32_16x16x32_bf16 v[20:23], v[228:231], v[76:79], v[20:23]\n" \
  "ds_read_b128 v[68:71], v10 offset:57344\n" \
  "v_mfma_f32_16x16x32_bf16 v[28:31], a[100:103], v[76:79], v[28:31]\n" \
  "ds_read_b128 v[72:75], v10 offset:59392\n" \
  "v_mfma_f32_16x16x32_bf16 v[36:39], v[76:79], a[228:231], v[36:39]\n" \
  "v_mfma_f32_16x16x32_bf16 v[24:27], v[228:231], v[80:83], v[24:27]\n" \
  "s_add_u32 m0, s91, 16384\n" \
  "s_nop 0\n" \
  "buffer_load_dwordx4 v7, s[72:75], s87 offen lds\n" \
  "s_add_u32 s87, s87, 128\n" \
  "v_mfma_f32_16x16x32_bf16 v[32:35], a[100:103], v[80:83], v[32:35]\n" \
  "v_mfma_f32_16x16x32_bf16 v[40:43], v[80:83], a[228:231], v[40:43]\n" \
  "s_waitcnt lgkmcnt(4)\n" \
  "v_mfma_f32_16x16x32_bf16 v[20:23], v[232:235], v[84:87], v[20:23]\n" \
  "ds_read_b128 v[76:79], v11 offset:57344\n" \
  "v_mfma_f32_16x16x32_bf16 v[28:31], a[104:107], v[84:87], v[28:31]\n" \
  "ds_read_b128 v[80:83], v11 offset:59392\n" \
  "v_mfma_f32_16x16x32_bf16 v[36:39], v[84:87], a[232:235], v[36:39]\n" \
  "v_mfma_f32_16x16x32_bf16 v[24:27], v[232:235], v[88:91], v[24:27]\n" \
  "s_add_u32 m0, s91, 20480\n" \
  "s_nop 0\n" \
  "buffer_load_dwordx4 v7, s[72:75], s87 offen lds\n" \
  "s_add_u32 s87, s87, 128\n" \
  "v_mfma_f32_16x16x32_bf16 v[32:35], a[104:107], v[88:91], v[32:35]\n" \
  "v_mfma_f32_16x16x32_bf16 v[40:43], v[88:91], a[232:235], v[40:43]\n" \
  "s_waitcnt lgkmcnt(4)\n" \
  "v_mfma_f32_16x16x32_bf16 v[20:23], v[236:239], v[92:95], v[20:23]\n" \
  "ds_read_b128 v[84:87], v10 offset:61440\n" \
  "v_mfma_f32_16x16x32_bf16 v[28:31], a[108:111], v[92:95], v[28:31]\n" \
  "ds_read_b128 v[88:91], v10 offset:63488\n" \
  "v_mfma_f32_16x16x32_bf16 v[36:39], v[92:95], a[236:239], v[36:39]\n" \
  "v_mfma_f32_16x16x32_bf16 v[24:27], v[236:239], v[96:99], v[24:27]\n" \
  "s_add_u32 m0, s91, 24576\n" \
  "s_nop 0\n" \
  "buffer_load_dwordx4 v7, s[72:75], s87 offen lds\n" \
  "s_add_u32 s87, s87, 128\n" \
  "v_mfma_f32_16x16x32_bf16 v[32:35], a[108:111], v[96:99], v[32:35]\n" \
  "v_mfma_f32_16x16x32_bf16 v[40:43], v[96:99], a[236:239], v[40:43]\n" \
  "s_waitcnt lgkmcnt(4)\n" \
  "v_mfma_f32_16x16x32_bf16 v[20:23], v[240:243], v[68:71], v[20:23]\n" \
  "ds_read_b128 v[92:95], v11 offset:61440\n" \
  "v_mfma_f32_16x16x32_bf16 v[28:31], a[112:115], v[68:71], v[28:31]\n" \
  "ds_read_b128 v[96:99], v11 offset:63488\n" \
  "v_mfma_f32_16x16x32_bf16 v[36:39], v[68:71], a[240:243], v[36:39]\n" \
  "v_mfma_f32_16x16x32_bf16 v[24:27], v[240:243], v[72:75], v[24:27]\n" \
  "s_add_u32 m0, s91, 28672\n" \
  "s_nop 0\n" \
  "buffer_load_dwordx4 v7, s[72:75], s87 offen lds\n" \
  "s_add_u32 s87, s87, 128\n" \
  "v_mfma_f32_16x16x32_bf16 v[32:35], a[112:115], v[72:75], v[32:35]\n" \
  "v_mfma_f32_16x16x32_bf16 v[40:43], v[72:75], a[240:243], v[40:43]\n" \
  "s_waitcnt lgkmcnt(4)\n" \
  "v_mfma_f32_16x16x32_bf16 v[20:23], v[244:247], v[76:79], v[20:23]\n" \
  "ds_read_b128 v[68:71], v12 offset:0\n" \
  "v_mfma_f32_16x16x32_bf16 v[28:31], a[116:119], v[76:79], v[28:31]\n" \
  "ds_read_b128 v[72:75], v12 offset:2048\n" \
  "v_mfma_f32_16x16x32_bf16 v[36:39], v[76:79], a[244:247], v[36:39]\n" \
  "v_mfma_f32_16x16x32_bf16 v[24:27], v[244:247], v[80:83], v[24:27]\n" \
  "v_mfma_f32_16x16x32_bf16 v[32:35], a[116:119], v[80:83], v[32:35]\n" \
  "v_mfma_f32_16x16x32_bf16 v[40:43], v[80:83], a[244:247], v[40:43]\n" \
  "s_waitcnt lgkmcnt(4)\n" \
  "v_mfma_f32_16x16x32_bf16 v[20:23], v[248:251], v[84:87], v[20:23]\n" \
  "ds_read_b128 v[76:79], v13 offset:0\n" \
  "v_mfma_f32_16x16x32_bf16 v[28:31], a[120:123], v[84:87], v[28:31]\n" \
  "ds_read_b128 v[80:83], v13 offset:2048\n" \
  "v_mfma_f32_16x16x32_bf16 v[36:39], v[84:87], a[248:251], v[36:39]\n" \
  "v_mfma_f32_16x16x32_bf16 v[24:27], v[248:251], v[88:91], v[24:27]\n" \
  "v_mfma_f32_16x16x32_bf16 v[32:35], a[120:123], v[88:91], v[32:35]\n" \
  "v_mfma_f32_16x16x32_bf16 v[40:43], v[88:91], a[248:251], v[40:43]\n" \
  "s_waitcnt lgkmcnt(4)\n" \
  "v_mfma_f32_16x16x32_bf16 v[20:23], v[252:255], v[92:95], v[20:23]\n" \
  "ds_read_b128 v[84:87], v12 offset:4096\n" \
  "v_mfma_f32_16x16x32_bf16 v[28:31], a[124:127], v[92:95], v[28:31]\n" \
  "ds_read_b128 v[88:91], v12 offset:6144\n" \
  "v_mfma_f32_16x16x32_bf16 v[36:39], v[92:95], a[252:255], v[36:39]\n" \
  "v_mfma_f32_16x16x32_bf16 v[24:27], v[252:255], v[96:99], v[24:27]\n" \
  "v_mfma_f32_16x16x32_bf16 v[32:35], a[124:127], v[96:99], v[32:35]\n" \
  "v_mfma_f32_16x16x32_bf16 v[40:43], v[96:99], a[252:255], v[40:43]\n" \
  "s_cmp_eq_u32 s85, 64\n" \
  "s_cbranch_scc1 LX1_%=\n" \
  "s_waitcnt vmcnt(8)\n" \
  "s_barrier\n" \
  "s_waitcnt lgkmcnt(4)\n" \
  "v_mfma_f32_16x16x32_bf16 v[44:47], v[128:131], v[68:71], 0\n" \
  "ds_read_b128 v[92:95], v13 offset:4096\n" \
  "s_sub_u32 s89, s85, 1\n" \
  "s_and_b32 s89, s89, 63\n" \
  "v_mfma_f32_16x16x32_bf16 v[52:55], a[0:3], v[68:71], 0\n" \
  "ds_read_b128 v[96:99], v13 offset:6144\n" \
  "s_lshr_b32 s84, s89, 2\n" \
  "s_xnor_b32 s84, s84, %0\n" \
  "v_mfma_f32_16x16x32_bf16 v[60:63], v[68:71], a[128:131], 0\n" \
  "s_and_b32 s84, s84, 1\n" \
  "s_lshl_b32 s84, s84, 13\n" \
  "v_mfma_f32_16x16x32_bf16 v[48:51], v[128:131], v[72:75], 0\n" \
  "s_lshl_b32 s88, s89, 19\n" \
  "s_or_b32 s88, s88, s84\n" \
  "v_mfma_f32_16x16x32_bf16 v[56:59], a[0:3], v[72:75], 0\n" \
  "s_add_u32 s89, s88, " STR(QKV_SZ32) "\n" \
  "s_add_u32 s90, s89, " STR(QKV_SZ32) "\n" \
  "v_mfma_f32_16x16x32_bf16 v[64:67], v[72:75], a[128:131], 0\n" \
  "s_add_u32 s85, s85, 1\n" \
  "s_waitcnt lgkmcnt(4)\n" \
  "v_mfma_f32_16x16x32_bf16 v[44:47], v[132:135], v[76:79], v[44:47]\n" \
  "ds_read_b128 v[68:71], v12 offset:8192\n" \
  "v_cvt_pk_bf16_f32 v116, v20, v21\n" \
  "v_mfma_f32_16x16x32_bf16 v[52:55], a[4:7], v[76:79], v[52:55]\n" \
  "ds_read_b128 v[72:75], v12 offset:10240\n" \
  "v_cvt_pk_bf16_f32 v117, v22, v23\n" \
  "v_mfma_f32_16x16x32_bf16 v[60:63], v[76:79], a[132:135], v[60:63]\n" \
  "v_cvt_pk_bf16_f32 v118, v24, v25\n" \
  "v_mfma_f32_16x16x32_bf16 v[48:51], v[132:135], v[80:83], v[48:51]\n" \
  "v_cvt_pk_bf16_f32 v119, v26, v27\n" \
  "v_mfma_f32_16x16x32_bf16 v[56:59], a[4:7], v[80:83], v[56:59]\n" \
  "v_cvt_pk_bf16_f32 v120, v28, v29\n" \
  "v_mfma_f32_16x16x32_bf16 v[64:67], v[80:83], a[132:135], v[64:67]\n" \
  "v_cvt_pk_bf16_f32 v121, v30, v31\n" \
  "s_waitcnt lgkmcnt(4)\n" \
  "v_mfma_f32_16x16x32_bf16 v[44:47], v[136:139], v[84:87], v[44:47]\n" \
  "ds_read_b128 v[76:79], v13 offset:8192\n" \
  "v_cvt_pk_bf16_f32 v122, v32, v33\n" \
  "v_mfma_f32_16x16x32_bf16 v[52:55], a[8:11], v[84:87], v[52:55]\n" \
  "ds_read_b128 v[80:83], v13 offset:10240\n" \
  "v_cvt_pk_bf16_f32 v123, v34, v35\n" \
  "v_mfma_f32_16x16x32_bf16 v[60:63], v[84:87], a[136:139], v[60:63]\n" \
  "v_cvt_pk_bf16_f32 v124, v36, v37\n" \
  "v_mfma_f32_16x16x32_bf16 v[48:51], v[136:139], v[88:91], v[48:51]\n" \
  "v_cvt_pk_bf16_f32 v125, v38, v39\n" \
  "v_mfma_f32_16x16x32_bf16 v[56:59], a[8:11], v[88:91], v[56:59]\n" \
  "v_cvt_pk_bf16_f32 v126, v40, v41\n" \
  "v_mfma_f32_16x16x32_bf16 v[64:67], v[88:91], a[136:139], v[64:67]\n" \
  "v_cvt_pk_bf16_f32 v127, v42, v43\n" \
  "s_waitcnt lgkmcnt(4)\n" \
  "v_mfma_f32_16x16x32_bf16 v[44:47], v[140:143], v[92:95], v[44:47]\n" \
  "ds_read_b128 v[84:87], v12 offset:12288\n" \
  "v_permlane16_swap_b32 v116, v118\n" \
  "v_mfma_f32_16x16x32_bf16 v[52:55], a[12:15], v[92:95], v[52:55]\n" \
  "ds_read_b128 v[88:91], v12 offset:14336\n" \
  "v_permlane16_swap_b32 v117, v119\n" \
  "v_mfma_f32_16x16x32_bf16 v[60:63], v[92:95], a[140:143], v[60:63]\n" \
  "v_permlane16_swap_b32 v120, v122\n" \
  "v_mfma_f32_16x16x32_bf16 v[48:51], v[140:143], v[96:99], v[48:51]\n" \
  "v_permlane16_swap_b32 v121, v123\n" \
  "v_mfma_f32_16x16x32_bf16 v[56:59], a[12:15], v[96:99], v[56:59]\n" \
  "v_mfma_f32_16x16x32_bf16 v[64:67], v[96:99], a[140:143], v[64:67]\n" \
  "s_waitcnt lgkmcnt(4)\n" \
  "v_mfma_f32_16x16x32_bf16 v[44:47], v[144:147], v[68:71], v[44:47]\n" \
  "ds_read_b128 v[92:95], v13 offset:12288\n" \
  "buffer_store_dwordx4 v[116:119], v14, s[76:79], s88 offen sc1\n" \
  "v_mfma_f32_16x16x32_bf16 v[52:55], a[16:19], v[68:71], v[52:55]\n" \
  "ds_read_b128 v[96:99], v13 offset:14336\n" \
  "buffer_store_dwordx4 v[120:123], v18, s[76:79], s89 offen sc1\n" \
  "v_mfma_f32_16x16x32_bf16 v[60:63], v[68:71], a[144:147], v[60:63]\n" \
  "buffer_store_dwordx4 v[124:127], v15, s[76:79], s90 offen sc1\n" \
  "v_mfma_f32_16x16x32_bf16 v[48:51], v[144:147], v[72:75], v[48:51]\n" \
  "v_mfma_f32_16x16x32_bf16 v[56:59], a[16:19], v[72:75], v[56:59]\n" \
  "v_mfma_f32_16x16x32_bf16 v[64:67], v[72:75], a[144:147], v[64:67]\n" \
  "s_waitcnt lgkmcnt(4)\n" \
  "v_mfma_f32_16x16x32_bf16 v[44:47], v[148:151], v[76:79], v[44:47]\n" \
  "ds_read_b128 v[68:71], v12 offset:16384\n" \
  "v_mfma_f32_16x16x32_bf16 v[52:55], a[20:23], v[76:79], v[52:55]\n" \
  "ds_read_b128 v[72:75], v12 offset:18432\n" \
  "v_mfma_f32_16x16x32_bf16 v[60:63], v[76:79], a[148:151], v[60:63]\n" \
  "v_mfma_f32_16x16x32_bf16 v[48:51], v[148:151], v[80:83], v[48:51]\n" \
  "s_add_u32 m0, s91, 32768\n" \
  "s_nop 0\n" \
  "buffer_load_dwordx4 v7, s[72:75], s87 offen lds\n" \
  "s_add_u32 s87, s87, 128\n" \
  "v_mfma_f32_16x16x32_bf16 v[56:59], a[20:23], v[80:83], v[56:59]\n" \
  "v_mfma_f32_16x16x32_bf16 v[64:67], v[80:83], a[148:151], v[64:67]\n" \
  "s_waitcnt lgkmcnt(4)\n" \
  "v_mfma_f32_16x16x32_bf16 v[44:47], v[152:155], v[84:87], v[44:47]\n" \
  "ds_read_b128 v[76:79], v13 offset:16384\n" \
  "v_mfma_f32_16x16x32_bf16 v[52:55], a[24:27], v[84:87], v[52:55]\n" \
  "ds_read_b128 v[80:83], v13 offset:18432\n" \
  "v_mfma_f32_16x16x32_bf16 v[60:63], v[84:87], a[152:155], v[60:63]\n" \
  "v_mfma_f32_16x16x32_bf16 v[48:51], v[152:155], v[88:91], v[48:51]\n" \
  "s_add_u32 m0, s91, 36864\n" \
  "s_nop 0\n" \
  "buffer_load_dwordx4 v7, s[72:75], s87 offen lds\n" \
  "s_add_u32 s87, s87, 128\n" \
  "v_mfma_f32_16x16x32_bf16 v[56:59], a[24:27], v[88:91], v[56:59]\n" \
  "v_mfma_f32_16x16x32_bf16 v[64:67], v[88:91], a[152:155], v[64:67]\n" \
  "s_waitcnt lgkmcnt(4)\n" \
  "v_mfma_f32_16x16x32_bf16 v[44:47], v[156:159], v[92:95], v[44:47]\n" \
  "ds_read_b128 v[84:87], v12 offset:20480\n" \
  "v_mfma_f32_16x16x32_bf16 v[52:55], a[28:31], v[92:95], v[52:55]\n" \
  "ds_read_b128 v[88:91], v12 offset:22528\n" \
  "v_mfma_f32_16x16x32_bf16 v[60:63], v[92:95], a[156:159], v[60:63]\n" \
  "v_mfma_f32_16x16x32_bf16 v[48:51], v[156:159], v[96:99], v[48:51]\n" \
  "s_add_u32 m0, s91, 40960\n" \
  "s_nop 0\n" \
  "buffer_load_dwordx4 v7, s[72:75], s87 offen lds\n" \
  "s_add_u32 s87, s87, 128\n" \
  "v_mfma_f32_16x16x32_bf16 v[56:59], a[28:31], v[96:99], v[56:59]\n" \
  "v_mfma_f32_16x16x32_bf16 v[64:67], v[96:99], a[156:159], v[64:67]\n" \
  "s_waitcnt lgkmcnt(4)\n" \
  "v_mfma_f32_16x16x32_bf16 v[44:47], v[160:163], v[68:71], v[44:47]\n" \
  "ds_read_b128 v[92:95], v13 offset:20480\n" \
  "v_mfma_f32_16x16x32_bf16 v[52:55], a[32:35], v[68:71], v[52:55]\n" \
  "ds_read_b128 v[96:99], v13 offset:22528\n" \
  "v_mfma_f32_16x16x32_bf16 v[60:63], v[68:71], a[160:163], v[60:63]\n" \
  "v_mfma_f32_16x16x32_bf16 v[48:51], v[160:163], v[72:75], v[48:51]\n" \
  "s_add_u32 m0, s91, 45056\n" \
  "s_nop 0\n" \
  "buffer_load_dwordx4 v7, s[72:75], s87 offen lds\n" \
  "s_add_u32 s87, s87, 128\n" \
  "v_mfma_f32_16x16x32_bf16 v[56:59], a[32:35], v[72:75], v[56:59]\n" \
  "v_mfma_f32_16x16x32_bf16 v[64:67], v[72:75], a[160:163], v[64:67]\n" \
  "s_waitcnt lgkmcnt(4)\n" \
  "v_mfma_f32_16x16x32_bf16 v[44:47], v[164:167], v[76:79], v[44:47]\n" \
  "ds_read_b128 v[68:71], v12 offset:24576\n" \
  "v_mfma_f32_16x16x32_bf16 v[52:55], a[36:39], v[76:79], v[52:55]\n" \
  "ds_read_b128 v[72:75], v12 offset:26624\n" \
  "v_mfma_f32_16x16x32_bf16 v[60:63], v[76:79], a[164:167], v[60:63]\n" \
  "v_mfma_f32_16x16x32_bf16 v[48:51], v[164:167], v[80:83], v[48:51]\n" \
  "s_add_u32 m0, s91, 49152\n" \
  "s_nop 0\n" \
  "buffer_load_dwordx4 v7, s[72:75], s87 offen lds\n" \
  "s_add_u32 s87, s87, 128\n" \
  "v_mfma_f32_16x16x32_bf16 v[56:59], a[36:39], v[80:83], v[56:59]\n" \
  "v_mfma_f32_16x16x32_bf16 v[64:67], v[80:83], a[164:167], v[64:67]\n" \
  "s_waitcnt lgkmcnt(4)\n" \
  "v_mfma_f32_16x16x32_bf16 v[44:47], v[168:171], v[84:87], v[44:47]\n" \
  "ds_read_b128 v[76:79], v13 offset:24576\n" \
  "v_mfma_f32_16x16x32_bf16 v[52:55], a[40:43], v[84:87], v[52:55]\n" \
  "ds_read_b128 v[80:83], v13 offset:26624\n" \
  "v_mfma_f32_16x16x32_bf16 v[60:63], v[84:87], a[168:171], v[60:63]\n" \
  "v_mfma_f32_16x16x32_bf16 v[48:51], v[168:171], v[88:91], v[48:51]\n" \
  "s_add_u32 m0, s91, 53248\n" \
  "s_nop 0\n" \
  "buffer_load_dwordx4 v7, s[72:75], s87 offen lds\n" \
  "s_add_u32 s87, s87, 128\n" \
  "v_mfma_f32_16x16x32_bf16 v[56:59], a[40:43], v[88:91], v[56:59]\n" \
  "v_mfma_f32_16x16x32_bf16 v[64:67], v[88:91], a[168:171], v[64:67]\n" \
  "s_waitcnt lgkmcnt(4)\n" \
  "v_mfma_f32_16x16x32_bf16 v[44:47], v[172:175], v[92:95], v[44:47]\n" \
  "ds_read_b128 v[84:87], v12 offset:28672\n" \
  "v_mfma_f32_16x16x32_bf16 v[52:55], a[44:47], v[92:95], v[52:55]\n" \
  "ds_read_b128 v[88:91], v12 offset:30720\n" \
  "v_mfma_f32_16x16x32_bf16 v[60:63], v[92:95], a[172:175], v[60:63]\n" \
  "v_mfma_f32_16x16x32_bf16 v[48:51], v[172:175], v[96:99], v[48:51]\n" \
  "s_add_u32 m0, s91, 57344\n" \
  "s_nop 0\n" \
  "buffer_load_dwordx4 v7, s[72:75], s87 offen lds\n" \
  "s_add_u32 s87, s87, 128\n" \
  "v_mfma_f32_16x16x32_bf16 v[56:59], a[44:47], v[96:99], v[56:59]\n" \
  "v_mfma_f32_16x16x32_bf16 v[64:67], v[96:99], a[172:175], v[64:67]\n" \
  "s_waitcnt lgkmcnt(4)\n" \
  "v_mfma_f32_16x16x32_bf16 v[44:47], v[176:179], v[68:71], v[44:47]\n" \
  "ds_read_b128 v[92:95], v13 offset:28672\n" \
  "v_mfma_f32_16x16x32_bf16 v[52:55], a[48:51], v[68:71], v[52:55]\n" \
  "ds_read_b128 v[96:99], v13 offset:30720\n" \
  "v_mfma_f32_16x16x32_bf16 v[60:63], v[68:71], a[176:179], v[60:63]\n" \
  "v_mfma_f32_16x16x32_bf16 v[48:51], v[176:179], v[72:75], v[48:51]\n" \
  "s_add_u32 m0, s91, 61440\n" \
  "s_nop 0\n" \
  "buffer_load_dwordx4 v7, s[72:75], s87 offen lds\n" \
  "s_add_u32 s87, s87, 63616\n" \
  "s_min_u32 s87, s87, s96\n" \
  "v_mfma_f32_16x16x32_bf16 v[56:59], a[48:51], v[72:75], v[56:59]\n" \
  "v_mfma_f32_16x16x32_bf16 v[64:67], v[72:75], a[176:179], v[64:67]\n" \
  "s_waitcnt lgkmcnt(4)\n" \
  "v_mfma_f32_16x16x32_bf16 v[44:47], v[180:183], v[76:79], v[44:47]\n" \
  "ds_read_b128 v[68:71], v12 offset:32768\n" \
  "v_mfma_f32_16x16x32_bf16 v[52:55], a[52:55], v[76:79], v[52:55]\n" \
  "ds_read_b128 v[72:75], v12 offset:34816\n" \
  "v_mfma_f32_16x16x32_bf16 v[60:63], v[76:79], a[180:183], v[60:63]\n" \
  "v_mfma_f32_16x16x32_bf16 v[48:51], v[180:183], v[80:83], v[48:51]\n" \
  "v_mfma_f32_16x16x32_bf16 v[56:59], a[52:55], v[80:83], v[56:59]\n" \
  "v_mfma_f32_16x16x32_bf16 v[64:67], v[80:83], a[180:183], v[64:67]\n" \
  "s_waitcnt lgkmcnt(4)\n" \
  "v_mfma_f32_16x16x32_bf16 v[44:47], v[184:187], v[84:87], v[44:47]\n" \
  "ds_read_b128 v[76:79], v13 offset:32768\n" \
  "v_mfma_f32_16x16x32_bf16 v[52:55], a[56:59], v[84:87], v[52:55]\n" \
  "ds_read_b128 v[80:83], v13 offset:34816\n" \
  "v_mfma_f32_16x16x32_bf16 v[60:63], v[84:87], a[184:187], v[60:63]\n" \
  "v_mfma_f32_16x16x32_bf16 v[48:51], v[184:187], v[88:91], v[48:51]\n" \
  "v_mfma_f32_16x16x32_bf16 v[56:59], a[56:59], v[88:91], v[56:59]\n" \
  "v_mfma_f32_16x16x32_bf16 v[64:67], v[88:91], a[184:187], v[64:67]\n" \
  "s_waitcnt lgkmcnt(4)\n" \
  "v_mfma_f32_16x16x32_bf16 v[44:47], v[188:191], v[92:95], v[44:47]\n" \
  "ds_read_b128 v[84:87], v12 offset:36864\n" \
  "v_mfma_f32_16x16x32_bf16 v[52:55], a[60:63], v[92:95], v[52:55]\n" \
  "ds_read_b128 v[88:91], v12 offset:38912\n" \
  "v_mfma_f32_16x16x32_bf16 v[60:63], v[92:95], a[188:191], v[60:63]\n" \
  "v_mfma_f32_16x16x32_bf16 v[48:51], v[188:191], v[96:99], v[48:51]\n" \
  "v_mfma_f32_16x16x32_bf16 v[56:59], a[60:63], v[96:99], v[56:59]\n" \
  "v_mfma_f32_16x16x32_bf16 v[64:67], v[96:99], a[188:191], v[64:67]\n" \
  "s_waitcnt vmcnt(8)\n" \
  "s_barrier\n" \
  "s_waitcnt lgkmcnt(4)\n" \
  "v_mfma_f32_16x16x32_bf16 v[44:47], v[192:195], v[68:71], v[44:47]\n" \
  "ds_read_b128 v[92:95], v13 offset:36864\n" \
  "buffer_store_dword v17, v16, s[80:83], 0 offen sc1\n" \
  "v_mfma_f32_16x16x32_bf16 v[52:55], a[64:67], v[68:71], v[52:55]\n" \
  "ds_read_b128 v[96:99], v13 offset:38912\n" \
  "v_add_u32 v17, 1, v17\n" \
  "v_mfma_f32_16x16x32_bf16 v[60:63], v[68:71], a[192:195], v[60:63]\n" \
  "v_and_b32 v14, 0x7fffffff, v14\n" \
  "v_mfma_f32_16x16x32_bf16 v[48:51], v[192:195], v[72:75], v[48:51]\n" \
  "v_and_b32 v15, 0x7fffffff, v15\n" \
  "v_mfma_f32_16x16x32_bf16 v[56:59], a[64:67], v[72:75], v[56:59]\n" \
  "v_and_b32 v18, 0x7fffffff, v18\n" \
  "v_mfma_f32_16x16x32_bf16 v[64:67], v[72:75], a[192:195], v[64:67]\n" \
  "s_waitcnt lgkmcnt(4)\n" \
  "v_mfma_f32_16x16x32_bf16 v[44:47], v[196:199], v[76:79], v[44:47]\n" \
  "ds_read_b128 v[68:71], v12 offset:40960\n" \
  "v_mfma_f32_16x16x32_bf16 v[52:55], a[68:71], v[76:79], v[52:55]\n" \
  "ds_read_b128 v[72:75], v12 offset:43008\n" \
  "v_mfma_f32_16x16x32_bf16 v[60:63], v[76:79], a[196:199], v[60:63]\n" \
  "v_mfma_f32_16x16x32_bf16 v[48:51], v[196:199], v[80:83], v[48:51]\n" \
  "v_mfma_f32_16x16x32_bf16 v[56:59], a[68:71], v[80:83], v[56:59]\n" \
  "v_mfma_f32_16x16x32_bf16 v[64:67], v[80:83], a[196:199], v[64:67]\n" \
  "s_waitcnt lgkmcnt(4)\n" \
  "v_mfma_f32_16x16x32_bf16 v[44:47], v[200:203], v[84:87], v[44:47]\n" \
  "ds_read_b128 v[76:79], v13 offset:40960\n" \
  "v_mfma_f32_16x16x32_bf16 v[52:55], a[72:75], v[84:87], v[52:55]\n" \
  "ds_read_b128 v[80:83], v13 offset:43008\n" \
  "v_mfma_f32_16x16x32_bf16 v[60:63], v[84:87], a[200:203], v[60:63]\n" \
  "v_mfma_f32_16x16x32_bf16 v[48:51], v[200:203], v[88:91], v[48:51]\n" \
  "v_mfma_f32_16x16x32_bf16 v[56:59], a[72:75], v[88:91], v[56:59]\n" \
  "v_mfma_f32_16x16x32_bf16 v[64:67], v[88:91], a[200:203], v[64:67]\n" \
  "s_waitcnt lgkmcnt(4)\n" \
  "v_mfma_f32_16x16x32_bf16 v[44:47], v[204:207], v[92:95], v[44:47]\n" \
  "ds_read_b128 v[84:87], v12 offset:45056\n" \
  "v_mfma_f32_16x16x32_bf16 v[52:55], a[76:79], v[92:95], v[52:55]\n" \
  "ds_read_b128 v[88:91], v12 offset:47104\n" \
  "v_mfma_f32_16x16x32_bf16 v[60:63], v[92:95], a[204:207], v[60:63]\n" \
  "v_mfma_f32_16x16x32_bf16 v[48:51], v[204:207], v[96:99], v[48:51]\n" \
  "v_mfma_f32_16x16x32_bf16 v[56:59], a[76:79], v[96:99], v[56:59]\n" \
  "v_mfma_f32_16x16x32_bf16 v[64:67], v[96:99], a[204:207], v[64:67]\n" \
  "s_waitcnt lgkmcnt(4)\n" \
  "v_mfma_f32_16x16x32_bf16 v[44:47], v[208:211], v[68:71], v[44:47]\n" \
  "ds_read_b128 v[92:95], v13 offset:45056\n" \
  "v_mfma_f32_16x16x32_bf16 v[52:55], a[80:83], v[68:71], v[52:55]\n" \
  "ds_read_b128 v[96:99], v13 offset:47104\n" \
  "v_mfma_f32_16x16x32_bf16 v[60:63], v[68:71], a[208:211], v[60:63]\n" \
  "v_mfma_f32_16x16x32_bf16 v[48:51], v[208:211], v[72:75], v[48:51]\n" \
  "v_mfma_f32_16x16x32_bf16 v[56:59], a[80:83], v[72:75], v[56:59]\n" \
  "v_mfma_f32_16x16x32_bf16 v[64:67], v[72:75], a[208:211], v[64:67]\n" \
  "s_waitcnt lgkmcnt(4)\n" \
  "v_mfma_f32_16x16x32_bf16 v[44:47], v[212:215], v[76:79], v[44:47]\n" \
  "ds_read_b128 v[68:71], v12 offset:49152\n" \
  "v_mfma_f32_16x16x32_bf16 v[52:55], a[84:87], v[76:79], v[52:55]\n" \
  "ds_read_b128 v[72:75], v12 offset:51200\n" \
  "v_mfma_f32_16x16x32_bf16 v[60:63], v[76:79], a[212:215], v[60:63]\n" \
  "v_mfma_f32_16x16x32_bf16 v[48:51], v[212:215], v[80:83], v[48:51]\n" \
  "s_add_u32 m0, s91, 65536\n" \
  "s_nop 0\n" \
  "buffer_load_dwordx4 v7, s[72:75], s87 offen lds\n" \
  "s_add_u32 s87, s87, 128\n" \
  "v_mfma_f32_16x16x32_bf16 v[56:59], a[84:87], v[80:83], v[56:59]\n" \
  "v_mfma_f32_16x16x32_bf16 v[64:67], v[80:83], a[212:215], v[64:67]\n" \
  "s_waitcnt lgkmcnt(4)\n" \
  "v_mfma_f32_16x16x32_bf16 v[44:47], v[216:219], v[84:87], v[44:47]\n" \
  "ds_read_b128 v[76:79], v13 offset:49152\n" \
  "v_mfma_f32_16x16x32_bf16 v[52:55], a[88:91], v[84:87], v[52:55]\n" \
  "ds_read_b128 v[80:83], v13 offset:51200\n" \
  "v_mfma_f32_16x16x32_bf16 v[60:63], v[84:87], a[216:219], v[60:63]\n" \
  "v_mfma_f32_16x16x32_bf16 v[48:51], v[216:219], v[88:91], v[48:51]\n" \
  "s_add_u32 m0, s91, 69632\n" \
  "s_nop 0\n" \
  "buffer_load_dwordx4 v7, s[72:75], s87 offen lds\n" \
  "s_add_u32 s87, s87, 128\n" \
  "v_mfma_f32_16x16x32_bf16 v[56:59], a[88:91], v[88:91], v[56:59]\n" \
  "v_mfma_f32_16x16x32_bf16 v[64:67], v[88:91], a[216:219], v[64:67]\n" \
  "s_waitcnt lgkmcnt(4)\n" \
  "v_mfma_f32_16x16x32_bf16 v[44:47], v[220:223], v[92:95], v[44:47]\n" \
  "ds_read_b128 v[84:87], v12 offset:53248\n" \
  "v_mfma_f32_16x16x32_bf16 v[52:55], a[92:95], v[92:95], v[52:55]\n" \
  "ds_read_b128 v[88:91], v12 offset:55296\n" \
  "v_mfma_f32_16x16x32_bf16 v[60:63], v[92:95], a[220:223], v[60:63]\n" \
  "v_mfma_f32_16x16x32_bf16 v[48:51], v[220:223], v[96:99], v[48:51]\n" \
  "s_add_u32 m0, s91, 73728\n" \
  "s_nop 0\n" \
  "buffer_load_dwordx4 v7, s[72:75], s87 offen lds\n" \
  "s_add_u32 s87, s87, 128\n" \
  "v_mfma_f32_16x16x32_bf16 v[56:59], a[92:95], v[96:99], v[56:59]\n" \
  "v_mfma_f32_16x16x32_bf16 v[64:67], v[96:99], a[220:223], v[64:67]\n" \
  "s_waitcnt lgkmcnt(4)\n" \
  "v_mfma_f32_16x16x32_bf16 v[44:47], v[224:227], v[68:71], v[44:47]\n" \
  "ds_read_b128 v[92:95], v13 offset:53248\n" \
  "v_mfma_f32_16x16x32_bf16 v[52:55], a[96:99], v[68:71], v[52:55]\n" \
  "ds_read_b128 v[96:99], v13 offset:55296\n" \
  "v_mfma_f32_16x16x32_bf16 v[60:63], v[68:71], a[224:227], v[60:63]\n" \
  "v_mfma_f32_16x16x32_bf16 v[48:51], v[224:227], v[72:75], v[48:51]\n" \
  "s_add_u32 m0, s91, 77824\n" \
  "s_nop 0\n" \
  "buffer_load_dwordx4 v7, s[72:75], s87 offen lds\n" \
  "s_add_u32 s87, s87, 128\n" \
  "v_mfma_f32_16x16x32_bf16 v[56:59], a[96:99], v[72:75], v[56:59]\n" \
  "v_mfma_f32_16x16x32_bf16 v[64:67], v[72:75], a[224:227], v[64:67]\n" \
  "s_waitcnt lgkmcnt(4)\n" \
  "v_mfma_f32_16x16x32_bf16 v[44:47], v[228:231], v[76:79], v[44:47]\n" \
  "ds_read_b128 v[68:71], v12 offset:57344\n" \
  "v_mfma_f32_16x16x32_bf16 v[52:55], a[100:103], v[76:79], v[52:55]\n" \
  "ds_read_b128 v[72:75], v12 offset:59392\n" \
  "v_mfma_f32_16x16x32_bf16 v[60:63], v[76:79], a[228:231], v[60:63]\n" \
  "v_mfma_f32_16x16x32_bf16 v[48:51], v[228:231], v[80:83], v[48:51]\n" \
  "s_add_u32 m0, s91, 81920\n" \
  "s_nop 0\n" \
  "buffer_load_dwordx4 v7, s[72:75], s87 offen lds\n" \
  "s_add_u32 s87, s87, 128\n" \
  "v_mfma_f32_16x16x32_bf16 v[56:59], a[100:103], v[80:83], v[56:59]\n" \
  "v_mfma_f32_16x16x32_bf16 v[64:67], v[80:83], a[228:231], v[64:67]\n" \
  "s_waitcnt lgkmcnt(4)\n" \
  "v_mfma_f32_16x16x32_bf16 v[44:47], v[232:235], v[84:87], v[44:47]\n" \
  "ds_read_b128 v[76:79], v13 offset:57344\n" \
  "v_mfma_f32_16x16x32_bf16 v[52:55], a[104:107], v[84:87], v[52:55]\n" \
  "ds_read_b128 v[80:83], v13 offset:59392\n" \
  "v_mfma_f32_16x16x32_bf16 v[60:63], v[84:87], a[232:235], v[60:63]\n" \
  "v_mfma_f32_16x16x32_bf16 v[48:51], v[232:235], v[88:91], v[48:51]\n" \
  "s_add_u32 m0, s91, 86016\n" \
  "s_nop 0\n" \
  "buffer_load_dwordx4 v7, s[72:75], s87 offen lds\n" \
  "s_add_u32 s87, s87, 128\n" \
  "v_mfma_f32_16x16x32_bf16 v[56:59], a[104:107], v[88:91], v[56:59]\n" \
  "v_mfma_f32_16x16x32_bf16 v[64:67], v[88:91], a[232:235], v[64:67]\n" \
  "s_waitcnt lgkmcnt(4)\n" \
  "v_mfma_f32_16x16x32_bf16 v[44:47], v[236:239], v[92:95], v[44:47]\n" \
  "ds_read_b128 v[84:87], v12 offset:61440\n" \
  "v_mfma_f32_16x16x32_bf16 v[52:55], a[108:111], v[92:95], v[52:55]\n" \
  "ds_read_b128 v[88:91], v12 offset:63488\n" \
  "v_mfma_f32_16x16x32_bf16 v[60:63], v[92:95], a[236:239], v[60:63]\n" \
  "v_mfma_f32_16x16x32_bf16 v[48:51], v[236:239], v[96:99], v[48:51]\n" \
  "s_add_u32 m0, s91, 90112\n" \
  "s_nop 0\n" \
  "buffer_load_dwordx4 v7, s[72:75], s87 offen lds\n" \
  "s_add_u32 s87, s87, 128\n" \
  "v_mfma_f32_16x16x32_bf16 v[56:59], a[108:111], v[96:99], v[56:59]\n" \
  "v_mfma_f32_16x16x32_bf16 v[64:67], v[96:99], a[236:239], v[64:67]\n" \
  "s_waitcnt lgkmcnt(4)\n" \
  "v_mfma_f32_16x16x32_bf16 v[44:47], v[240:243], v[68:71], v[44:47]\n" \
  "ds_read_b128 v[92:95], v13 offset:61440\n" \
  "v_mfma_f32_16x16x32_bf16 v[52:55], a[112:115], v[68:71], v[52:55]\n" \
  "ds_read_b128 v[96:99], v13 offset:63488\n" \
  "v_mfma_f32_16x16x32_bf16 v[60:63], v[68:71], a[240:243], v[60:63]\n" \
  "v_mfma_f32_16x16x32_bf16 v[48:51], v[240:243], v[72:75], v[48:51]\n" \
  "s_add_u32 m0, s91, 94208\n" \
  "s_nop 0\n" \
  "buffer_load_dwordx4 v7, s[72:75], s87 offen lds\n" \
  "s_add_u32 s87, s87, 128\n" \
  "v_mfma_f32_16x16x32_bf16 v[56:59], a[112:115], v[72:75], v[56:59]\n" \
  "v_mfma_f32_16x16x32_bf16 v[64:67], v[72:75], a[240:243], v[64:67]\n" \
  "s_waitcnt lgkmcnt(4)\n" \
  "v_mfma_f32_16x16x32_bf16 v[44:47], v[244:247], v[76:79], v[44:47]\n" \
  "ds_read_b128 v[68:71], v10 offset:0\n" \
  "v_mfma_f32_16x16x32_bf16 v[52:55], a[116:119], v[76:79], v[52:55]\n" \
  "ds_read_b128 v[72:75], v10 offset:2048\n" \
  "v_mfma_f32_16x16x32_bf16 v[60:63], v[76:79], a[244:247], v[60:63]\n" \
  "v_mfma_f32_16x16x32_bf16 v[48:51], v[244:247], v[80:83], v[48:51]\n" \
  "v_mfma_f32_16x16x32_bf16 v[56:59], a[116:119], v[80:83], v[56:59]\n" \
  "v_mfma_f32_16x16x32_bf16 v[64:67], v[80:83], a[244:247], v[64:67]\n" \
  "s_waitcnt lgkmcnt(4)\n" \
  "v_mfma_f32_16x16x32_bf16 v[44:47], v[248:251], v[84:87], v[44:47]\n" \
  "ds_read_b128 v[76:79], v11 offset:0\n" \
  "v_mfma_f32_16x16x32_bf16 v[52:55], a[120:123], v[84:87], v[52:55]\n" \
  "ds_read_b128 v[80:83], v11 offset:2048\n" \
  "v_mfma_f32_16x16x32_bf16 v[60:63], v[84:87], a[248:251], v[60:63]\n" \
  "v_mfma_f32_16x16x32_bf16 v[48:51], v[248:251], v[88:91], v[48:51]\n" \
  "v_mfma_f32_16x16x32_bf16 v[56:59], a[120:123], v[88:91], v[56:59]\n" \
  "v_mfma_f32_16x16x32_bf16 v[64:67], v[88:91], a[248:251], v[64:67]\n" \
  "s_waitcnt lgkmcnt(4)\n" \
  "v_mfma_f32_16x16x32_bf16 v[44:47], v[252:255], v[92:95], v[44:47]\n" \
  "ds_read_b128 v[84:87], v10 offset:4096\n" \
  "v_mfma_f32_16x16x32_bf16 v[52:55], a[124:127], v[92:95], v[52:55]\n" \
  "ds_read_b128 v[88:91], v10 offset:6144\n" \
  "v_mfma_f32_16x16x32_bf16 v[60:63], v[92:95], a[252:255], v[60:63]\n" \
  "v_mfma_f32_16x16x32_bf16 v[48:51], v[252:255], v[96:99], v[48:51]\n" \
  "v_mfma_f32_16x16x32_bf16 v[56:59], a[124:127], v[96:99], v[56:59]\n" \
  "v_mfma_f32_16x16x32_bf16 v[64:67], v[96:99], a[252:255], v[64:67]\n" \
  "s_branch LG_%=\n" \
  "LX0_%=:\n" \
  "s_sub_u32 s89, s85, 1\n" \
  "s_and_b32 s89, s89, 63\n" \
  "s_lshr_b32 s84, s89, 2\n" \
  "s_xnor_b32 s84, s84, %0\n" \
  "s_and_b32 s84, s84, 1\n" \
  "s_lshl_b32 s84, s84, 13\n" \
  "s_lshl_b32 s88, s89, 19\n" \
  "s_or_b32 s88, s88, s84\n" \
  "s_add_u32 s89, s88, " STR(QKV_SZ32) "\n" \
  "s_add_u32 s90, s89, " STR(QKV_SZ32) "\n" \
  "s_add_u32 s85, s85, 1\n" \
  "v_cvt_pk_bf16_f32 v116, v44, v45\n" \
  "v_cvt_pk_bf16_f32 v117, v46, v47\n" \
  "v_cvt_pk_bf16_f32 v118, v48, v49\n" \
  "v_cvt_pk_bf16_f32 v119, v50, v51\n" \
  "v_cvt_pk_bf16_f32 v120, v52, v53\n" \
  "v_cvt_pk_bf16_f32 v121, v54, v55\n" \
  "v_cvt_pk_bf16_f32 v122, v56, v57\n" \
  "v_cvt_pk_bf16_f32 v123, v58, v59\n" \
  "v_cvt_pk_bf16_f32 v124, v60, v61\n" \
  "v_cvt_pk_bf16_f32 v125, v62, v63\n" \
  "v_cvt_pk_bf16_f32 v126, v64, v65\n" \
  "v_cvt_pk_bf16_f32 v127, v66, v67\n" \
  "s_nop 1\n" \
  "v_permlane16_swap_b32 v116, v118\n" \
  "v_permlane16_swap_b32 v117, v119\n" \
  "v_permlane16_swap_b32 v120, v122\n" \
  "v_permlane16_swap_b32 v121, v123\n" \
  "buffer_store_dwordx4 v[116:119], v14, s[76:79], s88 offen sc1\n" \
  "buffer_store_dwordx4 v[120:123], v18, s[76:79], s89 offen sc1\n" \
  "buffer_store_dwordx4 v[124:127], v15, s[76:79], s90 offen sc1\n" \
  "s_branch LZ_%=\n" \
  "LX1_%=:\n" \
  "s_sub_u32 s89, s85, 1\n" \
  "s_and_b32 s89, s89, 63\n" \
  "s_lshr_b32 s84, s89, 2\n" \
  "s_xnor_b32 s84, s84, %0\n" \
  "s_and_b32 s84, s84, 1\n" \
  "s_lshl_b32 s84, s84, 13\n" \
  "s_lshl_b32 s88, s89, 19\n" \
  "s_or_b32 s88, s88, s84\n" \
  "s_add_u32 s89, s88, " STR(QKV_SZ32) "\n" \
  "s_add_u32 s90, s89, " STR(QKV_SZ32) "\n" \
  "s_add_u32 s85, s85, 1\n" \
  "v_cvt_pk_bf16_f32 v116, v20, v21\n" \
  "v_cvt_pk_bf16_f32 v117, v22, v23\n" \
  "v_cvt_pk_bf16_f32 v118, v24, v25\n" \
  "v_cvt_pk_bf16_f32 v119, v26, v27\n" \
  "v_cvt_pk_bf16_f32 v120, v28, v29\n" \
  "v_cvt_pk_bf16_f32 v121, v30, v31\n" \
  "v_cvt_pk_bf16_f32 v122, v32, v33\n" \
  "v_cvt_pk_bf16_f32 v123, v34, v35\n" \
  "v_cvt_pk_bf16_f32 v124, v36, v37\n" \
  "v_cvt_pk_bf16_f32 v125, v38, v39\n" \
  "v_cvt_pk_bf16_f32 v126, v40, v41\n" \
  "v_cvt_pk_bf16_f32 v127, v42, v43\n" \
  "s_nop 1\n" \
  "v_permlane16_swap_b32 v116, v118\n" \
  "v_permlane16_swap_b32 v117, v119\n" \
  "v_permlane16_swap_b32 v120, v122\n" \
  "v_permlane16_swap_b32 v121, v123\n" \
  "buffer_store_dwordx4 v[116:119], v14, s[76:79], s88 offen sc1\n" \
  "buffer_store_dwordx4 v[120:123], v18, s[76:79], s89 offen sc1\n" \
  "buffer_store_dwordx4 v[124:127], v15, s[76:79], s90 offen sc1\n" \
  "s_branch LZ_%=\n" \
  "LZ_%=:\n" \
  "s_waitcnt vmcnt(0) lgkmcnt(0)\n" \
  "s_barrier\n" \
  "buffer_store_dword v17, v16, s[80:83], 0 offen sc1\n" \
  "s_waitcnt vmcnt(0)\n" \
  ""

#define XO_ASM \
  "s_mov_b64 s[68:69], %0\n" \
  "s_and_b32 s69, s69, 0xffff\n" \
  "s_mov_b32 s70, 8388608\n" \
  "s_mov_b32 s71, 0x00020000\n" \
  "s_mov_b64 s[72:73], %1\n" \
  "s_and_b32 s73, s73, 0xffff\n" \
  "s_mov_b32 s74, " STR(QKV_SZ32) "\n" \
  "s_mov_b32 s75, 0x00020000\n" \
  "s_mov_b64 s[76:77], %2\n" \
  "s_and_b32 s77, s77, 0xffff\n" \
  "s_mov_b32 s78, 8388608\n" \
  "s_mov_b32 s79, 0x00020000\n" \
  "s_mov_b64 s[80:81], %3\n" \
  "s_and_b32 s81, s81, 0xffff\n" \
  "s_mov_b32 s82, " STR(CTRL_BYTES) "\n" \
  "s_mov_b32 s83, 0x00020000\n" \
  "v_mbcnt_lo_u32_b32 v4, -1, 0\n" \
  "v_mbcnt_hi_u32_b32 v4, -1, v4\n" \
  "v_and_b32 v20, 15, v4\n" \
  "v_lshrrev_b32 v21, 4, v4\n" \
  "v_lshlrev_b32 v6, 4, v4\n" \
  "s_lshl_b32 s86, %5, 15\n" \
  "s_add_u32 s86, s86, %6\n" \
  "s_lshl_b32 s90, %5, 21\n" \
  "v_lshrrev_b32 v5, 3, v4\n" \
  "v_and_b32 v15, 7, v4\n" \
  "v_lshlrev_b32 v7, 11, v5\n" \
  "v_lshl_add_u32 v7, v15, 4, v7\n" \
  "v_lshlrev_b32 v5, 5, v21\n" \
  "v_xor_b32 v7, v7, v5\n" \
  "s_add_u32 m0, s86, 0\n" \
  "s_nop 0\n" \
  "buffer_load_dwordx4 v7, s[68:71], s90 offen lds\n" \
  "s_add_u32 s90, s90, 16384\n" \
  "s_add_u32 m0, s86, 1024\n" \
  "s_nop 0\n" \
  "buffer_load_dwordx4 v7, s[68:71], s90 offen lds\n" \
  "s_add_u32 s90, s90, 16384\n" \
  "s_add_u32 m0, s86, 2048\n" \
  "s_nop 0\n" \
  "buffer_load_dwordx4 v7, s[68:71], s90 offen lds\n" \
  "s_add_u32 s90, s90, 16384\n" \
  "s_add_u32 m0, s86, 3072\n" \
  "s_nop 0\n" \
  "buffer_load_dwordx4 v7, s[68:71], s90 offen lds\n" \
  "s_add_u32 s90, s90, 16384\n" \
  "s_add_u32 m0, s86, 4096\n" \
  "s_nop 0\n" \
  "buffer_load_dwordx4 v7, s[68:71], s90 offen lds\n" \
  "s_add_u32 s90, s90, 16384\n" \
  "s_add_u32 m0, s86, 5120\n" \
  "s_nop 0\n" \
  "buffer_load_dwordx4 v7, s[68:71], s90 offen lds\n" \
  "s_add_u32 s90, s90, 16384\n" \
  "s_add_u32 m0, s86, 6144\n" \
  "s_nop 0\n" \
  "buffer_load_dwordx4 v7, s[68:71], s90 offen lds\n" \
  "s_add_u32 s90, s90, 16384\n" \
  "s_add_u32 m0, s86, 7168\n" \
  "s_nop 0\n" \
  "buffer_load_dwordx4 v7, s[68:71], s90 offen lds\n" \
  "s_add_u32 s90, s90, 16384\n" \
  "s_add_u32 m0, s86, 8192\n" \
  "s_nop 0\n" \
  "buffer_load_dwordx4 v7, s[68:71], s90 offen lds\n" \
  "s_add_u32 s90, s90, 16384\n" \
  "s_add_u32 m0, s86, 9216\n" \
  "s_nop 0\n" \
  "buffer_load_dwordx4 v7, s[68:71], s90 offen lds\n" \
  "s_add_u32 s90, s90, 16384\n" \
  "s_add_u32 m0, s86, 10240\n" \
  "s_nop 0\n" \
  "buffer_load_dwordx4 v7, s[68:71], s90 offen lds\n" \
  "s_add_u32 s90, s90, 16384\n" \
  "s_add_u32 m0, s86, 11264\n" \
  "s_nop 0\n" \
  "buffer_load_dwordx4 v7, s[68:71], s90 offen lds\n" \
  "s_add_u32 s90, s90, 16384\n" \
  "s_add_u32 m0, s86, 12288\n" \
  "s_nop 0\n" \
  "buffer_load_dwordx4 v7, s[68:71], s90 offen lds\n" \
  "s_add_u32 s90, s90, 16384\n" \
  "s_add_u32 m0, s86, 13312\n" \
  "s_nop 0\n" \
  "buffer_load_dwordx4 v7, s[68:71], s90 offen lds\n" \
  "s_add_u32 s90, s90, 16384\n" \
  "s_add_u32 m0, s86, 14336\n" \
  "s_nop 0\n" \
  "buffer_load_dwordx4 v7, s[68:71], s90 offen lds\n" \
  "s_add_u32 s90, s90, 16384\n" \
  "s_add_u32 m0, s86, 15360\n" \
  "s_nop 0\n" \
  "buffer_load_dwordx4 v7, s[68:71], s90 offen lds\n" \
  "s_add_u32 s90, s90, 16384\n" \
  "s_add_u32 m0, s86, 16384\n" \
  "s_nop 0\n" \
  "buffer_load_dwordx4 v7, s[68:71], s90 offen lds\n" \
  "s_add_u32 s90, s90, 16384\n" \
  "s_add_u32 m0, s86, 17408\n" \
  "s_nop 0\n" \
  "buffer_load_dwordx4 v7, s[68:71], s90 offen lds\n" \
  "s_add_u32 s90, s90, 16384\n" \
  "s_add_u32 m0, s86, 18432\n" \
  "s_nop 0\n" \
  "buffer_load_dwordx4 v7, s[68:71], s90 offen lds\n" \
  "s_add_u32 s90, s90, 16384\n" \
  "s_add_u32 m0, s86, 19456\n" \
  "s_nop 0\n" \
  "buffer_load_dwordx4 v7, s[68:71], s90 offen lds\n" \
  "s_add_u32 s90, s90, 16384\n" \
  "s_add_u32 m0, s86, 20480\n" \
  "s_nop 0\n" \
  "buffer_load_dwordx4 v7, s[68:71], s90 offen lds\n" \
  "s_add_u32 s90, s90, 16384\n" \
  "s_add_u32 m0, s86, 21504\n" \
  "s_nop 0\n" \
  "buffer_load_dwordx4 v7, s[68:71], s90 offen lds\n" \
  "s_add_u32 s90, s90, 16384\n" \
  "s_add_u32 m0, s86, 22528\n" \
  "s_nop 0\n" \
  "buffer_load_dwordx4 v7, s[68:71], s90 offen lds\n" \
  "s_add_u32 s90, s90, 16384\n" \
  "s_add_u32 m0, s86, 23552\n" \
  "s_nop 0\n" \
  "buffer_load_dwordx4 v7, s[68:71], s90 offen lds\n" \
  "s_add_u32 s90, s90, 16384\n" \
  "s_add_u32 m0, s86, 24576\n" \
  "s_nop 0\n" \
  "buffer_load_dwordx4 v7, s[68:71], s90 offen lds\n" \
  "s_add_u32 s90, s90, 16384\n" \
  "s_add_u32 m0, s86, 25600\n" \
  "s_nop 0\n" \
  "buffer_load_dwordx4 v7, s[68:71], s90 offen lds\n" \
  "s_add_u32 s90, s90, 16384\n" \
  "s_add_u32 m0, s86, 26624\n" \
  "s_nop 0\n" \
  "buffer_load_dwordx4 v7, s[68:71], s90 offen lds\n" \
  "s_add_u32 s90, s90, 16384\n" \
  "s_add_u32 m0, s86, 27648\n" \
  "s_nop 0\n" \
  "buffer_load_dwordx4 v7, s[68:71], s90 offen lds\n" \
  "s_add_u32 s90, s90, 16384\n" \
  "s_add_u32 m0, s86, 28672\n" \
  "s_nop 0\n" \
  "buffer_load_dwordx4 v7, s[68:71], s90 offen lds\n" \
  "s_add_u32 s90, s90, 16384\n" \
  "s_add_u32 m0, s86, 29696\n" \
  "s_nop 0\n" \
  "buffer_load_dwordx4 v7, s[68:71], s90 offen lds\n" \
  "s_add_u32 s90, s90, 16384\n" \
  "s_add_u32 m0, s86, 30720\n" \
  "s_nop 0\n" \
  "buffer_load_dwordx4 v7, s[68:71], s90 offen lds\n" \
  "s_add_u32 s90, s90, 16384\n" \
  "s_add_u32 m0, s86, 31744\n" \
  "s_nop 0\n" \
  "buffer_load_dwordx4 v7, s[68:71], s90 offen lds\n" \
  "s_add_u32 s90, s90, 16384\n" \
  "v_lshrrev_b32 v5, 2, v20\n" \
  "v_lshl_add_u32 v5, v21, 2, v5\n" \
  "v_lshlrev_b32 v15, 7, v5\n" \
  "v_lshrrev_b32 v5, 1, v5\n" \
  "v_and_b32 v5, 3, v5\n" \
  "v_lshl_add_u32 v15, v5, 5, v15\n" \
  "v_and_b32 v5, 3, v4\n" \
  "v_lshl_add_u32 v15, v5, 3, v15\n" \
  "v_xor_b32 v5, 0, v15\n" \
  "v_add_u32 v16, s86, v5\n" \
  "v_xor_b32 v5, 32, v15\n" \
  "v_add_u32 v17, s86, v5\n" \
  "v_xor_b32 v5, 64, v15\n" \
  "v_add_u32 v18, s86, v5\n" \
  "v_xor_b32 v5, 96, v15\n" \
  "v_add_u32 v19, s86, v5\n" \
  "s_lshl_b32 s84, %5, 12\n" \
  "s_add_u32 s84, s84, 131072\n" \
  "s_add_u32 s84, s84, %6\n" \
  "v_xor_b32 v5, v21, v20\n" \
  "v_lshlrev_b32 v5, 4, v5\n" \
  "v_lshl_add_u32 v5, v20, 8, v5\n" \
  "v_add_u32 v5, s84, v5\n" \
  "v_xor_b32 v12, 64, v5\n" \
  "v_xor_b32 v13, 128, v5\n" \
  "v_xor_b32 v14, 192, v5\n" \
  "s_lshl_b32 s84, %5, 2\n" \
  "v_add_u32 v5, s84, v21\n" \
  "v_lshlrev_b32 v10, 12, v5\n" \
  "v_lshl_add_u32 v10, v20, 3, v10\n" \
  "v_xor_b32 v15, v5, v20\n" \
  "v_lshlrev_b32 v15, 4, v15\n" \
  "v_lshl_add_u32 v15, v5, 8, v15\n" \
  "s_add_u32 s84, %6, 131072\n" \
  "v_add_u32 v11, s84, v15\n" \
  "s_and_b32 s84, %4, 1\n" \
  "s_lshl_b32 s96, s84, 18\n" \
  "s_lshl_b32 s85, %5, 16\n" \
  "s_add_u32 s96, s96, s85\n" \
  "s_lshl_b32 s88, s84, 16\n" \
  "s_lshr_b32 s84, %4, 1\n" \
  "s_lshl_b32 s84, s84, 7\n" \
  "s_add_u32 s88, s88, s84\n" \
  "v_and_b32 v8, 7, v4\n" \
  "v_lshlrev_b32 v8, 7, v8\n" \
  "s_lshl_b32 s84, %5, 10\n" \
  "s_add_u32 s84, s84, " STR(CW_XPROG) "*4\n" \
  "v_add_u32 v8, s84, v8\n" \
  "s_mov_b32 s91, 0\n" \
  "s_mov_b32 s89, 0\n" \
  "s_lshr_b32 s84, s89, 2\n" \
  "s_xnor_b32 s84, s84, %7\n" \
  "s_and_b32 s84, s84, 1\n" \
  "s_lshl_b32 s84, s84, 13\n" \
  "s_lshl_b32 s87, s89, 19\n" \
  "s_or_b32 s87, s87, s84\n" \
  "s_add_u32 s87, s87, s96\n" \
  "s_waitcnt vmcnt(16)\n" \
  "ds_read_b64_tr_b16 v[128:129], v16 offset:0\n" \
  "ds_read_b64_tr_b16 v[130:131], v16 offset:2048\n" \
  "ds_read_b64_tr_b16 v[132:133], v17 offset:0\n" \
  "ds_read_b64_tr_b16 v[134:135], v17 offset:2048\n" \
  "ds_read_b64_tr_b16 v[136:137], v18 offset:0\n" \
  "ds_read_b64_tr_b16 v[138:139], v18 offset:2048\n" \
  "ds_read_b64_tr_b16 v[140:141], v19 offset:0\n" \
  "ds_read_b64_tr_b16 v[142:143], v19 offset:2048\n" \
  "ds_read_b64_tr_b16 v[144:145], v16 offset:4096\n" \
  "ds_read_b64_tr_b16 v[146:147], v16 offset:6144\n" \
  "ds_read_b64_tr_b16 v[148:149], v17 offset:4096\n" \
  "ds_read_b64_tr_b16 v[150:151], v17 offset:6144\n" \
  "ds_read_b64_tr_b16 v[152:153], v18 offset:4096\n" \
  "ds_read_b64_tr_b16 v[154:155], v18 offset:6144\n" \
  "ds_read_b64_tr_b16 v[156:157], v19 offset:4096\n" \
  "ds_read_b64_tr_b16 v[158:159], v19 offset:6144\n" \
  "ds_read_b64_tr_b16 v[160:161], v16 offset:8192\n" \
  "ds_read_b64_tr_b16 v[162:163], v16 offset:10240\n" \
  "ds_read_b64_tr_b16 v[164:165], v17 offset:8192\n" \
  "ds_read_b64_tr_b16 v[166:167], v17 offset:10240\n" \
  "ds_read_b64_tr_b16 v[168:169], v18 offset:8192\n" \
  "ds_read_b64_tr_b16 v[170:171], v18 offset:10240\n" \
  "ds_read_b64_tr_b16 v[172:173], v19 offset:8192\n" \
  "ds_read_b64_tr_b16 v[174:175], v19 offset:10240\n" \
  "ds_read_b64_tr_b16 v[176:177], v16 offset:12288\n" \
  "ds_read_b64_tr_b16 v[178:179], v16 offset:14336\n" \
  "ds_read_b64_tr_b16 v[180:181], v17 offset:12288\n" \
  "ds_read_b64_tr_b16 v[182:183], v17 offset:14336\n" \
  "ds_read_b64_tr_b16 v[184:185], v18 offset:12288\n" \
  "ds_read_b64_tr_b16 v[186:187], v18 offset:14336\n" \
  "ds_read_b64_tr_b16 v[188:189], v19 offset:12288\n" \
  "ds_read_b64_tr_b16 v[190:191], v19 offset:14336\n" \
  "s_waitcnt lgkmcnt(0)\n" \
  "s_add_u32 m0, s86, 0\n" \
  "s_nop 0\n" \
  "buffer_load_dwordx4 v7, s[68:71], s90 offen lds\n" \
  "s_add_u32 s90, s90, 16384\n" \
  "s_add_u32 m0, s86, 1024\n" \
  "s_nop 0\n" \
  "buffer_load_dwordx4 v7, s[68:71], s90 offen lds\n" \
  "s_add_u32 s90, s90, 16384\n" \
  "s_add_u32 m0, s86, 2048\n" \
  "s_nop 0\n" \
  "buffer_load_dwordx4 v7, s[68:71], s90 offen lds\n" \
  "s_add_u32 s90, s90, 16384\n" \
  "s_add_u32 m0, s86, 3072\n" \
  "s_nop 0\n" \
  "buffer_load_dwordx4 v7, s[68:71], s90 offen lds\n" \
  "s_add_u32 s90, s90, 16384\n" \
  "s_add_u32 m0, s86, 4096\n" \
  "s_nop 0\n" \
  "buffer_load_dwordx4 v7, s[68:71], s90 offen lds\n" \
  "s_add_u32 s90, s90, 16384\n" \
  "s_add_u32 m0, s86, 5120\n" \
  "s_nop 0\n" \
  "buffer_load_dwordx4 v7, s[68:71], s90 offen lds\n" \
  "s_add_u32 s90, s90, 16384\n" \
  "s_add_u32 m0, s86, 6144\n" \
  "s_nop 0\n" \
  "buffer_load_dwordx4 v7, s[68:71], s90 offen lds\n" \
  "s_add_u32 s90, s90, 16384\n" \
  "s_add_u32 m0, s86, 7168\n" \
  "s_nop 0\n" \
  "buffer_load_dwordx4 v7, s[68:71], s90 offen lds\n" \
  "s_add_u32 s90, s90, 16384\n" \
  "s_add_u32 m0, s86, 8192\n" \
  "s_nop 0\n" \
  "buffer_load_dwordx4 v7, s[68:71], s90 offen lds\n" \
  "s_add_u32 s90, s90, 16384\n" \
  "s_add_u32 m0, s86, 9216\n" \
  "s_nop 0\n" \
  "buffer_load_dwordx4 v7, s[68:71], s90 offen lds\n" \
  "s_add_u32 s90, s90, 16384\n" \
  "s_add_u32 m0, s86, 10240\n" \
  "s_nop 0\n" \
  "buffer_load_dwordx4 v7, s[68:71], s90 offen lds\n" \
  "s_add_u32 s90, s90, 16384\n" \
  "s_add_u32 m0, s86, 11264\n" \
  "s_nop 0\n" \
  "buffer_load_dwordx4 v7, s[68:71], s90 offen lds\n" \
  "s_add_u32 s90, s90, 16384\n" \
  "s_add_u32 m0, s86, 12288\n" \
  "s_nop 0\n" \
  "buffer_load_dwordx4 v7, s[68:71], s90 offen lds\n" \
  "s_add_u32 s90, s90, 16384\n" \
  "s_add_u32 m0, s86, 13312\n" \
  "s_nop 0\n" \
  "buffer_load_dwordx4 v7, s[68:71], s90 offen lds\n" \
  "s_add_u32 s90, s90, 16384\n" \
  "s_add_u32 m0, s86, 14336\n" \
  "s_nop 0\n" \
  "buffer_load_dwordx4 v7, s[68:71], s90 offen lds\n" \
  "s_add_u32 s90, s90, 16384\n" \
  "s_add_u32 m0, s86, 15360\n" \
  "s_nop 0\n" \
  "buffer_load_dwordx4 v7, s[68:71], s90 offen lds\n" \
  "s_add_u32 s90, s90, 16384\n" \
  "s_waitcnt vmcnt(16)\n" \
  "ds_read_b64_tr_b16 v[192:193], v16 offset:16384\n" \
  "ds_read_b64_tr_b16 v[194:195], v16 offset:18432\n" \
  "ds_read_b64_tr_b16 v[196:197], v17 offset:16384\n" \
  "ds_read_b64_tr_b16 v[198:199], v17 offset:18432\n" \
  "ds_read_b64_tr_b16 v[200:201], v18 offset:16384\n" \
  "ds_read_b64_tr_b16 v[202:203], v18 offset:18432\n" \
  "ds_read_b64_tr_b16 v[204:205], v19 offset:16384\n" \
  "ds_read_b64_tr_b16 v[206:207], v19 offset:18432\n" \
  "ds_read_b64_tr_b16 v[208:209], v16 offset:20480\n" \
  "ds_read_b64_tr_b16 v[210:211], v16 offset:22528\n" \
  "ds_read_b64_tr_b16 v[212:213], v17 offset:20480\n" \
  "ds_read_b64_tr_b16 v[214:215], v17 offset:22528\n" \
  "ds_read_b64_tr_b16 v[216:217], v18 offset:20480\n" \
  "ds_read_b64_tr_b16 v[218:219], v18 offset:22528\n" \
  "ds_read_b64_tr_b16 v[220:221], v19 offset:20480\n" \
  "ds_read_b64_tr_b16 v[222:223], v19 offset:22528\n" \
  "ds_read_b64_tr_b16 v[224:225], v16 offset:24576\n" \
  "ds_read_b64_tr_b16 v[226:227], v16 offset:26624\n" \
  "ds_read_b64_tr_b16 v[228:229], v17 offset:24576\n" \
  "ds_read_b64_tr_b16 v[230:231], v17 offset:26624\n" \
  "ds_read_b64_tr_b16 v[232:233], v18 offset:24576\n" \
  "ds_read_b64_tr_b16 v[234:235], v18 offset:26624\n" \
  "ds_read_b64_tr_b16 v[236:237], v19 offset:24576\n" \
  "ds_read_b64_tr_b16 v[238:239], v19 offset:26624\n" \
  "ds_read_b64_tr_b16 v[240:241], v16 offset:28672\n" \
  "ds_read_b64_tr_b16 v[242:243], v16 offset:30720\n" \
  "ds_read_b64_tr_b16 v[244:245], v17 offset:28672\n" \
  "ds_read_b64_tr_b16 v[246:247], v17 offset:30720\n" \
  "ds_read_b64_tr_b16 v[248:249], v18 offset:28672\n" \
  "ds_read_b64_tr_b16 v[250:251], v18 offset:30720\n" \
  "ds_read_b64_tr_b16 v[252:253], v19 offset:28672\n" \
  "ds_read_b64_tr_b16 v[254:255], v19 offset:30720\n" \
  "s_waitcnt lgkmcnt(0)\n" \
  "s_add_u32 m0, s86, 16384\n" \
  "s_nop 0\n" \
  "buffer_load_dwordx4 v7, s[68:71], s90 offen lds\n" \
  "s_add_u32 s90, s90, 16384\n" \
  "s_add_u32 m0, s86, 17408\n" \
  "s_nop 0\n" \
  "buffer_load_dwordx4 v7, s[68:71], s90 offen lds\n" \
  "s_add_u32 s90, s90, 16384\n" \
  "s_add_u32 m0, s86, 18432\n" \
  "s_nop 0\n" \
  "buffer_load_dwordx4 v7, s[68:71], s90 offen lds\n" \
  "s_add_u32 s90, s90, 16384\n" \
  "s_add_u32 m0, s86, 19456\n" \
  "s_nop 0\n" \
  "buffer_load_dwordx4 v7, s[68:71], s90 offen lds\n" \
  "s_add_u32 s90, s90, 16384\n" \
  "s_add_u32 m0, s86, 20480\n" \
  "s_nop 0\n" \
  "buffer_load_dwordx4 v7, s[68:71], s90 offen lds\n" \
  "s_add_u32 s90, s90, 16384\n" \
  "s_add_u32 m0, s86, 21504\n" \
  "s_nop 0\n" \
  "buffer_load_dwordx4 v7, s[68:71], s90 offen lds\n" \
  "s_add_u32 s90, s90, 16384\n" \
  "s_add_u32 m0, s86, 22528\n" \
  "s_nop 0\n" \
  "buffer_load_dwordx4 v7, s[68:71], s90 offen lds\n" \
  "s_add_u32 s90, s90, 16384\n" \
  "s_add_u32 m0, s86, 23552\n" \
  "s_nop 0\n" \
  "buffer_load_dwordx4 v7, s[68:71], s90 offen lds\n" \
  "s_add_u32 s90, s90, 16384\n" \
  "s_add_u32 m0, s86, 24576\n" \
  "s_nop 0\n" \
  "buffer_load_dwordx4 v7, s[68:71], s90 offen lds\n" \
  "s_add_u32 s90, s90, 16384\n" \
  "s_add_u32 m0, s86, 25600\n" \
  "s_nop 0\n" \
  "buffer_load_dwordx4 v7, s[68:71], s90 offen lds\n" \
  "s_add_u32 s90, s90, 16384\n" \
  "s_add_u32 m0, s86, 26624\n" \
  "s_nop 0\n" \
  "buffer_load_dwordx4 v7, s[68:71], s90 offen lds\n" \
  "s_add_u32 s90, s90, 16384\n" \
  "s_add_u32 m0, s86, 27648\n" \
  "s_nop 0\n" \
  "buffer_load_dwordx4 v7, s[68:71], s90 offen lds\n" \
  "s_add_u32 s90, s90, 16384\n" \
  "s_add_u32 m0, s86, 28672\n" \
  "s_nop 0\n" \
  "buffer_load_dwordx4 v7, s[68:71], s90 offen lds\n" \
  "s_add_u32 s90, s90, 16384\n" \
  "s_add_u32 m0, s86, 29696\n" \
  "s_nop 0\n" \
  "buffer_load_dwordx4 v7, s[68:71], s90 offen lds\n" \
  "s_add_u32 s90, s90, 16384\n" \
  "s_add_u32 m0, s86, 30720\n" \
  "s_nop 0\n" \
  "buffer_load_dwordx4 v7, s[68:71], s90 offen lds\n" \
  "s_add_u32 s90, s90, 16384\n" \
  "s_add_u32 m0, s86, 31744\n" \
  "s_nop 0\n" \
  "buffer_load_dwordx4 v7, s[68:71], s90 offen lds\n" \
  "s_add_u32 s90, s90, 16384\n" \
  "s_waitcnt vmcnt(16)\n" \
  "ds_read_b64_tr_b16 a[0:1], v16 offset:0\n" \
  "ds_read_b64_tr_b16 a[2:3], v16 offset:2048\n" \
  "ds_read_b64_tr_b16 a[4:5], v17 offset:0\n" \
  "ds_read_b64_tr_b16 a[6:7], v17 offset:2048\n" \
  "ds_read_b64_tr_b16 a[8:9], v18 offset:0\n" \
  "ds_read_b64_tr_b16 a[10:11], v18 offset:2048\n" \
  "ds_read_b64_tr_b16 a[12:13], v19 offset:0\n" \
  "ds_read_b64_tr_b16 a[14:15], v19 offset:2048\n" \
  "ds_read_b64_tr_b16 a[16:17], v16 offset:4096\n" \
  "ds_read_b64_tr_b16 a[18:19], v16 offset:6144\n" \
  "ds_read_b64_tr_b16 a[20:21], v17 offset:4096\n" \
  "ds_read_b64_tr_b16 a[22:23], v17 offset:6144\n" \
  "ds_read_b64_tr_b16 a[24:25], v18 offset:4096\n" \
  "ds_read_b64_tr_b16 a[26:27], v18 offset:6144\n" \
  "ds_read_b64_tr_b16 a[28:29], v19 offset:4096\n" \
  "ds_read_b64_tr_b16 a[30:31], v19 offset:6144\n" \
  "ds_read_b64_tr_b16 a[32:33], v16 offset:8192\n" \
  "ds_read_b64_tr_b16 a[34:35], v16 offset:10240\n" \
  "ds_read_b64_tr_b16 a[36:37], v17 offset:8192\n" \
  "ds_read_b64_tr_b16 a[38:39], v17 offset:10240\n" \
  "ds_read_b64_tr_b16 a[40:41], v18 offset:8192\n" \
  "ds_read_b64_tr_b16 a[42:43], v18 offset:10240\n" \
  "ds_read_b64_tr_b16 a[44:45], v19 offset:8192\n" \
  "ds_read_b64_tr_b16 a[46:47], v19 offset:10240\n" \
  "ds_read_b64_tr_b16 a[48:49], v16 offset:12288\n" \
  "ds_read_b64_tr_b16 a[50:51], v16 offset:14336\n" \
  "ds_read_b64_tr_b16 a[52:53], v17 offset:12288\n" \
  "ds_read_b64_tr_b16 a[54:55], v17 offset:14336\n" \
  "ds_read_b64_tr_b16 a[56:57], v18 offset:12288\n" \
  "ds_read_b64_tr_b16 a[58:59], v18 offset:14336\n" \
  "ds_read_b64_tr_b16 a[60:61], v19 offset:12288\n" \
  "ds_read_b64_tr_b16 a[62:63], v19 offset:14336\n" \
  "s_waitcnt lgkmcnt(0)\n" \
  "s_add_u32 m0, s86, 0\n" \
  "s_nop 0\n" \
  "buffer_load_dwordx4 v7, s[68:71], s90 offen lds\n" \
  "s_add_u32 s90, s90, 16384\n" \
  "s_add_u32 m0, s86, 1024\n" \
  "s_nop 0\n" \
  "buffer_load_dwordx4 v7, s[68:71], s90 offen lds\n" \
  "s_add_u32 s90, s90, 16384\n" \
  "s_add_u32 m0, s86, 2048\n" \
  "s_nop 0\n" \
  "buffer_load_dwordx4 v7, s[68:71], s90 offen lds\n" \
  "s_add_u32 s90, s90, 16384\n" \
  "s_add_u32 m0, s86, 3072\n" \
  "s_nop 0\n" \
  "buffer_load_dwordx4 v7, s[68:71], s90 offen lds\n" \
  "s_add_u32 s90, s90, 16384\n" \
  "s_add_u32 m0, s86, 4096\n" \
  "s_nop 0\n" \
  "buffer_load_dwordx4 v7, s[68:71], s90 offen lds\n" \
  "s_add_u32 s90, s90, 16384\n" \
  "s_add_u32 m0, s86, 5120\n" \
  "s_nop 0\n" \
  "buffer_load_dwordx4 v7, s[68:71], s90 offen lds\n" \
  "s_add_u32 s90, s90, 16384\n" \
  "s_add_u32 m0, s86, 6144\n" \
  "s_nop 0\n" \
  "buffer_load_dwordx4 v7, s[68:71], s90 offen lds\n" \
  "s_add_u32 s90, s90, 16384\n" \
  "s_add_u32 m0, s86, 7168\n" \
  "s_nop 0\n" \
  "buffer_load_dwordx4 v7, s[68:71], s90 offen lds\n" \
  "s_add_u32 s90, s90, 16384\n" \
  "s_add_u32 m0, s86, 8192\n" \
  "s_nop 0\n" \
  "buffer_load_dwordx4 v7, s[68:71], s90 offen lds\n" \
  "s_add_u32 s90, s90, 16384\n" \
  "s_add_u32 m0, s86, 9216\n" \
  "s_nop 0\n" \
  "buffer_load_dwordx4 v7, s[68:71], s90 offen lds\n" \
  "s_add_u32 s90, s90, 16384\n" \
  "s_add_u32 m0, s86, 10240\n" \
  "s_nop 0\n" \
  "buffer_load_dwordx4 v7, s[68:71], s90 offen lds\n" \
  "s_add_u32 s90, s90, 16384\n" \
  "s_add_u32 m0, s86, 11264\n" \
  "s_nop 0\n" \
  "buffer_load_dwordx4 v7, s[68:71], s90 offen lds\n" \
  "s_add_u32 s90, s90, 16384\n" \
  "s_add_u32 m0, s86, 12288\n" \
  "s_nop 0\n" \
  "buffer_load_dwordx4 v7, s[68:71], s90 offen lds\n" \
  "s_add_u32 s90, s90, 16384\n" \
  "s_add_u32 m0, s86, 13312\n" \
  "s_nop 0\n" \
  "buffer_load_dwordx4 v7, s[68:71], s90 offen lds\n" \
  "s_add_u32 s90, s90, 16384\n" \
  "s_add_u32 m0, s86, 14336\n" \
  "s_nop 0\n" \
  "buffer_load_dwordx4 v7, s[68:71], s90 offen lds\n" \
  "s_add_u32 s90, s90, 16384\n" \
  "s_add_u32 m0, s86, 15360\n" \
  "s_nop 0\n" \
  "buffer_load_dwordx4 v7, s[68:71], s90 offen lds\n" \
  "s_add_u32 s90, s90, 16384\n" \
  "s_waitcnt vmcnt(16)\n" \
  "ds_read_b64_tr_b16 a[64:65], v16 offset:16384\n" \
  "ds_read_b64_tr_b16 a[66:67], v16 offset:18432\n" \
  "ds_read_b64_tr_b16 a[68:69], v17 offset:16384\n" \
  "ds_read_b64_tr_b16 a[70:71], v17 offset:18432\n" \
  "ds_read_b64_tr_b16 a[72:73], v18 offset:16384\n" \
  "ds_read_b64_tr_b16 a[74:75], v18 offset:18432\n" \
  "ds_read_b64_tr_b16 a[76:77], v19 offset:16384\n" \
  "ds_read_b64_tr_b16 a[78:79], v19 offset:18432\n" \
  "ds_read_b64_tr_b16 a[80:81], v16 offset:20480\n" \
  "ds_read_b64_tr_b16 a[82:83], v16 offset:22528\n" \
  "ds_read_b64_tr_b16 a[84:85], v17 offset:20480\n" \
  "ds_read_b64_tr_b16 a[86:87], v17 offset:22528\n" \
  "ds_read_b64_tr_b16 a[88:89], v18 offset:20480\n" \
  "ds_read_b64_tr_b16 a[90:91], v18 offset:22528\n" \
  "ds_read_b64_tr_b16 a[92:93], v19 offset:20480\n" \
  "ds_read_b64_tr_b16 a[94:95], v19 offset:22528\n" \
  "ds_read_b64_tr_b16 a[96:97], v16 offset:24576\n" \
  "ds_read_b64_tr_b16 a[98:99], v16 offset:26624\n" \
  "ds_read_b64_tr_b16 a[100:101], v17 offset:24576\n" \
  "ds_read_b64_tr_b16 a[102:103], v17 offset:26624\n" \
  "ds_read_b64_tr_b16 a[104:105], v18 offset:24576\n" \
  "ds_read_b64_tr_b16 a[106:107], v18 offset:26624\n" \
  "ds_read_b64_tr_b16 a[108:109], v19 offset:24576\n" \
  "ds_read_b64_tr_b16 a[110:111], v19 offset:26624\n" \
  "ds_read_b64_tr_b16 a[112:113], v16 offset:28672\n" \
  "ds_read_b64_tr_b16 a[114:115], v16 offset:30720\n" \
  "ds_read_b64_tr_b16 a[116:117], v17 offset:28672\n" \
  "ds_read_b64_tr_b16 a[118:119], v17 offset:30720\n" \
  "ds_read_b64_tr_b16 a[120:121], v18 offset:28672\n" \
  "ds_read_b64_tr_b16 a[122:123], v18 offset:30720\n" \
  "ds_read_b64_tr_b16 a[124:125], v19 offset:28672\n" \
  "ds_read_b64_tr_b16 a[126:127], v19 offset:30720\n" \
  "s_waitcnt lgkmcnt(0)\n" \
  "s_add_u32 m0, s86, 16384\n" \
  "s_nop 0\n" \
  "buffer_load_dwordx4 v7, s[68:71], s90 offen lds\n" \
  "s_add_u32 s90, s90, 16384\n" \
  "s_add_u32 m0, s86, 17408\n" \
  "s_nop 0\n" \
  "buffer_load_dwordx4 v7, s[68:71], s90 offen lds\n" \
  "s_add_u32 s90, s90, 16384\n" \
  "s_add_u32 m0, s86, 18432\n" \
  "s_nop 0\n" \
  "buffer_load_dwordx4 v7, s[68:71], s90 offen lds\n" \
  "s_add_u32 s90, s90, 16384\n" \
  "s_add_u32 m0, s86, 19456\n" \
  "s_nop 0\n" \
  "buffer_load_dwordx4 v7, s[68:71], s90 offen lds\n" \
  "s_add_u32 s90, s90, 16384\n" \
  "s_add_u32 m0, s86, 20480\n" \
  "s_nop 0\n" \
  "buffer_load_dwordx4 v7, s[68:71], s90 offen lds\n" \
  "s_add_u32 s90, s90, 16384\n" \
  "s_add_u32 m0, s86, 21504\n" \
  "s_nop 0\n" \
  "buffer_load_dwordx4 v7, s[68:71], s90 offen lds\n" \
  "s_add_u32 s90, s90, 16384\n" \
  "s_add_u32 m0, s86, 22528\n" \
  "s_nop 0\n" \
  "buffer_load_dwordx4 v7, s[68:71], s90 offen lds\n" \
  "s_add_u32 s90, s90, 16384\n" \
  "s_add_u32 m0, s86, 23552\n" \
  "s_nop 0\n" \
  "buffer_load_dwordx4 v7, s[68:71], s90 offen lds\n" \
  "s_add_u32 s90, s90, 16384\n" \
  "s_add_u32 m0, s86, 24576\n" \
  "s_nop 0\n" \
  "buffer_load_dwordx4 v7, s[68:71], s90 offen lds\n" \
  "s_add_u32 s90, s90, 16384\n" \
  "s_add_u32 m0, s86, 25600\n" \
  "s_nop 0\n" \
  "buffer_load_dwordx4 v7, s[68:71], s90 offen lds\n" \
  "s_add_u32 s90, s90, 16384\n" \
  "s_add_u32 m0, s86, 26624\n" \
  "s_nop 0\n" \
  "buffer_load_dwordx4 v7, s[68:71], s90 offen lds\n" \
  "s_add_u32 s90, s90, 16384\n" \
  "s_add_u32 m0, s86, 27648\n" \
  "s_nop 0\n" \
  "buffer_load_dwordx4 v7, s[68:71], s90 offen lds\n" \
  "s_add_u32 s90, s90, 16384\n" \
  "s_add_u32 m0, s86, 28672\n" \
  "s_nop 0\n" \
  "buffer_load_dwordx4 v7, s[68:71], s90 offen lds\n" \
  "s_add_u32 s90, s90, 16384\n" \
  "s_add_u32 m0, s86, 29696\n" \
  "s_nop 0\n" \
  "buffer_load_dwordx4 v7, s[68:71], s90 offen lds\n" \
  "s_add_u32 s90, s90, 16384\n" \
  "s_add_u32 m0, s86, 30720\n" \
  "s_nop 0\n" \
  "buffer_load_dwordx4 v7, s[68:71], s90 offen lds\n" \
  "s_add_u32 s90, s90, 16384\n" \
  "s_add_u32 m0, s86, 31744\n" \
  "s_nop 0\n" \
  "buffer_load_dwordx4 v7, s[68:71], s90 offen lds\n" \
  "s_add_u32 s90, s90, 16384\n" \
  "s_waitcnt vmcnt(16)\n" \
  "ds_read_b64_tr_b16 a[128:129], v16 offset:0\n" \
  "ds_read_b64_tr_b16 a[130:131], v16 offset:2048\n" \
  "ds_read_b64_tr_b16 a[132:133], v17 offset:0\n" \
  "ds_read_b64_tr_b16 a[134:135], v17 offset:2048\n" \
  "ds_read_b64_tr_b16 a[136:137], v18 offset:0\n" \
  "ds_read_b64_tr_b16 a[138:139], v18 offset:2048\n" \
  "ds_read_b64_tr_b16 a[140:141], v19 offset:0\n" \
  "ds_read_b64_tr_b16 a[142:143], v19 offset:2048\n" \
  "ds_read_b64_tr_b16 a[144:145], v16 offset:4096\n" \
  "ds_read_b64_tr_b16 a[146:147], v16 offset:6144\n" \
  "ds_read_b64_tr_b16 a[148:149], v17 offset:4096\n" \
  "ds_read_b64_tr_b16 a[150:151], v17 offset:6144\n" \
  "ds_read_b64_tr_b16 a[152:153], v18 offset:4096\n" \
  "ds_read_b64_tr_b16 a[154:155], v18 offset:6144\n" \
  "ds_read_b64_tr_b16 a[156:157], v19 offset:4096\n" \
  "ds_read_b64_tr_b16 a[158:159], v19 offset:6144\n" \
  "ds_read_b64_tr_b16 a[160:161], v16 offset:8192\n" \
  "ds_read_b64_tr_b16 a[162:163], v16 offset:10240\n" \
  "ds_read_b64_tr_b16 a[164:165], v17 offset:8192\n" \
  "ds_read_b64_tr_b16 a[166:167], v17 offset:10240\n" \
  "ds_read_b64_tr_b16 a[168:169], v18 offset:8192\n" \
  "ds_read_b64_tr_b16 a[170:171], v18 offset:10240\n" \
  "ds_read_b64_tr_b16 a[172:173], v19 offset:8192\n" \
  "ds_read_b64_tr_b16 a[174:175], v19 offset:10240\n" \
  "ds_read_b64_tr_b16 a[176:177], v16 offset:12288\n" \
  "ds_read_b64_tr_b16 a[178:179], v16 offset:14336\n" \
  "ds_read_b64_tr_b16 a[180:181], v17 offset:12288\n" \
  "ds_read_b64_tr_b16 a[182:183], v17 offset:14336\n" \
  "ds_read_b64_tr_b16 a[184:185], v18 offset:12288\n" \
  "ds_read_b64_tr_b16 a[186:187], v18 offset:14336\n" \
  "ds_read_b64_tr_b16 a[188:189], v19 offset:12288\n" \
  "ds_read_b64_tr_b16 a[190:191], v19 offset:14336\n" \
  "s_waitcnt lgkmcnt(0)\n" \
  "s_add_u32 m0, s86, 0\n" \
  "s_nop 0\n" \
  "buffer_load_dwordx4 v7, s[68:71], s90 offen lds\n" \
  "s_add_u32 s90, s90, 16384\n" \
  "s_add_u32 m0, s86, 1024\n" \
  "s_nop 0\n" \
  "buffer_load_dwordx4 v7, s[68:71], s90 offen lds\n" \
  "s_add_u32 s90, s90, 16384\n" \
  "s_add_u32 m0, s86, 2048\n" \
  "s_nop 0\n" \
  "buffer_load_dwordx4 v7, s[68:71], s90 offen lds\n" \
  "s_add_u32 s90, s90, 16384\n" \
  "s_add_u32 m0, s86, 3072\n" \
  "s_nop 0\n" \
  "buffer_load_dwordx4 v7, s[68:71], s90 offen lds\n" \
  "s_add_u32 s90, s90, 16384\n" \
  "s_add_u32 m0, s86, 4096\n" \
  "s_nop 0\n" \
  "buffer_load_dwordx4 v7, s[68:71], s90 offen lds\n" \
  "s_add_u32 s90, s90, 16384\n" \
  "s_add_u32 m0, s86, 5120\n" \
  "s_nop 0\n" \
  "buffer_load_dwordx4 v7, s[68:71], s90 offen lds\n" \
  "s_add_u32 s90, s90, 16384\n" \
  "s_add_u32 m0, s86, 6144\n" \
  "s_nop 0\n" \
  "buffer_load_dwordx4 v7, s[68:71], s90 offen lds\n" \
  "s_add_u32 s90, s90, 16384\n" \
  "s_add_u32 m0, s86, 7168\n" \
  "s_nop 0\n" \
  "buffer_load_dwordx4 v7, s[68:71], s90 offen lds\n" \
  "s_add_u32 s90, s90, 16384\n" \
  "s_add_u32 m0, s86, 8192\n" \
  "s_nop 0\n" \
  "buffer_load_dwordx4 v7, s[68:71], s90 offen lds\n" \
  "s_add_u32 s90, s90, 16384\n" \
  "s_add_u32 m0, s86, 9216\n" \
  "s_nop 0\n" \
  "buffer_load_dwordx4 v7, s[68:71], s90 offen lds\n" \
  "s_add_u32 s90, s90, 16384\n" \
  "s_add_u32 m0, s86, 10240\n" \
  "s_nop 0\n" \
  "buffer_load_dwordx4 v7, s[68:71], s90 offen lds\n" \
  "s_add_u32 s90, s90, 16384\n" \
  "s_add_u32 m0, s86, 11264\n" \
  "s_nop 0\n" \
  "buffer_load_dwordx4 v7, s[68:71], s90 offen lds\n" \
  "s_add_u32 s90, s90, 16384\n" \
  "s_add_u32 m0, s86, 12288\n" \
  "s_nop 0\n" \
  "buffer_load_dwordx4 v7, s[68:71], s90 offen lds\n" \
  "s_add_u32 s90, s90, 16384\n" \
  "s_add_u32 m0, s86, 13312\n" \
  "s_nop 0\n" \
  "buffer_load_dwordx4 v7, s[68:71], s90 offen lds\n" \
  "s_add_u32 s90, s90, 16384\n" \
  "s_add_u32 m0, s86, 14336\n" \
  "s_nop 0\n" \
  "buffer_load_dwordx4 v7, s[68:71], s90 offen lds\n" \
  "s_add_u32 s90, s90, 16384\n" \
  "s_add_u32 m0, s86, 15360\n" \
  "s_nop 0\n" \
  "buffer_load_dwordx4 v7, s[68:71], s90 offen lds\n" \
  "s_add_u32 s90, s90, 16384\n" \
  "s_waitcnt vmcnt(16)\n" \
  "ds_read_b64_tr_b16 a[192:193], v16 offset:16384\n" \
  "ds_read_b64_tr_b16 a[194:195], v16 offset:18432\n" \
  "ds_read_b64_tr_b16 a[196:197], v17 offset:16384\n" \
  "ds_read_b64_tr_b16 a[198:199], v17 offset:18432\n" \
  "ds_read_b64_tr_b16 a[200:201], v18 offset:16384\n" \
  "ds_read_b64_tr_b16 a[202:203], v18 offset:18432\n" \
  "ds_read_b64_tr_b16 a[204:205], v19 offset:16384\n" \
  "ds_read_b64_tr_b16 a[206:207], v19 offset:18432\n" \
  "ds_read_b64_tr_b16 a[208:209], v16 offset:20480\n" \
  "ds_read_b64_tr_b16 a[210:211], v16 offset:22528\n" \
  "ds_read_b64_tr_b16 a[212:213], v17 offset:20480\n" \
  "ds_read_b64_tr_b16 a[214:215], v17 offset:22528\n" \
  "ds_read_b64_tr_b16 a[216:217], v18 offset:20480\n" \
  "ds_read_b64_tr_b16 a[218:219], v18 offset:22528\n" \
  "ds_read_b64_tr_b16 a[220:221], v19 offset:20480\n" \
  "ds_read_b64_tr_b16 a[222:223], v19 offset:22528\n" \
  "ds_read_b64_tr_b16 a[224:225], v16 offset:24576\n" \
  "ds_read_b64_tr_b16 a[226:227], v16 offset:26624\n" \
  "ds_read_b64_tr_b16 a[228:229], v17 offset:24576\n" \
  "ds_read_b64_tr_b16 a[230:231], v17 offset:26624\n" \
  "ds_read_b64_tr_b16 a[232:233], v18 offset:24576\n" \
  "ds_read_b64_tr_b16 a[234:235], v18 offset:26624\n" \
  "ds_read_b64_tr_b16 a[236:237], v19 offset:24576\n" \
  "ds_read_b64_tr_b16 a[238:239], v19 offset:26624\n" \
  "ds_read_b64_tr_b16 a[240:241], v16 offset:28672\n" \
  "ds_read_b64_tr_b16 a[242:243], v16 offset:30720\n" \
  "ds_read_b64_tr_b16 a[244:245], v17 offset:28672\n" \
  "ds_read_b64_tr_b16 a[246:247], v17 offset:30720\n" \
  "ds_read_b64_tr_b16 a[248:249], v18 offset:28672\n" \
  "ds_read_b64_tr_b16 a[250:251], v18 offset:30720\n" \
  "ds_read_b64_tr_b16 a[252:253], v19 offset:28672\n" \
  "ds_read_b64_tr_b16 a[254:255], v19 offset:30720\n" \
  "s_waitcnt lgkmcnt(0)\n" \
  "s_add_u32 m0, s86, 16384\n" \
  "s_nop 0\n" \
  "buffer_load_dwordx4 v7, s[68:71], s90 offen lds\n" \
  "s_add_u32 s90, s90, 16384\n" \
  "s_add_u32 m0, s86, 17408\n" \
  "s_nop 0\n" \
  "buffer_load_dwordx4 v7, s[68:71], s90 offen lds\n" \
  "s_add_u32 s90, s90, 16384\n" \
  "s_add_u32 m0, s86, 18432\n" \
  "s_nop 0\n" \
  "buffer_load_dwordx4 v7, s[68:71], s90 offen lds\n" \
  "s_add_u32 s90, s90, 16384\n" \
  "s_add_u32 m0, s86, 19456\n" \
  "s_nop 0\n" \
  "buffer_load_dwordx4 v7, s[68:71], s90 offen lds\n" \
  "s_add_u32 s90, s90, 16384\n" \
  "s_add_u32 m0, s86, 20480\n" \
  "s_nop 0\n" \
  "buffer_load_dwordx4 v7, s[68:71], s90 offen lds\n" \
  "s_add_u32 s90, s90, 16384\n" \
  "s_add_u32 m0, s86, 21504\n" \
  "s_nop 0\n" \
  "buffer_load_dwordx4 v7, s[68:71], s90 offen lds\n" \
  "s_add_u32 s90, s90, 16384\n" \
  "s_add_u32 m0, s86, 22528\n" \
  "s_nop 0\n" \
  "buffer_load_dwordx4 v7, s[68:71], s90 offen lds\n" \
  "s_add_u32 s90, s90, 16384\n" \
  "s_add_u32 m0, s86, 23552\n" \
  "s_nop 0\n" \
  "buffer_load_dwordx4 v7, s[68:71], s90 offen lds\n" \
  "s_add_u32 s90, s90, 16384\n" \
  "s_add_u32 m0, s86, 24576\n" \
  "s_nop 0\n" \
  "buffer_load_dwordx4 v7, s[68:71], s90 offen lds\n" \
  "s_add_u32 s90, s90, 16384\n" \
  "s_add_u32 m0, s86, 25600\n" \
  "s_nop 0\n" \
  "buffer_load_dwordx4 v7, s[68:71], s90 offen lds\n" \
  "s_add_u32 s90, s90, 16384\n" \
  "s_add_u32 m0, s86, 26624\n" \
  "s_nop 0\n" \
  "buffer_load_dwordx4 v7, s[68:71], s90 offen lds\n" \
  "s_add_u32 s90, s90, 16384\n" \
  "s_add_u32 m0, s86, 27648\n" \
  "s_nop 0\n" \
  "buffer_load_dwordx4 v7, s[68:71], s90 offen lds\n" \
  "s_add_u32 s90, s90, 16384\n" \
  "s_add_u32 m0, s86, 28672\n" \
  "s_nop 0\n" \
  "buffer_load_dwordx4 v7, s[68:71], s90 offen lds\n" \
  "s_add_u32 s90, s90, 16384\n" \
  "s_add_u32 m0, s86, 29696\n" \
  "s_nop 0\n" \
  "buffer_load_dwordx4 v7, s[68:71], s90 offen lds\n" \
  "s_add_u32 s90, s90, 16384\n" \
  "s_add_u32 m0, s86, 30720\n" \
  "s_nop 0\n" \
  "buffer_load_dwordx4 v7, s[68:71], s90 offen lds\n" \
  "s_add_u32 s90, s90, 16384\n" \
  "s_add_u32 m0, s86, 31744\n" \
  "s_nop 0\n" \
  "buffer_load_dwordx4 v7, s[68:71], s90 offen lds\n" \
  "s_add_u32 s90, s90, 16384\n" \
  "s_mov_b32 s92, 1\n" \
  "v_xor_b32 v7, 64, v12\n" \
  "LP0_%=:\n" \
  "buffer_load_dword v9, v8, s[80:83], 0 offen sc1\n" \
  "s_waitcnt vmcnt(0)\n" \
  "v_cmp_gt_u32 vcc, s92, v9\n" \
  "s_cbranch_vccz LP1_%=\n" \
  "s_sleep 8\n" \
  "s_add_u32 s91, s91, 1\n" \
  "s_cmp_lt_u32 s91, " STR(SPIN_CAP) "\n" \
  "s_cbranch_scc1 LP0_%=\n" \
  "v_mov_b32 v5, " STR(CW_DEAD) "*4\n" \
  "v_mov_b32 v15, 1\n" \
  "buffer_store_dword v15, v5, s[80:83], 0 offen sc1\n" \
  "s_branch LP1_%=\n" \
  "LP1_%=:\n" \
  "s_add_u32 s84, s87, 0\n" \
  "buffer_load_dwordx4 v[64:67], v6, s[72:75], s84 offen sc1\n" \
  "s_add_u32 s84, s87, 1024\n" \
  "buffer_load_dwordx4 v[68:71], v6, s[72:75], s84 offen sc1\n" \
  "s_add_u32 s84, s87, 2048\n" \
  "buffer_load_dwordx4 v[72:75], v6, s[72:75], s84 offen sc1\n" \
  "s_add_u32 s84, s87, 49152\n" \
  "buffer_load_dwordx4 v[76:79], v6, s[72:75], s84 offen sc1\n" \
  "s_add_u32 s84, s87, 3072\n" \
  "buffer_load_dwordx4 v[80:83], v6, s[72:75], s84 offen sc1\n" \
  "s_add_u32 s84, s87, 4096\n" \
  "buffer_load_dwordx4 v[84:87], v6, s[72:75], s84 offen sc1\n" \
  "s_add_u32 s84, s87, 5120\n" \
  "buffer_load_dwordx4 v[88:91], v6, s[72:75], s84 offen sc1\n" \
  "s_add_u32 s84, s87, 50176\n" \
  "buffer_load_dwordx4 v[92:95], v6, s[72:75], s84 offen sc1\n" \
  "s_add_u32 s84, s87, 6144\n" \
  "buffer_load_dwordx4 v[96:99], v6, s[72:75], s84 offen sc1\n" \
  "s_add_u32 s84, s87, 7168\n" \
  "buffer_load_dwordx4 v[100:103], v6, s[72:75], s84 offen sc1\n" \
  "s_add_u32 s84, s87, 16384\n" \
  "buffer_load_dwordx4 v[104:107], v6, s[72:75], s84 offen sc1\n" \
  "s_add_u32 s84, s87, 51200\n" \
  "buffer_load_dwordx4 v[108:111], v6, s[72:75], s84 offen sc1\n" \
  "s_add_u32 s84, s87, 17408\n" \
  "buffer_load_dwordx4 v[112:115], v6, s[72:75], s84 offen sc1\n" \
  "s_add_u32 s84, s87, 18432\n" \
  "buffer_load_dwordx4 v[116:119], v6, s[72:75], s84 offen sc1\n" \
  "s_add_u32 s84, s87, 19456\n" \
  "buffer_load_dwordx4 v[120:123], v6, s[72:75], s84 offen sc1\n" \
  "s_add_u32 s84, s87, 52224\n" \
  "buffer_load_dwordx4 v[124:127], v6, s[72:75], s84 offen sc1\n" \
  "v_or_b32 v10, 0x80000000, v10\n" \
  "s_sub_u32 s88, s88, 131072\n" \
  ".p2align 6\n" \
  "LX_%=:\n" \
  "s_waitcnt vmcnt(15)\n" \
  "v_mfma_f32_16x16x32_bf16 v[20:23], v[128:131], v[64:67], 0\n" \
  "v_mfma_f32_16x16x32_bf16 v[24:27], v[132:135], v[64:67], 0\n" \
  "v_mfma_f32_16x16x32_bf16 v[28:31], v[136:139], v[64:67], 0\n" \
  "v_mfma_f32_16x16x32_bf16 v[32:35], v[140:143], v[64:67], 0\n" \
  "s_add_u32 s84, s87, 20480\n" \
  "buffer_load_dwordx4 v[64:67], v6, s[72:75], s84 offen sc1\n" \
  "s_waitcnt vmcnt(15)\n" \
  "v_mfma_f32_16x16x32_bf16 v[20:23], v[144:147], v[68:71], v[20:23]\n" \
  "v_mfma_f32_16x16x32_bf16 v[24:27], v[148:151], v[68:71], v[24:27]\n" \
  "v_mfma_f32_16x16x32_bf16 v[28:31], v[152:155], v[68:71], v[28:31]\n" \
  "v_mfma_f32_16x16x32_bf16 v[32:35], v[156:159], v[68:71], v[32:35]\n" \
  "s_add_u32 s84, s87, 21504\n" \
  "buffer_load_dwordx4 v[68:71], v6, s[72:75], s84 offen sc1\n" \
  "s_waitcnt vmcnt(15)\n" \
  "v_mfma_f32_16x16x32_bf16 v[20:23], v[160:163], v[72:75], v[20:23]\n" \
  "ds_read_b64_tr_b16 v[40:41], v16 offset:0\n" \
  "ds_read_b64_tr_b16 v[42:43], v16 offset:2048\n" \
  "v_mfma_f32_16x16x32_bf16 v[24:27], v[164:167], v[72:75], v[24:27]\n" \
  "ds_read_b64_tr_b16 v[44:45], v17 offset:0\n" \
  "ds_read_b64_tr_b16 v[46:47], v17 offset:2048\n" \
  "v_mfma_f32_16x16x32_bf16 v[28:31], v[168:171], v[72:75], v[28:31]\n" \
  "ds_read_b64_tr_b16 v[48:49], v18 offset:0\n" \
  "ds_read_b64_tr_b16 v[50:51], v18 offset:2048\n" \
  "v_mfma_f32_16x16x32_bf16 v[32:35], v[172:175], v[72:75], v[32:35]\n" \
  "ds_read_b64_tr_b16 v[52:53], v19 offset:0\n" \
  "ds_read_b64_tr_b16 v[54:55], v19 offset:2048\n" \
  "s_add_u32 s84, s87, 22528\n" \
  "buffer_load_dwordx4 v[72:75], v6, s[72:75], s84 offen sc1\n" \
  "s_waitcnt vmcnt(15)\n" \
  "s_waitcnt lgkmcnt(6)\n" \
  "v_mfma_f32_16x16x32_bf16 v[20:23], v[40:43], v[76:79], v[20:23]\n" \
  "s_waitcnt lgkmcnt(4)\n" \
  "v_mfma_f32_16x16x32_bf16 v[24:27], v[44:47], v[76:79], v[24:27]\n" \
  "s_waitcnt lgkmcnt(2)\n" \
  "v_mfma_f32_16x16x32_bf16 v[28:31], v[48:51], v[76:79], v[28:31]\n" \
  "s_waitcnt lgkmcnt(0)\n" \
  "v_mfma_f32_16x16x32_bf16 v[32:35], v[52:55], v[76:79], v[32:35]\n" \
  "s_add_u32 s84, s87, 53248\n" \
  "buffer_load_dwordx4 v[76:79], v6, s[72:75], s84 offen sc1\n" \
  "s_waitcnt lgkmcnt(0)\n" \
  "s_barrier\n" \
  "ds_read_b128 v[52:55], v11 offset:0\n" \
  "ds_read_b128 v[56:59], v11 offset:4096\n" \
  "ds_read_b128 v[60:63], v11 offset:8192\n" \
  "ds_read_b128 v[36:39], v11 offset:12288\n" \
  "s_waitcnt vmcnt(15)\n" \
  "v_mfma_f32_16x16x32_bf16 v[20:23], v[176:179], v[80:83], v[20:23]\n" \
  "v_mfma_f32_16x16x32_bf16 v[24:27], v[180:183], v[80:83], v[24:27]\n" \
  "v_mfma_f32_16x16x32_bf16 v[28:31], v[184:187], v[80:83], v[28:31]\n" \
  "v_mfma_f32_16x16x32_bf16 v[32:35], v[188:191], v[80:83], v[32:35]\n" \
  "s_add_u32 s84, s87, 23552\n" \
  "buffer_load_dwordx4 v[80:83], v6, s[72:75], s84 offen sc1\n" \
  "s_waitcnt lgkmcnt(0)\n" \
  "s_waitcnt vmcnt(15)\n" \
  "v_mfma_f32_16x16x32_bf16 v[20:23], v[192:195], v[84:87], v[20:23]\n" \
  "v_add_f32 v36, v36, v52\n" \
  "v_add_f32 v37, v37, v53\n" \
  "v_add_f32 v38, v38, v54\n" \
  "v_add_f32 v39, v39, v55\n" \
  "v_mfma_f32_16x16x32_bf16 v[24:27], v[196:199], v[84:87], v[24:27]\n" \
  "v_add_f32 v56, v56, v60\n" \
  "v_add_f32 v57, v57, v61\n" \
  "v_add_f32 v58, v58, v62\n" \
  "v_add_f32 v59, v59, v63\n" \
  "v_mfma_f32_16x16x32_bf16 v[28:31], v[200:203], v[84:87], v[28:31]\n" \
  "v_add_f32 v36, v36, v56\n" \
  "v_add_f32 v37, v37, v57\n" \
  "v_add_f32 v38, v38, v58\n" \
  "v_add_f32 v39, v39, v59\n" \
  "v_mfma_f32_16x16x32_bf16 v[32:35], v[204:207], v[84:87], v[32:35]\n" \
  "v_cvt_pk_bf16_f32 v36, v36, v37\n" \
  "v_cvt_pk_bf16_f32 v37, v38, v39\n" \
  "buffer_store_dwordx2 v[36:37], v10, s[76:79], s88 offen sc1\n" \
  "s_add_u32 s88, s88, 131072\n" \
  "v_and_b32 v10, 0x7fffffff, v10\n" \
  "s_add_u32 s84, s87, 32768\n" \
  "buffer_load_dwordx4 v[84:87], v6, s[72:75], s84 offen sc1\n" \
  "s_waitcnt vmcnt(16)\n" \
  "v_mfma_f32_16x16x32_bf16 v[20:23], v[208:211], v[88:91], v[20:23]\n" \
  "ds_read_b64_tr_b16 v[40:41], v16 offset:4096\n" \
  "ds_read_b64_tr_b16 v[42:43], v16 offset:6144\n" \
  "v_mfma_f32_16x16x32_bf16 v[24:27], v[212:215], v[88:91], v[24:27]\n" \
  "ds_read_b64_tr_b16 v[44:45], v17 offset:4096\n" \
  "ds_read_b64_tr_b16 v[46:47], v17 offset:6144\n" \
  "v_mfma_f32_16x16x32_bf16 v[28:31], v[216:219], v[88:91], v[28:31]\n" \
  "ds_read_b64_tr_b16 v[48:49], v18 offset:4096\n" \
  "ds_read_b64_tr_b16 v[50:51], v18 offset:6144\n" \
  "v_mfma_f32_16x16x32_bf16 v[32:35], v[220:223], v[88:91], v[32:35]\n" \
  "ds_read_b64_tr_b16 v[52:53], v19 offset:4096\n" \
  "ds_read_b64_tr_b16 v[54:55], v19 offset:6144\n" \
  "s_add_u32 s84, s87, 33792\n" \
  "buffer_load_dwordx4 v[88:91], v6, s[72:75], s84 offen sc1\n" \
  "s_waitcnt vmcnt(16)\n" \
  "s_waitcnt lgkmcnt(6)\n" \
  "v_mfma_f32_16x16x32_bf16 v[20:23], v[40:43], v[92:95], v[20:23]\n" \
  "s_waitcnt lgkmcnt(4)\n" \
  "v_mfma_f32_16x16x32_bf16 v[24:27], v[44:47], v[92:95], v[24:27]\n" \
  "s_waitcnt lgkmcnt(2)\n" \
  "v_mfma_f32_16x16x32_bf16 v[28:31], v[48:51], v[92:95], v[28:31]\n" \
  "s_waitcnt lgkmcnt(0)\n" \
  "v_mfma_f32_16x16x32_bf16 v[32:35], v[52:55], v[92:95], v[32:35]\n" \
  "s_add_u32 s84, s87, 54272\n" \
  "buffer_load_dwordx4 v[92:95], v6, s[72:75], s84 offen sc1\n" \
  "s_waitcnt vmcnt(16)\n" \
  "v_mfma_f32_16x16x32_bf16 v[20:23], v[224:227], v[96:99], v[20:23]\n" \
  "buffer_load_dword v9, v8, s[80:83], 0 offen sc1\n" \
  "v_mfma_f32_16x16x32_bf16 v[24:27], v[228:231], v[96:99], v[24:27]\n" \
  "v_xor_b32 v11, 0x4000, v11\n" \
  "v_xor_b32 v7, 0x4000, v7\n" \
  "v_mfma_f32_16x16x32_bf16 v[28:31], v[232:235], v[96:99], v[28:31]\n" \
  "v_xor_b32 v12, 0x4000, v12\n" \
  "v_xor_b32 v13, 0x4000, v13\n" \
  "v_xor_b32 v14, 0x4000, v14\n" \
  "v_mfma_f32_16x16x32_bf16 v[32:35], v[236:239], v[96:99], v[32:35]\n" \
  "s_add_u32 s84, s87, 34816\n" \
  "buffer_load_dwordx4 v[96:99], v6, s[72:75], s84 offen sc1\n" \
  "s_waitcnt vmcnt(17)\n" \
  "v_mfma_f32_16x16x32_bf16 v[20:23], v[240:243], v[100:103], v[20:23]\n" \
  "v_mfma_f32_16x16x32_bf16 v[24:27], v[244:247], v[100:103], v[24:27]\n" \
  "v_mfma_f32_16x16x32_bf16 v[28:31], v[248:251], v[100:103], v[28:31]\n" \
  "v_mfma_f32_16x16x32_bf16 v[32:35], v[252:255], v[100:103], v[32:35]\n" \
  "s_add_u32 s84, s87, 35840\n" \
  "buffer_load_dwordx4 v[100:103], v6, s[72:75], s84 offen sc1\n" \
  "s_waitcnt vmcnt(17)\n" \
  "v_mfma_f32_16x16x32_bf16 v[20:23], a[0:3], v[104:107], v[20:23]\n" \
  "ds_read_b64_tr_b16 v[40:41], v16 offset:8192\n" \
  "ds_read_b64_tr_b16 v[42:43], v16 offset:10240\n" \
  "v_mfma_f32_16x16x32_bf16 v[24:27], a[4:7], v[104:107], v[24:27]\n" \
  "ds_read_b64_tr_b16 v[44:45], v17 offset:8192\n" \
  "ds_read_b64_tr_b16 v[46:47], v17 offset:10240\n" \
  "v_mfma_f32_16x16x32_bf16 v[28:31], a[8:11], v[104:107], v[28:31]\n" \
  "ds_read_b64_tr_b16 v[48:49], v18 offset:8192\n" \
  "ds_read_b64_tr_b16 v[50:51], v18 offset:10240\n" \
  "v_mfma_f32_16x16x32_bf16 v[32:35], a[12:15], v[104:107], v[32:35]\n" \
  "ds_read_b64_tr_b16 v[52:53], v19 offset:8192\n" \
  "ds_read_b64_tr_b16 v[54:55], v19 offset:10240\n" \
  "s_add_u32 s84, s87, 36864\n" \
  "buffer_load_dwordx4 v[104:107], v6, s[72:75], s84 offen sc1\n" \
  "s_waitcnt vmcnt(17)\n" \
  "s_waitcnt lgkmcnt(6)\n" \
  "v_mfma_f32_16x16x32_bf16 v[20:23], v[40:43], v[108:111], v[20:23]\n" \
  "s_waitcnt lgkmcnt(4)\n" \
  "v_mfma_f32_16x16x32_bf16 v[24:27], v[44:47], v[108:111], v[24:27]\n" \
  "s_waitcnt lgkmcnt(2)\n" \
  "v_mfma_f32_16x16x32_bf16 v[28:31], v[48:51], v[108:111], v[28:31]\n" \
  "s_waitcnt lgkmcnt(0)\n" \
  "v_mfma_f32_16x16x32_bf16 v[32:35], v[52:55], v[108:111], v[32:35]\n" \
  "s_add_u32 s84, s87, 55296\n" \
  "buffer_load_dwordx4 v[108:111], v6, s[72:75], s84 offen sc1\n" \
  "s_waitcnt vmcnt(17)\n" \
  "v_mfma_f32_16x16x32_bf16 v[20:23], a[16:19], v[112:115], v[20:23]\n" \
  "v_mfma_f32_16x16x32_bf16 v[24:27], a[20:23], v[112:115], v[24:27]\n" \
  "v_mfma_f32_16x16x32_bf16 v[28:31], a[24:27], v[112:115], v[28:31]\n" \
  "v_mfma_f32_16x16x32_bf16 v[32:35], a[28:31], v[112:115], v[32:35]\n" \
  "s_add_u32 s84, s87, 37888\n" \
  "buffer_load_dwordx4 v[112:115], v6, s[72:75], s84 offen sc1\n" \
  "s_waitcnt vmcnt(17)\n" \
  "v_mfma_f32_16x16x32_bf16 v[20:23], a[32:35], v[116:119], v[20:23]\n" \
  "v_mfma_f32_16x16x32_bf16 v[24:27], a[36:39], v[116:119], v[24:27]\n" \
  "v_mfma_f32_16x16x32_bf16 v[28:31], a[40:43], v[116:119], v[28:31]\n" \
  "v_mfma_f32_16x16x32_bf16 v[32:35], a[44:47], v[116:119], v[32:35]\n" \
  "s_add_u32 s84, s87, 38912\n" \
  "buffer_load_dwordx4 v[116:119], v6, s[72:75], s84 offen sc1\n" \
  "s_waitcnt vmcnt(17)\n" \
  "v_mfma_f32_16x16x32_bf16 v[20:23], a[48:51], v[120:123], v[20:23]\n" \
  "ds_read_b64_tr_b16 v[40:41], v16 offset:12288\n" \
  "ds_read_b64_tr_b16 v[42:43], v16 offset:14336\n" \
  "v_mfma_f32_16x16x32_bf16 v[24:27], a[52:55], v[120:123], v[24:27]\n" \
  "ds_read_b64_tr_b16 v[44:45], v17 offset:12288\n" \
  "ds_read_b64_tr_b16 v[46:47], v17 offset:14336\n" \
  "v_mfma_f32_16x16x32_bf16 v[28:31], a[56:59], v[120:123], v[28:31]\n" \
  "ds_read_b64_tr_b16 v[48:49], v18 offset:12288\n" \
  "ds_read_b64_tr_b16 v[50:51], v18 offset:14336\n" \
  "v_mfma_f32_16x16x32_bf16 v[32:35], a[60:63], v[120:123], v[32:35]\n" \
  "ds_read_b64_tr_b16 v[52:53], v19 offset:12288\n" \
  "ds_read_b64_tr_b16 v[54:55], v19 offset:14336\n" \
  "s_add_u32 s84, s87, 39936\n" \
  "buffer_load_dwordx4 v[120:123], v6, s[72:75], s84 offen sc1\n" \
  "s_waitcnt vmcnt(17)\n" \
  "s_waitcnt lgkmcnt(6)\n" \
  "v_mfma_f32_16x16x32_bf16 v[20:23], v[40:43], v[124:127], v[20:23]\n" \
  "s_waitcnt lgkmcnt(4)\n" \
  "v_mfma_f32_16x16x32_bf16 v[24:27], v[44:47], v[124:127], v[24:27]\n" \
  "s_waitcnt lgkmcnt(2)\n" \
  "v_mfma_f32_16x16x32_bf16 v[28:31], v[48:51], v[124:127], v[28:31]\n" \
  "s_waitcnt lgkmcnt(0)\n" \
  "v_mfma_f32_16x16x32_bf16 v[32:35], v[52:55], v[124:127], v[32:35]\n" \
  "s_add_u32 s84, s87, 56320\n" \
  "buffer_load_dwordx4 v[124:127], v6, s[72:75], s84 offen sc1\n" \
  "s_waitcnt vmcnt(8)\n" \
  "s_add_u32 s92, s89, 2\n" \
  "s_min_u32 s92, s92, 64\n" \
  "v_cmp_gt_u32 vcc, s92, v9\n" \
  "s_sub_u32 s85, s92, 1\n" \
  "s_lshr_b32 s84, s85, 2\n" \
  "s_xnor_b32 s84, s84, %7\n" \
  "s_and_b32 s84, s84, 1\n" \
  "s_lshl_b32 s84, s84, 13\n" \
  "s_lshl_b32 s87, s85, 19\n" \
  "s_or_b32 s87, s87, s84\n" \
  "s_add_u32 s87, s87, s96\n" \
  "s_cbranch_vccnz LP2_%=\n" \
  "LP3_%=:\n" \
  "s_waitcnt vmcnt(17)\n" \
  "v_mfma_f32_16x16x32_bf16 v[20:23], a[64:67], v[64:67], v[20:23]\n" \
  "v_mfma_f32_16x16x32_bf16 v[24:27], a[68:71], v[64:67], v[24:27]\n" \
  "v_mfma_f32_16x16x32_bf16 v[28:31], a[72:75], v[64:67], v[28:31]\n" \
  "v_mfma_f32_16x16x32_bf16 v[32:35], a[76:79], v[64:67], v[32:35]\n" \
  "s_add_u32 s84, s87, 0\n" \
  "buffer_load_dwordx4 v[64:67], v6, s[72:75], s84 offen sc1\n" \
  "s_waitcnt vmcnt(17)\n" \
  "v_mfma_f32_16x16x32_bf16 v[20:23], a[80:83], v[68:71], v[20:23]\n" \
  "v_mfma_f32_16x16x32_bf16 v[24:27], a[84:87], v[68:71], v[24:27]\n" \
  "v_mfma_f32_16x16x32_bf16 v[28:31], a[88:91], v[68:71], v[28:31]\n" \
  "v_mfma_f32_16x16x32_bf16 v[32:35], a[92:95], v[68:71], v[32:35]\n" \
  "s_add_u32 s84, s87, 1024\n" \
  "buffer_load_dwordx4 v[68:71], v6, s[72:75], s84 offen sc1\n" \
  "s_waitcnt vmcnt(17)\n" \
  "v_mfma_f32_16x16x32_bf16 v[20:23], a[96:99], v[72:75], v[20:23]\n" \
  "ds_read_b64_tr_b16 v[40:41], v16 offset:16384\n" \
  "ds_read_b64_tr_b16 v[42:43], v16 offset:18432\n" \
  "v_mfma_f32_16x16x32_bf16 v[24:27], a[100:103], v[72:75], v[24:27]\n" \
  "ds_read_b64_tr_b16 v[44:45], v17 offset:16384\n" \
  "ds_read_b64_tr_b16 v[46:47], v17 offset:18432\n" \
  "v_mfma_f32_16x16x32_bf16 v[28:31], a[104:107], v[72:75], v[28:31]\n" \
  "ds_read_b64_tr_b16 v[48:49], v18 offset:16384\n" \
  "ds_read_b64_tr_b16 v[50:51], v18 offset:18432\n" \
  "v_mfma_f32_16x16x32_bf16 v[32:35], a[108:111], v[72:75], v[32:35]\n" \
  "ds_read_b64_tr_b16 v[52:53], v19 offset:16384\n" \
  "ds_read_b64_tr_b16 v[54:55], v19 offset:18432\n" \
  "s_add_u32 s84, s87, 2048\n" \
  "buffer_load_dwordx4 v[72:75], v6, s[72:75], s84 offen sc1\n" \
  "s_waitcnt vmcnt(17)\n" \
  "s_waitcnt lgkmcnt(6)\n" \
  "v_mfma_f32_16x16x32_bf16 v[20:23], v[40:43], v[76:79], v[20:23]\n" \
  "s_waitcnt lgkmcnt(4)\n" \
  "v_mfma_f32_16x16x32_bf16 v[24:27], v[44:47], v[76:79], v[24:27]\n" \
  "s_waitcnt lgkmcnt(2)\n" \
  "v_mfma_f32_16x16x32_bf16 v[28:31], v[48:51], v[76:79], v[28:31]\n" \
  "s_waitcnt lgkmcnt(0)\n" \
  "v_mfma_f32_16x16x32_bf16 v[32:35], v[52:55], v[76:79], v[32:35]\n" \
  "s_add_u32 s84, s87, 49152\n" \
  "buffer_load_dwordx4 v[76:79], v6, s[72:75], s84 offen sc1\n" \
  "s_waitcnt vmcnt(17)\n" \
  "v_mfma_f32_16x16x32_bf16 v[20:23], a[112:115], v[80:83], v[20:23]\n" \
  "v_mfma_f32_16x16x32_bf16 v[24:27], a[116:119], v[80:83], v[24:27]\n" \
  "v_mfma_f32_16x16x32_bf16 v[28:31], a[120:123], v[80:83], v[28:31]\n" \
  "v_mfma_f32_16x16x32_bf16 v[32:35], a[124:127], v[80:83], v[32:35]\n" \
  "s_add_u32 s84, s87, 3072\n" \
  "buffer_load_dwordx4 v[80:83], v6, s[72:75], s84 offen sc1\n" \
  "s_waitcnt vmcnt(16)\n" \
  "v_mfma_f32_16x16x32_bf16 v[20:23], a[128:131], v[84:87], v[20:23]\n" \
  "v_mfma_f32_16x16x32_bf16 v[24:27], a[132:135], v[84:87], v[24:27]\n" \
  "v_mfma_f32_16x16x32_bf16 v[28:31], a[136:139], v[84:87], v[28:31]\n" \
  "v_mfma_f32_16x16x32_bf16 v[32:35], a[140:143], v[84:87], v[32:35]\n" \
  "s_add_u32 s84, s87, 4096\n" \
  "buffer_load_dwordx4 v[84:87], v6, s[72:75], s84 offen sc1\n" \
  "s_waitcnt vmcnt(16)\n" \
  "v_mfma_f32_16x16x32_bf16 v[20:23], a[144:147], v[88:91], v[20:23]\n" \
  "ds_read_b64_tr_b16 v[40:41], v16 offset:20480\n" \
  "ds_read_b64_tr_b16 v[42:43], v16 offset:22528\n" \
  "v_mfma_f32_16x16x32_bf16 v[24:27], a[148:151], v[88:91], v[24:27]\n" \
  "ds_read_b64_tr_b16 v[44:45], v17 offset:20480\n" \
  "ds_read_b64_tr_b16 v[46:47], v17 offset:22528\n" \
  "v_mfma_f32_16x16x32_bf16 v[28:31], a[152:155], v[88:91], v[28:31]\n" \
  "ds_read_b64_tr_b16 v[48:49], v18 offset:20480\n" \
  "ds_read_b64_tr_b16 v[50:51], v18 offset:22528\n" \
  "v_mfma_f32_16x16x32_bf16 v[32:35], a[156:159], v[88:91], v[32:35]\n" \
  "ds_read_b64_tr_b16 v[52:53], v19 offset:20480\n" \
  "ds_read_b64_tr_b16 v[54:55], v19 offset:22528\n" \
  "s_add_u32 s84, s87, 5120\n" \
  "buffer_load_dwordx4 v[88:91], v6, s[72:75], s84 offen sc1\n" \
  "s_waitcnt vmcnt(16)\n" \
  "s_waitcnt lgkmcnt(6)\n" \
  "v_mfma_f32_16x16x32_bf16 v[20:23], v[40:43], v[92:95], v[20:23]\n" \
  "s_waitcnt lgkmcnt(4)\n" \
  "v_mfma_f32_16x16x32_bf16 v[24:27], v[44:47], v[92:95], v[24:27]\n" \
  "s_waitcnt lgkmcnt(2)\n" \
  "v_mfma_f32_16x16x32_bf16 v[28:31], v[48:51], v[92:95], v[28:31]\n" \
  "s_waitcnt lgkmcnt(0)\n" \
  "v_mfma_f32_16x16x32_bf16 v[32:35], v[52:55], v[92:95], v[32:35]\n" \
  "s_add_u32 s84, s87, 50176\n" \
  "buffer_load_dwordx4 v[92:95], v6, s[72:75], s84 offen sc1\n" \
  "s_waitcnt vmcnt(15)\n" \
  "v_mfma_f32_16x16x32_bf16 v[20:23], a[160:163], v[96:99], v[20:23]\n" \
  "v_mfma_f32_16x16x32_bf16 v[24:27], a[164:167], v[96:99], v[24:27]\n" \
  "v_mfma_f32_16x16x32_bf16 v[28:31], a[168:171], v[96:99], v[28:31]\n" \
  "v_mfma_f32_16x16x32_bf16 v[32:35], a[172:175], v[96:99], v[32:35]\n" \
  "s_add_u32 s84, s87, 6144\n" \
  "buffer_load_dwordx4 v[96:99], v6, s[72:75], s84 offen sc1\n" \
  "s_waitcnt vmcnt(15)\n" \
  "v_mfma_f32_16x16x32_bf16 v[20:23], a[176:179], v[100:103], v[20:23]\n" \
  "v_mfma_f32_16x16x32_bf16 v[24:27], a[180:183], v[100:103], v[24:27]\n" \
  "v_mfma_f32_16x16x32_bf16 v[28:31], a[184:187], v[100:103], v[28:31]\n" \
  "v_mfma_f32_16x16x32_bf16 v[32:35], a[188:191], v[100:103], v[32:35]\n" \
  "s_add_u32 s84, s87, 7168\n" \
  "buffer_load_dwordx4 v[100:103], v6, s[72:75], s84 offen sc1\n" \
  "s_waitcnt vmcnt(15)\n" \
  "v_mfma_f32_16x16x32_bf16 v[20:23], a[192:195], v[104:107], v[20:23]\n" \
  "ds_read_b64_tr_b16 v[40:41], v16 offset:24576\n" \
  "ds_read_b64_tr_b16 v[42:43], v16 offset:26624\n" \
  "v_mfma_f32_16x16x32_bf16 v[24:27], a[196:199], v[104:107], v[24:27]\n" \
  "ds_read_b64_tr_b16 v[44:45], v17 offset:24576\n" \
  "ds_read_b64_tr_b16 v[46:47], v17 offset:26624\n" \
  "v_mfma_f32_16x16x32_bf16 v[28:31], a[200:203], v[104:107], v[28:31]\n" \
  "ds_read_b64_tr_b16 v[48:49], v18 offset:24576\n" \
  "ds_read_b64_tr_b16 v[50:51], v18 offset:26624\n" \
  "v_mfma_f32_16x16x32_bf16 v[32:35], a[204:207], v[104:107], v[32:35]\n" \
  "ds_read_b64_tr_b16 v[52:53], v19 offset:24576\n" \
  "ds_read_b64_tr_b16 v[54:55], v19 offset:26624\n" \
  "s_add_u32 s84, s87, 16384\n" \
  "buffer_load_dwordx4 v[104:107], v6, s[72:75], s84 offen sc1\n" \
  "s_waitcnt vmcnt(15)\n" \
  "s_waitcnt lgkmcnt(6)\n" \
  "v_mfma_f32_16x16x32_bf16 v[20:23], v[40:43], v[108:111], v[20:23]\n" \
  "s_waitcnt lgkmcnt(4)\n" \
  "v_mfma_f32_16x16x32_bf16 v[24:27], v[44:47], v[108:111], v[24:27]\n" \
  "s_waitcnt lgkmcnt(2)\n" \
  "v_mfma_f32_16x16x32_bf16 v[28:31], v[48:51], v[108:111], v[28:31]\n" \
  "s_waitcnt lgkmcnt(0)\n" \
  "v_mfma_f32_16x16x32_bf16 v[32:35], v[52:55], v[108:111], v[32:35]\n" \
  "s_add_u32 s84, s87, 51200\n" \
  "buffer_load_dwordx4 v[108:111], v6, s[72:75], s84 offen sc1\n" \
  "s_waitcnt vmcnt(15)\n" \
  "v_mfma_f32_16x16x32_bf16 v[20:23], a[208:211], v[112:115], v[20:23]\n" \
  "v_mfma_f32_16x16x32_bf16 v[24:27], a[212:215], v[112:115], v[24:27]\n" \
  "v_mfma_f32_16x16x32_bf16 v[28:31], a[216:219], v[112:115], v[28:31]\n" \
  "v_mfma_f32_16x16x32_bf16 v[32:35], a[220:223], v[112:115], v[32:35]\n" \
  "s_add_u32 s84, s87, 17408\n" \
  "buffer_load_dwordx4 v[112:115], v6, s[72:75], s84 offen sc1\n" \
  "s_waitcnt vmcnt(15)\n" \
  "v_mfma_f32_16x16x32_bf16 v[20:23], a[224:227], v[116:119], v[20:23]\n" \
  "v_mfma_f32_16x16x32_bf16 v[24:27], a[228:231], v[116:119], v[24:27]\n" \
  "v_mfma_f32_16x16x32_bf16 v[28:31], a[232:235], v[116:119], v[28:31]\n" \
  "v_mfma_f32_16x16x32_bf16 v[32:35], a[236:239], v[116:119], v[32:35]\n" \
  "s_add_u32 s84, s87, 18432\n" \
  "buffer_load_dwordx4 v[116:119], v6, s[72:75], s84 offen sc1\n" \
  "s_waitcnt vmcnt(15)\n" \
  "v_mfma_f32_16x16x32_bf16 v[20:23], a[240:243], v[120:123], v[20:23]\n" \
  "ds_read_b64_tr_b16 v[40:41], v16 offset:28672\n" \
  "ds_read_b64_tr_b16 v[42:43], v16 offset:30720\n" \
  "v_mfma_f32_16x16x32_bf16 v[24:27], a[244:247], v[120:123], v[24:27]\n" \
  "ds_read_b64_tr_b16 v[44:45], v17 offset:28672\n" \
  "ds_read_b64_tr_b16 v[46:47], v17 offset:30720\n" \
  "v_mfma_f32_16x16x32_bf16 v[28:31], a[248:251], v[120:123], v[28:31]\n" \
  "ds_read_b64_tr_b16 v[48:49], v18 offset:28672\n" \
  "ds_read_b64_tr_b16 v[50:51], v18 offset:30720\n" \
  "v_mfma_f32_16x16x32_bf16 v[32:35], a[252:255], v[120:123], v[32:35]\n" \
  "ds_read_b64_tr_b16 v[52:53], v19 offset:28672\n" \
  "ds_read_b64_tr_b16 v[54:55], v19 offset:30720\n" \
  "s_add_u32 s84, s87, 19456\n" \
  "buffer_load_dwordx4 v[120:123], v6, s[72:75], s84 offen sc1\n" \
  "s_waitcnt vmcnt(15)\n" \
  "s_waitcnt lgkmcnt(6)\n" \
  "v_mfma_f32_16x16x32_bf16 v[20:23], v[40:43], v[124:127], v[20:23]\n" \
  "s_waitcnt lgkmcnt(4)\n" \
  "v_mfma_f32_16x16x32_bf16 v[24:27], v[44:47], v[124:127], v[24:27]\n" \
  "s_waitcnt lgkmcnt(2)\n" \
  "v_mfma_f32_16x16x32_bf16 v[28:31], v[48:51], v[124:127], v[28:31]\n" \
  "s_waitcnt lgkmcnt(0)\n" \
  "v_mfma_f32_16x16x32_bf16 v[32:35], v[52:55], v[124:127], v[32:35]\n" \
  "s_add_u32 s84, s87, 52224\n" \
  "buffer_load_dwordx4 v[124:127], v6, s[72:75], s84 offen sc1\n" \
  "s_add_u32 s89, s89, 1\n" \
  "s_cmp_eq_u32 s89, 64\n" \
  "ds_write_b128 v7, v[20:23]\n" \
  "ds_write_b128 v12, v[24:27]\n" \
  "ds_write_b128 v13, v[28:31]\n" \
  "s_nop 0\n" \
  "ds_write_b128 v14, v[32:35]\n" \
  "s_cbranch_scc0 LX_%=\n" \
  "s_waitcnt lgkmcnt(0)\n" \
  "s_barrier\n" \
  "ds_read_b128 v[52:55], v11 offset:0\n" \
  "ds_read_b128 v[56:59], v11 offset:4096\n" \
  "ds_read_b128 v[60:63], v11 offset:8192\n" \
  "ds_read_b128 v[36:39], v11 offset:12288\n" \
  "s_waitcnt lgkmcnt(0)\n" \
  "v_add_f32 v36, v36, v52\n" \
  "v_add_f32 v37, v37, v53\n" \
  "v_add_f32 v38, v38, v54\n" \
  "v_add_f32 v39, v39, v55\n" \
  "v_add_f32 v56, v56, v60\n" \
  "v_add_f32 v57, v57, v61\n" \
  "v_add_f32 v58, v58, v62\n" \
  "v_add_f32 v59, v59, v63\n" \
  "v_add_f32 v36, v36, v56\n" \
  "v_add_f32 v37, v37, v57\n" \
  "v_add_f32 v38, v38, v58\n" \
  "v_add_f32 v39, v39, v59\n" \
  "v_cvt_pk_bf16_f32 v36, v36, v37\n" \
  "v_cvt_pk_bf16_f32 v37, v38, v39\n" \
  "buffer_store_dwordx2 v[36:37], v10, s[76:79], s88 offen sc1\n" \
  "s_add_u32 s88, s88, 131072\n" \
  "v_and_b32 v10, 0x7fffffff, v10\n" \
  "s_waitcnt vmcnt(0)\n" \
  "s_branch LE_%=\n" \
  "LP2_%=:\n" \
  "buffer_load_dword v9, v8, s[80:83], 0 offen sc1\n" \
  "s_waitcnt vmcnt(0)\n" \
  "v_cmp_gt_u32 vcc, s92, v9\n" \
  "s_cbranch_vccz LP3_%=\n" \
  "s_sleep 2\n" \
  "s_add_u32 s91, s91, 1\n" \
  "s_cmp_lt_u32 s91, " STR(SPIN_CAP) "\n" \
  "s_cbranch_scc1 LP2_%=\n" \
  "v_mov_b32 v5, " STR(CW_DEAD) "*4\n" \
  "v_mov_b32 v15, 1\n" \
  "buffer_store_dword v15, v5, s[80:83], 0 offen sc1\n" \
  "s_branch LP3_%=\n" \
  "LE_%=:\n" \
  ""

#define XA_ASM \
  "s_mov_b64 s[68:69], %0\n" \
  "s_and_b32 s69, s69, 0xffff\n" \
  "s_mov_b32 s70, " STR(EX_BYTES32) "\n" \
  "s_mov_b32 s71, 0x00020000\n" \
  "s_mov_b64 s[72:73], %1\n" \
  "s_and_b32 s73, s73, 0xffff\n" \
  "s_mov_b32 s74, 4*" STR(QKV_SZ32) "\n" \
  "s_mov_b32 s75, 0x00020000\n" \
  "s_mov_b64 s[76:77], %2\n" \
  "s_and_b32 s77, s77, 0xffff\n" \
  "s_mov_b32 s78, " STR(CTRL_BYTES) "\n" \
  "s_mov_b32 s79, 0x00020000\n" \
  "v_mbcnt_lo_u32_b32 v4, -1, 0\n" \
  "v_mbcnt_hi_u32_b32 v4, -1, v4\n" \
  "v_and_b32 v116, 15, v4\n" \
  "v_lshrrev_b32 v117, 4, v4\n" \
  "v_lshlrev_b32 v7, 4, v4\n" \
  "v_add_u32 v8, 4096, v7\n" \
  "v_add_u32 v17, 0x40000, v7\n" \
  "s_and_b32 s80, %3, 1\n" \
  "s_lshl_b32 s80, s80, 11\n" \
  "s_add_u32 s80, s80, 32768\n" \
  "v_add_u32 v18, s80, v7\n" \
  "v_add_u32 v11, %5, v7\n" \
  "v_lshrrev_b32 v5, 2, v116\n" \
  "v_and_b32 v5, 1, v5\n" \
  "v_lshl_add_u32 v5, v117, 2, v5\n" \
  "v_and_b32 v6, 2, v4\n" \
  "v_add_lshl_u32 v5, v5, v6, 4\n" \
  "v_and_b32 v6, 1, v4\n" \
  "v_lshl_add_u32 v5, v6, 3, v5\n" \
  "v_add_u32 v12, %5, v5\n" \
  "s_lshl_b32 s80, %3, 7\n" \
  "s_add_u32 s81, s80, " STR(CW_XPROG) "*4\n" \
  "v_lshl_add_u32 v15, v4, 24, s81\n" \
  "s_add_u32 s81, s80, " STR(CW_PROG) "*4\n" \
  "v_mov_b32 v13, s81\n" \
  "s_lshr_b32 s80, %3, 1\n" \
  "s_lshl_b32 s80, s80, 9\n" \
  "s_add_u32 s80, s80, " STR(CW_GPROG) "*4\n" \
  "v_writelane_b32 v13, s80, 1\n" \
  "s_add_u32 s80, s80, 128\n" \
  "v_writelane_b32 v13, s80, 2\n" \
  "s_add_u32 s80, s80, 128\n" \
  "v_writelane_b32 v13, s80, 3\n" \
  "s_add_u32 s80, s80, 128\n" \
  "v_writelane_b32 v13, s80, 4\n" \
  "v_mov_b32 v16, 0\n" \
  "v_mov_b32 v6, 0xff800000\n" \
  "v_lshlrev_b32 v5, 2, v117\n" \
  "v_cmp_gt_u32 vcc, v5, v116\n" \
  "v_mov_b32 v24, 0x3f803f80\n" \
  "v_add_u32 v5, 1, v5\n" \
  "v_cndmask_b32 v20, 0, v6, vcc\n" \
  "v_cmp_gt_u32 vcc, v5, v116\n" \
  "v_mov_b32 v25, 0x3f803f80\n" \
  "v_add_u32 v5, 1, v5\n" \
  "v_cndmask_b32 v21, 0, v6, vcc\n" \
  "v_cmp_gt_u32 vcc, v5, v116\n" \
  "v_mov_b32 v26, 0x3f803f80\n" \
  "v_add_u32 v5, 1, v5\n" \
  "v_cndmask_b32 v22, 0, v6, vcc\n" \
  "v_cmp_gt_u32 vcc, v5, v116\n" \
  "v_mov_b32 v27, 0x3f803f80\n" \
  "v_add_u32 v5, 1, v5\n" \
  "v_cndmask_b32 v23, 0, v6, vcc\n" \
  "v_mov_b64 v[130:131], 0\n" \
  "v_mov_b32 v156, 0\n" \
  "v_mov_b32 v158, 0\n" \
  "s_mov_b32 s89, 0x3e0293ee\n" \
  "s_mov_b32 s88, 0\n" \
  "s_mov_b32 s82, 0\n" \
  "s_mov_b32 s90, 0\n" \
  "s_lshr_b32 s80, s82, 2\n" \
  "s_xnor_b32 s80, s80, %4\n" \
  "s_and_b32 s80, s80, 1\n" \
  "s_lshl_b32 s80, s80, 13\n" \
  "s_lshl_b32 s83, s82, 19\n" \
  "s_or_b32 s83, s83, s80\n" \
  "s_lshl_b32 s80, %3, 14\n" \
  "s_add_u32 s83, s83, s80\n" \
  "s_add_u32 s84, s83, " STR(QKV_SZ32) "\n" \
  "s_add_u32 s85, s84, " STR(QKV_SZ32) "\n" \
  "s_mov_b32 s81, 1\n" \
  "LP0_%=:\n" \
  "buffer_load_dword v14, v13, s[76:79], 0 offen sc1\n" \
  "s_waitcnt vmcnt(0)\n" \
  "v_readlane_b32 s91, v14, 0\n" \
  "v_readlane_b32 s92, v14, 1\n" \
  "v_readlane_b32 s80, v14, 2\n" \
  "s_min_u32 s92, s92, s80\n" \
  "v_readlane_b32 s80, v14, 3\n" \
  "s_min_u32 s92, s92, s80\n" \
  "v_readlane_b32 s80, v14, 4\n" \
  "s_min_u32 s92, s92, s80\n" \
  "s_cmp_ge_u32 s92, s81\n" \
  "s_cbranch_scc1 LP1_%=\n" \
  "s_sleep 16\n" \
  "s_add_u32 s88, s88, 1\n" \
  "s_cmp_lt_u32 s88, " STR(SPIN_CAP) "\n" \
  "s_cbranch_scc1 LP0_%=\n" \
  "v_mov_b32 v5, " STR(CW_DEAD) "*4\n" \
  "v_mov_b32 v6, 1\n" \
  "buffer_store_dword v6, v5, s[76:79], 0 offen sc1\n" \
  "s_branch LP1_%=\n" \
  "LP1_%=:\n" \
  "buffer_load_dwordx4 a[64:67], v7, s[72:75], s84 offen offset:0 sc1\n" \
  "buffer_load_dwordx4 a[68:71], v7, s[72:75], s84 offen offset:1024 sc1\n" \
  "buffer_load_dwordx4 a[72:75], v7, s[72:75], s84 offen offset:2048 sc1\n" \
  "buffer_load_dwordx4 a[76:79], v7, s[72:75], s84 offen offset:3072 sc1\n" \
  "buffer_load_dwordx4 a[80:83], v8, s[72:75], s84 offen offset:0 sc1\n" \
  "buffer_load_dwordx4 a[84:87], v8, s[72:75], s84 offen offset:1024 sc1\n" \
  "buffer_load_dwordx4 a[88:91], v8, s[72:75], s84 offen offset:2048 sc1\n" \
  "buffer_load_dwordx4 a[92:95], v8, s[72:75], s84 offen offset:3072 sc1\n" \
  "buffer_load_dwordx4 a[0:3], v7, s[72:75], s83 offen offset:0 sc1\n" \
  "buffer_load_dwordx4 a[4:7], v7, s[72:75], s83 offen offset:1024 sc1\n" \
  "buffer_load_dwordx4 a[8:11], v7, s[72:75], s83 offen offset:2048 sc1\n" \
  "buffer_load_dwordx4 a[12:15], v7, s[72:75], s83 offen offset:3072 sc1\n" \
  "buffer_load_dwordx4 a[16:19], v8, s[72:75], s83 offen offset:0 sc1\n" \
  "buffer_load_dwordx4 a[20:23], v8, s[72:75], s83 offen offset:1024 sc1\n" \
  "buffer_load_dwordx4 a[24:27], v8, s[72:75], s83 offen offset:2048 sc1\n" \
  "buffer_load_dwordx4 a[28:31], v8, s[72:75], s83 offen offset:3072 sc1\n" \
  "buffer_load_dwordx4 a[96:99], v7, s[72:75], s85 offen offset:0 sc1\n" \
  "buffer_load_dwordx4 a[100:103], v7, s[72:75], s85 offen offset:1024 sc1\n" \
  "buffer_load_dwordx4 a[104:107], v7, s[72:75], s85 offen offset:2048 sc1\n" \
  "buffer_load_dwordx4 a[108:111], v7, s[72:75], s85 offen offset:3072 sc1\n" \
  "buffer_load_dwordx4 a[112:115], v8, s[72:75], s85 offen offset:0 sc1\n" \
  "buffer_load_dwordx4 a[116:119], v8, s[72:75], s85 offen offset:1024 sc1\n" \
  "buffer_load_dwordx4 a[120:123], v8, s[72:75], s85 offen offset:2048 sc1\n" \
  "buffer_load_dwordx4 a[124:127], v8, s[72:75], s85 offen offset:3072 sc1\n" \
  "s_lshl_b32 s87, s82, 19\n" \
  "s_add_u32 s87, s87, " STR(EXP_B) "<<21\n" \
  "s_xor_b32 s87, s87, %6\n" \
  "s_lshr_b32 s80, %3, 3\n" \
  "s_xor_b32 s80, s80, s82\n" \
  "s_and_b32 s80, s80, 7\n" \
  "s_mul_i32 s80, s80, 0x4800\n" \
  "s_and_b32 s80, s80, 0x7000\n" \
  "s_xor_b32 s87, s87, s80\n" \
  "s_and_b32 s80, %3, 7\n" \
  "s_lshl_b32 s80, s80, 16\n" \
  "s_or_b32 s87, s87, s80\n" \
  "s_waitcnt vmcnt(0)\n" \
  ".p2align 6\n" \
  "LA_%=:\n" \
  "s_waitcnt vmcnt(24)\n" \
  "s_add_u32 s81, s82, 2\n" \
  "s_min_u32 s81, s81, 64\n" \
  "s_sub_u32 s84, s81, 1\n" \
  "v_mfma_f32_16x16x32_bf16 v[116:119], a[64:67], a[0:3], v[20:23]\n" \
  "s_lshr_b32 s80, s84, 2\n" \
  "s_xnor_b32 s80, s80, %4\n" \
  "v_mfma_f32_16x16x32_bf16 v[120:123], a[64:67], a[16:19], 0\n" \
  "s_and_b32 s80, s80, 1\n" \
  "s_lshl_b32 s80, s80, 13\n" \
  "v_mfma_f32_16x16x32_bf16 v[124:127], a[80:83], a[16:19], v[20:23]\n" \
  "s_lshl_b32 s83, s84, 19\n" \
  "s_or_b32 s83, s83, s80\n" \
  "v_mfma_f32_16x16x32_bf16 v[116:119], a[68:71], a[4:7], v[116:119]\n" \
  "s_lshl_b32 s80, %3, 14\n" \
  "s_add_u32 s83, s83, s80\n" \
  "v_mfma_f32_16x16x32_bf16 v[120:123], a[68:71], a[20:23], v[120:123]\n" \
  "s_add_u32 s84, s83, " STR(QKV_SZ32) "\n" \
  "s_add_u32 s85, s84, " STR(QKV_SZ32) "\n" \
  "v_mfma_f32_16x16x32_bf16 v[124:127], a[84:87], a[20:23], v[124:127]\n" \
  "s_lshr_b32 s80, s82, 2\n" \
  "s_xnor_b32 s80, s80, %4\n" \
  "v_mfma_f32_16x16x32_bf16 v[116:119], a[72:75], a[8:11], v[116:119]\n" \
  "s_and_b32 s80, s80, 1\n" \
  "s_lshl_b32 s80, s80, 13\n" \
  "v_mfma_f32_16x16x32_bf16 v[120:123], a[72:75], a[24:27], v[120:123]\n" \
  "s_lshl_b32 s86, s82, 19\n" \
  "s_or_b32 s86, s86, s80\n" \
  "v_mfma_f32_16x16x32_bf16 v[124:127], a[88:91], a[24:27], v[124:127]\n" \
  "s_lshr_b32 s80, %3, 1\n" \
  "s_lshl_b32 s80, s80, 14\n" \
  "v_mfma_f32_16x16x32_bf16 v[116:119], a[76:79], a[12:15], v[116:119]\n" \
  "s_add_u32 s86, s86, s80\n" \
  "s_and_b32 s80, %3, 1\n" \
  "v_mfma_f32_16x16x32_bf16 v[120:123], a[76:79], a[28:31], v[120:123]\n" \
  "s_lshl_b32 s80, s80, 12\n" \
  "s_add_u32 s86, s86, s80\n" \
  "v_mfma_f32_16x16x32_bf16 v[124:127], a[92:95], a[28:31], v[124:127]\n" \
  "s_add_u32 s86, s86, 3*" STR(QKV_SZ32) "\n" \
  "s_cmp_lt_u32 s92, s81\n" \
  "s_cbranch_scc1 LG0_%=\n" \
  "LH0_%=:\n" \
  "buffer_load_dwordx4 a[64:67], v7, s[72:75], s84 offen offset:0 sc1\n" \
  "buffer_load_dwordx4 a[68:71], v7, s[72:75], s84 offen offset:1024 sc1\n" \
  "buffer_load_dwordx4 a[72:75], v7, s[72:75], s84 offen offset:2048 sc1\n" \
  "buffer_load_dwordx4 a[76:79], v7, s[72:75], s84 offen offset:3072 sc1\n" \
  "buffer_load_dwordx4 a[80:83], v8, s[72:75], s84 offen offset:0 sc1\n" \
  "buffer_load_dwordx4 a[84:87], v8, s[72:75], s84 offen offset:1024 sc1\n" \
  "v_max3_f32 v144, v116, v117, v118\n" \
  "v_max3_f32 v145, v120, v121, v122\n" \
  "v_max_f32 v144, v144, v119\n" \
  "v_max3_f32 v145, v145, v123, v124\n" \
  "v_max3_f32 v145, v145, v125, v126\n" \
  "v_max_f32 v145, v145, v127\n" \
  "v_mov_b32 v5, v144\n" \
  "v_mov_b32 v6, v145\n" \
  "s_nop 0\n" \
  "v_permlane16_swap_b32 v144, v5\n" \
  "v_permlane16_swap_b32 v145, v6\n" \
  "v_max_f32 v144, v144, v5\n" \
  "v_max_f32 v145, v145, v6\n" \
  "v_mov_b32 v5, v144\n" \
  "v_mov_b32 v6, v145\n" \
  "s_nop 0\n" \
  "v_permlane32_swap_b32 v144, v5\n" \
  "v_permlane32_swap_b32 v145, v6\n" \
  "v_max_f32 v144, v144, v5\n" \
  "v_max_f32 v145, v145, v6\n" \
  "v_mul_f32 v144, s89, v144\n" \
  "v_mul_f32 v145, s89, v145\n" \
  "v_fma_f32 v116, v116, s89, -v144\n" \
  "v_exp_f32 v116, v116\n" \
  "buffer_load_dwordx4 a[88:91], v8, s[72:75], s84 offen offset:2048 sc1\n" \
  "v_fma_f32 v117, v117, s89, -v144\n" \
  "v_exp_f32 v117, v117\n" \
  "buffer_load_dwordx4 a[92:95], v8, s[72:75], s84 offen offset:3072 sc1\n" \
  "v_fma_f32 v118, v118, s89, -v144\n" \
  "v_exp_f32 v118, v118\n" \
  "buffer_load_dwordx4 a[32:35], v7, s[72:75], s83 offen offset:0 sc1\n" \
  "v_fma_f32 v119, v119, s89, -v144\n" \
  "v_exp_f32 v119, v119\n" \
  "buffer_load_dwordx4 a[36:39], v7, s[72:75], s83 offen offset:1024 sc1\n" \
  "v_fma_f32 v120, v120, s89, -v145\n" \
  "v_exp_f32 v120, v120\n" \
  "buffer_load_dwordx4 a[40:43], v7, s[72:75], s83 offen offset:2048 sc1\n" \
  "v_fma_f32 v121, v121, s89, -v145\n" \
  "v_exp_f32 v121, v121\n" \
  "buffer_load_dwordx4 a[44:47], v7, s[72:75], s83 offen offset:3072 sc1\n" \
  "v_fma_f32 v122, v122, s89, -v145\n" \
  "v_exp_f32 v122, v122\n" \
  "buffer_load_dwordx4 a[48:51], v8, s[72:75], s83 offen offset:0 sc1\n" \
  "v_fma_f32 v123, v123, s89, -v145\n" \
  "v_exp_f32 v123, v123\n" \
  "buffer_load_dwordx4 a[52:55], v8, s[72:75], s83 offen offset:1024 sc1\n" \
  "v_fma_f32 v124, v124, s89, -v145\n" \
  "v_exp_f32 v124, v124\n" \
  "buffer_load_dwordx4 a[56:59], v8, s[72:75], s83 offen offset:2048 sc1\n" \
  "v_fma_f32 v125, v125, s89, -v145\n" \
  "v_exp_f32 v125, v125\n" \
  "buffer_load_dwordx4 a[60:63], v8, s[72:75], s83 offen offset:3072 sc1\n" \
  "v_fma_f32 v126, v126, s89, -v145\n" \
  "v_exp_f32 v126, v126\n" \
  "v_fma_f32 v127, v127, s89, -v145\n" \
  "v_exp_f32 v127, v127\n" \
  "s_nop 0\n" \
  "v_cvt_pk_bf16_f32 v128, v116, v117\n" \
  "v_cvt_pk_bf16_f32 v129, v118, v119\n" \
  "v_cvt_pk_bf16_f32 v132, v120, v121\n" \
  "v_cvt_pk_bf16_f32 v133, v122, v123\n" \
  "v_cvt_pk_bf16_f32 v134, v124, v125\n" \
  "v_cvt_pk_bf16_f32 v135, v126, v127\n" \
  "s_waitcnt vmcnt(22)\n" \
  "buffer_store_dword v16, v15, s[76:79], 0 offen sc1\n" \
  "v_mfma_f32_16x16x32_bf16 v[136:139], v[24:27], v[128:131], 0\n" \
  "v_mfma_f32_16x16x32_bf16 v[140:143], v[24:27], v[132:135], 0\n" \
  "v_add_u32 v16, 1, v16\n" \
  "v_mfma_f32_16x16x32_bf16 v[52:55], a[96:99], v[128:131], 0\n" \
  "v_mfma_f32_16x16x32_bf16 v[84:87], a[96:99], v[132:135], 0\n" \
  "buffer_load_dwordx4 a[96:99], v7, s[72:75], s85 offen offset:0 sc1\n" \
  "v_mfma_f32_16x16x32_bf16 v[56:59], a[100:103], v[128:131], 0\n" \
  "v_mfma_f32_16x16x32_bf16 v[88:91], a[100:103], v[132:135], 0\n" \
  "buffer_load_dwordx4 a[100:103], v7, s[72:75], s85 offen offset:1024 sc1\n" \
  "v_mfma_f32_16x16x32_bf16 v[60:63], a[104:107], v[128:131], 0\n" \
  "v_mfma_f32_16x16x32_bf16 v[92:95], a[104:107], v[132:135], 0\n" \
  "buffer_load_dwordx4 a[104:107], v7, s[72:75], s85 offen offset:2048 sc1\n" \
  "v_mfma_f32_16x16x32_bf16 v[64:67], a[108:111], v[128:131], 0\n" \
  "v_mfma_f32_16x16x32_bf16 v[96:99], a[108:111], v[132:135], 0\n" \
  "buffer_load_dwordx4 a[108:111], v7, s[72:75], s85 offen offset:3072 sc1\n" \
  "v_mfma_f32_16x16x32_bf16 v[68:71], a[112:115], v[128:131], 0\n" \
  "v_mfma_f32_16x16x32_bf16 v[100:103], a[112:115], v[132:135], 0\n" \
  "buffer_load_dwordx4 a[112:115], v8, s[72:75], s85 offen offset:0 sc1\n" \
  "v_mfma_f32_16x16x32_bf16 v[72:75], a[116:119], v[128:131], 0\n" \
  "v_mfma_f32_16x16x32_bf16 v[104:107], a[116:119], v[132:135], 0\n" \
  "buffer_load_dwordx4 a[116:119], v8, s[72:75], s85 offen offset:1024 sc1\n" \
  "v_mfma_f32_16x16x32_bf16 v[76:79], a[120:123], v[128:131], 0\n" \
  "v_mfma_f32_16x16x32_bf16 v[108:111], a[120:123], v[132:135], 0\n" \
  "buffer_load_dwordx4 a[120:123], v8, s[72:75], s85 offen offset:2048 sc1\n" \
  "v_mfma_f32_16x16x32_bf16 v[80:83], a[124:127], v[128:131], 0\n" \
  "v_mfma_f32_16x16x32_bf16 v[112:115], a[124:127], v[132:135], 0\n" \
  "buffer_load_dwordx4 a[124:127], v8, s[72:75], s85 offen offset:3072 sc1\n" \
  "buffer_load_dword v14, v13, s[76:79], 0 offen sc1\n" \
  "s_cmp_eq_u32 s90, 0\n" \
  "s_cbranch_scc1 LW0_%=\n" \
  "s_waitcnt vmcnt(26)\n" \
  "LM0_%=:\n" \
  "ds_write_b128 v11, v[44:47]\n" \
  "ds_write_b128 v11, v[48:51] offset:1024\n" \
  "v_mfma_f32_16x16x32_bf16 v[148:151], v[28:31], a[0:3], 0\n" \
  "v_max3_u32 v5, v28, v29, v30\n" \
  "v_max3_u32 v5, v5, v31, v32\n" \
  "v_mfma_f32_16x16x32_bf16 v[152:155], v[28:31], a[16:19], 0\n" \
  "v_max3_u32 v5, v5, v33, v34\n" \
  "v_max3_u32 v5, v5, v35, v36\n" \
  "v_mfma_f32_16x16x32_bf16 v[148:151], v[32:35], a[4:7], v[148:151]\n" \
  "v_max3_u32 v5, v5, v37, v38\n" \
  "v_max3_u32 v5, v5, v39, v40\n" \
  "v_mfma_f32_16x16x32_bf16 v[152:155], v[32:35], a[20:23], v[152:155]\n" \
  "v_max3_u32 v5, v5, v41, v42\n" \
  "v_max3_u32 v5, v5, v43, v44\n" \
  "v_mfma_f32_16x16x32_bf16 v[148:151], v[36:39], a[8:11], v[148:151]\n" \
  "v_max3_u32 v5, v5, v45, v46\n" \
  "v_max3_u32 v5, v5, v47, v48\n" \
  "v_mfma_f32_16x16x32_bf16 v[152:155], v[36:39], a[24:27], v[152:155]\n" \
  "v_max3_u32 v5, v5, v49, v50\n" \
  "v_max_u32 v5, v5, v51\n" \
  "v_mfma_f32_16x16x32_bf16 v[148:151], v[40:43], a[12:15], v[148:151]\n" \
  "v_cmp_eq_u32 vcc, -1, v5\n" \
  "v_mfma_f32_16x16x32_bf16 v[152:155], v[40:43], a[28:31], v[152:155]\n" \
  "ds_read_b64_tr_b16 a[128:129], v12 offset:0\n" \
  "ds_read_b64_tr_b16 a[130:131], v12 offset:256\n" \
  "ds_read_b64_tr_b16 a[132:133], v12 offset:512\n" \
  "ds_read_b64_tr_b16 a[134:135], v12 offset:768\n" \
  "ds_read_b64_tr_b16 a[136:137], v12 offset:1024\n" \
  "ds_read_b64_tr_b16 a[138:139], v12 offset:1280\n" \
  "ds_read_b64_tr_b16 a[140:141], v12 offset:1536\n" \
  "ds_read_b64_tr_b16 a[142:143], v12 offset:1792\n" \
  "s_cbranch_vccnz LR0_%=\n" \
  "v_max_f32 v146, v150, v151\n" \
  "v_max_f32 v147, v154, v155\n" \
  "v_mov_b32 v5, v146\n" \
  "v_mov_b32 v6, v147\n" \
  "s_nop 0\n" \
  "v_permlane16_swap_b32 v146, v5\n" \
  "v_permlane16_swap_b32 v147, v6\n" \
  "v_max_f32 v146, v146, v5\n" \
  "v_max_f32 v147, v147, v6\n" \
  "v_mov_b32 v5, v146\n" \
  "v_mov_b32 v6, v147\n" \
  "s_nop 0\n" \
  "v_permlane32_swap_b32 v146, v5\n" \
  "v_permlane32_swap_b32 v147, v6\n" \
  "v_max_f32 v146, v146, v5\n" \
  "v_max_f32 v147, v147, v6\n" \
  "v_mul_f32 v146, s89, v146\n" \
  "v_mul_f32 v147, s89, v147\n" \
  "v_max_f32 v146, v146, v144\n" \
  "v_max_f32 v147, v147, v145\n" \
  "v_sub_f32 v160, v144, v146\n" \
  "v_sub_f32 v162, v145, v147\n" \
  "v_fma_f32 v150, v150, s89, -v146\n" \
  "v_fma_f32 v151, v151, s89, -v146\n" \
  "v_fma_f32 v154, v154, s89, -v147\n" \
  "v_fma_f32 v155, v155, s89, -v147\n" \
  "v_exp_f32 v150, v150\n" \
  "v_exp_f32 v151, v151\n" \
  "v_exp_f32 v154, v154\n" \
  "v_exp_f32 v155, v155\n" \
  "v_exp_f32 v160, v160\n" \
  "v_exp_f32 v162, v162\n" \
  "v_add_f32 v146, v150, v151\n" \
  "v_add_f32 v147, v154, v155\n" \
  "v_mov_b32 v5, v146\n" \
  "v_mov_b32 v6, v147\n" \
  "s_nop 0\n" \
  "v_permlane16_swap_b32 v146, v5\n" \
  "v_permlane16_swap_b32 v147, v6\n" \
  "v_add_f32 v146, v146, v5\n" \
  "v_add_f32 v147, v147, v6\n" \
  "v_mov_b32 v5, v146\n" \
  "v_mov_b32 v6, v147\n" \
  "s_nop 0\n" \
  "v_permlane32_swap_b32 v146, v5\n" \
  "v_permlane32_swap_b32 v147, v6\n" \
  "v_add_f32 v146, v146, v5\n" \
  "v_add_f32 v147, v147, v6\n" \
  "v_fma_f32 v146, v160, v136, v146\n" \
  "v_fma_f32 v147, v162, v140, v147\n" \
  "v_rcp_f32 v146, v146\n" \
  "v_rcp_f32 v147, v147\n" \
  "v_mul_f32 v160, v160, v146\n" \
  "v_mul_f32 v162, v162, v147\n" \
  "v_mul_f32 v150, v150, v146\n" \
  "v_mul_f32 v151, v151, v146\n" \
  "v_mul_f32 v154, v154, v147\n" \
  "v_mul_f32 v155, v155, v147\n" \
  "v_cvt_pk_bf16_f32 v157, v150, v151\n" \
  "v_cvt_pk_bf16_f32 v159, v154, v155\n" \
  "v_pk_mul_f32 v[52:53], v[52:53], v[160:161] op_sel_hi:[1,0]\n" \
  "v_pk_mul_f32 v[54:55], v[54:55], v[160:161] op_sel_hi:[1,0]\n" \
  "v_pk_mul_f32 v[56:57], v[56:57], v[160:161] op_sel_hi:[1,0]\n" \
  "v_pk_mul_f32 v[58:59], v[58:59], v[160:161] op_sel_hi:[1,0]\n" \
  "v_pk_mul_f32 v[60:61], v[60:61], v[160:161] op_sel_hi:[1,0]\n" \
  "v_pk_mul_f32 v[62:63], v[62:63], v[160:161] op_sel_hi:[1,0]\n" \
  "v_pk_mul_f32 v[64:65], v[64:65], v[160:161] op_sel_hi:[1,0]\n" \
  "v_pk_mul_f32 v[66:67], v[66:67], v[160:161] op_sel_hi:[1,0]\n" \
  "v_pk_mul_f32 v[68:69], v[68:69], v[160:161] op_sel_hi:[1,0]\n" \
  "v_pk_mul_f32 v[70:71], v[70:71], v[160:161] op_sel_hi:[1,0]\n" \
  "v_pk_mul_f32 v[72:73], v[72:73], v[160:161] op_sel_hi:[1,0]\n" \
  "v_pk_mul_f32 v[74:75], v[74:75], v[160:161] op_sel_hi:[1,0]\n" \
  "v_pk_mul_f32 v[76:77], v[76:77], v[160:161] op_sel_hi:[1,0]\n" \
  "v_pk_mul_f32 v[78:79], v[78:79], v[160:161] op_sel_hi:[1,0]\n" \
  "v_pk_mul_f32 v[80:81], v[80:81], v[160:161] op_sel_hi:[1,0]\n" \
  "v_pk_mul_f32 v[82:83], v[82:83], v[160:161] op_sel_hi:[1,0]\n" \
  "v_pk_mul_f32 v[84:85], v[84:85], v[162:163] op_sel_hi:[1,0]\n" \
  "v_pk_mul_f32 v[86:87], v[86:87], v[162:163] op_sel_hi:[1,0]\n" \
  "v_pk_mul_f32 v[88:89], v[88:89], v[162:163] op_sel_hi:[1,0]\n" \
  "v_pk_mul_f32 v[90:91], v[90:91], v[162:163] op_sel_hi:[1,0]\n" \
  "v_pk_mul_f32 v[92:93], v[92:93], v[162:163] op_sel_hi:[1,0]\n" \
  "v_pk_mul_f32 v[94:95], v[94:95], v[162:163] op_sel_hi:[1,0]\n" \
  "v_pk_mul_f32 v[96:97], v[96:97], v[162:163] op_sel_hi:[1,0]\n" \
  "v_pk_mul_f32 v[98:99], v[98:99], v[162:163] op_sel_hi:[1,0]\n" \
  "v_pk_mul_f32 v[100:101], v[100:101], v[162:163] op_sel_hi:[1,0]\n" \
  "v_pk_mul_f32 v[102:103], v[102:103], v[162:163] op_sel_hi:[1,0]\n" \
  "v_pk_mul_f32 v[104:105], v[104:105], v[162:163] op_sel_hi:[1,0]\n" \
  "v_pk_mul_f32 v[106:107], v[106:107], v[162:163] op_sel_hi:[1,0]\n" \
  "v_pk_mul_f32 v[108:109], v[108:109], v[162:163] op_sel_hi:[1,0]\n" \
  "v_pk_mul_f32 v[110:111], v[110:111], v[162:163] op_sel_hi:[1,0]\n" \
  "v_pk_mul_f32 v[112:113], v[112:113], v[162:163] op_sel_hi:[1,0]\n" \
  "v_pk_mul_f32 v[114:115], v[114:115], v[162:163] op_sel_hi:[1,0]\n" \
  "s_waitcnt lgkmcnt(0)\n" \
  "v_mfma_f32_16x16x16_bf16 v[52:55], a[128:129], v[156:157], v[52:55]\n" \
  "v_mfma_f32_16x16x16_bf16 v[56:59], a[130:131], v[156:157], v[56:59]\n" \
  "v_mfma_f32_16x16x16_bf16 v[60:63], a[132:133], v[156:157], v[60:63]\n" \
  "v_mfma_f32_16x16x16_bf16 v[64:67], a[134:135], v[156:157], v[64:67]\n" \
  "v_mfma_f32_16x16x16_bf16 v[68:71], a[136:137], v[156:157], v[68:71]\n" \
  "v_cvt_pk_bf16_f32 v164, v52, v53\n" \
  "v_cvt_pk_bf16_f32 v165, v54, v55\n" \
  "v_mfma_f32_16x16x16_bf16 v[72:75], a[138:139], v[156:157], v[72:75]\n" \
  "v_cvt_pk_bf16_f32 v166, v56, v57\n" \
  "v_cvt_pk_bf16_f32 v167, v58, v59\n" \
  "buffer_store_dwordx4 v[164:167], v7, s[72:75], s86 offen offset:0 sc1\n" \
  "v_mfma_f32_16x16x16_bf16 v[76:79], a[140:141], v[156:157], v[76:79]\n" \
  "v_cvt_pk_bf16_f32 v168, v60, v61\n" \
  "v_cvt_pk_bf16_f32 v169, v62, v63\n" \
  "v_mfma_f32_16x16x16_bf16 v[80:83], a[142:143], v[156:157], v[80:83]\n" \
  "v_cvt_pk_bf16_f32 v170, v64, v65\n" \
  "v_cvt_pk_bf16_f32 v171, v66, v67\n" \
  "buffer_store_dwordx4 v[168:171], v7, s[72:75], s86 offen offset:1024 sc1\n" \
  "v_mfma_f32_16x16x16_bf16 v[84:87], a[128:129], v[158:159], v[84:87]\n" \
  "v_cvt_pk_bf16_f32 v172, v68, v69\n" \
  "v_cvt_pk_bf16_f32 v173, v70, v71\n" \
  "v_mfma_f32_16x16x16_bf16 v[88:91], a[130:131], v[158:159], v[88:91]\n" \
  "v_cvt_pk_bf16_f32 v174, v72, v73\n" \
  "v_cvt_pk_bf16_f32 v175, v74, v75\n" \
  "buffer_store_dwordx4 v[172:175], v7, s[72:75], s86 offen offset:2048 sc1\n" \
  "v_mfma_f32_16x16x16_bf16 v[92:95], a[132:133], v[158:159], v[92:95]\n" \
  "v_cvt_pk_bf16_f32 v176, v76, v77\n" \
  "v_cvt_pk_bf16_f32 v177, v78, v79\n" \
  "v_mfma_f32_16x16x16_bf16 v[96:99], a[134:135], v[158:159], v[96:99]\n" \
  "v_cvt_pk_bf16_f32 v178, v80, v81\n" \
  "v_cvt_pk_bf16_f32 v179, v82, v83\n" \
  "buffer_store_dwordx4 v[176:179], v7, s[72:75], s86 offen offset:3072 sc1\n" \
  "v_mfma_f32_16x16x16_bf16 v[100:103], a[136:137], v[158:159], v[100:103]\n" \
  "v_cvt_pk_bf16_f32 v180, v84, v85\n" \
  "v_cvt_pk_bf16_f32 v181, v86, v87\n" \
  "v_mfma_f32_16x16x16_bf16 v[104:107], a[138:139], v[158:159], v[104:107]\n" \
  "v_cvt_pk_bf16_f32 v182, v88, v89\n" \
  "v_cvt_pk_bf16_f32 v183, v90, v91\n" \
  "buffer_store_dwordx4 v[180:183], v17, s[72:75], s86 offen offset:0 sc1\n" \
  "v_mfma_f32_16x16x16_bf16 v[108:111], a[140:141], v[158:159], v[108:111]\n" \
  "v_cvt_pk_bf16_f32 v184, v92, v93\n" \
  "v_cvt_pk_bf16_f32 v185, v94, v95\n" \
  "v_mfma_f32_16x16x16_bf16 v[112:115], a[142:143], v[158:159], v[112:115]\n" \
  "v_cvt_pk_bf16_f32 v186, v96, v97\n" \
  "v_cvt_pk_bf16_f32 v187, v98, v99\n" \
  "buffer_store_dwordx4 v[184:187], v17, s[72:75], s86 offen offset:1024 sc1\n" \
  "v_cvt_pk_bf16_f32 v188, v100, v101\n" \
  "v_cvt_pk_bf16_f32 v189, v102, v103\n" \
  "v_cvt_pk_bf16_f32 v190, v104, v105\n" \
  "v_cvt_pk_bf16_f32 v191, v106, v107\n" \
  "buffer_store_dwordx4 v[188:191], v17, s[72:75], s86 offen offset:2048 sc1\n" \
  "s_nop 1\n" \
  "v_cvt_pk_bf16_f32 v192, v108, v109\n" \
  "v_cvt_pk_bf16_f32 v193, v110, v111\n" \
  "v_cvt_pk_bf16_f32 v194, v112, v113\n" \
  "v_cvt_pk_bf16_f32 v195, v114, v115\n" \
  "buffer_store_dwordx4 v[192:195], v17, s[72:75], s86 offen offset:3072 sc1\n" \
  "s_add_u32 s82, s82, 1\n" \
  "s_cmp_eq_u32 s82, 64\n" \
  "s_cbranch_scc1 LZ_%=\n" \
  "s_lshl_b32 s87, s82, 19\n" \
  "s_add_u32 s87, s87, " STR(EXP_B) "<<21\n" \
  "s_xor_b32 s87, s87, %6\n" \
  "s_lshr_b32 s80, %3, 3\n" \
  "s_xor_b32 s80, s80, s82\n" \
  "s_and_b32 s80, s80, 7\n" \
  "s_mul_i32 s80, s80, 0x4800\n" \
  "s_and_b32 s80, s80, 0x7000\n" \
  "s_xor_b32 s87, s87, s80\n" \
  "s_and_b32 s80, %3, 7\n" \
  "s_lshl_b32 s80, s80, 16\n" \
  "s_or_b32 s87, s87, s80\n" \
  "s_waitcnt vmcnt(8)\n" \
  "v_readlane_b32 s91, v14, 0\n" \
  "v_readlane_b32 s92, v14, 1\n" \
  "v_readlane_b32 s80, v14, 2\n" \
  "s_min_u32 s92, s92, s80\n" \
  "v_readlane_b32 s80, v14, 3\n" \
  "s_min_u32 s92, s92, s80\n" \
  "v_readlane_b32 s80, v14, 4\n" \
  "s_min_u32 s92, s92, s80\n" \
  "s_cmp_eq_u32 s82, 63\n" \
  "s_addc_u32 s91, s91, 0\n" \
  "s_cmp_gt_u32 s91, s82\n" \
  "s_cselect_b32 s90, 1, 0\n" \
  "s_cselect_b32 s80, 0, 0x80000000\n" \
  "v_or_b32 v9, s80, v7\n" \
  "v_or_b32 v10, s80, v18\n" \
  "buffer_load_dwordx4 v[28:31], v9, s[68:71], s87 offen offset:0 sc1\n" \
  "buffer_load_dwordx4 v[32:35], v9, s[68:71], s87 offen offset:1024 sc1\n" \
  "buffer_load_dwordx4 v[36:39], v9, s[68:71], s87 offen offset:2048 sc1\n" \
  "buffer_load_dwordx4 v[40:43], v9, s[68:71], s87 offen offset:3072 sc1\n" \
  "buffer_load_dwordx4 v[44:47], v10, s[68:71], s87 offen offset:0 sc1\n" \
  "buffer_load_dwordx4 v[48:51], v10, s[68:71], s87 offen offset:1024 sc1\n" \
  "s_waitcnt vmcnt(24)\n" \
  "s_add_u32 s81, s82, 2\n" \
  "s_min_u32 s81, s81, 64\n" \
  "s_sub_u32 s84, s81, 1\n" \
  "v_mfma_f32_16x16x32_bf16 v[116:119], a[64:67], a[32:35], v[20:23]\n" \
  "s_lshr_b32 s80, s84, 2\n" \
  "s_xnor_b32 s80, s80, %4\n" \
  "v_mfma_f32_16x16x32_bf16 v[120:123], a[64:67], a[48:51], 0\n" \
  "s_and_b32 s80, s80, 1\n" \
  "s_lshl_b32 s80, s80, 13\n" \
  "v_mfma_f32_16x16x32_bf16 v[124:127], a[80:83], a[48:51], v[20:23]\n" \
  "s_lshl_b32 s83, s84, 19\n" \
  "s_or_b32 s83, s83, s80\n" \
  "v_mfma_f32_16x16x32_bf16 v[116:119], a[68:71], a[36:39], v[116:119]\n" \
  "s_lshl_b32 s80, %3, 14\n" \
  "s_add_u32 s83, s83, s80\n" \
  "v_mfma_f32_16x16x32_bf16 v[120:123], a[68:71], a[52:55], v[120:123]\n" \
  "s_add_u32 s84, s83, " STR(QKV_SZ32) "\n" \
  "s_add_u32 s85, s84, " STR(QKV_SZ32) "\n" \
  "v_mfma_f32_16x16x32_bf16 v[124:127], a[84:87], a[52:55], v[124:127]\n" \
  "s_lshr_b32 s80, s82, 2\n" \
  "s_xnor_b32 s80, s80, %4\n" \
  "v_mfma_f32_16x16x32_bf16 v[116:119], a[72:75], a[40:43], v[116:119]\n" \
  "s_and_b32 s80, s80, 1\n" \
  "s_lshl_b32 s80, s80, 13\n" \
  "v_mfma_f32_16x16x32_bf16 v[120:123], a[72:75], a[56:59], v[120:123]\n" \
  "s_lshl_b32 s86, s82, 19\n" \
  "s_or_b32 s86, s86, s80\n" \
  "v_mfma_f32_16x16x32_bf16 v[124:127], a[88:91], a[56:59], v[124:127]\n" \
  "s_lshr_b32 s80, %3, 1\n" \
  "s_lshl_b32 s80, s80, 14\n" \
  "v_mfma_f32_16x16x32_bf16 v[116:119], a[76:79], a[44:47], v[116:119]\n" \
  "s_add_u32 s86, s86, s80\n" \
  "s_and_b32 s80, %3, 1\n" \
  "v_mfma_f32_16x16x32_bf16 v[120:123], a[76:79], a[60:63], v[120:123]\n" \
  "s_lshl_b32 s80, s80, 12\n" \
  "s_add_u32 s86, s86, s80\n" \
  "v_mfma_f32_16x16x32_bf16 v[124:127], a[92:95], a[60:63], v[124:127]\n" \
  "s_add_u32 s86, s86, 3*" STR(QKV_SZ32) "\n" \
  "s_cmp_lt_u32 s92, s81\n" \
  "s_cbranch_scc1 LG1_%=\n" \
  "LH1_%=:\n" \
  "buffer_load_dwordx4 a[64:67], v7, s[72:75], s84 offen offset:0 sc1\n" \
  "buffer_load_dwordx4 a[68:71], v7, s[72:75], s84 offen offset:1024 sc1\n" \
  "buffer_load_dwordx4 a[72:75], v7, s[72:75], s84 offen offset:2048 sc1\n" \
  "buffer_load_dwordx4 a[76:79], v7, s[72:75], s84 offen offset:3072 sc1\n" \
  "buffer_load_dwordx4 a[80:83], v8, s[72:75], s84 offen offset:0 sc1\n" \
  "buffer_load_dwordx4 a[84:87], v8, s[72:75], s84 offen offset:1024 sc1\n" \
  "v_max3_f32 v144, v116, v117, v118\n" \
  "v_max3_f32 v145, v120, v121, v122\n" \
  "v_max_f32 v144, v144, v119\n" \
  "v_max3_f32 v145, v145, v123, v124\n" \
  "v_max3_f32 v145, v145, v125, v126\n" \
  "v_max_f32 v145, v145, v127\n" \
  "v_mov_b32 v5, v144\n" \
  "v_mov_b32 v6, v145\n" \
  "s_nop 0\n" \
  "v_permlane16_swap_b32 v144, v5\n" \
  "v_permlane16_swap_b32 v145, v6\n" \
  "v_max_f32 v144, v144, v5\n" \
  "v_max_f32 v145, v145, v6\n" \
  "v_mov_b32 v5, v144\n" \
  "v_mov_b32 v6, v145\n" \
  "s_nop 0\n" \
  "v_permlane32_swap_b32 v144, v5\n" \
  "v_permlane32_swap_b32 v145, v6\n" \
  "v_max_f32 v144, v144, v5\n" \
  "v_max_f32 v145, v145, v6\n" \
  "v_mul_f32 v144, s89, v144\n" \
  "v_mul_f32 v145, s89, v145\n" \
  "v_fma_f32 v116, v116, s89, -v144\n" \
  "v_exp_f32 v116, v116\n" \
  "buffer_load_dwordx4 a[88:91], v8, s[72:75], s84 offen offset:2048 sc1\n" \
  "v_fma_f32 v117, v117, s89, -v144\n" \
  "v_exp_f32 v117, v117\n" \
  "buffer_load_dwordx4 a[92:95], v8, s[72:75], s84 offen offset:3072 sc1\n" \
  "v_fma_f32 v118, v118, s89, -v144\n" \
  "v_exp_f32 v118, v118\n" \
  "buffer_load_dwordx4 a[0:3], v7, s[72:75], s83 offen offset:0 sc1\n" \
  "v_fma_f32 v119, v119, s89, -v144\n" \
  "v_exp_f32 v119, v119\n" \
  "buffer_load_dwordx4 a[4:7], v7, s[72:75], s83 offen offset:1024 sc1\n" \
  "v_fma_f32 v120, v120, s89, -v145\n" \
  "v_exp_f32 v120, v120\n" \
  "buffer_load_dwordx4 a[8:11], v7, s[72:75], s83 offen offset:2048 sc1\n" \
  "v_fma_f32 v121, v121, s89, -v145\n" \
  "v_exp_f32 v121, v121\n" \
  "buffer_load_dwordx4 a[12:15], v7, s[72:75], s83 offen offset:3072 sc1\n" \
  "v_fma_f32 v122, v122, s89, -v145\n" \
  "v_exp_f32 v122, v122\n" \
  "buffer_load_dwordx4 a[16:19], v8, s[72:75], s83 offen offset:0 sc1\n" \
  "v_fma_f32 v123, v123, s89, -v145\n" \
  "v_exp_f32 v123, v123\n" \
  "buffer_load_dwordx4 a[20:23], v8, s[72:75], s83 offen offset:1024 sc1\n" \
  "v_fma_f32 v124, v124, s89, -v145\n" \
  "v_exp_f32 v124, v124\n" \
  "buffer_load_dwordx4 a[24:27], v8, s[72:75], s83 offen offset:2048 sc1\n" \
  "v_fma_f32 v125, v125, s89, -v145\n" \
  "v_exp_f32 v125, v125\n" \
  "buffer_load_dwordx4 a[28:31], v8, s[72:75], s83 offen offset:3072 sc1\n" \
  "v_fma_f32 v126, v126, s89, -v145\n" \
  "v_exp_f32 v126, v126\n" \
  "v_fma_f32 v127, v127, s89, -v145\n" \
  "v_exp_f32 v127, v127\n" \
  "s_nop 0\n" \
  "v_cvt_pk_bf16_f32 v128, v116, v117\n" \
  "v_cvt_pk_bf16_f32 v129, v118, v119\n" \
  "v_cvt_pk_bf16_f32 v132, v120, v121\n" \
  "v_cvt_pk_bf16_f32 v133, v122, v123\n" \
  "v_cvt_pk_bf16_f32 v134, v124, v125\n" \
  "v_cvt_pk_bf16_f32 v135, v126, v127\n" \
  "s_waitcnt vmcnt(22)\n" \
  "buffer_store_dword v16, v15, s[76:79], 0 offen sc1\n" \
  "v_mfma_f32_16x16x32_bf16 v[136:139], v[24:27], v[128:131], 0\n" \
  "v_mfma_f32_16x16x32_bf16 v[140:143], v[24:27], v[132:135], 0\n" \
  "v_add_u32 v16, 1, v16\n" \
  "v_mfma_f32_16x16x32_bf16 v[52:55], a[96:99], v[128:131], 0\n" \
  "v_mfma_f32_16x16x32_bf16 v[84:87], a[96:99], v[132:135], 0\n" \
  "buffer_load_dwordx4 a[96:99], v7, s[72:75], s85 offen offset:0 sc1\n" \
  "v_mfma_f32_16x16x32_bf16 v[56:59], a[100:103], v[128:131], 0\n" \
  "v_mfma_f32_16x16x32_bf16 v[88:91], a[100:103], v[132:135], 0\n" \
  "buffer_load_dwordx4 a[100:103], v7, s[72:75], s85 offen offset:1024 sc1\n" \
  "v_mfma_f32_16x16x32_bf16 v[60:63], a[104:107], v[128:131], 0\n" \
  "v_mfma_f32_16x16x32_bf16 v[92:95], a[104:107], v[132:135], 0\n" \
  "buffer_load_dwordx4 a[104:107], v7, s[72:75], s85 offen offset:2048 sc1\n" \
  "v_mfma_f32_16x16x32_bf16 v[64:67], a[108:111], v[128:131], 0\n" \
  "v_mfma_f32_16x16x32_bf16 v[96:99], a[108:111], v[132:135], 0\n" \
  "buffer_load_dwordx4 a[108:111], v7, s[72:75], s85 offen offset:3072 sc1\n" \
  "v_mfma_f32_16x16x32_bf16 v[68:71], a[112:115], v[128:131], 0\n" \
  "v_mfma_f32_16x16x32_bf16 v[100:103], a[112:115], v[132:135], 0\n" \
  "buffer_load_dwordx4 a[112:115], v8, s[72:75], s85 offen offset:0 sc1\n" \
  "v_mfma_f32_16x16x32_bf16 v[72:75], a[116:119], v[128:131], 0\n" \
  "v_mfma_f32_16x16x32_bf16 v[104:107], a[116:119], v[132:135], 0\n" \
  "buffer_load_dwordx4 a[116:119], v8, s[72:75], s85 offen offset:1024 sc1\n" \
  "v_mfma_f32_16x16x32_bf16 v[76:79], a[120:123], v[128:131], 0\n" \
  "v_mfma_f32_16x16x32_bf16 v[108:111], a[120:123], v[132:135], 0\n" \
  "buffer_load_dwordx4 a[120:123], v8, s[72:75], s85 offen offset:2048 sc1\n" \
  "v_mfma_f32_16x16x32_bf16 v[80:83], a[124:127], v[128:131], 0\n" \
  "v_mfma_f32_16x16x32_bf16 v[112:115], a[124:127], v[132:135], 0\n" \
  "buffer_load_dwordx4 a[124:127], v8, s[72:75], s85 offen offset:3072 sc1\n" \
  "buffer_load_dword v14, v13, s[76:79], 0 offen sc1\n" \
  "s_cmp_eq_u32 s90, 0\n" \
  "s_cbranch_scc1 LW1_%=\n" \
  "s_waitcnt vmcnt(26)\n" \
  "LM1_%=:\n" \
  "ds_write_b128 v11, v[44:47]\n" \
  "ds_write_b128 v11, v[48:51] offset:1024\n" \
  "v_mfma_f32_16x16x32_bf16 v[148:151], v[28:31], a[32:35], 0\n" \
  "v_max3_u32 v5, v28, v29, v30\n" \
  "v_max3_u32 v5, v5, v31, v32\n" \
  "v_mfma_f32_16x16x32_bf16 v[152:155], v[28:31], a[48:51], 0\n" \
  "v_max3_u32 v5, v5, v33, v34\n" \
  "v_max3_u32 v5, v5, v35, v36\n" \
  "v_mfma_f32_16x16x32_bf16 v[148:151], v[32:35], a[36:39], v[148:151]\n" \
  "v_max3_u32 v5, v5, v37, v38\n" \
  "v_max3_u32 v5, v5, v39, v40\n" \
  "v_mfma_f32_16x16x32_bf16 v[152:155], v[32:35], a[52:55], v[152:155]\n" \
  "v_max3_u32 v5, v5, v41, v42\n" \
  "v_max3_u32 v5, v5, v43, v44\n" \
  "v_mfma_f32_16x16x32_bf16 v[148:151], v[36:39], a[40:43], v[148:151]\n" \
  "v_max3_u32 v5, v5, v45, v46\n" \
  "v_max3_u32 v5, v5, v47, v48\n" \
  "v_mfma_f32_16x16x32_bf16 v[152:155], v[36:39], a[56:59], v[152:155]\n" \
  "v_max3_u32 v5, v5, v49, v50\n" \
  "v_max_u32 v5, v5, v51\n" \
  "v_mfma_f32_16x16x32_bf16 v[148:151], v[40:43], a[44:47], v[148:151]\n" \
  "v_cmp_eq_u32 vcc, -1, v5\n" \
  "v_mfma_f32_16x16x32_bf16 v[152:155], v[40:43], a[60:63], v[152:155]\n" \
  "ds_read_b64_tr_b16 a[128:129], v12 offset:0\n" \
  "ds_read_b64_tr_b16 a[130:131], v12 offset:256\n" \
  "ds_read_b64_tr_b16 a[132:133], v12 offset:512\n" \
  "ds_read_b64_tr_b16 a[134:135], v12 offset:768\n" \
  "ds_read_b64_tr_b16 a[136:137], v12 offset:1024\n" \
  "ds_read_b64_tr_b16 a[138:139], v12 offset:1280\n" \
  "ds_read_b64_tr_b16 a[140:141], v12 offset:1536\n" \
  "ds_read_b64_tr_b16 a[142:143], v12 offset:1792\n" \
  "s_cbranch_vccnz LR1_%=\n" \
  "v_max_f32 v146, v150, v151\n" \
  "v_max_f32 v147, v154, v155\n" \
  "v_mov_b32 v5, v146\n" \
  "v_mov_b32 v6, v147\n" \
  "s_nop 0\n" \
  "v_permlane16_swap_b32 v146, v5\n" \
  "v_permlane16_swap_b32 v147, v6\n" \
  "v_max_f32 v146, v146, v5\n" \
  "v_max_f32 v147, v147, v6\n" \
  "v_mov_b32 v5, v146\n" \
  "v_mov_b32 v6, v147\n" \
  "s_nop 0\n" \
  "v_permlane32_swap_b32 v146, v5\n" \
  "v_permlane32_swap_b32 v147, v6\n" \
  "v_max_f32 v146, v146, v5\n" \
  "v_max_f32 v147, v147, v6\n" \
  "v_mul_f32 v146, s89, v146\n" \
  "v_mul_f32 v147, s89, v147\n" \
  "v_max_f32 v146, v146, v144\n" \
  "v_max_f32 v147, v147, v145\n" \
  "v_sub_f32 v160, v144, v146\n" \
  "v_sub_f32 v162, v145, v147\n" \
  "v_fma_f32 v150, v150, s89, -v146\n" \
  "v_fma_f32 v151, v151, s89, -v146\n" \
  "v_fma_f32 v154, v154, s89, -v147\n" \
  "v_fma_f32 v155, v155, s89, -v147\n" \
  "v_exp_f32 v150, v150\n" \
  "v_exp_f32 v151, v151\n" \
  "v_exp_f32 v154, v154\n" \
  "v_exp_f32 v155, v155\n" \
  "v_exp_f32 v160, v160\n" \
  "v_exp_f32 v162, v162\n" \
  "v_add_f32 v146, v150, v151\n" \
  "v_add_f32 v147, v154, v155\n" \
  "v_mov_b32 v5, v146\n" \
  "v_mov_b32 v6, v147\n" \
  "s_nop 0\n" \
  "v_permlane16_swap_b32 v146, v5\n" \
  "v_permlane16_swap_b32 v147, v6\n" \
  "v_add_f32 v146, v146, v5\n" \
  "v_add_f32 v147, v147, v6\n" \
  "v_mov_b32 v5, v146\n" \
  "v_mov_b32 v6, v147\n" \
  "s_nop 0\n" \
  "v_permlane32_swap_b32 v146, v5\n" \
  "v_permlane32_swap_b32 v147, v6\n" \
  "v_add_f32 v146, v146, v5\n" \
  "v_add_f32 v147, v147, v6\n" \
  "v_fma_f32 v146, v160, v136, v146\n" \
  "v_fma_f32 v147, v162, v140, v147\n" \
  "v_rcp_f32 v146, v146\n" \
  "v_rcp_f32 v147, v147\n" \
  "v_mul_f32 v160, v160, v146\n" \
  "v_mul_f32 v162, v162, v147\n" \
  "v_mul_f32 v150, v150, v146\n" \
  "v_mul_f32 v151, v151, v146\n" \
  "v_mul_f32 v154, v154, v147\n" \
  "v_mul_f32 v155, v155, v147\n" \
  "v_cvt_pk_bf16_f32 v157, v150, v151\n" \
  "v_cvt_pk_bf16_f32 v159, v154, v155\n" \
  "v_pk_mul_f32 v[52:53], v[52:53], v[160:161] op_sel_hi:[1,0]\n" \
  "v_pk_mul_f32 v[54:55], v[54:55], v[160:161] op_sel_hi:[1,0]\n" \
  "v_pk_mul_f32 v[56:57], v[56:57], v[160:161] op_sel_hi:[1,0]\n" \
  "v_pk_mul_f32 v[58:59], v[58:59], v[160:161] op_sel_hi:[1,0]\n" \
  "v_pk_mul_f32 v[60:61], v[60:61], v[160:161] op_sel_hi:[1,0]\n" \
  "v_pk_mul_f32 v[62:63], v[62:63], v[160:161] op_sel_hi:[1,0]\n" \
  "v_pk_mul_f32 v[64:65], v[64:65], v[160:161] op_sel_hi:[1,0]\n" \
  "v_pk_mul_f32 v[66:67], v[66:67], v[160:161] op_sel_hi:[1,0]\n" \
  "v_pk_mul_f32 v[68:69], v[68:69], v[160:161] op_sel_hi:[1,0]\n" \
  "v_pk_mul_f32 v[70:71], v[70:71], v[160:161] op_sel_hi:[1,0]\n" \
  "v_pk_mul_f32 v[72:73], v[72:73], v[160:161] op_sel_hi:[1,0]\n" \
  "v_pk_mul_f32 v[74:75], v[74:75], v[160:161] op_sel_hi:[1,0]\n" \
  "v_pk_mul_f32 v[76:77], v[76:77], v[160:161] op_sel_hi:[1,0]\n" \
  "v_pk_mul_f32 v[78:79], v[78:79], v[160:161] op_sel_hi:[1,0]\n" \
  "v_pk_mul_f32 v[80:81], v[80:81], v[160:161] op_sel_hi:[1,0]\n" \
  "v_pk_mul_f32 v[82:83], v[82:83], v[160:161] op_sel_hi:[1,0]\n" \
  "v_pk_mul_f32 v[84:85], v[84:85], v[162:163] op_sel_hi:[1,0]\n" \
  "v_pk_mul_f32 v[86:87], v[86:87], v[162:163] op_sel_hi:[1,0]\n" \
  "v_pk_mul_f32 v[88:89], v[88:89], v[162:163] op_sel_hi:[1,0]\n" \
  "v_pk_mul_f32 v[90:91], v[90:91], v[162:163] op_sel_hi:[1,0]\n" \
  "v_pk_mul_f32 v[92:93], v[92:93], v[162:163] op_sel_hi:[1,0]\n" \
  "v_pk_mul_f32 v[94:95], v[94:95], v[162:163] op_sel_hi:[1,0]\n" \
  "v_pk_mul_f32 v[96:97], v[96:97], v[162:163] op_sel_hi:[1,0]\n" \
  "v_pk_mul_f32 v[98:99], v[98:99], v[162:163] op_sel_hi:[1,0]\n" \
  "v_pk_mul_f32 v[100:101], v[100:101], v[162:163] op_sel_hi:[1,0]\n" \
  "v_pk_mul_f32 v[102:103], v[102:103], v[162:163] op_sel_hi:[1,0]\n" \
  "v_pk_mul_f32 v[104:105], v[104:105], v[162:163] op_sel_hi:[1,0]\n" \
  "v_pk_mul_f32 v[106:107], v[106:107], v[162:163] op_sel_hi:[1,0]\n" \
  "v_pk_mul_f32 v[108:109], v[108:109], v[162:163] op_sel_hi:[1,0]\n" \
  "v_pk_mul_f32 v[110:111], v[110:111], v[162:163] op_sel_hi:[1,0]\n" \
  "v_pk_mul_f32 v[112:113], v[112:113], v[162:163] op_sel_hi:[1,0]\n" \
  "v_pk_mul_f32 v[114:115], v[114:115], v[162:163] op_sel_hi:[1,0]\n" \
  "s_waitcnt lgkmcnt(0)\n" \
  "v_mfma_f32_16x16x16_bf16 v[52:55], a[128:129], v[156:157], v[52:55]\n" \
  "v_mfma_f32_16x16x16_bf16 v[56:59], a[130:131], v[156:157], v[56:59]\n" \
  "v_mfma_f32_16x16x16_bf16 v[60:63], a[132:133], v[156:157], v[60:63]\n" \
  "v_mfma_f32_16x16x16_bf16 v[64:67], a[134:135], v[156:157], v[64:67]\n" \
  "v_mfma_f32_16x16x16_bf16 v[68:71], a[136:137], v[156:157], v[68:71]\n" \
  "v_cvt_pk_bf16_f32 v164, v52, v53\n" \
  "v_cvt_pk_bf16_f32 v165, v54, v55\n" \
  "v_mfma_f32_16x16x16_bf16 v[72:75], a[138:139], v[156:157], v[72:75]\n" \
  "v_cvt_pk_bf16_f32 v166, v56, v57\n" \
  "v_cvt_pk_bf16_f32 v167, v58, v59\n" \
  "buffer_store_dwordx4 v[164:167], v7, s[72:75], s86 offen offset:0 sc1\n" \
  "v_mfma_f32_16x16x16_bf16 v[76:79], a[140:141], v[156:157], v[76:79]\n" \
  "v_cvt_pk_bf16_f32 v168, v60, v61\n" \
  "v_cvt_pk_bf16_f32 v169, v62, v63\n" \
  "v_mfma_f32_16x16x16_bf16 v[80:83], a[142:143], v[156:157], v[80:83]\n" \
  "v_cvt_pk_bf16_f32 v170, v64, v65\n" \
  "v_cvt_pk_bf16_f32 v171, v66, v67\n" \
  "buffer_store_dwordx4 v[168:171], v7, s[72:75], s86 offen offset:1024 sc1\n" \
  "v_mfma_f32_16x16x16_bf16 v[84:87], a[128:129], v[158:159], v[84:87]\n" \
  "v_cvt_pk_bf16_f32 v172, v68, v69\n" \
  "v_cvt_pk_bf16_f32 v173, v70, v71\n" \
  "v_mfma_f32_16x16x16_bf16 v[88:91], a[130:131], v[158:159], v[88:91]\n" \
  "v_cvt_pk_bf16_f32 v174, v72, v73\n" \
  "v_cvt_pk_bf16_f32 v175, v74, v75\n" \
  "buffer_store_dwordx4 v[172:175], v7, s[72:75], s86 offen offset:2048 sc1\n" \
  "v_mfma_f32_16x16x16_bf16 v[92:95], a[132:133], v[158:159], v[92:95]\n" \
  "v_cvt_pk_bf16_f32 v176, v76, v77\n" \
  "v_cvt_pk_bf16_f32 v177, v78, v79\n" \
  "v_mfma_f32_16x16x16_bf16 v[96:99], a[134:135], v[158:159], v[96:99]\n" \
  "v_cvt_pk_bf16_f32 v178, v80, v81\n" \
  "v_cvt_pk_bf16_f32 v179, v82, v83\n" \
  "buffer_store_dwordx4 v[176:179], v7, s[72:75], s86 offen offset:3072 sc1\n" \
  "v_mfma_f32_16x16x16_bf16 v[100:103], a[136:137], v[158:159], v[100:103]\n" \
  "v_cvt_pk_bf16_f32 v180, v84, v85\n" \
  "v_cvt_pk_bf16_f32 v181, v86, v87\n" \
  "v_mfma_f32_16x16x16_bf16 v[104:107], a[138:139], v[158:159], v[104:107]\n" \
  "v_cvt_pk_bf16_f32 v182, v88, v89\n" \
  "v_cvt_pk_bf16_f32 v183, v90, v91\n" \
  "buffer_store_dwordx4 v[180:183], v17, s[72:75], s86 offen offset:0 sc1\n" \
  "v_mfma_f32_16x16x16_bf16 v[108:111], a[140:141], v[158:159], v[108:111]\n" \
  "v_cvt_pk_bf16_f32 v184, v92, v93\n" \
  "v_cvt_pk_bf16_f32 v185, v94, v95\n" \
  "v_mfma_f32_16x16x16_bf16 v[112:115], a[142:143], v[158:159], v[112:115]\n" \
  "v_cvt_pk_bf16_f32 v186, v96, v97\n" \
  "v_cvt_pk_bf16_f32 v187, v98, v99\n" \
  "buffer_store_dwordx4 v[184:187], v17, s[72:75], s86 offen offset:1024 sc1\n" \
  "v_cvt_pk_bf16_f32 v188, v100, v101\n" \
  "v_cvt_pk_bf16_f32 v189, v102, v103\n" \
  "v_cvt_pk_bf16_f32 v190, v104, v105\n" \
  "v_cvt_pk_bf16_f32 v191, v106, v107\n" \
  "buffer_store_dwordx4 v[188:191], v17, s[72:75], s86 offen offset:2048 sc1\n" \
  "s_nop 1\n" \
  "v_cvt_pk_bf16_f32 v192, v108, v109\n" \
  "v_cvt_pk_bf16_f32 v193, v110, v111\n" \
  "v_cvt_pk_bf16_f32 v194, v112, v113\n" \
  "v_cvt_pk_bf16_f32 v195, v114, v115\n" \
  "buffer_store_dwordx4 v[192:195], v17, s[72:75], s86 offen offset:3072 sc1\n" \
  "s_add_u32 s82, s82, 1\n" \
  "s_cmp_eq_u32 s82, 64\n" \
  "s_cbranch_scc1 LZ_%=\n" \
  "s_lshl_b32 s87, s82, 19\n" \
  "s_add_u32 s87, s87, " STR(EXP_B) "<<21\n" \
  "s_xor_b32 s87, s87, %6\n" \
  "s_lshr_b32 s80, %3, 3\n" \
  "s_xor_b32 s80, s80, s82\n" \
  "s_and_b32 s80, s80, 7\n" \
  "s_mul_i32 s80, s80, 0x4800\n" \
  "s_and_b32 s80, s80, 0x7000\n" \
  "s_xor_b32 s87, s87, s80\n" \
  "s_and_b32 s80, %3, 7\n" \
  "s_lshl_b32 s80, s80, 16\n" \
  "s_or_b32 s87, s87, s80\n" \
  "s_waitcnt vmcnt(8)\n" \
  "v_readlane_b32 s91, v14, 0\n" \
  "v_readlane_b32 s92, v14, 1\n" \
  "v_readlane_b32 s80, v14, 2\n" \
  "s_min_u32 s92, s92, s80\n" \
  "v_readlane_b32 s80, v14, 3\n" \
  "s_min_u32 s92, s92, s80\n" \
  "v_readlane_b32 s80, v14, 4\n" \
  "s_min_u32 s92, s92, s80\n" \
  "s_cmp_eq_u32 s82, 63\n" \
  "s_addc_u32 s91, s91, 0\n" \
  "s_cmp_gt_u32 s91, s82\n" \
  "s_cselect_b32 s90, 1, 0\n" \
  "s_cselect_b32 s80, 0, 0x80000000\n" \
  "v_or_b32 v9, s80, v7\n" \
  "v_or_b32 v10, s80, v18\n" \
  "buffer_load_dwordx4 v[28:31], v9, s[68:71], s87 offen offset:0 sc1\n" \
  "buffer_load_dwordx4 v[32:35], v9, s[68:71], s87 offen offset:1024 sc1\n" \
  "buffer_load_dwordx4 v[36:39], v9, s[68:71], s87 offen offset:2048 sc1\n" \
  "buffer_load_dwordx4 v[40:43], v9, s[68:71], s87 offen offset:3072 sc1\n" \
  "buffer_load_dwordx4 v[44:47], v10, s[68:71], s87 offen offset:0 sc1\n" \
  "buffer_load_dwordx4 v[48:51], v10, s[68:71], s87 offen offset:1024 sc1\n" \
  "s_branch LA_%=\n" \
  "LG0_%=:\n" \
  "buffer_load_dword v14, v13, s[76:79], 0 offen sc1\n" \
  "s_waitcnt vmcnt(0)\n" \
  "v_readlane_b32 s91, v14, 0\n" \
  "v_readlane_b32 s92, v14, 1\n" \
  "v_readlane_b32 s80, v14, 2\n" \
  "s_min_u32 s92, s92, s80\n" \
  "v_readlane_b32 s80, v14, 3\n" \
  "s_min_u32 s92, s92, s80\n" \
  "v_readlane_b32 s80, v14, 4\n" \
  "s_min_u32 s92, s92, s80\n" \
  "s_cmp_ge_u32 s92, s81\n" \
  "s_cbranch_scc1 LH0_%=\n" \
  "s_sleep 4\n" \
  "s_add_u32 s88, s88, 1\n" \
  "s_cmp_lt_u32 s88, " STR(SPIN_CAP) "\n" \
  "s_cbranch_scc1 LG0_%=\n" \
  "v_mov_b32 v5, " STR(CW_DEAD) "*4\n" \
  "v_mov_b32 v6, 1\n" \
  "buffer_store_dword v6, v5, s[76:79], 0 offen sc1\n" \
  "s_branch LH0_%=\n" \
  "LW0_%=:\n" \
  "s_add_u32 s81, s82, 1\n" \
  "LQ0_%=:\n" \
  "buffer_load_dword v14, v13, s[76:79], 0 offen sc1\n" \
  "s_waitcnt vmcnt(0)\n" \
  "v_readlane_b32 s91, v14, 0\n" \
  "v_readlane_b32 s92, v14, 1\n" \
  "v_readlane_b32 s80, v14, 2\n" \
  "s_min_u32 s92, s92, s80\n" \
  "v_readlane_b32 s80, v14, 3\n" \
  "s_min_u32 s92, s92, s80\n" \
  "v_readlane_b32 s80, v14, 4\n" \
  "s_min_u32 s92, s92, s80\n" \
  "s_cmp_ge_u32 s91, s81\n" \
  "s_cbranch_scc1 LR0_%=\n" \
  "s_sleep 2\n" \
  "s_add_u32 s88, s88, 1\n" \
  "s_cmp_lt_u32 s88, " STR(SPIN_CAP) "\n" \
  "s_cbranch_scc1 LQ0_%=\n" \
  "v_mov_b32 v5, " STR(CW_DEAD) "*4\n" \
  "v_mov_b32 v6, 1\n" \
  "buffer_store_dword v6, v5, s[76:79], 0 offen sc1\n" \
  "s_branch LR0_%=\n" \
  "LR0_%=:\n" \
  "buffer_load_dwordx4 v[28:31], v7, s[68:71], s87 offen offset:0 sc1\n" \
  "buffer_load_dwordx4 v[32:35], v7, s[68:71], s87 offen offset:1024 sc1\n" \
  "buffer_load_dwordx4 v[36:39], v7, s[68:71], s87 offen offset:2048 sc1\n" \
  "buffer_load_dwordx4 v[40:43], v7, s[68:71], s87 offen offset:3072 sc1\n" \
  "buffer_load_dwordx4 v[44:47], v18, s[68:71], s87 offen offset:0 sc1\n" \
  "buffer_load_dwordx4 v[48:51], v18, s[68:71], s87 offen offset:1024 sc1\n" \
  "s_waitcnt vmcnt(0)\n" \
  "v_max3_u32 v5, v28, v29, v30\n" \
  "v_max3_u32 v5, v5, v31, v32\n" \
  "v_max3_u32 v5, v5, v33, v34\n" \
  "v_max3_u32 v5, v5, v35, v36\n" \
  "v_max3_u32 v5, v5, v37, v38\n" \
  "v_max3_u32 v5, v5, v39, v40\n" \
  "v_max3_u32 v5, v5, v41, v42\n" \
  "v_max3_u32 v5, v5, v43, v44\n" \
  "v_max3_u32 v5, v5, v45, v46\n" \
  "v_max3_u32 v5, v5, v47, v48\n" \
  "v_max3_u32 v5, v5, v49, v50\n" \
  "v_max_u32 v5, v5, v51\n" \
  "v_cmp_eq_u32 vcc, -1, v5\n" \
  "s_cbranch_vccz LU0_%=\n" \
  "s_sleep 1\n" \
  "s_add_u32 s88, s88, 1\n" \
  "s_cmp_lt_u32 s88, " STR(SPIN_CAP) "\n" \
  "s_cbranch_scc1 LR0_%=\n" \
  "v_mov_b32 v5, " STR(CW_DEAD) "*4\n" \
  "v_mov_b32 v6, 1\n" \
  "buffer_store_dword v6, v5, s[76:79], 0 offen sc1\n" \
  "s_branch LV0_%=\n" \
  "LV0_%=:\n" \
  "v_mov_b64 v[28:29], 0\n" \
  "v_mov_b64 v[30:31], 0\n" \
  "v_mov_b64 v[32:33], 0\n" \
  "v_mov_b64 v[34:35], 0\n" \
  "v_mov_b64 v[36:37], 0\n" \
  "v_mov_b64 v[38:39], 0\n" \
  "v_mov_b64 v[40:41], 0\n" \
  "v_mov_b64 v[42:43], 0\n" \
  "v_mov_b64 v[44:45], 0\n" \
  "v_mov_b64 v[46:47], 0\n" \
  "v_mov_b64 v[48:49], 0\n" \
  "v_mov_b64 v[50:51], 0\n" \
  "LU0_%=:\n" \
  "s_nop 1\n" \
  "s_branch LM0_%=\n" \
  "LG1_%=:\n" \
  "buffer_load_dword v14, v13, s[76:79], 0 offen sc1\n" \
  "s_waitcnt vmcnt(0)\n" \
  "v_readlane_b32 s91, v14, 0\n" \
  "v_readlane_b32 s92, v14, 1\n" \
  "v_readlane_b32 s80, v14, 2\n" \
  "s_min_u32 s92, s92, s80\n" \
  "v_readlane_b32 s80, v14, 3\n" \
  "s_min_u32 s92, s92, s80\n" \
  "v_readlane_b32 s80, v14, 4\n" \
  "s_min_u32 s92, s92, s80\n" \
  "s_cmp_ge_u32 s92, s81\n" \
  "s_cbranch_scc1 LH1_%=\n" \
  "s_sleep 4\n" \
  "s_add_u32 s88, s88, 1\n" \
  "s_cmp_lt_u32 s88, " STR(SPIN_CAP) "\n" \
  "s_cbranch_scc1 LG1_%=\n" \
  "v_mov_b32 v5, " STR(CW_DEAD) "*4\n" \
  "v_mov_b32 v6, 1\n" \
  "buffer_store_dword v6, v5, s[76:79], 0 offen sc1\n" \
  "s_branch LH1_%=\n" \
  "LW1_%=:\n" \
  "s_add_u32 s81, s82, 1\n" \
  "LQ1_%=:\n" \
  "buffer_load_dword v14, v13, s[76:79], 0 offen sc1\n" \
  "s_waitcnt vmcnt(0)\n" \
  "v_readlane_b32 s91, v14, 0\n" \
  "v_readlane_b32 s92, v14, 1\n" \
  "v_readlane_b32 s80, v14, 2\n" \
  "s_min_u32 s92, s92, s80\n" \
  "v_readlane_b32 s80, v14, 3\n" \
  "s_min_u32 s92, s92, s80\n" \
  "v_readlane_b32 s80, v14, 4\n" \
  "s_min_u32 s92, s92, s80\n" \
  "s_cmp_ge_u32 s91, s81\n" \
  "s_cbranch_scc1 LR1_%=\n" \
  "s_sleep 2\n" \
  "s_add_u32 s88, s88, 1\n" \
  "s_cmp_lt_u32 s88, " STR(SPIN_CAP) "\n" \
  "s_cbranch_scc1 LQ1_%=\n" \
  "v_mov_b32 v5, " STR(CW_DEAD) "*4\n" \
  "v_mov_b32 v6, 1\n" \
  "buffer_store_dword v6, v5, s[76:79], 0 offen sc1\n" \
  "s_branch LR1_%=\n" \
  "LR1_%=:\n" \
  "buffer_load_dwordx4 v[28:31], v7, s[68:71], s87 offen offset:0 sc1\n" \
  "buffer_load_dwordx4 v[32:35], v7, s[68:71], s87 offen offset:1024 sc1\n" \
  "buffer_load_dwordx4 v[36:39], v7, s[68:71], s87 offen offset:2048 sc1\n" \
  "buffer_load_dwordx4 v[40:43], v7, s[68:71], s87 offen offset:3072 sc1\n" \
  "buffer_load_dwordx4 v[44:47], v18, s[68:71], s87 offen offset:0 sc1\n" \
  "buffer_load_dwordx4 v[48:51], v18, s[68:71], s87 offen offset:1024 sc1\n" \
  "s_waitcnt vmcnt(0)\n" \
  "v_max3_u32 v5, v28, v29, v30\n" \
  "v_max3_u32 v5, v5, v31, v32\n" \
  "v_max3_u32 v5, v5, v33, v34\n" \
  "v_max3_u32 v5, v5, v35, v36\n" \
  "v_max3_u32 v5, v5, v37, v38\n" \
  "v_max3_u32 v5, v5, v39, v40\n" \
  "v_max3_u32 v5, v5, v41, v42\n" \
  "v_max3_u32 v5, v5, v43, v44\n" \
  "v_max3_u32 v5, v5, v45, v46\n" \
  "v_max3_u32 v5, v5, v47, v48\n" \
  "v_max3_u32 v5, v5, v49, v50\n" \
  "v_max_u32 v5, v5, v51\n" \
  "v_cmp_eq_u32 vcc, -1, v5\n" \
  "s_cbranch_vccz LU1_%=\n" \
  "s_sleep 1\n" \
  "s_add_u32 s88, s88, 1\n" \
  "s_cmp_lt_u32 s88, " STR(SPIN_CAP) "\n" \
  "s_cbranch_scc1 LR1_%=\n" \
  "v_mov_b32 v5, " STR(CW_DEAD) "*4\n" \
  "v_mov_b32 v6, 1\n" \
  "buffer_store_dword v6, v5, s[76:79], 0 offen sc1\n" \
  "s_branch LV1_%=\n" \
  "LV1_%=:\n" \
  "v_mov_b64 v[28:29], 0\n" \
  "v_mov_b64 v[30:31], 0\n" \
  "v_mov_b64 v[32:33], 0\n" \
  "v_mov_b64 v[34:35], 0\n" \
  "v_mov_b64 v[36:37], 0\n" \
  "v_mov_b64 v[38:39], 0\n" \
  "v_mov_b64 v[40:41], 0\n" \
  "v_mov_b64 v[42:43], 0\n" \
  "v_mov_b64 v[44:45], 0\n" \
  "v_mov_b64 v[46:47], 0\n" \
  "v_mov_b64 v[48:49], 0\n" \
  "v_mov_b64 v[50:51], 0\n" \
  "LU1_%=:\n" \
  "s_nop 1\n" \
  "s_branch LM1_%=\n" \
  "LZ_%=:\n" \
  "s_waitcnt vmcnt(0)\n" \
  "buffer_store_dword v16, v15, s[76:79], 0 offen sc1\n" \
  "s_waitcnt vmcnt(0)\n" \
  ""

#define CONV_ASM \
  "s_waitcnt lgkmcnt(0)\n" \
  "ds_read_b128 v[176:179], %7 offset:0\n" \
  "ds_read_b128 v[180:183], %7 offset:16\n" \
  "ds_read_b128 v[184:187], %7 offset:32\n" \
  "ds_read_b128 v[188:191], %7 offset:48\n" \
  "ds_read_b64 a[0:1], %5 offset:32\n" \
  "ds_read_b64 a[2:3], %5 offset:40\n" \
  "ds_read_b128 a[4:7], %6 offset:2880\n" \
  "ds_read_b128 a[8:11], %6 offset:320\n" \
  "ds_read_b64 a[12:13], %5 offset:2080\n" \
  "ds_read_b64 a[14:15], %5 offset:2088\n" \
  "ds_read_b64 a[16:17], %5 offset:0\n" \
  "ds_read_b64 a[18:19], %5 offset:8\n" \
  "ds_read_b128 a[20:23], %6 offset:2912\n" \
  "ds_read_b128 a[24:27], %6 offset:352\n" \
  "ds_read_b64 a[28:29], %5 offset:2048\n" \
  "ds_read_b64 a[30:31], %5 offset:2056\n" \
  "v_mov_b64 v[192:193], 0\n" \
  "v_mov_b64 v[194:195], 0\n" \
  "v_mov_b64 v[196:197], 0\n" \
  "v_mov_b64 v[198:199], 0\n" \
  "v_mov_b64 v[200:201], 0\n" \
  "v_mov_b64 v[202:203], 0\n" \
  "v_mov_b64 v[204:205], 0\n" \
  "v_mov_b64 v[206:207], 0\n" \
  "v_mov_b64 v[208:209], 0\n" \
  "v_mov_b64 v[210:211], 0\n" \
  "v_mov_b64 v[212:213], 0\n" \
  "v_mov_b64 v[214:215], 0\n" \
  "v_mov_b64 v[216:217], 0\n" \
  "v_mov_b64 v[218:219], 0\n" \
  "v_mov_b64 v[220:221], 0\n" \
  "v_mov_b64 v[222:223], 0\n" \
  "s_movk_i32 s68, 8\n" \
  "s_mov_b32 s69, 0xffff0000\n" \
  ".p2align 5\n" \
  "LC_%=:\n" \
  "s_waitcnt lgkmcnt(6)\n" \
  "v_mfma_f32_32x32x16_bf16 %1, a[0:3], a[4:7], %1\n" \
  "ds_read_b64 a[32:33], %5 offset:96\n" \
  "ds_read_b64 a[34:35], %5 offset:104\n" \
  "v_mul_f32 v224, v176, %11\n" \
  "v_mul_f32 v225, v177, %11\n" \
  "v_mul_f32 v228, v176, %8\n" \
  "v_mfma_f32_16x16x32_bf16 %2, v[196:199], v[192:195], %2\n" \
  "v_mul_f32 v229, v177, %8\n" \
  "v_mul_f32 v232, v176, %9\n" \
  "v_mul_f32 v233, v177, %9\n" \
  "v_mfma_f32_16x16x32_bf16 %3, v[200:203], v[192:195], %3\n" \
  "v_mul_f32 v236, v176, %10\n" \
  "v_mul_f32 v237, v177, %10\n" \
  "v_exp_f32 v224, v224\n" \
  "v_mfma_f32_16x16x32_bf16 %4, v[204:207], v[192:195], %4\n" \
  "v_mfma_f32_32x32x16_bf16 %0, a[0:3], a[8:11], %0\n" \
  "ds_read_b128 a[36:39], %6 offset:2800\n" \
  "ds_read_b128 a[40:43], %6 offset:240\n" \
  "v_fract_f32 v225, v225\n" \
  "v_exp_f32 v228, v228\n" \
  "v_fract_f32 v229, v229\n" \
  "v_mfma_f32_16x16x32_bf16 %2, v[212:215], v[192:195], %2\n" \
  "v_exp_f32 v232, v232\n" \
  "v_fract_f32 v233, v233\n" \
  "v_exp_f32 v236, v236\n" \
  "v_mfma_f32_16x16x32_bf16 %3, v[216:219], v[192:195], %3\n" \
  "v_fract_f32 v237, v237\n" \
  "v_cos_f32 v226, v225\n" \
  "v_sin_f32 v227, v225\n" \
  "v_mfma_f32_16x16x32_bf16 %4, v[220:223], v[192:195], %4\n" \
  "v_mfma_f32_32x32x16_bf16 %1, a[12:15], a[8:11], %1\n" \
  "ds_read_b64 a[44:45], %5 offset:2144\n" \
  "ds_read_b64 a[46:47], %5 offset:2152\n" \
  "v_cos_f32 v230, v229\n" \
  "v_sin_f32 v231, v229\n" \
  "v_cos_f32 v234, v233\n" \
  "v_mfma_f32_16x16x32_bf16 %2, v[196:199], v[208:211], %2\n" \
  "v_sin_f32 v235, v233\n" \
  "v_cos_f32 v238, v237\n" \
  "v_sin_f32 v239, v237\n" \
  "v_mfma_f32_16x16x32_bf16 %3, v[200:203], v[208:211], %3\n" \
  "v_mul_f32 v226, v224, v226\n" \
  "v_mul_f32 v227, v224, v227\n" \
  "v_mul_f32 v230, v228, v230\n" \
  "v_mfma_f32_16x16x32_bf16 %4, v[204:207], v[208:211], %4\n" \
  "s_waitcnt lgkmcnt(6)\n" \
  "v_mfma_f32_32x32x16_bf16 %1, a[16:19], a[20:23], %1\n" \
  "ds_read_b64 a[48:49], %5 offset:64\n" \
  "ds_read_b64 a[50:51], %5 offset:72\n" \
  "v_mul_f32 v231, v228, v231\n" \
  "v_mul_f32 v234, v232, v234\n" \
  "v_mul_f32 v235, v232, v235\n" \
  "v_mul_f32 v238, v236, v238\n" \
  "v_mul_f32 v239, v236, v239\n" \
  "v_mul_f32 v224, v179, v227\n" \
  "v_mul_f32 v225, v179, v226\n" \
  "v_fma_f32 v226, v178, v226, -v224\n" \
  "v_fma_f32 v227, -v178, v227, -v225\n" \
  "ds_read_b128 v[176:179], %7 offset:256\n" \
  "v_cvt_pk_bf16_f32 v196, v230, v231\n" \
  "v_cvt_pk_bf16_f32 v200, v234, v235\n" \
  "v_mfma_f32_32x32x16_bf16 %0, a[16:19], a[24:27], %0\n" \
  "ds_read_b128 a[52:55], %6 offset:2832\n" \
  "ds_read_b128 a[56:59], %6 offset:272\n" \
  "v_cvt_pk_bf16_f32 v204, v238, v239\n" \
  "v_cvt_pk_bf16_f32 v192, v226, v227\n" \
  "v_lshlrev_b32 v228, 16, v196\n" \
  "v_and_b32 v229, s69, v196\n" \
  "v_lshlrev_b32 v232, 16, v200\n" \
  "v_and_b32 v233, s69, v200\n" \
  "v_lshlrev_b32 v236, 16, v204\n" \
  "v_and_b32 v237, s69, v204\n" \
  "v_lshlrev_b32 v224, 16, v192\n" \
  "v_and_b32 v225, s69, v192\n" \
  "v_sub_f32 v230, v230, v228\n" \
  "v_sub_f32 v231, v231, v229\n" \
  "v_mfma_f32_32x32x16_bf16 %1, a[28:31], a[24:27], %1\n" \
  "ds_read_b64 a[60:61], %5 offset:2112\n" \
  "ds_read_b64 a[62:63], %5 offset:2120\n" \
  "v_sub_f32 v234, v234, v232\n" \
  "v_sub_f32 v235, v235, v233\n" \
  "v_sub_f32 v238, v238, v236\n" \
  "v_sub_f32 v239, v239, v237\n" \
  "v_sub_f32 v226, v226, v224\n" \
  "v_sub_f32 v227, v227, v225\n" \
  "v_cvt_pk_bf16_f32 v212, v230, v231\n" \
  "v_cvt_pk_bf16_f32 v216, v234, v235\n" \
  "v_cvt_pk_bf16_f32 v220, v238, v239\n" \
  "v_cvt_pk_bf16_f32 v208, v226, v227\n" \
  "s_waitcnt lgkmcnt(7)\n" \
  "v_mfma_f32_32x32x16_bf16 %1, a[32:35], a[36:39], %1\n" \
  "ds_read_b64 a[0:1], %5 offset:160\n" \
  "ds_read_b64 a[2:3], %5 offset:168\n" \
  "v_mul_f32 v224, v180, %11\n" \
  "v_mul_f32 v225, v181, %11\n" \
  "v_mul_f32 v228, v180, %8\n" \
  "v_mul_f32 v229, v181, %8\n" \
  "v_mul_f32 v232, v180, %9\n" \
  "v_mul_f32 v233, v181, %9\n" \
  "v_mul_f32 v236, v180, %10\n" \
  "v_mul_f32 v237, v181, %10\n" \
  "v_exp_f32 v224, v224\n" \
  "v_fract_f32 v225, v225\n" \
  "v_exp_f32 v228, v228\n" \
  "v_mfma_f32_32x32x16_bf16 %0, a[32:35], a[40:43], %0\n" \
  "ds_read_b128 a[4:7], %6 offset:2720\n" \
  "ds_read_b128 a[8:11], %6 offset:160\n" \
  "v_fract_f32 v229, v229\n" \
  "v_exp_f32 v232, v232\n" \
  "v_fract_f32 v233, v233\n" \
  "v_exp_f32 v236, v236\n" \
  "v_fract_f32 v237, v237\n" \
  "v_cos_f32 v226, v225\n" \
  "v_sin_f32 v227, v225\n" \
  "v_cos_f32 v230, v229\n" \
  "v_sin_f32 v231, v229\n" \
  "v_cos_f32 v234, v233\n" \
  "v_sin_f32 v235, v233\n" \
  "v_mfma_f32_32x32x16_bf16 %1, a[44:47], a[40:43], %1\n" \
  "ds_read_b64 a[12:13], %5 offset:2208\n" \
  "ds_read_b64 a[14:15], %5 offset:2216\n" \
  "v_cos_f32 v238, v237\n" \
  "v_sin_f32 v239, v237\n" \
  "v_mul_f32 v226, v224, v226\n" \
  "v_mul_f32 v227, v224, v227\n" \
  "v_mul_f32 v230, v228, v230\n" \
  "v_mul_f32 v231, v228, v231\n" \
  "v_mul_f32 v234, v232, v234\n" \
  "v_mul_f32 v235, v232, v235\n" \
  "v_mul_f32 v238, v236, v238\n" \
  "v_mul_f32 v239, v236, v239\n" \
  "v_mul_f32 v224, v183, v227\n" \
  "s_waitcnt lgkmcnt(6)\n" \
  "v_mfma_f32_32x32x16_bf16 %1, a[48:51], a[52:55], %1\n" \
  "ds_read_b64 a[16:17], %5 offset:128\n" \
  "ds_read_b64 a[18:19], %5 offset:136\n" \
  "v_mul_f32 v225, v183, v226\n" \
  "v_fma_f32 v226, v182, v226, -v224\n" \
  "v_fma_f32 v227, -v182, v227, -v225\n" \
  "ds_read_b128 v[180:183], %7 offset:272\n" \
  "v_cvt_pk_bf16_f32 v197, v230, v231\n" \
  "v_cvt_pk_bf16_f32 v201, v234, v235\n" \
  "v_cvt_pk_bf16_f32 v205, v238, v239\n" \
  "v_cvt_pk_bf16_f32 v193, v226, v227\n" \
  "v_lshlrev_b32 v228, 16, v197\n" \
  "v_and_b32 v229, s69, v197\n" \
  "v_lshlrev_b32 v232, 16, v201\n" \
  "v_mfma_f32_32x32x16_bf16 %0, a[48:51], a[56:59], %0\n" \
  "ds_read_b128 a[20:23], %6 offset:2752\n" \
  "ds_read_b128 a[24:27], %6 offset:192\n" \
  "v_and_b32 v233, s69, v201\n" \
  "v_lshlrev_b32 v236, 16, v205\n" \
  "v_and_b32 v237, s69, v205\n" \
  "v_lshlrev_b32 v224, 16, v193\n" \
  "v_and_b32 v225, s69, v193\n" \
  "v_sub_f32 v230, v230, v228\n" \
  "v_sub_f32 v231, v231, v229\n" \
  "v_sub_f32 v234, v234, v232\n" \
  "v_sub_f32 v235, v235, v233\n" \
  "v_sub_f32 v238, v238, v236\n" \
  "v_sub_f32 v239, v239, v237\n" \
  "v_mfma_f32_32x32x16_bf16 %1, a[60:63], a[56:59], %1\n" \
  "ds_read_b64 a[28:29], %5 offset:2176\n" \
  "ds_read_b64 a[30:31], %5 offset:2184\n" \
  "v_sub_f32 v226, v226, v224\n" \
  "v_sub_f32 v227, v227, v225\n" \
  "v_cvt_pk_bf16_f32 v213, v230, v231\n" \
  "v_cvt_pk_bf16_f32 v217, v234, v235\n" \
  "v_cvt_pk_bf16_f32 v221, v238, v239\n" \
  "v_cvt_pk_bf16_f32 v209, v226, v227\n" \
  "s_waitcnt lgkmcnt(7)\n" \
  "v_mfma_f32_32x32x16_bf16 %1, a[0:3], a[4:7], %1\n" \
  "ds_read_b64 a[32:33], %5 offset:224\n" \
  "ds_read_b64 a[34:35], %5 offset:232\n" \
  "v_mul_f32 v224, v184, %11\n" \
  "v_mul_f32 v225, v185, %11\n" \
  "v_mul_f32 v228, v184, %8\n" \
  "v_mul_f32 v229, v185, %8\n" \
  "v_mul_f32 v232, v184, %9\n" \
  "v_mul_f32 v233, v185, %9\n" \
  "v_mul_f32 v236, v184, %10\n" \
  "v_mul_f32 v237, v185, %10\n" \
  "v_exp_f32 v224, v224\n" \
  "v_fract_f32 v225, v225\n" \
  "v_exp_f32 v228, v228\n" \
  "v_mfma_f32_32x32x16_bf16 %0, a[0:3], a[8:11], %0\n" \
  "ds_read_b128 a[36:39], %6 offset:2640\n" \
  "ds_read_b128 a[40:43], %6 offset:80\n" \
  "v_fract_f32 v229, v229\n" \
  "v_exp_f32 v232, v232\n" \
  "v_fract_f32 v233, v233\n" \
  "v_exp_f32 v236, v236\n" \
  "v_fract_f32 v237, v237\n" \
  "v_cos_f32 v226, v225\n" \
  "v_sin_f32 v227, v225\n" \
  "v_cos_f32 v230, v229\n" \
  "v_sin_f32 v231, v229\n" \
  "v_cos_f32 v234, v233\n" \
  "v_sin_f32 v235, v233\n" \
  "v_mfma_f32_32x32x16_bf16 %1, a[12:15], a[8:11], %1\n" \
  "ds_read_b64 a[44:45], %5 offset:2272\n" \
  "ds_read_b64 a[46:47], %5 offset:2280\n" \
  "v_cos_f32 v238, v237\n" \
  "v_sin_f32 v239, v237\n" \
  "v_mul_f32 v226, v224, v226\n" \
  "v_mul_f32 v227, v224, v227\n" \
  "v_mul_f32 v230, v228, v230\n" \
  "v_mul_f32 v231, v228, v231\n" \
  "v_mul_f32 v234, v232, v234\n" \
  "v_mul_f32 v235, v232, v235\n" \
  "v_mul_f32 v238, v236, v238\n" \
  "v_mul_f32 v239, v236, v239\n" \
  "v_mul_f32 v224, v187, v227\n" \
  "s_waitcnt lgkmcnt(6)\n" \
  "v_mfma_f32_32x32x16_bf16 %1, a[16:19], a[20:23], %1\n" \
  "ds_read_b64 a[48:49], %5 offset:192\n" \
  "ds_read_b64 a[50:51], %5 offset:200\n" \
  "v_mul_f32 v225, v187, v226\n" \
  "v_fma_f32 v226, v186, v226, -v224\n" \
  "v_fma_f32 v227, -v186, v227, -v225\n" \
  "ds_read_b128 v[184:187], %7 offset:288\n" \
  "v_cvt_pk_bf16_f32 v198, v230, v231\n" \
  "v_cvt_pk_bf16_f32 v202, v234, v235\n" \
  "v_cvt_pk_bf16_f32 v206, v238, v239\n" \
  "v_cvt_pk_bf16_f32 v194, v226, v227\n" \
  "v_lshlrev_b32 v228, 16, v198\n" \
  "v_and_b32 v229, s69, v198\n" \
  "v_lshlrev_b32 v232, 16, v202\n" \
  "v_mfma_f32_32x32x16_bf16 %0, a[16:19], a[24:27], %0\n" \
  "ds_read_b128 a[52:55], %6 offset:2672\n" \
  "ds_read_b128 a[56:59], %6 offset:112\n" \
  "v_and_b32 v233, s69, v202\n" \
  "v_lshlrev_b32 v236, 16, v206\n" \
  "v_and_b32 v237, s69, v206\n" \
  "v_lshlrev_b32 v224, 16, v194\n" \
  "v_and_b32 v225, s69, v194\n" \
  "v_sub_f32 v230, v230, v228\n" \
  "v_sub_f32 v231, v231, v229\n" \
  "v_sub_f32 v234, v234, v232\n" \
  "v_sub_f32 v235, v235, v233\n" \
  "v_sub_f32 v238, v238, v236\n" \
  "v_sub_f32 v239, v239, v237\n" \
  "v_mfma_f32_32x32x16_bf16 %1, a[28:31], a[24:27], %1\n" \
  "ds_read_b64 a[60:61], %5 offset:2240\n" \
  "ds_read_b64 a[62:63], %5 offset:2248\n" \
  "v_sub_f32 v226, v226, v224\n" \
  "v_sub_f32 v227, v227, v225\n" \
  "v_cvt_pk_bf16_f32 v214, v230, v231\n" \
  "v_cvt_pk_bf16_f32 v218, v234, v235\n" \
  "v_cvt_pk_bf16_f32 v222, v238, v239\n" \
  "v_cvt_pk_bf16_f32 v210, v226, v227\n" \
  "s_waitcnt lgkmcnt(7)\n" \
  "v_mfma_f32_32x32x16_bf16 %1, a[32:35], a[36:39], %1\n" \
  "ds_read_b64 a[0:1], %5 offset:288\n" \
  "ds_read_b64 a[2:3], %5 offset:296\n" \
  "v_mul_f32 v224, v188, %11\n" \
  "v_mul_f32 v225, v189, %11\n" \
  "v_mul_f32 v228, v188, %8\n" \
  "v_mul_f32 v229, v189, %8\n" \
  "v_mul_f32 v232, v188, %9\n" \
  "v_mul_f32 v233, v189, %9\n" \
  "v_mul_f32 v236, v188, %10\n" \
  "v_mul_f32 v237, v189, %10\n" \
  "v_exp_f32 v224, v224\n" \
  "v_fract_f32 v225, v225\n" \
  "v_exp_f32 v228, v228\n" \
  "v_mfma_f32_32x32x16_bf16 %0, a[32:35], a[40:43], %0\n" \
  "ds_read_b128 a[4:7], %6 offset:2560\n" \
  "ds_read_b128 a[8:11], %6 offset:0\n" \
  "v_fract_f32 v229, v229\n" \
  "v_exp_f32 v232, v232\n" \
  "v_fract_f32 v233, v233\n" \
  "v_exp_f32 v236, v236\n" \
  "v_fract_f32 v237, v237\n" \
  "v_cos_f32 v226, v225\n" \
  "v_sin_f32 v227, v225\n" \
  "v_cos_f32 v230, v229\n" \
  "v_sin_f32 v231, v229\n" \
  "v_cos_f32 v234, v233\n" \
  "v_sin_f32 v235, v233\n" \
  "v_mfma_f32_32x32x16_bf16 %1, a[44:47], a[40:43], %1\n" \
  "ds_read_b64 a[12:13], %5 offset:2336\n" \
  "ds_read_b64 a[14:15], %5 offset:2344\n" \
  "v_cos_f32 v238, v237\n" \
  "v_sin_f32 v239, v237\n" \
  "v_mul_f32 v226, v224, v226\n" \
  "v_mul_f32 v227, v224, v227\n" \
  "v_mul_f32 v230, v228, v230\n" \
  "v_mul_f32 v231, v228, v231\n" \
  "v_mul_f32 v234, v232, v234\n" \
  "v_mul_f32 v235, v232, v235\n" \
  "v_mul_f32 v238, v236, v238\n" \
  "v_mul_f32 v239, v236, v239\n" \
  "v_mul_f32 v224, v191, v227\n" \
  "s_waitcnt lgkmcnt(6)\n" \
  "v_mfma_f32_32x32x16_bf16 %1, a[48:51], a[52:55], %1\n" \
  "ds_read_b64 a[16:17], %5 offset:256\n" \
  "ds_read_b64 a[18:19], %5 offset:264\n" \
  "v_mul_f32 v225, v191, v226\n" \
  "v_fma_f32 v226, v190, v226, -v224\n" \
  "v_fma_f32 v227, -v190, v227, -v225\n" \
  "ds_read_b128 v[188:191], %7 offset:304\n" \
  "v_cvt_pk_bf16_f32 v199, v230, v231\n" \
  "v_cvt_pk_bf16_f32 v203, v234, v235\n" \
  "v_cvt_pk_bf16_f32 v207, v238, v239\n" \
  "v_cvt_pk_bf16_f32 v195, v226, v227\n" \
  "v_lshlrev_b32 v228, 16, v199\n" \
  "v_and_b32 v229, s69, v199\n" \
  "v_lshlrev_b32 v232, 16, v203\n" \
  "v_mfma_f32_32x32x16_bf16 %0, a[48:51], a[56:59], %0\n" \
  "ds_read_b128 a[20:23], %6 offset:2592\n" \
  "ds_read_b128 a[24:27], %6 offset:32\n" \
  "v_and_b32 v233, s69, v203\n" \
  "v_lshlrev_b32 v236, 16, v207\n" \
  "v_and_b32 v237, s69, v207\n" \
  "v_lshlrev_b32 v224, 16, v195\n" \
  "v_and_b32 v225, s69, v195\n" \
  "v_sub_f32 v230, v230, v228\n" \
  "v_sub_f32 v231, v231, v229\n" \
  "v_sub_f32 v234, v234, v232\n" \
  "v_sub_f32 v235, v235, v233\n" \
  "v_sub_f32 v238, v238, v236\n" \
  "v_sub_f32 v239, v239, v237\n" \
  "v_mfma_f32_32x32x16_bf16 %1, a[60:63], a[56:59], %1\n" \
  "ds_read_b64 a[28:29], %5 offset:2304\n" \
  "ds_read_b64 a[30:31], %5 offset:2312\n" \
  "v_sub_f32 v226, v226, v224\n" \
  "v_sub_f32 v227, v227, v225\n" \
  "v_cvt_pk_bf16_f32 v215, v230, v231\n" \
  "v_cvt_pk_bf16_f32 v219, v234, v235\n" \
  "v_cvt_pk_bf16_f32 v223, v238, v239\n" \
  "v_cvt_pk_bf16_f32 v211, v226, v227\n" \
  "v_add_u32 %5, 256, %5\n" \
  "v_subrev_u32 %6, 320, %6\n" \
  "v_add_u32 %7, 256, %7\n" \
  "s_sub_u32 s68, s68, 1\n" \
  "s_cmp_lg_u32 s68, 0\n" \
  "s_cbranch_scc1 LC_%=\n" \
  "v_mfma_f32_16x16x32_bf16 %2, v[196:199], v[192:195], %2\n" \
  "v_mfma_f32_16x16x32_bf16 %3, v[200:203], v[192:195], %3\n" \
  "v_mfma_f32_16x16x32_bf16 %4, v[204:207], v[192:195], %4\n" \
  "v_mfma_f32_16x16x32_bf16 %2, v[212:215], v[192:195], %2\n" \
  "v_mfma_f32_16x16x32_bf16 %3, v[216:219], v[192:195], %3\n" \
  "v_mfma_f32_16x16x32_bf16 %4, v[220:223], v[192:195], %4\n" \
  "v_mfma_f32_16x16x32_bf16 %2, v[196:199], v[208:211], %2\n" \
  "v_mfma_f32_16x16x32_bf16 %3, v[200:203], v[208:211], %3\n" \
  "v_mfma_f32_16x16x32_bf16 %4, v[204:207], v[208:211], %4\n" \
  "s_waitcnt lgkmcnt(0)\n" \
  "s_nop 15\n" \
  ""

#define CONV_CLOBBERS "a0", "a1", "a2", "a3", "a4", "a5", "a6", "a7", "a8", "a9", "a10", "a11", "a12", "a13", "a14", "a15", "a16", "a17", "a18", "a19", "a20", "a21", "a22", "a23", "a24", "a25", "a26", "a27", "a28", "a29", "a30", "a31", "a32", "a33", "a34", "a35", "a36", "a37", "a38", "a39", "a40", "a41", "a42", "a43", "a44", "a45", "a46", "a47", "a48", "a49", "a50", "a51", "a52", "a53", "a54", "a55", "a56", "a57", "a58", "a59", "a60", "a61", "a62", "a63", \
  "v176", "v177", "v178", "v179", "v180", "v181", "v182", "v183", "v184", "v185", "v186", "v187", "v188", "v189", "v190", "v191", "v192", "v193", "v194", "v195", "v196", "v197", "v198", "v199", "v200", "v201", "v202", "v203", "v204", "v205", "v206", "v207", "v208", "v209", "v210", "v211", "v212", "v213", "v214", "v215", "v216", "v217", "v218", "v219", "v220", "v221", "v222", "v223", "v224", "v225", "v226", "v227", "v228", "v229", "v230", "v231", "v232", "v233", "v234", "v235", "v236", "v237", "v238", "v239", "s68", "s69", "scc", "memory"
#define REC_CLOBBERS \
  "v1", "v2", "v3", "v4", "v5", "v6", "v7", "v8", "v9", "v10", "v11", "v12", "v13", "v14", "v15", "v16", "v17", "v18", "v19", "v20", "v21", "v22",\
  "v23", "v24", "v25", "v26", "v27", "v28", "v29", "v30", "v31", "v32", "v33", "v34", "v35", "v36", "v37", "v38", "v39", "v40", "v41", "v42", "v43",\
  "v44", "v45", "v46", "v47", "v48", "v49", "v50", "v51", "v52", "v53", "v54", "v55", "v56", "v57", "v58", "v59", "v60", "v61", "v62", "v63", "v64",\
  "v65", "v66", "v67", "v68", "v69", "v70", "v71", "v72", "v73", "v74", "v75", "v76", "v77", "v78", "v79", "v80", "v81", "v82", "v83", "v84", "v85",\
  "v86", "v87", "v88", "v89", "v90", "v91", "v92", "v93", "v94", "v95", "v96", "v97", "v98", "v99", "v100", "v101", "v102", "v103", "v104", "v105",\
  "v106", "v107", "v108", "v109", "v110", "v111", "v112", "v113", "v114", "v115", "v116", "v117", "v118", "v119", "v120", "v121", "v122", "v123",\
  "v124", "v125", "v126", "v127", "v128", "v129", "v130", "v131", "v132", "v133", "v134", "v135", "v136", "v137", "v138", "v139", "v140", "v141",\
  "v142", "v143", "v144", "v145", "v146", "v147", "v148", "v149", "v150", "v151", "v152", "v153", "v154", "v155", "v156", "v157", "v158", "v159",\
  "v160", "v161", "v162", "v163", "v164", "v165", "v166", "v167", "v168", "v169", "v170", "v171", "v172", "v173", "v174", "v175", "v176", "v177",\
  "v178", "v179", "v180", "v181", "v182", "v183", "v184", "v185", "v186", "v187", "v188", "v189", "v190", "v191", "v192", "v193", "v194", "v195",\
  "v196", "v197", "v198", "v199", "v200", "v201", "v202", "v203", "v204", "v205", "v206", "v207", "v208", "v209", "v210", "v211", "v212", "v213",\
  "v214", "v215", "v216", "v217", "v218", "v219", "v220", "v221", "v222", "v223", "v224", "v225", "v226", "v227", "v228", "v229", "v230", "v231",\
  "v232", "v233", "v234", "v235", "v236", "v237", "v238", "v239", "v240", "v241", "v242", "v243", "v244", "v245", "v246", "v247", "v248", "v249",\
  "v250", "v251", "v252", "v253", "v254", "v255", "a0", "a1", "a2", "a3", "a4", "a5", "a6", "a7", "a8", "a9", "a10", "a11", "a12", "a13", "a14", "a15",\
  "a16", "a17", "a18", "a19", "a20", "a21", "a22", "a23", "a24", "a25", "a26", "a27", "a28", "a29", "a30", "a31", "a32", "a33", "a34", "a35", "a36",\
  "a37", "a38", "a39", "a40", "a41", "a42", "a43", "a44", "a45", "a46", "a47", "a48", "a49", "a50", "a51", "a52", "a53", "a54", "a55", "a56", "a57",\
  "a58", "a59", "a60", "a61", "a62", "a63", "a64", "a65", "a66", "a67", "a68", "a69", "a70", "a71", "a72", "a73", "a74", "a75", "a76", "a77", "a78",\
  "a79", "a80", "a81", "a82", "a83", "a84", "a85", "a86", "a87", "a88", "a89", "a90", "a91", "a92", "a93", "a94", "a95", "a96", "a97", "a98", "a99",\
  "a100", "a101", "a102", "a103", "a104", "a105", "a106", "a107", "a108", "a109", "a110", "a111", "a112", "a113", "a114", "a115", "a116", "a117",\
  "a118", "a119", "a120", "a121", "a122", "a123", "a124", "a125", "a126", "a127", "a128", "a129", "a130", "a131", "a132", "a133", "a134", "a135",\
  "a136", "a137", "a138", "a139", "a140", "a141", "a142", "a143", "a144", "a145", "a146", "a147", "a148", "a149", "a150", "a151", "a152", "a153",\
  "a154", "a155", "a156", "a157", "a158", "a159", "a160", "a161", "a162", "a163", "a164", "a165", "a166", "a167", "a168", "a169", "a170", "a171",\
  "a172", "a173", "a174", "a175", "a176", "a177", "a178", "a179", "a180", "a181", "a182", "a183", "a184", "a185", "a186", "a187", "a188", "a189",\
  "a190", "a191", "a192", "a193", "a194", "a195", "a196", "a197", "a198", "a199", "a200", "a201", "a202", "a203", "a204", "a205", "a206", "a207",\
  "a208", "a209", "a210", "a211", "a212", "a213", "a214", "a215", "a216", "a217", "a218", "a219", "a220", "a221", "a222", "a223", "a224", "a225",\
  "a226", "a227", "a228", "a229", "a230", "a231", "a232", "a233", "a234", "a235", "a236", "a237", "a238", "a239", "a240", "a241", "a242", "a243",\
  "a244", "a245", "a246", "a247", "a248", "a249", "a250", "a251", "a252", "a253", "a254", "a255", "s28", "s29", "s30", "s31", "s34", "s35", "s36", "s37", "s38", "s39", "s40", "s41", "s42",\
  "s43", "s44", "s45", "s46", "s47", "s48", "s49", "s50", "s51", "s52", "s53", "s54", "s55", "s56", "s57", "s58", "s59", "s60", "s61", "s62", "s63",\
  "s64", "s65", "s66", "s67", "s68", "s69", "s70", "s71", "s72", "s73", "s74", "s75", "s76", "s77", "s78", "s79", "s80", "s81", "s82", "s83", "s84",\
  "s85", "s86", "s87", "s88", "s89", "s90", "s91", "s92", "s93", "s94", "s95", "s96", "s97", "s98", "s99", "vcc", "scc", "memory"
__device__ __forceinline__ void role_rec(const Args& a, char LAS* smem, const int rk, const u64 tag) {
  const int cu = rk;
  const int tid = threadIdx.x, w = __builtin_amdgcn_readfirstlane(tid >> 6);
  const int head = cu >> 2, j = cu & 3;
  const u16* pq = a.Wq + head * DH + 64 * (j >> 1);
  const u16* pk = a.Wk + head * DH + 64 * (j >> 1);
  const u16* pv = a.Wv + head * DH + 64 * ((j >> 1) ^ 1);
  const u16* po = a.Wo + (size_t)head * DH * DM + 256 * j;
  asm volatile(REC_ASM
               :: "s"(pq), "s"(pk), "s"(pv), "s"(po), "s"(a.ex), "s"(a.ctrl), "s"(cu), "s"(w), "s"(tag), "s"(a.mem0 + (size_t)(4 * (cu & 1)) * DM + 16 * (cu >> 1))
               : REC_CLOBBERS);
  __builtin_unreachable();
}
#define S_ROW 4160
#define S_WSZ (4 * S_ROW + 96 * 80 + 2048 + 64)
#define T_LD 132
__device__ __forceinline__ u32 offT(u32 pol, u32 ch) { return (ch >> 1) * 16384 + ((~(pol >> (ch >> 8)) & 1u) << 13) + (ch & 1) * 4096; }
__device__ __forceinline__ void wait_all(u32* ctrl, int word, int n, int slp) {
  const int lane = threadIdx.x & 63;
  for (int spins = 0;; ++spins) {
    asm volatile("" ::: "memory");
    const u32 v = __hip_atomic_load(ctrl + word + 32 * min(lane, n - 1), RLX_AGENT);
    if (__builtin_amdgcn_ballot_w64(v == 0) == 0) break;
    if (spins > SPIN_CAP) { set_dead(ctrl); break; }
    if ((spins & 63) == 63 && __builtin_amdgcn_readfirstlane((int)is_dead(ctrl))) break;
    sleepn(slp);
  }
  asm volatile("" ::: "memory");
}
__device__ __forceinline__ void ssm_in(const Args& a, char LAS* smem, const int rb, const u32 pol) {
  const int tid = threadIdx.x;
  const __amdgpu_buffer_rsrc_t rt = __builtin_amdgcn_make_buffer_rsrc(a.ex + WS_UT_OFF, 0, 8 << 20, 0x00020000);
#pragma unroll
  for (int q = 0; q < 4; ++q) {
    u32x4 x[8];
#pragma unroll
    for (int i = 0; i < 4; ++i) {
      const u16* p = a.u + (size_t)(64 * rb + 2 * (8 * q + 2 * i + (tid >> 7))) * SSM_D + (tid & 127) * 8;
      x[2 * i] = *(const u32x4*)p; x[2 * i + 1] = *(const u32x4*)(p + SSM_D);
    }
#pragma unroll
    for (int i = 0; i < 4; ++i)
#pragma unroll
      for (int e = 0; e < 4; ++e) {
        char LAS* o = smem + ((tid & 127) + 256 * e) * T_LD + (8 * q + 2 * i + (tid >> 7)) * 4;
        *(LAS u32*)o = __builtin_amdgcn_perm(x[2 * i + 1][e], x[2 * i][e], 0x05040100u);
        *(LAS u32*)(o + 128 * T_LD) = __builtin_amdgcn_perm(x[2 * i + 1][e], x[2 * i][e], 0x07060302u);
      }
  }
  __syncthreads();
#pragma unroll 4
  for (int i = 0; i < 32; ++i) {
    const int sl = 32 * i + (tid >> 3), ch = (sl & 127) * 8 + (sl >> 7);
    const LAS u32* p = (const LAS u32*)(smem + sl * T_LD + (tid & 7) * 16);
    __builtin_amdgcn_raw_buffer_store_b128((u32x4){p[0], p[1], p[2], p[3]}, rt, offT(pol, ch) + rb * 128 + (tid & 7) * 16, 0, SC1);
  }
  WAIT_VM0();
  __syncthreads();
  if (tid == 0) __hip_atomic_store(a.ctrl + CW_UIN + rb * 32, 1u, RLX_AGENT);
}
__device__ __forceinline__ void ssm_out(const Args& a, char LAS* smem, const int rb, const int hb, const u32 pol) {
  const int tid = threadIdx.x;
  const __amdgpu_buffer_rsrc_t rt = __builtin_amdgcn_make_buffer_rsrc(a.ex + WS_UT_OFF, 0, 8 << 20, 0x00020000);
  const __amdgpu_buffer_rsrc_t ro = __builtin_amdgcn_make_buffer_rsrc(a.out, 0, S_LEN * OUTLD * 2, 0x00020000);
  wait_all(a.ctrl, CW_YOUT + hb * 48 * 32, 48, 8);
  u32x4 x[16];
#pragma unroll
  for (int i = 0; i < 16; ++i) {
    const int sl = 32 * i + (tid >> 3), ch = (sl & 63) * 8 + (sl >> 6);
    x[i] = __builtin_amdgcn_raw_buffer_load_b128(rt, offT(pol, 512 * hb + ch) + rb * 128 + (tid & 7) * 16, 0, SC1);
  }
#pragma unroll
  for (int i = 0; i < 16; ++i) {
    LAS u32* p = (LAS u32*)(smem + (32 * i + (tid >> 3)) * T_LD + (tid & 7) * 16);
#pragma unroll
    for (int e = 0; e < 4; ++e) p[e] = x[i][e];
  }
  __syncthreads();
#pragma unroll 2
  for (int i = 0; i < 8; ++i) {
    const int rp = 4 * i + (tid >> 6);
    u32x4 v0, v1;
#pragma unroll
    for (int e = 0; e < 4; ++e) {
      const char LAS* o = smem + ((tid & 63) + 128 * e) * T_LD + rp * 4;
      const u32 x0 = *(const LAS u32*)o, x1 = *(const LAS u32*)(o + 64 * T_LD);
      v0[e] = __builtin_amdgcn_perm(x1, x0, 0x05040100u); v1[e] = __builtin_amdgcn_perm(x1, x0, 0x07060302u);
    }
    const u32 oo = ((64 * rb + 2 * rp) * OUTLD + DM + 512 * hb + (tid & 63) * 8) * 2;
    __builtin_amdgcn_raw_buffer_store_b128(v0, ro, oo, 0, OAUX);
    __builtin_amdgcn_raw_buffer_store_b128(v1, ro, oo + OUTLD * 2, 0, OAUX);
  }
}
__device__ __forceinline__ u32 lo_hi(u32 lo, u32 hi) { return __builtin_amdgcn_perm(hi, lo, 0x07060100u); }
__device__ __forceinline__ void role_ssm(const Args& a, char LAS* smem, const int cx, const int rank, const u64 tag) {
  u32 pol = 0;
  const int tid = threadIdx.x, lane = tid & 63, w = __builtin_amdgcn_readfirstlane(tid >> 6);
  const int c = lane & 15, g = lane >> 4, b = lane & 31, h = lane >> 5;
  const __amdgpu_buffer_rsrc_t rt = __builtin_amdgcn_make_buffer_rsrc(a.ex + WS_UT_OFF, 0, 8 << 20, 0x00020000);
  char LAS* lu = smem + w * S_WSZ;
  char LAS* lw = lu + S_ROW;
  char LAS* lk = lw + 3 * S_ROW + 31 * 80;
  char LAS* lp = lk + 65 * 80;
  if (lane < 4) *(LAS u32x4*)(lu + lane * 16) = (u32x4){0, 0, 0, 0};
  for (int i = lane; i < 160; i += 64) *(LAS u32x4*)(lw + 3 * S_ROW + i * 16) = (u32x4){0, 0, 0, 0};
  float la[3];
  la[0] = (float)c;
  la[1] = c < 8 ? (float)(256 + 2 * c) : (float)(512 + 256 * ((c - 8) >> 2) + 4 * ((c - 8) & 3));
  la[2] = c < 8 ? (float)(1024 + 256 * (c >> 1) + 8 * (c & 1)) : (c == 8 ? 2048.f : 0.f);
  const float ly = (float)(16 * c);
  u32x4 un[4];
  auto fetch_u = [&](int d) {
#pragma unroll
    for (int i = 0; i < 4; ++i) un[i] = __builtin_amdgcn_raw_buffer_load_b128(rt, lane * 16 + i * 1024, offT(pol, d), SC1);
  };
  u32 pr[6]; u16 pdt, pds;
  auto fetch_p = [&](int d) {
    const size_t o = (size_t)d * SSM_N + 2 * lane;
    pr[0] = *(const u32*)(a.A_re + o); pr[1] = *(const u32*)(a.A_im + o); pr[2] = *(const u32*)(a.B_re + o);
    pr[3] = *(const u32*)(a.B_im + o); pr[4] = *(const u32*)(a.C_re + o); pr[5] = *(const u32*)(a.C_im + o);
    pdt = a.log_dt[d]; pds = a.Dskip[d];
  };
#define SST(i)
  const int first = cx * 256 + rank * 3 + w, last = cx * 256 + 256;
  const int p0 = 32 + b - 8 * h - 7, cp = p0 & 3;
  f32x4 ka[3];
  f32x16 y0, y1;
  float dsn = 0.f;
  auto params = [&]() {
    const float dt = rbf(expf(bf2f(pdt)));
    dsn = bf2f(pds);
#pragma unroll
    for (int i = 0; i < 2; ++i) {
      auto el = [&](int k) { return __uint_as_float(i ? (pr[k] & 0xffff0000u) : (pr[k] << 16)); };
      const float br = dt * el(2), bi = dt * el(3), cr = el(4), ci = el(5);
      *(LAS f32x4*)(lp + (2 * lane + i) * 16) = (f32x4){dt * el(0) * 1.4426950408889634f, dt * el(1) * 0.15915494309189535f, cr * br - ci * bi, cr * bi + ci * br};
    }
  };
  auto products = [&]() {
    u32 ra = (u32)(uintptr_t)(lu + cp * S_ROW + (p0 - cp) * 2 - 32), rb = (u32)(uintptr_t)(lk + 16 * h + 80 * (b - 3)), rp = (u32)(uintptr_t)(lp + g * 64);
#pragma unroll
    for (int t = 0; t < 3; ++t) ka[t] = (f32x4){0, 0, 0, 0};
#pragma unroll
    for (int i = 0; i < 16; ++i) { y0[i] = 0.f; y1[i] = 0.f; }
    asm volatile(CONV_ASM : "+v"(y0), "+v"(y1), "+v"(ka[0]), "+v"(ka[1]), "+v"(ka[2]), "+v"(ra), "+v"(rb), "+v"(rp)
                 : "v"(la[0]), "v"(la[1]), "v"(la[2]), "v"(ly) : CONV_CLOBBERS);
  };
  fetch_p(first);
  {
    for (int d = first - 24; d < last; d += 24) {
      const int nxt = d + 24 < last ? d + 24 : d;
      const float ds = dsn;
      SST(0);
      if (d >= first) {
      {
        const int l = 16 * c + 4 * g;
        *(LAS u32x2*)(lk + (1 + (l >> 5)) * 80 + ((l & 31) ^ 4) * 2) = (u32x2){pk2(ka[0][3], ka[0][2]), pk2(ka[0][1], ka[0][0])};
        const int j1 = g < 2 ? 8 * c + 4 * g : 128 + 64 * (g - 2) + 4 * c;
        *(LAS u32x4*)(lp + j1 * 4) = (u32x4){pk2(ka[1][0], ka[1][0]), pk2(ka[1][1], ka[1][1]), pk2(ka[1][2], ka[1][2]), pk2(ka[1][3], ka[1][3])};
        if (g < 2) {
          *(LAS u32x2*)(lp + (256 + 64 * g + 2 * c) * 4) = (u32x2){pk2(ka[2][0], ka[2][0]), pk2(ka[2][1], ka[2][1])};
          *(LAS u32x2*)(lp + (256 + 64 * g + 32 + 2 * c) * 4) = (u32x2){pk2(ka[2][2], ka[2][2]), pk2(ka[2][3], ka[2][3])};
        } else if (lane == 32) *(LAS u32*)(lp + 384 * 4) = pk2(ka[2][0], ka[2][0]);
        if (lane < 32) {
          const u32x4 v = *(const LAS u32x4*)(lp + lane * 16);
          const u32 v4 = *(const LAS u32*)(lp + lane * 16 + 16);
          *(LAS u32x4*)(lk + (9 + (lane >> 2)) * 80 + (lane & 3) * 16) = (u32x4){lo_hi(v4, v[3]), v[2], lo_hi(v[2], v[1]), v[0]};
        }
        {
          const u32x2 v = *(const LAS u32x2*)(lp + 512 + lane * 8);
          const u32 v2 = *(const LAS u32*)(lp + 512 + lane * 8 + 8);
          *(LAS u32x4*)(lk + (17 + (lane >> 2)) * 80 + (lane & 3) * 16) = (u32x4){v2, v[1], lo_hi(v[1], v[0]), v[0]};
        }
        {
          const u32x2 v = *(const LAS u32x2*)(lp + 1024 + lane * 8);
          const u32 v2 = *(const LAS u32*)(lp + 1024 + lane * 8 + 8);
          char LAS* o = lk + (33 + (lane >> 1)) * 80 + (lane & 1) * 32;
          *(LAS u32x4*)o = (u32x4){v[1], lo_hi(v[1], v[0]), v[0], v[0]};
          *(LAS u32x4*)(o + 16) = (u32x4){v2, v2, v[1], v[1]};
        }
      }
      if (d == first) { pol = pol_of(wait_ready(a.ctrl, tag, 4 + cx)); wait_all(a.ctrl, CW_UIN, 32, 8); fetch_u(d); }
#pragma unroll
      for (int i = 0; i < 4; ++i) *(LAS u32x4*)(lu + 64 + lane * 16 + i * 1024) = un[i];
      if (nxt != d) fetch_u(nxt);
#pragma unroll
      for (int i = 0; i < 5; ++i) {
        const int q = lane + 64 * i;
        if (i < 4 || lane < 4) {
          const u32x4 x = *(const LAS u32x4*)(lu + q * 16);
          u32x2 y = *(const LAS u32x2*)(lu + q * 16 + 16);
          *(LAS u32x4*)(lw + q * 16) = (u32x4){__builtin_amdgcn_alignbit(x[1], x[0], 16), __builtin_amdgcn_alignbit(x[2], x[1], 16), __builtin_amdgcn_alignbit(x[3], x[2], 16), __builtin_amdgcn_alignbit(y[0], x[3], 16)};
          *(LAS u32x4*)(lw + S_ROW + q * 16) = (u32x4){x[1], x[2], x[3], y[0]};
          *(LAS u32x4*)(lw + 2 * S_ROW + q * 16) = (u32x4){__builtin_amdgcn_alignbit(x[2], x[1], 16), __builtin_amdgcn_alignbit(x[3], x[2], 16), __builtin_amdgcn_alignbit(y[0], x[3], 16), __builtin_amdgcn_alignbit(y[1], y[0], 16)};
        }
      }
      }
      SST(1);
      params();
      fetch_p(nxt + 24 < last ? nxt + 24 : nxt);
      SST(2);
      products();
      if (d < first) continue;
      SST(4);
#pragma unroll
      for (int cg = 0; cg < 2; ++cg)
#pragma unroll
        for (int i4 = 0; i4 < 4; ++i4) {
          LAS u32x2* up = (LAS u32x2*)(lu + 64 + (32 * (32 * cg + b) + 8 * i4 + 4 * h) * 2);
          const u32x2 uv = *up;
          float v[4];
#pragma unroll
          for (int e = 0; e < 4; ++e) v[e] = cg ? y1[4 * i4 + e] : y0[4 * i4 + e];
          const u32 ya = pk2(v[0], v[1]), yb = pk2(v[2], v[3]);
          const u32 sa = pk2(__uint_as_float(uv[0] << 16) * ds, __uint_as_float(uv[0] & 0xffff0000u) * ds);
          const u32 sb = pk2(__uint_as_float(uv[1] << 16) * ds, __uint_as_float(uv[1] & 0xffff0000u) * ds);
          *up = (u32x2){pk2(__uint_as_float(ya << 16) + __uint_as_float(sa << 16), __uint_as_float(ya & 0xffff0000u) + __uint_as_float(sa & 0xffff0000u)),
                        pk2(__uint_as_float(yb << 16) + __uint_as_float(sb << 16), __uint_as_float(yb & 0xffff0000u) + __uint_as_float(sb & 0xffff0000u))};
        }
#pragma unroll
      for (int i = 0; i < 4; ++i) __builtin_amdgcn_raw_buffer_store_b128(*(const LAS u32x4*)(lu + 64 + lane * 16 + i * 1024), rt, lane * 16 + i * 1024, offT(pol, d), SC1);
      SST(5);
    }
    WAIT_VM0();
    if (lane == 0) __hip_atomic_store(a.ctrl + CW_YOUT + (cx * 24 + rank * 3 + w) * 32, 1u, RLX_AGENT);
  }
}
#define BULK_CLOBBERS \
  "v4", "v5", "v6", "v7", "v8", "v9", "v10", "v11", "v12", "v13", "v14", "v15", "v16", "v17", "v18", "v19", "v20", "v21", "v22", "v23", "v24", "v25",\
  "v26", "v27", "v28", "v29", "v30", "v31", "v32", "v33", "v34", "v35", "v36", "v37", "v38", "v39", "v40", "v41", "v42", "v43", "v44", "v45", "v46",\
  "v47", "v48", "v49", "v50", "v51", "v52", "v53", "v54", "v55", "v56", "v57", "v58", "v59", "v60", "v61", "v62", "v63", "v64", "v65", "v66", "v67",\
  "v68", "v69", "v70", "v71", "v72", "v73", "v74", "v75", "v76", "v77", "v78", "v79", "v80", "v81", "v82", "v83", "v84", "v85", "v86", "v87", "v88",\
  "v89", "v90", "v91", "v92", "v93", "v94", "v95", "v96", "v97", "v98", "v99", "v100", "v101", "v102", "v103", "v104", "v105", "v106", "v107", "v108",\
  "v109", "v110", "v111", "v112", "v113", "v114", "v115", "v116", "v117", "v118", "v119", "v120", "v121", "v122", "v123", "v124", "v125", "v126",\
  "v127", "v128", "v129", "v130", "v131", "v132", "v133", "v134", "v135", "v136", "v137", "v138", "v139", "v140", "v141", "v142", "v143", "v144",\
  "v145", "v146", "v147", "v148", "v149", "v150", "v151", "v152", "v153", "v154", "v155", "v156", "v157", "v158", "v159", "v160", "v161", "v162",\
  "v163", "v164", "v165", "v166", "v167", "v168", "v169", "v170", "v171", "v172", "v173", "v174", "v175", "v176", "v177", "v178", "v179", "v180",\
  "v181", "v182", "v183", "v184", "v185", "v186", "v187", "v188", "v189", "v190", "v191", "v192", "v193", "v194", "v195", "v196", "v197", "v198",\
  "v199", "v200", "v201", "v202", "v203", "v204", "v205", "v206", "v207", "v208", "v209", "v210", "v211", "v212", "v213", "v214", "v215", "v216",\
  "v217", "v218", "v219", "v220", "v221", "v222", "v223", "v224", "v225", "v226", "v227", "v228", "v229", "v230", "v231", "v232", "v233", "v234",\
  "v235", "v236", "v237", "v238", "v239", "v240", "v241", "v242", "v243", "v244", "v245", "v246", "v247", "v248", "v249", "v250", "v251", "v252",\
  "v253", "v254", "v255", "a0", "a1", "a2", "a3", "a4", "a5", "a6", "a7", "a8", "a9", "a10", "a11", "a12", "a13", "a14", "a15", "a16", "a17", "a18",\
  "a19", "a20", "a21", "a22", "a23", "a24", "a25", "a26", "a27", "a28", "a29", "a30", "a31", "a32", "a33", "a34", "a35", "a36", "a37", "a38", "a39",\
  "a40", "a41", "a42", "a43", "a44", "a45", "a46", "a47", "a48", "a49", "a50", "a51", "a52", "a53", "a54", "a55", "a56", "a57", "a58", "a59", "a60",\
  "a61", "a62", "a63", "a64", "a65", "a66", "a67", "a68", "a69", "a70", "a71", "a72", "a73", "a74", "a75", "a76", "a77", "a78", "a79", "a80", "a81",\
  "a82", "a83", "a84", "a85", "a86", "a87", "a88", "a89", "a90", "a91", "a92", "a93", "a94", "a95", "a96", "a97", "a98", "a99", "a100", "a101", "a102",\
  "a103", "a104", "a105", "a106", "a107", "a108", "a109", "a110", "a111", "a112", "a113", "a114", "a115", "a116", "a117", "a118", "a119", "a120",\
  "a121", "a122", "a123", "a124", "a125", "a126", "a127", "a128", "a129", "a130", "a131", "a132", "a133", "a134", "a135", "a136", "a137", "a138",\
  "a139", "a140", "a141", "a142", "a143", "a144", "a145", "a146", "a147", "a148", "a149", "a150", "a151", "a152", "a153", "a154", "a155", "a156",\
  "a157", "a158", "a159", "a160", "a161", "a162", "a163", "a164", "a165", "a166", "a167", "a168", "a169", "a170", "a171", "a172", "a173", "a174",\
  "a175", "a176", "a177", "a178", "a179", "a180", "a181", "a182", "a183", "a184", "a185", "a186", "a187", "a188", "a189", "a190", "a191", "a192",\
  "a193", "a194", "a195", "a196", "a197", "a198", "a199", "a200", "a201", "a202", "a203", "a204", "a205", "a206", "a207", "a208", "a209", "a210",\
  "a211", "a212", "a213", "a214", "a215", "a216", "a217", "a218", "a219", "a220", "a221", "a222", "a223", "a224", "a225", "a226", "a227", "a228",\
  "a229", "a230", "a231", "a232", "a233", "a234", "a235", "a236", "a237", "a238", "a239", "a240", "a241", "a242", "a243", "a244", "a245", "a246",\
  "a247", "a248", "a249", "a250", "a251", "a252", "a253", "a254", "a255", "s68", "s69", "s70", "s71", "s72", "s73", "s74", "s75", "s76", "s77", "s78",\
  "s79", "s80", "s81", "s82", "s83", "s84", "s85", "s86", "s87", "s88", "s89", "s90", "s91", "s92", "s93", "s94", "s95", "s96", "vcc", "scc", "memory"
#define GEMM_CLOBBERS \
  "v4", "v5", "v6", "v7", "v8", "v9", "v10", "v11", "v12", "v13", "v14", "v15", "v16", "v17", "v18", "v19", "v20", "v21", "v22", "v23", "v24", "v25",\
  "v26", "v27", "v28", "v29", "v30", "v31", "v32", "v33", "v34", "v35", "v36", "v37", "v38", "v39", "v40", "v41", "v42", "v43", "v44", "v45", "v46",\
  "v47", "v48", "v49", "v50", "v51", "v52", "v53", "v54", "v55", "v56", "v57", "v58", "v59", "v60", "v61", "v62", "v63", "v64", "v65", "v66", "v67",\
  "v68", "v69", "v70", "v71", "v72", "v73", "v74", "v75", "v76", "v77", "v78", "v79", "v80", "v81", "v82", "v83", "v84", "v85", "v86", "v87", "v88",\
  "v89", "v90", "v91", "v92", "v93", "v94", "v95", "v96", "v97", "v98", "v99", "v116", "v117", "v118", "v119", "v120", "v121", "v122", "v123", "v124",\
  "v125", "v126", "v127", "v128", "v129", "v130", "v131", "v132", "v133", "v134", "v135", "v136", "v137", "v138", "v139", "v140", "v141", "v142",\
  "v143", "v144", "v145", "v146", "v147", "v148", "v149", "v150", "v151", "v152", "v153", "v154", "v155", "v156", "v157", "v158", "v159", "v160",\
  "v161", "v162", "v163", "v164", "v165", "v166", "v167", "v168", "v169", "v170", "v171", "v172", "v173", "v174", "v175", "v176", "v177", "v178",\
  "v179", "v180", "v181", "v182", "v183", "v184", "v185", "v186", "v187", "v188", "v189", "v190", "v191", "v192", "v193", "v194", "v195", "v196",\
  "v197", "v198", "v199", "v200", "v201", "v202", "v203", "v204", "v205", "v206", "v207", "v208", "v209", "v210", "v211", "v212", "v213", "v214",\
  "v215", "v216", "v217", "v218", "v219", "v220", "v221", "v222", "v223", "v224", "v225", "v226", "v227", "v228", "v229", "v230", "v231", "v232",\
  "v233", "v234", "v235", "v236", "v237", "v238", "v239", "v240", "v241", "v242", "v243", "v244", "v245", "v246", "v247", "v248", "v249", "v250",\
  "v251", "v252", "v253", "v254", "v255", "a0", "a1", "a2", "a3", "a4", "a5", "a6", "a7", "a8", "a9", "a10", "a11", "a12", "a13", "a14", "a15", "a16",\
  "a17", "a18", "a19", "a20", "a21", "a22", "a23", "a24", "a25", "a26", "a27", "a28", "a29", "a30", "a31", "a32", "a33", "a34", "a35", "a36", "a37",\
  "a38", "a39", "a40", "a41", "a42", "a43", "a44", "a45", "a46", "a47", "a48", "a49", "a50", "a51", "a52", "a53", "a54", "a55", "a56", "a57", "a58",\
  "a59", "a60", "a61", "a62", "a63", "a64", "a65", "a66", "a67", "a68", "a69", "a70", "a71", "a72", "a73", "a74", "a75", "a76", "a77", "a78", "a79",\
  "a80", "a81", "a82", "a83", "a84", "a85", "a86", "a87", "a88", "a89", "a90", "a91", "a92", "a93", "a94", "a95", "a96", "a97", "a98", "a99", "a100",\
  "a101", "a102", "a103", "a104", "a105", "a106", "a107", "a108", "a109", "a110", "a111", "a112", "a113", "a114", "a115", "a116", "a117", "a118",\
  "a119", "a120", "a121", "a122", "a123", "a124", "a125", "a126", "a127", "a128", "a129", "a130", "a131", "a132", "a133", "a134", "a135", "a136",\
  "a137", "a138", "a139", "a140", "a141", "a142", "a143", "a144", "a145", "a146", "a147", "a148", "a149", "a150", "a151", "a152", "a153", "a154",\
  "a155", "a156", "a157", "a158", "a159", "a160", "a161", "a162", "a163", "a164", "a165", "a166", "a167", "a168", "a169", "a170", "a171", "a172",\
  "a173", "a174", "a175", "a176", "a177", "a178", "a179", "a180", "a181", "a182", "a183", "a184", "a185", "a186", "a187", "a188", "a189", "a190",\
  "a191", "a192", "a193", "a194", "a195", "a196", "a197", "a198", "a199", "a200", "a201", "a202", "a203", "a204", "a205", "a206", "a207", "a208",\
  "a209", "a210", "a211", "a212", "a213", "a214", "a215", "a216", "a217", "a218", "a219", "a220", "a221", "a222", "a223", "a224", "a225", "a226",\
  "a227", "a228", "a229", "a230", "a231", "a232", "a233", "a234", "a235", "a236", "a237", "a238", "a239", "a240", "a241", "a242", "a243", "a244",\
  "a245", "a246", "a247", "a248", "a249", "a250", "a251", "a252", "a253", "a254", "a255", "s68", "s69", "s70", "s71", "s72", "s73", "s74", "s75", "s76",\
  "s77", "s78", "s79", "s80", "s81", "s82", "s83", "s84", "s85", "s86", "s87", "s88", "s89", "s90", "s91", "s92", "s93", "s94", "s95", "s96", "vcc", "scc", "memory"
__device__ __forceinline__ void role_gemm(const Args& a, char LAS* smem, const int gi, const u64 tag, const int grp) {
  const int w = __builtin_amdgcn_readfirstlane(threadIdx.x >> 6);
  u32 p13;
  asm volatile(GEMM_ASM
               : "=&s"(p13) : "s"(a.Wq + 64 * gi), "s"(a.Wk + 64 * (gi ^ 1)), "s"(a.Wv + 64 * (gi ^ 2)), "s"(a.x), "s"(a.ex + WS_Q_OFF), "s"(a.ctrl), "s"(gi), "s"(w), "s"((u32)(uintptr_t)smem), "s"(tag), "s"(grp)
               : GEMM_CLOBBERS);
  ssm_out(a, smem, gi & 31, gi >> 5, pol_of(p13 << 13));
}
__device__ __forceinline__ void role_xo(const Args& a, char LAS* smem, const int xi, const u64 tag, const int grp) {
  const int w = __builtin_amdgcn_readfirstlane(threadIdx.x >> 6);
  sleepn(XO_SLEEP);
  const u32 pol = pol_of(wait_ready(a.ctrl, tag, grp));
  ssm_in(a, smem, xi, pol);
  sleepn(XO_SLEEP2);
  asm volatile(XO_ASM
               :: "s"(a.Wo + 64 * (xi >> 1)), "s"(a.ex + WS_OX_OFF), "s"(a.out), "s"(a.ctrl), "s"(xi), "s"(w), "s"((u32)(uintptr_t)smem), "s"(pol)
               : BULK_CLOBBERS);
}
__device__ __forceinline__ void role_xa(const Args& a, char LAS* smem, const int head, const u64 tag, const int grp) {
  const int lane = threadIdx.x & 63;
  if (head == 0) make_ready(a.ctrl, tag);
  sleepn(XPRE);
  {
    u32 acc = 0;
    for (int i = 0; i < 16; ++i) acc |= *(const u32*)((const char*)a.x + ((size_t)(i * 32 + head) * 64 + lane) * 128);
    const u16* pp[6] = {a.A_re, a.A_im, a.B_re, a.B_im, a.C_re, a.C_im};
#pragma unroll
    for (int i = 0; i < 6; ++i) acc |= *(const u32*)((const char*)pp[i] + ((size_t)head * 64 + lane) * 128);
    asm volatile("" :: "v"(acc));
  }
  const u32 code = wait_ready(a.ctrl, tag, grp), pol = pol_of(code);
  asm volatile(XA_ASM
               :: "s"(a.ex), "s"(a.ex + WS_Q_OFF), "s"(a.ctrl), "s"(head), "s"(pol), "s"((u32)(uintptr_t)smem + 3 * S_WSZ), "s"(code)
               : BULK_CLOBBERS);
}
__global__ void __launch_bounds__(256, 1) mega_kernel(Args a) {
  __shared__ __attribute__((aligned(16))) char smem_raw[163840];
  char LAS* smem = (char LAS*)smem_raw;
  const int tid = threadIdx.x, b = blockIdx.x;
  const u32 xcc = (u32)__builtin_amdgcn_s_getreg(20 | (3 << 11)) & 7u;
  if (tid < 64) {
    u64 t = __hip_atomic_load((u64*)(a.ctrl + CW_READY + (tid & 7) * 32), RLX_AGENT);
#pragma unroll
    for (int k = 1; k < 8; k *= 2) { const u64 o = __shfl_xor(t, k); t = o > t ? o : t; }
    t += 1;
    if (tid == 0) {
      int role = -1;
      for (int k = 0; k < 256 && role < 0; ++k) {
        const int r = (int)(xcc * 32 + (b >> 3) + k) & 255;
        if (__hip_atomic_exchange((u64*)(a.ctrl + CW_CLAIM + r * 32), t, RLX_AGENT) != t) role = r;
      }
      *(LAS u64*)smem = t;
      *(LAS int*)(smem + 8) = role;
    }
  } else if (tid < 128) {
    const int pg = b >> 3;
    u32 b0 = 0, b1 = 0;
    if (xcc < 4 && pg < EX_PAGES) {
      const __amdgpu_buffer_rsrc_t rs = __builtin_amdgcn_make_buffer_rsrc(a.ex, 0, (int)EX_BYTES, 0x00020000);
      const u32 bit = (pg & 4) ? 16384u : 4096u, flip = ((pg & 4) ? xcc : xcc >> 1) & 1u;
      const u32 o0 = ((u32)pg << 21) + (b & 7) * 32768 + ((pg >> 1) & 1) * 8192 + (pg & 15) * 256 + flip * bit + (tid < 72 ? (tid - 64) * 16 : 0x80000000u), o1 = o0 ^ bit;
      b0 = b1 = 4095u;
#pragma unroll 1
      for (int rep = 0; rep < NPROBE; ++rep) {
        u64 t0, t1, t2;
        asm volatile("s_memtime %0\n\tbuffer_store_dwordx4 %3, %4, %6, 0 offen sc1\n\ts_waitcnt vmcnt(0)\n\ts_memtime %1\n\tbuffer_store_dwordx4 %3, %5, %6, 0 offen sc1\n\t"
                     "s_waitcnt vmcnt(0)\n\ts_memtime %2\n\ts_waitcnt lgkmcnt(0)"
                     : "=&s"(t0), "=&s"(t1), "=&s"(t2) : "v"((u32x4){SENT, SENT, SENT, SENT}), "v"(o0), "v"(o1), "s"(rs) : "memory");
        b0 = min(b0, (u32)(t1 - t0)); b1 = min(b1, (u32)(t2 - t1));
      }
    }
    if (tid == 64) *(LAS u32*)(smem + 12) = b0 | (b1 << 16);
  }
  __syncthreads();
  const u32x4 st = *(LAS u32x4*)smem;
  const int role = __builtin_amdgcn_readfirstlane(st[2]);
  const u64 tag = (u64)(u32)__builtin_amdgcn_readfirstlane(st[0]) | ((u64)(u32)__builtin_amdgcn_readfirstlane(st[1]) << 32);
  if (tid >= 64 && tid < 72)
    __builtin_amdgcn_raw_buffer_store_b128((u32x4){st[0], st[1], st[3], 0u}, __builtin_amdgcn_make_buffer_rsrc(a.ctrl, 0, CTRL_BYTES, 0x00020000), (tid - 64) * 16, (CW_MEAS + b * 32) * 4, SC1);
  __syncthreads();
  if (role < 0) return;
#define DONE(c, w)
  const int cx = (role - 128) >> 5, t = role & 31;
  if (role < 128) role_rec(args(), smem, role, tag);
  else if (t < 8) { role_xo(args(), smem, ((cx >> 1) * 8 + t) * 2 + (cx & 1), tag, 4 + cx); DONE(tid == 0, 6200 + role); }
  else if (t >= 16) { role_gemm(args(), smem, (cx >> 1) * 32 + (t - 16) * 2 + (cx & 1), tag, 4 + cx); DONE(tid == 0, 6200 + role); }
  else if (__builtin_amdgcn_readfirstlane(tid >> 6) == 3) { role_xa(args(), smem, cx * 8 + t - 8, tag, 4 + cx); DONE(tid == 192, 7320 + cx * 8 + t - 8); }
  else { role_ssm(args(), smem, cx, t - 8, tag); DONE(tid == 0, 6200 + role); }
}

extern "C" void kernel_launch(void* const* d_in, const int* in_sizes, int n_in, void* d_out, int out_size,
                              void* d_ws, size_t ws_size, hipStream_t stream) {
  Args a;
  a.x = (const u16*)d_in[0]; a.mem0 = (const u16*)d_in[1];
  a.Wq = (const u16*)d_in[2]; a.Wk = (const u16*)d_in[3]; a.Wv = (const u16*)d_in[4]; a.Wo = (const u16*)d_in[5];
  a.u = (const u16*)d_in[7]; a.A_re = (const u16*)d_in[8]; a.A_im = (const u16*)d_in[9];
  a.B_re = (const u16*)d_in[10]; a.B_im = (const u16*)d_in[11]; a.C_re = (const u16*)d_in[12]; a.C_im = (const u16*)d_in[13];
  a.log_dt = (const u16*)d_in[14]; a.Dskip = (const u16*)d_in[15];
  a.out = (u16*)d_out;
  char* ws = (char*)d_ws;
  a.ctrl = (u32*)ws;
  a.ex = (char*)(((uintptr_t)ws + CTRL_BYTES + (2u << 20) - 1) & ~(uintptr_t)((2u << 20) - 1));
  mega_kernel<<<256, 256, 0, stream>>>(a);
}
```

```cpp
#define X 1
#include <hip/hip_runtime.h>
#include <hip/hip_bf16.h>
#include <stdint.h>

#define S_LEN 2048
#define DM 1024
#define NH 32
#define DH 128
#define HD 4096
#define NSEG 64
#define SSM_L 2048
#define SSM_D 1024
#define SSM_N 128
#define OUTLD 2048

typedef unsigned short u16;
typedef unsigned int u32;
typedef unsigned long long u64;
typedef __attribute__((ext_vector_type(8))) short bf16x8;
typedef __attribute__((ext_vector_type(4))) short bf16x4;
typedef __attribute__((ext_vector_type(4))) float f32x4;
typedef __attribute__((ext_vector_type(16))) float f32x16;
typedef __attribute__((ext_vector_type(4))) unsigned int u32x4;
typedef __attribute__((ext_vector_type(2))) unsigned int u32x2;
typedef __attribute__((ext_vector_type(2))) float f32x2;
typedef __attribute__((ext_vector_type(2))) __bf16 bf2_t;
typedef short v4i16_t __attribute__((ext_vector_type(4)));
#define LAS __attribute__((address_space(3)))

#define SENT 0xFFFFFFFFu
#define RLX_AGENT __ATOMIC_RELAXED, __HIP_MEMORY_SCOPE_AGENT
#define SC1 16

__device__ __forceinline__ float bf2f(u16 v) { return __uint_as_float(((u32)v) << 16); }
__device__ __forceinline__ u32 pk2(float a, float b) {
  f32x2 f = {a, b};
  bf2_t h = __builtin_convertvector(f, bf2_t);
  return __builtin_bit_cast(u32, h);
}
__device__ __forceinline__ float rbf(float f) { return __uint_as_float(pk2(f, 0.f) << 16); }
__device__ __forceinline__ u32 nosent(u32 v) { return v < 0xFFFFFFFEu ? v : 0xFFFFFFFEu; }
__device__ __forceinline__ bool okv(u32x4 v) { return v[0] != SENT && v[1] != SENT && v[2] != SENT && v[3] != SENT; }
__device__ __forceinline__ bf16x8 asb(u32x4 v) { return __builtin_bit_cast(bf16x8, v); }
__device__ __forceinline__ void sleepn(int n) { for (int k = 0; k < n; ++k) __builtin_amdgcn_s_sleep(1); }
#define WAIT_VM0() asm volatile("s_waitcnt vmcnt(0)" ::: "memory")
#define BAR() do { asm volatile("s_waitcnt lgkmcnt(0)" ::: "memory"); __builtin_amdgcn_s_barrier(); asm volatile("" ::: "memory"); } while (0)

__device__ __forceinline__ float xmax16(float x) {
  auto r = __builtin_amdgcn_permlane16_swap(__float_as_uint(x), __float_as_uint(x), false, false);
  return fmaxf(__uint_as_float(r[0]), __uint_as_float(r[1]));
}
__device__ __forceinline__ float xmax32(float x) {
  auto r = __builtin_amdgcn_permlane32_swap(__float_as_uint(x), __float_as_uint(x), false, false);
  return fmaxf(__uint_as_float(r[0]), __uint_as_float(r[1]));
}
__device__ __forceinline__ float xadd16(float x) {
  auto r = __builtin_amdgcn_permlane16_swap(__float_as_uint(x), __float_as_uint(x), false, false);
  return __uint_as_float(r[0]) + __uint_as_float(r[1]);
}
__device__ __forceinline__ float xadd32(float x) {
  auto r = __builtin_amdgcn_permlane32_swap(__float_as_uint(x), __float_as_uint(x), false, false);
  return __uint_as_float(r[0]) + __uint_as_float(r[1]);
}
__device__ __forceinline__ u32 ror8(u32 x) { return (u32)__builtin_amdgcn_update_dpp(0, (int)x, 0x128, 0xf, 0xf, true); }
template <int N> __device__ __forceinline__ float rorf(float x) {
  return __uint_as_float((u32)__builtin_amdgcn_update_dpp(0, (int)__float_as_uint(x), 0x120 + N, 0xf, 0xf, true));
}
__device__ __forceinline__ float ror8f(float x) { return __uint_as_float(ror8(__float_as_uint(x))); }

#define MFMA32(a, b, c) __builtin_amdgcn_mfma_f32_16x16x32_bf16(a, b, c, 0, 0, 0)
#define MFMA16(a, b, c) __builtin_amdgcn_mfma_f32_16x16x16bf16_1k(a, b, c, 0, 0, 0)
#define MFMA_AV0(acc, wa, xb) asm volatile("v_mfma_f32_16x16x32_bf16 %0, %1, %2, 0" : "=&v"(acc) : "a"(wa), "v"(xb))
#define MFMA_AV(acc, wa, xb) asm volatile("v_mfma_f32_16x16x32_bf16 %0, %1, %2, %0" : "+v"(acc) : "a"(wa), "v"(xb))
#define MFMA_VA0(acc, xa, wb) asm volatile("v_mfma_f32_16x16x32_bf16 %0, %1, %2, 0" : "=&v"(acc) : "v"(xa), "a"(wb))
#define MFMA_VA(acc, xa, wb) asm volatile("v_mfma_f32_16x16x32_bf16 %0, %1, %2, %0" : "+v"(acc) : "v"(xa), "a"(wb))
#define MFMA_VV0(acc, xa, xb) asm volatile("v_mfma_f32_16x16x32_bf16 %0, %1, %2, 0" : "=&v"(acc) : "v"(xa), "v"(xb))
#define MFMA16_VV(acc, xa, xb) asm volatile("v_mfma_f32_16x16x16_bf16 %0, %1, %2, %0" : "+v"(acc) : "v"(xa), "v"(xb))
#define MFMA_ACC(acc, xa, xb) asm volatile("v_mfma_f32_16x16x32_bf16 %0, %1, %2, %0" : "+a"(acc) : "v"(xa), "v"(xb))
#define NOP_IN() asm volatile("s_nop 1" ::: "memory")
#define PIN(x) asm volatile("" : "+v"(x))
#define PINA(x) asm volatile("" : "+a"(x))

__device__ __forceinline__ bf16x4 trread(const char LAS* p) {
  return __builtin_amdgcn_ds_read_tr16_b64_v4i16((LAS v4i16_t*)p);
}
__device__ __forceinline__ bf16x8 cat8(bf16x4 a, bf16x4 b) {
  return __builtin_shufflevector(a, b, 0, 1, 2, 3, 4, 5, 6, 7);
}

#define CW_READY 0
#define CW_CLAIM 12288
#define CW_MEAS 20480
#define CW_INIT 288
#define CW_DEAD 320
#define CW_PROG 384
#define CW_XPROG 1408
#define CW_GPROG 3456
#define CW_UIN 2432
#define CW_YOUT 8192
#define CTRL_BYTES 114688
#define EX_PAGES 20
#define EX_BYTES ((u64)EX_PAGES << 21)
#define EXP_C 0
#define EXP_A 1
#define EXP_B 2
#define EXP_DBG 19
#define QKV_SZ ((u64)S_LEN * HD * 4)
#define WS_Q_OFF (EX_BYTES)
#define WS_K_OFF (WS_Q_OFF + QKV_SZ)
#define WS_VT_OFF (WS_K_OFF + QKV_SZ)
#define WS_OX_OFF (WS_VT_OFF + QKV_SZ)
#define WS_UT_OFF (WS_OX_OFF + QKV_SZ)
#define EX_BYTES32 41943040
#define QKV_SZ32 33554432
#define WS_K_OFF32 75497472
static_assert(EX_BYTES32 == EX_BYTES && QKV_SZ32 == QKV_SZ && WS_K_OFF32 == WS_K_OFF, "layout");

#define SPIN_CAP 300000
#define NPROBE 3
#define F_B 1460
#define F_C 3340
#define F_P 4220
#ifndef M_C
#define M_C 150
#endif
#ifndef M_A
#define M_A 150
#endif
#define T_Q 1300
#define T_TOL 100
#define D_K 15
#define DECAY 0.996
#define RI0 1.07
#define XPRE 300
#define O_N 0
#define T_SNAP 300
#define E_0 240
#define E_N 7
#define E_UP 64
#define E_DOWN 2
#define XO_SLEEP2 0
#define OAUX 16
#define T_LATE 0
#define O_A 110
#define O_B 130
#define O_C 40
#define GAP_B 4
#define GAP_V 0
#define GAP_0 2
#define XO_SLEEP 230
#define D_PF (-200)
#define STR_(x) #x
#define STR(x) STR_(x)

struct Args {
  const u16 *x, *mem0, *Wq, *Wk, *Wv, *Wo;
  const u16 *u, *A_re, *A_im, *B_re, *B_im, *C_re, *C_im, *log_dt, *Dskip;
  u16* out;
  u32* ctrl;
  char* ex;
};

typedef const __attribute__((address_space(4))) Args* ArgP;
__device__ __forceinline__ Args args() {
  ArgP p = (ArgP)__builtin_amdgcn_kernarg_segment_ptr();
  asm volatile("" : "+s"(p));
  Args a;
  a.x = p->x; a.mem0 = p->mem0; a.Wq = p->Wq; a.Wk = p->Wk; a.Wv = p->Wv; a.Wo = p->Wo; a.u = p->u; a.A_re = p->A_re; a.A_im = p->A_im;
  a.B_re = p->B_re; a.B_im = p->B_im; a.C_re = p->C_re; a.C_im = p->C_im; a.log_dt = p->log_dt; a.Dskip = p->Dskip; a.out = p->out; a.ctrl = p->ctrl; a.ex = p->ex;
  return a;
}
__device__ __forceinline__ bool is_dead(u32* ctrl) { return __hip_atomic_load(ctrl + CW_DEAD, RLX_AGENT) != 0; }
__device__ __forceinline__ void set_dead(u32* ctrl) { __hip_atomic_store(ctrl + CW_DEAD, 1u, RLX_AGENT); }
__device__ __forceinline__ void wait_ge(u32* ctrl, int word, u32 want, int slp) {
  for (int spins = 0;; ++spins) {
    asm volatile("" ::: "memory");
    u32 v = __builtin_amdgcn_readfirstlane(__hip_atomic_load(ctrl + word, RLX_AGENT));
    if (v >= want) break;
    if (spins > SPIN_CAP) { set_dead(ctrl); break; }
    if ((spins & 63) == 63 && __builtin_amdgcn_readfirstlane((int)is_dead(ctrl))) break;
    sleepn(slp);
  }
}
__device__ __forceinline__ u32 wait_ready(u32* ctrl, u64 tag, int grp) {
  const __amdgpu_buffer_rsrc_t rc = __builtin_amdgcn_make_buffer_rsrc(ctrl, 0, CTRL_BYTES, 0x00020000);
  u32 code = 0;
  for (int spins = 0; spins < SPIN_CAP; ++spins) {
    asm volatile("" ::: "memory");
    const u32x4 v = __builtin_amdgcn_raw_buffer_load_b128(rc, 0, grp * 128, SC1);
    code = __builtin_amdgcn_readfirstlane(v[2] ^ (u32)tag);
    const u32 bad = (v[0] ^ (u32)tag) | (v[1] ^ (u32)(tag >> 32)) | (v[3] ^ (u32)(tag >> 32) ^ code);
    if (__builtin_amdgcn_readfirstlane(bad) == 0) break;
    sleepn(16);
  }
  asm volatile("" ::: "memory");
  return code & 0x7000u;
}
__device__ __forceinline__ u32 pol_of(u32 code) { return (code & 0x2000u) ? 0x55555555u : 0xAAAAAAAAu; }
__device__ __forceinline__ void make_ready(u32* ctrl, u64 tag) {
  const int lane = threadIdx.x & 63;
  const __amdgpu_buffer_rsrc_t rc = __builtin_amdgcn_make_buffer_rsrc(ctrl, 0, CTRL_BYTES, 0x00020000);
#pragma unroll
  for (int i = 0; i < 5; ++i) __builtin_amdgcn_raw_buffer_store_b32(0u, rc, (i < 3 ? min(8 + i * 64 + lane, 172) : 64 + i * 64 + lane) * 128, 0, SC1);
  u32 tm[3];
  for (int spins = 0; spins < SPIN_CAP; ++spins) {
    asm volatile("" ::: "memory");
    u32 bad = 0;
#pragma unroll
    for (int i = 0; i < 4; ++i) {
      const u32x4 v = __builtin_amdgcn_raw_buffer_load_b128(rc, (i * 64 + lane) * 128, CW_MEAS * 4, SC1);
      bad |= (v[0] ^ (u32)tag) | (v[1] ^ (u32)(tag >> 32));
      if (i < 3) tm[i] = v[2];
    }
    if (__builtin_amdgcn_ballot_w64(bad != 0) == 0) break;
    sleepn(4);
  }
  WAIT_VM0();
  const int k = lane >> 3, same = ((k >> 1) ^ k ^ 1) & 1;
  int s = 0, d = 0;
#pragma unroll
  for (int i = 0; i < 3; ++i) {
    const int x0 = tm[i] & 0xfff, x1 = (tm[i] >> 16) & 0xfff;
    s += same ? x0 + x1 : -(x0 + x1);
    d += (x1 < x0) - (x1 > x0);
  }
#pragma unroll
  for (int m = 1; m < 64; m *= 2) s += __shfl_xor(s, m);
  if (same != (s < 0 ? 1 : 0)) d = 0;
#pragma unroll
  for (int m = 1; m < 32; m *= 2) d += __shfl_xor(d, m);
  const u32 code = (__shfl(d, 0) > 0 ? 0x1000u : 0u) | (__shfl(d, 32) > 0 ? 0x4000u : 0u) | (s < 0 ? 0u : 0x2000u);
  __builtin_amdgcn_raw_buffer_store_b128((u32x4){(u32)tag, (u32)(tag >> 32), (u32)tag ^ code, (u32)(tag >> 32) ^ code}, rc, lane * 16, 0, SC1);
}
__device__ __forceinline__ u32 max4(u32x4 v) { return max(max(v[0], v[1]), max(v[2], v[3])); }
#define POLL_FAIL(spins) ((++(spins) > SPIN_CAP) ? (set_dead(ctrl), true) : (((spins) & 255) == 255 && __builtin_amdgcn_readfirstlane((int)is_dead(ctrl))))
#define REC_ASM \
  "s_mov_b64 exec, -1\n" \
  "v_mbcnt_lo_u32_b32 v24, -1, 0\n" \
  "v_mbcnt_hi_u32_b32 v24, -1, v24\n" \
  "s_lshr_b32 s70, %6, 2\n" \
  "s_and_b32 s71, %6, 3\n" \
  "v_lshlrev_b32 v1, 4, v24\n" \
  "v_and_b32 v27, 7, v24\n" \
  "v_lshrrev_b32 v28, 3, v24\n" \
  "v_lshlrev_b32 v34, 11, v28\n" \
  "v_lshl_add_u32 v34, v27, 4, v34\n" \
  "v_lshlrev_b32 v32, 13, v28\n" \
  "v_lshl_add_u32 v32, v27, 4, v32\n" \
  "v_and_b32 v27, 32, v24\n" \
  "v_xor_b32 v32, v32, v27\n" \
  "v_xor_b32 v33, 64, v32\n" \
  "s_mul_i32 s88, %7, 40960\n" \
  "s_add_u32 s88, s88, 0\n" \
  "s_lshl_b32 s60, %7, 21\n" \
  "s_mov_b64 s[72:73], %0\n" \
  "s_and_b32 s73, s73, 0xffff\n" \
  "s_mov_b32 s74, 8388608\n" \
  "s_mov_b32 s75, 0x00020000\n" \
  "s_mov_b64 s[76:77], %1\n" \
  "s_and_b32 s77, s77, 0xffff\n" \
  "s_mov_b32 s78, 8388608\n" \
  "s_mov_b32 s79, 0x00020000\n" \
  "s_mov_b64 s[80:81], %2\n" \
  "s_and_b32 s81, s81, 0xffff\n" \
  "s_mov_b32 s82, 8388608\n" \
  "s_mov_b32 s83, 0x00020000\n" \
  "s_mov_b64 s[84:85], %3\n" \
  "s_and_b32 s85, s85, 0xffff\n" \
  "s_mov_b32 s86, 262144\n" \
  "s_mov_b32 s87, 0x00020000\n" \
  "s_mov_b64 s[90:91], %9\n" \
  "v_and_b32 v27, 1, v24\n" \
  "v_lshlrev_b32 v27, 11, v27\n" \
  "v_and_b32 v28, 4, v24\n" \
  "v_lshl_add_u32 v27, v28, 10, v27\n" \
  "v_and_b32 v28, 2, v24\n" \
  "v_lshl_add_u32 v27, v28, 3, v27\n" \
  "global_load_dwordx4 v[64:67], v27, s[90:91]\n" \
  "s_lshr_b32 s59, %6, 1\n" \
  "s_add_u32 s59, s59, %7\n" \
  "s_add_u32 s59, s59, 0\n" \
  "s_and_b32 s59, s59, 3\n" \
  "s_lshl_b32 s59, s59, 19\n" \
  "s_lshl_b32 s60, %7, 21\n" \
  "s_add_u32 s59, s59, s60\n" \
  "s_add_u32 s59, s59, 0\n" \
  "s_add_u32 m0, s88, 0\n" \
  "s_nop 0\n" \
  "buffer_load_dwordx4 v32, s[72:75], s59 offen lds\n" \
  "s_add_u32 m0, s88, 4096\n" \
  "s_nop 0\n" \
  "buffer_load_dwordx4 v32, s[76:79], s59 offen lds\n" \
  "s_add_u32 s59, s59, 65536\n" \
  "s_add_u32 m0, s88, 1024\n" \
  "s_nop 0\n" \
  "buffer_load_dwordx4 v33, s[72:75], s59 offen lds\n" \
  "s_add_u32 m0, s88, 5120\n" \
  "s_nop 0\n" \
  "buffer_load_dwordx4 v33, s[76:79], s59 offen lds\n" \
  "s_add_u32 s59, s59, 65536\n" \
  "s_add_u32 m0, s88, 2048\n" \
  "s_nop 0\n" \
  "buffer_load_dwordx4 v32, s[72:75], s59 offen lds\n" \
  "s_add_u32 m0, s88, 6144\n" \
  "s_nop 0\n" \
  "buffer_load_dwordx4 v32, s[76:79], s59 offen lds\n" \
  "s_add_u32 s59, s59, 65536\n" \
  "s_add_u32 m0, s88, 3072\n" \
  "s_nop 0\n" \
  "buffer_load_dwordx4 v33, s[72:75], s59 offen lds\n" \
  "s_add_u32 m0, s88, 7168\n" \
  "s_nop 0\n" \
  "buffer_load_dwordx4 v33, s[76:79], s59 offen lds\n" \
  "s_lshr_b32 s59, %6, 1\n" \
  "s_add_u32 s59, s59, %7\n" \
  "s_add_u32 s59, s59, 0\n" \
  "s_and_b32 s59, s59, 3\n" \
  "s_lshl_b32 s59, s59, 19\n" \
  "s_lshl_b32 s60, %7, 21\n" \
  "s_add_u32 s59, s59, s60\n" \
  "s_add_u32 s59, s59, 262144\n" \
  "s_add_u32 m0, s88, 8192\n" \
  "s_nop 0\n" \
  "buffer_load_dwordx4 v32, s[72:75], s59 offen lds\n" \
  "s_add_u32 m0, s88, 12288\n" \
  "s_nop 0\n" \
  "buffer_load_dwordx4 v32, s[76:79], s59 offen lds\n" \
  "s_add_u32 s59, s59, 65536\n" \
  "s_add_u32 m0, s88, 9216\n" \
  "s_nop 0\n" \
  "buffer_load_dwordx4 v33, s[72:75], s59 offen lds\n" \
  "s_add_u32 m0, s88, 13312\n" \
  "s_nop 0\n" \
  "buffer_load_dwordx4 v33, s[76:79], s59 offen lds\n" \
  "s_add_u32 s59, s59, 65536\n" \
  "s_add_u32 m0, s88, 10240\n" \
  "s_nop 0\n" \
  "buffer_load_dwordx4 v32, s[72:75], s59 offen lds\n" \
  "s_add_u32 m0, s88, 14336\n" \
  "s_nop 0\n" \
  "buffer_load_dwordx4 v32, s[76:79], s59 offen lds\n" \
  "s_add_u32 s59, s59, 65536\n" \
  "s_add_u32 m0, s88, 11264\n" \
  "s_nop 0\n" \
  "buffer_load_dwordx4 v33, s[72:75], s59 offen lds\n" \
  "s_add_u32 m0, s88, 15360\n" \
  "s_nop 0\n" \
  "buffer_load_dwordx4 v33, s[76:79], s59 offen lds\n" \
  "s_lshr_b32 s59, %6, 1\n" \
  "s_add_u32 s59, s59, %7\n" \
  "s_add_u32 s59, s59, 1\n" \
  "s_and_b32 s59, s59, 3\n" \
  "s_lshl_b32 s59, s59, 19\n" \
  "s_lshl_b32 s60, %7, 21\n" \
  "s_add_u32 s59, s59, s60\n" \
  "s_add_u32 s59, s59, 0\n" \
  "s_add_u32 m0, s88, 16384\n" \
  "s_nop 0\n" \
  "buffer_load_dwordx4 v32, s[72:75], s59 offen lds\n" \
  "s_add_u32 m0, s88, 20480\n" \
  "s_nop 0\n" \
  "buffer_load_dwordx4 v32, s[76:79], s59 offen lds\n" \
  "s_add_u32 s59, s59, 65536\n" \
  "s_add_u32 m0, s88, 17408\n" \
  "s_nop 0\n" \
  "buffer_load_dwordx4 v33, s[72:75], s59 offen lds\n" \
  "s_add_u32 m0, s88, 21504\n" \
  "s_nop 0\n" \
  "buffer_load_dwordx4 v33, s[76:79], s59 offen lds\n" \
  "s_add_u32 s59, s59, 65536\n" \
  "s_add_u32 m0, s88, 18432\n" \
  "s_nop 0\n" \
  "buffer_load_dwordx4 v32, s[72:75], s59 offen lds\n" \
  "s_add_u32 m0, s88, 22528\n" \
  "s_nop 0\n" \
  "buffer_load_dwordx4 v32, s[76:79], s59 offen lds\n" \
  "s_add_u32 s59, s59, 65536\n" \
  "s_add_u32 m0, s88, 19456\n" \
  "s_nop 0\n" \
  "buffer_load_dwordx4 v33, s[72:75], s59 offen lds\n" \
  "s_add_u32 m0, s88, 23552\n" \
  "s_nop 0\n" \
  "buffer_load_dwordx4 v33, s[76:79], s59 offen lds\n" \
  "v_and_b32 v36, 15, v24\n" \
  "v_lshrrev_b32 v37, 4, v24\n" \
  "v_lshrrev_b32 v38, 2, v36\n" \
  "v_and_b32 v39, 3, v24\n" \
  "v_lshrrev_b32 v46, 1, v37\n" \
  "v_lshlrev_b32 v46, 4, v46\n" \
  "v_and_b32 v27, 1, v37\n" \
  "v_lshl_add_u32 v46, v27, 2, v46\n" \
  "v_and_b32 v28, 1, v38\n" \
  "v_add_u32 v46, v46, v28\n" \
  "v_lshrrev_b32 v28, 1, v38\n" \
  "v_lshl_add_u32 v46, v28, 3, v46\n" \
  "v_lshl_add_u32 v27, v28, 1, v27\n" \
  "v_lshlrev_b32 v27, 5, v27\n" \
  "s_and_b32 s59, s71, 1\n" \
  "s_lshl_b32 s59, s59, 6\n" \
  "v_xor_b32 v27, s59, v27\n" \
  "v_lshl_add_u32 v27, v46, 7, v27\n" \
  "v_lshl_add_u32 v28, v39, 3, v27\n" \
  "v_add_u32 v42, s88, v28\n" \
  "v_xor_b32 v28, 32, v28\n" \
  "v_add_u32 v43, s88, v28\n" \
  "v_and_b32 v28, 1, v39\n" \
  "v_lshl_add_u32 v27, v28, 3, v27\n" \
  "v_lshrrev_b32 v28, 1, v39\n" \
  "v_lshl_add_u32 v27, v28, 12, v27\n" \
  "s_add_u32 s59, %7, 0\n" \
  "s_and_b32 s59, s59, 3\n" \
  "s_lshl_b32 s59, s59, 4\n" \
  "v_xor_b32 v28, s59, v27\n" \
  "v_add_u32 v52, s88, v28\n" \
  "s_add_u32 s59, %7, 1\n" \
  "s_and_b32 s59, s59, 3\n" \
  "s_lshl_b32 s59, s59, 4\n" \
  "v_xor_b32 v28, s59, v27\n" \
  "v_add_u32 v53, s88, v28\n" \
  "s_add_u32 s59, %7, 2\n" \
  "s_and_b32 s59, s59, 3\n" \
  "s_lshl_b32 s59, s59, 4\n" \
  "v_xor_b32 v28, s59, v27\n" \
  "v_add_u32 v54, s88, v28\n" \
  "s_add_u32 s59, %7, 3\n" \
  "s_and_b32 s59, s59, 3\n" \
  "s_lshl_b32 s59, s59, 4\n" \
  "v_xor_b32 v28, s59, v27\n" \
  "v_add_u32 v55, s88, v28\n" \
  "v_lshlrev_b32 v49, 7, v46\n" \
  "v_lshl_add_u32 v49, v39, 5, v49\n" \
  "v_add_u32 v49, s88, v49\n" \
  "s_mov_b64 s[44:45], %5\n" \
  "s_and_b32 s45, s45, 0xffff\n" \
  "s_mov_b32 s46, " STR(CTRL_BYTES) "\n" \
  "s_mov_b32 s47, 0x00020000\n" \
  "s_mov_b64 s[36:37], %4\n" \
  "s_and_b32 s37, s37, 0xffff\n" \
  "s_mov_b32 s38, " STR(EX_BYTES32) "\n" \
  "s_mov_b32 s39, 0x00020000\n" \
  "s_mov_b64 s[40:41], %4\n" \
  "s_add_u32 s40, s40, " STR(WS_K_OFF32) "\n" \
  "s_addc_u32 s41, s41, 0\n" \
  "s_and_b32 s41, s41, 0xffff\n" \
  "s_mov_b32 s42, 3*" STR(QKV_SZ32) "\n" \
  "s_mov_b32 s43, 0x00020000\n" \
  "v_lshrrev_b32 v50, 5, v24\n" \
  "v_and_b32 v51, 31, v24\n" \
  "v_mov_b32 v2, 0x4444\n" \
  "v_mov_b32 v27, 0xeeee\n" \
  "v_cmp_lt_u32 vcc, 1, v39\n" \
  "s_mul_i32 s59, %7, 1280\n" \
  "s_add_u32 s59, s59, 0\n" \
  "v_cndmask_b32 v2, v2, v27, vcc\n" \
  "v_cndmask_b32 v29, 0, -1, vcc\n" \
  "v_cndmask_b32 v4, -1, 0, vcc\n" \
  "v_mul_u32_u24 v27, 80, v37\n" \
  "v_add_lshl_u32 v27, v27, v36, 2\n" \
  "v_add_u32 v5, s59, v27\n" \
  "v_lshlrev_b32 v27, 3, v24\n" \
  "s_add_u32 s60, 0, 8192\n" \
  "v_add_u32 v27, s60, v27\n" \
  "s_mul_i32 s59, %7, 1536\n" \
  "v_add_u32 v6, s59, v27\n" \
  "s_add_u32 s59, %7, 1\n" \
  "s_and_b32 s59, s59, 3\n" \
  "s_mul_i32 s59, s59, 1536\n" \
  "v_add_u32 v3, s59, v27\n" \
  "s_add_u32 s59, %7, 2\n" \
  "s_and_b32 s59, s59, 3\n" \
  "s_mul_i32 s59, s59, 1536\n" \
  "v_add_u32 v244, s59, v27\n" \
  "s_add_u32 s59, %7, 3\n" \
  "s_and_b32 s59, s59, 3\n" \
  "s_mul_i32 s59, s59, 1536\n" \
  "v_add_u32 v245, s59, v27\n" \
  "v_and_b32 v27, 1, v37\n" \
  "v_lshlrev_b32 v27, 1, v27\n" \
  "v_and_b32 v28, 1, v38\n" \
  "v_add_u32 v27, v27, v28\n" \
  "v_mul_u32_u24 v27, 160, v27\n" \
  "v_lshrrev_b32 v28, 1, v38\n" \
  "v_lshl_add_u32 v27, v28, 5, v27\n" \
  "v_lshl_add_u32 v27, v39, 3, v27\n" \
  "s_and_b32 s59, %7, 1\n" \
  "s_mul_i32 s59, s59, 640\n" \
  "s_lshr_b32 s60, %7, 1\n" \
  "s_lshl_b32 s60, s60, 6\n" \
  "s_add_u32 s59, s59, s60\n" \
  "s_add_u32 s59, s59, 0\n" \
  "v_add_u32 v7, s59, v27\n" \
  "s_and_b32 s94, s70, 1\n" \
  "s_lshl_b32 s94, s94, 11\n" \
  "s_add_u32 s94, s94, 32768\n" \
  "v_add_u32 v17, s94, v1\n" \
  "s_lshl_b32 s59, s71, 2\n" \
  "s_add_u32 s59, s59, %7\n" \
  "s_lshl_b32 s59, s59, 8\n" \
  "v_and_b32 v8, 0x38, v24\n" \
  "v_lshlrev_b32 v8, 2, v8\n" \
  "v_and_b32 v27, 1, v24\n" \
  "v_lshl_add_u32 v8, v27, 4, v8\n" \
  "v_and_b32 v27, 6, v24\n" \
  "v_lshl_add_u32 v8, v27, 1, v8\n" \
  "v_add_u32 v8, s59, v8\n" \
  "v_and_b32 v27, 7, v24\n" \
  "v_lshlrev_b32 v27, 4, v27\n" \
  "v_lshrrev_b32 v28, 3, v24\n" \
  "v_add_u32 v15, s59, v1\n" \
  "s_xor_b32 s59, s71, 2\n" \
  "s_lshl_b32 s59, s59, 2\n" \
  "s_add_u32 s59, s59, %7\n" \
  "s_lshl_b32 s59, s59, 7\n" \
  "s_add_u32 s59, s59, s94\n" \
  "v_lshlrev_b32 v9, 2, v51\n" \
  "v_add_u32 v9, s59, v9\n" \
  "v_lshl_add_u32 v9, v50, 31, v9\n" \
  "v_add_u32 v25, s59, v27\n" \
  "v_cmp_eq_u32 vcc, 2, v28\n" \
  "s_and_b32 s60, %6, 31\n" \
  "s_lshl_b32 s60, s60, 2\n" \
  "s_or_b32 s60, s60, %7\n" \
  "v_cndmask_b32 v15, v15, v25, vcc\n" \
  "s_cmp_eq_u32 s60, 1\n" \
  "s_cselect_b32 s59, 0, 0x80000000\n" \
  "s_or_b32 s59, s59, 32768+2048\n" \
  "v_mov_b32 v25, 0x80000000\n" \
  "v_cmp_eq_u32 vcc, 3, v28\n" \
  "v_lshl_add_u32 v14, v24, 3, s59\n" \
  "s_nop 0\n" \
  "v_cndmask_b32 v15, v15, v25, vcc\n" \
  "v_lshl_add_u32 v25, v51, 4, s59\n" \
  "v_cmp_eq_u32 vcc, 1, v50\n" \
  "s_lshr_b32 s59, %6, 5\n" \
  "s_and_b32 s93, s59, 1\n" \
  "s_lshl_b32 s93, s93, 14\n" \
  "s_lshr_b32 s60, s59, 1\n" \
  "s_lshl_b32 s60, s60, 12\n" \
  "s_or_b32 s93, s93, s60\n" \
  "s_lshl_b32 s59, s59, 7\n" \
  "s_add_u32 s59, s59, 32768+2048\n" \
  "s_or_b32 s59, s59, s93\n" \
  "v_cndmask_b32 v15, v15, v25, vcc\n" \
  "v_and_b32 v25, 1, v39\n" \
  "v_lshlrev_b32 v25, 14, v25\n" \
  "v_lshrrev_b32 v13, 1, v39\n" \
  "v_lshl_or_b32 v13, v13, 12, v25\n" \
  "v_xor_b32 v13, s59, v13\n" \
  "s_xor_b32 s59, %7, 1\n" \
  "s_lshl_b32 s59, s59, 29\n" \
  "v_or_b32 v13, s59, v13\n" \
  "s_lshl_b32 s59, %7, 18\n" \
  "v_lshl_add_u32 v10, v28, 15, v27\n" \
  "v_add_u32 v10, s59, v10\n" \
  "s_lshl_b32 s60, %7, 4\n" \
  "s_add_u32 s60, s60, s70\n" \
  "v_lshl_add_u32 v25, v28, 1, s60\n" \
  "v_and_b32 v25, 31, v25\n" \
  "v_lshl_add_u32 v10, v25, 7, v10\n" \
  "s_and_b32 s59, %6, 31\n" \
  "s_lshl_b32 s59, s59, 15\n" \
  "s_lshl_b32 s60, %7, 10\n" \
  "s_add_u32 s59, s59, s60\n" \
  "v_add_u32 v11, s59, v1\n" \
  "s_mov_b32 s49, (" STR(EXP_A) "&1)<<13\n" \
  "s_mov_b32 s51, (" STR(EXP_C) "&1)<<13\n" \
  "s_lshr_b32 s59, %6, 5\n" \
  "s_lshl_b32 s59, s59, 15\n" \
  "s_and_b32 s60, %6, 31\n" \
  "s_lshl_b32 s60, s60, 7\n" \
  "s_add_u32 s59, s59, s60\n" \
  "s_or_b32 s49, s49, " STR(EXP_A) "<<21\n" \
  "s_or_b32 s59, s59, s49\n" \
  "v_lshrrev_b32 v25, 3, v36\n" \
  "v_lshlrev_b32 v25, 14, v25\n" \
  "v_lshl_or_b32 v25, v50, 12, v25\n" \
  "v_xor_b32 v12, s59, v25\n" \
  "v_add_u32 v12, v12, v27\n" \
  "v_lshl_add_u32 v12, v37, 31, v12\n" \
  "v_and_b32 v27, 1, v37\n" \
  "v_lshlrev_b32 v27, 5, v27\n" \
  "v_xor_b32 v27, v1, v27\n" \
  "s_add_u32 s59, 0, 32768\n" \
  "v_add_u32 v19, s59, v27\n" \
  "s_lshl_b32 s60, %7, 12\n" \
  "v_add_u32 v18, s60, v19\n" \
  "v_and_b32 v20, 15, v46\n" \
  "v_lshlrev_b32 v20, 4, v20\n" \
  "v_lshl_add_u32 v20, v50, 10, v20\n" \
  "v_lshl_add_u32 v20, v39, 8, v20\n" \
  "v_and_b32 v27, 1, v39\n" \
  "v_lshlrev_b32 v27, 5, v27\n" \
  "v_xor_b32 v20, v20, v27\n" \
  "v_xor_b32 v21, 32, v20\n" \
  "s_add_u32 s59, s59, 8192\n" \
  "v_add_u32 v20, s59, v20\n" \
  "v_add_u32 v21, s59, v21\n" \
  "v_and_b32 v22, 1, v24\n" \
  "v_lshlrev_b32 v22, 7, v22\n" \
  "s_lshr_b32 s59, %7, 1\n" \
  "s_xor_b32 s59, s59, s70\n" \
  "s_lshl_b32 s59, s59, 8\n" \
  "s_add_u32 s59, s59, " STR(CW_GPROG) "*4\n" \
  "v_add_u32 v22, s59, v22\n" \
  "s_lshr_b32 s59, %6, 1\n" \
  "s_add_u32 s59, s59, %7\n" \
  "s_add_u32 s59, s59, 0\n" \
  "s_and_b32 s59, s59, 3\n" \
  "s_lshl_b32 s59, s59, 10\n" \
  "v_add_u32 v80, s59, v1\n" \
  "v_add_u32 v84, s59, v19\n" \
  "v_mov_b32 v88, -1\n" \
  "s_lshr_b32 s59, %6, 1\n" \
  "s_add_u32 s59, s59, %7\n" \
  "s_add_u32 s59, s59, 1\n" \
  "s_and_b32 s59, s59, 3\n" \
  "s_lshl_b32 s59, s59, 10\n" \
  "v_add_u32 v81, s59, v1\n" \
  "v_add_u32 v85, s59, v19\n" \
  "v_mov_b32 v89, -1\n" \
  "s_lshr_b32 s59, %6, 1\n" \
  "s_add_u32 s59, s59, %7\n" \
  "s_add_u32 s59, s59, 2\n" \
  "s_and_b32 s59, s59, 3\n" \
  "s_lshl_b32 s59, s59, 10\n" \
  "v_add_u32 v82, s59, v1\n" \
  "v_add_u32 v86, s59, v19\n" \
  "v_mov_b32 v90, -1\n" \
  "s_lshr_b32 s59, %6, 1\n" \
  "s_add_u32 s59, s59, %7\n" \
  "s_add_u32 s59, s59, 3\n" \
  "s_and_b32 s59, s59, 3\n" \
  "s_lshl_b32 s59, s59, 10\n" \
  "v_add_u32 v83, s59, v1\n" \
  "v_add_u32 v87, s59, v19\n" \
  "v_mov_b32 v91, -1\n" \
  "v_mov_b32 v248, 0\n" \
  "v_mov_b32 v27, 0x3f80\n" \
  "v_and_b32 v25, 7, v36\n" \
  "v_cmp_eq_u32 vcc, 0, v25\n" \
  "v_mov_b32 v28, 1.0\n" \
  "s_nop 0\n" \
  "v_cndmask_b32 v248, v248, v27, vcc\n" \
  "v_cmp_eq_u32 vcc, 1, v25\n" \
  "s_nop 1\n" \
  "v_cndmask_b32 v248, v248, v28, vcc\n" \
  "v_mov_b32 v249, 0\n" \
  "v_mov_b32 v27, 0x3f80\n" \
  "v_and_b32 v25, 7, v36\n" \
  "v_cmp_eq_u32 vcc, 2, v25\n" \
  "v_mov_b32 v28, 1.0\n" \
  "s_nop 0\n" \
  "v_cndmask_b32 v249, v249, v27, vcc\n" \
  "v_cmp_eq_u32 vcc, 3, v25\n" \
  "s_nop 1\n" \
  "v_cndmask_b32 v249, v249, v28, vcc\n" \
  "v_mov_b32 v250, 0\n" \
  "v_mov_b32 v27, 0x3f80\n" \
  "v_and_b32 v25, 7, v36\n" \
  "v_cmp_eq_u32 vcc, 4, v25\n" \
  "v_mov_b32 v28, 1.0\n" \
  "s_nop 0\n" \
  "v_cndmask_b32 v250, v250, v27, vcc\n" \
  "v_cmp_eq_u32 vcc, 5, v25\n" \
  "s_nop 1\n" \
  "v_cndmask_b32 v250, v250, v28, vcc\n" \
  "v_mov_b32 v251, 0\n" \
  "v_mov_b32 v27, 0x3f80\n" \
  "v_and_b32 v25, 7, v36\n" \
  "v_cmp_eq_u32 vcc, 6, v25\n" \
  "v_mov_b32 v28, 1.0\n" \
  "s_nop 0\n" \
  "v_cndmask_b32 v251, v251, v27, vcc\n" \
  "v_cmp_eq_u32 vcc, 7, v25\n" \
  "s_nop 1\n" \
  "v_cndmask_b32 v251, v251, v28, vcc\n" \
  "v_and_b32 v27, 1, v24\n" \
  "v_cmp_eq_u32 vcc, 1, v27\n" \
  "v_mov_b32 v246, 0x05040100\n" \
  "v_mov_b32 v28, 0x03020706\n" \
  "v_cndmask_b32 v246, v246, v28, vcc\n" \
  "v_mov_b32 v252, 0x3f803f80\n" \
  "v_mov_b32 v253, 0x3f803f80\n" \
  "v_mov_b32 v254, 0x3f803f80\n" \
  "v_mov_b32 v255, 0\n" \
  "v_mov_b32 v30, 0\n" \
  "s_mov_b32 s92, 0x3e0293ee\n" \
  "s_mov_b32 s58, 0\n" \
  "s_mov_b32 s29, " STR(E_0) "\n" \
  "s_mov_b32 s48, 0\n" \
  "s_xor_b32 s49, s49, s93\n" \
  "s_lshl_b32 s59, %7, 15\n" \
  "s_add_u32 s49, s49, s59\n" \
  "s_mov_b32 s53, 0\n" \
  "s_or_b32 s51, s51, " STR(EXP_C) "<<21\n" \
  "s_xor_b32 s52, s51, s93\n" \
  "s_and_b32 s59, %6, 16\n" \
  "s_lshl_b32 s59, s59, 10\n" \
  "s_xor_b32 s59, s59, 1052672\n" \
  "s_xor_b32 s52, s52, s59\n" \
  "s_lshr_b32 s59, %7, 1\n" \
  "s_xor_b32 s59, s59, s71\n" \
  "s_and_b32 s59, s59, 1\n" \
  "s_lshl_b32 s59, s59, 14\n" \
  "s_xor_b32 s51, s51, s59\n" \
  "s_lshr_b32 s59, s71, 1\n" \
  "s_lshl_b32 s59, s59, 12\n" \
  "s_xor_b32 s51, s51, s59\n" \
  "s_xor_b32 s51, s51, 1052672\n" \
  "s_and_b32 s59, s70, 7\n" \
  "s_lshl_b32 s59, s59, 16\n" \
  "s_or_b32 s93, s93, s59\n" \
  "s_lshr_b32 s59, %7, 1\n" \
  "s_mul_i32 s59, s59, " STR(QKV_SZ32) "\n" \
  "s_lshl_b32 s60, s70, 14\n" \
  "s_add_u32 s59, s59, s60\n" \
  "s_and_b32 s60, %7, 1\n" \
  "s_lshl_b32 s60, s60, 12\n" \
  "s_add_u32 s59, s59, s60\n" \
  "v_add_u32 v247, s59, v1\n" \
  "s_lshr_b32 s61, %6, 5\n" \
  "s_lshl_b32 s61, s61, 7\n" \
  "s_mov_b64 s[90:91], %8\n" \
  "s_lshr_b32 s59, %6, 1\n" \
  "s_add_u32 s59, s59, %7\n" \
  "s_add_u32 s59, s59, 1\n" \
  "s_and_b32 s59, s59, 3\n" \
  "s_lshl_b32 s59, s59, 19\n" \
  "s_lshl_b32 s60, %7, 21\n" \
  "s_add_u32 s59, s59, s60\n" \
  "s_add_u32 s59, s59, 262144\n" \
  "s_add_u32 m0, s88, 24576\n" \
  "s_nop 0\n" \
  "buffer_load_dwordx4 v32, s[72:75], s59 offen lds\n" \
  "s_add_u32 m0, s88, 28672\n" \
  "s_nop 0\n" \
  "buffer_load_dwordx4 v32, s[76:79], s59 offen lds\n" \
  "s_add_u32 s59, s59, 65536\n" \
  "s_add_u32 m0, s88, 25600\n" \
  "s_nop 0\n" \
  "buffer_load_dwordx4 v33, s[72:75], s59 offen lds\n" \
  "s_add_u32 m0, s88, 29696\n" \
  "s_nop 0\n" \
  "buffer_load_dwordx4 v33, s[76:79], s59 offen lds\n" \
  "s_add_u32 s59, s59, 65536\n" \
  "s_add_u32 m0, s88, 26624\n" \
  "s_nop 0\n" \
  "buffer_load_dwordx4 v32, s[72:75], s59 offen lds\n" \
  "s_add_u32 m0, s88, 30720\n" \
  "s_nop 0\n" \
  "buffer_load_dwordx4 v32, s[76:79], s59 offen lds\n" \
  "s_add_u32 s59, s59, 65536\n" \
  "s_add_u32 m0, s88, 27648\n" \
  "s_nop 0\n" \
  "buffer_load_dwordx4 v33, s[72:75], s59 offen lds\n" \
  "s_add_u32 m0, s88, 31744\n" \
  "s_nop 0\n" \
  "buffer_load_dwordx4 v33, s[76:79], s59 offen lds\n" \
  "s_lshr_b32 s59, %6, 1\n" \
  "s_add_u32 s59, s59, %7\n" \
  "s_add_u32 s59, s59, 2\n" \
  "s_and_b32 s59, s59, 3\n" \
  "s_lshl_b32 s59, s59, 19\n" \
  "s_lshl_b32 s60, %7, 21\n" \
  "s_add_u32 s59, s59, s60\n" \
  "s_add_u32 s59, s59, 0\n" \
  "s_add_u32 m0, s88, 32768\n" \
  "s_nop 0\n" \
  "buffer_load_dwordx4 v32, s[72:75], s59 offen lds\n" \
  "s_add_u32 m0, s88, 36864\n" \
  "s_nop 0\n" \
  "buffer_load_dwordx4 v32, s[76:79], s59 offen lds\n" \
  "s_add_u32 s59, s59, 65536\n" \
  "s_add_u32 m0, s88, 33792\n" \
  "s_nop 0\n" \
  "buffer_load_dwordx4 v33, s[72:75], s59 offen lds\n" \
  "s_add_u32 m0, s88, 37888\n" \
  "s_nop 0\n" \
  "buffer_load_dwordx4 v33, s[76:79], s59 offen lds\n" \
  "s_add_u32 s59, s59, 65536\n" \
  "s_add_u32 m0, s88, 34816\n" \
  "s_nop 0\n" \
  "buffer_load_dwordx4 v32, s[72:75], s59 offen lds\n" \
  "s_add_u32 m0, s88, 38912\n" \
  "s_nop 0\n" \
  "buffer_load_dwordx4 v32, s[76:79], s59 offen lds\n" \
  "s_add_u32 s59, s59, 65536\n" \
  "s_add_u32 m0, s88, 35840\n" \
  "s_nop 0\n" \
  "buffer_load_dwordx4 v33, s[72:75], s59 offen lds\n" \
  "s_add_u32 m0, s88, 39936\n" \
  "s_nop 0\n" \
  "buffer_load_dwordx4 v33, s[76:79], s59 offen lds\n" \
  "s_waitcnt vmcnt(32)\n" \
  "ds_read_b64_tr_b16 a[0:1], v52 offset:0\n" \
  "ds_read_b64_tr_b16 a[2:3], v52 offset:256\n" \
  "ds_read_b64_tr_b16 a[8:9], v53 offset:0\n" \
  "ds_read_b64_tr_b16 a[10:11], v53 offset:256\n" \
  "ds_read_b64_tr_b16 a[16:17], v54 offset:0\n" \
  "ds_read_b64_tr_b16 a[18:19], v54 offset:256\n" \
  "ds_read_b64_tr_b16 a[24:25], v55 offset:0\n" \
  "ds_read_b64_tr_b16 a[26:27], v55 offset:256\n" \
  "s_waitcnt lgkmcnt(0)\n" \
  "s_lshr_b32 s59, %6, 1\n" \
  "s_add_u32 s59, s59, %7\n" \
  "s_add_u32 s59, s59, 2\n" \
  "s_and_b32 s59, s59, 3\n" \
  "s_lshl_b32 s59, s59, 19\n" \
  "s_lshl_b32 s60, %7, 21\n" \
  "s_add_u32 s59, s59, s60\n" \
  "s_add_u32 s59, s59, 262144\n" \
  "s_add_u32 m0, s88, 0\n" \
  "s_nop 0\n" \
  "buffer_load_dwordx4 v32, s[72:75], s59 offen lds\n" \
  "s_add_u32 m0, s88, 4096\n" \
  "s_nop 0\n" \
  "buffer_load_dwordx4 v32, s[76:79], s59 offen lds\n" \
  "s_add_u32 s59, s59, 65536\n" \
  "s_add_u32 m0, s88, 1024\n" \
  "s_nop 0\n" \
  "buffer_load_dwordx4 v33, s[72:75], s59 offen lds\n" \
  "s_add_u32 m0, s88, 5120\n" \
  "s_nop 0\n" \
  "buffer_load_dwordx4 v33, s[76:79], s59 offen lds\n" \
  "s_add_u32 s59, s59, 65536\n" \
  "s_add_u32 m0, s88, 2048\n" \
  "s_nop 0\n" \
  "buffer_load_dwordx4 v32, s[72:75], s59 offen lds\n" \
  "s_add_u32 m0, s88, 6144\n" \
  "s_nop 0\n" \
  "buffer_load_dwordx4 v32, s[76:79], s59 offen lds\n" \
  "s_add_u32 s59, s59, 65536\n" \
  "s_add_u32 m0, s88, 3072\n" \
  "s_nop 0\n" \
  "buffer_load_dwordx4 v33, s[72:75], s59 offen lds\n" \
  "s_add_u32 m0, s88, 7168\n" \
  "s_nop 0\n" \
  "buffer_load_dwordx4 v33, s[76:79], s59 offen lds\n" \
  "s_waitcnt vmcnt(32)\n" \
  "ds_read_b64_tr_b16 a[4:5], v52 offset:8192\n" \
  "ds_read_b64_tr_b16 a[6:7], v52 offset:8448\n" \
  "ds_read_b64_tr_b16 a[12:13], v53 offset:8192\n" \
  "ds_read_b64_tr_b16 a[14:15], v53 offset:8448\n" \
  "ds_read_b64_tr_b16 a[20:21], v54 offset:8192\n" \
  "ds_read_b64_tr_b16 a[22:23], v54 offset:8448\n" \
  "ds_read_b64_tr_b16 a[28:29], v55 offset:8192\n" \
  "ds_read_b64_tr_b16 a[30:31], v55 offset:8448\n" \
  "buffer_load_dwordx4 v[68:71], off, s[44:47], s61 sc1\n" \
  "s_waitcnt lgkmcnt(0)\n" \
  "s_lshr_b32 s59, %6, 1\n" \
  "s_add_u32 s59, s59, %7\n" \
  "s_add_u32 s59, s59, 3\n" \
  "s_and_b32 s59, s59, 3\n" \
  "s_lshl_b32 s59, s59, 19\n" \
  "s_lshl_b32 s60, %7, 21\n" \
  "s_add_u32 s59, s59, s60\n" \
  "s_add_u32 s59, s59, 0\n" \
  "s_add_u32 m0, s88, 8192\n" \
  "s_nop 0\n" \
  "buffer_load_dwordx4 v32, s[72:75], s59 offen lds\n" \
  "s_add_u32 m0, s88, 12288\n" \
  "s_nop 0\n" \
  "buffer_load_dwordx4 v32, s[76:79], s59 offen lds\n" \
  "s_add_u32 s59, s59, 65536\n" \
  "s_add_u32 m0, s88, 9216\n" \
  "s_nop 0\n" \
  "buffer_load_dwordx4 v33, s[72:75], s59 offen lds\n" \
  "s_add_u32 m0, s88, 13312\n" \
  "s_nop 0\n" \
  "buffer_load_dwordx4 v33, s[76:79], s59 offen lds\n" \
  "s_add_u32 s59, s59, 65536\n" \
  "s_add_u32 m0, s88, 10240\n" \
  "s_nop 0\n" \
  "buffer_load_dwordx4 v32, s[72:75], s59 offen lds\n" \
  "s_add_u32 m0, s88, 14336\n" \
  "s_nop 0\n" \
  "buffer_load_dwordx4 v32, s[76:79], s59 offen lds\n" \
  "s_add_u32 s59, s59, 65536\n" \
  "s_add_u32 m0, s88, 11264\n" \
  "s_nop 0\n" \
  "buffer_load_dwordx4 v33, s[72:75], s59 offen lds\n" \
  "s_add_u32 m0, s88, 15360\n" \
  "s_nop 0\n" \
  "buffer_load_dwordx4 v33, s[76:79], s59 offen lds\n" \
  "s_waitcnt vmcnt(33)\n" \
  "ds_read_b64_tr_b16 a[32:33], v52 offset:16384\n" \
  "ds_read_b64_tr_b16 a[34:35], v52 offset:16640\n" \
  "ds_read_b64_tr_b16 a[40:41], v53 offset:16384\n" \
  "ds_read_b64_tr_b16 a[42:43], v53 offset:16640\n" \
  "ds_read_b64_tr_b16 a[48:49], v54 offset:16384\n" \
  "ds_read_b64_tr_b16 a[50:51], v54 offset:16640\n" \
  "ds_read_b64_tr_b16 a[56:57], v55 offset:16384\n" \
  "ds_read_b64_tr_b16 a[58:59], v55 offset:16640\n" \
  "s_waitcnt lgkmcnt(0)\n" \
  "s_lshr_b32 s59, %6, 1\n" \
  "s_add_u32 s59, s59, %7\n" \
  "s_add_u32 s59, s59, 3\n" \
  "s_and_b32 s59, s59, 3\n" \
  "s_lshl_b32 s59, s59, 19\n" \
  "s_lshl_b32 s60, %7, 21\n" \
  "s_add_u32 s59, s59, s60\n" \
  "s_add_u32 s59, s59, 262144\n" \
  "s_add_u32 m0, s88, 16384\n" \
  "s_nop 0\n" \
  "buffer_load_dwordx4 v32, s[72:75], s59 offen lds\n" \
  "s_add_u32 m0, s88, 20480\n" \
  "s_nop 0\n" \
  "buffer_load_dwordx4 v32, s[76:79], s59 offen lds\n" \
  "s_add_u32 s59, s59, 65536\n" \
  "s_add_u32 m0, s88, 17408\n" \
  "s_nop 0\n" \
  "buffer_load_dwordx4 v33, s[72:75], s59 offen lds\n" \
  "s_add_u32 m0, s88, 21504\n" \
  "s_nop 0\n" \
  "buffer_load_dwordx4 v33, s[76:79], s59 offen lds\n" \
  "s_add_u32 s59, s59, 65536\n" \
  "s_add_u32 m0, s88, 18432\n" \
  "s_nop 0\n" \
  "buffer_load_dwordx4 v32, s[72:75], s59 offen lds\n" \
  "s_add_u32 m0, s88, 22528\n" \
  "s_nop 0\n" \
  "buffer_load_dwordx4 v32, s[76:79], s59 offen lds\n" \
  "s_add_u32 s59, s59, 65536\n" \
  "s_add_u32 m0, s88, 19456\n" \
  "s_nop 0\n" \
  "buffer_load_dwordx4 v33, s[72:75], s59 offen lds\n" \
  "s_add_u32 m0, s88, 23552\n" \
  "s_nop 0\n" \
  "buffer_load_dwordx4 v33, s[76:79], s59 offen lds\n" \
  "s_waitcnt vmcnt(33)\n" \
  "ds_read_b64_tr_b16 a[36:37], v52 offset:24576\n" \
  "ds_read_b64_tr_b16 a[38:39], v52 offset:24832\n" \
  "ds_read_b64_tr_b16 a[44:45], v53 offset:24576\n" \
  "ds_read_b64_tr_b16 a[46:47], v53 offset:24832\n" \
  "ds_read_b64_tr_b16 a[52:53], v54 offset:24576\n" \
  "ds_read_b64_tr_b16 a[54:55], v54 offset:24832\n" \
  "ds_read_b64_tr_b16 a[60:61], v55 offset:24576\n" \
  "ds_read_b64_tr_b16 a[62:63], v55 offset:24832\n" \
  "s_waitcnt lgkmcnt(0)\n" \
  "s_lshr_b32 s59, %6, 1\n" \
  "s_add_u32 s59, s59, %7\n" \
  "s_add_u32 s59, s59, 0\n" \
  "s_and_b32 s59, s59, 3\n" \
  "s_lshl_b32 s59, s59, 19\n" \
  "s_lshl_b32 s60, %7, 21\n" \
  "s_add_u32 s59, s59, s60\n" \
  "s_add_u32 s59, s59, 0\n" \
  "s_add_u32 m0, s88, 24576\n" \
  "s_nop 0\n" \
  "buffer_load_dwordx4 v32, s[80:83], s59 offen lds\n" \
  "s_add_u32 s59, s59, 65536\n" \
  "s_add_u32 m0, s88, 25600\n" \
  "s_nop 0\n" \
  "buffer_load_dwordx4 v33, s[80:83], s59 offen lds\n" \
  "s_add_u32 s59, s59, 65536\n" \
  "s_add_u32 m0, s88, 26624\n" \
  "s_nop 0\n" \
  "buffer_load_dwordx4 v32, s[80:83], s59 offen lds\n" \
  "s_add_u32 s59, s59, 65536\n" \
  "s_add_u32 m0, s88, 27648\n" \
  "s_nop 0\n" \
  "buffer_load_dwordx4 v33, s[80:83], s59 offen lds\n" \
  "s_add_u32 s59, s59, 65536\n" \
  "s_add_u32 m0, s88, 28672\n" \
  "s_nop 0\n" \
  "buffer_load_dwordx4 v32, s[80:83], s59 offen lds\n" \
  "s_add_u32 s59, s59, 65536\n" \
  "s_add_u32 m0, s88, 29696\n" \
  "s_nop 0\n" \
  "buffer_load_dwordx4 v33, s[80:83], s59 offen lds\n" \
  "s_add_u32 s59, s59, 65536\n" \
  "s_add_u32 m0, s88, 30720\n" \
  "s_nop 0\n" \
  "buffer_load_dwordx4 v32, s[80:83], s59 offen lds\n" \
  "s_add_u32 s59, s59, 65536\n" \
  "s_add_u32 m0, s88, 31744\n" \
  "s_nop 0\n" \
  "buffer_load_dwordx4 v33, s[80:83], s59 offen lds\n" \
  "s_waitcnt vmcnt(33)\n" \
  "ds_read_b64_tr_b16 a[64:65], v52 offset:32768\n" \
  "ds_read_b64_tr_b16 a[66:67], v52 offset:33024\n" \
  "ds_read_b64_tr_b16 a[72:73], v53 offset:32768\n" \
  "ds_read_b64_tr_b16 a[74:75], v53 offset:33024\n" \
  "ds_read_b64_tr_b16 a[80:81], v54 offset:32768\n" \
  "ds_read_b64_tr_b16 a[82:83], v54 offset:33024\n" \
  "ds_read_b64_tr_b16 a[88:89], v55 offset:32768\n" \
  "ds_read_b64_tr_b16 a[90:91], v55 offset:33024\n" \
  "s_waitcnt lgkmcnt(0)\n" \
  "s_lshr_b32 s59, %6, 1\n" \
  "s_add_u32 s59, s59, %7\n" \
  "s_add_u32 s59, s59, 1\n" \
  "s_and_b32 s59, s59, 3\n" \
  "s_lshl_b32 s59, s59, 19\n" \
  "s_lshl_b32 s60, %7, 21\n" \
  "s_add_u32 s59, s59, s60\n" \
  "s_add_u32 s59, s59, 0\n" \
  "s_add_u32 m0, s88, 32768\n" \
  "s_nop 0\n" \
  "buffer_load_dwordx4 v32, s[80:83], s59 offen lds\n" \
  "s_add_u32 s59, s59, 65536\n" \
  "s_add_u32 m0, s88, 33792\n" \
  "s_nop 0\n" \
  "buffer_load_dwordx4 v33, s[80:83], s59 offen lds\n" \
  "s_add_u32 s59, s59, 65536\n" \
  "s_add_u32 m0, s88, 34816\n" \
  "s_nop 0\n" \
  "buffer_load_dwordx4 v32, s[80:83], s59 offen lds\n" \
  "s_add_u32 s59, s59, 65536\n" \
  "s_add_u32 m0, s88, 35840\n" \
  "s_nop 0\n" \
  "buffer_load_dwordx4 v33, s[80:83], s59 offen lds\n" \
  "s_add_u32 s59, s59, 65536\n" \
  "s_add_u32 m0, s88, 36864\n" \
  "s_nop 0\n" \
  "buffer_load_dwordx4 v32, s[80:83], s59 offen lds\n" \
  "s_add_u32 s59, s59, 65536\n" \
  "s_add_u32 m0, s88, 37888\n" \
  "s_nop 0\n" \
  "buffer_load_dwordx4 v33, s[80:83], s59 offen lds\n" \
  "s_add_u32 s59, s59, 65536\n" \
  "s_add_u32 m0, s88, 38912\n" \
  "s_nop 0\n" \
  "buffer_load_dwordx4 v32, s[80:83], s59 offen lds\n" \
  "s_add_u32 s59, s59, 65536\n" \
  "s_add_u32 m0, s88, 39936\n" \
  "s_nop 0\n" \
  "buffer_load_dwordx4 v33, s[80:83], s59 offen lds\n" \
  "s_waitcnt vmcnt(33)\n" \
  "ds_read_b64_tr_b16 a[68:69], v52 offset:0\n" \
  "ds_read_b64_tr_b16 a[70:71], v52 offset:256\n" \
  "ds_read_b64_tr_b16 a[76:77], v53 offset:0\n" \
  "ds_read_b64_tr_b16 a[78:79], v53 offset:256\n" \
  "ds_read_b64_tr_b16 a[84:85], v54 offset:0\n" \
  "ds_read_b64_tr_b16 a[86:87], v54 offset:256\n" \
  "ds_read_b64_tr_b16 a[92:93], v55 offset:0\n" \
  "ds_read_b64_tr_b16 a[94:95], v55 offset:256\n" \
  "s_waitcnt lgkmcnt(0)\n" \
  "s_lshr_b32 s59, %6, 1\n" \
  "s_add_u32 s59, s59, %7\n" \
  "s_add_u32 s59, s59, 2\n" \
  "s_and_b32 s59, s59, 3\n" \
  "s_lshl_b32 s59, s59, 19\n" \
  "s_lshl_b32 s60, %7, 21\n" \
  "s_add_u32 s59, s59, s60\n" \
  "s_add_u32 s59, s59, 0\n" \
  "s_add_u32 m0, s88, 0\n" \
  "s_nop 0\n" \
  "buffer_load_dwordx4 v32, s[80:83], s59 offen lds\n" \
  "s_add_u32 s59, s59, 65536\n" \
  "s_add_u32 m0, s88, 1024\n" \
  "s_nop 0\n" \
  "buffer_load_dwordx4 v33, s[80:83], s59 offen lds\n" \
  "s_add_u32 s59, s59, 65536\n" \
  "s_add_u32 m0, s88, 2048\n" \
  "s_nop 0\n" \
  "buffer_load_dwordx4 v32, s[80:83], s59 offen lds\n" \
  "s_add_u32 s59, s59, 65536\n" \
  "s_add_u32 m0, s88, 3072\n" \
  "s_nop 0\n" \
  "buffer_load_dwordx4 v33, s[80:83], s59 offen lds\n" \
  "s_add_u32 s59, s59, 65536\n" \
  "s_add_u32 m0, s88, 4096\n" \
  "s_nop 0\n" \
  "buffer_load_dwordx4 v32, s[80:83], s59 offen lds\n" \
  "s_add_u32 s59, s59, 65536\n" \
  "s_add_u32 m0, s88, 5120\n" \
  "s_nop 0\n" \
  "buffer_load_dwordx4 v33, s[80:83], s59 offen lds\n" \
  "s_add_u32 s59, s59, 65536\n" \
  "s_add_u32 m0, s88, 6144\n" \
  "s_nop 0\n" \
  "buffer_load_dwordx4 v32, s[80:83], s59 offen lds\n" \
  "s_add_u32 s59, s59, 65536\n" \
  "s_add_u32 m0, s88, 7168\n" \
  "s_nop 0\n" \
  "buffer_load_dwordx4 v33, s[80:83], s59 offen lds\n" \
  "s_waitcnt vmcnt(32)\n" \
  "ds_read_b64_tr_b16 a[96:97], v52 offset:8192\n" \
  "ds_read_b64_tr_b16 a[98:99], v52 offset:8448\n" \
  "ds_read_b64_tr_b16 a[104:105], v53 offset:8192\n" \
  "ds_read_b64_tr_b16 a[106:107], v53 offset:8448\n" \
  "ds_read_b64_tr_b16 a[112:113], v54 offset:8192\n" \
  "ds_read_b64_tr_b16 a[114:115], v54 offset:8448\n" \
  "ds_read_b64_tr_b16 a[120:121], v55 offset:8192\n" \
  "ds_read_b64_tr_b16 a[122:123], v55 offset:8448\n" \
  "LR1_%=:\n" \
  "v_readfirstlane_b32 s59, v68\n" \
  "v_readfirstlane_b32 s60, v69\n" \
  "v_readfirstlane_b32 s94, v70\n" \
  "v_readfirstlane_b32 s28, v71\n" \
  "s_xor_b32 s59, s59, s90\n" \
  "s_xor_b32 s60, s60, s91\n" \
  "s_xor_b32 s94, s94, s90\n" \
  "s_xor_b32 s28, s28, s91\n" \
  "s_or_b32 s59, s59, s60\n" \
  "s_xor_b32 s28, s28, s94\n" \
  "s_or_b32 s59, s59, s28\n" \
  "s_cbranch_scc1 LR2_%=\n" \
  "LR3_%=:\n" \
  "s_and_b32 s94, s94, 0x7000\n" \
  "s_lshr_b32 s28, s94, 13\n" \
  "s_xor_b32 s49, s49, s94\n" \
  "s_xor_b32 s51, s51, s94\n" \
  "s_xor_b32 s52, s52, s94\n" \
  "s_xor_b32 s93, s93, s94\n" \
  "v_xor_b32 v12, s94, v12\n" \
  "s_lshr_b32 s59, s48, 2\n" \
  "s_xnor_b32 s59, s59, s28\n" \
  "s_and_b32 s59, s59, 1\n" \
  "s_lshl_b32 s59, s59, 13\n" \
  "s_lshl_b32 s55, s48, 19\n" \
  "s_or_b32 s55, s55, s59\n" \
  "s_mov_b32 s60, 0\n" \
  "s_lshl_b32 s50, s60, 19\n" \
  "s_add_u32 s50, s50, " STR(EXP_B) "<<21\n" \
  "s_xor_b32 s50, s50, s93\n" \
  "s_and_b32 s59, s60, 7\n" \
  "s_mul_i32 s59, s59, 0x4800\n" \
  "s_and_b32 s59, s59, 0x7000\n" \
  "s_xor_b32 s50, s50, s59\n" \
  "buffer_store_dwordx4 v[88:91], v15, s[36:39], s50 offen sc1\n" \
  "s_mov_b32 s60, 1\n" \
  "s_lshl_b32 s50, s60, 19\n" \
  "s_add_u32 s50, s50, " STR(EXP_B) "<<21\n" \
  "s_xor_b32 s50, s50, s93\n" \
  "s_and_b32 s59, s60, 7\n" \
  "s_mul_i32 s59, s59, 0x4800\n" \
  "s_and_b32 s59, s59, 0x7000\n" \
  "s_xor_b32 s50, s50, s59\n" \
  "buffer_store_dwordx4 v[88:91], v15, s[36:39], s50 offen sc1\n" \
  "s_mov_b32 s60, 2\n" \
  "s_lshl_b32 s50, s60, 19\n" \
  "s_add_u32 s50, s50, " STR(EXP_B) "<<21\n" \
  "s_xor_b32 s50, s50, s93\n" \
  "s_and_b32 s59, s60, 7\n" \
  "s_mul_i32 s59, s59, 0x4800\n" \
  "s_and_b32 s59, s59, 0x7000\n" \
  "s_xor_b32 s50, s50, s59\n" \
  "buffer_store_dwordx4 v[88:91], v15, s[36:39], s50 offen sc1\n" \
  "s_mov_b32 s60, 3\n" \
  "s_lshl_b32 s50, s60, 19\n" \
  "s_add_u32 s50, s50, " STR(EXP_B) "<<21\n" \
  "s_xor_b32 s50, s50, s93\n" \
  "s_and_b32 s59, s60, 7\n" \
  "s_mul_i32 s59, s59, 0x4800\n" \
  "s_and_b32 s59, s59, 0x7000\n" \
  "s_xor_b32 s50, s50, s59\n" \
  "buffer_store_dwordx4 v[88:91], v15, s[36:39], s50 offen sc1\n" \
  "s_xor_b32 s59, s52, 1052672\n" \
  "buffer_store_dwordx4 v[88:91], v11, s[36:39], s59 offen sc1\n" \
  "buffer_store_dwordx4 v[88:91], v11, s[36:39], s52 offen sc1\n" \
  "s_max_u32 s59, %7, 1\n" \
  "s_lshl_b32 s59, s59, 17\n" \
  "buffer_store_dwordx4 v[88:91], v12, s[36:39], s59 offen sc1\n" \
  "v_min_u32 v64, -2, v64\n" \
  "v_min_u32 v65, -2, v65\n" \
  "v_min_u32 v66, -2, v66\n" \
  "v_min_u32 v67, -2, v67\n" \
  "s_lshl_b32 s59, %7, 29\n" \
  "v_or_b32 v27, s59, v12\n" \
  "buffer_store_dwordx4 v[64:67], v27, s[36:39], 0 offen sc1\n" \
  "s_waitcnt lgkmcnt(0)\n" \
  "s_lshr_b32 s59, %6, 1\n" \
  "s_add_u32 s59, s59, %7\n" \
  "s_add_u32 s59, s59, 3\n" \
  "s_and_b32 s59, s59, 3\n" \
  "s_lshl_b32 s59, s59, 19\n" \
  "s_lshl_b32 s60, %7, 21\n" \
  "s_add_u32 s59, s59, s60\n" \
  "s_add_u32 s59, s59, 0\n" \
  "s_add_u32 m0, s88, 8192\n" \
  "s_nop 0\n" \
  "buffer_load_dwordx4 v32, s[80:83], s59 offen lds\n" \
  "s_add_u32 s59, s59, 65536\n" \
  "s_add_u32 m0, s88, 9216\n" \
  "s_nop 0\n" \
  "buffer_load_dwordx4 v33, s[80:83], s59 offen lds\n" \
  "s_add_u32 s59, s59, 65536\n" \
  "s_add_u32 m0, s88, 10240\n" \
  "s_nop 0\n" \
  "buffer_load_dwordx4 v32, s[80:83], s59 offen lds\n" \
  "s_add_u32 s59, s59, 65536\n" \
  "s_add_u32 m0, s88, 11264\n" \
  "s_nop 0\n" \
  "buffer_load_dwordx4 v33, s[80:83], s59 offen lds\n" \
  "s_add_u32 s59, s59, 65536\n" \
  "s_add_u32 m0, s88, 12288\n" \
  "s_nop 0\n" \
  "buffer_load_dwordx4 v32, s[80:83], s59 offen lds\n" \
  "s_add_u32 s59, s59, 65536\n" \
  "s_add_u32 m0, s88, 13312\n" \
  "s_nop 0\n" \
  "buffer_load_dwordx4 v33, s[80:83], s59 offen lds\n" \
  "s_add_u32 s59, s59, 65536\n" \
  "s_add_u32 m0, s88, 14336\n" \
  "s_nop 0\n" \
  "buffer_load_dwordx4 v32, s[80:83], s59 offen lds\n" \
  "s_add_u32 s59, s59, 65536\n" \
  "s_add_u32 m0, s88, 15360\n" \
  "s_nop 0\n" \
  "buffer_load_dwordx4 v33, s[80:83], s59 offen lds\n" \
  "s_waitcnt vmcnt(40)\n" \
  "ds_read_b64_tr_b16 a[100:101], v52 offset:16384\n" \
  "ds_read_b64_tr_b16 a[102:103], v52 offset:16640\n" \
  "ds_read_b64_tr_b16 a[108:109], v53 offset:16384\n" \
  "ds_read_b64_tr_b16 a[110:111], v53 offset:16640\n" \
  "ds_read_b64_tr_b16 a[116:117], v54 offset:16384\n" \
  "ds_read_b64_tr_b16 a[118:119], v54 offset:16640\n" \
  "ds_read_b64_tr_b16 a[124:125], v55 offset:16384\n" \
  "ds_read_b64_tr_b16 a[126:127], v55 offset:16640\n" \
  "s_waitcnt lgkmcnt(0)\n" \
  "s_lshl_b32 s59, %7, 7\n" \
  "s_add_u32 s59, s59, 0\n" \
  "s_add_u32 m0, s88, 16384\n" \
  "s_nop 0\n" \
  "buffer_load_dwordx4 v34, s[84:87], s59 offen lds\n" \
  "s_add_u32 s59, s59, 16384\n" \
  "s_add_u32 m0, s88, 17408\n" \
  "s_nop 0\n" \
  "buffer_load_dwordx4 v34, s[84:87], s59 offen lds\n" \
  "s_add_u32 s59, s59, 16384\n" \
  "s_add_u32 m0, s88, 18432\n" \
  "s_nop 0\n" \
  "buffer_load_dwordx4 v34, s[84:87], s59 offen lds\n" \
  "s_add_u32 s59, s59, 16384\n" \
  "s_add_u32 m0, s88, 19456\n" \
  "s_nop 0\n" \
  "buffer_load_dwordx4 v34, s[84:87], s59 offen lds\n" \
  "s_add_u32 s59, s59, 16384\n" \
  "s_add_u32 m0, s88, 20480\n" \
  "s_nop 0\n" \
  "buffer_load_dwordx4 v34, s[84:87], s59 offen lds\n" \
  "s_add_u32 s59, s59, 16384\n" \
  "s_add_u32 m0, s88, 21504\n" \
  "s_nop 0\n" \
  "buffer_load_dwordx4 v34, s[84:87], s59 offen lds\n" \
  "s_add_u32 s59, s59, 16384\n" \
  "s_add_u32 m0, s88, 22528\n" \
  "s_nop 0\n" \
  "buffer_load_dwordx4 v34, s[84:87], s59 offen lds\n" \
  "s_add_u32 s59, s59, 16384\n" \
  "s_add_u32 m0, s88, 23552\n" \
  "s_nop 0\n" \
  "buffer_load_dwordx4 v34, s[84:87], s59 offen lds\n" \
  "s_waitcnt vmcnt(40)\n" \
  "ds_read_b64_tr_b16 a[128:129], v42 offset:24576\n" \
  "ds_read_b64_tr_b16 a[130:131], v42 offset:24832\n" \
  "ds_read_b64_tr_b16 a[132:133], v42 offset:28672\n" \
  "ds_read_b64_tr_b16 a[134:135], v42 offset:28928\n" \
  "ds_read_b64_tr_b16 a[136:137], v43 offset:24576\n" \
  "ds_read_b64_tr_b16 a[138:139], v43 offset:24832\n" \
  "ds_read_b64_tr_b16 a[140:141], v43 offset:28672\n" \
  "ds_read_b64_tr_b16 a[142:143], v43 offset:28928\n" \
  "s_waitcnt lgkmcnt(0)\n" \
  "s_lshl_b32 s59, %7, 7\n" \
  "s_add_u32 s59, s59, 131072\n" \
  "s_add_u32 m0, s88, 24576\n" \
  "s_nop 0\n" \
  "buffer_load_dwordx4 v34, s[84:87], s59 offen lds\n" \
  "s_add_u32 s59, s59, 16384\n" \
  "s_add_u32 m0, s88, 25600\n" \
  "s_nop 0\n" \
  "buffer_load_dwordx4 v34, s[84:87], s59 offen lds\n" \
  "s_add_u32 s59, s59, 16384\n" \
  "s_add_u32 m0, s88, 26624\n" \
  "s_nop 0\n" \
  "buffer_load_dwordx4 v34, s[84:87], s59 offen lds\n" \
  "s_add_u32 s59, s59, 16384\n" \
  "s_add_u32 m0, s88, 27648\n" \
  "s_nop 0\n" \
  "buffer_load_dwordx4 v34, s[84:87], s59 offen lds\n" \
  "s_add_u32 s59, s59, 16384\n" \
  "s_add_u32 m0, s88, 28672\n" \
  "s_nop 0\n" \
  "buffer_load_dwordx4 v34, s[84:87], s59 offen lds\n" \
  "s_add_u32 s59, s59, 16384\n" \
  "s_add_u32 m0, s88, 29696\n" \
  "s_nop 0\n" \
  "buffer_load_dwordx4 v34, s[84:87], s59 offen lds\n" \
  "s_add_u32 s59, s59, 16384\n" \
  "s_add_u32 m0, s88, 30720\n" \
  "s_nop 0\n" \
  "buffer_load_dwordx4 v34, s[84:87], s59 offen lds\n" \
  "s_add_u32 s59, s59, 16384\n" \
  "s_add_u32 m0, s88, 31744\n" \
  "s_nop 0\n" \
  "buffer_load_dwordx4 v34, s[84:87], s59 offen lds\n" \
  "s_memtime s[72:73]\n" \
  "s_memrealtime s[74:75]\n" \
  "s_nop 1\n" \
  "s_memrealtime s[76:77]\n" \
  "s_nop 1\n" \
  "s_memrealtime s[78:79]\n" \
  "s_nop 1\n" \
  "s_memrealtime s[80:81]\n" \
  "s_nop 1\n" \
  "s_memrealtime s[82:83]\n" \
  "s_nop 1\n" \
  "s_memrealtime s[84:85]\n" \
  "s_nop 1\n" \
  "s_memrealtime s[86:87]\n" \
  "s_nop 1\n" \
  "s_memrealtime s[88:89]\n" \
  "s_nop 1\n" \
  "s_memrealtime s[90:91]\n" \
  "s_mov_b32 s59, 0\n" \
  "s_waitcnt lgkmcnt(0)\n" \
  "s_cmp_eq_u32 s76, s74\n" \
  "s_addc_u32 s59, s59, 0\n" \
  "s_cmp_eq_u32 s78, s74\n" \
  "s_addc_u32 s59, s59, 0\n" \
  "s_cmp_eq_u32 s80, s74\n" \
  "s_addc_u32 s59, s59, 0\n" \
  "s_cmp_eq_u32 s82, s74\n" \
  "s_addc_u32 s59, s59, 0\n" \
  "s_cmp_eq_u32 s84, s74\n" \
  "s_addc_u32 s59, s59, 0\n" \
  "s_cmp_eq_u32 s86, s74\n" \
  "s_addc_u32 s59, s59, 0\n" \
  "s_cmp_eq_u32 s88, s74\n" \
  "s_addc_u32 s59, s59, 0\n" \
  "s_cmp_eq_u32 s90, s74\n" \
  "s_addc_u32 s59, s59, 0\n" \
  "s_mul_i32 s59, s59, 12\n" \
  "s_add_u32 s68, s72, s59\n" \
  "s_add_u32 s59, s74, 4\n" \
  "s_mul_i32 s69, s59, 24\n" \
  "s_mov_b32 s62, s69\n" \
  "s_mov_b32 s56, s68\n" \
  "s_waitcnt vmcnt(40)\n" \
  "ds_read_b64_tr_b16 a[144:145], v42 offset:32768\n" \
  "ds_read_b64_tr_b16 a[146:147], v42 offset:33024\n" \
  "ds_read_b64_tr_b16 a[148:149], v42 offset:36864\n" \
  "ds_read_b64_tr_b16 a[150:151], v42 offset:37120\n" \
  "ds_read_b64_tr_b16 a[152:153], v43 offset:32768\n" \
  "ds_read_b64_tr_b16 a[154:155], v43 offset:33024\n" \
  "ds_read_b64_tr_b16 a[156:157], v43 offset:36864\n" \
  "ds_read_b64_tr_b16 a[158:159], v43 offset:37120\n" \
  "s_waitcnt vmcnt(32)\n" \
  "ds_read_b64_tr_b16 a[160:161], v42 offset:0\n" \
  "ds_read_b64_tr_b16 a[162:163], v42 offset:256\n" \
  "ds_read_b64_tr_b16 a[164:165], v42 offset:4096\n" \
  "ds_read_b64_tr_b16 a[166:167], v42 offset:4352\n" \
  "ds_read_b64_tr_b16 a[168:169], v43 offset:0\n" \
  "ds_read_b64_tr_b16 a[170:171], v43 offset:256\n" \
  "ds_read_b64_tr_b16 a[172:173], v43 offset:4096\n" \
  "ds_read_b64_tr_b16 a[174:175], v43 offset:4352\n" \
  "s_waitcnt vmcnt(16)\n" \
  "ds_read_b64_tr_b16 a[176:177], v42 offset:8192\n" \
  "ds_read_b64_tr_b16 a[178:179], v42 offset:8448\n" \
  "ds_read_b64_tr_b16 a[180:181], v42 offset:12288\n" \
  "ds_read_b64_tr_b16 a[182:183], v42 offset:12544\n" \
  "ds_read_b64_tr_b16 a[184:185], v43 offset:8192\n" \
  "ds_read_b64_tr_b16 a[186:187], v43 offset:8448\n" \
  "ds_read_b64_tr_b16 a[188:189], v43 offset:12288\n" \
  "ds_read_b64_tr_b16 a[190:191], v43 offset:12544\n" \
  "s_barrier\n" \
  "v_mov_b32 v27, " STR(CW_INIT) "*4\n" \
  "v_mov_b32 v28, 1\n" \
  "v_or_b32 v25, %7, v24\n" \
  "v_lshl_add_u32 v27, v25, 24, v27\n" \
  "buffer_atomic_add v28, v27, s[44:47], 0 offen\n" \
  "s_waitcnt vmcnt(9)\n" \
  "ds_read_b64_tr_b16 a[192:193], v49 offset:16384\n" \
  "ds_read_b64_tr_b16 a[194:195], v49 offset:16640\n" \
  "ds_read_b64_tr_b16 a[196:197], v49 offset:20480\n" \
  "ds_read_b64_tr_b16 a[198:199], v49 offset:20736\n" \
  "ds_read_b64_tr_b16 a[208:209], v49 offset:16400\n" \
  "ds_read_b64_tr_b16 a[210:211], v49 offset:16656\n" \
  "ds_read_b64_tr_b16 a[212:213], v49 offset:20496\n" \
  "ds_read_b64_tr_b16 a[214:215], v49 offset:20752\n" \
  "ds_read_b64_tr_b16 a[224:225], v49 offset:16392\n" \
  "ds_read_b64_tr_b16 a[226:227], v49 offset:16648\n" \
  "ds_read_b64_tr_b16 a[228:229], v49 offset:20488\n" \
  "ds_read_b64_tr_b16 a[230:231], v49 offset:20744\n" \
  "ds_read_b64_tr_b16 a[240:241], v49 offset:16408\n" \
  "ds_read_b64_tr_b16 a[242:243], v49 offset:16664\n" \
  "ds_read_b64_tr_b16 a[244:245], v49 offset:20504\n" \
  "ds_read_b64_tr_b16 a[246:247], v49 offset:20760\n" \
  "s_waitcnt vmcnt(1)\n" \
  "ds_read_b64_tr_b16 a[200:201], v49 offset:24576\n" \
  "ds_read_b64_tr_b16 a[202:203], v49 offset:24832\n" \
  "ds_read_b64_tr_b16 a[204:205], v49 offset:28672\n" \
  "ds_read_b64_tr_b16 a[206:207], v49 offset:28928\n" \
  "ds_read_b64_tr_b16 a[216:217], v49 offset:24592\n" \
  "ds_read_b64_tr_b16 a[218:219], v49 offset:24848\n" \
  "ds_read_b64_tr_b16 a[220:221], v49 offset:28688\n" \
  "ds_read_b64_tr_b16 a[222:223], v49 offset:28944\n" \
  "ds_read_b64_tr_b16 a[232:233], v49 offset:24584\n" \
  "ds_read_b64_tr_b16 a[234:235], v49 offset:24840\n" \
  "ds_read_b64_tr_b16 a[236:237], v49 offset:28680\n" \
  "ds_read_b64_tr_b16 a[238:239], v49 offset:28936\n" \
  "ds_read_b64_tr_b16 a[248:249], v49 offset:24600\n" \
  "ds_read_b64_tr_b16 a[250:251], v49 offset:24856\n" \
  "ds_read_b64_tr_b16 a[252:253], v49 offset:28696\n" \
  "ds_read_b64_tr_b16 a[254:255], v49 offset:28952\n" \
  "s_waitcnt lgkmcnt(0)\n" \
  "s_barrier\n" \
  "LI2a_%=:\n" \
  "buffer_load_dword v23, v22, s[44:47], 0 offen sc1\n" \
  "s_waitcnt vmcnt(0)\n" \
  "v_cmp_gt_u32 vcc, 1, v23\n" \
  "s_cbranch_vccz LI2b_%=\n" \
  "s_sleep 8\n" \
  "s_add_u32 s58, s58, 1\n" \
  "s_cmp_lt_u32 s58, " STR(SPIN_CAP) "\n" \
  "s_cbranch_scc1 LI2a_%=\n" \
  "v_mov_b32 v27, " STR(CW_DEAD) "*4\n" \
  "v_mov_b32 v28, 1\n" \
  "buffer_store_dword v28, v27, s[44:47], 0 offen sc1\n" \
  "s_branch LI2b_%=\n" \
  "LI2b_%=:\n" \
  "buffer_load_dwordx4 v[200:203], v247, s[40:43], s55 offen offset:0 sc1\n" \
  "buffer_load_dwordx4 v[204:207], v247, s[40:43], s55 offen offset:1024 sc1\n" \
  "buffer_load_dwordx4 v[208:211], v247, s[40:43], s55 offen offset:2048 sc1\n" \
  "buffer_load_dwordx4 v[212:215], v247, s[40:43], s55 offen offset:3072 sc1\n" \
  "s_cmp_lg_u32 %7, 2\n" \
  "s_cbranch_scc1 LI6_%=\n" \
  "v_mov_b32 v25, " STR(CW_INIT) "*4\n" \
  "LI1a_%=:\n" \
  "buffer_load_dword v23, v25, s[44:47], 0 offen sc1\n" \
  "s_waitcnt vmcnt(0)\n" \
  "v_cmp_gt_u32 vcc, 128, v23\n" \
  "s_cbranch_vccz LI1b_%=\n" \
  "s_sleep 12\n" \
  "s_add_u32 s58, s58, 1\n" \
  "s_cmp_lt_u32 s58, " STR(SPIN_CAP) "\n" \
  "s_cbranch_scc1 LI1a_%=\n" \
  "v_mov_b32 v27, " STR(CW_DEAD) "*4\n" \
  "v_mov_b32 v28, 1\n" \
  "buffer_store_dword v28, v27, s[44:47], 0 offen sc1\n" \
  "s_branch LI1b_%=\n" \
  "LI1b_%=:\n" \
  "LI6_%=:\n" \
  "s_memtime s[72:73]\n" \
  "s_memrealtime s[74:75]\n" \
  "s_nop 1\n" \
  "s_memrealtime s[76:77]\n" \
  "s_nop 1\n" \
  "s_memrealtime s[78:79]\n" \
  "s_nop 1\n" \
  "s_memrealtime s[80:81]\n" \
  "s_nop 1\n" \
  "s_memrealtime s[82:83]\n" \
  "s_nop 1\n" \
  "s_memrealtime s[84:85]\n" \
  "s_nop 1\n" \
  "s_memrealtime s[86:87]\n" \
  "s_nop 1\n" \
  "s_memrealtime s[88:89]\n" \
  "s_nop 1\n" \
  "s_memrealtime s[90:91]\n" \
  "s_mov_b32 s59, 0\n" \
  "s_waitcnt lgkmcnt(0)\n" \
  "s_cmp_eq_u32 s76, s74\n" \
  "s_addc_u32 s59, s59, 0\n" \
  "s_cmp_eq_u32 s78, s74\n" \
  "s_addc_u32 s59, s59, 0\n" \
  "s_cmp_eq_u32 s80, s74\n" \
  "s_addc_u32 s59, s59, 0\n" \
  "s_cmp_eq_u32 s82, s74\n" \
  "s_addc_u32 s59, s59, 0\n" \
  "s_cmp_eq_u32 s84, s74\n" \
  "s_addc_u32 s59, s59, 0\n" \
  "s_cmp_eq_u32 s86, s74\n" \
  "s_addc_u32 s59, s59, 0\n" \
  "s_cmp_eq_u32 s88, s74\n" \
  "s_addc_u32 s59, s59, 0\n" \
  "s_cmp_eq_u32 s90, s74\n" \
  "s_addc_u32 s59, s59, 0\n" \
  "s_mul_i32 s59, s59, 12\n" \
  "s_add_u32 s68, s72, s59\n" \
  "s_add_u32 s59, s74, 4\n" \
  "s_mul_i32 s69, s59, 24\n" \
  "s_sub_u32 s60, s69, s62\n" \
  "s_sub_u32 s59, s68, s56\n" \
  "s_mov_b32 s62, s69\n" \
  "s_mov_b32 s56, s68\n" \
  "v_cvt_f32_u32 v27, s59\n" \
  "v_cvt_f32_u32 v28, s60\n" \
  "v_rcp_f32 v26, v27\n" \
  "v_rcp_f32 v19, v28\n" \
  "v_mul_f32 v26, v26, v28\n" \
  "v_mul_f32 v95, " STR(RI0) ", v26\n" \
  "v_mul_f32 v19, v19, v27\n" \
  "v_max_f32 v95, v95, v26\n" \
  "v_cvt_f32_u32 v28, " STR(F_P) "\n" \
  "v_mul_f32 v27, v95, v28\n" \
  "v_cvt_u32_f32 v27, v27\n" \
  "v_min_u32 v27, 0x7fff, v27\n" \
  "s_nop 0\n" \
  "v_readfirstlane_b32 s63, v27\n" \
  "s_add_u32 s59, s74, 4+63\n" \
  "s_and_b32 s59, s59, -64\n" \
  "s_mul_i32 s59, s59, 24\n" \
  "s_sub_u32 s59, s59, s69\n" \
  "v_cvt_f32_u32 v27, s59\n" \
  "v_mul_f32 v28, 0x44c00000, v19\n" \
  "v_mul_f32 v27, v27, v19\n" \
  "v_cvt_u32_f32 v28, v28\n" \
  "v_cvt_u32_f32 v27, v27\n" \
  "v_max_u32 v28, " STR(T_Q) ", v28\n" \
  "v_min_u32 v28, 2047, v28\n" \
  "v_readfirstlane_b32 s59, v27\n" \
  "v_readfirstlane_b32 s95, v28\n" \
  "v_subrev_u32 v28, " STR(T_Q) ", v28\n" \
  "v_lshlrev_b32 v28, 2, v28\n" \
  "v_mov_b32 v27, 0\n" \
  "s_add_u32 s59, s59, s68\n" \
  "s_cmp_lg_u32 %7, 1\n" \
  "s_cbranch_scc1 LI8_%=\n" \
  "ds_write_b32 v255, v28 offset:14344\n" \
  "ds_write_b32 v255, v27 offset:14356\n" \
  "v_mov_b32 v92, s59\n" \
  "v_mov_b32 v93, 0\n" \
  "ds_write_b64 v255, v[92:93] offset:14336\n" \
  "LI8_%=:\n" \
  "s_waitcnt lgkmcnt(0)\n" \
  "s_lshl_b32 s59, s70, 7\n" \
  "s_add_u32 s59, s59, " STR(CW_PROG) "*4\n" \
  "s_lshl_b32 s60, s71, 2\n" \
  "s_or_b32 s60, s60, %7\n" \
  "s_cmp_eq_u32 s60, 3\n" \
  "s_cselect_b32 s59, s59, 0x80000000\n" \
  "v_lshl_add_u32 v24, v24, 24, s59\n" \
  "v_mov_b64 v[48:49], 0\n" \
  "v_mov_b64 v[50:51], 0\n" \
  "v_mov_b64 v[52:53], 0\n" \
  "v_mov_b64 v[54:55], 0\n" \
  "v_mov_b64 v[56:57], 0\n" \
  "v_mov_b64 v[58:59], 0\n" \
  "v_mov_b64 v[60:61], 0\n" \
  "v_mov_b64 v[62:63], 0\n" \
  "v_mov_b64 v[64:65], 0\n" \
  "v_mov_b64 v[66:67], 0\n" \
  "v_mov_b64 v[68:69], 0\n" \
  "v_mov_b64 v[70:71], 0\n" \
  "s_waitcnt vmcnt(0)\n" \
  "ds_write_b128 v18, v[200:203] offset:0\n" \
  "ds_write_b128 v18, v[204:207] offset:1024\n" \
  "ds_write_b128 v18, v[208:211] offset:2048\n" \
  "ds_write_b128 v18, v[212:215] offset:3072\n" \
  "ds_write_b32 v255, v255 offset:14360\n" \
  "s_waitcnt lgkmcnt(0)\n" \
  "s_barrier\n" \
  "s_cmp_eq_u32 %7, 0\n" \
  "s_cbranch_scc1 LL_%=\n" \
  "ds_read_b64_tr_b16 v[136:137], v20 offset:0\n" \
  "ds_read_b64_tr_b16 v[138:139], v21 offset:0\n" \
  "ds_read_b64_tr_b16 v[152:153], v20 offset:8\n" \
  "ds_read_b64_tr_b16 v[154:155], v21 offset:8\n" \
  "ds_read_b64_tr_b16 v[140:141], v20 offset:2048\n" \
  "ds_read_b64_tr_b16 v[142:143], v21 offset:2048\n" \
  "ds_read_b64_tr_b16 v[156:157], v20 offset:2056\n" \
  "ds_read_b64_tr_b16 v[158:159], v21 offset:2056\n" \
  "ds_read_b64_tr_b16 v[144:145], v20 offset:4096\n" \
  "ds_read_b64_tr_b16 v[146:147], v21 offset:4096\n" \
  "ds_read_b64_tr_b16 v[160:161], v20 offset:4104\n" \
  "ds_read_b64_tr_b16 v[162:163], v21 offset:4104\n" \
  "ds_read_b64_tr_b16 v[148:149], v20 offset:6144\n" \
  "ds_read_b64_tr_b16 v[150:151], v21 offset:6144\n" \
  "ds_read_b64_tr_b16 v[164:165], v20 offset:6152\n" \
  "ds_read_b64_tr_b16 v[166:167], v21 offset:6152\n" \
  "ds_read_b128 v[104:107], v84 offset:0\n" \
  "ds_read_b128 v[108:111], v85 offset:0\n" \
  "ds_read_b128 v[112:115], v86 offset:0\n" \
  "ds_read_b128 v[116:119], v87 offset:0\n" \
  "ds_read_b128 v[120:123], v84 offset:4096\n" \
  "ds_read_b128 v[124:127], v85 offset:4096\n" \
  "ds_read_b128 v[128:131], v86 offset:4096\n" \
  "ds_read_b128 v[132:135], v87 offset:4096\n" \
  "s_waitcnt lgkmcnt(0)\n" \
  "s_branch LM_%=\n" \
  ".p2align 6\n" \
  "s_nop 1\n" \
  "s_nop 1\n" \
  "s_nop 1\n" \
  "s_nop 1\n" \
  "s_nop 1\n" \
  "s_nop 1\n" \
  "s_nop 1\n" \
  "s_nop 1\n" \
  "s_nop 1\n" \
  "s_nop 1\n" \
  "s_nop 1\n" \
  "s_nop 1\n" \
  "s_nop 1\n" \
  "s_nop 1\n" \
  "s_nop 1\n" \
  "s_nop 1\n" \
  "s_nop 1\n" \
  "s_nop 1\n" \
  "s_nop 1\n" \
  "s_nop 1\n" \
  "s_nop 1\n" \
  "s_nop 1\n" \
  "s_nop 1\n" \
  "s_nop 1\n" \
  "s_nop 1\n" \
  "s_nop 1\n" \
  "s_nop 1\n" \
  "s_nop 1\n" \
  "s_nop 1\n" \
  "s_nop 1\n" \
  "s_nop 1\n" \
  "s_nop 1\n" \
  "s_nop 1\n" \
  "s_nop 1\n" \
  "s_nop 1\n" \
  "s_nop 1\n" \
  "s_nop 1\n" \
  "s_nop 1\n" \
  "s_nop 1\n" \
  "s_nop 1\n" \
  "s_nop 1\n" \
  "s_nop 1\n" \
  "s_nop 1\n" \
  "s_nop 1\n" \
  "s_nop 1\n" \
  "s_nop 1\n" \
  "s_nop 1\n" \
  "s_nop 1\n" \
  "s_nop 1\n" \
  "s_nop 1\n" \
  "s_nop 1\n" \
  "s_nop 1\n" \
  "s_nop 1\n" \
  "s_nop 1\n" \
  "s_nop 1\n" \
  "s_nop 1\n" \
  "s_nop 1\n" \
  "s_nop 1\n" \
  "s_nop 1\n" \
  "s_nop 1\n" \
  "s_nop 1\n" \
  "s_nop 1\n" \
  "s_nop 1\n" \
  "s_nop 1\n" \
  "s_nop 1\n" \
  "s_nop 1\n" \
  "s_nop 1\n" \
  "s_nop 1\n" \
  "s_nop 1\n" \
  "s_nop 1\n" \
  "s_nop 1\n" \
  "s_nop 1\n" \
  "s_nop 1\n" \
  "s_nop 1\n" \
  "s_nop 1\n" \
  "s_nop 1\n" \
  "s_nop 1\n" \
  "s_nop 1\n" \
  "s_nop 1\n" \
  "s_nop 1\n" \
  "s_nop 1\n" \
  "s_nop 1\n" \
  "s_nop 1\n" \
  "s_nop 1\n" \
  "s_nop 1\n" \
  "s_nop 1\n" \
  "s_nop 1\n" \
  "s_nop 1\n" \
  "s_nop 1\n" \
  "s_nop 1\n" \
  "s_nop 1\n" \
  "s_nop 1\n" \
  "s_nop 1\n" \
  "s_nop 1\n" \
  "s_nop 1\n" \
  "s_nop 1\n" \
  "LB_a_%=:\n" \
  "LL_%=:\n" \
  "s_barrier\n" \
  "buffer_load_dwordx4 v[32:35], v80, s[36:39], s49 offen sc1\n" \
  "ds_read_b64_tr_b16 v[136:137], v20 offset:0\n" \
  "ds_read_b64_tr_b16 v[138:139], v21 offset:0\n" \
  "ds_read_b64_tr_b16 v[152:153], v20 offset:8\n" \
  "ds_read_b64_tr_b16 v[154:155], v21 offset:8\n" \
  "ds_read_b64_tr_b16 v[140:141], v20 offset:2048\n" \
  "ds_read_b64_tr_b16 v[142:143], v21 offset:2048\n" \
  "ds_read_b64_tr_b16 v[156:157], v20 offset:2056\n" \
  "buffer_load_dwordx4 v[36:39], v81, s[36:39], s49 offen sc1\n" \
  "ds_read_b64_tr_b16 v[158:159], v21 offset:2056\n" \
  "ds_read_b64_tr_b16 v[144:145], v20 offset:4096\n" \
  "ds_read_b64_tr_b16 v[146:147], v21 offset:4096\n" \
  "ds_read_b64_tr_b16 v[160:161], v20 offset:4104\n" \
  "ds_read_b64_tr_b16 v[162:163], v21 offset:4104\n" \
  "ds_read_b64_tr_b16 v[148:149], v20 offset:6144\n" \
  "ds_read_b64_tr_b16 v[150:151], v21 offset:6144\n" \
  "buffer_load_dwordx4 v[40:43], v82, s[36:39], s49 offen sc1\n" \
  "ds_read_b64_tr_b16 v[164:165], v20 offset:6152\n" \
  "ds_read_b64_tr_b16 v[166:167], v21 offset:6152\n" \
  "s_waitcnt lgkmcnt(12)\n" \
  "v_mfma_f32_16x16x32_bf16 v[184:187], v[136:139], a[192:195], 0\n" \
  "ds_read_b128 v[104:107], v84 offset:0\n" \
  "s_xor_b32 s51, s51, 1052672\n" \
  "s_add_u32 s53, s53, 1<<17\n" \
  "v_mfma_f32_16x16x32_bf16 v[188:191], v[152:155], a[192:195], 0\n" \
  "ds_read_b128 v[108:111], v85 offset:0\n" \
  "s_and_b32 s53, s53, 0x60000\n" \
  "s_lshl_b32 s50, s48, 19\n" \
  "v_mfma_f32_16x16x32_bf16 v[192:195], v[136:139], a[208:211], 0\n" \
  "ds_read_b128 v[112:115], v86 offset:0\n" \
  "s_add_u32 s50, s50, " STR(EXP_B) "<<21\n" \
  "s_xor_b32 s50, s50, s93\n" \
  "buffer_load_dwordx4 v[44:47], v83, s[36:39], s49 offen sc1\n" \
  "v_mfma_f32_16x16x32_bf16 v[196:199], v[152:155], a[208:211], 0\n" \
  "ds_read_b128 v[116:119], v87 offset:0\n" \
  "s_and_b32 s59, s48, 7\n" \
  "s_mul_i32 s59, s59, 0x4800\n" \
  "s_waitcnt lgkmcnt(12)\n" \
  "v_mfma_f32_16x16x32_bf16 v[184:187], v[140:143], a[196:199], v[184:187]\n" \
  "ds_read_b128 v[120:123], v84 offset:4096\n" \
  "s_and_b32 s59, s59, 0x7000\n" \
  "s_xor_b32 s50, s50, s59\n" \
  "v_mfma_f32_16x16x32_bf16 v[188:191], v[156:159], a[196:199], v[188:191]\n" \
  "ds_read_b128 v[124:127], v85 offset:4096\n" \
  "s_add_u32 s60, s48, 4\n" \
  "s_lshl_b32 s54, s60, 19\n" \
  "v_mfma_f32_16x16x32_bf16 v[192:195], v[140:143], a[212:215], v[192:195]\n" \
  "ds_read_b128 v[128:131], v86 offset:4096\n" \
  "s_add_u32 s54, s54, " STR(EXP_B) "<<21\n" \
  "s_xor_b32 s54, s54, s93\n" \
  "v_mfma_f32_16x16x32_bf16 v[196:199], v[156:159], a[212:215], v[196:199]\n" \
  "ds_read_b128 v[132:135], v87 offset:4096\n" \
  "s_and_b32 s59, s60, 7\n" \
  "s_mul_i32 s59, s59, 0x4800\n" \
  "s_waitcnt lgkmcnt(12)\n" \
  "v_mfma_f32_16x16x32_bf16 v[184:187], v[144:147], a[200:203], v[184:187]\n" \
  "s_and_b32 s59, s59, 0x7000\n" \
  "s_xor_b32 s54, s54, s59\n" \
  "v_mfma_f32_16x16x32_bf16 v[188:191], v[160:163], a[200:203], v[188:191]\n" \
  "s_xor_b32 s59, %7, 3\n" \
  "s_lshl_b32 s59, s59, 29\n" \
  "v_mfma_f32_16x16x32_bf16 v[192:195], v[144:147], a[216:219], v[192:195]\n" \
  "s_xor_b32 s60, s53, 0x40000\n" \
  "s_or_b32 s59, s59, s60\n" \
  "v_mfma_f32_16x16x32_bf16 v[196:199], v[160:163], a[216:219], v[196:199]\n" \
  "s_waitcnt lgkmcnt(8)\n" \
  "v_mfma_f32_16x16x32_bf16 v[184:187], v[148:151], a[204:207], v[184:187]\n" \
  "v_mfma_f32_16x16x32_bf16 v[188:191], v[164:167], a[204:207], v[188:191]\n" \
  "v_mfma_f32_16x16x32_bf16 v[192:195], v[148:151], a[220:223], v[192:195]\n" \
  "v_mfma_f32_16x16x32_bf16 v[196:199], v[164:167], a[220:223], v[196:199]\n" \
  "v_add_u32 v26, s59, v12\n" \
  "LS1_%=:\n" \
  "s_waitcnt vmcnt(3)\n" \
  "v_smfmac_f32_16x16x64_bf16 v[52:55], v[32:35], a[8:15], v2\n" \
  "buffer_store_dwordx2 v[236:237], v14, s[36:39], s50 offen sc1\n" \
  "v_smfmac_f32_16x16x64_bf16 v[56:59], v[32:35], a[16:23], v2\n" \
  "v_cvt_pk_bf16_f32 v168, v184, v185\n" \
  "v_smfmac_f32_16x16x64_bf16 v[60:63], v[32:35], a[24:31], v2\n" \
  "buffer_store_dwordx4 v[88:91], v11, s[36:39], s52 offen sc1\n" \
  "v_smfmac_f32_16x16x64_bf16 v[48:51], v[32:35], a[0:7], v2\n" \
  "v_cvt_pk_bf16_f32 v169, v186, v187\n" \
  "s_waitcnt vmcnt(4)\n" \
  "v_smfmac_f32_16x16x64_bf16 v[52:55], v[36:39], a[40:47], v2\n" \
  "buffer_store_dwordx4 v[88:91], v15, s[36:39], s54 offen sc1\n" \
  "v_smfmac_f32_16x16x64_bf16 v[56:59], v[36:39], a[48:55], v2\n" \
  "v_cvt_pk_bf16_f32 v170, v188, v189\n" \
  "v_smfmac_f32_16x16x64_bf16 v[60:63], v[36:39], a[56:63], v2\n" \
  "buffer_load_dword v23, v22, s[44:47], 0 offen sc1\n" \
  "v_smfmac_f32_16x16x64_bf16 v[48:51], v[36:39], a[32:39], v2\n" \
  "v_cvt_pk_bf16_f32 v171, v190, v191\n" \
  "s_waitcnt vmcnt(5)\n" \
  "v_smfmac_f32_16x16x64_bf16 v[52:55], v[40:43], a[72:79], v2\n" \
  "v_cvt_pk_bf16_f32 v172, v192, v193\n" \
  "v_smfmac_f32_16x16x64_bf16 v[56:59], v[40:43], a[80:87], v2\n" \
  "buffer_store_dwordx4 v[88:91], v26, s[36:39], 0 offen sc1\n" \
  "v_smfmac_f32_16x16x64_bf16 v[60:63], v[40:43], a[88:95], v2\n" \
  "v_cvt_pk_bf16_f32 v173, v194, v195\n" \
  "v_smfmac_f32_16x16x64_bf16 v[48:51], v[40:43], a[64:71], v2\n" \
  "v_cvt_pk_bf16_f32 v174, v196, v197\n" \
  "s_waitcnt vmcnt(5)\n" \
  "v_smfmac_f32_16x16x64_bf16 v[52:55], v[44:47], a[104:111], v2\n" \
  "v_cvt_pk_bf16_f32 v175, v198, v199\n" \
  "v_smfmac_f32_16x16x64_bf16 v[56:59], v[44:47], a[112:119], v2\n" \
  "v_smfmac_f32_16x16x64_bf16 v[60:63], v[44:47], a[120:127], v2\n" \
  "v_add_f32 v52, v52, v54\n" \
  "v_add_f32 v53, v53, v55\n" \
  "v_smfmac_f32_16x16x64_bf16 v[48:51], v[44:47], a[96:103], v2\n" \
  "v_add_f32 v56, v56, v58\n" \
  "v_add_f32 v57, v57, v59\n" \
  "ds_write_b64 v3, v[52:53] offset:0\n" \
  "v_add_f32 v60, v60, v62\n" \
  "v_add_f32 v61, v61, v63\n" \
  "v_smfmac_f32_16x16x64_bf16 v[64:67], v[32:35], a[128:135], v2\n" \
  "ds_write_b64 v244, v[56:57] offset:512\n" \
  "v_smfmac_f32_16x16x64_bf16 v[68:71], v[32:35], a[136:143], v2\n" \
  "ds_write_b64 v245, v[60:61] offset:1024\n" \
  "v_cmp_u_f32 vcc, v52, v53\n" \
  "v_smfmac_f32_16x16x64_bf16 v[64:67], v[36:39], a[144:151], v2\n" \
  "v_add_f32 v48, v48, v50\n" \
  "v_add_f32 v49, v49, v51\n" \
  "v_smfmac_f32_16x16x64_bf16 v[68:71], v[36:39], a[152:159], v2\n" \
  "v_smfmac_f32_16x16x64_bf16 v[64:67], v[40:43], a[160:167], v2\n" \
  "s_cbranch_vccnz LA_%=\n" \
  "LA4_%=:\n" \
  "s_waitcnt lgkmcnt(0)\n" \
  "s_barrier\n" \
  "ds_read_b64 v[72:73], v6 offset:0\n" \
  "ds_read_b64 v[74:75], v6 offset:512\n" \
  "ds_read_b64 v[76:77], v6 offset:1024\n" \
  "ds_read_b96 v[92:94], v255 offset:14336\n" \
  "v_smfmac_f32_16x16x64_bf16 v[68:71], v[40:43], a[168:175], v2\n" \
  "v_smfmac_f32_16x16x64_bf16 v[64:67], v[44:47], a[176:183], v2\n" \
  "s_waitcnt lgkmcnt(3)\n" \
  "v_add_f32 v48, v48, v72\n" \
  "v_add_f32 v49, v49, v73\n" \
  "s_waitcnt lgkmcnt(1)\n" \
  "v_add_f32 v74, v74, v76\n" \
  "v_add_f32 v75, v75, v77\n" \
  "v_add_f32 v48, v48, v74\n" \
  "v_add_f32 v49, v49, v75\n" \
  "v_cvt_pk_bf16_f32 v27, v48, v49\n" \
  "v_smfmac_f32_16x16x64_bf16 v[68:71], v[44:47], a[184:191], v2\n" \
  "v_mov_b32_dpp v28, v27 quad_perm:[1,0,3,2] row_mask:0xf bank_mask:0xf\n" \
  "v_perm_b32 v27, v28, v27, v246\n" \
  "buffer_store_dword v27, v8, s[36:39], s50 offen sc1\n" \
  "v_add_f32 v64, v64, v66\n" \
  "v_add_f32 v65, v65, v67\n" \
  "s_nop 2\n" \
  "v_add_f32 v68, v68, v70\n" \
  "v_add_f32 v69, v69, v71\n" \
  "ds_write2_b32 v5, v64, v65 offset0:0 offset1:40\n" \
  "ds_write2_b32 v5, v68, v69 offset0:16 offset1:56\n" \
  "v_mov_b64 v[184:185], 0\n" \
  "s_andn2_b32 s49, s49, 0x60000\n" \
  "s_or_b32 s49, s49, s53\n" \
  "s_xor_b32 s52, s52, 1052672\n" \
  "v_mov_b64 v[186:187], 0\n" \
  "s_getpc_b64 s[82:83]\n" \
  "LG_b_%=:\n" \
  "s_add_u32 s82, s82, LB_b_%=-LG_b_%=\n" \
  "s_addc_u32 s83, s83, 0\n" \
  "v_mov_b64 v[188:189], 0\n" \
  "s_waitcnt lgkmcnt(0)\n" \
  "s_barrier\n" \
  "s_memtime s[66:67]\n" \
  "ds_read_b64 v[72:73], v7 offset:0\n" \
  "ds_read_b64 v[74:75], v7 offset:1280\n" \
  "ds_read_b64 v[76:77], v7 offset:2560\n" \
  "ds_read_b64 v[78:79], v7 offset:3840\n" \
  "v_readfirstlane_b32 s64, v92\n" \
  "v_readfirstlane_b32 s59, v93\n" \
  "v_readfirstlane_b32 s60, v94\n" \
  "v_mov_b64 v[190:191], 0\n" \
  "s_add_u32 s64, s64, s59\n" \
  "s_lshr_b32 s95, s60, 2\n" \
  "s_add_u32 s61, s64, " STR(F_B) "\n" \
  "s_add_u32 s61, s61, s95\n" \
  "s_add_u32 s95, s95, " STR(T_Q) "\n" \
  "s_cmp_ge_u32 s48, 63\n" \
  "s_cbranch_scc1 LD_%=\n" \
  "s_waitcnt lgkmcnt(0)\n" \
  "s_sub_i32 s61, s61, s66\n" \
  "v_pk_add_f32 v[72:73], v[72:73], v[74:75]\n" \
  "v_pk_add_f32 v[76:77], v[76:77], v[78:79]\n" \
  "v_pk_add_f32 v[72:73], v[72:73], v[76:77]\n" \
  "v_cvt_pk_bf16_f32 v27, v72, v73\n" \
  "buffer_store_dword v27, v9, s[36:39], s50 offen sc1\n" \
  "s_add_i32 s59, s61, " STR(T_SNAP) "\n" \
  "s_cmp_gt_u32 s59, 768+" STR(T_SNAP) "\n" \
  "s_cbranch_scc1 LW_b_%=\n" \
  "LV_b_%=:\n" \
  "s_max_i32 s61, s61, 0\n" \
  "s_lshr_b32 s61, s61, 3\n" \
  "s_lshl_b32 s61, s61, 2\n" \
  "s_sub_u32 s82, s82, s61\n" \
  "s_subb_u32 s83, s83, 0\n" \
  "s_setpc_b64 s[82:83]\n" \
  "s_nop 1\n" \
  "s_nop 1\n" \
  "s_nop 1\n" \
  "s_nop 1\n" \
  "s_nop 1\n" \
  "s_nop 1\n" \
  "s_nop 1\n" \
  "s_nop 1\n" \
  "s_nop 1\n" \
  "s_nop 1\n" \
  "s_nop 1\n" \
  "s_nop 1\n" \
  "s_nop 1\n" \
  "s_nop 1\n" \
  "s_nop 1\n" \
  "s_nop 1\n" \
  "s_nop 1\n" \
  "s_nop 1\n" \
  "s_nop 1\n" \
  "s_nop 1\n" \
  "s_nop 1\n" \
  "s_nop 1\n" \
  "s_nop 1\n" \
  "s_nop 1\n" \
  "s_nop 1\n" \
  "s_nop 1\n" \
  "s_nop 1\n" \
  "s_nop 1\n" \
  "s_nop 1\n" \
  "s_nop 1\n" \
  "s_nop 1\n" \
  "s_nop 1\n" \
  "s_nop 1\n" \
  "s_nop 1\n" \
  "s_nop 1\n" \
  "s_nop 1\n" \
  "s_nop 1\n" \
  "s_nop 1\n" \
  "s_nop 1\n" \
  "s_nop 1\n" \
  "s_nop 1\n" \
  "s_nop 1\n" \
  "s_nop 1\n" \
  "s_nop 1\n" \
  "s_nop 1\n" \
  "s_nop 1\n" \
  "s_nop 1\n" \
  "s_nop 1\n" \
  "s_nop 1\n" \
  "s_nop 1\n" \
  "s_nop 1\n" \
  "s_nop 1\n" \
  "s_nop 1\n" \
  "s_nop 1\n" \
  "s_nop 1\n" \
  "s_nop 1\n" \
  "s_nop 1\n" \
  "s_nop 1\n" \
  "s_nop 1\n" \
  "s_nop 1\n" \
  "s_nop 1\n" \
  "s_nop 1\n" \
  "s_nop 1\n" \
  "s_nop 1\n" \
  "s_nop 1\n" \
  "s_nop 1\n" \
  "s_nop 1\n" \
  "s_nop 1\n" \
  "s_nop 1\n" \
  "s_nop 1\n" \
  "s_nop 1\n" \
  "s_nop 1\n" \
  "s_nop 1\n" \
  "s_nop 1\n" \
  "s_nop 1\n" \
  "s_nop 1\n" \
  "s_nop 1\n" \
  "s_nop 1\n" \
  "s_nop 1\n" \
  "s_nop 1\n" \
  "s_nop 1\n" \
  "s_nop 1\n" \
  "s_nop 1\n" \
  "s_nop 1\n" \
  "s_nop 1\n" \
  "s_nop 1\n" \
  "s_nop 1\n" \
  "s_nop 1\n" \
  "s_nop 1\n" \
  "s_nop 1\n" \
  "s_nop 1\n" \
  "s_nop 1\n" \
  "s_nop 1\n" \
  "s_nop 1\n" \
  "s_nop 1\n" \
  "s_nop 1\n" \
  "LB_b_%=:\n" \
  "buffer_load_dwordx4 v[32:35], v80, s[36:39], s50 offen sc1\n" \
  "v_mfma_f32_16x16x32_bf16 v[200:203], v[136:139], a[224:227], 0\n" \
  "s_sub_u32 s96, s60, s95\n" \
  "v_mfma_f32_16x16x32_bf16 v[204:207], v[152:155], a[224:227], 0\n" \
  "s_add_u32 s96, s96, " STR(F_C) "+" STR(T_Q) "\n" \
  "v_mfma_f32_16x16x32_bf16 v[208:211], v[136:139], a[240:243], 0\n" \
  "s_add_u32 s60, s48, 1\n" \
  "buffer_load_dwordx4 v[36:39], v81, s[36:39], s50 offen sc1\n" \
  "v_mfma_f32_16x16x32_bf16 v[212:215], v[152:155], a[240:243], 0\n" \
  "s_lshr_b32 s59, s60, 2\n" \
  "v_mfma_f32_16x16x32_bf16 v[200:203], v[140:143], a[228:231], v[200:203]\n" \
  "s_xnor_b32 s59, s59, s28\n" \
  "v_mfma_f32_16x16x32_bf16 v[204:207], v[156:159], a[228:231], v[204:207]\n" \
  "s_and_b32 s59, s59, 1\n" \
  "buffer_load_dwordx4 v[40:43], v82, s[36:39], s50 offen sc1\n" \
  "v_mfma_f32_16x16x32_bf16 v[208:211], v[140:143], a[244:247], v[208:211]\n" \
  "s_lshl_b32 s59, s59, 13\n" \
  "v_mfma_f32_16x16x32_bf16 v[212:215], v[156:159], a[244:247], v[212:215]\n" \
  "s_lshl_b32 s55, s60, 19\n" \
  "v_mfma_f32_16x16x32_bf16 v[200:203], v[144:147], a[232:235], v[200:203]\n" \
  "s_or_b32 s55, s55, s59\n" \
  "buffer_load_dwordx4 v[44:47], v83, s[36:39], s50 offen sc1\n" \
  "v_mfma_f32_16x16x32_bf16 v[204:207], v[160:163], a[232:235], v[204:207]\n" \
  "v_mov_b64 v[192:193], 0\n" \
  "v_mfma_f32_16x16x32_bf16 v[208:211], v[144:147], a[248:251], v[208:211]\n" \
  "v_mov_b64 v[194:195], 0\n" \
  "v_mfma_f32_16x16x32_bf16 v[212:215], v[160:163], a[248:251], v[212:215]\n" \
  "v_mov_b64 v[196:197], 0\n" \
  "s_nop " STR(GAP_V) "\n" \
  "buffer_load_dwordx4 v[96:99], v17, s[36:39], s50 offen offset:0 sc1\n" \
  "v_mfma_f32_16x16x32_bf16 v[200:203], v[148:151], a[236:239], v[200:203]\n" \
  "v_mov_b64 v[198:199], 0\n" \
  "v_mfma_f32_16x16x32_bf16 v[204:207], v[164:167], a[236:239], v[204:207]\n" \
  "v_mfma_f32_16x16x32_bf16 v[208:211], v[148:151], a[252:255], v[208:211]\n" \
  "buffer_load_dwordx4 v[100:103], v17, s[36:39], s50 offen offset:1024 sc1\n" \
  "v_mfma_f32_16x16x32_bf16 v[212:215], v[164:167], a[252:255], v[212:215]\n" \
  "s_andn2_b32 s59, s50, 0x70000\n" \
  "v_xor_b32 v28, s59, v13\n" \
  "buffer_load_dwordx2 v[92:93], v28, s[36:39], 0 offen sc1\n" \
  "s_waitcnt vmcnt(9)\n" \
  "v_cmp_ge_u32 vcc, s60, v23\n" \
  "s_nop 1\n" \
  "v_cvt_pk_bf16_f32 v176, v200, v201\n" \
  "v_cvt_pk_bf16_f32 v177, v202, v203\n" \
  "v_cvt_pk_bf16_f32 v178, v204, v205\n" \
  "v_cvt_pk_bf16_f32 v179, v206, v207\n" \
  "v_cvt_pk_bf16_f32 v180, v208, v209\n" \
  "v_cvt_pk_bf16_f32 v181, v210, v211\n" \
  "v_cvt_pk_bf16_f32 v182, v212, v213\n" \
  "v_cvt_pk_bf16_f32 v183, v214, v215\n" \
  "s_cmp_lg_u64 vcc, 0\n" \
  "s_cselect_b32 s54, 0x80000000, 0\n" \
  "v_or_b32 v16, s54, v247\n" \
  "s_mov_b32 vcc_lo, 0x33333333\n" \
  "s_mov_b32 vcc_hi, 0x33333333\n" \
  "LS2_%=:\n" \
  "s_waitcnt vmcnt(6)\n" \
  "v_mfma_f32_16x16x32_bf16 v[228:231], v[120:123], v[32:35], 0\n" \
  "v_mfma_f32_16x16x32_bf16 v[224:227], v[104:107], v[32:35], 0\n" \
  "v_mfma_f32_16x16x32_bf16 v[232:235], v[32:35], v[32:35], 0\n" \
  "buffer_load_dwordx4 v[200:203], v16, s[40:43], s55 offen offset:0 sc1\n" \
  "s_waitcnt vmcnt(6)\n" \
  "v_mfma_f32_16x16x32_bf16 v[228:231], v[124:127], v[36:39], v[228:231]\n" \
  "v_mfma_f32_16x16x32_bf16 v[224:227], v[108:111], v[36:39], v[224:227]\n" \
  "v_mfma_f32_16x16x32_bf16 v[232:235], v[36:39], v[36:39], v[232:235]\n" \
  "buffer_load_dwordx4 v[204:207], v16, s[40:43], s55 offen offset:1024 sc1\n" \
  "s_waitcnt vmcnt(6)\n" \
  "v_mfma_f32_16x16x32_bf16 v[228:231], v[128:131], v[40:43], v[228:231]\n" \
  "v_mfma_f32_16x16x32_bf16 v[224:227], v[112:115], v[40:43], v[224:227]\n" \
  "v_mfma_f32_16x16x32_bf16 v[232:235], v[40:43], v[40:43], v[232:235]\n" \
  "buffer_load_dwordx4 v[208:211], v16, s[40:43], s55 offen offset:2048 sc1\n" \
  "s_waitcnt vmcnt(6)\n" \
  "v_mfma_f32_16x16x32_bf16 v[228:231], v[132:135], v[44:47], v[228:231]\n" \
  "v_mfma_f32_16x16x32_bf16 v[224:227], v[116:119], v[44:47], v[224:227]\n" \
  "v_mfma_f32_16x16x32_bf16 v[232:235], v[44:47], v[44:47], v[232:235]\n" \
  "buffer_load_dwordx4 v[212:215], v16, s[40:43], s55 offen offset:3072 sc1\n" \
  "s_nop 2\n" \
  "v_cndmask_b32_dpp v224, v228, v224, vcc quad_perm:[2,3,0,1] row_mask:0xf bank_mask:0xf\n" \
  "v_cndmask_b32_dpp v225, v229, v225, vcc quad_perm:[2,3,0,1] row_mask:0xf bank_mask:0xf\n" \
  "v_cndmask_b32_dpp v226, v230, v226, vcc quad_perm:[2,3,0,1] row_mask:0xf bank_mask:0xf\n" \
  "v_cndmask_b32_dpp v227, v231, v227, vcc quad_perm:[2,3,0,1] row_mask:0xf bank_mask:0xf\n" \
  "v_cndmask_b32_dpp v234, v235, v234, vcc quad_perm:[2,3,0,1] row_mask:0xf bank_mask:0xf\n" \
  "v_cmp_u_f32 vcc, v228, v228\n" \
  "v_max3_f32 v25, v224, v225, v226\n" \
  "v_max3_f32 v25, v25, v227, v234\n" \
  "s_waitcnt vmcnt(6)\n" \
  "v_smfmac_f32_16x16x64_bf16 v[184:187], v[96:99], a[192:199], v2\n" \
  "v_max_f32_dpp v26, v25, v25 quad_perm:[2,3,0,1] row_mask:0xf bank_mask:0xf\n" \
  "v_max_f32_dpp v25, v25, v25 quad_perm:[2,3,0,1] row_mask:0xf bank_mask:0xf\n" \
  "v_smfmac_f32_16x16x64_bf16 v[188:191], v[96:99], a[208:215], v2\n" \
  "v_permlane16_swap_b32 v25, v26\n" \
  "v_max_f32 v27, v25, v26\n" \
  "v_max_f32 v25, v25, v26\n" \
  "v_smfmac_f32_16x16x64_bf16 v[192:195], v[96:99], a[224:231], v2\n" \
  "s_cbranch_vccnz LBE_%=\n" \
  "LBH_%=:\n" \
  "v_permlane32_swap_b32 v25, v27\n" \
  "v_max_f32 v25, v25, v27\n" \
  "v_mul_f32 v25, s92, v25\n" \
  "v_fma_f32 v224, v224, s92, -v25\n" \
  "v_smfmac_f32_16x16x64_bf16 v[196:199], v[96:99], a[240:247], v2\n" \
  "v_fma_f32 v225, v225, s92, -v25\n" \
  "v_exp_f32 v224, v224\n" \
  "v_exp_f32 v225, v225\n" \
  "s_waitcnt vmcnt(5)\n" \
  "v_smfmac_f32_16x16x64_bf16 v[184:187], v[100:103], a[200:207], v2\n" \
  "v_fma_f32 v226, v226, s92, -v25\n" \
  "v_smfmac_f32_16x16x64_bf16 v[188:191], v[100:103], a[216:223], v2\n" \
  "v_fma_f32 v227, v227, s92, -v25\n" \
  "v_smfmac_f32_16x16x64_bf16 v[192:195], v[100:103], a[232:239], v2\n" \
  "v_exp_f32 v226, v226\n" \
  "v_smfmac_f32_16x16x64_bf16 v[196:199], v[100:103], a[248:255], v2\n" \
  "v_exp_f32 v227, v227\n" \
  "v_cvt_pk_bf16_f32 v228, v224, v225\n" \
  "v_cvt_pk_bf16_f32 v229, v226, v227\n" \
  "v_and_b32 v236, v228, v4\n" \
  "v_and_b32 v237, v229, v4\n" \
  "v_and_b32_dpp v238, v228, v4 quad_perm:[2,3,0,1] row_mask:0xf bank_mask:0xf\n" \
  "v_and_b32_dpp v239, v229, v4 quad_perm:[2,3,0,1] row_mask:0xf bank_mask:0xf\n" \
  "v_fma_f32 v234, v234, s92, -v25\n" \
  "v_and_b32_dpp v240, v228, v29 quad_perm:[2,3,0,1] row_mask:0xf bank_mask:0xf\n" \
  "v_mfma_f32_16x16x32_bf16 v[48:51], v[168:171], v[236:239], 0\n" \
  "v_and_b32_dpp v241, v229, v29 quad_perm:[2,3,0,1] row_mask:0xf bank_mask:0xf\n" \
  "v_and_b32 v242, v228, v29\n" \
  "v_mfma_f32_16x16x32_bf16 v[52:55], v[176:179], v[236:239], 0\n" \
  "v_and_b32 v243, v229, v29\n" \
  "v_exp_f32 v234, v234\n" \
  "v_cvt_pk_bf16_f32 v216, v184, v185\n" \
  "v_mfma_f32_16x16x32_bf16 v[48:51], v[172:175], v[240:243], v[48:51]\n" \
  "v_mov_b32_dpp v25, v234 quad_perm:[0,1,0,1] row_mask:0xf bank_mask:0xf\n" \
  "v_mov_b32_dpp v26, v234 quad_perm:[2,3,2,3] row_mask:0xf bank_mask:0xf\n" \
  "v_mfma_f32_16x16x32_bf16 v[52:55], v[180:183], v[240:243], v[52:55]\n" \
  "v_cvt_pk_bf16_f32 v25, v25, v26\n" \
  "v_and_b32 v230, v25, v4\n" \
  "v_and_b32 v231, v25, v4\n" \
  "v_and_b32 v232, v25, v29\n" \
  "v_and_b32 v233, v25, v29\n" \
  "v_mfma_f32_16x16x32_bf16 v[56:59], v[252:255], v[228:231], 0\n" \
  "v_cvt_pk_bf16_f32 v217, v186, v187\n" \
  "v_cvt_pk_bf16_f32 v218, v188, v189\n" \
  "v_cvt_pk_bf16_f32 v219, v190, v191\n" \
  "v_cvt_pk_bf16_f32 v220, v192, v193\n" \
  "v_cvt_pk_bf16_f32 v221, v194, v195\n" \
  "v_mfma_f32_16x16x32_bf16 v[48:51], v[216:219], v[230:233], v[48:51]\n" \
  "v_cvt_pk_bf16_f32 v222, v196, v197\n" \
  "v_cvt_pk_bf16_f32 v223, v198, v199\n" \
  "v_add_f32_dpp v56, v56, v56 quad_perm:[2,3,0,1] row_mask:0xf bank_mask:0xf\n" \
  "v_rcp_f32 v56, v56\n" \
  "v_mfma_f32_16x16x32_bf16 v[52:55], v[220:223], v[230:233], v[52:55]\n" \
  "s_nop 2\n" \
  "v_cmp_u_f32 vcc, v48, v48\n" \
  "v_mul_f32 v48, v48, v56\n" \
  "v_mul_f32 v49, v49, v56\n" \
  "v_mul_f32 v50, v50, v56\n" \
  "v_mul_f32 v51, v51, v56\n" \
  "v_pk_mul_f32 v[52:53], v[52:53], v[56:57] op_sel_hi:[1,0]\n" \
  "v_pk_mul_f32 v[54:55], v[54:55], v[56:57] op_sel_hi:[1,0]\n" \
  "v_cvt_pk_bf16_f32 v72, v48, v49\n" \
  "v_cvt_pk_bf16_f32 v73, v50, v51\n" \
  "v_cvt_pk_bf16_f32 v74, v52, v53\n" \
  "v_cvt_pk_bf16_f32 v75, v54, v55\n" \
  "s_cbranch_vccnz LBF_%=\n" \
  "LBG_%=:\n" \
  "buffer_store_dwordx4 v[72:75], v10, s[36:39], s51 offen sc1\n" \
  "s_memtime s[66:67]\n" \
  "s_add_u32 s61, s64, s96\n" \
  "s_cmp_lg_u32 %7, 0\n" \
  "s_cbranch_scc1 LO_%=\n" \
  "s_getpc_b64 s[82:83]\n" \
  "LG_c_%=:\n" \
  "s_add_u32 s82, s82, LB_c_%=-LG_c_%=\n" \
  "s_addc_u32 s83, s83, 0\n" \
  "s_waitcnt lgkmcnt(0)\n" \
  "s_sub_i32 s61, s61, s66\n" \
  "s_add_i32 s59, s61, " STR(T_SNAP) "\n" \
  "s_cmp_gt_u32 s59, 768+" STR(T_SNAP) "\n" \
  "s_cbranch_scc1 LW_c_%=\n" \
  "LV_c_%=:\n" \
  "s_max_i32 s61, s61, " STR(M_C) "\n" \
  "s_lshr_b32 s61, s61, 3\n" \
  "s_lshl_b32 s61, s61, 2\n" \
  "s_sub_u32 s82, s82, s61\n" \
  "s_subb_u32 s83, s83, 0\n" \
  "s_setpc_b64 s[82:83]\n" \
  "s_nop 1\n" \
  "s_nop 1\n" \
  "s_nop 1\n" \
  "s_nop 1\n" \
  "s_nop 1\n" \
  "s_nop 1\n" \
  "s_nop 1\n" \
  "s_nop 1\n" \
  "s_nop 1\n" \
  "s_nop 1\n" \
  "s_nop 1\n" \
  "s_nop 1\n" \
  "s_nop 1\n" \
  "s_nop 1\n" \
  "s_nop 1\n" \
  "s_nop 1\n" \
  "s_nop 1\n" \
  "s_nop 1\n" \
  "s_nop 1\n" \
  "s_nop 1\n" \
  "s_nop 1\n" \
  "s_nop 1\n" \
  "s_nop 1\n" \
  "s_nop 1\n" \
  "s_nop 1\n" \
  "s_nop 1\n" \
  "s_nop 1\n" \
  "s_nop 1\n" \
  "s_nop 1\n" \
  "s_nop 1\n" \
  "s_nop 1\n" \
  "s_nop 1\n" \
  "s_nop 1\n" \
  "s_nop 1\n" \
  "s_nop 1\n" \
  "s_nop 1\n" \
  "s_nop 1\n" \
  "s_nop 1\n" \
  "s_nop 1\n" \
  "s_nop 1\n" \
  "s_nop 1\n" \
  "s_nop 1\n" \
  "s_nop 1\n" \
  "s_nop 1\n" \
  "s_nop 1\n" \
  "s_nop 1\n" \
  "s_nop 1\n" \
  "s_nop 1\n" \
  "s_nop 1\n" \
  "s_nop 1\n" \
  "s_nop 1\n" \
  "s_nop 1\n" \
  "s_nop 1\n" \
  "s_nop 1\n" \
  "s_nop 1\n" \
  "s_nop 1\n" \
  "s_nop 1\n" \
  "s_nop 1\n" \
  "s_nop 1\n" \
  "s_nop 1\n" \
  "s_nop 1\n" \
  "s_nop 1\n" \
  "s_nop 1\n" \
  "s_nop 1\n" \
  "s_nop 1\n" \
  "s_nop 1\n" \
  "s_nop 1\n" \
  "s_nop 1\n" \
  "s_nop 1\n" \
  "s_nop 1\n" \
  "s_nop 1\n" \
  "s_nop 1\n" \
  "s_nop 1\n" \
  "s_nop 1\n" \
  "s_nop 1\n" \
  "s_nop 1\n" \
  "s_nop 1\n" \
  "s_nop 1\n" \
  "s_nop 1\n" \
  "s_nop 1\n" \
  "s_nop 1\n" \
  "s_nop 1\n" \
  "s_nop 1\n" \
  "s_nop 1\n" \
  "s_nop 1\n" \
  "s_nop 1\n" \
  "s_nop 1\n" \
  "s_nop 1\n" \
  "s_nop 1\n" \
  "s_nop 1\n" \
  "s_nop 1\n" \
  "s_nop 1\n" \
  "s_nop 1\n" \
  "s_nop 1\n" \
  "s_nop 1\n" \
  "s_nop 1\n" \
  "LB_c_%=:\n" \
  "buffer_load_dwordx4 v[32:35], v11, s[36:39], s52 offen offset:0 sc1\n" \
  "buffer_load_dwordx4 v[36:39], v11, s[36:39], s52 offen offset:1024 sc1\n" \
  "buffer_load_dwordx4 v[40:43], v11, s[36:39], s52 offen offset:2048 sc1\n" \
  "buffer_load_dwordx4 v[44:47], v11, s[36:39], s52 offen offset:3072 sc1\n" \
  "v_mov_b64 v[48:49], 0\n" \
  "v_mov_b64 v[50:51], 0\n" \
  "v_mov_b64 v[52:53], 0\n" \
  "v_mov_b64 v[54:55], 0\n" \
  "v_mov_b64 v[56:57], 0\n" \
  "v_mov_b64 v[58:59], 0\n" \
  "v_mov_b64 v[60:61], 0\n" \
  "v_mov_b64 v[62:63], 0\n" \
  "v_mov_b64 v[64:65], 0\n" \
  "v_mov_b64 v[66:67], 0\n" \
  "v_mov_b64 v[68:69], 0\n" \
  "v_mov_b64 v[70:71], 0\n" \
  "s_add_u32 s48, s48, 1\n" \
  "s_cmp_lg_u32 s54, 0\n" \
  "s_cbranch_scc1 LS3_%=\n" \
  "s_waitcnt vmcnt(5)\n" \
  "ds_write_b128 v18, v[200:203] offset:0\n" \
  "ds_write_b128 v18, v[204:207] offset:1024\n" \
  "ds_write_b128 v18, v[208:211] offset:2048\n" \
  "ds_write_b128 v18, v[212:215] offset:3072\n" \
  "s_mov_b64 exec, 1\n" \
  "ds_sub_u32 v255, v88 offset:14360\n" \
  "s_mov_b64 exec, -1\n" \
  "LS3_%=:\n" \
  "s_nop " STR(D_K) "\n" \
  "ds_read_b64 v[92:93], v255 offset:14352\n" \
  "s_waitcnt lgkmcnt(0)\n" \
  "v_readfirstlane_b32 s62, v93\n" \
  "v_readfirstlane_b32 s63, v92\n" \
  "s_getpc_b64 s[82:83]\n" \
  "LG_a_%=:\n" \
  "s_sub_u32 s82, s82, LG_a_%=-LB_a_%=\n" \
  "s_subb_u32 s83, s83, 0\n" \
  "s_xor_b32 s62, s62, s48\n" \
  "s_or_b32 s62, s62, s54\n" \
  "s_sub_i32 s59, s63, s64\n" \
  "s_cmp_lt_i32 s59, " STR(F_P) "-300\n" \
  "s_cselect_b32 s62, 1, s62\n" \
  "s_movk_i32 s68, -(" STR(T_LATE) ")\n" \
  "s_cmp_eq_u32 s62, 0\n" \
  "s_cselect_b32 s68, s68, 0x7fffffff\n" \
  "s_cselect_b32 s63, s63, s64\n" \
  "v_mov_b32 v92, s63\n" \
  "v_mov_b32 v93, 0\n" \
  "ds_write_b64 v255, v[92:93] offset:14336\n" \
  "s_waitcnt vmcnt(3)\n" \
  "v_mfma_f32_16x16x32_bf16 v[224:227], v[248:251], v[32:35], 0\n" \
  "s_waitcnt vmcnt(2)\n" \
  "v_mfma_f32_16x16x32_bf16 v[224:227], v[248:251], v[36:39], v[224:227]\n" \
  "s_waitcnt vmcnt(1)\n" \
  "v_mfma_f32_16x16x32_bf16 v[224:227], v[248:251], v[40:43], v[224:227]\n" \
  "s_waitcnt vmcnt(0)\n" \
  "v_mfma_f32_16x16x32_bf16 v[224:227], v[248:251], v[44:47], v[224:227]\n" \
  "s_nop 6\n" \
  "s_memtime s[66:67]\n" \
  "v_cmp_u_f32 vcc, v224, v224\n" \
  "v_add_f32_dpp v224, v224, v224 row_ror:8 row_mask:0xf bank_mask:0xf\n" \
  "v_add_f32_dpp v225, v225, v225 row_ror:8 row_mask:0xf bank_mask:0xf\n" \
  "v_add_f32_dpp v226, v226, v226 row_ror:8 row_mask:0xf bank_mask:0xf\n" \
  "v_add_f32_dpp v227, v227, v227 row_ror:8 row_mask:0xf bank_mask:0xf\n" \
  "v_cvt_pk_bf16_f32 v228, v224, v225\n" \
  "v_cvt_pk_bf16_f32 v229, v226, v227\n" \
  "s_cbranch_vccnz LC_%=\n" \
  "LC2_%=:\n" \
  "v_permlane16_swap_b32 v228, v230\n" \
  "v_permlane16_swap_b32 v229, v231\n" \
  "buffer_store_dwordx4 v[228:231], v12, s[36:39], s53 offen sc1\n" \
  "s_waitcnt lgkmcnt(0)\n" \
  "s_sub_i32 s61, s63, s66\n" \
  "s_cmp_lt_i32 s61, s68\n" \
  "s_cbranch_scc1 LN_%=\n" \
  "LN2_%=:\n" \
  "s_cmp_gt_i32 s61, 768\n" \
  "s_cbranch_scc1 LW_a_%=\n" \
  "LV_a_%=:\n" \
  "s_max_i32 s61, s61, 0\n" \
  "s_lshr_b32 s61, s61, 3\n" \
  "s_lshl_b32 s61, s61, 2\n" \
  "s_sub_u32 s82, s82, s61\n" \
  "s_subb_u32 s83, s83, 0\n" \
  "s_setpc_b64 s[82:83]\n" \
  "LN_%=:\n" \
  "s_mov_b32 s68, s66\n" \
  "s_cmp_lg_u32 s54, 0\n" \
  "s_cbranch_scc1 LFa_%=\n" \
  "LFa2_%=:\n" \
  "LK4_%=:\n" \
  "ds_read_b64 v[92:93], v255 offset:14352\n" \
  "s_waitcnt lgkmcnt(0)\n" \
  "v_readfirstlane_b32 s62, v93\n" \
  "v_readfirstlane_b32 s63, v92\n" \
  "s_cmp_lg_u32 s62, s48\n" \
  "s_cbranch_scc1 LK4_%=\n" \
  "s_sub_i32 s59, s63, s64\n" \
  "s_sub_i32 s59, s59, " STR(F_P) "-300\n" \
  "s_sub_i32 s60, s63, s68\n" \
  "s_add_i32 s60, s60, " STR(T_LATE) "\n" \
  "s_min_i32 s59, s59, s60\n" \
  "s_sub_u32 s63, s63, s95\n" \
  "LN3_%=:\n" \
  "s_add_u32 s63, s63, s95\n" \
  "s_add_i32 s59, s59, s95\n" \
  "s_cmp_lt_i32 s59, s95\n" \
  "s_cbranch_scc1 LN3_%=\n" \
  "v_mov_b32 v92, s63\n" \
  "v_mov_b32 v93, 0\n" \
  "ds_write_b64 v255, v[92:93] offset:14336\n" \
  "s_memtime s[66:67]\n" \
  "s_waitcnt lgkmcnt(0)\n" \
  "s_sub_i32 s61, s63, s66\n" \
  "s_add_i32 s61, s61, " STR(O_N) "\n" \
  "s_branch LN2_%=\n" \
  "LO_%=:\n" \
  "s_cmp_eq_u32 %7, 1\n" \
  "s_waitcnt lgkmcnt(0)\n" \
  "s_cbranch_scc1 LK_%=\n" \
  "s_add_u32 s59, s48, 1\n" \
  "v_mov_b32 v28, s59\n" \
  "buffer_store_dword v28, v24, s[44:47], 0 offen sc1\n" \
  "v_mov_b64 v[48:49], 0\n" \
  "v_mov_b64 v[50:51], 0\n" \
  "v_mov_b64 v[52:53], 0\n" \
  "v_mov_b64 v[54:55], 0\n" \
  "v_mov_b64 v[56:57], 0\n" \
  "v_mov_b64 v[58:59], 0\n" \
  "v_mov_b64 v[60:61], 0\n" \
  "v_mov_b64 v[62:63], 0\n" \
  "v_mov_b64 v[64:65], 0\n" \
  "v_mov_b64 v[66:67], 0\n" \
  "v_mov_b64 v[68:69], 0\n" \
  "v_mov_b64 v[70:71], 0\n" \
  "s_add_u32 s48, s48, 1\n" \
  "s_cmp_lg_u32 s54, 0\n" \
  "s_cbranch_scc1 LFb_%=\n" \
  "s_waitcnt vmcnt(2)\n" \
  "ds_write_b128 v18, v[200:203] offset:0\n" \
  "ds_write_b128 v18, v[204:207] offset:1024\n" \
  "ds_write_b128 v18, v[208:211] offset:2048\n" \
  "ds_write_b128 v18, v[212:215] offset:3072\n" \
  "s_mov_b64 exec, 1\n" \
  "ds_sub_u32 v255, v88 offset:14360\n" \
  "s_mov_b64 exec, -1\n" \
  "LFb2_%=:\n" \
  "s_lshl_b32 s60, s48, 2\n" \
  "LHb_%=:\n" \
  "ds_read_b32 v27, v255 offset:14360\n" \
  "s_waitcnt lgkmcnt(0)\n" \
  "v_readfirstlane_b32 s59, v27\n" \
  "s_cmp_lt_u32 s59, s60\n" \
  "s_cbranch_scc1 LHb_%=\n" \
  "ds_read_b64_tr_b16 v[136:137], v20 offset:0\n" \
  "ds_read_b64_tr_b16 v[138:139], v21 offset:0\n" \
  "ds_read_b64_tr_b16 v[152:153], v20 offset:8\n" \
  "ds_read_b64_tr_b16 v[154:155], v21 offset:8\n" \
  "ds_read_b64_tr_b16 v[140:141], v20 offset:2048\n" \
  "ds_read_b64_tr_b16 v[142:143], v21 offset:2048\n" \
  "ds_read_b64_tr_b16 v[156:157], v20 offset:2056\n" \
  "ds_read_b64_tr_b16 v[158:159], v21 offset:2056\n" \
  "ds_read_b64_tr_b16 v[144:145], v20 offset:4096\n" \
  "ds_read_b64_tr_b16 v[146:147], v21 offset:4096\n" \
  "ds_read_b64_tr_b16 v[160:161], v20 offset:4104\n" \
  "ds_read_b64_tr_b16 v[162:163], v21 offset:4104\n" \
  "ds_read_b64_tr_b16 v[148:149], v20 offset:6144\n" \
  "ds_read_b64_tr_b16 v[150:151], v21 offset:6144\n" \
  "ds_read_b64_tr_b16 v[164:165], v20 offset:6152\n" \
  "ds_read_b64_tr_b16 v[166:167], v21 offset:6152\n" \
  "ds_read_b128 v[104:107], v84 offset:0\n" \
  "ds_read_b128 v[108:111], v85 offset:0\n" \
  "ds_read_b128 v[112:115], v86 offset:0\n" \
  "ds_read_b128 v[116:119], v87 offset:0\n" \
  "ds_read_b128 v[120:123], v84 offset:4096\n" \
  "ds_read_b128 v[124:127], v85 offset:4096\n" \
  "ds_read_b128 v[128:131], v86 offset:4096\n" \
  "ds_read_b128 v[132:135], v87 offset:4096\n" \
  "s_waitcnt lgkmcnt(0)\n" \
  "s_branch LM_%=\n" \
  "LK_%=:\n" \
  "s_mov_b32 s94, s63\n" \
  "s_memtime s[72:73]\n" \
  "s_memrealtime s[74:75]\n" \
  "s_nop 1\n" \
  "s_memrealtime s[76:77]\n" \
  "s_nop 1\n" \
  "s_memrealtime s[78:79]\n" \
  "s_nop 1\n" \
  "s_memrealtime s[80:81]\n" \
  "s_nop 1\n" \
  "s_memrealtime s[82:83]\n" \
  "s_nop 1\n" \
  "s_memrealtime s[84:85]\n" \
  "s_nop 1\n" \
  "s_memrealtime s[86:87]\n" \
  "s_nop 1\n" \
  "s_memrealtime s[88:89]\n" \
  "s_nop 1\n" \
  "s_memrealtime s[90:91]\n" \
  "s_mov_b32 s59, 0\n" \
  "s_waitcnt lgkmcnt(0)\n" \
  "s_cmp_eq_u32 s76, s74\n" \
  "s_addc_u32 s59, s59, 0\n" \
  "s_cmp_eq_u32 s78, s74\n" \
  "s_addc_u32 s59, s59, 0\n" \
  "s_cmp_eq_u32 s80, s74\n" \
  "s_addc_u32 s59, s59, 0\n" \
  "s_cmp_eq_u32 s82, s74\n" \
  "s_addc_u32 s59, s59, 0\n" \
  "s_cmp_eq_u32 s84, s74\n" \
  "s_addc_u32 s59, s59, 0\n" \
  "s_cmp_eq_u32 s86, s74\n" \
  "s_addc_u32 s59, s59, 0\n" \
  "s_cmp_eq_u32 s88, s74\n" \
  "s_addc_u32 s59, s59, 0\n" \
  "s_cmp_eq_u32 s90, s74\n" \
  "s_addc_u32 s59, s59, 0\n" \
  "s_mul_i32 s59, s59, 12\n" \
  "s_add_u32 s68, s72, s59\n" \
  "s_add_u32 s59, s74, 4\n" \
  "s_mul_i32 s69, s59, 24\n" \
  "s_sub_u32 s59, s68, s56\n" \
  "s_sub_u32 s60, s95, " STR(T_Q) "\n" \
  "s_lshl_b32 s60, s60, 2\n" \
  "s_add_u32 s60, s60, " STR(F_P) "+650\n" \
  "s_cmp_gt_u32 s59, s60\n" \
  "s_cselect_b32 s59, " STR(E_UP) ", -(" STR(E_DOWN) ")\n" \
  "s_cmp_gt_u32 s48, " STR(E_N) "\n" \
  "s_cselect_b32 s59, s59, -(" STR(E_0) "/(" STR(E_N) "+1))\n" \
  "s_add_i32 s29, s29, s59\n" \
  "s_max_i32 s29, s29, 0\n" \
  "s_min_i32 s29, s29, 1500\n" \
  "s_sub_u32 s60, s69, s62\n" \
  "s_sub_u32 s59, s68, s56\n" \
  "s_mov_b32 s62, s69\n" \
  "s_mov_b32 s56, s68\n" \
  "v_cvt_f32_u32 v27, s59\n" \
  "v_cvt_f32_u32 v28, s60\n" \
  "v_rcp_f32 v26, v27\n" \
  "v_rcp_f32 v19, v28\n" \
  "v_mul_f32 v26, v26, v28\n" \
  "v_mul_f32 v95, " STR(DECAY) ", v95\n" \
  "v_mul_f32 v19, v19, v27\n" \
  "v_max_f32 v95, v95, v26\n" \
  "v_cvt_f32_u32 v28, " STR(F_P) "\n" \
  "v_mul_f32 v27, v95, v28\n" \
  "v_cvt_u32_f32 v27, v27\n" \
  "v_min_u32 v27, 0x7fff, v27\n" \
  "s_nop 0\n" \
  "v_readfirstlane_b32 s63, v27\n" \
  "s_waitcnt vmcnt(5)\n" \
  "LK2_%=:\n" \
  "v_max_u32 v26, v92, v93\n" \
  "v_cmp_eq_u32 vcc, -1, v26\n" \
  "v_subrev_u32 v25, s62, v92\n" \
  "v_subrev_u32 v26, s94, v93\n" \
  "s_nop 0\n" \
  "v_max_i32_dpp v25, v25, v25 quad_perm:[1,0,3,2] row_mask:0xf bank_mask:0xf\n" \
  "v_max_i32_dpp v26, v26, v26 quad_perm:[1,0,3,2] row_mask:0xf bank_mask:0xf\n" \
  "s_cbranch_vccnz LK3_%=\n" \
  "v_max_i32_dpp v25, v25, v25 quad_perm:[2,3,0,1] row_mask:0xf bank_mask:0xf\n" \
  "v_max_i32_dpp v26, v26, v26 quad_perm:[2,3,0,1] row_mask:0xf bank_mask:0xf\n" \
  "LK5_%=:\n" \
  "v_cvt_f32_i32 v25, v25\n" \
  "s_add_u32 s59, s48, 1\n" \
  "v_mov_b32 v93, s59\n" \
  "v_mul_f32 v25, v25, v19\n" \
  "v_cvt_i32_f32 v25, v25\n" \
  "v_add_u32 v92, s68, v25\n" \
  "ds_write_b64 v255, v[92:93] offset:14352\n" \
  "v_max_i32 v26, 0, v26\n" \
  "v_min_i32 v26, 2000, v26\n" \
  "ds_write_b32 v255, v26 offset:14344\n" \
  "v_mov_b64 v[48:49], 0\n" \
  "v_mov_b64 v[50:51], 0\n" \
  "v_mov_b64 v[52:53], 0\n" \
  "v_mov_b64 v[54:55], 0\n" \
  "v_mov_b64 v[56:57], 0\n" \
  "v_mov_b64 v[58:59], 0\n" \
  "v_mov_b64 v[60:61], 0\n" \
  "v_mov_b64 v[62:63], 0\n" \
  "v_mov_b64 v[64:65], 0\n" \
  "v_mov_b64 v[66:67], 0\n" \
  "v_mov_b64 v[68:69], 0\n" \
  "v_mov_b64 v[70:71], 0\n" \
  "s_add_u32 s48, s48, 1\n" \
  "s_cmp_lg_u32 s54, 0\n" \
  "s_cbranch_scc1 LFc_%=\n" \
  "s_waitcnt vmcnt(1)\n" \
  "ds_write_b128 v18, v[200:203] offset:0\n" \
  "ds_write_b128 v18, v[204:207] offset:1024\n" \
  "ds_write_b128 v18, v[208:211] offset:2048\n" \
  "ds_write_b128 v18, v[212:215] offset:3072\n" \
  "s_mov_b64 exec, 1\n" \
  "ds_sub_u32 v255, v88 offset:14360\n" \
  "s_mov_b64 exec, -1\n" \
  "LFc2_%=:\n" \
  "s_lshl_b32 s60, s48, 2\n" \
  "LHc_%=:\n" \
  "ds_read_b32 v27, v255 offset:14360\n" \
  "s_waitcnt lgkmcnt(0)\n" \
  "v_readfirstlane_b32 s59, v27\n" \
  "s_cmp_lt_u32 s59, s60\n" \
  "s_cbranch_scc1 LHc_%=\n" \
  "ds_read_b64_tr_b16 v[136:137], v20 offset:0\n" \
  "ds_read_b64_tr_b16 v[138:139], v21 offset:0\n" \
  "ds_read_b64_tr_b16 v[152:153], v20 offset:8\n" \
  "ds_read_b64_tr_b16 v[154:155], v21 offset:8\n" \
  "ds_read_b64_tr_b16 v[140:141], v20 offset:2048\n" \
  "ds_read_b64_tr_b16 v[142:143], v21 offset:2048\n" \
  "ds_read_b64_tr_b16 v[156:157], v20 offset:2056\n" \
  "ds_read_b64_tr_b16 v[158:159], v21 offset:2056\n" \
  "ds_read_b64_tr_b16 v[144:145], v20 offset:4096\n" \
  "ds_read_b64_tr_b16 v[146:147], v21 offset:4096\n" \
  "ds_read_b64_tr_b16 v[160:161], v20 offset:4104\n" \
  "ds_read_b64_tr_b16 v[162:163], v21 offset:4104\n" \
  "ds_read_b64_tr_b16 v[148:149], v20 offset:6144\n" \
  "ds_read_b64_tr_b16 v[150:151], v21 offset:6144\n" \
  "ds_read_b64_tr_b16 v[164:165], v20 offset:6152\n" \
  "ds_read_b64_tr_b16 v[166:167], v21 offset:6152\n" \
  "ds_read_b128 v[104:107], v84 offset:0\n" \
  "ds_read_b128 v[108:111], v85 offset:0\n" \
  "ds_read_b128 v[112:115], v86 offset:0\n" \
  "ds_read_b128 v[116:119], v87 offset:0\n" \
  "ds_read_b128 v[120:123], v84 offset:4096\n" \
  "ds_read_b128 v[124:127], v85 offset:4096\n" \
  "ds_read_b128 v[128:131], v86 offset:4096\n" \
  "ds_read_b128 v[132:135], v87 offset:4096\n" \
  "s_waitcnt lgkmcnt(0)\n" \
  "s_branch LM_%=\n" \
  "LR2_%=:\n" \
  "s_sleep 16\n" \
  "buffer_load_dwordx4 v[68:71], off, s[44:47], s61 sc1\n" \
  "s_waitcnt vmcnt(0)\n" \
  "s_add_u32 s58, s58, 1\n" \
  "s_cmp_lt_u32 s58, " STR(SPIN_CAP) "\n" \
  "s_cbranch_scc1 LR1_%=\n" \
  "v_mov_b32 v27, " STR(CW_DEAD) "*4\n" \
  "v_mov_b32 v28, 1\n" \
  "buffer_store_dword v28, v27, s[44:47], 0 offen sc1\n" \
  "s_branch LR3_%=\n" \
  ".p2align 6\n" \
  "LM_%=:\n" \
  "s_barrier\n" \
  "buffer_load_dwordx4 v[32:35], v80, s[36:39], s49 offen sc1\n" \
  "ds_read_b32 v236, v255 offset:14336\n" \
  "v_mfma_f32_16x16x32_bf16 v[184:187], v[136:139], a[192:195], 0\n" \
  "s_xor_b32 s51, s51, 1052672\n" \
  "s_add_u32 s53, s53, 1<<17\n" \
  "v_mfma_f32_16x16x32_bf16 v[188:191], v[152:155], a[192:195], 0\n" \
  "s_and_b32 s53, s53, 0x60000\n" \
  "s_lshl_b32 s50, s48, 19\n" \
  "v_mfma_f32_16x16x32_bf16 v[192:195], v[136:139], a[208:211], 0\n" \
  "s_add_u32 s50, s50, " STR(EXP_B) "<<21\n" \
  "s_xor_b32 s50, s50, s93\n" \
  "buffer_load_dwordx4 v[36:39], v81, s[36:39], s49 offen sc1\n" \
  "v_mfma_f32_16x16x32_bf16 v[196:199], v[152:155], a[208:211], 0\n" \
  "s_and_b32 s59, s48, 7\n" \
  "s_mul_i32 s59, s59, 0x4800\n" \
  "v_mfma_f32_16x16x32_bf16 v[184:187], v[140:143], a[196:199], v[184:187]\n" \
  "s_and_b32 s59, s59, 0x7000\n" \
  "s_xor_b32 s50, s50, s59\n" \
  "v_mfma_f32_16x16x32_bf16 v[188:191], v[156:159], a[196:199], v[188:191]\n" \
  "s_add_u32 s60, s48, 4\n" \
  "s_lshl_b32 s54, s60, 19\n" \
  "buffer_load_dwordx4 v[40:43], v82, s[36:39], s49 offen sc1\n" \
  "v_mfma_f32_16x16x32_bf16 v[192:195], v[140:143], a[212:215], v[192:195]\n" \
  "s_add_u32 s54, s54, " STR(EXP_B) "<<21\n" \
  "s_xor_b32 s54, s54, s93\n" \
  "v_mfma_f32_16x16x32_bf16 v[196:199], v[156:159], a[212:215], v[196:199]\n" \
  "s_and_b32 s59, s60, 7\n" \
  "s_mul_i32 s59, s59, 0x4800\n" \
  "v_mfma_f32_16x16x32_bf16 v[184:187], v[144:147], a[200:203], v[184:187]\n" \
  "s_waitcnt lgkmcnt(0)\n" \
  "s_sub_u32 s61, " STR(F_P) ", s56\n" \
  "s_add_u32 s61, s61, s29\n" \
  "s_and_b32 s59, s59, 0x7000\n" \
  "s_xor_b32 s54, s54, s59\n" \
  "buffer_load_dwordx4 v[44:47], v83, s[36:39], s49 offen sc1\n" \
  "v_mfma_f32_16x16x32_bf16 v[188:191], v[160:163], a[200:203], v[188:191]\n" \
  "v_add_u32 v236, s61, v236\n" \
  "s_xor_b32 s59, %7, 3\n" \
  "s_lshl_b32 s59, s59, 29\n" \
  "v_mfma_f32_16x16x32_bf16 v[192:195], v[144:147], a[216:219], v[192:195]\n" \
  "v_cvt_f32_i32 v236, v236\n" \
  "s_xor_b32 s60, s53, 0x40000\n" \
  "s_or_b32 s59, s59, s60\n" \
  "v_mfma_f32_16x16x32_bf16 v[196:199], v[160:163], a[216:219], v[196:199]\n" \
  "s_add_u32 s61, s63, s29\n" \
  "v_mov_b32 v237, s61\n" \
  "v_mfma_f32_16x16x32_bf16 v[184:187], v[148:151], a[204:207], v[184:187]\n" \
  "v_mul_f32 v236, v236, v95\n" \
  "v_mfma_f32_16x16x32_bf16 v[188:191], v[164:167], a[204:207], v[188:191]\n" \
  "v_cvt_i32_f32 v236, v236\n" \
  "v_mfma_f32_16x16x32_bf16 v[192:195], v[148:151], a[220:223], v[192:195]\n" \
  "v_add_u32 v236, s62, v236\n" \
  "v_mfma_f32_16x16x32_bf16 v[196:199], v[164:167], a[220:223], v[196:199]\n" \
  "v_min_u32 v236, -2, v236\n" \
  "v_add_u32 v26, s59, v12\n" \
  "s_branch LS1_%=\n" \
  "LA_%=:\n" \
  "ds_read_b64 v[92:93], v255 offset:14336\n" \
  "v_max3_u32 v25, v32, v33, v34\n" \
  "v_max3_u32 v25, v25, v35, v36\n" \
  "v_max3_u32 v25, v25, v37, v38\n" \
  "v_max3_u32 v25, v25, v39, v40\n" \
  "v_max3_u32 v25, v25, v41, v42\n" \
  "v_max3_u32 v25, v25, v43, v44\n" \
  "v_max3_u32 v25, v25, v45, v46\n" \
  "v_max_u32 v25, v25, v47\n" \
  "v_cmp_eq_u32 vcc, -1, v25\n" \
  "s_cbranch_vccz LA4_%=\n" \
  "s_add_u32 s58, s58, 1\n" \
  "s_cmp_lt_u32 s58, " STR(SPIN_CAP) "\n" \
  "s_cbranch_scc1 LA1_%=\n" \
  "v_mov_b32 v27, " STR(CW_DEAD) "*4\n" \
  "v_mov_b32 v28, 1\n" \
  "buffer_store_dword v28, v27, s[44:47], 0 offen sc1\n" \
  "s_branch LA4_%=\n" \
  "LA1_%=:\n" \
  "s_waitcnt lgkmcnt(0)\n" \
  "v_readfirstlane_b32 s64, v92\n" \
  "s_memtime s[66:67]\n" \
  "s_waitcnt lgkmcnt(0)\n" \
  "s_sub_u32 s59, " STR(O_A) ", s66\n" \
  "LQ_ra_%=:\n" \
  "s_add_u32 s64, s64, s95\n" \
  "s_add_i32 s61, s64, s59\n" \
  "s_cmp_lt_i32 s61, -" STR(T_TOL) "\n" \
  "s_cbranch_scc1 LQ_ra_%=\n" \
  "s_max_i32 s61, s61, 0\n" \
  "s_min_i32 s61, s61, 4000\n" \
  "s_bitcmp0_b32 s61, 4\n" \
  "s_cbranch_scc1 LX_ra_%=\n" \
  "s_nop 8\n" \
  "LX_ra_%=:\n" \
  "s_lshr_b32 s61, s61, 5\n" \
  "s_cbranch_scc0 LB_ra_%=\n" \
  ".p2align 4\n" \
  "LY_ra_%=:\n" \
  "s_sub_u32 s61, s61, 1\n" \
  "s_cmp_lg_u32 s61, 0\n" \
  "s_cbranch_scc1 LY_ra_%=\n" \
  "LB_ra_%=:\n" \
  "buffer_load_dwordx4 v[32:35], v80, s[36:39], s49 offen sc1\n" \
  "s_nop 11\n" \
  "buffer_load_dwordx4 v[36:39], v81, s[36:39], s49 offen sc1\n" \
  "s_nop 11\n" \
  "buffer_load_dwordx4 v[40:43], v82, s[36:39], s49 offen sc1\n" \
  "s_nop 11\n" \
  "buffer_load_dwordx4 v[44:47], v83, s[36:39], s49 offen sc1\n" \
  "v_mov_b64 v[48:49], 0\n" \
  "v_mov_b64 v[50:51], 0\n" \
  "v_mov_b64 v[52:53], 0\n" \
  "v_mov_b64 v[54:55], 0\n" \
  "v_mov_b64 v[56:57], 0\n" \
  "v_mov_b64 v[58:59], 0\n" \
  "v_mov_b64 v[60:61], 0\n" \
  "v_mov_b64 v[62:63], 0\n" \
  "v_mov_b64 v[64:65], 0\n" \
  "v_mov_b64 v[66:67], 0\n" \
  "v_mov_b64 v[68:69], 0\n" \
  "v_mov_b64 v[70:71], 0\n" \
  "v_sub_u32 v27, s64, v92\n" \
  "ds_max_u32 v255, v27 offset:14340\n" \
  "s_branch LS1_%=\n" \
  "LW_b_%=:\n" \
  "s_cmp_ge_i32 s61, -(" STR(T_SNAP) ")\n" \
  "s_cbranch_scc1 LU_b_%=\n" \
  "s_add_u32 s64, s64, s95\n" \
  "s_add_i32 s61, s61, s95\n" \
  "s_sub_i32 s61, s61, 48\n" \
  "s_branch LW_b_%=\n" \
  "LU_b_%=:\n" \
  "s_min_i32 s61, s61, 4000\n" \
  "s_sub_i32 s61, s61, 40\n" \
  ".p2align 4\n" \
  "LY_b_%=:\n" \
  "s_sub_i32 s61, s61, 32\n" \
  "s_cmp_gt_i32 s61, 704\n" \
  "s_cbranch_scc1 LY_b_%=\n" \
  "s_branch LV_b_%=\n" \
  "LBE_%=:\n" \
  "v_max3_u32 v26, v32, v33, v34\n" \
  "v_max3_u32 v26, v26, v35, v36\n" \
  "v_max3_u32 v26, v26, v37, v38\n" \
  "v_max3_u32 v26, v26, v39, v40\n" \
  "v_max3_u32 v26, v26, v41, v42\n" \
  "v_max3_u32 v26, v26, v43, v44\n" \
  "v_max3_u32 v26, v26, v45, v46\n" \
  "v_max_u32 v26, v26, v47\n" \
  "v_cmp_eq_u32 vcc, -1, v26\n" \
  "s_cbranch_vccz LBH_%=\n" \
  "s_add_u32 s58, s58, 1\n" \
  "s_cmp_lt_u32 s58, " STR(SPIN_CAP) "\n" \
  "s_cbranch_scc1 LB1_%=\n" \
  "v_mov_b32 v27, " STR(CW_DEAD) "*4\n" \
  "v_mov_b32 v28, 1\n" \
  "buffer_store_dword v28, v27, s[44:47], 0 offen sc1\n" \
  "s_branch LBI_%=\n" \
  "LBI_%=:\n" \
  "v_mov_b32 v27, v25\n" \
  "s_nop 1\n" \
  "s_branch LBH_%=\n" \
  "LBF_%=:\n" \
  "v_max3_u32 v25, v32, v33, v34\n" \
  "v_max3_u32 v25, v25, v35, v36\n" \
  "v_max3_u32 v25, v25, v37, v38\n" \
  "v_max3_u32 v25, v25, v39, v40\n" \
  "v_max3_u32 v25, v25, v41, v42\n" \
  "v_max3_u32 v25, v25, v43, v44\n" \
  "v_max3_u32 v25, v25, v45, v46\n" \
  "v_max_u32 v25, v25, v47\n" \
  "v_max3_u32 v25, v25, v96, v97\n" \
  "v_max3_u32 v25, v25, v98, v99\n" \
  "v_max3_u32 v25, v25, v100, v101\n" \
  "v_max3_u32 v25, v25, v102, v103\n" \
  "v_cmp_eq_u32 vcc, -1, v25\n" \
  "s_cbranch_vccz LBG_%=\n" \
  "s_add_u32 s58, s58, 1\n" \
  "s_cmp_lt_u32 s58, " STR(SPIN_CAP) "\n" \
  "s_cbranch_scc1 LB1_%=\n" \
  "v_mov_b32 v27, " STR(CW_DEAD) "*4\n" \
  "v_mov_b32 v28, 1\n" \
  "buffer_store_dword v28, v27, s[44:47], 0 offen sc1\n" \
  "s_branch LBG_%=\n" \
  "LB1_%=:\n" \
  "s_waitcnt vmcnt(0)\n" \
  "v_mov_b64 v[184:185], 0\n" \
  "v_mov_b64 v[186:187], 0\n" \
  "v_mov_b64 v[188:189], 0\n" \
  "v_mov_b64 v[190:191], 0\n" \
  "v_mov_b64 v[192:193], 0\n" \
  "v_mov_b64 v[194:195], 0\n" \
  "v_mov_b64 v[196:197], 0\n" \
  "v_mov_b64 v[198:199], 0\n" \
  "s_add_u32 s60, s95, " STR(F_B) "+" STR(O_B) "-" STR(T_Q) "\n" \
  "s_memtime s[66:67]\n" \
  "s_waitcnt lgkmcnt(0)\n" \
  "s_sub_u32 s59, s60, s66\n" \
  "LQ_rb_%=:\n" \
  "s_add_u32 s64, s64, s95\n" \
  "s_add_i32 s61, s64, s59\n" \
  "s_cmp_lt_i32 s61, -" STR(T_TOL) "\n" \
  "s_cbranch_scc1 LQ_rb_%=\n" \
  "s_max_i32 s61, s61, 0\n" \
  "s_min_i32 s61, s61, 4000\n" \
  "s_bitcmp0_b32 s61, 4\n" \
  "s_cbranch_scc1 LX_rb_%=\n" \
  "s_nop 8\n" \
  "LX_rb_%=:\n" \
  "s_lshr_b32 s61, s61, 5\n" \
  "s_cbranch_scc0 LB_rb_%=\n" \
  ".p2align 4\n" \
  "LY_rb_%=:\n" \
  "s_sub_u32 s61, s61, 1\n" \
  "s_cmp_lg_u32 s61, 0\n" \
  "s_cbranch_scc1 LY_rb_%=\n" \
  "LB_rb_%=:\n" \
  "buffer_load_dwordx4 v[32:35], v80, s[36:39], s50 offen sc1\n" \
  "s_nop 11\n" \
  "buffer_load_dwordx4 v[36:39], v81, s[36:39], s50 offen sc1\n" \
  "s_nop 11\n" \
  "buffer_load_dwordx4 v[40:43], v82, s[36:39], s50 offen sc1\n" \
  "s_nop 11\n" \
  "buffer_load_dwordx4 v[44:47], v83, s[36:39], s50 offen sc1\n" \
  "s_nop 11\n" \
  "buffer_load_dwordx4 v[96:99], v17, s[36:39], s50 offen offset:0 sc1\n" \
  "s_nop 11\n" \
  "buffer_load_dwordx4 v[100:103], v17, s[36:39], s50 offen offset:1024 sc1\n" \
  "buffer_store_dword v27, v88, s[36:39], 0 offen sc1\n" \
  "s_mov_b32 vcc_lo, 0x33333333\n" \
  "s_mov_b32 vcc_hi, 0x33333333\n" \
  "s_branch LS2_%=\n" \
  "LW_c_%=:\n" \
  "s_cmp_ge_i32 s61, -(" STR(T_SNAP) ")\n" \
  "s_cbranch_scc1 LU_c_%=\n" \
  "s_add_u32 s64, s64, s95\n" \
  "s_add_i32 s61, s61, s95\n" \
  "s_sub_i32 s61, s61, 48\n" \
  "s_branch LW_c_%=\n" \
  "LU_c_%=:\n" \
  "s_min_i32 s61, s61, 4000\n" \
  "s_sub_i32 s61, s61, 40\n" \
  ".p2align 4\n" \
  "LY_c_%=:\n" \
  "s_sub_i32 s61, s61, 32\n" \
  "s_cmp_gt_i32 s61, 704\n" \
  "s_cbranch_scc1 LY_c_%=\n" \
  "s_branch LV_c_%=\n" \
  "LW_a_%=:\n" \
  "s_min_i32 s61, s61, 4000\n" \
  "s_sub_i32 s61, s61, 24\n" \
  ".p2align 4\n" \
  "LY_a_%=:\n" \
  "s_sub_i32 s61, s61, 32\n" \
  "s_cmp_gt_i32 s61, 704\n" \
  "s_cbranch_scc1 LY_a_%=\n" \
  "s_branch LV_a_%=\n" \
  "LFa_%=:\n" \
  "buffer_load_dword v23, v22, s[44:47], 0 offen sc1\n" \
  "s_waitcnt vmcnt(0)\n" \
  "v_cmp_ge_u32 vcc, s48, v23\n" \
  "s_cbranch_vccz LFa3_%=\n" \
  "s_sleep 2\n" \
  "s_add_u32 s58, s58, 1\n" \
  "s_cmp_lt_u32 s58, " STR(SPIN_CAP) "\n" \
  "s_cbranch_scc1 LFa_%=\n" \
  "v_mov_b32 v27, " STR(CW_DEAD) "*4\n" \
  "v_mov_b32 v28, 1\n" \
  "buffer_store_dword v28, v27, s[44:47], 0 offen sc1\n" \
  "s_branch LFa3_%=\n" \
  "LFa3_%=:\n" \
  "buffer_load_dwordx4 v[200:203], v247, s[40:43], s55 offen offset:0 sc1\n" \
  "buffer_load_dwordx4 v[204:207], v247, s[40:43], s55 offen offset:1024 sc1\n" \
  "buffer_load_dwordx4 v[208:211], v247, s[40:43], s55 offen offset:2048 sc1\n" \
  "buffer_load_dwordx4 v[212:215], v247, s[40:43], s55 offen offset:3072 sc1\n" \
  "s_waitcnt vmcnt(0)\n" \
  "ds_write_b128 v18, v[200:203] offset:0\n" \
  "ds_write_b128 v18, v[204:207] offset:1024\n" \
  "ds_write_b128 v18, v[208:211] offset:2048\n" \
  "ds_write_b128 v18, v[212:215] offset:3072\n" \
  "s_mov_b64 exec, 1\n" \
  "ds_sub_u32 v255, v88 offset:14360\n" \
  "s_mov_b64 exec, -1\n" \
  "s_branch LFa2_%=\n" \
  "LC_%=:\n" \
  "v_max3_u32 v25, v32, v33, v34\n" \
  "v_max3_u32 v25, v25, v35, v36\n" \
  "v_max3_u32 v25, v25, v37, v38\n" \
  "v_max3_u32 v25, v25, v39, v40\n" \
  "v_max3_u32 v25, v25, v41, v42\n" \
  "v_max3_u32 v25, v25, v43, v44\n" \
  "v_max3_u32 v25, v25, v45, v46\n" \
  "v_max_u32 v25, v25, v47\n" \
  "v_cmp_eq_u32 vcc, -1, v25\n" \
  "s_cbranch_vccz LC2_%=\n" \
  "s_add_u32 s58, s58, 1\n" \
  "s_cmp_lt_u32 s58, " STR(SPIN_CAP) "\n" \
  "s_cbranch_scc1 LC1_%=\n" \
  "v_mov_b32 v27, " STR(CW_DEAD) "*4\n" \
  "v_mov_b32 v28, 1\n" \
  "buffer_store_dword v28, v27, s[44:47], 0 offen sc1\n" \
  "s_branch LC2_%=\n" \
  "LC1_%=:\n" \
  "s_add_u32 s60, s96, " STR(O_C) "\n" \
  "s_memtime s[66:67]\n" \
  "s_waitcnt lgkmcnt(0)\n" \
  "s_sub_u32 s59, s60, s66\n" \
  "LQ_rc_%=:\n" \
  "s_add_u32 s64, s64, s95\n" \
  "s_add_i32 s61, s64, s59\n" \
  "s_cmp_lt_i32 s61, -" STR(T_TOL) "\n" \
  "s_cbranch_scc1 LQ_rc_%=\n" \
  "s_max_i32 s61, s61, 0\n" \
  "s_min_i32 s61, s61, 4000\n" \
  "s_bitcmp0_b32 s61, 4\n" \
  "s_cbranch_scc1 LX_rc_%=\n" \
  "s_nop 8\n" \
  "LX_rc_%=:\n" \
  "s_lshr_b32 s61, s61, 5\n" \
  "s_cbranch_scc0 LB_rc_%=\n" \
  ".p2align 4\n" \
  "LY_rc_%=:\n" \
  "s_sub_u32 s61, s61, 1\n" \
  "s_cmp_lg_u32 s61, 0\n" \
  "s_cbranch_scc1 LY_rc_%=\n" \
  "LB_rc_%=:\n" \
  "buffer_load_dwordx4 v[32:35], v11, s[36:39], s52 offen offset:0 sc1\n" \
  "buffer_load_dwordx4 v[36:39], v11, s[36:39], s52 offen offset:1024 sc1\n" \
  "buffer_load_dwordx4 v[40:43], v11, s[36:39], s52 offen offset:2048 sc1\n" \
  "buffer_load_dwordx4 v[44:47], v11, s[36:39], s52 offen offset:3072 sc1\n" \
  "s_branch LS3_%=\n" \
  "LFb_%=:\n" \
  "buffer_load_dword v23, v22, s[44:47], 0 offen sc1\n" \
  "s_waitcnt vmcnt(0)\n" \
  "v_cmp_ge_u32 vcc, s48, v23\n" \
  "s_cbranch_vccz LFb3_%=\n" \
  "s_sleep 2\n" \
  "s_add_u32 s58, s58, 1\n" \
  "s_cmp_lt_u32 s58, " STR(SPIN_CAP) "\n" \
  "s_cbranch_scc1 LFb_%=\n" \
  "v_mov_b32 v27, " STR(CW_DEAD) "*4\n" \
  "v_mov_b32 v28, 1\n" \
  "buffer_store_dword v28, v27, s[44:47], 0 offen sc1\n" \
  "s_branch LFb3_%=\n" \
  "LFb3_%=:\n" \
  "buffer_load_dwordx4 v[200:203], v247, s[40:43], s55 offen offset:0 sc1\n" \
  "buffer_load_dwordx4 v[204:207], v247, s[40:43], s55 offen offset:1024 sc1\n" \
  "buffer_load_dwordx4 v[208:211], v247, s[40:43], s55 offen offset:2048 sc1\n" \
  "buffer_load_dwordx4 v[212:215], v247, s[40:43], s55 offen offset:3072 sc1\n" \
  "s_waitcnt vmcnt(0)\n" \
  "ds_write_b128 v18, v[200:203] offset:0\n" \
  "ds_write_b128 v18, v[204:207] offset:1024\n" \
  "ds_write_b128 v18, v[208:211] offset:2048\n" \
  "ds_write_b128 v18, v[212:215] offset:3072\n" \
  "s_mov_b64 exec, 1\n" \
  "ds_sub_u32 v255, v88 offset:14360\n" \
  "s_mov_b64 exec, -1\n" \
  "s_branch LFb2_%=\n" \
  "LFc_%=:\n" \
  "buffer_load_dword v23, v22, s[44:47], 0 offen sc1\n" \
  "s_waitcnt vmcnt(0)\n" \
  "v_cmp_ge_u32 vcc, s48, v23\n" \
  "s_cbranch_vccz LFc3_%=\n" \
  "s_sleep 2\n" \
  "s_add_u32 s58, s58, 1\n" \
  "s_cmp_lt_u32 s58, " STR(SPIN_CAP) "\n" \
  "s_cbranch_scc1 LFc_%=\n" \
  "v_mov_b32 v27, " STR(CW_DEAD) "*4\n" \
  "v_mov_b32 v28, 1\n" \
  "buffer_store_dword v28, v27, s[44:47], 0 offen sc1\n" \
  "s_branch LFc3_%=\n" \
  "LFc3_%=:\n" \
  "buffer_load_dwordx4 v[200:203], v247, s[40:43], s55 offen offset:0 sc1\n" \
  "buffer_load_dwordx4 v[204:207], v247, s[40:43], s55 offen offset:1024 sc1\n" \
  "buffer_load_dwordx4 v[208:211], v247, s[40:43], s55 offen offset:2048 sc1\n" \
  "buffer_load_dwordx4 v[212:215], v247, s[40:43], s55 offen offset:3072 sc1\n" \
  "s_waitcnt vmcnt(0)\n" \
  "ds_write_b128 v18, v[200:203] offset:0\n" \
  "ds_write_b128 v18, v[204:207] offset:1024\n" \
  "ds_write_b128 v18, v[208:211] offset:2048\n" \
  "ds_write_b128 v18, v[212:215] offset:3072\n" \
  "s_mov_b64 exec, 1\n" \
  "ds_sub_u32 v255, v88 offset:14360\n" \
  "s_mov_b64 exec, -1\n" \
  "s_branch LFc2_%=\n" \
  "LK3_%=:\n" \
  "s_sleep 2\n" \
  "s_andn2_b32 s59, s50, 0x70000\n" \
  "v_xor_b32 v28, s59, v13\n" \
  "buffer_load_dwordx2 v[92:93], v28, s[36:39], 0 offen sc1\n" \
  "s_waitcnt vmcnt(0)\n" \
  "s_add_u32 s58, s58, 1\n" \
  "s_cmp_lt_u32 s58, " STR(SPIN_CAP) "\n" \
  "s_cbranch_scc1 LK2_%=\n" \
  "v_mov_b32 v27, " STR(CW_DEAD) "*4\n" \
  "v_mov_b32 v28, 1\n" \
  "buffer_store_dword v28, v27, s[44:47], 0 offen sc1\n" \
  "s_branch LK6_%=\n" \
  "LK6_%=:\n" \
  "v_mov_b32 v25, 0\n" \
  "s_branch LK5_%=\n" \
  "LD_%=:\n" \
  "s_waitcnt lgkmcnt(0)\n" \
  "v_pk_add_f32 v[72:73], v[72:73], v[74:75]\n" \
  "v_pk_add_f32 v[76:77], v[76:77], v[78:79]\n" \
  "v_pk_add_f32 v[72:73], v[72:73], v[76:77]\n" \
  "v_cvt_pk_bf16_f32 v27, v72, v73\n" \
  "buffer_store_dword v27, v9, s[36:39], s50 offen sc1\n" \
  "LE_%=:\n" \
  "s_add_u32 s59, s48, 1\n" \
  "v_mov_b32 v28, s59\n" \
  "buffer_store_dword v28, v24, s[44:47], 0 offen sc1\n" \
  "s_endpgm\n" \
  ""

#define GEMM_ASM \
  "v_mbcnt_lo_u32_b32 v4, -1, 0\n" \
  "v_mbcnt_hi_u32_b32 v4, -1, v4\n" \
  "v_and_b32 v116, 15, v4\n" \
  "v_lshrrev_b32 v117, 4, v4\n" \
  "s_lshl_b32 s86, %8, 13\n" \
  "s_add_u32 s86, s86, %9\n" \
  "v_lshrrev_b32 v5, 3, v4\n" \
  "v_and_b32 v6, 7, v4\n" \
  "v_lshlrev_b32 v8, 13, v5\n" \
  "v_lshl_add_u32 v8, v6, 4, v8\n" \
  "v_and_b32 v9, 1, v117\n" \
  "v_lshlrev_b32 v9, 5, v9\n" \
  "v_xor_b32 v8, v8, v9\n" \
  "v_xor_b32 v9, 64, v8\n" \
  "s_mov_b64 s[68:69], %2\n" \
  "s_and_b32 s69, s69, 0xffff\n" \
  "s_mov_b32 s70, 8388608\n" \
  "s_mov_b32 s71, 0x00020000\n" \
  "s_add_u32 m0, s86, 0\n" \
  "s_mov_b64 s[68:69], %2\n" \
  "s_and_b32 s69, s69, 0xffff\n" \
  "s_lshl_b32 s92, %8, 19\n" \
  "s_add_u32 s92, s92, 0\n" \
  "buffer_load_dwordx4 v8, s[68:71], s92 offen lds\n" \
  "s_add_u32 m0, s86, 1024\n" \
  "s_add_u32 s92, s92, 65536\n" \
  "buffer_load_dwordx4 v9, s[68:71], s92 offen lds\n" \
  "s_add_u32 m0, s86, 2048\n" \
  "s_add_u32 s92, s92, 65536\n" \
  "buffer_load_dwordx4 v8, s[68:71], s92 offen lds\n" \
  "s_add_u32 m0, s86, 3072\n" \
  "s_add_u32 s92, s92, 65536\n" \
  "buffer_load_dwordx4 v9, s[68:71], s92 offen lds\n" \
  "s_add_u32 m0, s86, 4096\n" \
  "s_add_u32 s92, s92, 65536\n" \
  "buffer_load_dwordx4 v8, s[68:71], s92 offen lds\n" \
  "s_add_u32 m0, s86, 5120\n" \
  "s_add_u32 s92, s92, 65536\n" \
  "buffer_load_dwordx4 v9, s[68:71], s92 offen lds\n" \
  "s_add_u32 m0, s86, 6144\n" \
  "s_add_u32 s92, s92, 65536\n" \
  "buffer_load_dwordx4 v8, s[68:71], s92 offen lds\n" \
  "s_add_u32 m0, s86, 7168\n" \
  "s_add_u32 s92, s92, 65536\n" \
  "buffer_load_dwordx4 v9, s[68:71], s92 offen lds\n" \
  "s_add_u32 m0, s86, 32768\n" \
  "s_mov_b64 s[68:69], %3\n" \
  "s_and_b32 s69, s69, 0xffff\n" \
  "s_lshl_b32 s92, %8, 19\n" \
  "s_add_u32 s92, s92, 0\n" \
  "buffer_load_dwordx4 v8, s[68:71], s92 offen lds\n" \
  "s_add_u32 m0, s86, 33792\n" \
  "s_add_u32 s92, s92, 65536\n" \
  "buffer_load_dwordx4 v9, s[68:71], s92 offen lds\n" \
  "s_add_u32 m0, s86, 34816\n" \
  "s_add_u32 s92, s92, 65536\n" \
  "buffer_load_dwordx4 v8, s[68:71], s92 offen lds\n" \
  "s_add_u32 m0, s86, 35840\n" \
  "s_add_u32 s92, s92, 65536\n" \
  "buffer_load_dwordx4 v9, s[68:71], s92 offen lds\n" \
  "s_add_u32 m0, s86, 36864\n" \
  "s_add_u32 s92, s92, 65536\n" \
  "buffer_load_dwordx4 v8, s[68:71], s92 offen lds\n" \
  "s_add_u32 m0, s86, 37888\n" \
  "s_add_u32 s92, s92, 65536\n" \
  "buffer_load_dwordx4 v9, s[68:71], s92 offen lds\n" \
  "s_add_u32 m0, s86, 38912\n" \
  "s_add_u32 s92, s92, 65536\n" \
  "buffer_load_dwordx4 v8, s[68:71], s92 offen lds\n" \
  "s_add_u32 m0, s86, 39936\n" \
  "s_add_u32 s92, s92, 65536\n" \
  "buffer_load_dwordx4 v9, s[68:71], s92 offen lds\n" \
  "s_add_u32 m0, s86, 65536\n" \
  "s_mov_b64 s[68:69], %1\n" \
  "s_and_b32 s69, s69, 0xffff\n" \
  "s_lshl_b32 s92, %8, 19\n" \
  "s_add_u32 s92, s92, 0\n" \
  "buffer_load_dwordx4 v8, s[68:71], s92 offen lds\n" \
  "s_add_u32 m0, s86, 66560\n" \
  "s_add_u32 s92, s92, 65536\n" \
  "buffer_load_dwordx4 v9, s[68:71], s92 offen lds\n" \
  "s_add_u32 m0, s86, 67584\n" \
  "s_add_u32 s92, s92, 65536\n" \
  "buffer_load_dwordx4 v8, s[68:71], s92 offen lds\n" \
  "s_add_u32 m0, s86, 68608\n" \
  "s_add_u32 s92, s92, 65536\n" \
  "buffer_load_dwordx4 v9, s[68:71], s92 offen lds\n" \
  "s_add_u32 m0, s86, 69632\n" \
  "s_add_u32 s92, s92, 65536\n" \
  "buffer_load_dwordx4 v8, s[68:71], s92 offen lds\n" \
  "s_add_u32 m0, s86, 70656\n" \
  "s_add_u32 s92, s92, 65536\n" \
  "buffer_load_dwordx4 v9, s[68:71], s92 offen lds\n" \
  "s_add_u32 m0, s86, 71680\n" \
  "s_add_u32 s92, s92, 65536\n" \
  "buffer_load_dwordx4 v8, s[68:71], s92 offen lds\n" \
  "s_add_u32 m0, s86, 72704\n" \
  "s_add_u32 s92, s92, 65536\n" \
  "buffer_load_dwordx4 v9, s[68:71], s92 offen lds\n" \
  "s_mov_b64 s[72:73], %4\n" \
  "s_and_b32 s73, s73, 0xffff\n" \
  "s_mov_b32 s74, 4194304\n" \
  "s_mov_b32 s75, 0x00020000\n" \
  "s_mov_b64 s[76:77], %5\n" \
  "s_and_b32 s77, s77, 0xffff\n" \
  "s_mov_b32 s78, 3*" STR(QKV_SZ32) "\n" \
  "s_mov_b32 s79, 0x00020000\n" \
  "s_mov_b64 s[80:81], %6\n" \
  "s_and_b32 s81, s81, 0xffff\n" \
  "s_mov_b32 s82, " STR(CTRL_BYTES) "\n" \
  "s_mov_b32 s83, 0x00020000\n" \
  "v_xor_b32 v6, v6, v117\n" \
  "s_and_b32 s84, %8, 1\n" \
  "s_lshl_b32 s84, s84, 2\n" \
  "v_xor_b32 v6, s84, v6\n" \
  "v_lshlrev_b32 v7, 11, v5\n" \
  "v_lshl_add_u32 v7, v6, 4, v7\n" \
  "s_lshl_b32 s87, %8, 14\n" \
  "s_add_u32 s96, s87, 63*65536\n" \
  "v_lshrrev_b32 v5, 1, v116\n" \
  "v_xor_b32 v5, v5, v117\n" \
  "v_lshlrev_b32 v5, 4, v5\n" \
  "v_lshl_add_u32 v5, v116, 7, v5\n" \
  "v_add_u32 v10, %9, v5\n" \
  "v_xor_b32 v5, 64, v5\n" \
  "v_add_u32 v11, %9, v5\n" \
  "v_add_u32 v12, 0x10000, v10\n" \
  "v_add_u32 v13, 0x10000, v11\n" \
  "s_lshr_b32 s84, %7, 1\n" \
  "s_lshl_b32 s84, s84, 14\n" \
  "s_and_b32 s85, %7, 1\n" \
  "s_lshl_b32 s85, s85, 2\n" \
  "s_add_u32 s85, s85, %8\n" \
  "s_lshl_b32 s91, s85, 9\n" \
  "s_add_u32 s91, s91, s84\n" \
  "v_and_b32 v5, 1, v117\n" \
  "v_lshlrev_b32 v14, 12, v5\n" \
  "v_lshrrev_b32 v5, 1, v117\n" \
  "v_lshl_add_u32 v14, v5, 8, v14\n" \
  "v_lshl_add_u32 v14, v116, 4, v14\n" \
  "v_add_u32 v14, s91, v14\n" \
  "s_lshl_b32 s91, s85, 10\n" \
  "s_add_u32 s91, s91, s84\n" \
  "v_lshlrev_b32 v15, 4, v4\n" \
  "v_add_u32 v15, s91, v15\n" \
  "v_xor_b32 v18, 0x800, v14\n" \
  "v_xor_b32 v15, 0x4000, v15\n" \
  "s_lshl_b32 s84, %7, 7\n" \
  "s_add_u32 s84, s84, " STR(CW_GPROG) "*4\n" \
  "v_or_b32 v5, %8, v4\n" \
  "v_lshl_add_u32 v16, v5, 24, s84\n" \
  "v_mov_b32 v17, 0\n" \
  "v_lshrrev_b32 v5, 2, v116\n" \
  "v_lshl_add_u32 v6, v117, 3, v5\n" \
  "v_lshlrev_b32 v6, 7, v6\n" \
  "v_lshrrev_b32 v5, 1, v5\n" \
  "v_and_b32 v119, 1, v117\n" \
  "v_lshl_add_u32 v5, v119, 1, v5\n" \
  "v_xor_b32 v5, %8, v5\n" \
  "v_lshl_add_u32 v6, v5, 5, v6\n" \
  "v_and_b32 v5, 3, v4\n" \
  "v_lshl_add_u32 v6, v5, 3, v6\n" \
  "v_add_u32 v118, %9, v6\n" \
  "v_add_u32 v119, 0x10000, v118\n" \
  "v_add_u32 v120, 0x20000, v118\n" \
  "s_lshl_b32 s84, %8, 10\n" \
  "s_add_u32 s91, s84, %9\n" \
  "v_add_u32 v121, 0x20000, v10\n" \
  "v_add_u32 v122, 0x20000, v11\n" \
  "s_add_u32 s87, s87, 2*65536\n" \
  "s_waitcnt vmcnt(16) lgkmcnt(0)\n" \
  "s_barrier\n" \
  "s_add_u32 m0, s91, 98304\n" \
  "s_sub_u32 s92, s96, 4128768\n" \
  "buffer_load_dwordx4 v7, s[72:75], s92 offen lds\n" \
  "s_add_u32 m0, s91, 102400\n" \
  "s_sub_u32 s92, s96, 4128640\n" \
  "buffer_load_dwordx4 v7, s[72:75], s92 offen lds\n" \
  "s_add_u32 m0, s91, 106496\n" \
  "s_sub_u32 s92, s96, 4128512\n" \
  "buffer_load_dwordx4 v7, s[72:75], s92 offen lds\n" \
  "s_add_u32 m0, s91, 110592\n" \
  "s_sub_u32 s92, s96, 4128384\n" \
  "buffer_load_dwordx4 v7, s[72:75], s92 offen lds\n" \
  "s_add_u32 m0, s91, 114688\n" \
  "s_sub_u32 s92, s96, 4063232\n" \
  "buffer_load_dwordx4 v7, s[72:75], s92 offen lds\n" \
  "s_add_u32 m0, s91, 118784\n" \
  "s_sub_u32 s92, s96, 4063104\n" \
  "buffer_load_dwordx4 v7, s[72:75], s92 offen lds\n" \
  "s_add_u32 m0, s91, 122880\n" \
  "s_sub_u32 s92, s96, 4062976\n" \
  "buffer_load_dwordx4 v7, s[72:75], s92 offen lds\n" \
  "s_add_u32 m0, s91, 126976\n" \
  "s_sub_u32 s92, s96, 4062848\n" \
  "buffer_load_dwordx4 v7, s[72:75], s92 offen lds\n" \
  "ds_read_b64_tr_b16 a[0:1], v118 offset:0\n" \
  "ds_read_b64_tr_b16 a[2:3], v118 offset:512\n" \
  "ds_read_b64_tr_b16 a[4:5], v118 offset:4096\n" \
  "ds_read_b64_tr_b16 a[6:7], v118 offset:4608\n" \
  "ds_read_b64_tr_b16 a[8:9], v118 offset:8192\n" \
  "ds_read_b64_tr_b16 a[10:11], v118 offset:8704\n" \
  "ds_read_b64_tr_b16 a[12:13], v118 offset:12288\n" \
  "ds_read_b64_tr_b16 a[14:15], v118 offset:12800\n" \
  "ds_read_b64_tr_b16 a[16:17], v118 offset:16384\n" \
  "ds_read_b64_tr_b16 a[18:19], v118 offset:16896\n" \
  "ds_read_b64_tr_b16 a[20:21], v118 offset:20480\n" \
  "ds_read_b64_tr_b16 a[22:23], v118 offset:20992\n" \
  "ds_read_b64_tr_b16 a[24:25], v118 offset:24576\n" \
  "ds_read_b64_tr_b16 a[26:27], v118 offset:25088\n" \
  "ds_read_b64_tr_b16 a[28:29], v118 offset:28672\n" \
  "ds_read_b64_tr_b16 a[30:31], v118 offset:29184\n" \
  "s_waitcnt vmcnt(16) lgkmcnt(0)\n" \
  "s_barrier\n" \
  "s_add_u32 m0, s86, 0\n" \
  "s_mov_b64 s[68:69], %2\n" \
  "s_and_b32 s69, s69, 0xffff\n" \
  "s_lshl_b32 s92, %8, 19\n" \
  "s_add_u32 s92, s92, 2097152\n" \
  "buffer_load_dwordx4 v8, s[68:71], s92 offen lds\n" \
  "s_add_u32 m0, s86, 1024\n" \
  "s_add_u32 s92, s92, 65536\n" \
  "buffer_load_dwordx4 v9, s[68:71], s92 offen lds\n" \
  "s_add_u32 m0, s86, 2048\n" \
  "s_add_u32 s92, s92, 65536\n" \
  "buffer_load_dwordx4 v8, s[68:71], s92 offen lds\n" \
  "s_add_u32 m0, s86, 3072\n" \
  "s_add_u32 s92, s92, 65536\n" \
  "buffer_load_dwordx4 v9, s[68:71], s92 offen lds\n" \
  "s_add_u32 m0, s86, 4096\n" \
  "s_add_u32 s92, s92, 65536\n" \
  "buffer_load_dwordx4 v8, s[68:71], s92 offen lds\n" \
  "s_add_u32 m0, s86, 5120\n" \
  "s_add_u32 s92, s92, 65536\n" \
  "buffer_load_dwordx4 v9, s[68:71], s92 offen lds\n" \
  "s_add_u32 m0, s86, 6144\n" \
  "s_add_u32 s92, s92, 65536\n" \
  "buffer_load_dwordx4 v8, s[68:71], s92 offen lds\n" \
  "s_add_u32 m0, s86, 7168\n" \
  "s_add_u32 s92, s92, 65536\n" \
  "buffer_load_dwordx4 v9, s[68:71], s92 offen lds\n" \
  "ds_read_b64_tr_b16 a[128:129], v118 offset:32768\n" \
  "ds_read_b64_tr_b16 a[130:131], v118 offset:33280\n" \
  "ds_read_b64_tr_b16 a[132:133], v118 offset:36864\n" \
  "ds_read_b64_tr_b16 a[134:135], v118 offset:37376\n" \
  "ds_read_b64_tr_b16 a[136:137], v118 offset:40960\n" \
  "ds_read_b64_tr_b16 a[138:139], v118 offset:41472\n" \
  "ds_read_b64_tr_b16 a[140:141], v118 offset:45056\n" \
  "ds_read_b64_tr_b16 a[142:143], v118 offset:45568\n" \
  "ds_read_b64_tr_b16 a[144:145], v118 offset:49152\n" \
  "ds_read_b64_tr_b16 a[146:147], v118 offset:49664\n" \
  "ds_read_b64_tr_b16 a[148:149], v118 offset:53248\n" \
  "ds_read_b64_tr_b16 a[150:151], v118 offset:53760\n" \
  "ds_read_b64_tr_b16 a[152:153], v118 offset:57344\n" \
  "ds_read_b64_tr_b16 a[154:155], v118 offset:57856\n" \
  "ds_read_b64_tr_b16 a[156:157], v118 offset:61440\n" \
  "ds_read_b64_tr_b16 a[158:159], v118 offset:61952\n" \
  "s_waitcnt vmcnt(16) lgkmcnt(0)\n" \
  "s_barrier\n" \
  "s_add_u32 m0, s86, 32768\n" \
  "s_mov_b64 s[68:69], %3\n" \
  "s_and_b32 s69, s69, 0xffff\n" \
  "s_lshl_b32 s92, %8, 19\n" \
  "s_add_u32 s92, s92, 2097152\n" \
  "buffer_load_dwordx4 v8, s[68:71], s92 offen lds\n" \
  "s_add_u32 m0, s86, 33792\n" \
  "s_add_u32 s92, s92, 65536\n" \
  "buffer_load_dwordx4 v9, s[68:71], s92 offen lds\n" \
  "s_add_u32 m0, s86, 34816\n" \
  "s_add_u32 s92, s92, 65536\n" \
  "buffer_load_dwordx4 v8, s[68:71], s92 offen lds\n" \
  "s_add_u32 m0, s86, 35840\n" \
  "s_add_u32 s92, s92, 65536\n" \
  "buffer_load_dwordx4 v9, s[68:71], s92 offen lds\n" \
  "s_add_u32 m0, s86, 36864\n" \
  "s_add_u32 s92, s92, 65536\n" \
  "buffer_load_dwordx4 v8, s[68:71], s92 offen lds\n" \
  "s_add_u32 m0, s86, 37888\n" \
  "s_add_u32 s92, s92, 65536\n" \
  "buffer_load_dwordx4 v9, s[68:71], s92 offen lds\n" \
  "s_add_u32 m0, s86, 38912\n" \
  "s_add_u32 s92, s92, 65536\n" \
  "buffer_load_dwordx4 v8, s[68:71], s92 offen lds\n" \
  "s_add_u32 m0, s86, 39936\n" \
  "s_add_u32 s92, s92, 65536\n" \
  "buffer_load_dwordx4 v9, s[68:71], s92 offen lds\n" \
  "ds_read_b64_tr_b16 v[128:129], v119 offset:0\n" \
  "ds_read_b64_tr_b16 v[130:131], v119 offset:512\n" \
  "ds_read_b64_tr_b16 v[132:133], v119 offset:4096\n" \
  "ds_read_b64_tr_b16 v[134:135], v119 offset:4608\n" \
  "ds_read_b64_tr_b16 v[136:137], v119 offset:8192\n" \
  "ds_read_b64_tr_b16 v[138:139], v119 offset:8704\n" \
  "ds_read_b64_tr_b16 v[140:141], v119 offset:12288\n" \
  "ds_read_b64_tr_b16 v[142:143], v119 offset:12800\n" \
  "ds_read_b64_tr_b16 v[144:145], v119 offset:16384\n" \
  "ds_read_b64_tr_b16 v[146:147], v119 offset:16896\n" \
  "ds_read_b64_tr_b16 v[148:149], v119 offset:20480\n" \
  "ds_read_b64_tr_b16 v[150:151], v119 offset:20992\n" \
  "ds_read_b64_tr_b16 v[152:153], v119 offset:24576\n" \
  "ds_read_b64_tr_b16 v[154:155], v119 offset:25088\n" \
  "ds_read_b64_tr_b16 v[156:157], v119 offset:28672\n" \
  "ds_read_b64_tr_b16 v[158:159], v119 offset:29184\n" \
  "s_waitcnt vmcnt(16) lgkmcnt(0)\n" \
  "s_barrier\n" \
  "s_add_u32 m0, s86, 65536\n" \
  "s_mov_b64 s[68:69], %1\n" \
  "s_and_b32 s69, s69, 0xffff\n" \
  "s_lshl_b32 s92, %8, 19\n" \
  "s_add_u32 s92, s92, 2097152\n" \
  "buffer_load_dwordx4 v8, s[68:71], s92 offen lds\n" \
  "s_add_u32 m0, s86, 66560\n" \
  "s_add_u32 s92, s92, 65536\n" \
  "buffer_load_dwordx4 v9, s[68:71], s92 offen lds\n" \
  "s_add_u32 m0, s86, 67584\n" \
  "s_add_u32 s92, s92, 65536\n" \
  "buffer_load_dwordx4 v8, s[68:71], s92 offen lds\n" \
  "s_add_u32 m0, s86, 68608\n" \
  "s_add_u32 s92, s92, 65536\n" \
  "buffer_load_dwordx4 v9, s[68:71], s92 offen lds\n" \
  "s_add_u32 m0, s86, 69632\n" \
  "s_add_u32 s92, s92, 65536\n" \
  "buffer_load_dwordx4 v8, s[68:71], s92 offen lds\n" \
  "s_add_u32 m0, s86, 70656\n" \
  "s_add_u32 s92, s92, 65536\n" \
  "buffer_load_dwordx4 v9, s[68:71], s92 offen lds\n" \
  "s_add_u32 m0, s86, 71680\n" \
  "s_add_u32 s92, s92, 65536\n" \
  "buffer_load_dwordx4 v8, s[68:71], s92 offen lds\n" \
  "s_add_u32 m0, s86, 72704\n" \
  "s_add_u32 s92, s92, 65536\n" \
  "buffer_load_dwordx4 v9, s[68:71], s92 offen lds\n" \
  "s_waitcnt vmcnt(16) lgkmcnt(0)\n" \
  "s_barrier\n" \
  "ds_read_b128 v[68:71], v12 offset:32768\n" \
  "ds_read_b128 v[72:75], v12 offset:34816\n" \
  "ds_read_b128 v[76:79], v13 offset:32768\n" \
  "ds_read_b128 v[80:83], v13 offset:34816\n" \
  "ds_read_b128 v[84:87], v12 offset:36864\n" \
  "ds_read_b128 v[88:91], v12 offset:38912\n" \
  "ds_read_b128 v[92:95], v13 offset:36864\n" \
  "ds_read_b128 v[96:99], v13 offset:38912\n" \
  "s_add_u32 m0, s91, 131072\n" \
  "s_sub_u32 s92, s96, 4128256\n" \
  "buffer_load_dwordx4 v7, s[72:75], s92 offen lds\n" \
  "s_waitcnt lgkmcnt(0)\n" \
  "v_mfma_f32_16x16x32_bf16 v[20:23], v[128:131], v[68:71], 0\n" \
  "ds_read_b64_tr_b16 a[32:33], v118 offset:0\n" \
  "v_mfma_f32_16x16x32_bf16 v[28:31], a[0:3], v[68:71], 0\n" \
  "ds_read_b64_tr_b16 a[34:35], v118 offset:512\n" \
  "v_mfma_f32_16x16x32_bf16 v[36:39], v[68:71], a[128:131], 0\n" \
  "s_add_u32 m0, s91, 135168\n" \
  "s_sub_u32 s92, s96, 4128128\n" \
  "buffer_load_dwordx4 v7, s[72:75], s92 offen lds\n" \
  "v_mfma_f32_16x16x32_bf16 v[24:27], v[128:131], v[72:75], 0\n" \
  "ds_read_b64_tr_b16 a[36:37], v118 offset:4096\n" \
  "v_mfma_f32_16x16x32_bf16 v[32:35], a[0:3], v[72:75], 0\n" \
  "ds_read_b64_tr_b16 a[38:39], v118 offset:4608\n" \
  "v_mfma_f32_16x16x32_bf16 v[40:43], v[72:75], a[128:131], 0\n" \
  "s_add_u32 m0, s91, 139264\n" \
  "s_sub_u32 s92, s96, 4128000\n" \
  "buffer_load_dwordx4 v7, s[72:75], s92 offen lds\n" \
  "v_mfma_f32_16x16x32_bf16 v[20:23], v[132:135], v[76:79], v[20:23]\n" \
  "ds_read_b64_tr_b16 a[40:41], v118 offset:8192\n" \
  "v_mfma_f32_16x16x32_bf16 v[28:31], a[4:7], v[76:79], v[28:31]\n" \
  "ds_read_b64_tr_b16 a[42:43], v118 offset:8704\n" \
  "v_mfma_f32_16x16x32_bf16 v[36:39], v[76:79], a[132:135], v[36:39]\n" \
  "s_add_u32 m0, s91, 143360\n" \
  "s_sub_u32 s92, s96, 4127872\n" \
  "buffer_load_dwordx4 v7, s[72:75], s92 offen lds\n" \
  "v_mfma_f32_16x16x32_bf16 v[24:27], v[132:135], v[80:83], v[24:27]\n" \
  "ds_read_b64_tr_b16 a[44:45], v118 offset:12288\n" \
  "v_mfma_f32_16x16x32_bf16 v[32:35], a[4:7], v[80:83], v[32:35]\n" \
  "ds_read_b64_tr_b16 a[46:47], v118 offset:12800\n" \
  "v_mfma_f32_16x16x32_bf16 v[40:43], v[80:83], a[132:135], v[40:43]\n" \
  "s_add_u32 m0, s91, 147456\n" \
  "s_sub_u32 s92, s96, 4062720\n" \
  "buffer_load_dwordx4 v7, s[72:75], s92 offen lds\n" \
  "v_mfma_f32_16x16x32_bf16 v[20:23], v[136:139], v[84:87], v[20:23]\n" \
  "ds_read_b64_tr_b16 a[48:49], v118 offset:16384\n" \
  "v_mfma_f32_16x16x32_bf16 v[28:31], a[8:11], v[84:87], v[28:31]\n" \
  "ds_read_b64_tr_b16 a[50:51], v118 offset:16896\n" \
  "v_mfma_f32_16x16x32_bf16 v[36:39], v[84:87], a[136:139], v[36:39]\n" \
  "s_add_u32 m0, s91, 151552\n" \
  "s_sub_u32 s92, s96, 4062592\n" \
  "buffer_load_dwordx4 v7, s[72:75], s92 offen lds\n" \
  "v_mfma_f32_16x16x32_bf16 v[24:27], v[136:139], v[88:91], v[24:27]\n" \
  "ds_read_b64_tr_b16 a[52:53], v118 offset:20480\n" \
  "v_mfma_f32_16x16x32_bf16 v[32:35], a[8:11], v[88:91], v[32:35]\n" \
  "ds_read_b64_tr_b16 a[54:55], v118 offset:20992\n" \
  "v_mfma_f32_16x16x32_bf16 v[40:43], v[88:91], a[136:139], v[40:43]\n" \
  "s_add_u32 m0, s91, 155648\n" \
  "s_sub_u32 s92, s96, 4062464\n" \
  "buffer_load_dwordx4 v7, s[72:75], s92 offen lds\n" \
  "v_mfma_f32_16x16x32_bf16 v[20:23], v[140:143], v[92:95], v[20:23]\n" \
  "ds_read_b64_tr_b16 a[56:57], v118 offset:24576\n" \
  "v_mfma_f32_16x16x32_bf16 v[28:31], a[12:15], v[92:95], v[28:31]\n" \
  "ds_read_b64_tr_b16 a[58:59], v118 offset:25088\n" \
  "v_mfma_f32_16x16x32_bf16 v[36:39], v[92:95], a[140:143], v[36:39]\n" \
  "s_add_u32 m0, s91, 159744\n" \
  "s_sub_u32 s92, s96, 4062336\n" \
  "buffer_load_dwordx4 v7, s[72:75], s92 offen lds\n" \
  "v_mfma_f32_16x16x32_bf16 v[24:27], v[140:143], v[96:99], v[24:27]\n" \
  "ds_read_b64_tr_b16 a[60:61], v118 offset:28672\n" \
  "v_mfma_f32_16x16x32_bf16 v[32:35], a[12:15], v[96:99], v[32:35]\n" \
  "ds_read_b64_tr_b16 a[62:63], v118 offset:29184\n" \
  "v_mfma_f32_16x16x32_bf16 v[40:43], v[96:99], a[140:143], v[40:43]\n" \
  "s_waitcnt vmcnt(16) lgkmcnt(0)\n" \
  "s_barrier\n" \
  "ds_read_b128 v[68:71], v12 offset:40960\n" \
  "ds_read_b128 v[72:75], v12 offset:43008\n" \
  "ds_read_b128 v[76:79], v13 offset:40960\n" \
  "ds_read_b128 v[80:83], v13 offset:43008\n" \
  "ds_read_b128 v[84:87], v12 offset:45056\n" \
  "ds_read_b128 v[88:91], v12 offset:47104\n" \
  "ds_read_b128 v[92:95], v13 offset:45056\n" \
  "ds_read_b128 v[96:99], v13 offset:47104\n" \
  "s_add_u32 m0, s86, 0\n" \
  "s_mov_b64 s[68:69], %2\n" \
  "s_and_b32 s69, s69, 0xffff\n" \
  "s_lshl_b32 s92, %8, 19\n" \
  "s_add_u32 s92, s92, 4194304\n" \
  "buffer_load_dwordx4 v8, s[68:71], s92 offen lds\n" \
  "s_waitcnt lgkmcnt(0)\n" \
  "v_mfma_f32_16x16x32_bf16 v[20:23], v[144:147], v[68:71], v[20:23]\n" \
  "ds_read_b64_tr_b16 a[160:161], v118 offset:32768\n" \
  "v_mfma_f32_16x16x32_bf16 v[28:31], a[16:19], v[68:71], v[28:31]\n" \
  "ds_read_b64_tr_b16 a[162:163], v118 offset:33280\n" \
  "v_mfma_f32_16x16x32_bf16 v[36:39], v[68:71], a[144:147], v[36:39]\n" \
  "s_add_u32 m0, s86, 1024\n" \
  "s_add_u32 s92, s92, 65536\n" \
  "buffer_load_dwordx4 v9, s[68:71], s92 offen lds\n" \
  "v_mfma_f32_16x16x32_bf16 v[24:27], v[144:147], v[72:75], v[24:27]\n" \
  "ds_read_b64_tr_b16 a[164:165], v118 offset:36864\n" \
  "v_mfma_f32_16x16x32_bf16 v[32:35], a[16:19], v[72:75], v[32:35]\n" \
  "ds_read_b64_tr_b16 a[166:167], v118 offset:37376\n" \
  "v_mfma_f32_16x16x32_bf16 v[40:43], v[72:75], a[144:147], v[40:43]\n" \
  "s_add_u32 m0, s86, 2048\n" \
  "s_add_u32 s92, s92, 65536\n" \
  "buffer_load_dwordx4 v8, s[68:71], s92 offen lds\n" \
  "v_mfma_f32_16x16x32_bf16 v[20:23], v[148:151], v[76:79], v[20:23]\n" \
  "ds_read_b64_tr_b16 a[168:169], v118 offset:40960\n" \
  "v_mfma_f32_16x16x32_bf16 v[28:31], a[20:23], v[76:79], v[28:31]\n" \
  "ds_read_b64_tr_b16 a[170:171], v118 offset:41472\n" \
  "v_mfma_f32_16x16x32_bf16 v[36:39], v[76:79], a[148:151], v[36:39]\n" \
  "s_add_u32 m0, s86, 3072\n" \
  "s_add_u32 s92, s92, 65536\n" \
  "buffer_load_dwordx4 v9, s[68:71], s92 offen lds\n" \
  "v_mfma_f32_16x16x32_bf16 v[24:27], v[148:151], v[80:83], v[24:27]\n" \
  "ds_read_b64_tr_b16 a[172:173], v118 offset:45056\n" \
  "v_mfma_f32_16x16x32_bf16 v[32:35], a[20:23], v[80:83], v[32:35]\n" \
  "ds_read_b64_tr_b16 a[174:175], v118 offset:45568\n" \
  "v_mfma_f32_16x16x32_bf16 v[40:43], v[80:83], a[148:151], v[40:43]\n" \
  "s_add_u32 m0, s86, 4096\n" \
  "s_add_u32 s92, s92, 65536\n" \
  "buffer_load_dwordx4 v8, s[68:71], s92 offen lds\n" \
  "v_mfma_f32_16x16x32_bf16 v[20:23], v[152:155], v[84:87], v[20:23]\n" \
  "ds_read_b64_tr_b16 a[176:177], v118 offset:49152\n" \
  "v_mfma_f32_16x16x32_bf16 v[28:31], a[24:27], v[84:87], v[28:31]\n" \
  "ds_read_b64_tr_b16 a[178:179], v118 offset:49664\n" \
  "v_mfma_f32_16x16x32_bf16 v[36:39], v[84:87], a[152:155], v[36:39]\n" \
  "s_add_u32 m0, s86, 5120\n" \
  "s_add_u32 s92, s92, 65536\n" \
  "buffer_load_dwordx4 v9, s[68:71], s92 offen lds\n" \
  "v_mfma_f32_16x16x32_bf16 v[24:27], v[152:155], v[88:91], v[24:27]\n" \
  "ds_read_b64_tr_b16 a[180:181], v118 offset:53248\n" \
  "v_mfma_f32_16x16x32_bf16 v[32:35], a[24:27], v[88:91], v[32:35]\n" \
  "ds_read_b64_tr_b16 a[182:183], v118 offset:53760\n" \
  "v_mfma_f32_16x16x32_bf16 v[40:43], v[88:91], a[152:155], v[40:43]\n" \
  "s_add_u32 m0, s86, 6144\n" \
  "s_add_u32 s92, s92, 65536\n" \
  "buffer_load_dwordx4 v8, s[68:71], s92 offen lds\n" \
  "v_mfma_f32_16x16x32_bf16 v[20:23], v[156:159], v[92:95], v[20:23]\n" \
  "ds_read_b64_tr_b16 a[184:185], v118 offset:57344\n" \
  "v_mfma_f32_16x16x32_bf16 v[28:31], a[28:31], v[92:95], v[28:31]\n" \
  "ds_read_b64_tr_b16 a[186:187], v118 offset:57856\n" \
  "v_mfma_f32_16x16x32_bf16 v[36:39], v[92:95], a[156:159], v[36:39]\n" \
  "s_add_u32 m0, s86, 7168\n" \
  "s_add_u32 s92, s92, 65536\n" \
  "buffer_load_dwordx4 v9, s[68:71], s92 offen lds\n" \
  "v_mfma_f32_16x16x32_bf16 v[24:27], v[156:159], v[96:99], v[24:27]\n" \
  "ds_read_b64_tr_b16 a[188:189], v118 offset:61440\n" \
  "v_mfma_f32_16x16x32_bf16 v[32:35], a[28:31], v[96:99], v[32:35]\n" \
  "ds_read_b64_tr_b16 a[190:191], v118 offset:61952\n" \
  "v_mfma_f32_16x16x32_bf16 v[40:43], v[96:99], a[156:159], v[40:43]\n" \
  "s_waitcnt vmcnt(16) lgkmcnt(0)\n" \
  "s_barrier\n" \
  "ds_read_b128 v[68:71], v12 offset:49152\n" \
  "ds_read_b128 v[72:75], v12 offset:51200\n" \
  "ds_read_b128 v[76:79], v13 offset:49152\n" \
  "ds_read_b128 v[80:83], v13 offset:51200\n" \
  "ds_read_b128 v[84:87], v12 offset:53248\n" \
  "ds_read_b128 v[88:91], v12 offset:55296\n" \
  "ds_read_b128 v[92:95], v13 offset:53248\n" \
  "ds_read_b128 v[96:99], v13 offset:55296\n" \
  "s_add_u32 m0, s86, 32768\n" \
  "s_mov_b64 s[68:69], %3\n" \
  "s_and_b32 s69, s69, 0xffff\n" \
  "s_lshl_b32 s92, %8, 19\n" \
  "s_add_u32 s92, s92, 4194304\n" \
  "buffer_load_dwordx4 v8, s[68:71], s92 offen lds\n" \
  "s_waitcnt lgkmcnt(0)\n" \
  "v_mfma_f32_16x16x32_bf16 v[44:47], v[128:131], v[68:71], 0\n" \
  "ds_read_b64_tr_b16 v[160:161], v119 offset:0\n" \
  "v_mfma_f32_16x16x32_bf16 v[52:55], a[0:3], v[68:71], 0\n" \
  "ds_read_b64_tr_b16 v[162:163], v119 offset:512\n" \
  "v_mfma_f32_16x16x32_bf16 v[60:63], v[68:71], a[128:131], 0\n" \
  "s_add_u32 m0, s86, 33792\n" \
  "s_add_u32 s92, s92, 65536\n" \
  "buffer_load_dwordx4 v9, s[68:71], s92 offen lds\n" \
  "v_mfma_f32_16x16x32_bf16 v[48:51], v[128:131], v[72:75], 0\n" \
  "ds_read_b64_tr_b16 v[164:165], v119 offset:4096\n" \
  "v_mfma_f32_16x16x32_bf16 v[56:59], a[0:3], v[72:75], 0\n" \
  "ds_read_b64_tr_b16 v[166:167], v119 offset:4608\n" \
  "v_mfma_f32_16x16x32_bf16 v[64:67], v[72:75], a[128:131], 0\n" \
  "s_add_u32 m0, s86, 34816\n" \
  "s_add_u32 s92, s92, 65536\n" \
  "buffer_load_dwordx4 v8, s[68:71], s92 offen lds\n" \
  "v_mfma_f32_16x16x32_bf16 v[44:47], v[132:135], v[76:79], v[44:47]\n" \
  "ds_read_b64_tr_b16 v[168:169], v119 offset:8192\n" \
  "v_mfma_f32_16x16x32_bf16 v[52:55], a[4:7], v[76:79], v[52:55]\n" \
  "ds_read_b64_tr_b16 v[170:171], v119 offset:8704\n" \
  "v_mfma_f32_16x16x32_bf16 v[60:63], v[76:79], a[132:135], v[60:63]\n" \
  "s_add_u32 m0, s86, 35840\n" \
  "s_add_u32 s92, s92, 65536\n" \
  "buffer_load_dwordx4 v9, s[68:71], s92 offen lds\n" \
  "v_mfma_f32_16x16x32_bf16 v[48:51], v[132:135], v[80:83], v[48:51]\n" \
  "ds_read_b64_tr_b16 v[172:173], v119 offset:12288\n" \
  "v_mfma_f32_16x16x32_bf16 v[56:59], a[4:7], v[80:83], v[56:59]\n" \
  "ds_read_b64_tr_b16 v[174:175], v119 offset:12800\n" \
  "v_mfma_f32_16x16x32_bf16 v[64:67], v[80:83], a[132:135], v[64:67]\n" \
  "s_add_u32 m0, s86, 36864\n" \
  "s_add_u32 s92, s92, 65536\n" \
  "buffer_load_dwordx4 v8, s[68:71], s92 offen lds\n" \
  "v_mfma_f32_16x16x32_bf16 v[44:47], v[136:139], v[84:87], v[44:47]\n" \
  "ds_read_b64_tr_b16 v[176:177], v119 offset:16384\n" \
  "v_mfma_f32_16x16x32_bf16 v[52:55], a[8:11], v[84:87], v[52:55]\n" \
  "ds_read_b64_tr_b16 v[178:179], v119 offset:16896\n" \
  "v_mfma_f32_16x16x32_bf16 v[60:63], v[84:87], a[136:139], v[60:63]\n" \
  "s_add_u32 m0, s86, 37888\n" \
  "s_add_u32 s92, s92, 65536\n" \
  "buffer_load_dwordx4 v9, s[68:71], s92 offen lds\n" \
  "v_mfma_f32_16x16x32_bf16 v[48:51], v[136:139], v[88:91], v[48:51]\n" \
  "ds_read_b64_tr_b16 v[180:181], v119 offset:20480\n" \
  "v_mfma_f32_16x16x32_bf16 v[56:59], a[8:11], v[88:91], v[56:59]\n" \
  "ds_read_b64_tr_b16 v[182:183], v119 offset:20992\n" \
  "v_mfma_f32_16x16x32_bf16 v[64:67], v[88:91], a[136:139], v[64:67]\n" \
  "s_add_u32 m0, s86, 38912\n" \
  "s_add_u32 s92, s92, 65536\n" \
  "buffer_load_dwordx4 v8, s[68:71], s92 offen lds\n" \
  "v_mfma_f32_16x16x32_bf16 v[44:47], v[140:143], v[92:95], v[44:47]\n" \
  "ds_read_b64_tr_b16 v[184:185], v119 offset:24576\n" \
  "v_mfma_f32_16x16x32_bf16 v[52:55], a[12:15], v[92:95], v[52:55]\n" \
  "ds_read_b64_tr_b16 v[186:187], v119 offset:25088\n" \
  "v_mfma_f32_16x16x32_bf16 v[60:63], v[92:95], a[140:143], v[60:63]\n" \
  "s_add_u32 m0, s86, 39936\n" \
  "s_add_u32 s92, s92, 65536\n" \
  "buffer_load_dwordx4 v9, s[68:71], s92 offen lds\n" \
  "v_mfma_f32_16x16x32_bf16 v[48:51], v[140:143], v[96:99], v[48:51]\n" \
  "ds_read_b64_tr_b16 v[188:189], v119 offset:28672\n" \
  "v_mfma_f32_16x16x32_bf16 v[56:59], a[12:15], v[96:99], v[56:59]\n" \
  "ds_read_b64_tr_b16 v[190:191], v119 offset:29184\n" \
  "v_mfma_f32_16x16x32_bf16 v[64:67], v[96:99], a[140:143], v[64:67]\n" \
  "s_waitcnt vmcnt(16) lgkmcnt(0)\n" \
  "s_barrier\n" \
  "ds_read_b128 v[68:71], v12 offset:57344\n" \
  "ds_read_b128 v[72:75], v12 offset:59392\n" \
  "ds_read_b128 v[76:79], v13 offset:57344\n" \
  "ds_read_b128 v[80:83], v13 offset:59392\n" \
  "ds_read_b128 v[84:87], v12 offset:61440\n" \
  "ds_read_b128 v[88:91], v12 offset:63488\n" \
  "ds_read_b128 v[92:95], v13 offset:61440\n" \
  "ds_read_b128 v[96:99], v13 offset:63488\n" \
  "s_add_u32 m0, s86, 65536\n" \
  "s_mov_b64 s[68:69], %1\n" \
  "s_and_b32 s69, s69, 0xffff\n" \
  "s_lshl_b32 s92, %8, 19\n" \
  "s_add_u32 s92, s92, 4194304\n" \
  "buffer_load_dwordx4 v8, s[68:71], s92 offen lds\n" \
  "s_waitcnt lgkmcnt(0)\n" \
  "v_mfma_f32_16x16x32_bf16 v[44:47], v[144:147], v[68:71], v[44:47]\n" \
  "v_mfma_f32_16x16x32_bf16 v[52:55], a[16:19], v[68:71], v[52:55]\n" \
  "v_mfma_f32_16x16x32_bf16 v[60:63], v[68:71], a[144:147], v[60:63]\n" \
  "s_add_u32 m0, s86, 66560\n" \
  "s_add_u32 s92, s92, 65536\n" \
  "buffer_load_dwordx4 v9, s[68:71], s92 offen lds\n" \
  "v_mfma_f32_16x16x32_bf16 v[48:51], v[144:147], v[72:75], v[48:51]\n" \
  "v_mfma_f32_16x16x32_bf16 v[56:59], a[16:19], v[72:75], v[56:59]\n" \
  "v_mfma_f32_16x16x32_bf16 v[64:67], v[72:75], a[144:147], v[64:67]\n" \
  "s_add_u32 m0, s86, 67584\n" \
  "s_add_u32 s92, s92, 65536\n" \
  "buffer_load_dwordx4 v8, s[68:71], s92 offen lds\n" \
  "v_mfma_f32_16x16x32_bf16 v[44:47], v[148:151], v[76:79], v[44:47]\n" \
  "v_mfma_f32_16x16x32_bf16 v[52:55], a[20:23], v[76:79], v[52:55]\n" \
  "v_mfma_f32_16x16x32_bf16 v[60:63], v[76:79], a[148:151], v[60:63]\n" \
  "s_add_u32 m0, s86, 68608\n" \
  "s_add_u32 s92, s92, 65536\n" \
  "buffer_load_dwordx4 v9, s[68:71], s92 offen lds\n" \
  "v_mfma_f32_16x16x32_bf16 v[48:51], v[148:151], v[80:83], v[48:51]\n" \
  "v_mfma_f32_16x16x32_bf16 v[56:59], a[20:23], v[80:83], v[56:59]\n" \
  "v_mfma_f32_16x16x32_bf16 v[64:67], v[80:83], a[148:151], v[64:67]\n" \
  "s_add_u32 m0, s86, 69632\n" \
  "s_add_u32 s92, s92, 65536\n" \
  "buffer_load_dwordx4 v8, s[68:71], s92 offen lds\n" \
  "v_mfma_f32_16x16x32_bf16 v[44:47], v[152:155], v[84:87], v[44:47]\n" \
  "v_mfma_f32_16x16x32_bf16 v[52:55], a[24:27], v[84:87], v[52:55]\n" \
  "v_mfma_f32_16x16x32_bf16 v[60:63], v[84:87], a[152:155], v[60:63]\n" \
  "s_add_u32 m0, s86, 70656\n" \
  "s_add_u32 s92, s92, 65536\n" \
  "buffer_load_dwordx4 v9, s[68:71], s92 offen lds\n" \
  "v_mfma_f32_16x16x32_bf16 v[48:51], v[152:155], v[88:91], v[48:51]\n" \
  "v_mfma_f32_16x16x32_bf16 v[56:59], a[24:27], v[88:91], v[56:59]\n" \
  "v_mfma_f32_16x16x32_bf16 v[64:67], v[88:91], a[152:155], v[64:67]\n" \
  "s_add_u32 m0, s86, 71680\n" \
  "s_add_u32 s92, s92, 65536\n" \
  "buffer_load_dwordx4 v8, s[68:71], s92 offen lds\n" \
  "v_mfma_f32_16x16x32_bf16 v[44:47], v[156:159], v[92:95], v[44:47]\n" \
  "v_mfma_f32_16x16x32_bf16 v[52:55], a[28:31], v[92:95], v[52:55]\n" \
  "v_mfma_f32_16x16x32_bf16 v[60:63], v[92:95], a[156:159], v[60:63]\n" \
  "s_add_u32 m0, s86, 72704\n" \
  "s_add_u32 s92, s92, 65536\n" \
  "buffer_load_dwordx4 v9, s[68:71], s92 offen lds\n" \
  "v_mfma_f32_16x16x32_bf16 v[48:51], v[156:159], v[96:99], v[48:51]\n" \
  "v_mfma_f32_16x16x32_bf16 v[56:59], a[28:31], v[96:99], v[56:59]\n" \
  "v_mfma_f32_16x16x32_bf16 v[64:67], v[96:99], a[156:159], v[64:67]\n" \
  "s_waitcnt vmcnt(16) lgkmcnt(0)\n" \
  "s_barrier\n" \
  "ds_read_b128 v[68:71], v121 offset:0\n" \
  "ds_read_b128 v[72:75], v121 offset:2048\n" \
  "ds_read_b128 v[76:79], v122 offset:0\n" \
  "ds_read_b128 v[80:83], v122 offset:2048\n" \
  "ds_read_b128 v[84:87], v121 offset:4096\n" \
  "ds_read_b128 v[88:91], v121 offset:6144\n" \
  "ds_read_b128 v[92:95], v122 offset:4096\n" \
  "ds_read_b128 v[96:99], v122 offset:6144\n" \
  "s_add_u32 m0, s91, 98304\n" \
  "s_sub_u32 s92, s96, 4127744\n" \
  "buffer_load_dwordx4 v7, s[72:75], s92 offen lds\n" \
  "s_waitcnt lgkmcnt(0)\n" \
  "v_mfma_f32_16x16x32_bf16 v[20:23], v[160:163], v[68:71], v[20:23]\n" \
  "ds_read_b64_tr_b16 a[64:65], v118 offset:0\n" \
  "v_mfma_f32_16x16x32_bf16 v[28:31], a[32:35], v[68:71], v[28:31]\n" \
  "ds_read_b64_tr_b16 a[66:67], v118 offset:512\n" \
  "v_mfma_f32_16x16x32_bf16 v[36:39], v[68:71], a[160:163], v[36:39]\n" \
  "s_add_u32 m0, s91, 102400\n" \
  "s_sub_u32 s92, s96, 4127616\n" \
  "buffer_load_dwordx4 v7, s[72:75], s92 offen lds\n" \
  "v_mfma_f32_16x16x32_bf16 v[24:27], v[160:163], v[72:75], v[24:27]\n" \
  "ds_read_b64_tr_b16 a[68:69], v118 offset:4096\n" \
  "v_mfma_f32_16x16x32_bf16 v[32:35], a[32:35], v[72:75], v[32:35]\n" \
  "ds_read_b64_tr_b16 a[70:71], v118 offset:4608\n" \
  "v_mfma_f32_16x16x32_bf16 v[40:43], v[72:75], a[160:163], v[40:43]\n" \
  "s_add_u32 m0, s91, 106496\n" \
  "s_sub_u32 s92, s96, 4127488\n" \
  "buffer_load_dwordx4 v7, s[72:75], s92 offen lds\n" \
  "v_mfma_f32_16x16x32_bf16 v[20:23], v[164:167], v[76:79], v[20:23]\n" \
  "ds_read_b64_tr_b16 a[72:73], v118 offset:8192\n" \
  "v_mfma_f32_16x16x32_bf16 v[28:31], a[36:39], v[76:79], v[28:31]\n" \
  "ds_read_b64_tr_b16 a[74:75], v118 offset:8704\n" \
  "v_mfma_f32_16x16x32_bf16 v[36:39], v[76:79], a[164:167], v[36:39]\n" \
  "s_add_u32 m0, s91, 110592\n" \
  "s_sub_u32 s92, s96, 4127360\n" \
  "buffer_load_dwordx4 v7, s[72:75], s92 offen lds\n" \
  "v_mfma_f32_16x16x32_bf16 v[24:27], v[164:167], v[80:83], v[24:27]\n" \
  "ds_read_b64_tr_b16 a[76:77], v118 offset:12288\n" \
  "v_mfma_f32_16x16x32_bf16 v[32:35], a[36:39], v[80:83], v[32:35]\n" \
  "ds_read_b64_tr_b16 a[78:79], v118 offset:12800\n" \
  "v_mfma_f32_16x16x32_bf16 v[40:43], v[80:83], a[164:167], v[40:43]\n" \
  "s_add_u32 m0, s91, 114688\n" \
  "s_sub_u32 s92, s96, 4062208\n" \
  "buffer_load_dwordx4 v7, s[72:75], s92 offen lds\n" \
  "v_mfma_f32_16x16x32_bf16 v[20:23], v[168:171], v[84:87], v[20:23]\n" \
  "ds_read_b64_tr_b16 a[80:81], v118 offset:16384\n" \
  "v_mfma_f32_16x16x32_bf16 v[28:31], a[40:43], v[84:87], v[28:31]\n" \
  "ds_read_b64_tr_b16 a[82:83], v118 offset:16896\n" \
  "v_mfma_f32_16x16x32_bf16 v[36:39], v[84:87], a[168:171], v[36:39]\n" \
  "s_add_u32 m0, s91, 118784\n" \
  "s_sub_u32 s92, s96, 4062080\n" \
  "buffer_load_dwordx4 v7, s[72:75], s92 offen lds\n" \
  "v_mfma_f32_16x16x32_bf16 v[24:27], v[168:171], v[88:91], v[24:27]\n" \
  "ds_read_b64_tr_b16 a[84:85], v118 offset:20480\n" \
  "v_mfma_f32_16x16x32_bf16 v[32:35], a[40:43], v[88:91], v[32:35]\n" \
  "ds_read_b64_tr_b16 a[86:87], v118 offset:20992\n" \
  "v_mfma_f32_16x16x32_bf16 v[40:43], v[88:91], a[168:171], v[40:43]\n" \
  "s_add_u32 m0, s91, 122880\n" \
  "s_sub_u32 s92, s96, 4061952\n" \
  "buffer_load_dwordx4 v7, s[72:75], s92 offen lds\n" \
  "v_mfma_f32_16x16x32_bf16 v[20:23], v[172:175], v[92:95], v[20:23]\n" \
  "ds_read_b64_tr_b16 a[88:89], v118 offset:24576\n" \
  "v_mfma_f32_16x16x32_bf16 v[28:31], a[44:47], v[92:95], v[28:31]\n" \
  "ds_read_b64_tr_b16 a[90:91], v118 offset:25088\n" \
  "v_mfma_f32_16x16x32_bf16 v[36:39], v[92:95], a[172:175], v[36:39]\n" \
  "s_add_u32 m0, s91, 126976\n" \
  "s_sub_u32 s92, s96, 4061824\n" \
  "buffer_load_dwordx4 v7, s[72:75], s92 offen lds\n" \
  "v_mfma_f32_16x16x32_bf16 v[24:27], v[172:175], v[96:99], v[24:27]\n" \
  "ds_read_b64_tr_b16 a[92:93], v118 offset:28672\n" \
  "v_mfma_f32_16x16x32_bf16 v[32:35], a[44:47], v[96:99], v[32:35]\n" \
  "ds_read_b64_tr_b16 a[94:95], v118 offset:29184\n" \
  "v_mfma_f32_16x16x32_bf16 v[40:43], v[96:99], a[172:175], v[40:43]\n" \
  "s_waitcnt vmcnt(16) lgkmcnt(0)\n" \
  "s_barrier\n" \
  "ds_read_b128 v[68:71], v121 offset:8192\n" \
  "ds_read_b128 v[72:75], v121 offset:10240\n" \
  "ds_read_b128 v[76:79], v122 offset:8192\n" \
  "ds_read_b128 v[80:83], v122 offset:10240\n" \
  "ds_read_b128 v[84:87], v121 offset:12288\n" \
  "ds_read_b128 v[88:91], v121 offset:14336\n" \
  "ds_read_b128 v[92:95], v122 offset:12288\n" \
  "ds_read_b128 v[96:99], v122 offset:14336\n" \
  "s_add_u32 m0, s86, 0\n" \
  "s_mov_b64 s[68:69], %2\n" \
  "s_and_b32 s69, s69, 0xffff\n" \
  "s_lshl_b32 s92, %8, 19\n" \
  "s_add_u32 s92, s92, 6291456\n" \
  "buffer_load_dwordx4 v8, s[68:71], s92 offen lds\n" \
  "s_waitcnt lgkmcnt(0)\n" \
  "v_mfma_f32_16x16x32_bf16 v[20:23], v[176:179], v[68:71], v[20:23]\n" \
  "ds_read_b64_tr_b16 a[192:193], v118 offset:32768\n" \
  "v_mfma_f32_16x16x32_bf16 v[28:31], a[48:51], v[68:71], v[28:31]\n" \
  "ds_read_b64_tr_b16 a[194:195], v118 offset:33280\n" \
  "v_mfma_f32_16x16x32_bf16 v[36:39], v[68:71], a[176:179], v[36:39]\n" \
  "s_add_u32 m0, s86, 1024\n" \
  "s_add_u32 s92, s92, 65536\n" \
  "buffer_load_dwordx4 v9, s[68:71], s92 offen lds\n" \
  "v_mfma_f32_16x16x32_bf16 v[24:27], v[176:179], v[72:75], v[24:27]\n" \
  "ds_read_b64_tr_b16 a[196:197], v118 offset:36864\n" \
  "v_mfma_f32_16x16x32_bf16 v[32:35], a[48:51], v[72:75], v[32:35]\n" \
  "ds_read_b64_tr_b16 a[198:199], v118 offset:37376\n" \
  "v_mfma_f32_16x16x32_bf16 v[40:43], v[72:75], a[176:179], v[40:43]\n" \
  "s_add_u32 m0, s86, 2048\n" \
  "s_add_u32 s92, s92, 65536\n" \
  "buffer_load_dwordx4 v8, s[68:71], s92 offen lds\n" \
  "v_mfma_f32_16x16x32_bf16 v[20:23], v[180:183], v[76:79], v[20:23]\n" \
  "ds_read_b64_tr_b16 a[200:201], v118 offset:40960\n" \
  "v_mfma_f32_16x16x32_bf16 v[28:31], a[52:55], v[76:79], v[28:31]\n" \
  "ds_read_b64_tr_b16 a[202:203], v118 offset:41472\n" \
  "v_mfma_f32_16x16x32_bf16 v[36:39], v[76:79], a[180:183], v[36:39]\n" \
  "s_add_u32 m0, s86, 3072\n" \
  "s_add_u32 s92, s92, 65536\n" \
  "buffer_load_dwordx4 v9, s[68:71], s92 offen lds\n" \
  "v_mfma_f32_16x16x32_bf16 v[24:27], v[180:183], v[80:83], v[24:27]\n" \
  "ds_read_b64_tr_b16 a[204:205], v118 offset:45056\n" \
  "v_mfma_f32_16x16x32_bf16 v[32:35], a[52:55], v[80:83], v[32:35]\n" \
  "ds_read_b64_tr_b16 a[206:207], v118 offset:45568\n" \
  "v_mfma_f32_16x16x32_bf16 v[40:43], v[80:83], a[180:183], v[40:43]\n" \
  "s_add_u32 m0, s86, 4096\n" \
  "s_add_u32 s92, s92, 65536\n" \
  "buffer_load_dwordx4 v8, s[68:71], s92 offen lds\n" \
  "v_mfma_f32_16x16x32_bf16 v[20:23], v[184:187], v[84:87], v[20:23]\n" \
  "ds_read_b64_tr_b16 a[208:209], v118 offset:49152\n" \
  "v_mfma_f32_16x16x32_bf16 v[28:31], a[56:59], v[84:87], v[28:31]\n" \
  "ds_read_b64_tr_b16 a[210:211], v118 offset:49664\n" \
  "v_mfma_f32_16x16x32_bf16 v[36:39], v[84:87], a[184:187], v[36:39]\n" \
  "s_add_u32 m0, s86, 5120\n" \
  "s_add_u32 s92, s92, 65536\n" \
  "buffer_load_dwordx4 v9, s[68:71], s92 offen lds\n" \
  "v_mfma_f32_16x16x32_bf16 v[24:27], v[184:187], v[88:91], v[24:27]\n" \
  "ds_read_b64_tr_b16 a[212:213], v118 offset:53248\n" \
  "v_mfma_f32_16x16x32_bf16 v[32:35], a[56:59], v[88:91], v[32:35]\n" \
  "ds_read_b64_tr_b16 a[214:215], v118 offset:53760\n" \
  "v_mfma_f32_16x16x32_bf16 v[40:43], v[88:91], a[184:187], v[40:43]\n" \
  "s_add_u32 m0, s86, 6144\n" \
  "s_add_u32 s92, s92, 65536\n" \
  "buffer_load_dwordx4 v8, s[68:71], s92 offen lds\n" \
  "v_mfma_f32_16x16x32_bf16 v[20:23], v[188:191], v[92:95], v[20:23]\n" \
  "ds_read_b64_tr_b16 a[216:217], v118 offset:57344\n" \
  "v_mfma_f32_16x16x32_bf16 v[28:31], a[60:63], v[92:95], v[28:31]\n" \
  "ds_read_b64_tr_b16 a[218:219], v118 offset:57856\n" \
  "v_mfma_f32_16x16x32_bf16 v[36:39], v[92:95], a[188:191], v[36:39]\n" \
  "s_add_u32 m0, s86, 7168\n" \
  "s_add_u32 s92, s92, 65536\n" \
  "buffer_load_dwordx4 v9, s[68:71], s92 offen lds\n" \
  "v_mfma_f32_16x16x32_bf16 v[24:27], v[188:191], v[96:99], v[24:27]\n" \
  "ds_read_b64_tr_b16 a[220:221], v118 offset:61440\n" \
  "v_mfma_f32_16x16x32_bf16 v[32:35], a[60:63], v[96:99], v[32:35]\n" \
  "ds_read_b64_tr_b16 a[222:223], v118 offset:61952\n" \
  "v_mfma_f32_16x16x32_bf16 v[40:43], v[96:99], a[188:191], v[40:43]\n" \
  "s_waitcnt vmcnt(16) lgkmcnt(0)\n" \
  "s_barrier\n" \
  "s_lshl_b32 s84, %11, 7\n" \
  "buffer_load_dwordx4 v[124:127], off, s[80:83], s84 sc1\n" \
  "ds_read_b128 v[68:71], v121 offset:16384\n" \
  "ds_read_b128 v[72:75], v121 offset:18432\n" \
  "ds_read_b128 v[76:79], v122 offset:16384\n" \
  "ds_read_b128 v[80:83], v122 offset:18432\n" \
  "ds_read_b128 v[84:87], v121 offset:20480\n" \
  "ds_read_b128 v[88:91], v121 offset:22528\n" \
  "ds_read_b128 v[92:95], v122 offset:20480\n" \
  "ds_read_b128 v[96:99], v122 offset:22528\n" \
  "s_add_u32 m0, s86, 32768\n" \
  "s_mov_b64 s[68:69], %3\n" \
  "s_and_b32 s69, s69, 0xffff\n" \
  "s_lshl_b32 s92, %8, 19\n" \
  "s_add_u32 s92, s92, 6291456\n" \
  "buffer_load_dwordx4 v8, s[68:71], s92 offen lds\n" \
  "s_waitcnt lgkmcnt(0)\n" \
  "v_mfma_f32_16x16x32_bf16 v[44:47], v[160:163], v[68:71], v[44:47]\n" \
  "ds_read_b64_tr_b16 v[192:193], v119 offset:0\n" \
  "v_mfma_f32_16x16x32_bf16 v[52:55], a[32:35], v[68:71], v[52:55]\n" \
  "ds_read_b64_tr_b16 v[194:195], v119 offset:512\n" \
  "v_mfma_f32_16x16x32_bf16 v[60:63], v[68:71], a[160:163], v[60:63]\n" \
  "s_add_u32 m0, s86, 33792\n" \
  "s_add_u32 s92, s92, 65536\n" \
  "buffer_load_dwordx4 v9, s[68:71], s92 offen lds\n" \
  "v_mfma_f32_16x16x32_bf16 v[48:51], v[160:163], v[72:75], v[48:51]\n" \
  "ds_read_b64_tr_b16 v[196:197], v119 offset:4096\n" \
  "v_mfma_f32_16x16x32_bf16 v[56:59], a[32:35], v[72:75], v[56:59]\n" \
  "ds_read_b64_tr_b16 v[198:199], v119 offset:4608\n" \
  "v_mfma_f32_16x16x32_bf16 v[64:67], v[72:75], a[160:163], v[64:67]\n" \
  "s_add_u32 m0, s86, 34816\n" \
  "s_add_u32 s92, s92, 65536\n" \
  "buffer_load_dwordx4 v8, s[68:71], s92 offen lds\n" \
  "v_mfma_f32_16x16x32_bf16 v[44:47], v[164:167], v[76:79], v[44:47]\n" \
  "ds_read_b64_tr_b16 v[200:201], v119 offset:8192\n" \
  "v_mfma_f32_16x16x32_bf16 v[52:55], a[36:39], v[76:79], v[52:55]\n" \
  "ds_read_b64_tr_b16 v[202:203], v119 offset:8704\n" \
  "v_mfma_f32_16x16x32_bf16 v[60:63], v[76:79], a[164:167], v[60:63]\n" \
  "s_add_u32 m0, s86, 35840\n" \
  "s_add_u32 s92, s92, 65536\n" \
  "buffer_load_dwordx4 v9, s[68:71], s92 offen lds\n" \
  "v_mfma_f32_16x16x32_bf16 v[48:51], v[164:167], v[80:83], v[48:51]\n" \
  "ds_read_b64_tr_b16 v[204:205], v119 offset:12288\n" \
  "v_mfma_f32_16x16x32_bf16 v[56:59], a[36:39], v[80:83], v[56:59]\n" \
  "ds_read_b64_tr_b16 v[206:207], v119 offset:12800\n" \
  "v_mfma_f32_16x16x32_bf16 v[64:67], v[80:83], a[164:167], v[64:67]\n" \
  "s_add_u32 m0, s86, 36864\n" \
  "s_add_u32 s92, s92, 65536\n" \
  "buffer_load_dwordx4 v8, s[68:71], s92 offen lds\n" \
  "v_mfma_f32_16x16x32_bf16 v[44:47], v[168:171], v[84:87], v[44:47]\n" \
  "ds_read_b64_tr_b16 v[208:209], v119 offset:16384\n" \
  "v_mfma_f32_16x16x32_bf16 v[52:55], a[40:43], v[84:87], v[52:55]\n" \
  "ds_read_b64_tr_b16 v[210:211], v119 offset:16896\n" \
  "v_mfma_f32_16x16x32_bf16 v[60:63], v[84:87], a[168:171], v[60:63]\n" \
  "s_add_u32 m0, s86, 37888\n" \
  "s_add_u32 s92, s92, 65536\n" \
  "buffer_load_dwordx4 v9, s[68:71], s92 offen lds\n" \
  "v_mfma_f32_16x16x32_bf16 v[48:51], v[168:171], v[88:91], v[48:51]\n" \
  "ds_read_b64_tr_b16 v[212:213], v119 offset:20480\n" \
  "v_mfma_f32_16x16x32_bf16 v[56:59], a[40:43], v[88:91], v[56:59]\n" \
  "ds_read_b64_tr_b16 v[214:215], v119 offset:20992\n" \
  "v_mfma_f32_16x16x32_bf16 v[64:67], v[88:91], a[168:171], v[64:67]\n" \
  "s_add_u32 m0, s86, 38912\n" \
  "s_add_u32 s92, s92, 65536\n" \
  "buffer_load_dwordx4 v8, s[68:71], s92 offen lds\n" \
  "v_mfma_f32_16x16x32_bf16 v[44:47], v[172:175], v[92:95], v[44:47]\n" \
  "ds_read_b64_tr_b16 v[216:217], v119 offset:24576\n" \
  "v_mfma_f32_16x16x32_bf16 v[52:55], a[44:47], v[92:95], v[52:55]\n" \
  "ds_read_b64_tr_b16 v[218:219], v119 offset:25088\n" \
  "v_mfma_f32_16x16x32_bf16 v[60:63], v[92:95], a[172:175], v[60:63]\n" \
  "s_add_u32 m0, s86, 39936\n" \
  "s_add_u32 s92, s92, 65536\n" \
  "buffer_load_dwordx4 v9, s[68:71], s92 offen lds\n" \
  "v_mfma_f32_16x16x32_bf16 v[48:51], v[172:175], v[96:99], v[48:51]\n" \
  "ds_read_b64_tr_b16 v[220:221], v119 offset:28672\n" \
  "v_mfma_f32_16x16x32_bf16 v[56:59], a[44:47], v[96:99], v[56:59]\n" \
  "ds_read_b64_tr_b16 v[222:223], v119 offset:29184\n" \
  "v_mfma_f32_16x16x32_bf16 v[64:67], v[96:99], a[172:175], v[64:67]\n" \
  "s_waitcnt vmcnt(17) lgkmcnt(0)\n" \
  "s_barrier\n" \
  "ds_read_b128 v[68:71], v121 offset:24576\n" \
  "ds_read_b128 v[72:75], v121 offset:26624\n" \
  "ds_read_b128 v[76:79], v122 offset:24576\n" \
  "ds_read_b128 v[80:83], v122 offset:26624\n" \
  "ds_read_b128 v[84:87], v121 offset:28672\n" \
  "ds_read_b128 v[88:91], v121 offset:30720\n" \
  "ds_read_b128 v[92:95], v122 offset:28672\n" \
  "ds_read_b128 v[96:99], v122 offset:30720\n" \
  "s_add_u32 m0, s86, 65536\n" \
  "s_mov_b64 s[68:69], %1\n" \
  "s_and_b32 s69, s69, 0xffff\n" \
  "s_lshl_b32 s92, %8, 19\n" \
  "s_add_u32 s92, s92, 6291456\n" \
  "buffer_load_dwordx4 v8, s[68:71], s92 offen lds\n" \
  "s_waitcnt lgkmcnt(0)\n" \
  "v_mfma_f32_16x16x32_bf16 v[44:47], v[176:179], v[68:71], v[44:47]\n" \
  "v_mfma_f32_16x16x32_bf16 v[52:55], a[48:51], v[68:71], v[52:55]\n" \
  "v_mfma_f32_16x16x32_bf16 v[60:63], v[68:71], a[176:179], v[60:63]\n" \
  "s_add_u32 m0, s86, 66560\n" \
  "s_add_u32 s92, s92, 65536\n" \
  "buffer_load_dwordx4 v9, s[68:71], s92 offen lds\n" \
  "v_mfma_f32_16x16x32_bf16 v[48:51], v[176:179], v[72:75], v[48:51]\n" \
  "v_mfma_f32_16x16x32_bf16 v[56:59], a[48:51], v[72:75], v[56:59]\n" \
  "v_mfma_f32_16x16x32_bf16 v[64:67], v[72:75], a[176:179], v[64:67]\n" \
  "s_add_u32 m0, s86, 67584\n" \
  "s_add_u32 s92, s92, 65536\n" \
  "buffer_load_dwordx4 v8, s[68:71], s92 offen lds\n" \
  "v_mfma_f32_16x16x32_bf16 v[44:47], v[180:183], v[76:79], v[44:47]\n" \
  "v_mfma_f32_16x16x32_bf16 v[52:55], a[52:55], v[76:79], v[52:55]\n" \
  "v_mfma_f32_16x16x32_bf16 v[60:63], v[76:79], a[180:183], v[60:63]\n" \
  "s_add_u32 m0, s86, 68608\n" \
  "s_add_u32 s92, s92, 65536\n" \
  "buffer_load_dwordx4 v9, s[68:71], s92 offen lds\n" \
  "v_mfma_f32_16x16x32_bf16 v[48:51], v[180:183], v[80:83], v[48:51]\n" \
  "v_mfma_f32_16x16x32_bf16 v[56:59], a[52:55], v[80:83], v[56:59]\n" \
  "v_mfma_f32_16x16x32_bf16 v[64:67], v[80:83], a[180:183], v[64:67]\n" \
  "s_add_u32 m0, s86, 69632\n" \
  "s_add_u32 s92, s92, 65536\n" \
  "buffer_load_dwordx4 v8, s[68:71], s92 offen lds\n" \
  "v_mfma_f32_16x16x32_bf16 v[44:47], v[184:187], v[84:87], v[44:47]\n" \
  "v_mfma_f32_16x16x32_bf16 v[52:55], a[56:59], v[84:87], v[52:55]\n" \
  "v_mfma_f32_16x16x32_bf16 v[60:63], v[84:87], a[184:187], v[60:63]\n" \
  "s_add_u32 m0, s86, 70656\n" \
  "s_add_u32 s92, s92, 65536\n" \
  "buffer_load_dwordx4 v9, s[68:71], s92 offen lds\n" \
  "v_mfma_f32_16x16x32_bf16 v[48:51], v[184:187], v[88:91], v[48:51]\n" \
  "v_mfma_f32_16x16x32_bf16 v[56:59], a[56:59], v[88:91], v[56:59]\n" \
  "v_mfma_f32_16x16x32_bf16 v[64:67], v[88:91], a[184:187], v[64:67]\n" \
  "s_add_u32 m0, s86, 71680\n" \
  "s_add_u32 s92, s92, 65536\n" \
  "buffer_load_dwordx4 v8, s[68:71], s92 offen lds\n" \
  "v_mfma_f32_16x16x32_bf16 v[44:47], v[188:191], v[92:95], v[44:47]\n" \
  "v_mfma_f32_16x16x32_bf16 v[52:55], a[60:63], v[92:95], v[52:55]\n" \
  "v_mfma_f32_16x16x32_bf16 v[60:63], v[92:95], a[188:191], v[60:63]\n" \
  "s_add_u32 m0, s86, 72704\n" \
  "s_add_u32 s92, s92, 65536\n" \
  "buffer_load_dwordx4 v9, s[68:71], s92 offen lds\n" \
  "v_mfma_f32_16x16x32_bf16 v[48:51], v[188:191], v[96:99], v[48:51]\n" \
  "v_mfma_f32_16x16x32_bf16 v[56:59], a[60:63], v[96:99], v[56:59]\n" \
  "v_mfma_f32_16x16x32_bf16 v[64:67], v[96:99], a[188:191], v[64:67]\n" \
  "s_waitcnt vmcnt(17) lgkmcnt(0)\n" \
  "s_barrier\n" \
  "ds_read_b128 v[68:71], v12 offset:32768\n" \
  "ds_read_b128 v[72:75], v12 offset:34816\n" \
  "ds_read_b128 v[76:79], v13 offset:32768\n" \
  "ds_read_b128 v[80:83], v13 offset:34816\n" \
  "ds_read_b128 v[84:87], v12 offset:36864\n" \
  "ds_read_b128 v[88:91], v12 offset:38912\n" \
  "ds_read_b128 v[92:95], v13 offset:36864\n" \
  "ds_read_b128 v[96:99], v13 offset:38912\n" \
  "s_add_u32 m0, s91, 131072\n" \
  "s_sub_u32 s92, s96, 4127232\n" \
  "buffer_load_dwordx4 v7, s[72:75], s92 offen lds\n" \
  "s_waitcnt lgkmcnt(0)\n" \
  "v_mfma_f32_16x16x32_bf16 v[20:23], v[192:195], v[68:71], v[20:23]\n" \
  "ds_read_b64_tr_b16 a[96:97], v118 offset:0\n" \
  "v_mfma_f32_16x16x32_bf16 v[28:31], a[64:67], v[68:71], v[28:31]\n" \
  "ds_read_b64_tr_b16 a[98:99], v118 offset:512\n" \
  "v_mfma_f32_16x16x32_bf16 v[36:39], v[68:71], a[192:195], v[36:39]\n" \
  "s_add_u32 m0, s91, 135168\n" \
  "s_sub_u32 s92, s96, 4127104\n" \
  "buffer_load_dwordx4 v7, s[72:75], s92 offen lds\n" \
  "v_mfma_f32_16x16x32_bf16 v[24:27], v[192:195], v[72:75], v[24:27]\n" \
  "ds_read_b64_tr_b16 a[100:101], v118 offset:4096\n" \
  "v_mfma_f32_16x16x32_bf16 v[32:35], a[64:67], v[72:75], v[32:35]\n" \
  "ds_read_b64_tr_b16 a[102:103], v118 offset:4608\n" \
  "v_mfma_f32_16x16x32_bf16 v[40:43], v[72:75], a[192:195], v[40:43]\n" \
  "s_add_u32 m0, s91, 139264\n" \
  "s_sub_u32 s92, s96, 4126976\n" \
  "buffer_load_dwordx4 v7, s[72:75], s92 offen lds\n" \
  "v_mfma_f32_16x16x32_bf16 v[20:23], v[196:199], v[76:79], v[20:23]\n" \
  "ds_read_b64_tr_b16 a[104:105], v118 offset:8192\n" \
  "v_mfma_f32_16x16x32_bf16 v[28:31], a[68:71], v[76:79], v[28:31]\n" \
  "ds_read_b64_tr_b16 a[106:107], v118 offset:8704\n" \
  "v_mfma_f32_16x16x32_bf16 v[36:39], v[76:79], a[196:199], v[36:39]\n" \
  "s_add_u32 m0, s91, 143360\n" \
  "s_sub_u32 s92, s96, 4126848\n" \
  "buffer_load_dwordx4 v7, s[72:75], s92 offen lds\n" \
  "v_mfma_f32_16x16x32_bf16 v[24:27], v[196:199], v[80:83], v[24:27]\n" \
  "ds_read_b64_tr_b16 a[108:109], v118 offset:12288\n" \
  "v_mfma_f32_16x16x32_bf16 v[32:35], a[68:71], v[80:83], v[32:35]\n" \
  "ds_read_b64_tr_b16 a[110:111], v118 offset:12800\n" \
  "v_mfma_f32_16x16x32_bf16 v[40:43], v[80:83], a[196:199], v[40:43]\n" \
  "s_add_u32 m0, s91, 147456\n" \
  "s_sub_u32 s92, s96, 4061696\n" \
  "buffer_load_dwordx4 v7, s[72:75], s92 offen lds\n" \
  "v_mfma_f32_16x16x32_bf16 v[20:23], v[200:203], v[84:87], v[20:23]\n" \
  "ds_read_b64_tr_b16 a[112:113], v118 offset:16384\n" \
  "v_mfma_f32_16x16x32_bf16 v[28:31], a[72:75], v[84:87], v[28:31]\n" \
  "ds_read_b64_tr_b16 a[114:115], v118 offset:16896\n" \
  "v_mfma_f32_16x16x32_bf16 v[36:39], v[84:87], a[200:203], v[36:39]\n" \
  "s_add_u32 m0, s91, 151552\n" \
  "s_sub_u32 s92, s96, 4061568\n" \
  "buffer_load_dwordx4 v7, s[72:75], s92 offen lds\n" \
  "v_mfma_f32_16x16x32_bf16 v[24:27], v[200:203], v[88:91], v[24:27]\n" \
  "ds_read_b64_tr_b16 a[116:117], v118 offset:20480\n" \
  "v_mfma_f32_16x16x32_bf16 v[32:35], a[72:75], v[88:91], v[32:35]\n" \
  "ds_read_b64_tr_b16 a[118:119], v118 offset:20992\n" \
  "v_mfma_f32_16x16x32_bf16 v[40:43], v[88:91], a[200:203], v[40:43]\n" \
  "s_add_u32 m0, s91, 155648\n" \
  "s_sub_u32 s92, s96, 4061440\n" \
  "buffer_load_dwordx4 v7, s[72:75], s92 offen lds\n" \
  "v_mfma_f32_16x16x32_bf16 v[20:23], v[204:207], v[92:95], v[20:23]\n" \
  "ds_read_b64_tr_b16 a[120:121], v118 offset:24576\n" \
  "v_mfma_f32_16x16x32_bf16 v[28:31], a[76:79], v[92:95], v[28:31]\n" \
  "ds_read_b64_tr_b16 a[122:123], v118 offset:25088\n" \
  "v_mfma_f32_16x16x32_bf16 v[36:39], v[92:95], a[204:207], v[36:39]\n" \
  "s_add_u32 m0, s91, 159744\n" \
  "s_sub_u32 s92, s96, 4061312\n" \
  "buffer_load_dwordx4 v7, s[72:75], s92 offen lds\n" \
  "v_mfma_f32_16x16x32_bf16 v[24:27], v[204:207], v[96:99], v[24:27]\n" \
  "ds_read_b64_tr_b16 a[124:125], v118 offset:28672\n" \
  "v_mfma_f32_16x16x32_bf16 v[32:35], a[76:79], v[96:99], v[32:35]\n" \
  "ds_read_b64_tr_b16 a[126:127], v118 offset:29184\n" \
  "v_mfma_f32_16x16x32_bf16 v[40:43], v[96:99], a[204:207], v[40:43]\n" \
  "s_waitcnt vmcnt(16) lgkmcnt(0)\n" \
  "s_barrier\n" \
  "ds_read_b128 v[68:71], v12 offset:40960\n" \
  "ds_read_b128 v[72:75], v12 offset:43008\n" \
  "ds_read_b128 v[76:79], v13 offset:40960\n" \
  "ds_read_b128 v[80:83], v13 offset:43008\n" \
  "ds_read_b128 v[84:87], v12 offset:45056\n" \
  "ds_read_b128 v[88:91], v12 offset:47104\n" \
  "ds_read_b128 v[92:95], v13 offset:45056\n" \
  "ds_read_b128 v[96:99], v13 offset:47104\n" \
  "s_waitcnt lgkmcnt(0)\n" \
  "v_mfma_f32_16x16x32_bf16 v[20:23], v[208:211], v[68:71], v[20:23]\n" \
  "ds_read_b64_tr_b16 a[224:225], v118 offset:32768\n" \
  "v_mfma_f32_16x16x32_bf16 v[28:31], a[80:83], v[68:71], v[28:31]\n" \
  "ds_read_b64_tr_b16 a[226:227], v118 offset:33280\n" \
  "v_mfma_f32_16x16x32_bf16 v[36:39], v[68:71], a[208:211], v[36:39]\n" \
  "v_mfma_f32_16x16x32_bf16 v[24:27], v[208:211], v[72:75], v[24:27]\n" \
  "ds_read_b64_tr_b16 a[228:229], v118 offset:36864\n" \
  "v_mfma_f32_16x16x32_bf16 v[32:35], a[80:83], v[72:75], v[32:35]\n" \
  "ds_read_b64_tr_b16 a[230:231], v118 offset:37376\n" \
  "v_mfma_f32_16x16x32_bf16 v[40:43], v[72:75], a[208:211], v[40:43]\n" \
  "v_mfma_f32_16x16x32_bf16 v[20:23], v[212:215], v[76:79], v[20:23]\n" \
  "ds_read_b64_tr_b16 a[232:233], v118 offset:40960\n" \
  "v_mfma_f32_16x16x32_bf16 v[28:31], a[84:87], v[76:79], v[28:31]\n" \
  "ds_read_b64_tr_b16 a[234:235], v118 offset:41472\n" \
  "v_mfma_f32_16x16x32_bf16 v[36:39], v[76:79], a[212:215], v[36:39]\n" \
  "v_mfma_f32_16x16x32_bf16 v[24:27], v[212:215], v[80:83], v[24:27]\n" \
  "ds_read_b64_tr_b16 a[236:237], v118 offset:45056\n" \
  "v_mfma_f32_16x16x32_bf16 v[32:35], a[84:87], v[80:83], v[32:35]\n" \
  "ds_read_b64_tr_b16 a[238:239], v118 offset:45568\n" \
  "v_mfma_f32_16x16x32_bf16 v[40:43], v[80:83], a[212:215], v[40:43]\n" \
  "v_mfma_f32_16x16x32_bf16 v[20:23], v[216:219], v[84:87], v[20:23]\n" \
  "ds_read_b64_tr_b16 a[240:241], v118 offset:49152\n" \
  "v_mfma_f32_16x16x32_bf16 v[28:31], a[88:91], v[84:87], v[28:31]\n" \
  "ds_read_b64_tr_b16 a[242:243], v118 offset:49664\n" \
  "v_mfma_f32_16x16x32_bf16 v[36:39], v[84:87], a[216:219], v[36:39]\n" \
  "v_mfma_f32_16x16x32_bf16 v[24:27], v[216:219], v[88:91], v[24:27]\n" \
  "ds_read_b64_tr_b16 a[244:245], v118 offset:53248\n" \
  "v_mfma_f32_16x16x32_bf16 v[32:35], a[88:91], v[88:91], v[32:35]\n" \
  "ds_read_b64_tr_b16 a[246:247], v118 offset:53760\n" \
  "v_mfma_f32_16x16x32_bf16 v[40:43], v[88:91], a[216:219], v[40:43]\n" \
  "v_mfma_f32_16x16x32_bf16 v[20:23], v[220:223], v[92:95], v[20:23]\n" \
  "ds_read_b64_tr_b16 a[248:249], v118 offset:57344\n" \
  "v_mfma_f32_16x16x32_bf16 v[28:31], a[92:95], v[92:95], v[28:31]\n" \
  "ds_read_b64_tr_b16 a[250:251], v118 offset:57856\n" \
  "v_mfma_f32_16x16x32_bf16 v[36:39], v[92:95], a[220:223], v[36:39]\n" \
  "v_mfma_f32_16x16x32_bf16 v[24:27], v[220:223], v[96:99], v[24:27]\n" \
  "ds_read_b64_tr_b16 a[252:253], v118 offset:61440\n" \
  "v_mfma_f32_16x16x32_bf16 v[32:35], a[92:95], v[96:99], v[32:35]\n" \
  "ds_read_b64_tr_b16 a[254:255], v118 offset:61952\n" \
  "v_mfma_f32_16x16x32_bf16 v[40:43], v[96:99], a[220:223], v[40:43]\n" \
  "s_waitcnt vmcnt(8) lgkmcnt(0)\n" \
  "s_barrier\n" \
  "ds_read_b128 v[68:71], v12 offset:49152\n" \
  "ds_read_b128 v[72:75], v12 offset:51200\n" \
  "ds_read_b128 v[76:79], v13 offset:49152\n" \
  "ds_read_b128 v[80:83], v13 offset:51200\n" \
  "ds_read_b128 v[84:87], v12 offset:53248\n" \
  "ds_read_b128 v[88:91], v12 offset:55296\n" \
  "ds_read_b128 v[92:95], v13 offset:53248\n" \
  "ds_read_b128 v[96:99], v13 offset:55296\n" \
  "s_waitcnt lgkmcnt(0)\n" \
  "v_mfma_f32_16x16x32_bf16 v[44:47], v[192:195], v[68:71], v[44:47]\n" \
  "ds_read_b64_tr_b16 v[224:225], v119 offset:0\n" \
  "v_mfma_f32_16x16x32_bf16 v[52:55], a[64:67], v[68:71], v[52:55]\n" \
  "ds_read_b64_tr_b16 v[226:227], v119 offset:512\n" \
  "v_mfma_f32_16x16x32_bf16 v[60:63], v[68:71], a[192:195], v[60:63]\n" \
  "v_mfma_f32_16x16x32_bf16 v[48:51], v[192:195], v[72:75], v[48:51]\n" \
  "ds_read_b64_tr_b16 v[228:229], v119 offset:4096\n" \
  "v_mfma_f32_16x16x32_bf16 v[56:59], a[64:67], v[72:75], v[56:59]\n" \
  "ds_read_b64_tr_b16 v[230:231], v119 offset:4608\n" \
  "v_mfma_f32_16x16x32_bf16 v[64:67], v[72:75], a[192:195], v[64:67]\n" \
  "v_mfma_f32_16x16x32_bf16 v[44:47], v[196:199], v[76:79], v[44:47]\n" \
  "ds_read_b64_tr_b16 v[232:233], v119 offset:8192\n" \
  "v_mfma_f32_16x16x32_bf16 v[52:55], a[68:71], v[76:79], v[52:55]\n" \
  "ds_read_b64_tr_b16 v[234:235], v119 offset:8704\n" \
  "v_mfma_f32_16x16x32_bf16 v[60:63], v[76:79], a[196:199], v[60:63]\n" \
  "v_mfma_f32_16x16x32_bf16 v[48:51], v[196:199], v[80:83], v[48:51]\n" \
  "ds_read_b64_tr_b16 v[236:237], v119 offset:12288\n" \
  "v_mfma_f32_16x16x32_bf16 v[56:59], a[68:71], v[80:83], v[56:59]\n" \
  "ds_read_b64_tr_b16 v[238:239], v119 offset:12800\n" \
  "v_mfma_f32_16x16x32_bf16 v[64:67], v[80:83], a[196:199], v[64:67]\n" \
  "v_mfma_f32_16x16x32_bf16 v[44:47], v[200:203], v[84:87], v[44:47]\n" \
  "ds_read_b64_tr_b16 v[240:241], v119 offset:16384\n" \
  "v_mfma_f32_16x16x32_bf16 v[52:55], a[72:75], v[84:87], v[52:55]\n" \
  "ds_read_b64_tr_b16 v[242:243], v119 offset:16896\n" \
  "v_mfma_f32_16x16x32_bf16 v[60:63], v[84:87], a[200:203], v[60:63]\n" \
  "v_mfma_f32_16x16x32_bf16 v[48:51], v[200:203], v[88:91], v[48:51]\n" \
  "ds_read_b64_tr_b16 v[244:245], v119 offset:20480\n" \
  "v_mfma_f32_16x16x32_bf16 v[56:59], a[72:75], v[88:91], v[56:59]\n" \
  "ds_read_b64_tr_b16 v[246:247], v119 offset:20992\n" \
  "v_mfma_f32_16x16x32_bf16 v[64:67], v[88:91], a[200:203], v[64:67]\n" \
  "v_mfma_f32_16x16x32_bf16 v[44:47], v[204:207], v[92:95], v[44:47]\n" \
  "ds_read_b64_tr_b16 v[248:249], v119 offset:24576\n" \
  "v_mfma_f32_16x16x32_bf16 v[52:55], a[76:79], v[92:95], v[52:55]\n" \
  "ds_read_b64_tr_b16 v[250:251], v119 offset:25088\n" \
  "v_mfma_f32_16x16x32_bf16 v[60:63], v[92:95], a[204:207], v[60:63]\n" \
  "v_mfma_f32_16x16x32_bf16 v[48:51], v[204:207], v[96:99], v[48:51]\n" \
  "ds_read_b64_tr_b16 v[252:253], v119 offset:28672\n" \
  "v_mfma_f32_16x16x32_bf16 v[56:59], a[76:79], v[96:99], v[56:59]\n" \
  "ds_read_b64_tr_b16 v[254:255], v119 offset:29184\n" \
  "v_mfma_f32_16x16x32_bf16 v[64:67], v[96:99], a[204:207], v[64:67]\n" \
  "s_waitcnt vmcnt(0) lgkmcnt(0)\n" \
  "s_barrier\n" \
  "ds_read_b128 v[68:71], v12 offset:57344\n" \
  "ds_read_b128 v[72:75], v12 offset:59392\n" \
  "ds_read_b128 v[76:79], v13 offset:57344\n" \
  "ds_read_b128 v[80:83], v13 offset:59392\n" \
  "ds_read_b128 v[84:87], v12 offset:61440\n" \
  "ds_read_b128 v[88:91], v12 offset:63488\n" \
  "ds_read_b128 v[92:95], v13 offset:61440\n" \
  "ds_read_b128 v[96:99], v13 offset:63488\n" \
  "s_add_u32 m0, s91, 0\n" \
  "s_nop 0\n" \
  "buffer_load_dwordx4 v7, s[72:75], s87 offen lds\n" \
  "s_add_u32 s87, s87, 128\n" \
  "s_waitcnt lgkmcnt(0)\n" \
  "v_mfma_f32_16x16x32_bf16 v[44:47], v[208:211], v[68:71], v[44:47]\n" \
  "v_mfma_f32_16x16x32_bf16 v[52:55], a[80:83], v[68:71], v[52:55]\n" \
  "v_mfma_f32_16x16x32_bf16 v[60:63], v[68:71], a[208:211], v[60:63]\n" \
  "s_add_u32 m0, s91, 4096\n" \
  "s_nop 0\n" \
  "buffer_load_dwordx4 v7, s[72:75], s87 offen lds\n" \
  "s_add_u32 s87, s87, 128\n" \
  "v_mfma_f32_16x16x32_bf16 v[48:51], v[208:211], v[72:75], v[48:51]\n" \
  "v_mfma_f32_16x16x32_bf16 v[56:59], a[80:83], v[72:75], v[56:59]\n" \
  "v_mfma_f32_16x16x32_bf16 v[64:67], v[72:75], a[208:211], v[64:67]\n" \
  "s_add_u32 m0, s91, 8192\n" \
  "s_nop 0\n" \
  "buffer_load_dwordx4 v7, s[72:75], s87 offen lds\n" \
  "s_add_u32 s87, s87, 128\n" \
  "v_mfma_f32_16x16x32_bf16 v[44:47], v[212:215], v[76:79], v[44:47]\n" \
  "v_mfma_f32_16x16x32_bf16 v[52:55], a[84:87], v[76:79], v[52:55]\n" \
  "v_mfma_f32_16x16x32_bf16 v[60:63], v[76:79], a[212:215], v[60:63]\n" \
  "s_add_u32 m0, s91, 12288\n" \
  "s_nop 0\n" \
  "buffer_load_dwordx4 v7, s[72:75], s87 offen lds\n" \
  "s_add_u32 s87, s87, 128\n" \
  "v_mfma_f32_16x16x32_bf16 v[48:51], v[212:215], v[80:83], v[48:51]\n" \
  "v_mfma_f32_16x16x32_bf16 v[56:59], a[84:87], v[80:83], v[56:59]\n" \
  "v_mfma_f32_16x16x32_bf16 v[64:67], v[80:83], a[212:215], v[64:67]\n" \
  "s_add_u32 m0, s91, 16384\n" \
  "s_nop 0\n" \
  "buffer_load_dwordx4 v7, s[72:75], s87 offen lds\n" \
  "s_add_u32 s87, s87, 128\n" \
  "v_mfma_f32_16x16x32_bf16 v[44:47], v[216:219], v[84:87], v[44:47]\n" \
  "v_mfma_f32_16x16x32_bf16 v[52:55], a[88:91], v[84:87], v[52:55]\n" \
  "v_mfma_f32_16x16x32_bf16 v[60:63], v[84:87], a[216:219], v[60:63]\n" \
  "s_add_u32 m0, s91, 20480\n" \
  "s_nop 0\n" \
  "buffer_load_dwordx4 v7, s[72:75], s87 offen lds\n" \
  "s_add_u32 s87, s87, 128\n" \
  "v_mfma_f32_16x16x32_bf16 v[48:51], v[216:219], v[88:91], v[48:51]\n" \
  "v_mfma_f32_16x16x32_bf16 v[56:59], a[88:91], v[88:91], v[56:59]\n" \
  "v_mfma_f32_16x16x32_bf16 v[64:67], v[88:91], a[216:219], v[64:67]\n" \
  "s_add_u32 m0, s91, 24576\n" \
  "s_nop 0\n" \
  "buffer_load_dwordx4 v7, s[72:75], s87 offen lds\n" \
  "s_add_u32 s87, s87, 128\n" \
  "v_mfma_f32_16x16x32_bf16 v[44:47], v[220:223], v[92:95], v[44:47]\n" \
  "v_mfma_f32_16x16x32_bf16 v[52:55], a[92:95], v[92:95], v[52:55]\n" \
  "v_mfma_f32_16x16x32_bf16 v[60:63], v[92:95], a[220:223], v[60:63]\n" \
  "s_add_u32 m0, s91, 28672\n" \
  "s_nop 0\n" \
  "buffer_load_dwordx4 v7, s[72:75], s87 offen lds\n" \
  "s_add_u32 s87, s87, 128\n" \
  "v_mfma_f32_16x16x32_bf16 v[48:51], v[220:223], v[96:99], v[48:51]\n" \
  "v_mfma_f32_16x16x32_bf16 v[56:59], a[92:95], v[96:99], v[56:59]\n" \
  "v_mfma_f32_16x16x32_bf16 v[64:67], v[96:99], a[220:223], v[64:67]\n" \
  "ds_read_b128 v[68:71], v121 offset:0\n" \
  "ds_read_b128 v[72:75], v121 offset:2048\n" \
  "ds_read_b128 v[76:79], v122 offset:0\n" \
  "ds_read_b128 v[80:83], v122 offset:2048\n" \
  "ds_read_b128 v[84:87], v121 offset:4096\n" \
  "ds_read_b128 v[88:91], v121 offset:6144\n" \
  "s_waitcnt lgkmcnt(4)\n" \
  "v_mfma_f32_16x16x32_bf16 v[20:23], v[224:227], v[68:71], v[20:23]\n" \
  "ds_read_b128 v[92:95], v122 offset:4096\n" \
  "v_mfma_f32_16x16x32_bf16 v[28:31], a[96:99], v[68:71], v[28:31]\n" \
  "ds_read_b128 v[96:99], v122 offset:6144\n" \
  "v_mfma_f32_16x16x32_bf16 v[36:39], v[68:71], a[224:227], v[36:39]\n" \
  "v_mfma_f32_16x16x32_bf16 v[24:27], v[224:227], v[72:75], v[24:27]\n" \
  "v_mfma_f32_16x16x32_bf16 v[32:35], a[96:99], v[72:75], v[32:35]\n" \
  "v_mfma_f32_16x16x32_bf16 v[40:43], v[72:75], a[224:227], v[40:43]\n" \
  "s_waitcnt lgkmcnt(4)\n" \
  "v_mfma_f32_16x16x32_bf16 v[20:23], v[228:231], v[76:79], v[20:23]\n" \
  "ds_read_b128 v[68:71], v121 offset:8192\n" \
  "v_mfma_f32_16x16x32_bf16 v[28:31], a[100:103], v[76:79], v[28:31]\n" \
  "ds_read_b128 v[72:75], v121 offset:10240\n" \
  "v_mfma_f32_16x16x32_bf16 v[36:39], v[76:79], a[228:231], v[36:39]\n" \
  "v_mfma_f32_16x16x32_bf16 v[24:27], v[228:231], v[80:83], v[24:27]\n" \
  "v_mfma_f32_16x16x32_bf16 v[32:35], a[100:103], v[80:83], v[32:35]\n" \
  "v_mfma_f32_16x16x32_bf16 v[40:43], v[80:83], a[228:231], v[40:43]\n" \
  "s_waitcnt lgkmcnt(4)\n" \
  "v_mfma_f32_16x16x32_bf16 v[20:23], v[232:235], v[84:87], v[20:23]\n" \
  "ds_read_b128 v[76:79], v122 offset:8192\n" \
  "v_mfma_f32_16x16x32_bf16 v[28:31], a[104:107], v[84:87], v[28:31]\n" \
  "ds_read_b128 v[80:83], v122 offset:10240\n" \
  "v_mfma_f32_16x16x32_bf16 v[36:39], v[84:87], a[232:235], v[36:39]\n" \
  "v_mfma_f32_16x16x32_bf16 v[24:27], v[232:235], v[88:91], v[24:27]\n" \
  "v_mfma_f32_16x16x32_bf16 v[32:35], a[104:107], v[88:91], v[32:35]\n" \
  "v_mfma_f32_16x16x32_bf16 v[40:43], v[88:91], a[232:235], v[40:43]\n" \
  "s_waitcnt lgkmcnt(4)\n" \
  "v_mfma_f32_16x16x32_bf16 v[20:23], v[236:239], v[92:95], v[20:23]\n" \
  "ds_read_b128 v[84:87], v121 offset:12288\n" \
  "v_mfma_f32_16x16x32_bf16 v[28:31], a[108:111], v[92:95], v[28:31]\n" \
  "ds_read_b128 v[88:91], v121 offset:14336\n" \
  "v_mfma_f32_16x16x32_bf16 v[36:39], v[92:95], a[236:239], v[36:39]\n" \
  "v_mfma_f32_16x16x32_bf16 v[24:27], v[236:239], v[96:99], v[24:27]\n" \
  "v_mfma_f32_16x16x32_bf16 v[32:35], a[108:111], v[96:99], v[32:35]\n" \
  "v_mfma_f32_16x16x32_bf16 v[40:43], v[96:99], a[236:239], v[40:43]\n" \
  "s_waitcnt lgkmcnt(4)\n" \
  "v_mfma_f32_16x16x32_bf16 v[20:23], v[240:243], v[68:71], v[20:23]\n" \
  "ds_read_b128 v[92:95], v122 offset:12288\n" \
  "v_mfma_f32_16x16x32_bf16 v[28:31], a[112:115], v[68:71], v[28:31]\n" \
  "ds_read_b128 v[96:99], v122 offset:14336\n" \
  "v_mfma_f32_16x16x32_bf16 v[36:39], v[68:71], a[240:243], v[36:39]\n" \
  "v_mfma_f32_16x16x32_bf16 v[24:27], v[240:243], v[72:75], v[24:27]\n" \
  "v_mfma_f32_16x16x32_bf16 v[32:35], a[112:115], v[72:75], v[32:35]\n" \
  "v_mfma_f32_16x16x32_bf16 v[40:43], v[72:75], a[240:243], v[40:43]\n" \
  "s_waitcnt lgkmcnt(4)\n" \
  "v_mfma_f32_16x16x32_bf16 v[20:23], v[244:247], v[76:79], v[20:23]\n" \
  "ds_read_b128 v[68:71], v121 offset:16384\n" \
  "v_mfma_f32_16x16x32_bf16 v[28:31], a[116:119], v[76:79], v[28:31]\n" \
  "ds_read_b128 v[72:75], v121 offset:18432\n" \
  "v_mfma_f32_16x16x32_bf16 v[36:39], v[76:79], a[244:247], v[36:39]\n" \
  "v_mfma_f32_16x16x32_bf16 v[24:27], v[244:247], v[80:83], v[24:27]\n" \
  "v_mfma_f32_16x16x32_bf16 v[32:35], a[116:119], v[80:83], v[32:35]\n" \
  "v_mfma_f32_16x16x32_bf16 v[40:43], v[80:83], a[244:247], v[40:43]\n" \
  "s_waitcnt lgkmcnt(4)\n" \
  "v_mfma_f32_16x16x32_bf16 v[20:23], v[248:251], v[84:87], v[20:23]\n" \
  "ds_read_b128 v[76:79], v122 offset:16384\n" \
  "v_mfma_f32_16x16x32_bf16 v[28:31], a[120:123], v[84:87], v[28:31]\n" \
  "ds_read_b128 v[80:83], v122 offset:18432\n" \
  "v_mfma_f32_16x16x32_bf16 v[36:39], v[84:87], a[248:251], v[36:39]\n" \
  "v_mfma_f32_16x16x32_bf16 v[24:27], v[248:251], v[88:91], v[24:27]\n" \
  "v_mfma_f32_16x16x32_bf16 v[32:35], a[120:123], v[88:91], v[32:35]\n" \
  "v_mfma_f32_16x16x32_bf16 v[40:43], v[88:91], a[248:251], v[40:43]\n" \
  "s_waitcnt lgkmcnt(4)\n" \
  "v_mfma_f32_16x16x32_bf16 v[20:23], v[252:255], v[92:95], v[20:23]\n" \
  "ds_read_b128 v[84:87], v121 offset:20480\n" \
  "v_mfma_f32_16x16x32_bf16 v[28:31], a[124:127], v[92:95], v[28:31]\n" \
  "ds_read_b128 v[88:91], v121 offset:22528\n" \
  "v_mfma_f32_16x16x32_bf16 v[36:39], v[92:95], a[252:255], v[36:39]\n" \
  "v_mfma_f32_16x16x32_bf16 v[24:27], v[252:255], v[96:99], v[24:27]\n" \
  "v_mfma_f32_16x16x32_bf16 v[32:35], a[124:127], v[96:99], v[32:35]\n" \
  "v_mfma_f32_16x16x32_bf16 v[40:43], v[96:99], a[252:255], v[40:43]\n" \
  "s_waitcnt lgkmcnt(4)\n" \
  "v_mfma_f32_16x16x32_bf16 v[44:47], v[224:227], v[68:71], v[44:47]\n" \
  "ds_read_b128 v[92:95], v122 offset:20480\n" \
  "v_mfma_f32_16x16x32_bf16 v[52:55], a[96:99], v[68:71], v[52:55]\n" \
  "ds_read_b128 v[96:99], v122 offset:22528\n" \
  "v_mfma_f32_16x16x32_bf16 v[60:63], v[68:71], a[224:227], v[60:63]\n" \
  "v_mfma_f32_16x16x32_bf16 v[48:51], v[224:227], v[72:75], v[48:51]\n" \
  "v_mfma_f32_16x16x32_bf16 v[56:59], a[96:99], v[72:75], v[56:59]\n" \
  "v_mfma_f32_16x16x32_bf16 v[64:67], v[72:75], a[224:227], v[64:67]\n" \
  "s_waitcnt lgkmcnt(4)\n" \
  "v_mfma_f32_16x16x32_bf16 v[44:47], v[228:231], v[76:79], v[44:47]\n" \
  "ds_read_b128 v[68:71], v121 offset:24576\n" \
  "v_mfma_f32_16x16x32_bf16 v[52:55], a[100:103], v[76:79], v[52:55]\n" \
  "ds_read_b128 v[72:75], v121 offset:26624\n" \
  "v_mfma_f32_16x16x32_bf16 v[60:63], v[76:79], a[228:231], v[60:63]\n" \
  "v_mfma_f32_16x16x32_bf16 v[48:51], v[228:231], v[80:83], v[48:51]\n" \
  "v_mfma_f32_16x16x32_bf16 v[56:59], a[100:103], v[80:83], v[56:59]\n" \
  "v_mfma_f32_16x16x32_bf16 v[64:67], v[80:83], a[228:231], v[64:67]\n" \
  "s_waitcnt lgkmcnt(4)\n" \
  "v_mfma_f32_16x16x32_bf16 v[44:47], v[232:235], v[84:87], v[44:47]\n" \
  "ds_read_b128 v[76:79], v122 offset:24576\n" \
  "v_mfma_f32_16x16x32_bf16 v[52:55], a[104:107], v[84:87], v[52:55]\n" \
  "ds_read_b128 v[80:83], v122 offset:26624\n" \
  "v_mfma_f32_16x16x32_bf16 v[60:63], v[84:87], a[232:235], v[60:63]\n" \
  "v_mfma_f32_16x16x32_bf16 v[48:51], v[232:235], v[88:91], v[48:51]\n" \
  "v_mfma_f32_16x16x32_bf16 v[56:59], a[104:107], v[88:91], v[56:59]\n" \
  "v_mfma_f32_16x16x32_bf16 v[64:67], v[88:91], a[232:235], v[64:67]\n" \
  "s_waitcnt lgkmcnt(4)\n" \
  "v_mfma_f32_16x16x32_bf16 v[44:47], v[236:239], v[92:95], v[44:47]\n" \
  "ds_read_b128 v[84:87], v121 offset:28672\n" \
  "v_mfma_f32_16x16x32_bf16 v[52:55], a[108:111], v[92:95], v[52:55]\n" \
  "ds_read_b128 v[88:91], v121 offset:30720\n" \
  "v_mfma_f32_16x16x32_bf16 v[60:63], v[92:95], a[236:239], v[60:63]\n" \
  "v_mfma_f32_16x16x32_bf16 v[48:51], v[236:239], v[96:99], v[48:51]\n" \
  "v_mfma_f32_16x16x32_bf16 v[56:59], a[108:111], v[96:99], v[56:59]\n" \
  "v_mfma_f32_16x16x32_bf16 v[64:67], v[96:99], a[236:239], v[64:67]\n" \
  "s_waitcnt lgkmcnt(4)\n" \
  "v_mfma_f32_16x16x32_bf16 v[44:47], v[240:243], v[68:71], v[44:47]\n" \
  "ds_read_b128 v[92:95], v122 offset:28672\n" \
  "v_mfma_f32_16x16x32_bf16 v[52:55], a[112:115], v[68:71], v[52:55]\n" \
  "ds_read_b128 v[96:99], v122 offset:30720\n" \
  "v_mfma_f32_16x16x32_bf16 v[60:63], v[68:71], a[240:243], v[60:63]\n" \
  "v_mfma_f32_16x16x32_bf16 v[48:51], v[240:243], v[72:75], v[48:51]\n" \
  "v_mfma_f32_16x16x32_bf16 v[56:59], a[112:115], v[72:75], v[56:59]\n" \
  "v_mfma_f32_16x16x32_bf16 v[64:67], v[72:75], a[240:243], v[64:67]\n" \
  "s_waitcnt lgkmcnt(4)\n" \
  "v_mfma_f32_16x16x32_bf16 v[44:47], v[244:247], v[76:79], v[44:47]\n" \
  "v_mfma_f32_16x16x32_bf16 v[52:55], a[116:119], v[76:79], v[52:55]\n" \
  "v_mfma_f32_16x16x32_bf16 v[60:63], v[76:79], a[244:247], v[60:63]\n" \
  "v_mfma_f32_16x16x32_bf16 v[48:51], v[244:247], v[80:83], v[48:51]\n" \
  "v_mfma_f32_16x16x32_bf16 v[56:59], a[116:119], v[80:83], v[56:59]\n" \
  "v_mfma_f32_16x16x32_bf16 v[64:67], v[80:83], a[244:247], v[64:67]\n" \
  "s_waitcnt lgkmcnt(2)\n" \
  "v_mfma_f32_16x16x32_bf16 v[44:47], v[248:251], v[84:87], v[44:47]\n" \
  "v_mfma_f32_16x16x32_bf16 v[52:55], a[120:123], v[84:87], v[52:55]\n" \
  "v_mfma_f32_16x16x32_bf16 v[60:63], v[84:87], a[248:251], v[60:63]\n" \
  "v_mfma_f32_16x16x32_bf16 v[48:51], v[248:251], v[88:91], v[48:51]\n" \
  "v_mfma_f32_16x16x32_bf16 v[56:59], a[120:123], v[88:91], v[56:59]\n" \
  "v_mfma_f32_16x16x32_bf16 v[64:67], v[88:91], a[248:251], v[64:67]\n" \
  "s_waitcnt lgkmcnt(0)\n" \
  "v_mfma_f32_16x16x32_bf16 v[44:47], v[252:255], v[92:95], v[44:47]\n" \
  "v_mfma_f32_16x16x32_bf16 v[52:55], a[124:127], v[92:95], v[52:55]\n" \
  "v_mfma_f32_16x16x32_bf16 v[60:63], v[92:95], a[252:255], v[60:63]\n" \
  "v_mfma_f32_16x16x32_bf16 v[48:51], v[252:255], v[96:99], v[48:51]\n" \
  "v_mfma_f32_16x16x32_bf16 v[56:59], a[124:127], v[96:99], v[56:59]\n" \
  "v_mfma_f32_16x16x32_bf16 v[64:67], v[96:99], a[252:255], v[64:67]\n" \
  "s_mov_b64 s[68:69], %10\n" \
  "s_mov_b32 s70, 0\n" \
  "LR1_%=:\n" \
  "v_readfirstlane_b32 s84, v124\n" \
  "v_readfirstlane_b32 s85, v125\n" \
  "v_readfirstlane_b32 %0, v126\n" \
  "v_readfirstlane_b32 s92, v127\n" \
  "s_xor_b32 s84, s84, s68\n" \
  "s_xor_b32 s85, s85, s69\n" \
  "s_xor_b32 %0, %0, s68\n" \
  "s_xor_b32 s92, s92, s69\n" \
  "s_or_b32 s84, s84, s85\n" \
  "s_xor_b32 s92, s92, %0\n" \
  "s_or_b32 s84, s84, s92\n" \
  "s_cbranch_scc0 LR3_%=\n" \
  "s_sleep 16\n" \
  "s_lshl_b32 s84, %11, 7\n" \
  "buffer_load_dwordx4 v[124:127], off, s[80:83], s84 sc1\n" \
  "s_waitcnt vmcnt(0)\n" \
  "s_add_u32 s70, s70, 1\n" \
  "s_cmp_lt_u32 s70, " STR(SPIN_CAP) "\n" \
  "s_cbranch_scc1 LR1_%=\n" \
  "LR3_%=:\n" \
  "s_lshr_b32 %0, %0, 13\n" \
  "s_mov_b32 s85, 1\n" \
  "s_sub_u32 s89, s85, 1\n" \
  "s_and_b32 s89, s89, 63\n" \
  "s_lshr_b32 s84, s89, 2\n" \
  "s_xnor_b32 s84, s84, %0\n" \
  "s_and_b32 s84, s84, 1\n" \
  "s_lshl_b32 s84, s84, 13\n" \
  "s_lshl_b32 s88, s89, 19\n" \
  "s_or_b32 s88, s88, s84\n" \
  "s_add_u32 s89, s88, " STR(QKV_SZ32) "\n" \
  "s_add_u32 s90, s89, " STR(QKV_SZ32) "\n" \
  "s_add_u32 s85, s85, 1\n" \
  "v_cvt_pk_bf16_f32 v116, v20, v21\n" \
  "v_cvt_pk_bf16_f32 v117, v22, v23\n" \
  "v_cvt_pk_bf16_f32 v118, v24, v25\n" \
  "v_cvt_pk_bf16_f32 v119, v26, v27\n" \
  "v_cvt_pk_bf16_f32 v120, v28, v29\n" \
  "v_cvt_pk_bf16_f32 v121, v30, v31\n" \
  "v_cvt_pk_bf16_f32 v122, v32, v33\n" \
  "v_cvt_pk_bf16_f32 v123, v34, v35\n" \
  "v_cvt_pk_bf16_f32 v124, v36, v37\n" \
  "v_cvt_pk_bf16_f32 v125, v38, v39\n" \
  "v_cvt_pk_bf16_f32 v126, v40, v41\n" \
  "v_cvt_pk_bf16_f32 v127, v42, v43\n" \
  "s_nop 1\n" \
  "v_permlane16_swap_b32 v116, v118\n" \
  "v_permlane16_swap_b32 v117, v119\n" \
  "v_permlane16_swap_b32 v120, v122\n" \
  "v_permlane16_swap_b32 v121, v123\n" \
  "buffer_store_dwordx4 v[116:119], v14, s[76:79], s88 offen sc1\n" \
  "buffer_store_dwordx4 v[120:123], v18, s[76:79], s89 offen sc1\n" \
  "buffer_store_dwordx4 v[124:127], v15, s[76:79], s90 offen sc1\n" \
  "s_sub_u32 s89, s85, 1\n" \
  "s_and_b32 s89, s89, 63\n" \
  "s_lshr_b32 s84, s89, 2\n" \
  "s_xnor_b32 s84, s84, %0\n" \
  "s_and_b32 s84, s84, 1\n" \
  "s_lshl_b32 s84, s84, 13\n" \
  "s_lshl_b32 s88, s89, 19\n" \
  "s_or_b32 s88, s88, s84\n" \
  "s_add_u32 s89, s88, " STR(QKV_SZ32) "\n" \
  "s_add_u32 s90, s89, " STR(QKV_SZ32) "\n" \
  "s_add_u32 s85, s85, 1\n" \
  "v_cvt_pk_bf16_f32 v116, v44, v45\n" \
  "v_cvt_pk_bf16_f32 v117, v46, v47\n" \
  "v_cvt_pk_bf16_f32 v118, v48, v49\n" \
  "v_cvt_pk_bf16_f32 v119, v50, v51\n" \
  "v_cvt_pk_bf16_f32 v120, v52, v53\n" \
  "v_cvt_pk_bf16_f32 v121, v54, v55\n" \
  "v_cvt_pk_bf16_f32 v122, v56, v57\n" \
  "v_cvt_pk_bf16_f32 v123, v58, v59\n" \
  "v_cvt_pk_bf16_f32 v124, v60, v61\n" \
  "v_cvt_pk_bf16_f32 v125, v62, v63\n" \
  "v_cvt_pk_bf16_f32 v126, v64, v65\n" \
  "v_cvt_pk_bf16_f32 v127, v66, v67\n" \
  "s_nop 1\n" \
  "v_permlane16_swap_b32 v116, v118\n" \
  "v_permlane16_swap_b32 v117, v119\n" \
  "v_permlane16_swap_b32 v120, v122\n" \
  "v_permlane16_swap_b32 v121, v123\n" \
  "buffer_store_dwordx4 v[116:119], v14, s[76:79], s88 offen sc1\n" \
  "buffer_store_dwordx4 v[120:123], v18, s[76:79], s89 offen sc1\n" \
  "buffer_store_dwordx4 v[124:127], v15, s[76:79], s90 offen sc1\n" \
  "s_add_u32 m0, s91, 32768\n" \
  "s_nop 0\n" \
  "buffer_load_dwordx4 v7, s[72:75], s87 offen lds\n" \
  "s_add_u32 s87, s87, 128\n" \
  "s_add_u32 m0, s91, 36864\n" \
  "s_nop 0\n" \
  "buffer_load_dwordx4 v7, s[72:75], s87 offen lds\n" \
  "s_add_u32 s87, s87, 128\n" \
  "s_add_u32 m0, s91, 40960\n" \
  "s_nop 0\n" \
  "buffer_load_dwordx4 v7, s[72:75], s87 offen lds\n" \
  "s_add_u32 s87, s87, 128\n" \
  "s_add_u32 m0, s91, 45056\n" \
  "s_nop 0\n" \
  "buffer_load_dwordx4 v7, s[72:75], s87 offen lds\n" \
  "s_add_u32 s87, s87, 128\n" \
  "s_add_u32 m0, s91, 49152\n" \
  "s_nop 0\n" \
  "buffer_load_dwordx4 v7, s[72:75], s87 offen lds\n" \
  "s_add_u32 s87, s87, 128\n" \
  "s_add_u32 m0, s91, 53248\n" \
  "s_nop 0\n" \
  "buffer_load_dwordx4 v7, s[72:75], s87 offen lds\n" \
  "s_add_u32 s87, s87, 128\n" \
  "s_add_u32 m0, s91, 57344\n" \
  "s_nop 0\n" \
  "buffer_load_dwordx4 v7, s[72:75], s87 offen lds\n" \
  "s_add_u32 s87, s87, 128\n" \
  "s_add_u32 m0, s91, 61440\n" \
  "s_nop 0\n" \
  "buffer_load_dwordx4 v7, s[72:75], s87 offen lds\n" \
  "s_add_u32 s87, s87, 63616\n" \
  "s_min_u32 s87, s87, s96\n" \
  "v_mov_b32 v17, 2\n" \
  "s_waitcnt vmcnt(8)\n" \
  "s_barrier\n" \
  "buffer_store_dword v17, v16, s[80:83], 0 offen sc1\n" \
  "s_add_u32 m0, s91, 65536\n" \
  "s_nop 0\n" \
  "buffer_load_dwordx4 v7, s[72:75], s87 offen lds\n" \
  "s_add_u32 s87, s87, 128\n" \
  "s_add_u32 m0, s91, 69632\n" \
  "s_nop 0\n" \
  "buffer_load_dwordx4 v7, s[72:75], s87 offen lds\n" \
  "s_add_u32 s87, s87, 128\n" \
  "s_add_u32 m0, s91, 73728\n" \
  "s_nop 0\n" \
  "buffer_load_dwordx4 v7, s[72:75], s87 offen lds\n" \
  "s_add_u32 s87, s87, 128\n" \
  "s_add_u32 m0, s91, 77824\n" \
  "s_nop 0\n" \
  "buffer_load_dwordx4 v7, s[72:75], s87 offen lds\n" \
  "s_add_u32 s87, s87, 128\n" \
  "s_add_u32 m0, s91, 81920\n" \
  "s_nop 0\n" \
  "buffer_load_dwordx4 v7, s[72:75], s87 offen lds\n" \
  "s_add_u32 s87, s87, 128\n" \
  "s_add_u32 m0, s91, 86016\n" \
  "s_nop 0\n" \
  "buffer_load_dwordx4 v7, s[72:75], s87 offen lds\n" \
  "s_add_u32 s87, s87, 128\n" \
  "s_add_u32 m0, s91, 90112\n" \
  "s_nop 0\n" \
  "buffer_load_dwordx4 v7, s[72:75], s87 offen lds\n" \
  "s_add_u32 s87, s87, 128\n" \
  "s_add_u32 m0, s91, 94208\n" \
  "s_nop 0\n" \
  "buffer_load_dwordx4 v7, s[72:75], s87 offen lds\n" \
  "s_add_u32 s87, s87, 128\n" \
  "ds_read_b128 v[68:71], v10 offset:0\n" \
  "ds_read_b128 v[72:75], v10 offset:2048\n" \
  "ds_read_b128 v[76:79], v11 offset:0\n" \
  "ds_read_b128 v[80:83], v11 offset:2048\n" \
  "ds_read_b128 v[84:87], v10 offset:4096\n" \
  "ds_read_b128 v[88:91], v10 offset:6144\n" \
  "s_mov_b32 s85, 2\n" \
  ".p2align 6\n" \
  "LG_%=:\n" \
  "s_cmp_eq_u32 s85, 64\n" \
  "s_cbranch_scc1 LX0_%=\n" \
  "s_waitcnt vmcnt(8)\n" \
  "s_barrier\n" \
  "s_waitcnt lgkmcnt(4)\n" \
  "v_mfma_f32_16x16x32_bf16 v[20:23], v[128:131], v[68:71], 0\n" \
  "ds_read_b128 v[92:95], v11 offset:4096\n" \
  "s_sub_u32 s89, s85, 1\n" \
  "s_and_b32 s89, s89, 63\n" \
  "v_mfma_f32_16x16x32_bf16 v[28:31], a[0:3], v[68:71], 0\n" \
  "ds_read_b128 v[96:99], v11 offset:6144\n" \
  "s_lshr_b32 s84, s89, 2\n" \
  "s_xnor_b32 s84, s84, %0\n" \
  "v_mfma_f32_16x16x32_bf16 v[36:39], v[68:71], a[128:131], 0\n" \
  "s_and_b32 s84, s84, 1\n" \
  "s_lshl_b32 s84, s84, 13\n" \
  "v_mfma_f32_16x16x32_bf16 v[24:27], v[128:131], v[72:75], 0\n" \
  "s_lshl_b32 s88, s89, 19\n" \
  "s_or_b32 s88, s88, s84\n" \
  "v_mfma_f32_16x16x32_bf16 v[32:35], a[0:3], v[72:75], 0\n" \
  "s_add_u32 s89, s88, " STR(QKV_SZ32) "\n" \
  "s_add_u32 s90, s89, " STR(QKV_SZ32) "\n" \
  "v_mfma_f32_16x16x32_bf16 v[40:43], v[72:75], a[128:131], 0\n" \
  "s_add_u32 s85, s85, 1\n" \
  "s_waitcnt lgkmcnt(4)\n" \
  "v_mfma_f32_16x16x32_bf16 v[20:23], v[132:135], v[76:79], v[20:23]\n" \
  "ds_read_b128 v[68:71], v10 offset:8192\n" \
  "v_cvt_pk_bf16_f32 v116, v44, v45\n" \
  "v_mfma_f32_16x16x32_bf16 v[28:31], a[4:7], v[76:79], v[28:31]\n" \
  "ds_read_b128 v[72:75], v10 offset:10240\n" \
  "v_cvt_pk_bf16_f32 v117, v46, v47\n" \
  "v_mfma_f32_16x16x32_bf16 v[36:39], v[76:79], a[132:135], v[36:39]\n" \
  "v_cvt_pk_bf16_f32 v118, v48, v49\n" \
  "v_mfma_f32_16x16x32_bf16 v[24:27], v[132:135], v[80:83], v[24:27]\n" \
  "v_cvt_pk_bf16_f32 v119, v50, v51\n" \
  "v_mfma_f32_16x16x32_bf16 v[32:35], a[4:7], v[80:83], v[32:35]\n" \
  "v_cvt_pk_bf16_f32 v120, v52, v53\n" \
  "v_mfma_f32_16x16x32_bf16 v[40:43], v[80:83], a[132:135], v[40:43]\n" \
  "v_cvt_pk_bf16_f32 v121, v54, v55\n" \
  "s_waitcnt lgkmcnt(4)\n" \
  "v_mfma_f32_16x16x32_bf16 v[20:23], v[136:139], v[84:87], v[20:23]\n" \
  "ds_read_b128 v[76:79], v11 offset:8192\n" \
  "v_cvt_pk_bf16_f32 v122, v56, v57\n" \
  "v_mfma_f32_16x16x32_bf16 v[28:31], a[8:11], v[84:87], v[28:31]\n" \
  "ds_read_b128 v[80:83], v11 offset:10240\n" \
  "v_cvt_pk_bf16_f32 v123, v58, v59\n" \
  "v_mfma_f32_16x16x32_bf16 v[36:39], v[84:87], a[136:139], v[36:39]\n" \
  "v_cvt_pk_bf16_f32 v124, v60, v61\n" \
  "v_mfma_f32_16x16x32_bf16 v[24:27], v[136:139], v[88:91], v[24:27]\n" \
  "v_cvt_pk_bf16_f32 v125, v62, v63\n" \
  "v_mfma_f32_16x16x32_bf16 v[32:35], a[8:11], v[88:91], v[32:35]\n" \
  "v_cvt_pk_bf16_f32 v126, v64, v65\n" \
  "v_mfma_f32_16x16x32_bf16 v[40:43], v[88:91], a[136:139], v[40:43]\n" \
  "v_cvt_pk_bf16_f32 v127, v66, v67\n" \
  "s_waitcnt lgkmcnt(4)\n" \
  "v_mfma_f32_16x16x32_bf16 v[20:23], v[140:143], v[92:95], v[20:23]\n" \
  "ds_read_b128 v[84:87], v10 offset:12288\n" \
  "v_permlane16_swap_b32 v116, v118\n" \
  "v_mfma_f32_16x16x32_bf16 v[28:31], a[12:15], v[92:95], v[28:31]\n" \
  "ds_read_b128 v[88:91], v10 offset:14336\n" \
  "v_permlane16_swap_b32 v117, v119\n" \
  "v_mfma_f32_16x16x32_bf16 v[36:39], v[92:95], a[140:143], v[36:39]\n" \
  "v_permlane16_swap_b32 v120, v122\n" \
  "v_mfma_f32_16x16x32_bf16 v[24:27], v[140:143], v[96:99], v[24:27]\n" \
  "v_permlane16_swap_b32 v121, v123\n" \
  "v_mfma_f32_16x16x32_bf16 v[32:35], a[12:15], v[96:99], v[32:35]\n" \
  "v_mfma_f32_16x16x32_bf16 v[40:43], v[96:99], a[140:143], v[40:43]\n" \
  "s_waitcnt lgkmcnt(4)\n" \
  "v_mfma_f32_16x16x32_bf16 v[20:23], v[144:147], v[68:71], v[20:23]\n" \
  "ds_read_b128 v[92:95], v11 offset:12288\n" \
  "buffer_store_dwordx4 v[116:119], v14, s[76:79], s88 offen sc1\n" \
  "v_mfma_f32_16x16x32_bf16 v[28:31], a[16:19], v[68:71], v[28:31]\n" \
  "ds_read_b128 v[96:99], v11 offset:14336\n" \
  "buffer_store_dwordx4 v[120:123], v18, s[76:79], s89 offen sc1\n" \
  "v_mfma_f32_16x16x32_bf16 v[36:39], v[68:71], a[144:147], v[36:39]\n" \
  "buffer_store_dwordx4 v[124:127], v15, s[76:79], s90 offen sc1\n" \
  "v_mfma_f32_16x16x32_bf16 v[24:27], v[144:147], v[72:75], v[24:27]\n" \
  "v_mfma_f32_16x16x32_bf16 v[32:35], a[16:19], v[72:75], v[32:35]\n" \
  "v_mfma_f32_16x16x32_bf16 v[40:43], v[72:75], a[144:147], v[40:43]\n" \
  "s_waitcnt lgkmcnt(4)\n" \
  "v_mfma_f32_16x16x32_bf16 v[20:23], v[148:151], v[76:79], v[20:23]\n" \
  "ds_read_b128 v[68:71], v10 offset:16384\n" \
  "v_mfma_f32_16x16x32_bf16 v[28:31], a[20:23], v[76:79], v[28:31]\n" \
  "ds_read_b128 v[72:75], v10 offset:18432\n" \
  "v_mfma_f32_16x16x32_bf16 v[36:39], v[76:79], a[148:151], v[36:39]\n" \
  "v_mfma_f32_16x16x32_bf16 v[24:27], v[148:151], v[80:83], v[24:27]\n" \
  "s_add_u32 m0, s91, 98304\n" \
  "s_nop 0\n" \
  "buffer_load_dwordx4 v7, s[72:75], s87 offen lds\n" \
  "s_add_u32 s87, s87, 128\n" \
  "v_mfma_f32_16x16x32_bf16 v[32:35], a[20:23], v[80:83], v[32:35]\n" \
  "v_mfma_f32_16x16x32_bf16 v[40:43], v[80:83], a[148:151], v[40:43]\n" \
  "s_waitcnt lgkmcnt(4)\n" \
  "v_mfma_f32_16x16x32_bf16 v[20:23], v[152:155], v[84:87], v[20:23]\n" \
  "ds_read_b128 v[76:79], v11 offset:16384\n" \
  "v_mfma_f32_16x16x32_bf16 v[28:31], a[24:27], v[84:87], v[28:31]\n" \
  "ds_read_b128 v[80:83], v11 offset:18432\n" \
  "v_mfma_f32_16x16x32_bf16 v[36:39], v[84:87], a[152:155], v[36:39]\n" \
  "v_mfma_f32_16x16x32_bf16 v[24:27], v[152:155], v[88:91], v[24:27]\n" \
  "s_add_u32 m0, s91, 102400\n" \
  "s_nop 0\n" \
  "buffer_load_dwordx4 v7, s[72:75], s87 offen lds\n" \
  "s_add_u32 s87, s87, 128\n" \
  "v_mfma_f32_16x16x32_bf16 v[32:35], a[24:27], v[88:91], v[32:35]\n" \
  "v_mfma_f32_16x16x32_bf16 v[40:43], v[88:91], a[152:155], v[40:43]\n" \
  "s_waitcnt lgkmcnt(4)\n" \
  "v_mfma_f32_16x16x32_bf16 v[20:23], v[156:159], v[92:95], v[20:23]\n" \
  "ds_read_b128 v[84:87], v10 offset:20480\n" \
  "v_mfma_f32_16x16x32_bf16 v[28:31], a[28:31], v[92:95], v[28:31]\n" \
  "ds_read_b128 v[88:91], v10 offset:22528\n" \
  "v_mfma_f32_16x16x32_bf16 v[36:39], v[92:95], a[156:159], v[36:39]\n" \
  "v_mfma_f32_16x16x32_bf16 v[24:27], v[156:159], v[96:99], v[24:27]\n" \
  "s_add_u32 m0, s91, 106496\n" \
  "s_nop 0\n" \
  "buffer_load_dwordx4 v7, s[72:75], s87 offen lds\n" \
  "s_add_u32 s87, s87, 128\n" \
  "v_mfma_f32_16x16x32_bf16 v[32:35], a[28:31], v[96:99], v[32:35]\n" \
  "v_mfma_f32_16x16x32_bf16 v[40:43], v[96:99], a[156:159], v[40:43]\n" \
  "s_waitcnt lgkmcnt(4)\n" \
  "v_mfma_f32_16x16x32_bf16 v[20:23], v[160:163], v[68:71], v[20:23]\n" \
  "ds_read_b128 v[92:95], v11 offset:20480\n" \
  "v_mfma_f32_16x16x32_bf16 v[28:31], a[32:35], v[68:71], v[28:31]\n" \
  "ds_read_b128 v[96:99], v11 offset:22528\n" \
  "v_mfma_f32_16x16x32_bf16 v[36:39], v[68:71], a[160:163], v[36:39]\n" \
  "v_mfma_f32_16x16x32_bf16 v[24:27], v[160:163], v[72:75], v[24:27]\n" \
  "s_add_u32 m0, s91, 110592\n" \
  "s_nop 0\n" \
  "buffer_load_dwordx4 v7, s[72:75], s87 offen lds\n" \
  "s_add_u32 s87, s87, 128\n" \
  "v_mfma_f32_16x16x32_bf16 v[32:35], a[32:35], v[72:75], v[32:35]\n" \
  "v_mfma_f32_16x16x32_bf16 v[40:43], v[72:75], a[160:163], v[40:43]\n" \
  "s_waitcnt lgkmcnt(4)\n" \
  "v_mfma_f32_16x16x32_bf16 v[20:23], v[164:167], v[76:79], v[20:23]\n" \
  "ds_read_b128 v[68:71], v10 offset:24576\n" \
  "v_mfma_f32_16x16x32_bf16 v[28:31], a[36:39], v[76:79], v[28:31]\n" \
  "ds_read_b128 v[72:75], v10 offset:26624\n" \
  "v_mfma_f32_16x16x32_bf16 v[36:39], v[76:79], a[164:167], v[36:39]\n" \
  "v_mfma_f32_16x16x32_bf16 v[24:27], v[164:167], v[80:83], v[24:27]\n" \
  "s_add_u32 m0, s91, 114688\n" \
  "s_nop 0\n" \
  "buffer_load_dwordx4 v7, s[72:75], s87 offen lds\n" \
  "s_add_u32 s87, s87, 128\n" \
  "v_mfma_f32_16x16x32_bf16 v[32:35], a[36:39], v[80:83], v[32:35]\n" \
  "v_mfma_f32_16x16x32_bf16 v[40:43], v[80:83], a[164:167], v[40:43]\n" \
  "s_waitcnt lgkmcnt(4)\n" \
  "v_mfma_f32_16x16x32_bf16 v[20:23], v[168:171], v[84:87], v[20:23]\n" \
  "ds_read_b128 v[76:79], v11 offset:24576\n" \
  "v_mfma_f32_16x16x32_bf16 v[28:31], a[40:43], v[84:87], v[28:31]\n" \
  "ds_read_b128 v[80:83], v11 offset:26624\n" \
  "v_mfma_f32_16x16x32_bf16 v[36:39], v[84:87], a[168:171], v[36:39]\n" \
  "v_mfma_f32_16x16x32_bf16 v[24:27], v[168:171], v[88:91], v[24:27]\n" \
  "s_add_u32 m0, s91, 118784\n" \
  "s_nop 0\n" \
  "buffer_load_dwordx4 v7, s[72:75], s87 offen lds\n" \
  "s_add_u32 s87, s87, 128\n" \
  "v_mfma_f32_16x16x32_bf16 v[32:35], a[40:43], v[88:91], v[32:35]\n" \
  "v_mfma_f32_16x16x32_bf16 v[40:43], v[88:91], a[168:171], v[40:43]\n" \
  "s_waitcnt lgkmcnt(4)\n" \
  "v_mfma_f32_16x16x32_bf16 v[20:23], v[172:175], v[92:95], v[20:23]\n" \
  "ds_read_b128 v[84:87], v10 offset:28672\n" \
  "v_mfma_f32_16x16x32_bf16 v[28:31], a[44:47], v[92:95], v[28:31]\n" \
  "ds_read_b128 v[88:91], v10 offset:30720\n" \
  "v_mfma_f32_16x16x32_bf16 v[36:39], v[92:95], a[172:175], v[36:39]\n" \
  "v_mfma_f32_16x16x32_bf16 v[24:27], v[172:175], v[96:99], v[24:27]\n" \
  "s_add_u32 m0, s91, 122880\n" \
  "s_nop 0\n" \
  "buffer_load_dwordx4 v7, s[72:75], s87 offen lds\n" \
  "s_add_u32 s87, s87, 128\n" \
  "v_mfma_f32_16x16x32_bf16 v[32:35], a[44:47], v[96:99], v[32:35]\n" \
  "v_mfma_f32_16x16x32_bf16 v[40:43], v[96:99], a[172:175], v[40:43]\n" \
  "s_waitcnt lgkmcnt(4)\n" \
  "v_mfma_f32_16x16x32_bf16 v[20:23], v[176:179], v[68:71], v[20:23]\n" \
  "ds_read_b128 v[92:95], v11 offset:28672\n" \
  "v_mfma_f32_16x16x32_bf16 v[28:31], a[48:51], v[68:71], v[28:31]\n" \
  "ds_read_b128 v[96:99], v11 offset:30720\n" \
  "v_mfma_f32_16x16x32_bf16 v[36:39], v[68:71], a[176:179], v[36:39]\n" \
  "v_mfma_f32_16x16x32_bf16 v[24:27], v[176:179], v[72:75], v[24:27]\n" \
  "s_add_u32 m0, s91, 126976\n" \
  "s_nop 0\n" \
  "buffer_load_dwordx4 v7, s[72:75], s87 offen lds\n" \
  "s_add_u32 s87, s87, 63616\n" \
  "s_min_u32 s87, s87, s96\n" \
  "v_mfma_f32_16x16x32_bf16 v[32:35], a[48:51], v[72:75], v[32:35]\n" \
  "v_mfma_f32_16x16x32_bf16 v[40:43], v[72:75], a[176:179], v[40:43]\n" \
  "s_waitcnt lgkmcnt(4)\n" \
  "v_mfma_f32_16x16x32_bf16 v[20:23], v[180:183], v[76:79], v[20:23]\n" \
  "ds_read_b128 v[68:71], v10 offset:32768\n" \
  "v_mfma_f32_16x16x32_bf16 v[28:31], a[52:55], v[76:79], v[28:31]\n" \
  "ds_read_b128 v[72:75], v10 offset:34816\n" \
  "v_mfma_f32_16x16x32_bf16 v[36:39], v[76:79], a[180:183], v[36:39]\n" \
  "v_mfma_f32_16x16x32_bf16 v[24:27], v[180:183], v[80:83], v[24:27]\n" \
  "v_mfma_f32_16x16x32_bf16 v[32:35], a[52:55], v[80:83], v[32:35]\n" \
  "v_mfma_f32_16x16x32_bf16 v[40:43], v[80:83], a[180:183], v[40:43]\n" \
  "s_waitcnt lgkmcnt(4)\n" \
  "v_mfma_f32_16x16x32_bf16 v[20:23], v[184:187], v[84:87], v[20:23]\n" \
  "ds_read_b128 v[76:79], v11 offset:32768\n" \
  "v_mfma_f32_16x16x32_bf16 v[28:31], a[56:59], v[84:87], v[28:31]\n" \
  "ds_read_b128 v[80:83], v11 offset:34816\n" \
  "v_mfma_f32_16x16x32_bf16 v[36:39], v[84:87], a[184:187], v[36:39]\n" \
  "v_mfma_f32_16x16x32_bf16 v[24:27], v[184:187], v[88:91], v[24:27]\n" \
  "v_mfma_f32_16x16x32_bf16 v[32:35], a[56:59], v[88:91], v[32:35]\n" \
  "v_mfma_f32_16x16x32_bf16 v[40:43], v[88:91], a[184:187], v[40:43]\n" \
  "s_waitcnt lgkmcnt(4)\n" \
  "v_mfma_f32_16x16x32_bf16 v[20:23], v[188:191], v[92:95], v[20:23]\n" \
  "ds_read_b128 v[84:87], v10 offset:36864\n" \
  "v_mfma_f32_16x16x32_bf16 v[28:31], a[60:63], v[92:95], v[28:31]\n" \
  "ds_read_b128 v[88:91], v10 offset:38912\n" \
  "v_mfma_f32_16x16x32_bf16 v[36:39], v[92:95], a[188:191], v[36:39]\n" \
  "v_mfma_f32_16x16x32_bf16 v[24:27], v[188:191], v[96:99], v[24:27]\n" \
  "v_mfma_f32_16x16x32_bf16 v[32:35], a[60:63], v[96:99], v[32:35]\n" \
  "v_mfma_f32_16x16x32_bf16 v[40:43], v[96:99], a[188:191], v[40:43]\n" \
  "s_waitcnt vmcnt(8)\n" \
  "s_barrier\n" \
  "s_waitcnt lgkmcnt(4)\n" \
  "v_mfma_f32_16x16x32_bf16 v[20:23], v[192:195], v[68:71], v[20:23]\n" \
  "ds_read_b128 v[92:95], v11 offset:36864\n" \
  "buffer_store_dword v17, v16, s[80:83], 0 offen sc1\n" \
  "v_mfma_f32_16x16x32_bf16 v[28:31], a[64:67], v[68:71], v[28:31]\n" \
  "ds_read_b128 v[96:99], v11 offset:38912\n" \
  "v_add_u32 v17, 1, v17\n" \
  "v_mfma_f32_16x16x32_bf16 v[36:39], v[68:71], a[192:195], v[36:39]\n" \
  "v_mfma_f32_16x16x32_bf16 v[24:27], v[192:195], v[72:75], v[24:27]\n" \
  "v_mfma_f32_16x16x32_bf16 v[32:35], a[64:67], v[72:75], v[32:35]\n" \
  "v_mfma_f32_16x16x32_bf16 v[40:43], v[72:75], a[192:195], v[40:43]\n" \
  "s_waitcnt lgkmcnt(4)\n" \
  "v_mfma_f32_16x16x32_bf16 v[20:23], v[196:199], v[76:79], v[20:23]\n" \
  "ds_read_b128 v[68:71], v10 offset:40960\n" \
  "v_mfma_f32_16x16x32_bf16 v[28:31], a[68:71], v[76:79], v[28:31]\n" \
  "ds_read_b128 v[72:75], v10 offset:43008\n" \
  "v_mfma_f32_16x16x32_bf16 v[36:39], v[76:79], a[196:199], v[36:39]\n" \
  "v_mfma_f32_16x16x32_bf16 v[24:27], v[196:199], v[80:83], v[24:27]\n" \
  "v_mfma_f32_16x16x32_bf16 v[32:35], a[68:71], v[80:83], v[32:35]\n" \
  "v_mfma_f32_16x16x32_bf16 v[40:43], v[80:83], a[196:199], v[40:43]\n" \
  "s_waitcnt lgkmcnt(4)\n" \
  "v_mfma_f32_16x16x32_bf16 v[20:23], v[200:203], v[84:87], v[20:23]\n" \
  "ds_read_b128 v[76:79], v11 offset:40960\n" \
  "v_mfma_f32_16x16x32_bf16 v[28:31], a[72:75], v[84:87], v[28:31]\n" \
  "ds_read_b128 v[80:83], v11 offset:43008\n" \
  "v_mfma_f32_16x16x32_bf16 v[36:39], v[84:87], a[200:203], v[36:39]\n" \
  "v_mfma_f32_16x16x32_bf16 v[24:27], v[200:203], v[88:91], v[24:27]\n" \
  "v_mfma_f32_16x16x32_bf16 v[32:35], a[72:75], v[88:91], v[32:35]\n" \
  "v_mfma_f32_16x16x32_bf16 v[40:43], v[88:91], a[200:203], v[40:43]\n" \
  "s_waitcnt lgkmcnt(4)\n" \
  "v_mfma_f32_16x16x32_bf16 v[20:23], v[204:207], v[92:95], v[20:23]\n" \
  "ds_read_b128 v[84:87], v10 offset:45056\n" \
  "v_mfma_f32_16x16x32_bf16 v[28:31], a[76:79], v[92:95], v[28:31]\n" \
  "ds_read_b128 v[88:91], v10 offset:47104\n" \
  "v_mfma_f32_16x16x32_bf16 v[36:39], v[92:95], a[204:207], v[36:39]\n" \
  "v_mfma_f32_16x16x32_bf16 v[24:27], v[204:207], v[96:99], v[24:27]\n" \
  "v_mfma_f32_16x16x32_bf16 v[32:35], a[76:79], v[96:99], v[32:35]\n" \
  "v_mfma_f32_16x16x32_bf16 v[40:43], v[96:99], a[204:207], v[40:43]\n" \
  "s_waitcnt lgkmcnt(4)\n" \
  "v_mfma_f32_16x16x32_bf16 v[20:23], v[208:211], v[68:71], v[20:23]\n" \
  "ds_read_b128 v[92:95], v11 offset:45056\n" \
  "v_mfma_f32_16x16x32_bf16 v[28:31], a[80:83], v[68:71], v[28:31]\n" \
  "ds_read_b128 v[96:99], v11 offset:47104\n" \
  "v_mfma_f32_16x16x32_bf16 v[36:39], v[68:71], a[208:211], v[36:39]\n" \
  "v_mfma_f32_16x16x32_bf16 v[24:27], v[208:211], v[72:75], v[24:27]\n" \
  "v_mfma_f32_16x16x32_bf16 v[32:35], a[80:83], v[72:75], v[32:35]\n" \
  "v_mfma_f32_16x16x32_bf16 v[40:43], v[72:75], a[208:211], v[40:43]\n" \
  "s_waitcnt lgkmcnt(4)\n" \
  "v_mfma_f32_16x16x32_bf16 v[20:23], v[212:215], v[76:79], v[20:23]\n" \
  "ds_read_b128 v[68:71], v10 offset:49152\n" \
  "v_mfma_f32_16x16x32_bf16 v[28:31], a[84:87], v[76:79], v[28:31]\n" \
  "ds_read_b128 v[72:75], v10 offset:51200\n" \
  "v_mfma_f32_16x16x32_bf16 v[36:39], v[76:79], a[212:215], v[36:39]\n" \
  "v_mfma_f32_16x16x32_bf16 v[24:27], v[212:215], v[80:83], v[24:27]\n" \
  "s_add_u32 m0, s91, 0\n" \
  "s_nop 0\n" \
  "buffer_load_dwordx4 v7, s[72:75], s87 offen lds\n" \
  "s_add_u32 s87, s87, 128\n" \
  "v_mfma_f32_16x16x32_bf16 v[32:35], a[84:87], v[80:83], v[32:35]\n" \
  "v_mfma_f32_16x16x32_bf16 v[40:43], v[80:83], a[212:215], v[40:43]\n" \
  "s_waitcnt lgkmcnt(4)\n" \
  "v_mfma_f32_16x16x32_bf16 v[20:23], v[216:219], v[84:87], v[20:23]\n" \
  "ds_read_b128 v[76:79], v11 offset:49152\n" \
  "v_mfma_f32_16x16x32_bf16 v[28:31], a[88:91], v[84:87], v[28:31]\n" \
  "ds_read_b128 v[80:83], v11 offset:51200\n" \
  "v_mfma_f32_16x16x32_bf16 v[36:39], v[84:87], a[216:219], v[36:39]\n" \
  "v_mfma_f32_16x16x32_bf16 v[24:27], v[216:219], v[88:91], v[24:27]\n" \
  "s_add_u32 m0, s91, 4096\n" \
  "s_nop 0\n" \
  "buffer_load_dwordx4 v7, s[72:75], s87 offen lds\n" \
  "s_add_u32 s87, s87, 128\n" \
  "v_mfma_f32_16x16x32_bf16 v[32:35], a[88:91], v[88:91], v[32:35]\n" \
  "v_mfma_f32_16x16x32_bf16 v[40:43], v[88:91], a[216:219], v[40:43]\n" \
  "s_waitcnt lgkmcnt(4)\n" \
  "v_mfma_f32_16x16x32_bf16 v[20:23], v[220:223], v[92:95], v[20:23]\n" \
  "ds_read_b128 v[84:87], v10 offset:53248\n" \
  "v_mfma_f32_16x16x32_bf16 v[28:31], a[92:95], v[92:95], v[28:31]\n" \
  "ds_read_b128 v[88:91], v10 offset:55296\n" \
  "v_mfma_f32_16x16x32_bf16 v[36:39], v[92:95], a[220:223], v[36:39]\n" \
  "v_mfma_f32_16x16x32_bf16 v[24:27], v[220:223], v[96:99], v[24:27]\n" \
  "s_add_u32 m0, s91, 8192\n" \
  "s_nop 0\n" \
  "buffer_load_dwordx4 v7, s[72:75], s87 offen lds\n" \
  "s_add_u32 s87, s87, 128\n" \
  "v_mfma_f32_16x16x32_bf16 v[32:35], a[92:95], v[96:99], v[32:35]\n" \
  "v_mfma_f32_16x16x32_bf16 v[40:43], v[96:99], a[220:223], v[40:43]\n" \
  "s_waitcnt lgkmcnt(4)\n" \
  "v_mfma_f32_16x16x32_bf16 v[20:23], v[224:227], v[68:71], v[20:23]\n" \
  "ds_read_b128 v[92:95], v11 offset:53248\n" \
  "v_mfma_f32_16x16x32_bf16 v[28:31], a[96:99], v[68:71], v[28:31]\n" \
  "ds_read_b128 v[96:99], v11 offset:55296\n" \
  "v_mfma_f32_16x16x32_bf16 v[36:39], v[68:71], a[224:227], v[36:39]\n" \
  "v_mfma_f32_16x16x32_bf16 v[24:27], v[224:227], v[72:75], v[24:27]\n" \
  "s_add_u32 m0, s91, 12288\n" \
  "s_nop 0\n" \
  "buffer_load_dwordx4 v7, s[72:75], s87 offen lds\n" \
  "s_add_u32 s87, s87, 128\n" \
  "v_mfma_f32_16x16x32_bf16 v[32:35], a[96:99], v[72:75], v[32:35]\n" \
  "v_mfma_f32_16x16x32_bf16 v[40:43], v[72:75], a[224:227], v[40:43]\n" \
  "s_waitcnt lgkmcnt(4)\n" \
  "v_mfma_f32_16x16x32_bf16 v[20:23], v[228:231], v[76:79], v[20:23]\n" \
  "ds_read_b128 v[68:71], v10 offset:57344\n" \
  "v_mfma_f32_16x16x32_bf16 v[28:31], a[100:103], v[76:79], v[28:31]\n" \
  "ds_read_b128 v[72:75], v10 offset:59392\n" \
  "v_mfma_f32_16x16x32_bf16 v[36:39], v[76:79], a[228:231], v[36:39]\n" \
  "v_mfma_f32_16x16x32_bf16 v[24:27], v[228:231], v[80:83], v[24:27]\n" \
  "s_add_u32 m0, s91, 16384\n" \
  "s_nop 0\n" \
  "buffer_load_dwordx4 v7, s[72:75], s87 offen lds\n" \
  "s_add_u32 s87, s87, 128\n" \
  "v_mfma_f32_16x16x32_bf16 v[32:35], a[100:103], v[80:83], v[32:35]\n" \
  "v_mfma_f32_16x16x32_bf16 v[40:43], v[80:83], a[228:231], v[40:43]\n" \
  "s_waitcnt lgkmcnt(4)\n" \
  "v_mfma_f32_16x16x32_bf16 v[20:23], v[232:235], v[84:87], v[20:23]\n" \
  "ds_read_b128 v[76:79], v11 offset:57344\n" \
  "v_mfma_f32_16x16x32_bf16 v[28:31], a[104:107], v[84:87], v[28:31]\n" \
  "ds_read_b128 v[80:83], v11 offset:59392\n" \
  "v_mfma_f32_16x16x32_bf16 v[36:39], v[84:87], a[232:235], v[36:39]\n" \
  "v_mfma_f32_16x16x32_bf16 v[24:27], v[232:235], v[88:91], v[24:27]\n" \
  "s_add_u32 m0, s91, 20480\n" \
  "s_nop 0\n" \
  "buffer_load_dwordx4 v7, s[72:75], s87 offen lds\n" \
  "s_add_u32 s87, s87, 128\n" \
  "v_mfma_f32_16x16x32_bf16 v[32:35], a[104:107], v[88:91], v[32:35]\n" \
  "v_mfma_f32_16x16x32_bf16 v[40:43], v[88:91], a[232:235], v[40:43]\n" \
  "s_waitcnt lgkmcnt(4)\n" \
  "v_mfma_f32_16x16x32_bf16 v[20:23], v[236:239], v[92:95], v[20:23]\n" \
  "ds_read_b128 v[84:87], v10 offset:61440\n" \
  "v_mfma_f32_16x16x32_bf16 v[28:31], a[108:111], v[92:95], v[28:31]\n" \
  "ds_read_b128 v[88:91], v10 offset:63488\n" \
  "v_mfma_f32_16x16x32_bf16 v[36:39], v[92:95], a[236:239], v[36:39]\n" \
  "v_mfma_f32_16x16x32_bf16 v[24:27], v[236:239], v[96:99], v[24:27]\n" \
  "s_add_u32 m0, s91, 24576\n" \
  "s_nop 0\n" \
  "buffer_load_dwordx4 v7, s[72:75], s87 offen lds\n" \
  "s_add_u32 s87, s87, 128\n" \
  "v_mfma_f32_16x16x32_bf16 v[32:35], a[108:111], v[96:99], v[32:35]\n" \
  "v_mfma_f32_16x16x32_bf16 v[40:43], v[96:99], a[236:239], v[40:43]\n" \
  "s_waitcnt lgkmcnt(4)\n" \
  "v_mfma_f32_16x16x32_bf16 v[20:23], v[240:243], v[68:71], v[20:23]\n" \
  "ds_read_b128 v[92:95], v11 offset:61440\n" \
  "v_mfma_f32_16x16x32_bf16 v[28:31], a[112:115], v[68:71], v[28:31]\n" \
  "ds_read_b128 v[96:99], v11 offset:63488\n" \
  "v_mfma_f32_16x16x32_bf16 v[36:39], v[68:71], a[240:243], v[36:39]\n" \
  "v_mfma_f32_16x16x32_bf16 v[24:27], v[240:243], v[72:75], v[24:27]\n" \
  "s_add_u32 m0, s91, 28672\n" \
  "s_nop 0\n" \
  "buffer_load_dwordx4 v7, s[72:75], s87 offen lds\n" \
  "s_add_u32 s87, s87, 128\n" \
  "v_mfma_f32_16x16x32_bf16 v[32:35], a[112:115], v[72:75], v[32:35]\n" \
  "v_mfma_f32_16x16x32_bf16 v[40:43], v[72:75], a[240:243], v[40:43]\n" \
  "s_waitcnt lgkmcnt(4)\n" \
  "v_mfma_f32_16x16x32_bf16 v[20:23], v[244:247], v[76:79], v[20:23]\n" \
  "ds_read_b128 v[68:71], v12 offset:0\n" \
  "v_mfma_f32_16x16x32_bf16 v[28:31], a[116:119], v[76:79], v[28:31]\n" \
  "ds_read_b128 v[72:75], v12 offset:2048\n" \
  "v_mfma_f32_16x16x32_bf16 v[36:39], v[76:79], a[244:247], v[36:39]\n" \
  "v_mfma_f32_16x16x32_bf16 v[24:27], v[244:247], v[80:83], v[24:27]\n" \
  "v_mfma_f32_16x16x32_bf16 v[32:35], a[116:119], v[80:83], v[32:35]\n" \
  "v_mfma_f32_16x16x32_bf16 v[40:43], v[80:83], a[244:247], v[40:43]\n" \
  "s_waitcnt lgkmcnt(4)\n" \
  "v_mfma_f32_16x16x32_bf16 v[20:23], v[248:251], v[84:87], v[20:23]\n" \
  "ds_read_b128 v[76:79], v13 offset:0\n" \
  "v_mfma_f32_16x16x32_bf16 v[28:31], a[120:123], v[84:87], v[28:31]\n" \
  "ds_read_b128 v[80:83], v13 offset:2048\n" \
  "v_mfma_f32_16x16x32_bf16 v[36:39], v[84:87], a[248:251], v[36:39]\n" \
  "v_mfma_f32_16x16x32_bf16 v[24:27], v[248:251], v[88:91], v[24:27]\n" \
  "v_mfma_f32_16x16x32_bf16 v[32:35], a[120:123], v[88:91], v[32:35]\n" \
  "v_mfma_f32_16x16x32_bf16 v[40:43], v[88:91], a[248:251], v[40:43]\n" \
  "s_waitcnt lgkmcnt(4)\n" \
  "v_mfma_f32_16x16x32_bf16 v[20:23], v[252:255], v[92:95], v[20:23]\n" \
  "ds_read_b128 v[84:87], v12 offset:4096\n" \
  "v_mfma_f32_16x16x32_bf16 v[28:31], a[124:127], v[92:95], v[28:31]\n" \
  "ds_read_b128 v[88:91], v12 offset:6144\n" \
  "v_mfma_f32_16x16x32_bf16 v[36:39], v[92:95], a[252:255], v[36:39]\n" \
  "v_mfma_f32_16x16x32_bf16 v[24:27], v[252:255], v[96:99], v[24:27]\n" \
  "v_mfma_f32_16x16x32_bf16 v[32:35], a[124:127], v[96:99], v[32:35]\n" \
  "v_mfma_f32_16x16x32_bf16 v[40:43], v[96:99], a[252:255], v[40:43]\n" \
  "s_cmp_eq_u32 s85, 64\n" \
  "s_cbranch_scc1 LX1_%=\n" \
  "s_waitcnt vmcnt(8)\n" \
  "s_barrier\n" \
  "s_waitcnt lgkmcnt(4)\n" \
  "v_mfma_f32_16x16x32_bf16 v[44:47], v[128:131], v[68:71], 0\n" \
  "ds_read_b128 v[92:95], v13 offset:4096\n" \
  "s_sub_u32 s89, s85, 1\n" \
  "s_and_b32 s89, s89, 63\n" \
  "v_mfma_f32_16x16x32_bf16 v[52:55], a[0:3], v[68:71], 0\n" \
  "ds_read_b128 v[96:99], v13 offset:6144\n" \
  "s_lshr_b32 s84, s89, 2\n" \
  "s_xnor_b32 s84, s84, %0\n" \
  "v_mfma_f32_16x16x32_bf16 v[60:63], v[68:71], a[128:131], 0\n" \
  "s_and_b32 s84, s84, 1\n" \
  "s_lshl_b32 s84, s84, 13\n" \
  "v_mfma_f32_16x16x32_bf16 v[48:51], v[128:131], v[72:75], 0\n" \
  "s_lshl_b32 s88, s89, 19\n" \
  "s_or_b32 s88, s88, s84\n" \
  "v_mfma_f32_16x16x32_bf16 v[56:59], a[0:3], v[72:75], 0\n" \
  "s_add_u32 s89, s88, " STR(QKV_SZ32) "\n" \
  "s_add_u32 s90, s89, " STR(QKV_SZ32) "\n" \
  "v_mfma_f32_16x16x32_bf16 v[64:67], v[72:75], a[128:131], 0\n" \
  "s_add_u32 s85, s85, 1\n" \
  "s_waitcnt lgkmcnt(4)\n" \
  "v_mfma_f32_16x16x32_bf16 v[44:47], v[132:135], v[76:79], v[44:47]\n" \
  "ds_read_b128 v[68:71], v12 offset:8192\n" \
  "v_cvt_pk_bf16_f32 v116, v20, v21\n" \
  "v_mfma_f32_16x16x32_bf16 v[52:55], a[4:7], v[76:79], v[52:55]\n" \
  "ds_read_b128 v[72:75], v12 offset:10240\n" \
  "v_cvt_pk_bf16_f32 v117, v22, v23\n" \
  "v_mfma_f32_16x16x32_bf16 v[60:63], v[76:79], a[132:135], v[60:63]\n" \
  "v_cvt_pk_bf16_f32 v118, v24, v25\n" \
  "v_mfma_f32_16x16x32_bf16 v[48:51], v[132:135], v[80:83], v[48:51]\n" \
  "v_cvt_pk_bf16_f32 v119, v26, v27\n" \
  "v_mfma_f32_16x16x32_bf16 v[56:59], a[4:7], v[80:83], v[56:59]\n" \
  "v_cvt_pk_bf16_f32 v120, v28, v29\n" \
  "v_mfma_f32_16x16x32_bf16 v[64:67], v[80:83], a[132:135], v[64:67]\n" \
  "v_cvt_pk_bf16_f32 v121, v30, v31\n" \
  "s_waitcnt lgkmcnt(4)\n" \
  "v_mfma_f32_16x16x32_bf16 v[44:47], v[136:139], v[84:87], v[44:47]\n" \
  "ds_read_b128 v[76:79], v13 offset:8192\n" \
  "v_cvt_pk_bf16_f32 v122, v32, v33\n" \
  "v_mfma_f32_16x16x32_bf16 v[52:55], a[8:11], v[84:87], v[52:55]\n" \
  "ds_read_b128 v[80:83], v13 offset:10240\n" \
  "v_cvt_pk_bf16_f32 v123, v34, v35\n" \
  "v_mfma_f32_16x16x32_bf16 v[60:63], v[84:87], a[136:139], v[60:63]\n" \
  "v_cvt_pk_bf16_f32 v124, v36, v37\n" \
  "v_mfma_f32_16x16x32_bf16 v[48:51], v[136:139], v[88:91], v[48:51]\n" \
  "v_cvt_pk_bf16_f32 v125, v38, v39\n" \
  "v_mfma_f32_16x16x32_bf16 v[56:59], a[8:11], v[88:91], v[56:59]\n" \
  "v_cvt_pk_bf16_f32 v126, v40, v41\n" \
  "v_mfma_f32_16x16x32_bf16 v[64:67], v[88:91], a[136:139], v[64:67]\n" \
  "v_cvt_pk_bf16_f32 v127, v42, v43\n" \
  "s_waitcnt lgkmcnt(4)\n" \
  "v_mfma_f32_16x16x32_bf16 v[44:47], v[140:143], v[92:95], v[44:47]\n" \
  "ds_read_b128 v[84:87], v12 offset:12288\n" \
  "v_permlane16_swap_b32 v116, v118\n" \
  "v_mfma_f32_16x16x32_bf16 v[52:55], a[12:15], v[92:95], v[52:55]\n" \
  "ds_read_b128 v[88:91], v12 offset:14336\n" \
  "v_permlane16_swap_b32 v117, v119\n" \
  "v_mfma_f32_16x16x32_bf16 v[60:63], v[92:95], a[140:143], v[60:63]\n" \
  "v_permlane16_swap_b32 v120, v122\n" \
  "v_mfma_f32_16x16x32_bf16 v[48:51], v[140:143], v[96:99], v[48:51]\n" \
  "v_permlane16_swap_b32 v121, v123\n" \
  "v_mfma_f32_16x16x32_bf16 v[56:59], a[12:15], v[96:99], v[56:59]\n" \
  "v_mfma_f32_16x16x32_bf16 v[64:67], v[96:99], a[140:143], v[64:67]\n" \
  "s_waitcnt lgkmcnt(4)\n" \
  "v_mfma_f32_16x16x32_bf16 v[44:47], v[144:147], v[68:71], v[44:47]\n" \
  "ds_read_b128 v[92:95], v13 offset:12288\n" \
  "buffer_store_dwordx4 v[116:119], v14, s[76:79], s88 offen sc1\n" \
  "v_mfma_f32_16x16x32_bf16 v[52:55], a[16:19], v[68:71], v[52:55]\n" \
  "ds_read_b128 v[96:99], v13 offset:14336\n" \
  "buffer_store_dwordx4 v[120:123], v18, s[76:79], s89 offen sc1\n" \
  "v_mfma_f32_16x16x32_bf16 v[60:63], v[68:71], a[144:147], v[60:63]\n" \
  "buffer_store_dwordx4 v[124:127], v15, s[76:79], s90 offen sc1\n" \
  "v_mfma_f32_16x16x32_bf16 v[48:51], v[144:147], v[72:75], v[48:51]\n" \
  "v_mfma_f32_16x16x32_bf16 v[56:59], a[16:19], v[72:75], v[56:59]\n" \
  "v_mfma_f32_16x16x32_bf16 v[64:67], v[72:75], a[144:147], v[64:67]\n" \
  "s_waitcnt lgkmcnt(4)\n" \
  "v_mfma_f32_16x16x32_bf16 v[44:47], v[148:151], v[76:79], v[44:47]\n" \
  "ds_read_b128 v[68:71], v12 offset:16384\n" \
  "v_mfma_f32_16x16x32_bf16 v[52:55], a[20:23], v[76:79], v[52:55]\n" \
  "ds_read_b128 v[72:75], v12 offset:18432\n" \
  "v_mfma_f32_16x16x32_bf16 v[60:63], v[76:79], a[148:151], v[60:63]\n" \
  "v_mfma_f32_16x16x32_bf16 v[48:51], v[148:151], v[80:83], v[48:51]\n" \
  "s_add_u32 m0, s91, 32768\n" \
  "s_nop 0\n" \
  "buffer_load_dwordx4 v7, s[72:75], s87 offen lds\n" \
  "s_add_u32 s87, s87, 128\n" \
  "v_mfma_f32_16x16x32_bf16 v[56:59], a[20:23], v[80:83], v[56:59]\n" \
  "v_mfma_f32_16x16x32_bf16 v[64:67], v[80:83], a[148:151], v[64:67]\n" \
  "s_waitcnt lgkmcnt(4)\n" \
  "v_mfma_f32_16x16x32_bf16 v[44:47], v[152:155], v[84:87], v[44:47]\n" \
  "ds_read_b128 v[76:79], v13 offset:16384\n" \
  "v_mfma_f32_16x16x32_bf16 v[52:55], a[24:27], v[84:87], v[52:55]\n" \
  "ds_read_b128 v[80:83], v13 offset:18432\n" \
  "v_mfma_f32_16x16x32_bf16 v[60:63], v[84:87], a[152:155], v[60:63]\n" \
  "v_mfma_f32_16x16x32_bf16 v[48:51], v[152:155], v[88:91], v[48:51]\n" \
  "s_add_u32 m0, s91, 36864\n" \
  "s_nop 0\n" \
  "buffer_load_dwordx4 v7, s[72:75], s87 offen lds\n" \
  "s_add_u32 s87, s87, 128\n" \
  "v_mfma_f32_16x16x32_bf16 v[56:59], a[24:27], v[88:91], v[56:59]\n" \
  "v_mfma_f32_16x16x32_bf16 v[64:67], v[88:91], a[152:155], v[64:67]\n" \
  "s_waitcnt lgkmcnt(4)\n" \
  "v_mfma_f32_16x16x32_bf16 v[44:47], v[156:159], v[92:95], v[44:47]\n" \
  "ds_read_b128 v[84:87], v12 offset:20480\n" \
  "v_mfma_f32_16x16x32_bf16 v[52:55], a[28:31], v[92:95], v[52:55]\n" \
  "ds_read_b128 v[88:91], v12 offset:22528\n" \
  "v_mfma_f32_16x16x32_bf16 v[60:63], v[92:95], a[156:159], v[60:63]\n" \
  "v_mfma_f32_16x16x32_bf16 v[48:51], v[156:159], v[96:99], v[48:51]\n" \
  "s_add_u32 m0, s91, 40960\n" \
  "s_nop 0\n" \
  "buffer_load_dwordx4 v7, s[72:75], s87 offen lds\n" \
  "s_add_u32 s87, s87, 128\n" \
  "v_mfma_f32_16x16x32_bf16 v[56:59], a[28:31], v[96:99], v[56:59]\n" \
  "v_mfma_f32_16x16x32_bf16 v[64:67], v[96:99], a[156:159], v[64:67]\n" \
  "s_waitcnt lgkmcnt(4)\n" \
  "v_mfma_f32_16x16x32_bf16 v[44:47], v[160:163], v[68:71], v[44:47]\n" \
  "ds_read_b128 v[92:95], v13 offset:20480\n" \
  "v_mfma_f32_16x16x32_bf16 v[52:55], a[32:35], v[68:71], v[52:55]\n" \
  "ds_read_b128 v[96:99], v13 offset:22528\n" \
  "v_mfma_f32_16x16x32_bf16 v[60:63], v[68:71], a[160:163], v[60:63]\n" \
  "v_mfma_f32_16x16x32_bf16 v[48:51], v[160:163], v[72:75], v[48:51]\n" \
  "s_add_u32 m0, s91, 45056\n" \
  "s_nop 0\n" \
  "buffer_load_dwordx4 v7, s[72:75], s87 offen lds\n" \
  "s_add_u32 s87, s87, 128\n" \
  "v_mfma_f32_16x16x32_bf16 v[56:59], a[32:35], v[72:75], v[56:59]\n" \
  "v_mfma_f32_16x16x32_bf16 v[64:67], v[72:75], a[160:163], v[64:67]\n" \
  "s_waitcnt lgkmcnt(4)\n" \
  "v_mfma_f32_16x16x32_bf16 v[44:47], v[164:167], v[76:79], v[44:47]\n" \
  "ds_read_b128 v[68:71], v12 offset:24576\n" \
  "v_mfma_f32_16x16x32_bf16 v[52:55], a[36:39], v[76:79], v[52:55]\n" \
  "ds_read_b128 v[72:75], v12 offset:26624\n" \
  "v_mfma_f32_16x16x32_bf16 v[60:63], v[76:79], a[164:167], v[60:63]\n" \
  "v_mfma_f32_16x16x32_bf16 v[48:51], v[164:167], v[80:83], v[48:51]\n" \
  "s_add_u32 m0, s91, 49152\n" \
  "s_nop 0\n" \
  "buffer_load_dwordx4 v7, s[72:75], s87 offen lds\n" \
  "s_add_u32 s87, s87, 128\n" \
  "v_mfma_f32_16x16x32_bf16 v[56:59], a[36:39], v[80:83], v[56:59]\n" \
  "v_mfma_f32_16x16x32_bf16 v[64:67], v[80:83], a[164:167], v[64:67]\n" \
  "s_waitcnt lgkmcnt(4)\n" \
  "v_mfma_f32_16x16x32_bf16 v[44:47], v[168:171], v[84:87], v[44:47]\n" \
  "ds_read_b128 v[76:79], v13 offset:24576\n" \
  "v_mfma_f32_16x16x32_bf16 v[52:55], a[40:43], v[84:87], v[52:55]\n" \
  "ds_read_b128 v[80:83], v13 offset:26624\n" \
  "v_mfma_f32_16x16x32_bf16 v[60:63], v[84:87], a[168:171], v[60:63]\n" \
  "v_mfma_f32_16x16x32_bf16 v[48:51], v[168:171], v[88:91], v[48:51]\n" \
  "s_add_u32 m0, s91, 53248\n" \
  "s_nop 0\n" \
  "buffer_load_dwordx4 v7, s[72:75], s87 offen lds\n" \
  "s_add_u32 s87, s87, 128\n" \
  "v_mfma_f32_16x16x32_bf16 v[56:59], a[40:43], v[88:91], v[56:59]\n" \
  "v_mfma_f32_16x16x32_bf16 v[64:67], v[88:91], a[168:171], v[64:67]\n" \
  "s_waitcnt lgkmcnt(4)\n" \
  "v_mfma_f32_16x16x32_bf16 v[44:47], v[172:175], v[92:95], v[44:47]\n" \
  "ds_read_b128 v[84:87], v12 offset:28672\n" \
  "v_mfma_f32_16x16x32_bf16 v[52:55], a[44:47], v[92:95], v[52:55]\n" \
  "ds_read_b128 v[88:91], v12 offset:30720\n" \
  "v_mfma_f32_16x16x32_bf16 v[60:63], v[92:95], a[172:175], v[60:63]\n" \
  "v_mfma_f32_16x16x32_bf16 v[48:51], v[172:175], v[96:99], v[48:51]\n" \
  "s_add_u32 m0, s91, 57344\n" \
  "s_nop 0\n" \
  "buffer_load_dwordx4 v7, s[72:75], s87 offen lds\n" \
  "s_add_u32 s87, s87, 128\n" \
  "v_mfma_f32_16x16x32_bf16 v[56:59], a[44:47], v[96:99], v[56:59]\n" \
  "v_mfma_f32_16x16x32_bf16 v[64:67], v[96:99], a[172:175], v[64:67]\n" \
  "s_waitcnt lgkmcnt(4)\n" \
  "v_mfma_f32_16x16x32_bf16 v[44:47], v[176:179], v[68:71], v[44:47]\n" \
  "ds_read_b128 v[92:95], v13 offset:28672\n" \
  "v_mfma_f32_16x16x32_bf16 v[52:55], a[48:51], v[68:71], v[52:55]\n" \
  "ds_read_b128 v[96:99], v13 offset:30720\n" \
  "v_mfma_f32_16x16x32_bf16 v[60:63], v[68:71], a[176:179], v[60:63]\n" \
  "v_mfma_f32_16x16x32_bf16 v[48:51], v[176:179], v[72:75], v[48:51]\n" \
  "s_add_u32 m0, s91, 61440\n" \
  "s_nop 0\n" \
  "buffer_load_dwordx4 v7, s[72:75], s87 offen lds\n" \
  "s_add_u32 s87, s87, 63616\n" \
  "s_min_u32 s87, s87, s96\n" \
  "v_mfma_f32_16x16x32_bf16 v[56:59], a[48:51], v[72:75], v[56:59]\n" \
  "v_mfma_f32_16x16x32_bf16 v[64:67], v[72:75], a[176:179], v[64:67]\n" \
  "s_waitcnt lgkmcnt(4)\n" \
  "v_mfma_f32_16x16x32_bf16 v[44:47], v[180:183], v[76:79], v[44:47]\n" \
  "ds_read_b128 v[68:71], v12 offset:32768\n" \
  "v_mfma_f32_16x16x32_bf16 v[52:55], a[52:55], v[76:79], v[52:55]\n" \
  "ds_read_b128 v[72:75], v12 offset:34816\n" \
  "v_mfma_f32_16x16x32_bf16 v[60:63], v[76:79], a[180:183], v[60:63]\n" \
  "v_mfma_f32_16x16x32_bf16 v[48:51], v[180:183], v[80:83], v[48:51]\n" \
  "v_mfma_f32_16x16x32_bf16 v[56:59], a[52:55], v[80:83], v[56:59]\n" \
  "v_mfma_f32_16x16x32_bf16 v[64:67], v[80:83], a[180:183], v[64:67]\n" \
  "s_waitcnt lgkmcnt(4)\n" \
  "v_mfma_f32_16x16x32_bf16 v[44:47], v[184:187], v[84:87], v[44:47]\n" \
  "ds_read_b128 v[76:79], v13 offset:32768\n" \
  "v_mfma_f32_16x16x32_bf16 v[52:55], a[56:59], v[84:87], v[52:55]\n" \
  "ds_read_b128 v[80:83], v13 offset:34816\n" \
  "v_mfma_f32_16x16x32_bf16 v[60:63], v[84:87], a[184:187], v[60:63]\n" \
  "v_mfma_f32_16x16x32_bf16 v[48:51], v[184:187], v[88:91], v[48:51]\n" \
  "v_mfma_f32_16x16x32_bf16 v[56:59], a[56:59], v[88:91], v[56:59]\n" \
  "v_mfma_f32_16x16x32_bf16 v[64:67], v[88:91], a[184:187], v[64:67]\n" \
  "s_waitcnt lgkmcnt(4)\n" \
  "v_mfma_f32_16x16x32_bf16 v[44:47], v[188:191], v[92:95], v[44:47]\n" \
  "ds_read_b128 v[84:87], v12 offset:36864\n" \
  "v_mfma_f32_16x16x32_bf16 v[52:55], a[60:63], v[92:95], v[52:55]\n" \
  "ds_read_b128 v[88:91], v12 offset:38912\n" \
  "v_mfma_f32_16x16x32_bf16 v[60:63], v[92:95], a[188:191], v[60:63]\n" \
  "v_mfma_f32_16x16x32_bf16 v[48:51], v[188:191], v[96:99], v[48:51]\n" \
  "v_mfma_f32_16x16x32_bf16 v[56:59], a[60:63], v[96:99], v[56:59]\n" \
  "v_mfma_f32_16x16x32_bf16 v[64:67], v[96:99], a[188:191], v[64:67]\n" \
  "s_waitcnt vmcnt(8)\n" \
  "s_barrier\n" \
  "s_waitcnt lgkmcnt(4)\n" \
  "v_mfma_f32_16x16x32_bf16 v[44:47], v[192:195], v[68:71], v[44:47]\n" \
  "ds_read_b128 v[92:95], v13 offset:36864\n" \
  "buffer_store_dword v17, v16, s[80:83], 0 offen sc1\n" \
  "v_mfma_f32_16x16x32_bf16 v[52:55], a[64:67], v[68:71], v[52:55]\n" \
  "ds_read_b128 v[96:99], v13 offset:38912\n" \
  "v_add_u32 v17, 1, v17\n" \
  "v_mfma_f32_16x16x32_bf16 v[60:63], v[68:71], a[192:195], v[60:63]\n" \
  "v_mfma_f32_16x16x32_bf16 v[48:51], v[192:195], v[72:75], v[48:51]\n" \
  "v_mfma_f32_16x16x32_bf16 v[56:59], a[64:67], v[72:75], v[56:59]\n" \
  "v_mfma_f32_16x16x32_bf16 v[64:67], v[72:75], a[192:195], v[64:67]\n" \
  "s_waitcnt lgkmcnt(4)\n" \
  "v_mfma_f32_16x16x32_bf16 v[44:47], v[196:199], v[76:79], v[44:47]\n" \
  "ds_read_b128 v[68:71], v12 offset:40960\n" \
  "v_mfma_f32_16x16x32_bf16 v[52:55], a[68:71], v[76:79], v[52:55]\n" \
  "ds_read_b128 v[72:75], v12 offset:43008\n" \
  "v_mfma_f32_16x16x32_bf16 v[60:63], v[76:79], a[196:199], v[60:63]\n" \
  "v_mfma_f32_16x16x32_bf16 v[48:51], v[196:199], v[80:83], v[48:51]\n" \
  "v_mfma_f32_16x16x32_bf16 v[56:59], a[68:71], v[80:83], v[56:59]\n" \
  "v_mfma_f32_16x16x32_bf16 v[64:67], v[80:83], a[196:199], v[64:67]\n" \
  "s_waitcnt lgkmcnt(4)\n" \
  "v_mfma_f32_16x16x32_bf16 v[44:47], v[200:203], v[84:87], v[44:47]\n" \
  "ds_read_b128 v[76:79], v13 offset:40960\n" \
  "v_mfma_f32_16x16x32_bf16 v[52:55], a[72:75], v[84:87], v[52:55]\n" \
  "ds_read_b128 v[80:83], v13 offset:43008\n" \
  "v_mfma_f32_16x16x32_bf16 v[60:63], v[84:87], a[200:203], v[60:63]\n" \
  "v_mfma_f32_16x16x32_bf16 v[48:51], v[200:203], v[88:91], v[48:51]\n" \
  "v_mfma_f32_16x16x32_bf16 v[56:59], a[72:75], v[88:91], v[56:59]\n" \
  "v_mfma_f32_16x16x32_bf16 v[64:67], v[88:91], a[200:203], v[64:67]\n" \
  "s_waitcnt lgkmcnt(4)\n" \
  "v_mfma_f32_16x16x32_bf16 v[44:47], v[204:207], v[92:95], v[44:47]\n" \
  "ds_read_b128 v[84:87], v12 offset:45056\n" \
  "v_mfma_f32_16x16x32_bf16 v[52:55], a[76:79], v[92:95], v[52:55]\n" \
  "ds_read_b128 v[88:91], v12 offset:47104\n" \
  "v_mfma_f32_16x16x32_bf16 v[60:63], v[92:95], a[204:207], v[60:63]\n" \
  "v_mfma_f32_16x16x32_bf16 v[48:51], v[204:207], v[96:99], v[48:51]\n" \
  "v_mfma_f32_16x16x32_bf16 v[56:59], a[76:79], v[96:99], v[56:59]\n" \
  "v_mfma_f32_16x16x32_bf16 v[64:67], v[96:99], a[204:207], v[64:67]\n" \
  "s_waitcnt lgkmcnt(4)\n" \
  "v_mfma_f32_16x16x32_bf16 v[44:47], v[208:211], v[68:71], v[44:47]\n" \
  "ds_read_b128 v[92:95], v13 offset:45056\n" \
  "v_mfma_f32_16x16x32_bf16 v[52:55], a[80:83], v[68:71], v[52:55]\n" \
  "ds_read_b128 v[96:99], v13 offset:47104\n" \
  "v_mfma_f32_16x16x32_bf16 v[60:63], v[68:71], a[208:211], v[60:63]\n" \
  "v_mfma_f32_16x16x32_bf16 v[48:51], v[208:211], v[72:75], v[48:51]\n" \
  "v_mfma_f32_16x16x32_bf16 v[56:59], a[80:83], v[72:75], v[56:59]\n" \
  "v_mfma_f32_16x16x32_bf16 v[64:67], v[72:75], a[208:211], v[64:67]\n" \
  "s_waitcnt lgkmcnt(4)\n" \
  "v_mfma_f32_16x16x32_bf16 v[44:47], v[212:215], v[76:79], v[44:47]\n" \
  "ds_read_b128 v[68:71], v12 offset:49152\n" \
  "v_mfma_f32_16x16x32_bf16 v[52:55], a[84:87], v[76:79], v[52:55]\n" \
  "ds_read_b128 v[72:75], v12 offset:51200\n" \
  "v_mfma_f32_16x16x32_bf16 v[60:63], v[76:79], a[212:215], v[60:63]\n" \
  "v_mfma_f32_16x16x32_bf16 v[48:51], v[212:215], v[80:83], v[48:51]\n" \
  "s_add_u32 m0, s91, 65536\n" \
  "s_nop 0\n" \
  "buffer_load_dwordx4 v7, s[72:75], s87 offen lds\n" \
  "s_add_u32 s87, s87, 128\n" \
  "v_mfma_f32_16x16x32_bf16 v[56:59], a[84:87], v[80:83], v[56:59]\n" \
  "v_mfma_f32_16x16x32_bf16 v[64:67], v[80:83], a[212:215], v[64:67]\n" \
  "s_waitcnt lgkmcnt(4)\n" \
  "v_mfma_f32_16x16x32_bf16 v[44:47], v[216:219], v[84:87], v[44:47]\n" \
  "ds_read_b128 v[76:79], v13 offset:49152\n" \
  "v_mfma_f32_16x16x32_bf16 v[52:55], a[88:91], v[84:87], v[52:55]\n" \
  "ds_read_b128 v[80:83], v13 offset:51200\n" \
  "v_mfma_f32_16x16x32_bf16 v[60:63], v[84:87], a[216:219], v[60:63]\n" \
  "v_mfma_f32_16x16x32_bf16 v[48:51], v[216:219], v[88:91], v[48:51]\n" \
  "s_add_u32 m0, s91, 69632\n" \
  "s_nop 0\n" \
  "buffer_load_dwordx4 v7, s[72:75], s87 offen lds\n" \
  "s_add_u32 s87, s87, 128\n" \
  "v_mfma_f32_16x16x32_bf16 v[56:59], a[88:91], v[88:91], v[56:59]\n" \
  "v_mfma_f32_16x16x32_bf16 v[64:67], v[88:91], a[216:219], v[64:67]\n" \
  "s_waitcnt lgkmcnt(4)\n" \
  "v_mfma_f32_16x16x32_bf16 v[44:47], v[220:223], v[92:95], v[44:47]\n" \
  "ds_read_b128 v[84:87], v12 offset:53248\n" \
  "v_mfma_f32_16x16x32_bf16 v[52:55], a[92:95], v[92:95], v[52:55]\n" \
  "ds_read_b128 v[88:91], v12 offset:55296\n" \
  "v_mfma_f32_16x16x32_bf16 v[60:63], v[92:95], a[220:223], v[60:63]\n" \
  "v_mfma_f32_16x16x32_bf16 v[48:51], v[220:223], v[96:99], v[48:51]\n" \
  "s_add_u32 m0, s91, 73728\n" \
  "s_nop 0\n" \
  "buffer_load_dwordx4 v7, s[72:75], s87 offen lds\n" \
  "s_add_u32 s87, s87, 128\n" \
  "v_mfma_f32_16x16x32_bf16 v[56:59], a[92:95], v[96:99], v[56:59]\n" \
  "v_mfma_f32_16x16x32_bf16 v[64:67], v[96:99], a[220:223], v[64:67]\n" \
  "s_waitcnt lgkmcnt(4)\n" \
  "v_mfma_f32_16x16x32_bf16 v[44:47], v[224:227], v[68:71], v[44:47]\n" \
  "ds_read_b128 v[92:95], v13 offset:53248\n" \
  "v_mfma_f32_16x16x32_bf16 v[52:55], a[96:99], v[68:71], v[52:55]\n" \
  "ds_read_b128 v[96:99], v13 offset:55296\n" \
  "v_mfma_f32_16x16x32_bf16 v[60:63], v[68:71], a[224:227], v[60:63]\n" \
  "v_mfma_f32_16x16x32_bf16 v[48:51], v[224:227], v[72:75], v[48:51]\n" \
  "s_add_u32 m0, s91, 77824\n" \
  "s_nop 0\n" \
  "buffer_load_dwordx4 v7, s[72:75], s87 offen lds\n" \
  "s_add_u32 s87, s87, 128\n" \
  "v_mfma_f32_16x16x32_bf16 v[56:59], a[96:99], v[72:75], v[56:59]\n" \
  "v_mfma_f32_16x16x32_bf16 v[64:67], v[72:75], a[224:227], v[64:67]\n" \
  "s_waitcnt lgkmcnt(4)\n" \
  "v_mfma_f32_16x16x32_bf16 v[44:47], v[228:231], v[76:79], v[44:47]\n" \
  "ds_read_b128 v[68:71], v12 offset:57344\n" \
  "v_mfma_f32_16x16x32_bf16 v[52:55], a[100:103], v[76:79], v[52:55]\n" \
  "ds_read_b128 v[72:75], v12 offset:59392\n" \
  "v_mfma_f32_16x16x32_bf16 v[60:63], v[76:79], a[228:231], v[60:63]\n" \
  "v_mfma_f32_16x16x32_bf16 v[48:51], v[228:231], v[80:83], v[48:51]\n" \
  "s_add_u32 m0, s91, 81920\n" \
  "s_nop 0\n" \
  "buffer_load_dwordx4 v7, s[72:75], s87 offen lds\n" \
  "s_add_u32 s87, s87, 128\n" \
  "v_mfma_f32_16x16x32_bf16 v[56:59], a[100:103], v[80:83], v[56:59]\n" \
  "v_mfma_f32_16x16x32_bf16 v[64:67], v[80:83], a[228:231], v[64:67]\n" \
  "s_waitcnt lgkmcnt(4)\n" \
  "v_mfma_f32_16x16x32_bf16 v[44:47], v[232:235], v[84:87], v[44:47]\n" \
  "ds_read_b128 v[76:79], v13 offset:57344\n" \
  "v_mfma_f32_16x16x32_bf16 v[52:55], a[104:107], v[84:87], v[52:55]\n" \
  "ds_read_b128 v[80:83], v13 offset:59392\n" \
  "v_mfma_f32_16x16x32_bf16 v[60:63], v[84:87], a[232:235], v[60:63]\n" \
  "v_mfma_f32_16x16x32_bf16 v[48:51], v[232:235], v[88:91], v[48:51]\n" \
  "s_add_u32 m0, s91, 86016\n" \
  "s_nop 0\n" \
  "buffer_load_dwordx4 v7, s[72:75], s87 offen lds\n" \
  "s_add_u32 s87, s87, 128\n" \
  "v_mfma_f32_16x16x32_bf16 v[56:59], a[104:107], v[88:91], v[56:59]\n" \
  "v_mfma_f32_16x16x32_bf16 v[64:67], v[88:91], a[232:235], v[64:67]\n" \
  "s_waitcnt lgkmcnt(4)\n" \
  "v_mfma_f32_16x16x32_bf16 v[44:47], v[236:239], v[92:95], v[44:47]\n" \
  "ds_read_b128 v[84:87], v12 offset:61440\n" \
  "v_mfma_f32_16x16x32_bf16 v[52:55], a[108:111], v[92:95], v[52:55]\n" \
  "ds_read_b128 v[88:91], v12 offset:63488\n" \
  "v_mfma_f32_16x16x32_bf16 v[60:63], v[92:95], a[236:239], v[60:63]\n" \
  "v_mfma_f32_16x16x32_bf16 v[48:51], v[236:239], v[96:99], v[48:51]\n" \
  "s_add_u32 m0, s91, 90112\n" \
  "s_nop 0\n" \
  "buffer_load_dwordx4 v7, s[72:75], s87 offen lds\n" \
  "s_add_u32 s87, s87, 128\n" \
  "v_mfma_f32_16x16x32_bf16 v[56:59], a[108:111], v[96:99], v[56:59]\n" \
  "v_mfma_f32_16x16x32_bf16 v[64:67], v[96:99], a[236:239], v[64:67]\n" \
  "s_waitcnt lgkmcnt(4)\n" \
  "v_mfma_f32_16x16x32_bf16 v[44:47], v[240:243], v[68:71], v[44:47]\n" \
  "ds_read_b128 v[92:95], v13 offset:61440\n" \
  "v_mfma_f32_16x16x32_bf16 v[52:55], a[112:115], v[68:71], v[52:55]\n" \
  "ds_read_b128 v[96:99], v13 offset:63488\n" \
  "v_mfma_f32_16x16x32_bf16 v[60:63], v[68:71], a[240:243], v[60:63]\n" \
  "v_mfma_f32_16x16x32_bf16 v[48:51], v[240:243], v[72:75], v[48:51]\n" \
  "s_add_u32 m0, s91, 94208\n" \
  "s_nop 0\n" \
  "buffer_load_dwordx4 v7, s[72:75], s87 offen lds\n" \
  "s_add_u32 s87, s87, 128\n" \
  "v_mfma_f32_16x16x32_bf16 v[56:59], a[112:115], v[72:75], v[56:59]\n" \
  "v_mfma_f32_16x16x32_bf16 v[64:67], v[72:75], a[240:243], v[64:67]\n" \
  "s_waitcnt lgkmcnt(4)\n" \
  "v_mfma_f32_16x16x32_bf16 v[44:47], v[244:247], v[76:79], v[44:47]\n" \
  "ds_read_b128 v[68:71], v10 offset:0\n" \
  "v_mfma_f32_16x16x32_bf16 v[52:55], a[116:119], v[76:79], v[52:55]\n" \
  "ds_read_b128 v[72:75], v10 offset:2048\n" \
  "v_mfma_f32_16x16x32_bf16 v[60:63], v[76:79], a[244:247], v[60:63]\n" \
  "v_mfma_f32_16x16x32_bf16 v[48:51], v[244:247], v[80:83], v[48:51]\n" \
  "v_mfma_f32_16x16x32_bf16 v[56:59], a[116:119], v[80:83], v[56:59]\n" \
  "v_mfma_f32_16x16x32_bf16 v[64:67], v[80:83], a[244:247], v[64:67]\n" \
  "s_waitcnt lgkmcnt(4)\n" \
  "v_mfma_f32_16x16x32_bf16 v[44:47], v[248:251], v[84:87], v[44:47]\n" \
  "ds_read_b128 v[76:79], v11 offset:0\n" \
  "v_mfma_f32_16x16x32_bf16 v[52:55], a[120:123], v[84:87], v[52:55]\n" \
  "ds_read_b128 v[80:83], v11 offset:2048\n" \
  "v_mfma_f32_16x16x32_bf16 v[60:63], v[84:87], a[248:251], v[60:63]\n" \
  "v_mfma_f32_16x16x32_bf16 v[48:51], v[248:251], v[88:91], v[48:51]\n" \
  "v_mfma_f32_16x16x32_bf16 v[56:59], a[120:123], v[88:91], v[56:59]\n" \
  "v_mfma_f32_16x16x32_bf16 v[64:67], v[88:91], a[248:251], v[64:67]\n" \
  "s_waitcnt lgkmcnt(4)\n" \
  "v_mfma_f32_16x16x32_bf16 v[44:47], v[252:255], v[92:95], v[44:47]\n" \
  "ds_read_b128 v[84:87], v10 offset:4096\n" \
  "v_mfma_f32_16x16x32_bf16 v[52:55], a[124:127], v[92:95], v[52:55]\n" \
  "ds_read_b128 v[88:91], v10 offset:6144\n" \
  "v_mfma_f32_16x16x32_bf16 v[60:63], v[92:95], a[252:255], v[60:63]\n" \
  "v_mfma_f32_16x16x32_bf16 v[48:51], v[252:255], v[96:99], v[48:51]\n" \
  "v_mfma_f32_16x16x32_bf16 v[56:59], a[124:127], v[96:99], v[56:59]\n" \
  "v_mfma_f32_16x16x32_bf16 v[64:67], v[96:99], a[252:255], v[64:67]\n" \
  "s_branch LG_%=\n" \
  "LX0_%=:\n" \
  "s_sub_u32 s89, s85, 1\n" \
  "s_and_b32 s89, s89, 63\n" \
  "s_lshr_b32 s84, s89, 2\n" \
  "s_xnor_b32 s84, s84, %0\n" \
  "s_and_b32 s84, s84, 1\n" \
  "s_lshl_b32 s84, s84, 13\n" \
  "s_lshl_b32 s88, s89, 19\n" \
  "s_or_b32 s88, s88, s84\n" \
  "s_add_u32 s89, s88, " STR(QKV_SZ32) "\n" \
  "s_add_u32 s90, s89, " STR(QKV_SZ32) "\n" \
  "s_add_u32 s85, s85, 1\n" \
  "v_cvt_pk_bf16_f32 v116, v44, v45\n" \
  "v_cvt_pk_bf16_f32 v117, v46, v47\n" \
  "v_cvt_pk_bf16_f32 v118, v48, v49\n" \
  "v_cvt_pk_bf16_f32 v119, v50, v51\n" \
  "v_cvt_pk_bf16_f32 v120, v52, v53\n" \
  "v_cvt_pk_bf16_f32 v121, v54, v55\n" \
  "v_cvt_pk_bf16_f32 v122, v56, v57\n" \
  "v_cvt_pk_bf16_f32 v123, v58, v59\n" \
  "v_cvt_pk_bf16_f32 v124, v60, v61\n" \
  "v_cvt_pk_bf16_f32 v125, v62, v63\n" \
  "v_cvt_pk_bf16_f32 v126, v64, v65\n" \
  "v_cvt_pk_bf16_f32 v127, v66, v67\n" \
  "s_nop 1\n" \
  "v_permlane16_swap_b32 v116, v118\n" \
  "v_permlane16_swap_b32 v117, v119\n" \
  "v_permlane16_swap_b32 v120, v122\n" \
  "v_permlane16_swap_b32 v121, v123\n" \
  "buffer_store_dwordx4 v[116:119], v14, s[76:79], s88 offen sc1\n" \
  "buffer_store_dwordx4 v[120:123], v18, s[76:79], s89 offen sc1\n" \
  "buffer_store_dwordx4 v[124:127], v15, s[76:79], s90 offen sc1\n" \
  "s_branch LZ_%=\n" \
  "LX1_%=:\n" \
  "s_sub_u32 s89, s85, 1\n" \
  "s_and_b32 s89, s89, 63\n" \
  "s_lshr_b32 s84, s89, 2\n" \
  "s_xnor_b32 s84, s84, %0\n" \
  "s_and_b32 s84, s84, 1\n" \
  "s_lshl_b32 s84, s84, 13\n" \
  "s_lshl_b32 s88, s89, 19\n" \
  "s_or_b32 s88, s88, s84\n" \
  "s_add_u32 s89, s88, " STR(QKV_SZ32) "\n" \
  "s_add_u32 s90, s89, " STR(QKV_SZ32) "\n" \
  "s_add_u32 s85, s85, 1\n" \
  "v_cvt_pk_bf16_f32 v116, v20, v21\n" \
  "v_cvt_pk_bf16_f32 v117, v22, v23\n" \
  "v_cvt_pk_bf16_f32 v118, v24, v25\n" \
  "v_cvt_pk_bf16_f32 v119, v26, v27\n" \
  "v_cvt_pk_bf16_f32 v120, v28, v29\n" \
  "v_cvt_pk_bf16_f32 v121, v30, v31\n" \
  "v_cvt_pk_bf16_f32 v122, v32, v33\n" \
  "v_cvt_pk_bf16_f32 v123, v34, v35\n" \
  "v_cvt_pk_bf16_f32 v124, v36, v37\n" \
  "v_cvt_pk_bf16_f32 v125, v38, v39\n" \
  "v_cvt_pk_bf16_f32 v126, v40, v41\n" \
  "v_cvt_pk_bf16_f32 v127, v42, v43\n" \
  "s_nop 1\n" \
  "v_permlane16_swap_b32 v116, v118\n" \
  "v_permlane16_swap_b32 v117, v119\n" \
  "v_permlane16_swap_b32 v120, v122\n" \
  "v_permlane16_swap_b32 v121, v123\n" \
  "buffer_store_dwordx4 v[116:119], v14, s[76:79], s88 offen sc1\n" \
  "buffer_store_dwordx4 v[120:123], v18, s[76:79], s89 offen sc1\n" \
  "buffer_store_dwordx4 v[124:127], v15, s[76:79], s90 offen sc1\n" \
  "s_branch LZ_%=\n" \
  "LZ_%=:\n" \
  "s_waitcnt vmcnt(0) lgkmcnt(0)\n" \
  "s_barrier\n" \
  "buffer_store_dword v17, v16, s[80:83], 0 offen sc1\n" \
  "s_waitcnt vmcnt(0)\n" \
  ""

#define XO_ASM \
  "s_mov_b64 s[68:69], %0\n" \
  "s_and_b32 s69, s69, 0xffff\n" \
  "s_mov_b32 s70, 8388608\n" \
  "s_mov_b32 s71, 0x00020000\n" \
  "s_mov_b64 s[72:73], %1\n" \
  "s_and_b32 s73, s73, 0xffff\n" \
  "s_mov_b32 s74, " STR(QKV_SZ32) "\n" \
  "s_mov_b32 s75, 0x00020000\n" \
  "s_mov_b64 s[76:77], %2\n" \
  "s_and_b32 s77, s77, 0xffff\n" \
  "s_mov_b32 s78, 8388608\n" \
  "s_mov_b32 s79, 0x00020000\n" \
  "s_mov_b64 s[80:81], %3\n" \
  "s_and_b32 s81, s81, 0xffff\n" \
  "s_mov_b32 s82, " STR(CTRL_BYTES) "\n" \
  "s_mov_b32 s83, 0x00020000\n" \
  "v_mbcnt_lo_u32_b32 v4, -1, 0\n" \
  "v_mbcnt_hi_u32_b32 v4, -1, v4\n" \
  "v_and_b32 v20, 15, v4\n" \
  "v_lshrrev_b32 v21, 4, v4\n" \
  "v_lshlrev_b32 v6, 4, v4\n" \
  "s_lshl_b32 s86, %5, 15\n" \
  "s_add_u32 s86, s86, %6\n" \
  "s_lshl_b32 s90, %5, 21\n" \
  "v_lshrrev_b32 v5, 3, v4\n" \
  "v_and_b32 v15, 7, v4\n" \
  "v_lshlrev_b32 v7, 11, v5\n" \
  "v_lshl_add_u32 v7, v15, 4, v7\n" \
  "v_lshlrev_b32 v5, 5, v21\n" \
  "v_xor_b32 v7, v7, v5\n" \
  "s_add_u32 m0, s86, 0\n" \
  "s_nop 0\n" \
  "buffer_load_dwordx4 v7, s[68:71], s90 offen lds\n" \
  "s_add_u32 s90, s90, 16384\n" \
  "s_add_u32 m0, s86, 1024\n" \
  "s_nop 0\n" \
  "buffer_load_dwordx4 v7, s[68:71], s90 offen lds\n" \
  "s_add_u32 s90, s90, 16384\n" \
  "s_add_u32 m0, s86, 2048\n" \
  "s_nop 0\n" \
  "buffer_load_dwordx4 v7, s[68:71], s90 offen lds\n" \
  "s_add_u32 s90, s90, 16384\n" \
  "s_add_u32 m0, s86, 3072\n" \
  "s_nop 0\n" \
  "buffer_load_dwordx4 v7, s[68:71], s90 offen lds\n" \
  "s_add_u32 s90, s90, 16384\n" \
  "s_add_u32 m0, s86, 4096\n" \
  "s_nop 0\n" \
  "buffer_load_dwordx4 v7, s[68:71], s90 offen lds\n" \
  "s_add_u32 s90, s90, 16384\n" \
  "s_add_u32 m0, s86, 5120\n" \
  "s_nop 0\n" \
  "buffer_load_dwordx4 v7, s[68:71], s90 offen lds\n" \
  "s_add_u32 s90, s90, 16384\n" \
  "s_add_u32 m0, s86, 6144\n" \
  "s_nop 0\n" \
  "buffer_load_dwordx4 v7, s[68:71], s90 offen lds\n" \
  "s_add_u32 s90, s90, 16384\n" \
  "s_add_u32 m0, s86, 7168\n" \
  "s_nop 0\n" \
  "buffer_load_dwordx4 v7, s[68:71], s90 offen lds\n" \
  "s_add_u32 s90, s90, 16384\n" \
  "s_add_u32 m0, s86, 8192\n" \
  "s_nop 0\n" \
  "buffer_load_dwordx4 v7, s[68:71], s90 offen lds\n" \
  "s_add_u32 s90, s90, 16384\n" \
  "s_add_u32 m0, s86, 9216\n" \
  "s_nop 0\n" \
  "buffer_load_dwordx4 v7, s[68:71], s90 offen lds\n" \
  "s_add_u32 s90, s90, 16384\n" \
  "s_add_u32 m0, s86, 10240\n" \
  "s_nop 0\n" \
  "buffer_load_dwordx4 v7, s[68:71], s90 offen lds\n" \
  "s_add_u32 s90, s90, 16384\n" \
  "s_add_u32 m0, s86, 11264\n" \
  "s_nop 0\n" \
  "buffer_load_dwordx4 v7, s[68:71], s90 offen lds\n" \
  "s_add_u32 s90, s90, 16384\n" \
  "s_add_u32 m0, s86, 12288\n" \
  "s_nop 0\n" \
  "buffer_load_dwordx4 v7, s[68:71], s90 offen lds\n" \
  "s_add_u32 s90, s90, 16384\n" \
  "s_add_u32 m0, s86, 13312\n" \
  "s_nop 0\n" \
  "buffer_load_dwordx4 v7, s[68:71], s90 offen lds\n" \
  "s_add_u32 s90, s90, 16384\n" \
  "s_add_u32 m0, s86, 14336\n" \
  "s_nop 0\n" \
  "buffer_load_dwordx4 v7, s[68:71], s90 offen lds\n" \
  "s_add_u32 s90, s90, 16384\n" \
  "s_add_u32 m0, s86, 15360\n" \
  "s_nop 0\n" \
  "buffer_load_dwordx4 v7, s[68:71], s90 offen lds\n" \
  "s_add_u32 s90, s90, 16384\n" \
  "s_add_u32 m0, s86, 16384\n" \
  "s_nop 0\n" \
  "buffer_load_dwordx4 v7, s[68:71], s90 offen lds\n" \
  "s_add_u32 s90, s90, 16384\n" \
  "s_add_u32 m0, s86, 17408\n" \
  "s_nop 0\n" \
  "buffer_load_dwordx4 v7, s[68:71], s90 offen lds\n" \
  "s_add_u32 s90, s90, 16384\n" \
  "s_add_u32 m0, s86, 18432\n" \
  "s_nop 0\n" \
  "buffer_load_dwordx4 v7, s[68:71], s90 offen lds\n" \
  "s_add_u32 s90, s90, 16384\n" \
  "s_add_u32 m0, s86, 19456\n" \
  "s_nop 0\n" \
  "buffer_load_dwordx4 v7, s[68:71], s90 offen lds\n" \
  "s_add_u32 s90, s90, 16384\n" \
  "s_add_u32 m0, s86, 20480\n" \
  "s_nop 0\n" \
  "buffer_load_dwordx4 v7, s[68:71], s90 offen lds\n" \
  "s_add_u32 s90, s90, 16384\n" \
  "s_add_u32 m0, s86, 21504\n" \
  "s_nop 0\n" \
  "buffer_load_dwordx4 v7, s[68:71], s90 offen lds\n" \
  "s_add_u32 s90, s90, 16384\n" \
  "s_add_u32 m0, s86, 22528\n" \
  "s_nop 0\n" \
  "buffer_load_dwordx4 v7, s[68:71], s90 offen lds\n" \
  "s_add_u32 s90, s90, 16384\n" \
  "s_add_u32 m0, s86, 23552\n" \
  "s_nop 0\n" \
  "buffer_load_dwordx4 v7, s[68:71], s90 offen lds\n" \
  "s_add_u32 s90, s90, 16384\n" \
  "s_add_u32 m0, s86, 24576\n" \
  "s_nop 0\n" \
  "buffer_load_dwordx4 v7, s[68:71], s90 offen lds\n" \
  "s_add_u32 s90, s90, 16384\n" \
  "s_add_u32 m0, s86, 25600\n" \
  "s_nop 0\n" \
  "buffer_load_dwordx4 v7, s[68:71], s90 offen lds\n" \
  "s_add_u32 s90, s90, 16384\n" \
  "s_add_u32 m0, s86, 26624\n" \
  "s_nop 0\n" \
  "buffer_load_dwordx4 v7, s[68:71], s90 offen lds\n" \
  "s_add_u32 s90, s90, 16384\n" \
  "s_add_u32 m0, s86, 27648\n" \
  "s_nop 0\n" \
  "buffer_load_dwordx4 v7, s[68:71], s90 offen lds\n" \
  "s_add_u32 s90, s90, 16384\n" \
  "s_add_u32 m0, s86, 28672\n" \
  "s_nop 0\n" \
  "buffer_load_dwordx4 v7, s[68:71], s90 offen lds\n" \
  "s_add_u32 s90, s90, 16384\n" \
  "s_add_u32 m0, s86, 29696\n" \
  "s_nop 0\n" \
  "buffer_load_dwordx4 v7, s[68:71], s90 offen lds\n" \
  "s_add_u32 s90, s90, 16384\n" \
  "s_add_u32 m0, s86, 30720\n" \
  "s_nop 0\n" \
  "buffer_load_dwordx4 v7, s[68:71], s90 offen lds\n" \
  "s_add_u32 s90, s90, 16384\n" \
  "s_add_u32 m0, s86, 31744\n" \
  "s_nop 0\n" \
  "buffer_load_dwordx4 v7, s[68:71], s90 offen lds\n" \
  "s_add_u32 s90, s90, 16384\n" \
  "v_lshrrev_b32 v5, 2, v20\n" \
  "v_lshl_add_u32 v5, v21, 2, v5\n" \
  "v_lshlrev_b32 v15, 7, v5\n" \
  "v_lshrrev_b32 v5, 1, v5\n" \
  "v_and_b32 v5, 3, v5\n" \
  "v_lshl_add_u32 v15, v5, 5, v15\n" \
  "v_and_b32 v5, 3, v4\n" \
  "v_lshl_add_u32 v15, v5, 3, v15\n" \
  "v_xor_b32 v5, 0, v15\n" \
  "v_add_u32 v16, s86, v5\n" \
  "v_xor_b32 v5, 32, v15\n" \
  "v_add_u32 v17, s86, v5\n" \
  "v_xor_b32 v5, 64, v15\n" \
  "v_add_u32 v18, s86, v5\n" \
  "v_xor_b32 v5, 96, v15\n" \
  "v_add_u32 v19, s86, v5\n" \
  "s_lshl_b32 s84, %5, 12\n" \
  "s_add_u32 s84, s84, 131072\n" \
  "s_add_u32 s84, s84, %6\n" \
  "v_xor_b32 v5, v21, v20\n" \
  "v_lshlrev_b32 v5, 4, v5\n" \
  "v_lshl_add_u32 v5, v20, 8, v5\n" \
  "v_add_u32 v5, s84, v5\n" \
  "v_xor_b32 v12, 64, v5\n" \
  "v_xor_b32 v13, 128, v5\n" \
  "v_xor_b32 v14, 192, v5\n" \
  "s_lshl_b32 s84, %5, 2\n" \
  "v_add_u32 v5, s84, v21\n" \
  "v_lshlrev_b32 v10, 12, v5\n" \
  "v_lshl_add_u32 v10, v20, 3, v10\n" \
  "v_xor_b32 v15, v5, v20\n" \
  "v_lshlrev_b32 v15, 4, v15\n" \
  "v_lshl_add_u32 v15, v5, 8, v15\n" \
  "s_add_u32 s84, %6, 131072\n" \
  "v_add_u32 v11, s84, v15\n" \
  "s_and_b32 s84, %4, 1\n" \
  "s_lshl_b32 s96, s84, 18\n" \
  "s_lshl_b32 s85, %5, 16\n" \
  "s_add_u32 s96, s96, s85\n" \
  "s_lshl_b32 s88, s84, 16\n" \
  "s_lshr_b32 s84, %4, 1\n" \
  "s_lshl_b32 s84, s84, 7\n" \
  "s_add_u32 s88, s88, s84\n" \
  "v_and_b32 v8, 7, v4\n" \
  "v_lshlrev_b32 v8, 7, v8\n" \
  "s_lshl_b32 s84, %5, 10\n" \
  "s_add_u32 s84, s84, " STR(CW_XPROG) "*4\n" \
  "v_add_u32 v8, s84, v8\n" \
  "s_mov_b32 s91, 0\n" \
  "s_mov_b32 s89, 0\n" \
  "s_lshr_b32 s84, s89, 2\n" \
  "s_xnor_b32 s84, s84, %7\n" \
  "s_and_b32 s84, s84, 1\n" \
  "s_lshl_b32 s84, s84, 13\n" \
  "s_lshl_b32 s87, s89, 19\n" \
  "s_or_b32 s87, s87, s84\n" \
  "s_add_u32 s87, s87, s96\n" \
  "s_waitcnt vmcnt(16)\n" \
  "ds_read_b64_tr_b16 v[128:129], v16 offset:0\n" \
  "ds_read_b64_tr_b16 v[130:131], v16 offset:2048\n" \
  "ds_read_b64_tr_b16 v[132:133], v17 offset:0\n" \
  "ds_read_b64_tr_b16 v[134:135], v17 offset:2048\n" \
  "ds_read_b64_tr_b16 v[136:137], v18 offset:0\n" \
  "ds_read_b64_tr_b16 v[138:139], v18 offset:2048\n" \
  "ds_read_b64_tr_b16 v[140:141], v19 offset:0\n" \
  "ds_read_b64_tr_b16 v[142:143], v19 offset:2048\n" \
  "ds_read_b64_tr_b16 v[144:145], v16 offset:4096\n" \
  "ds_read_b64_tr_b16 v[146:147], v16 offset:6144\n" \
  "ds_read_b64_tr_b16 v[148:149], v17 offset:4096\n" \
  "ds_read_b64_tr_b16 v[150:151], v17 offset:6144\n" \
  "ds_read_b64_tr_b16 v[152:153], v18 offset:4096\n" \
  "ds_read_b64_tr_b16 v[154:155], v18 offset:6144\n" \
  "ds_read_b64_tr_b16 v[156:157], v19 offset:4096\n" \
  "ds_read_b64_tr_b16 v[158:159], v19 offset:6144\n" \
  "ds_read_b64_tr_b16 v[160:161], v16 offset:8192\n" \
  "ds_read_b64_tr_b16 v[162:163], v16 offset:10240\n" \
  "ds_read_b64_tr_b16 v[164:165], v17 offset:8192\n" \
  "ds_read_b64_tr_b16 v[166:167], v17 offset:10240\n" \
  "ds_read_b64_tr_b16 v[168:169], v18 offset:8192\n" \
  "ds_read_b64_tr_b16 v[170:171], v18 offset:10240\n" \
  "ds_read_b64_tr_b16 v[172:173], v19 offset:8192\n" \
  "ds_read_b64_tr_b16 v[174:175], v19 offset:10240\n" \
  "ds_read_b64_tr_b16 v[176:177], v16 offset:12288\n" \
  "ds_read_b64_tr_b16 v[178:179], v16 offset:14336\n" \
  "ds_read_b64_tr_b16 v[180:181], v17 offset:12288\n" \
  "ds_read_b64_tr_b16 v[182:183], v17 offset:14336\n" \
  "ds_read_b64_tr_b16 v[184:185], v18 offset:12288\n" \
  "ds_read_b64_tr_b16 v[186:187], v18 offset:14336\n" \
  "ds_read_b64_tr_b16 v[188:189], v19 offset:12288\n" \
  "ds_read_b64_tr_b16 v[190:191], v19 offset:14336\n" \
  "s_waitcnt lgkmcnt(0)\n" \
  "s_add_u32 m0, s86, 0\n" \
  "s_nop 0\n" \
  "buffer_load_dwordx4 v7, s[68:71], s90 offen lds\n" \
  "s_add_u32 s90, s90, 16384\n" \
  "s_add_u32 m0, s86, 1024\n" \
  "s_nop 0\n" \
  "buffer_load_dwordx4 v7, s[68:71], s90 offen lds\n" \
  "s_add_u32 s90, s90, 16384\n" \
  "s_add_u32 m0, s86, 2048\n" \
  "s_nop 0\n" \
  "buffer_load_dwordx4 v7, s[68:71], s90 offen lds\n" \
  "s_add_u32 s90, s90, 16384\n" \
  "s_add_u32 m0, s86, 3072\n" \
  "s_nop 0\n" \
  "buffer_load_dwordx4 v7, s[68:71], s90 offen lds\n" \
  "s_add_u32 s90, s90, 16384\n" \
  "s_add_u32 m0, s86, 4096\n" \
  "s_nop 0\n" \
  "buffer_load_dwordx4 v7, s[68:71], s90 offen lds\n" \
  "s_add_u32 s90, s90, 16384\n" \
  "s_add_u32 m0, s86, 5120\n" \
  "s_nop 0\n" \
  "buffer_load_dwordx4 v7, s[68:71], s90 offen lds\n" \
  "s_add_u32 s90, s90, 16384\n" \
  "s_add_u32 m0, s86, 6144\n" \
  "s_nop 0\n" \
  "buffer_load_dwordx4 v7, s[68:71], s90 offen lds\n" \
  "s_add_u32 s90, s90, 16384\n" \
  "s_add_u32 m0, s86, 7168\n" \
  "s_nop 0\n" \
  "buffer_load_dwordx4 v7, s[68:71], s90 offen lds\n" \
  "s_add_u32 s90, s90, 16384\n" \
  "s_add_u32 m0, s86, 8192\n" \
  "s_nop 0\n" \
  "buffer_load_dwordx4 v7, s[68:71], s90 offen lds\n" \
  "s_add_u32 s90, s90, 16384\n" \
  "s_add_u32 m0, s86, 9216\n" \
  "s_nop 0\n" \
  "buffer_load_dwordx4 v7, s[68:71], s90 offen lds\n" \
  "s_add_u32 s90, s90, 16384\n" \
  "s_add_u32 m0, s86, 10240\n" \
  "s_nop 0\n" \
  "buffer_load_dwordx4 v7, s[68:71], s90 offen lds\n" \
  "s_add_u32 s90, s90, 16384\n" \
  "s_add_u32 m0, s86, 11264\n" \
  "s_nop 0\n" \
  "buffer_load_dwordx4 v7, s[68:71], s90 offen lds\n" \
  "s_add_u32 s90, s90, 16384\n" \
  "s_add_u32 m0, s86, 12288\n" \
  "s_nop 0\n" \
  "buffer_load_dwordx4 v7, s[68:71], s90 offen lds\n" \
  "s_add_u32 s90, s90, 16384\n" \
  "s_add_u32 m0, s86, 13312\n" \
  "s_nop 0\n" \
  "buffer_load_dwordx4 v7, s[68:71], s90 offen lds\n" \
  "s_add_u32 s90, s90, 16384\n" \
  "s_add_u32 m0, s86, 14336\n" \
  "s_nop 0\n" \
  "buffer_load_dwordx4 v7, s[68:71], s90 offen lds\n" \
  "s_add_u32 s90, s90, 16384\n" \
  "s_add_u32 m0, s86, 15360\n" \
  "s_nop 0\n" \
  "buffer_load_dwordx4 v7, s[68:71], s90 offen lds\n" \
  "s_add_u32 s90, s90, 16384\n" \
  "s_waitcnt vmcnt(16)\n" \
  "ds_read_b64_tr_b16 v[192:193], v16 offset:16384\n" \
  "ds_read_b64_tr_b16 v[194:195], v16 offset:18432\n" \
  "ds_read_b64_tr_b16 v[196:197], v17 offset:16384\n" \
  "ds_read_b64_tr_b16 v[198:199], v17 offset:18432\n" \
  "ds_read_b64_tr_b16 v[200:201], v18 offset:16384\n" \
  "ds_read_b64_tr_b16 v[202:203], v18 offset:18432\n" \
  "ds_read_b64_tr_b16 v[204:205], v19 offset:16384\n" \
  "ds_read_b64_tr_b16 v[206:207], v19 offset:18432\n" \
  "ds_read_b64_tr_b16 v[208:209], v16 offset:20480\n" \
  "ds_read_b64_tr_b16 v[210:211], v16 offset:22528\n" \
  "ds_read_b64_tr_b16 v[212:213], v17 offset:20480\n" \
  "ds_read_b64_tr_b16 v[214:215], v17 offset:22528\n" \
  "ds_read_b64_tr_b16 v[216:217], v18 offset:20480\n" \
  "ds_read_b64_tr_b16 v[218:219], v18 offset:22528\n" \
  "ds_read_b64_tr_b16 v[220:221], v19 offset:20480\n" \
  "ds_read_b64_tr_b16 v[222:223], v19 offset:22528\n" \
  "ds_read_b64_tr_b16 v[224:225], v16 offset:24576\n" \
  "ds_read_b64_tr_b16 v[226:227], v16 offset:26624\n" \
  "ds_read_b64_tr_b16 v[228:229], v17 offset:24576\n" \
  "ds_read_b64_tr_b16 v[230:231], v17 offset:26624\n" \
  "ds_read_b64_tr_b16 v[232:233], v18 offset:24576\n" \
  "ds_read_b64_tr_b16 v[234:235], v18 offset:26624\n" \
  "ds_read_b64_tr_b16 v[236:237], v19 offset:24576\n" \
  "ds_read_b64_tr_b16 v[238:239], v19 offset:26624\n" \
  "ds_read_b64_tr_b16 v[240:241], v16 offset:28672\n" \
  "ds_read_b64_tr_b16 v[242:243], v16 offset:30720\n" \
  "ds_read_b64_tr_b16 v[244:245], v17 offset:28672\n" \
  "ds_read_b64_tr_b16 v[246:247], v17 offset:30720\n" \
  "ds_read_b64_tr_b16 v[248:249], v18 offset:28672\n" \
  "ds_read_b64_tr_b16 v[250:251], v18 offset:30720\n" \
  "ds_read_b64_tr_b16 v[252:253], v19 offset:28672\n" \
  "ds_read_b64_tr_b16 v[254:255], v19 offset:30720\n" \
  "s_waitcnt lgkmcnt(0)\n" \
  "s_add_u32 m0, s86, 16384\n" \
  "s_nop 0\n" \
  "buffer_load_dwordx4 v7, s[68:71], s90 offen lds\n" \
  "s_add_u32 s90, s90, 16384\n" \
  "s_add_u32 m0, s86, 17408\n" \
  "s_nop 0\n" \
  "buffer_load_dwordx4 v7, s[68:71], s90 offen lds\n" \
  "s_add_u32 s90, s90, 16384\n" \
  "s_add_u32 m0, s86, 18432\n" \
  "s_nop 0\n" \
  "buffer_load_dwordx4 v7, s[68:71], s90 offen lds\n" \
  "s_add_u32 s90, s90, 16384\n" \
  "s_add_u32 m0, s86, 19456\n" \
  "s_nop 0\n" \
  "buffer_load_dwordx4 v7, s[68:71], s90 offen lds\n" \
  "s_add_u32 s90, s90, 16384\n" \
  "s_add_u32 m0, s86, 20480\n" \
  "s_nop 0\n" \
  "buffer_load_dwordx4 v7, s[68:71], s90 offen lds\n" \
  "s_add_u32 s90, s90, 16384\n" \
  "s_add_u32 m0, s86, 21504\n" \
  "s_nop 0\n" \
  "buffer_load_dwordx4 v7, s[68:71], s90 offen lds\n" \
  "s_add_u32 s90, s90, 16384\n" \
  "s_add_u32 m0, s86, 22528\n" \
  "s_nop 0\n" \
  "buffer_load_dwordx4 v7, s[68:71], s90 offen lds\n" \
  "s_add_u32 s90, s90, 16384\n" \
  "s_add_u32 m0, s86, 23552\n" \
  "s_nop 0\n" \
  "buffer_load_dwordx4 v7, s[68:71], s90 offen lds\n" \
  "s_add_u32 s90, s90, 16384\n" \
  "s_add_u32 m0, s86, 24576\n" \
  "s_nop 0\n" \
  "buffer_load_dwordx4 v7, s[68:71], s90 offen lds\n" \
  "s_add_u32 s90, s90, 16384\n" \
  "s_add_u32 m0, s86, 25600\n" \
  "s_nop 0\n" \
  "buffer_load_dwordx4 v7, s[68:71], s90 offen lds\n" \
  "s_add_u32 s90, s90, 16384\n" \
  "s_add_u32 m0, s86, 26624\n" \
  "s_nop 0\n" \
  "buffer_load_dwordx4 v7, s[68:71], s90 offen lds\n" \
  "s_add_u32 s90, s90, 16384\n" \
  "s_add_u32 m0, s86, 27648\n" \
  "s_nop 0\n" \
  "buffer_load_dwordx4 v7, s[68:71], s90 offen lds\n" \
  "s_add_u32 s90, s90, 16384\n" \
  "s_add_u32 m0, s86, 28672\n" \
  "s_nop 0\n" \
  "buffer_load_dwordx4 v7, s[68:71], s90 offen lds\n" \
  "s_add_u32 s90, s90, 16384\n" \
  "s_add_u32 m0, s86, 29696\n" \
  "s_nop 0\n" \
  "buffer_load_dwordx4 v7, s[68:71], s90 offen lds\n" \
  "s_add_u32 s90, s90, 16384\n" \
  "s_add_u32 m0, s86, 30720\n" \
  "s_nop 0\n" \
  "buffer_load_dwordx4 v7, s[68:71], s90 offen lds\n" \
  "s_add_u32 s90, s90, 16384\n" \
  "s_add_u32 m0, s86, 31744\n" \
  "s_nop 0\n" \
  "buffer_load_dwordx4 v7, s[68:71], s90 offen lds\n" \
  "s_add_u32 s90, s90, 16384\n" \
  "s_waitcnt vmcnt(16)\n" \
  "ds_read_b64_tr_b16 a[0:1], v16 offset:0\n" \
  "ds_read_b64_tr_b16 a[2:3], v16 offset:2048\n" \
  "ds_read_b64_tr_b16 a[4:5], v17 offset:0\n" \
  "ds_read_b64_tr_b16 a[6:7], v17 offset:2048\n" \
  "ds_read_b64_tr_b16 a[8:9], v18 offset:0\n" \
  "ds_read_b64_tr_b16 a[10:11], v18 offset:2048\n" \
  "ds_read_b64_tr_b16 a[12:13], v19 offset:0\n" \
  "ds_read_b64_tr_b16 a[14:15], v19 offset:2048\n" \
  "ds_read_b64_tr_b16 a[16:17], v16 offset:4096\n" \
  "ds_read_b64_tr_b16 a[18:19], v16 offset:6144\n" \
  "ds_read_b64_tr_b16 a[20:21], v17 offset:4096\n" \
  "ds_read_b64_tr_b16 a[22:23], v17 offset:6144\n" \
  "ds_read_b64_tr_b16 a[24:25], v18 offset:4096\n" \
  "ds_read_b64_tr_b16 a[26:27], v18 offset:6144\n" \
  "ds_read_b64_tr_b16 a[28:29], v19 offset:4096\n" \
  "ds_read_b64_tr_b16 a[30:31], v19 offset:6144\n" \
  "ds_read_b64_tr_b16 a[32:33], v16 offset:8192\n" \
  "ds_read_b64_tr_b16 a[34:35], v16 offset:10240\n" \
  "ds_read_b64_tr_b16 a[36:37], v17 offset:8192\n" \
  "ds_read_b64_tr_b16 a[38:39], v17 offset:10240\n" \
  "ds_read_b64_tr_b16 a[40:41], v18 offset:8192\n" \
  "ds_read_b64_tr_b16 a[42:43], v18 offset:10240\n" \
  "ds_read_b64_tr_b16 a[44:45], v19 offset:8192\n" \
  "ds_read_b64_tr_b16 a[46:47], v19 offset:10240\n" \
  "ds_read_b64_tr_b16 a[48:49], v16 offset:12288\n" \
  "ds_read_b64_tr_b16 a[50:51], v16 offset:14336\n" \
  "ds_read_b64_tr_b16 a[52:53], v17 offset:12288\n" \
  "ds_read_b64_tr_b16 a[54:55], v17 offset:14336\n" \
  "ds_read_b64_tr_b16 a[56:57], v18 offset:12288\n" \
  "ds_read_b64_tr_b16 a[58:59], v18 offset:14336\n" \
  "ds_read_b64_tr_b16 a[60:61], v19 offset:12288\n" \
  "ds_read_b64_tr_b16 a[62:63], v19 offset:14336\n" \
  "s_waitcnt lgkmcnt(0)\n" \
  "s_add_u32 m0, s86, 0\n" \
  "s_nop 0\n" \
  "buffer_load_dwordx4 v7, s[68:71], s90 offen lds\n" \
  "s_add_u32 s90, s90, 16384\n" \
  "s_add_u32 m0, s86, 1024\n" \
  "s_nop 0\n" \
  "buffer_load_dwordx4 v7, s[68:71], s90 offen lds\n" \
  "s_add_u32 s90, s90, 16384\n" \
  "s_add_u32 m0, s86, 2048\n" \
  "s_nop 0\n" \
  "buffer_load_dwordx4 v7, s[68:71], s90 offen lds\n" \
  "s_add_u32 s90, s90, 16384\n" \
  "s_add_u32 m0, s86, 3072\n" \
  "s_nop 0\n" \
  "buffer_load_dwordx4 v7, s[68:71], s90 offen lds\n" \
  "s_add_u32 s90, s90, 16384\n" \
  "s_add_u32 m0, s86, 4096\n" \
  "s_nop 0\n" \
  "buffer_load_dwordx4 v7, s[68:71], s90 offen lds\n" \
  "s_add_u32 s90, s90, 16384\n" \
  "s_add_u32 m0, s86, 5120\n" \
  "s_nop 0\n" \
  "buffer_load_dwordx4 v7, s[68:71], s90 offen lds\n" \
  "s_add_u32 s90, s90, 16384\n" \
  "s_add_u32 m0, s86, 6144\n" \
  "s_nop 0\n" \
  "buffer_load_dwordx4 v7, s[68:71], s90 offen lds\n" \
  "s_add_u32 s90, s90, 16384\n" \
  "s_add_u32 m0, s86, 7168\n" \
  "s_nop 0\n" \
  "buffer_load_dwordx4 v7, s[68:71], s90 offen lds\n" \
  "s_add_u32 s90, s90, 16384\n" \
  "s_add_u32 m0, s86, 8192\n" \
  "s_nop 0\n" \
  "buffer_load_dwordx4 v7, s[68:71], s90 offen lds\n" \
  "s_add_u32 s90, s90, 16384\n" \
  "s_add_u32 m0, s86, 9216\n" \
  "s_nop 0\n" \
  "buffer_load_dwordx4 v7, s[68:71], s90 offen lds\n" \
  "s_add_u32 s90, s90, 16384\n" \
  "s_add_u32 m0, s86, 10240\n" \
  "s_nop 0\n" \
  "buffer_load_dwordx4 v7, s[68:71], s90 offen lds\n" \
  "s_add_u32 s90, s90, 16384\n" \
  "s_add_u32 m0, s86, 11264\n" \
  "s_nop 0\n" \
  "buffer_load_dwordx4 v7, s[68:71], s90 offen lds\n" \
  "s_add_u32 s90, s90, 16384\n" \
  "s_add_u32 m0, s86, 12288\n" \
  "s_nop 0\n" \
  "buffer_load_dwordx4 v7, s[68:71], s90 offen lds\n" \
  "s_add_u32 s90, s90, 16384\n" \
  "s_add_u32 m0, s86, 13312\n" \
  "s_nop 0\n" \
  "buffer_load_dwordx4 v7, s[68:71], s90 offen lds\n" \
  "s_add_u32 s90, s90, 16384\n" \
  "s_add_u32 m0, s86, 14336\n" \
  "s_nop 0\n" \
  "buffer_load_dwordx4 v7, s[68:71], s90 offen lds\n" \
  "s_add_u32 s90, s90, 16384\n" \
  "s_add_u32 m0, s86, 15360\n" \
  "s_nop 0\n" \
  "buffer_load_dwordx4 v7, s[68:71], s90 offen lds\n" \
  "s_add_u32 s90, s90, 16384\n" \
  "s_waitcnt vmcnt(16)\n" \
  "ds_read_b64_tr_b16 a[64:65], v16 offset:16384\n" \
  "ds_read_b64_tr_b16 a[66:67], v16 offset:18432\n" \
  "ds_read_b64_tr_b16 a[68:69], v17 offset:16384\n" \
  "ds_read_b64_tr_b16 a[70:71], v17 offset:18432\n" \
  "ds_read_b64_tr_b16 a[72:73], v18 offset:16384\n" \
  "ds_read_b64_tr_b16 a[74:75], v18 offset:18432\n" \
  "ds_read_b64_tr_b16 a[76:77], v19 offset:16384\n" \
  "ds_read_b64_tr_b16 a[78:79], v19 offset:18432\n" \
  "ds_read_b64_tr_b16 a[80:81], v16 offset:20480\n" \
  "ds_read_b64_tr_b16 a[82:83], v16 offset:22528\n" \
  "ds_read_b64_tr_b16 a[84:85], v17 offset:20480\n" \
  "ds_read_b64_tr_b16 a[86:87], v17 offset:22528\n" \
  "ds_read_b64_tr_b16 a[88:89], v18 offset:20480\n" \
  "ds_read_b64_tr_b16 a[90:91], v18 offset:22528\n" \
  "ds_read_b64_tr_b16 a[92:93], v19 offset:20480\n" \
  "ds_read_b64_tr_b16 a[94:95], v19 offset:22528\n" \
  "ds_read_b64_tr_b16 a[96:97], v16 offset:24576\n" \
  "ds_read_b64_tr_b16 a[98:99], v16 offset:26624\n" \
  "ds_read_b64_tr_b16 a[100:101], v17 offset:24576\n" \
  "ds_read_b64_tr_b16 a[102:103], v17 offset:26624\n" \
  "ds_read_b64_tr_b16 a[104:105], v18 offset:24576\n" \
  "ds_read_b64_tr_b16 a[106:107], v18 offset:26624\n" \
  "ds_read_b64_tr_b16 a[108:109], v19 offset:24576\n" \
  "ds_read_b64_tr_b16 a[110:111], v19 offset:26624\n" \
  "ds_read_b64_tr_b16 a[112:113], v16 offset:28672\n" \
  "ds_read_b64_tr_b16 a[114:115], v16 offset:30720\n" \
  "ds_read_b64_tr_b16 a[116:117], v17 offset:28672\n" \
  "ds_read_b64_tr_b16 a[118:119], v17 offset:30720\n" \
  "ds_read_b64_tr_b16 a[120:121], v18 offset:28672\n" \
  "ds_read_b64_tr_b16 a[122:123], v18 offset:30720\n" \
  "ds_read_b64_tr_b16 a[124:125], v19 offset:28672\n" \
  "ds_read_b64_tr_b16 a[126:127], v19 offset:30720\n" \
  "s_waitcnt lgkmcnt(0)\n" \
  "s_add_u32 m0, s86, 16384\n" \
  "s_nop 0\n" \
  "buffer_load_dwordx4 v7, s[68:71], s90 offen lds\n" \
  "s_add_u32 s90, s90, 16384\n" \
  "s_add_u32 m0, s86, 17408\n" \
  "s_nop 0\n" \
  "buffer_load_dwordx4 v7, s[68:71], s90 offen lds\n" \
  "s_add_u32 s90, s90, 16384\n" \
  "s_add_u32 m0, s86, 18432\n" \
  "s_nop 0\n" \
  "buffer_load_dwordx4 v7, s[68:71], s90 offen lds\n" \
  "s_add_u32 s90, s90, 16384\n" \
  "s_add_u32 m0, s86, 19456\n" \
  "s_nop 0\n" \
  "buffer_load_dwordx4 v7, s[68:71], s90 offen lds\n" \
  "s_add_u32 s90, s90, 16384\n" \
  "s_add_u32 m0, s86, 20480\n" \
  "s_nop 0\n" \
  "buffer_load_dwordx4 v7, s[68:71], s90 offen lds\n" \
  "s_add_u32 s90, s90, 16384\n" \
  "s_add_u32 m0, s86, 21504\n" \
  "s_nop 0\n" \
  "buffer_load_dwordx4 v7, s[68:71], s90 offen lds\n" \
  "s_add_u32 s90, s90, 16384\n" \
  "s_add_u32 m0, s86, 22528\n" \
  "s_nop 0\n" \
  "buffer_load_dwordx4 v7, s[68:71], s90 offen lds\n" \
  "s_add_u32 s90, s90, 16384\n" \
  "s_add_u32 m0, s86, 23552\n" \
  "s_nop 0\n" \
  "buffer_load_dwordx4 v7, s[68:71], s90 offen lds\n" \
  "s_add_u32 s90, s90, 16384\n" \
  "s_add_u32 m0, s86, 24576\n" \
  "s_nop 0\n" \
  "buffer_load_dwordx4 v7, s[68:71], s90 offen lds\n" \
  "s_add_u32 s90, s90, 16384\n" \
  "s_add_u32 m0, s86, 25600\n" \
  "s_nop 0\n" \
  "buffer_load_dwordx4 v7, s[68:71], s90 offen lds\n" \
  "s_add_u32 s90, s90, 16384\n" \
  "s_add_u32 m0, s86, 26624\n" \
  "s_nop 0\n" \
  "buffer_load_dwordx4 v7, s[68:71], s90 offen lds\n" \
  "s_add_u32 s90, s90, 16384\n" \
  "s_add_u32 m0, s86, 27648\n" \
  "s_nop 0\n" \
  "buffer_load_dwordx4 v7, s[68:71], s90 offen lds\n" \
  "s_add_u32 s90, s90, 16384\n" \
  "s_add_u32 m0, s86, 28672\n" \
  "s_nop 0\n" \
  "buffer_load_dwordx4 v7, s[68:71], s90 offen lds\n" \
  "s_add_u32 s90, s90, 16384\n" \
  "s_add_u32 m0, s86, 29696\n" \
  "s_nop 0\n" \
  "buffer_load_dwordx4 v7, s[68:71], s90 offen lds\n" \
  "s_add_u32 s90, s90, 16384\n" \
  "s_add_u32 m0, s86, 30720\n" \
  "s_nop 0\n" \
  "buffer_load_dwordx4 v7, s[68:71], s90 offen lds\n" \
  "s_add_u32 s90, s90, 16384\n" \
  "s_add_u32 m0, s86, 31744\n" \
  "s_nop 0\n" \
  "buffer_load_dwordx4 v7, s[68:71], s90 offen lds\n" \
  "s_add_u32 s90, s90, 16384\n" \
  "s_waitcnt vmcnt(16)\n" \
  "ds_read_b64_tr_b16 a[128:129], v16 offset:0\n" \
  "ds_read_b64_tr_b16 a[130:131], v16 offset:2048\n" \
  "ds_read_b64_tr_b16 a[132:133], v17 offset:0\n" \
  "ds_read_b64_tr_b16 a[134:135], v17 offset:2048\n" \
  "ds_read_b64_tr_b16 a[136:137], v18 offset:0\n" \
  "ds_read_b64_tr_b16 a[138:139], v18 offset:2048\n" \
  "ds_read_b64_tr_b16 a[140:141], v19 offset:0\n" \
  "ds_read_b64_tr_b16 a[142:143], v19 offset:2048\n" \
  "ds_read_b64_tr_b16 a[144:145], v16 offset:4096\n" \
  "ds_read_b64_tr_b16 a[146:147], v16 offset:6144\n" \
  "ds_read_b64_tr_b16 a[148:149], v17 offset:4096\n" \
  "ds_read_b64_tr_b16 a[150:151], v17 offset:6144\n" \
  "ds_read_b64_tr_b16 a[152:153], v18 offset:4096\n" \
  "ds_read_b64_tr_b16 a[154:155], v18 offset:6144\n" \
  "ds_read_b64_tr_b16 a[156:157], v19 offset:4096\n" \
  "ds_read_b64_tr_b16 a[158:159], v19 offset:6144\n" \
  "ds_read_b64_tr_b16 a[160:161], v16 offset:8192\n" \
  "ds_read_b64_tr_b16 a[162:163], v16 offset:10240\n" \
  "ds_read_b64_tr_b16 a[164:165], v17 offset:8192\n" \
  "ds_read_b64_tr_b16 a[166:167], v17 offset:10240\n" \
  "ds_read_b64_tr_b16 a[168:169], v18 offset:8192\n" \
  "ds_read_b64_tr_b16 a[170:171], v18 offset:10240\n" \
  "ds_read_b64_tr_b16 a[172:173], v19 offset:8192\n" \
  "ds_read_b64_tr_b16 a[174:175], v19 offset:10240\n" \
  "ds_read_b64_tr_b16 a[176:177], v16 offset:12288\n" \
  "ds_read_b64_tr_b16 a[178:179], v16 offset:14336\n" \
  "ds_read_b64_tr_b16 a[180:181], v17 offset:12288\n" \
  "ds_read_b64_tr_b16 a[182:183], v17 offset:14336\n" \
  "ds_read_b64_tr_b16 a[184:185], v18 offset:12288\n" \
  "ds_read_b64_tr_b16 a[186:187], v18 offset:14336\n" \
  "ds_read_b64_tr_b16 a[188:189], v19 offset:12288\n" \
  "ds_read_b64_tr_b16 a[190:191], v19 offset:14336\n" \
  "s_waitcnt lgkmcnt(0)\n" \
  "s_add_u32 m0, s86, 0\n" \
  "s_nop 0\n" \
  "buffer_load_dwordx4 v7, s[68:71], s90 offen lds\n" \
  "s_add_u32 s90, s90, 16384\n" \
  "s_add_u32 m0, s86, 1024\n" \
  "s_nop 0\n" \
  "buffer_load_dwordx4 v7, s[68:71], s90 offen lds\n" \
  "s_add_u32 s90, s90, 16384\n" \
  "s_add_u32 m0, s86, 2048\n" \
  "s_nop 0\n" \
  "buffer_load_dwordx4 v7, s[68:71], s90 offen lds\n" \
  "s_add_u32 s90, s90, 16384\n" \
  "s_add_u32 m0, s86, 3072\n" \
  "s_nop 0\n" \
  "buffer_load_dwordx4 v7, s[68:71], s90 offen lds\n" \
  "s_add_u32 s90, s90, 16384\n" \
  "s_add_u32 m0, s86, 4096\n" \
  "s_nop 0\n" \
  "buffer_load_dwordx4 v7, s[68:71], s90 offen lds\n" \
  "s_add_u32 s90, s90, 16384\n" \
  "s_add_u32 m0, s86, 5120\n" \
  "s_nop 0\n" \
  "buffer_load_dwordx4 v7, s[68:71], s90 offen lds\n" \
  "s_add_u32 s90, s90, 16384\n" \
  "s_add_u32 m0, s86, 6144\n" \
  "s_nop 0\n" \
  "buffer_load_dwordx4 v7, s[68:71], s90 offen lds\n" \
  "s_add_u32 s90, s90, 16384\n" \
  "s_add_u32 m0, s86, 7168\n" \
  "s_nop 0\n" \
  "buffer_load_dwordx4 v7, s[68:71], s90 offen lds\n" \
  "s_add_u32 s90, s90, 16384\n" \
  "s_add_u32 m0, s86, 8192\n" \
  "s_nop 0\n" \
  "buffer_load_dwordx4 v7, s[68:71], s90 offen lds\n" \
  "s_add_u32 s90, s90, 16384\n" \
  "s_add_u32 m0, s86, 9216\n" \
  "s_nop 0\n" \
  "buffer_load_dwordx4 v7, s[68:71], s90 offen lds\n" \
  "s_add_u32 s90, s90, 16384\n" \
  "s_add_u32 m0, s86, 10240\n" \
  "s_nop 0\n" \
  "buffer_load_dwordx4 v7, s[68:71], s90 offen lds\n" \
  "s_add_u32 s90, s90, 16384\n" \
  "s_add_u32 m0, s86, 11264\n" \
  "s_nop 0\n" \
  "buffer_load_dwordx4 v7, s[68:71], s90 offen lds\n" \
  "s_add_u32 s90, s90, 16384\n" \
  "s_add_u32 m0, s86, 12288\n" \
  "s_nop 0\n" \
  "buffer_load_dwordx4 v7, s[68:71], s90 offen lds\n" \
  "s_add_u32 s90, s90, 16384\n" \
  "s_add_u32 m0, s86, 13312\n" \
  "s_nop 0\n" \
  "buffer_load_dwordx4 v7, s[68:71], s90 offen lds\n" \
  "s_add_u32 s90, s90, 16384\n" \
  "s_add_u32 m0, s86, 14336\n" \
  "s_nop 0\n" \
  "buffer_load_dwordx4 v7, s[68:71], s90 offen lds\n" \
  "s_add_u32 s90, s90, 16384\n" \
  "s_add_u32 m0, s86, 15360\n" \
  "s_nop 0\n" \
  "buffer_load_dwordx4 v7, s[68:71], s90 offen lds\n" \
  "s_add_u32 s90, s90, 16384\n" \
  "s_waitcnt vmcnt(16)\n" \
  "ds_read_b64_tr_b16 a[192:193], v16 offset:16384\n" \
  "ds_read_b64_tr_b16 a[194:195], v16 offset:18432\n" \
  "ds_read_b64_tr_b16 a[196:197], v17 offset:16384\n" \
  "ds_read_b64_tr_b16 a[198:199], v17 offset:18432\n" \
  "ds_read_b64_tr_b16 a[200:201], v18 offset:16384\n" \
  "ds_read_b64_tr_b16 a[202:203], v18 offset:18432\n" \
  "ds_read_b64_tr_b16 a[204:205], v19 offset:16384\n" \
  "ds_read_b64_tr_b16 a[206:207], v19 offset:18432\n" \
  "ds_read_b64_tr_b16 a[208:209], v16 offset:20480\n" \
  "ds_read_b64_tr_b16 a[210:211], v16 offset:22528\n" \
  "ds_read_b64_tr_b16 a[212:213], v17 offset:20480\n" \
  "ds_read_b64_tr_b16 a[214:215], v17 offset:22528\n" \
  "ds_read_b64_tr_b16 a[216:217], v18 offset:20480\n" \
  "ds_read_b64_tr_b16 a[218:219], v18 offset:22528\n" \
  "ds_read_b64_tr_b16 a[220:221], v19 offset:20480\n" \
  "ds_read_b64_tr_b16 a[222:223], v19 offset:22528\n" \
  "ds_read_b64_tr_b16 a[224:225], v16 offset:24576\n" \
  "ds_read_b64_tr_b16 a[226:227], v16 offset:26624\n" \
  "ds_read_b64_tr_b16 a[228:229], v17 offset:24576\n" \
  "ds_read_b64_tr_b16 a[230:231], v17 offset:26624\n" \
  "ds_read_b64_tr_b16 a[232:233], v18 offset:24576\n" \
  "ds_read_b64_tr_b16 a[234:235], v18 offset:26624\n" \
  "ds_read_b64_tr_b16 a[236:237], v19 offset:24576\n" \
  "ds_read_b64_tr_b16 a[238:239], v19 offset:26624\n" \
  "ds_read_b64_tr_b16 a[240:241], v16 offset:28672\n" \
  "ds_read_b64_tr_b16 a[242:243], v16 offset:30720\n" \
  "ds_read_b64_tr_b16 a[244:245], v17 offset:28672\n" \
  "ds_read_b64_tr_b16 a[246:247], v17 offset:30720\n" \
  "ds_read_b64_tr_b16 a[248:249], v18 offset:28672\n" \
  "ds_read_b64_tr_b16 a[250:251], v18 offset:30720\n" \
  "ds_read_b64_tr_b16 a[252:253], v19 offset:28672\n" \
  "ds_read_b64_tr_b16 a[254:255], v19 offset:30720\n" \
  "s_waitcnt lgkmcnt(0)\n" \
  "s_add_u32 m0, s86, 16384\n" \
  "s_nop 0\n" \
  "buffer_load_dwordx4 v7, s[68:71], s90 offen lds\n" \
  "s_add_u32 s90, s90, 16384\n" \
  "s_add_u32 m0, s86, 17408\n" \
  "s_nop 0\n" \
  "buffer_load_dwordx4 v7, s[68:71], s90 offen lds\n" \
  "s_add_u32 s90, s90, 16384\n" \
  "s_add_u32 m0, s86, 18432\n" \
  "s_nop 0\n" \
  "buffer_load_dwordx4 v7, s[68:71], s90 offen lds\n" \
  "s_add_u32 s90, s90, 16384\n" \
  "s_add_u32 m0, s86, 19456\n" \
  "s_nop 0\n" \
  "buffer_load_dwordx4 v7, s[68:71], s90 offen lds\n" \
  "s_add_u32 s90, s90, 16384\n" \
  "s_add_u32 m0, s86, 20480\n" \
  "s_nop 0\n" \
  "buffer_load_dwordx4 v7, s[68:71], s90 offen lds\n" \
  "s_add_u32 s90, s90, 16384\n" \
  "s_add_u32 m0, s86, 21504\n" \
  "s_nop 0\n" \
  "buffer_load_dwordx4 v7, s[68:71], s90 offen lds\n" \
  "s_add_u32 s90, s90, 16384\n" \
  "s_add_u32 m0, s86, 22528\n" \
  "s_nop 0\n" \
  "buffer_load_dwordx4 v7, s[68:71], s90 offen lds\n" \
  "s_add_u32 s90, s90, 16384\n" \
  "s_add_u32 m0, s86, 23552\n" \
  "s_nop 0\n" \
  "buffer_load_dwordx4 v7, s[68:71], s90 offen lds\n" \
  "s_add_u32 s90, s90, 16384\n" \
  "s_add_u32 m0, s86, 24576\n" \
  "s_nop 0\n" \
  "buffer_load_dwordx4 v7, s[68:71], s90 offen lds\n" \
  "s_add_u32 s90, s90, 16384\n" \
  "s_add_u32 m0, s86, 25600\n" \
  "s_nop 0\n" \
  "buffer_load_dwordx4 v7, s[68:71], s90 offen lds\n" \
  "s_add_u32 s90, s90, 16384\n" \
  "s_add_u32 m0, s86, 26624\n" \
  "s_nop 0\n" \
  "buffer_load_dwordx4 v7, s[68:71], s90 offen lds\n" \
  "s_add_u32 s90, s90, 16384\n" \
  "s_add_u32 m0, s86, 27648\n" \
  "s_nop 0\n" \
  "buffer_load_dwordx4 v7, s[68:71], s90 offen lds\n" \
  "s_add_u32 s90, s90, 16384\n" \
  "s_add_u32 m0, s86, 28672\n" \
  "s_nop 0\n" \
  "buffer_load_dwordx4 v7, s[68:71], s90 offen lds\n" \
  "s_add_u32 s90, s90, 16384\n" \
  "s_add_u32 m0, s86, 29696\n" \
  "s_nop 0\n" \
  "buffer_load_dwordx4 v7, s[68:71], s90 offen lds\n" \
  "s_add_u32 s90, s90, 16384\n" \
  "s_add_u32 m0, s86, 30720\n" \
  "s_nop 0\n" \
  "buffer_load_dwordx4 v7, s[68:71], s90 offen lds\n" \
  "s_add_u32 s90, s90, 16384\n" \
  "s_add_u32 m0, s86, 31744\n" \
  "s_nop 0\n" \
  "buffer_load_dwordx4 v7, s[68:71], s90 offen lds\n" \
  "s_add_u32 s90, s90, 16384\n" \
  "s_mov_b32 s92, 1\n" \
  "v_xor_b32 v7, 64, v12\n" \
  "LP0_%=:\n" \
  "buffer_load_dword v9, v8, s[80:83], 0 offen sc1\n" \
  "s_waitcnt vmcnt(0)\n" \
  "v_cmp_gt_u32 vcc, s92, v9\n" \
  "s_cbranch_vccz LP1_%=\n" \
  "s_sleep 8\n" \
  "s_add_u32 s91, s91, 1\n" \
  "s_cmp_lt_u32 s91, " STR(SPIN_CAP) "\n" \
  "s_cbranch_scc1 LP0_%=\n" \
  "v_mov_b32 v5, " STR(CW_DEAD) "*4\n" \
  "v_mov_b32 v15, 1\n" \
  "buffer_store_dword v15, v5, s[80:83], 0 offen sc1\n" \
  "s_branch LP1_%=\n" \
  "LP1_%=:\n" \
  "s_add_u32 s84, s87, 0\n" \
  "buffer_load_dwordx4 v[64:67], v6, s[72:75], s84 offen sc1\n" \
  "s_add_u32 s84, s87, 1024\n" \
  "buffer_load_dwordx4 v[68:71], v6, s[72:75], s84 offen sc1\n" \
  "s_add_u32 s84, s87, 2048\n" \
  "buffer_load_dwordx4 v[72:75], v6, s[72:75], s84 offen sc1\n" \
  "s_add_u32 s84, s87, 49152\n" \
  "buffer_load_dwordx4 v[76:79], v6, s[72:75], s84 offen sc1\n" \
  "s_add_u32 s84, s87, 3072\n" \
  "buffer_load_dwordx4 v[80:83], v6, s[72:75], s84 offen sc1\n" \
  "s_add_u32 s84, s87, 4096\n" \
  "buffer_load_dwordx4 v[84:87], v6, s[72:75], s84 offen sc1\n" \
  "s_add_u32 s84, s87, 5120\n" \
  "buffer_load_dwordx4 v[88:91], v6, s[72:75], s84 offen sc1\n" \
  "s_add_u32 s84, s87, 50176\n" \
  "buffer_load_dwordx4 v[92:95], v6, s[72:75], s84 offen sc1\n" \
  "s_add_u32 s84, s87, 6144\n" \
  "buffer_load_dwordx4 v[96:99], v6, s[72:75], s84 offen sc1\n" \
  "s_add_u32 s84, s87, 7168\n" \
  "buffer_load_dwordx4 v[100:103], v6, s[72:75], s84 offen sc1\n" \
  "s_add_u32 s84, s87, 16384\n" \
  "buffer_load_dwordx4 v[104:107], v6, s[72:75], s84 offen sc1\n" \
  "s_add_u32 s84, s87, 51200\n" \
  "buffer_load_dwordx4 v[108:111], v6, s[72:75], s84 offen sc1\n" \
  "s_add_u32 s84, s87, 17408\n" \
  "buffer_load_dwordx4 v[112:115], v6, s[72:75], s84 offen sc1\n" \
  "s_add_u32 s84, s87, 18432\n" \
  "buffer_load_dwordx4 v[116:119], v6, s[72:75], s84 offen sc1\n" \
  "s_add_u32 s84, s87, 19456\n" \
  "buffer_load_dwordx4 v[120:123], v6, s[72:75], s84 offen sc1\n" \
  "s_add_u32 s84, s87, 52224\n" \
  "buffer_load_dwordx4 v[124:127], v6, s[72:75], s84 offen sc1\n" \
  "v_or_b32 v10, 0x80000000, v10\n" \
  "s_sub_u32 s88, s88, 131072\n" \
  ".p2align 6\n" \
  "LX_%=:\n" \
  "s_waitcnt vmcnt(15)\n" \
  "v_mfma_f32_16x16x32_bf16 v[20:23], v[128:131], v[64:67], 0\n" \
  "v_mfma_f32_16x16x32_bf16 v[24:27], v[132:135], v[64:67], 0\n" \
  "v_mfma_f32_16x16x32_bf16 v[28:31], v[136:139], v[64:67], 0\n" \
  "v_mfma_f32_16x16x32_bf16 v[32:35], v[140:143], v[64:67], 0\n" \
  "s_add_u32 s84, s87, 20480\n" \
  "buffer_load_dwordx4 v[64:67], v6, s[72:75], s84 offen sc1\n" \
  "s_waitcnt vmcnt(15)\n" \
  "v_mfma_f32_16x16x32_bf16 v[20:23], v[144:147], v[68:71], v[20:23]\n" \
  "v_mfma_f32_16x16x32_bf16 v[24:27], v[148:151], v[68:71], v[24:27]\n" \
  "v_mfma_f32_16x16x32_bf16 v[28:31], v[152:155], v[68:71], v[28:31]\n" \
  "v_mfma_f32_16x16x32_bf16 v[32:35], v[156:159], v[68:71], v[32:35]\n" \
  "s_add_u32 s84, s87, 21504\n" \
  "buffer_load_dwordx4 v[68:71], v6, s[72:75], s84 offen sc1\n" \
  "s_waitcnt vmcnt(15)\n" \
  "v_mfma_f32_16x16x32_bf16 v[20:23], v[160:163], v[72:75], v[20:23]\n" \
  "ds_read_b64_tr_b16 v[40:41], v16 offset:0\n" \
  "ds_read_b64_tr_b16 v[42:43], v16 offset:2048\n" \
  "v_mfma_f32_16x16x32_bf16 v[24:27], v[164:167], v[72:75], v[24:27]\n" \
  "ds_read_b64_tr_b16 v[44:45], v17 offset:0\n" \
  "ds_read_b64_tr_b16 v[46:47], v17 offset:2048\n" \
  "v_mfma_f32_16x16x32_bf16 v[28:31], v[168:171], v[72:75], v[28:31]\n" \
  "ds_read_b64_tr_b16 v[48:49], v18 offset:0\n" \
  "ds_read_b64_tr_b16 v[50:51], v18 offset:2048\n" \
  "v_mfma_f32_16x16x32_bf16 v[32:35], v[172:175], v[72:75], v[32:35]\n" \
  "ds_read_b64_tr_b16 v[52:53], v19 offset:0\n" \
  "ds_read_b64_tr_b16 v[54:55], v19 offset:2048\n" \
  "s_add_u32 s84, s87, 22528\n" \
  "buffer_load_dwordx4 v[72:75], v6, s[72:75], s84 offen sc1\n" \
  "s_waitcnt vmcnt(15)\n" \
  "s_waitcnt lgkmcnt(6)\n" \
  "v_mfma_f32_16x16x32_bf16 v[20:23], v[40:43], v[76:79], v[20:23]\n" \
  "s_waitcnt lgkmcnt(4)\n" \
  "v_mfma_f32_16x16x32_bf16 v[24:27], v[44:47], v[76:79], v[24:27]\n" \
  "s_waitcnt lgkmcnt(2)\n" \
  "v_mfma_f32_16x16x32_bf16 v[28:31], v[48:51], v[76:79], v[28:31]\n" \
  "s_waitcnt lgkmcnt(0)\n" \
  "v_mfma_f32_16x16x32_bf16 v[32:35], v[52:55], v[76:79], v[32:35]\n" \
  "s_add_u32 s84, s87, 53248\n" \
  "buffer_load_dwordx4 v[76:79], v6, s[72:75], s84 offen sc1\n" \
  "s_waitcnt lgkmcnt(0)\n" \
  "s_barrier\n" \
  "ds_read_b128 v[52:55], v11 offset:0\n" \
  "ds_read_b128 v[56:59], v11 offset:4096\n" \
  "ds_read_b128 v[60:63], v11 offset:8192\n" \
  "ds_read_b128 v[36:39], v11 offset:12288\n" \
  "s_waitcnt vmcnt(15)\n" \
  "v_mfma_f32_16x16x32_bf16 v[20:23], v[176:179], v[80:83], v[20:23]\n" \
  "v_mfma_f32_16x16x32_bf16 v[24:27], v[180:183], v[80:83], v[24:27]\n" \
  "v_mfma_f32_16x16x32_bf16 v[28:31], v[184:187], v[80:83], v[28:31]\n" \
  "v_mfma_f32_16x16x32_bf16 v[32:35], v[188:191], v[80:83], v[32:35]\n" \
  "s_add_u32 s84, s87, 23552\n" \
  "buffer_load_dwordx4 v[80:83], v6, s[72:75], s84 offen sc1\n" \
  "s_waitcnt lgkmcnt(0)\n" \
  "s_waitcnt vmcnt(15)\n" \
  "v_mfma_f32_16x16x32_bf16 v[20:23], v[192:195], v[84:87], v[20:23]\n" \
  "v_add_f32 v36, v36, v52\n" \
  "v_add_f32 v37, v37, v53\n" \
  "v_add_f32 v38, v38, v54\n" \
  "v_add_f32 v39, v39, v55\n" \
  "v_mfma_f32_16x16x32_bf16 v[24:27], v[196:199], v[84:87], v[24:27]\n" \
  "v_add_f32 v56, v56, v60\n" \
  "v_add_f32 v57, v57, v61\n" \
  "v_add_f32 v58, v58, v62\n" \
  "v_add_f32 v59, v59, v63\n" \
  "v_mfma_f32_16x16x32_bf16 v[28:31], v[200:203], v[84:87], v[28:31]\n" \
  "v_add_f32 v36, v36, v56\n" \
  "v_add_f32 v37, v37, v57\n" \
  "v_add_f32 v38, v38, v58\n" \
  "v_add_f32 v39, v39, v59\n" \
  "v_mfma_f32_16x16x32_bf16 v[32:35], v[204:207], v[84:87], v[32:35]\n" \
  "v_cvt_pk_bf16_f32 v36, v36, v37\n" \
  "v_cvt_pk_bf16_f32 v37, v38, v39\n" \
  "buffer_store_dwordx2 v[36:37], v10, s[76:79], s88 offen sc1\n" \
  "s_add_u32 s88, s88, 131072\n" \
  "v_and_b32 v10, 0x7fffffff, v10\n" \
  "s_add_u32 s84, s87, 32768\n" \
  "buffer_load_dwordx4 v[84:87], v6, s[72:75], s84 offen sc1\n" \
  "s_waitcnt vmcnt(16)\n" \
  "v_mfma_f32_16x16x32_bf16 v[20:23], v[208:211], v[88:91], v[20:23]\n" \
  "ds_read_b64_tr_b16 v[40:41], v16 offset:4096\n" \
  "ds_read_b64_tr_b16 v[42:43], v16 offset:6144\n" \
  "v_mfma_f32_16x16x32_bf16 v[24:27], v[212:215], v[88:91], v[24:27]\n" \
  "ds_read_b64_tr_b16 v[44:45], v17 offset:4096\n" \
  "ds_read_b64_tr_b16 v[46:47], v17 offset:6144\n" \
  "v_mfma_f32_16x16x32_bf16 v[28:31], v[216:219], v[88:91], v[28:31]\n" \
  "ds_read_b64_tr_b16 v[48:49], v18 offset:4096\n" \
  "ds_read_b64_tr_b16 v[50:51], v18 offset:6144\n" \
  "v_mfma_f32_16x16x32_bf16 v[32:35], v[220:223], v[88:91], v[32:35]\n" \
  "ds_read_b64_tr_b16 v[52:53], v19 offset:4096\n" \
  "ds_read_b64_tr_b16 v[54:55], v19 offset:6144\n" \
  "s_add_u32 s84, s87, 33792\n" \
  "buffer_load_dwordx4 v[88:91], v6, s[72:75], s84 offen sc1\n" \
  "s_waitcnt vmcnt(16)\n" \
  "s_waitcnt lgkmcnt(6)\n" \
  "v_mfma_f32_16x16x32_bf16 v[20:23], v[40:43], v[92:95], v[20:23]\n" \
  "s_waitcnt lgkmcnt(4)\n" \
  "v_mfma_f32_16x16x32_bf16 v[24:27], v[44:47], v[92:95], v[24:27]\n" \
  "s_waitcnt lgkmcnt(2)\n" \
  "v_mfma_f32_16x16x32_bf16 v[28:31], v[48:51], v[92:95], v[28:31]\n" \
  "s_waitcnt lgkmcnt(0)\n" \
  "v_mfma_f32_16x16x32_bf16 v[32:35], v[52:55], v[92:95], v[32:35]\n" \
  "s_add_u32 s84, s87, 54272\n" \
  "buffer_load_dwordx4 v[92:95], v6, s[72:75], s84 offen sc1\n" \
  "s_waitcnt vmcnt(16)\n" \
  "v_mfma_f32_16x16x32_bf16 v[20:23], v[224:227], v[96:99], v[20:23]\n" \
  "buffer_load_dword v9, v8, s[80:83], 0 offen sc1\n" \
  "v_mfma_f32_16x16x32_bf16 v[24:27], v[228:231], v[96:99], v[24:27]\n" \
  "v_xor_b32 v11, 0x4000, v11\n" \
  "v_xor_b32 v7, 0x4000, v7\n" \
  "v_mfma_f32_16x16x32_bf16 v[28:31], v[232:235], v[96:99], v[28:31]\n" \
  "v_xor_b32 v12, 0x4000, v12\n" \
  "v_xor_b32 v13, 0x4000, v13\n" \
  "v_xor_b32 v14, 0x4000, v14\n" \
  "v_mfma_f32_16x16x32_bf16 v[32:35], v[236:239], v[96:99], v[32:35]\n" \
  "s_add_u32 s84, s87, 34816\n" \
  "buffer_load_dwordx4 v[96:99], v6, s[72:75], s84 offen sc1\n" \
  "s_waitcnt vmcnt(17)\n" \
  "v_mfma_f32_16x16x32_bf16 v[20:23], v[240:243], v[100:103], v[20:23]\n" \
  "v_mfma_f32_16x16x32_bf16 v[24:27], v[244:247], v[100:103], v[24:27]\n" \
  "v_mfma_f32_16x16x32_bf16 v[28:31], v[248:251], v[100:103], v[28:31]\n" \
  "v_mfma_f32_16x16x32_bf16 v[32:35], v[252:255], v[100:103], v[32:35]\n" \
  "s_add_u32 s84, s87, 35840\n" \
  "buffer_load_dwordx4 v[100:103], v6, s[72:75], s84 offen sc1\n" \
  "s_waitcnt vmcnt(17)\n" \
  "v_mfma_f32_16x16x32_bf16 v[20:23], a[0:3], v[104:107], v[20:23]\n" \
  "ds_read_b64_tr_b16 v[40:41], v16 offset:8192\n" \
  "ds_read_b64_tr_b16 v[42:43], v16 offset:10240\n" \
  "v_mfma_f32_16x16x32_bf16 v[24:27], a[4:7], v[104:107], v[24:27]\n" \
  "ds_read_b64_tr_b16 v[44:45], v17 offset:8192\n" \
  "ds_read_b64_tr_b16 v[46:47], v17 offset:10240\n" \
  "v_mfma_f32_16x16x32_bf16 v[28:31], a[8:11], v[104:107], v[28:31]\n" \
  "ds_read_b64_tr_b16 v[48:49], v18 offset:8192\n" \
  "ds_read_b64_tr_b16 v[50:51], v18 offset:10240\n" \
  "v_mfma_f32_16x16x32_bf16 v[32:35], a[12:15], v[104:107], v[32:35]\n" \
  "ds_read_b64_tr_b16 v[52:53], v19 offset:8192\n" \
  "ds_read_b64_tr_b16 v[54:55], v19 offset:10240\n" \
  "s_add_u32 s84, s87, 36864\n" \
  "buffer_load_dwordx4 v[104:107], v6, s[72:75], s84 offen sc1\n" \
  "s_waitcnt vmcnt(17)\n" \
  "s_waitcnt lgkmcnt(6)\n" \
  "v_mfma_f32_16x16x32_bf16 v[20:23], v[40:43], v[108:111], v[20:23]\n" \
  "s_waitcnt lgkmcnt(4)\n" \
  "v_mfma_f32_16x16x32_bf16 v[24:27], v[44:47], v[108:111], v[24:27]\n" \
  "s_waitcnt lgkmcnt(2)\n" \
  "v_mfma_f32_16x16x32_bf16 v[28:31], v[48:51], v[108:111], v[28:31]\n" \
  "s_waitcnt lgkmcnt(0)\n" \
  "v_mfma_f32_16x16x32_bf16 v[32:35], v[52:55], v[108:111], v[32:35]\n" \
  "s_add_u32 s84, s87, 55296\n" \
  "buffer_load_dwordx4 v[108:111], v6, s[72:75], s84 offen sc1\n" \
  "s_waitcnt vmcnt(17)\n" \
  "v_mfma_f32_16x16x32_bf16 v[20:23], a[16:19], v[112:115], v[20:23]\n" \
  "v_mfma_f32_16x16x32_bf16 v[24:27], a[20:23], v[112:115], v[24:27]\n" \
  "v_mfma_f32_16x16x32_bf16 v[28:31], a[24:27], v[112:115], v[28:31]\n" \
  "v_mfma_f32_16x16x32_bf16 v[32:35], a[28:31], v[112:115], v[32:35]\n" \
  "s_add_u32 s84, s87, 37888\n" \
  "buffer_load_dwordx4 v[112:115], v6, s[72:75], s84 offen sc1\n" \
  "s_waitcnt vmcnt(17)\n" \
  "v_mfma_f32_16x16x32_bf16 v[20:23], a[32:35], v[116:119], v[20:23]\n" \
  "v_mfma_f32_16x16x32_bf16 v[24:27], a[36:39], v[116:119], v[24:27]\n" \
  "v_mfma_f32_16x16x32_bf16 v[28:31], a[40:43], v[116:119], v[28:31]\n" \
  "v_mfma_f32_16x16x32_bf16 v[32:35], a[44:47], v[116:119], v[32:35]\n" \
  "s_add_u32 s84, s87, 38912\n" \
  "buffer_load_dwordx4 v[116:119], v6, s[72:75], s84 offen sc1\n" \
  "s_waitcnt vmcnt(17)\n" \
  "v_mfma_f32_16x16x32_bf16 v[20:23], a[48:51], v[120:123], v[20:23]\n" \
  "ds_read_b64_tr_b16 v[40:41], v16 offset:12288\n" \
  "ds_read_b64_tr_b16 v[42:43], v16 offset:14336\n" \
  "v_mfma_f32_16x16x32_bf16 v[24:27], a[52:55], v[120:123], v[24:27]\n" \
  "ds_read_b64_tr_b16 v[44:45], v17 offset:12288\n" \
  "ds_read_b64_tr_b16 v[46:47], v17 offset:14336\n" \
  "v_mfma_f32_16x16x32_bf16 v[28:31], a[56:59], v[120:123], v[28:31]\n" \
  "ds_read_b64_tr_b16 v[48:49], v18 offset:12288\n" \
  "ds_read_b64_tr_b16 v[50:51], v18 offset:14336\n" \
  "v_mfma_f32_16x16x32_bf16 v[32:35], a[60:63], v[120:123], v[32:35]\n" \
  "ds_read_b64_tr_b16 v[52:53], v19 offset:12288\n" \
  "ds_read_b64_tr_b16 v[54:55], v19 offset:14336\n" \
  "s_add_u32 s84, s87, 39936\n" \
  "buffer_load_dwordx4 v[120:123], v6, s[72:75], s84 offen sc1\n" \
  "s_waitcnt vmcnt(17)\n" \
  "s_waitcnt lgkmcnt(6)\n" \
  "v_mfma_f32_16x16x32_bf16 v[20:23], v[40:43], v[124:127], v[20:23]\n" \
  "s_waitcnt lgkmcnt(4)\n" \
  "v_mfma_f32_16x16x32_bf16 v[24:27], v[44:47], v[124:127], v[24:27]\n" \
  "s_waitcnt lgkmcnt(2)\n" \
  "v_mfma_f32_16x16x32_bf16 v[28:31], v[48:51], v[124:127], v[28:31]\n" \
  "s_waitcnt lgkmcnt(0)\n" \
  "v_mfma_f32_16x16x32_bf16 v[32:35], v[52:55], v[124:127], v[32:35]\n" \
  "s_add_u32 s84, s87, 56320\n" \
  "buffer_load_dwordx4 v[124:127], v6, s[72:75], s84 offen sc1\n" \
  "s_waitcnt vmcnt(8)\n" \
  "s_add_u32 s92, s89, 2\n" \
  "s_min_u32 s92, s92, 64\n" \
  "v_cmp_gt_u32 vcc, s92, v9\n" \
  "s_sub_u32 s85, s92, 1\n" \
  "s_lshr_b32 s84, s85, 2\n" \
  "s_xnor_b32 s84, s84, %7\n" \
  "s_and_b32 s84, s84, 1\n" \
  "s_lshl_b32 s84, s84, 13\n" \
  "s_lshl_b32 s87, s85, 19\n" \
  "s_or_b32 s87, s87, s84\n" \
  "s_add_u32 s87, s87, s96\n" \
  "s_cbranch_vccnz LP2_%=\n" \
  "LP3_%=:\n" \
  "s_waitcnt vmcnt(17)\n" \
  "v_mfma_f32_16x16x32_bf16 v[20:23], a[64:67], v[64:67], v[20:23]\n" \
  "v_mfma_f32_16x16x32_bf16 v[24:27], a[68:71], v[64:67], v[24:27]\n" \
  "v_mfma_f32_16x16x32_bf16 v[28:31], a[72:75], v[64:67], v[28:31]\n" \
  "v_mfma_f32_16x16x32_bf16 v[32:35], a[76:79], v[64:67], v[32:35]\n" \
  "s_add_u32 s84, s87, 0\n" \
  "buffer_load_dwordx4 v[64:67], v6, s[72:75], s84 offen sc1\n" \
  "s_waitcnt vmcnt(17)\n" \
  "v_mfma_f32_16x16x32_bf16 v[20:23], a[80:83], v[68:71], v[20:23]\n" \
  "v_mfma_f32_16x16x32_bf16 v[24:27], a[84:87], v[68:71], v[24:27]\n" \
  "v_mfma_f32_16x16x32_bf16 v[28:31], a[88:91], v[68:71], v[28:31]\n" \
  "v_mfma_f32_16x16x32_bf16 v[32:35], a[92:95], v[68:71], v[32:35]\n" \
  "s_add_u32 s84, s87, 1024\n" \
  "buffer_load_dwordx4 v[68:71], v6, s[72:75], s84 offen sc1\n" \
  "s_waitcnt vmcnt(17)\n" \
  "v_mfma_f32_16x16x32_bf16 v[20:23], a[96:99], v[72:75], v[20:23]\n" \
  "ds_read_b64_tr_b16 v[40:41], v16 offset:16384\n" \
  "ds_read_b64_tr_b16 v[42:43], v16 offset:18432\n" \
  "v_mfma_f32_16x16x32_bf16 v[24:27], a[100:103], v[72:75], v[24:27]\n" \
  "ds_read_b64_tr_b16 v[44:45], v17 offset:16384\n" \
  "ds_read_b64_tr_b16 v[46:47], v17 offset:18432\n" \
  "v_mfma_f32_16x16x32_bf16 v[28:31], a[104:107], v[72:75], v[28:31]\n" \
  "ds_read_b64_tr_b16 v[48:49], v18 offset:16384\n" \
  "ds_read_b64_tr_b16 v[50:51], v18 offset:18432\n" \
  "v_mfma_f32_16x16x32_bf16 v[32:35], a[108:111], v[72:75], v[32:35]\n" \
  "ds_read_b64_tr_b16 v[52:53], v19 offset:16384\n" \
  "ds_read_b64_tr_b16 v[54:55], v19 offset:18432\n" \
  "s_add_u32 s84, s87, 2048\n" \
  "buffer_load_dwordx4 v[72:75], v6, s[72:75], s84 offen sc1\n" \
  "s_waitcnt vmcnt(17)\n" \
  "s_waitcnt lgkmcnt(6)\n" \
  "v_mfma_f32_16x16x32_bf16 v[20:23], v[40:43], v[76:79], v[20:23]\n" \
  "s_waitcnt lgkmcnt(4)\n" \
  "v_mfma_f32_16x16x32_bf16 v[24:27], v[44:47], v[76:79], v[24:27]\n" \
  "s_waitcnt lgkmcnt(2)\n" \
  "v_mfma_f32_16x16x32_bf16 v[28:31], v[48:51], v[76:79], v[28:31]\n" \
  "s_waitcnt lgkmcnt(0)\n" \
  "v_mfma_f32_16x16x32_bf16 v[32:35], v[52:55], v[76:79], v[32:35]\n" \
  "s_add_u32 s84, s87, 49152\n" \
  "buffer_load_dwordx4 v[76:79], v6, s[72:75], s84 offen sc1\n" \
  "s_waitcnt vmcnt(17)\n" \
  "v_mfma_f32_16x16x32_bf16 v[20:23], a[112:115], v[80:83], v[20:23]\n" \
  "v_mfma_f32_16x16x32_bf16 v[24:27], a[116:119], v[80:83], v[24:27]\n" \
  "v_mfma_f32_16x16x32_bf16 v[28:31], a[120:123], v[80:83], v[28:31]\n" \
  "v_mfma_f32_16x16x32_bf16 v[32:35], a[124:127], v[80:83], v[32:35]\n" \
  "s_add_u32 s84, s87, 3072\n" \
  "buffer_load_dwordx4 v[80:83], v6, s[72:75], s84 offen sc1\n" \
  "s_waitcnt vmcnt(16)\n" \
  "v_mfma_f32_16x16x32_bf16 v[20:23], a[128:131], v[84:87], v[20:23]\n" \
  "v_mfma_f32_16x16x32_bf16 v[24:27], a[132:135], v[84:87], v[24:27]\n" \
  "v_mfma_f32_16x16x32_bf16 v[28:31], a[136:139], v[84:87], v[28:31]\n" \
  "v_mfma_f32_16x16x32_bf16 v[32:35], a[140:143], v[84:87], v[32:35]\n" \
  "s_add_u32 s84, s87, 4096\n" \
  "buffer_load_dwordx4 v[84:87], v6, s[72:75], s84 offen sc1\n" \
  "s_waitcnt vmcnt(16)\n" \
  "v_mfma_f32_16x16x32_bf16 v[20:23], a[144:147], v[88:91], v[20:23]\n" \
  "ds_read_b64_tr_b16 v[40:41], v16 offset:20480\n" \
  "ds_read_b64_tr_b16 v[42:43], v16 offset:22528\n" \
  "v_mfma_f32_16x16x32_bf16 v[24:27], a[148:151], v[88:91], v[24:27]\n" \
  "ds_read_b64_tr_b16 v[44:45], v17 offset:20480\n" \
  "ds_read_b64_tr_b16 v[46:47], v17 offset:22528\n" \
  "v_mfma_f32_16x16x32_bf16 v[28:31], a[152:155], v[88:91], v[28:31]\n" \
  "ds_read_b64_tr_b16 v[48:49], v18 offset:20480\n" \
  "ds_read_b64_tr_b16 v[50:51], v18 offset:22528\n" \
  "v_mfma_f32_16x16x32_bf16 v[32:35], a[156:159], v[88:91], v[32:35]\n" \
  "ds_read_b64_tr_b16 v[52:53], v19 offset:20480\n" \
  "ds_read_b64_tr_b16 v[54:55], v19 offset:22528\n" \
  "s_add_u32 s84, s87, 5120\n" \
  "buffer_load_dwordx4 v[88:91], v6, s[72:75], s84 offen sc1\n" \
  "s_waitcnt vmcnt(16)\n" \
  "s_waitcnt lgkmcnt(6)\n" \
  "v_mfma_f32_16x16x32_bf16 v[20:23], v[40:43], v[92:95], v[20:23]\n" \
  "s_waitcnt lgkmcnt(4)\n" \
  "v_mfma_f32_16x16x32_bf16 v[24:27], v[44:47], v[92:95], v[24:27]\n" \
  "s_waitcnt lgkmcnt(2)\n" \
  "v_mfma_f32_16x16x32_bf16 v[28:31], v[48:51], v[92:95], v[28:31]\n" \
  "s_waitcnt lgkmcnt(0)\n" \
  "v_mfma_f32_16x16x32_bf16 v[32:35], v[52:55], v[92:95], v[32:35]\n" \
  "s_add_u32 s84, s87, 50176\n" \
  "buffer_load_dwordx4 v[92:95], v6, s[72:75], s84 offen sc1\n" \
  "s_waitcnt vmcnt(15)\n" \
  "v_mfma_f32_16x16x32_bf16 v[20:23], a[160:163], v[96:99], v[20:23]\n" \
  "v_mfma_f32_16x16x32_bf16 v[24:27], a[164:167], v[96:99], v[24:27]\n" \
  "v_mfma_f32_16x16x32_bf16 v[28:31], a[168:171], v[96:99], v[28:31]\n" \
  "v_mfma_f32_16x16x32_bf16 v[32:35], a[172:175], v[96:99], v[32:35]\n" \
  "s_add_u32 s84, s87, 6144\n" \
  "buffer_load_dwordx4 v[96:99], v6, s[72:75], s84 offen sc1\n" \
  "s_waitcnt vmcnt(15)\n" \
  "v_mfma_f32_16x16x32_bf16 v[20:23], a[176:179], v[100:103], v[20:23]\n" \
  "v_mfma_f32_16x16x32_bf16 v[24:27], a[180:183], v[100:103], v[24:27]\n" \
  "v_mfma_f32_16x16x32_bf16 v[28:31], a[184:187], v[100:103], v[28:31]\n" \
  "v_mfma_f32_16x16x32_bf16 v[32:35], a[188:191], v[100:103], v[32:35]\n" \
  "s_add_u32 s84, s87, 7168\n" \
  "buffer_load_dwordx4 v[100:103], v6, s[72:75], s84 offen sc1\n" \
  "s_waitcnt vmcnt(15)\n" \
  "v_mfma_f32_16x16x32_bf16 v[20:23], a[192:195], v[104:107], v[20:23]\n" \
  "ds_read_b64_tr_b16 v[40:41], v16 offset:24576\n" \
  "ds_read_b64_tr_b16 v[42:43], v16 offset:26624\n" \
  "v_mfma_f32_16x16x32_bf16 v[24:27], a[196:199], v[104:107], v[24:27]\n" \
  "ds_read_b64_tr_b16 v[44:45], v17 offset:24576\n" \
  "ds_read_b64_tr_b16 v[46:47], v17 offset:26624\n" \
  "v_mfma_f32_16x16x32_bf16 v[28:31], a[200:203], v[104:107], v[28:31]\n" \
  "ds_read_b64_tr_b16 v[48:49], v18 offset:24576\n" \
  "ds_read_b64_tr_b16 v[50:51], v18 offset:26624\n" \
  "v_mfma_f32_16x16x32_bf16 v[32:35], a[204:207], v[104:107], v[32:35]\n" \
  "ds_read_b64_tr_b16 v[52:53], v19 offset:24576\n" \
  "ds_read_b64_tr_b16 v[54:55], v19 offset:26624\n" \
  "s_add_u32 s84, s87, 16384\n" \
  "buffer_load_dwordx4 v[104:107], v6, s[72:75], s84 offen sc1\n" \
  "s_waitcnt vmcnt(15)\n" \
  "s_waitcnt lgkmcnt(6)\n" \
  "v_mfma_f32_16x16x32_bf16 v[20:23], v[40:43], v[108:111], v[20:23]\n" \
  "s_waitcnt lgkmcnt(4)\n" \
  "v_mfma_f32_16x16x32_bf16 v[24:27], v[44:47], v[108:111], v[24:27]\n" \
  "s_waitcnt lgkmcnt(2)\n" \
  "v_mfma_f32_16x16x32_bf16 v[28:31], v[48:51], v[108:111], v[28:31]\n" \
  "s_waitcnt lgkmcnt(0)\n" \
  "v_mfma_f32_16x16x32_bf16 v[32:35], v[52:55], v[108:111], v[32:35]\n" \
  "s_add_u32 s84, s87, 51200\n" \
  "buffer_load_dwordx4 v[108:111], v6, s[72:75], s84 offen sc1\n" \
  "s_waitcnt vmcnt(15)\n" \
  "v_mfma_f32_16x16x32_bf16 v[20:23], a[208:211], v[112:115], v[20:23]\n" \
  "v_mfma_f32_16x16x32_bf16 v[24:27], a[212:215], v[112:115], v[24:27]\n" \
  "v_mfma_f32_16x16x32_bf16 v[28:31], a[216:219], v[112:115], v[28:31]\n" \
  "v_mfma_f32_16x16x32_bf16 v[32:35], a[220:223], v[112:115], v[32:35]\n" \
  "s_add_u32 s84, s87, 17408\n" \
  "buffer_load_dwordx4 v[112:115], v6, s[72:75], s84 offen sc1\n" \
  "s_waitcnt vmcnt(15)\n" \
  "v_mfma_f32_16x16x32_bf16 v[20:23], a[224:227], v[116:119], v[20:23]\n" \
  "v_mfma_f32_16x16x32_bf16 v[24:27], a[228:231], v[116:119], v[24:27]\n" \
  "v_mfma_f32_16x16x32_bf16 v[28:31], a[232:235], v[116:119], v[28:31]\n" \
  "v_mfma_f32_16x16x32_bf16 v[32:35], a[236:239], v[116:119], v[32:35]\n" \
  "s_add_u32 s84, s87, 18432\n" \
  "buffer_load_dwordx4 v[116:119], v6, s[72:75], s84 offen sc1\n" \
  "s_waitcnt vmcnt(15)\n" \
  "v_mfma_f32_16x16x32_bf16 v[20:23], a[240:243], v[120:123], v[20:23]\n" \
  "ds_read_b64_tr_b16 v[40:41], v16 offset:28672\n" \
  "ds_read_b64_tr_b16 v[42:43], v16 offset:30720\n" \
  "v_mfma_f32_16x16x32_bf16 v[24:27], a[244:247], v[120:123], v[24:27]\n" \
  "ds_read_b64_tr_b16 v[44:45], v17 offset:28672\n" \
  "ds_read_b64_tr_b16 v[46:47], v17 offset:30720\n" \
  "v_mfma_f32_16x16x32_bf16 v[28:31], a[248:251], v[120:123], v[28:31]\n" \
  "ds_read_b64_tr_b16 v[48:49], v18 offset:28672\n" \
  "ds_read_b64_tr_b16 v[50:51], v18 offset:30720\n" \
  "v_mfma_f32_16x16x32_bf16 v[32:35], a[252:255], v[120:123], v[32:35]\n" \
  "ds_read_b64_tr_b16 v[52:53], v19 offset:28672\n" \
  "ds_read_b64_tr_b16 v[54:55], v19 offset:30720\n" \
  "s_add_u32 s84, s87, 19456\n" \
  "buffer_load_dwordx4 v[120:123], v6, s[72:75], s84 offen sc1\n" \
  "s_waitcnt vmcnt(15)\n" \
  "s_waitcnt lgkmcnt(6)\n" \
  "v_mfma_f32_16x16x32_bf16 v[20:23], v[40:43], v[124:127], v[20:23]\n" \
  "s_waitcnt lgkmcnt(4)\n" \
  "v_mfma_f32_16x16x32_bf16 v[24:27], v[44:47], v[124:127], v[24:27]\n" \
  "s_waitcnt lgkmcnt(2)\n" \
  "v_mfma_f32_16x16x32_bf16 v[28:31], v[48:51], v[124:127], v[28:31]\n" \
  "s_waitcnt lgkmcnt(0)\n" \
  "v_mfma_f32_16x16x32_bf16 v[32:35], v[52:55], v[124:127], v[32:35]\n" \
  "s_add_u32 s84, s87, 52224\n" \
  "buffer_load_dwordx4 v[124:127], v6, s[72:75], s84 offen sc1\n" \
  "s_add_u32 s89, s89, 1\n" \
  "s_cmp_eq_u32 s89, 64\n" \
  "ds_write_b128 v7, v[20:23]\n" \
  "ds_write_b128 v12, v[24:27]\n" \
  "ds_write_b128 v13, v[28:31]\n" \
  "s_nop 0\n" \
  "ds_write_b128 v14, v[32:35]\n" \
  "s_cbranch_scc0 LX_%=\n" \
  "s_waitcnt lgkmcnt(0)\n" \
  "s_barrier\n" \
  "ds_read_b128 v[52:55], v11 offset:0\n" \
  "ds_read_b128 v[56:59], v11 offset:4096\n" \
  "ds_read_b128 v[60:63], v11 offset:8192\n" \
  "ds_read_b128 v[36:39], v11 offset:12288\n" \
  "s_waitcnt lgkmcnt(0)\n" \
  "v_add_f32 v36, v36, v52\n" \
  "v_add_f32 v37, v37, v53\n" \
  "v_add_f32 v38, v38, v54\n" \
  "v_add_f32 v39, v39, v55\n" \
  "v_add_f32 v56, v56, v60\n" \
  "v_add_f32 v57, v57, v61\n" \
  "v_add_f32 v58, v58, v62\n" \
  "v_add_f32 v59, v59, v63\n" \
  "v_add_f32 v36, v36, v56\n" \
  "v_add_f32 v37, v37, v57\n" \
  "v_add_f32 v38, v38, v58\n" \
  "v_add_f32 v39, v39, v59\n" \
  "v_cvt_pk_bf16_f32 v36, v36, v37\n" \
  "v_cvt_pk_bf16_f32 v37, v38, v39\n" \
  "buffer_store_dwordx2 v[36:37], v10, s[76:79], s88 offen sc1\n" \
  "s_add_u32 s88, s88, 131072\n" \
  "v_and_b32 v10, 0x7fffffff, v10\n" \
  "s_waitcnt vmcnt(0)\n" \
  "s_branch LE_%=\n" \
  "LP2_%=:\n" \
  "buffer_load_dword v9, v8, s[80:83], 0 offen sc1\n" \
  "s_waitcnt vmcnt(0)\n" \
  "v_cmp_gt_u32 vcc, s92, v9\n" \
  "s_cbranch_vccz LP3_%=\n" \
  "s_sleep 2\n" \
  "s_add_u32 s91, s91, 1\n" \
  "s_cmp_lt_u32 s91, " STR(SPIN_CAP) "\n" \
  "s_cbranch_scc1 LP2_%=\n" \
  "v_mov_b32 v5, " STR(CW_DEAD) "*4\n" \
  "v_mov_b32 v15, 1\n" \
  "buffer_store_dword v15, v5, s[80:83], 0 offen sc1\n" \
  "s_branch LP3_%=\n" \
  "LE_%=:\n" \
  ""

#define XA_ASM \
  "s_mov_b64 s[68:69], %0\n" \
  "s_and_b32 s69, s69, 0xffff\n" \
  "s_mov_b32 s70, " STR(EX_BYTES32) "\n" \
  "s_mov_b32 s71, 0x00020000\n" \
  "s_mov_b64 s[72:73], %1\n" \
  "s_and_b32 s73, s73, 0xffff\n" \
  "s_mov_b32 s74, 4*" STR(QKV_SZ32) "\n" \
  "s_mov_b32 s75, 0x00020000\n" \
  "s_mov_b64 s[76:77], %2\n" \
  "s_and_b32 s77, s77, 0xffff\n" \
  "s_mov_b32 s78, " STR(CTRL_BYTES) "\n" \
  "s_mov_b32 s79, 0x00020000\n" \
  "v_mbcnt_lo_u32_b32 v4, -1, 0\n" \
  "v_mbcnt_hi_u32_b32 v4, -1, v4\n" \
  "v_and_b32 v116, 15, v4\n" \
  "v_lshrrev_b32 v117, 4, v4\n" \
  "v_lshlrev_b32 v7, 4, v4\n" \
  "v_add_u32 v8, 4096, v7\n" \
  "v_add_u32 v17, 0x40000, v7\n" \
  "s_and_b32 s80, %3, 1\n" \
  "s_lshl_b32 s80, s80, 11\n" \
  "s_add_u32 s80, s80, 32768\n" \
  "v_add_u32 v18, s80, v7\n" \
  "v_add_u32 v11, %5, v7\n" \
  "v_lshrrev_b32 v5, 2, v116\n" \
  "v_and_b32 v5, 1, v5\n" \
  "v_lshl_add_u32 v5, v117, 2, v5\n" \
  "v_and_b32 v6, 2, v4\n" \
  "v_add_lshl_u32 v5, v5, v6, 4\n" \
  "v_and_b32 v6, 1, v4\n" \
  "v_lshl_add_u32 v5, v6, 3, v5\n" \
  "v_add_u32 v12, %5, v5\n" \
  "s_lshl_b32 s80, %3, 7\n" \
  "s_add_u32 s81, s80, " STR(CW_XPROG) "*4\n" \
  "v_lshl_add_u32 v15, v4, 24, s81\n" \
  "s_add_u32 s81, s80, " STR(CW_PROG) "*4\n" \
  "v_mov_b32 v13, s81\n" \
  "s_lshr_b32 s80, %3, 1\n" \
  "s_lshl_b32 s80, s80, 9\n" \
  "s_add_u32 s80, s80, " STR(CW_GPROG) "*4\n" \
  "v_writelane_b32 v13, s80, 1\n" \
  "s_add_u32 s80, s80, 128\n" \
  "v_writelane_b32 v13, s80, 2\n" \
  "s_add_u32 s80, s80, 128\n" \
  "v_writelane_b32 v13, s80, 3\n" \
  "s_add_u32 s80, s80, 128\n" \
  "v_writelane_b32 v13, s80, 4\n" \
  "v_mov_b32 v16, 0\n" \
  "v_mov_b32 v6, 0xff800000\n" \
  "v_lshlrev_b32 v5, 2, v117\n" \
  "v_cmp_gt_u32 vcc, v5, v116\n" \
  "v_mov_b32 v24, 0x3f803f80\n" \
  "v_add_u32 v5, 1, v5\n" \
  "v_cndmask_b32 v20, 0, v6, vcc\n" \
  "v_cmp_gt_u32 vcc, v5, v116\n" \
  "v_mov_b32 v25, 0x3f803f80\n" \
  "v_add_u32 v5, 1, v5\n" \
  "v_cndmask_b32 v21, 0, v6, vcc\n" \
  "v_cmp_gt_u32 vcc, v5, v116\n" \
  "v_mov_b32 v26, 0x3f803f80\n" \
  "v_add_u32 v5, 1, v5\n" \
  "v_cndmask_b32 v22, 0, v6, vcc\n" \
  "v_cmp_gt_u32 vcc, v5, v116\n" \
  "v_mov_b32 v27, 0x3f803f80\n" \
  "v_add_u32 v5, 1, v5\n" \
  "v_cndmask_b32 v23, 0, v6, vcc\n" \
  "v_mov_b64 v[130:131], 0\n" \
  "v_mov_b32 v156, 0\n" \
  "v_mov_b32 v158, 0\n" \
  "s_mov_b32 s89, 0x3e0293ee\n" \
  "s_mov_b32 s88, 0\n" \
  "s_mov_b32 s82, 0\n" \
  "s_mov_b32 s90, 0\n" \
  "s_lshr_b32 s80, s82, 2\n" \
  "s_xnor_b32 s80, s80, %4\n" \
  "s_and_b32 s80, s80, 1\n" \
  "s_lshl_b32 s80, s80, 13\n" \
  "s_lshl_b32 s83, s82, 19\n" \
  "s_or_b32 s83, s83, s80\n" \
  "s_lshl_b32 s80, %3, 14\n" \
  "s_add_u32 s83, s83, s80\n" \
  "s_add_u32 s84, s83, " STR(QKV_SZ32) "\n" \
  "s_add_u32 s85, s84, " STR(QKV_SZ32) "\n" \
  "s_mov_b32 s81, 1\n" \
  "LP0_%=:\n" \
  "buffer_load_dword v14, v13, s[76:79], 0 offen sc1\n" \
  "s_waitcnt vmcnt(0)\n" \
  "v_readlane_b32 s91, v14, 0\n" \
  "v_readlane_b32 s92, v14, 1\n" \
  "v_readlane_b32 s80, v14, 2\n" \
  "s_min_u32 s92, s92, s80\n" \
  "v_readlane_b32 s80, v14, 3\n" \
  "s_min_u32 s92, s92, s80\n" \
  "v_readlane_b32 s80, v14, 4\n" \
  "s_min_u32 s92, s92, s80\n" \
  "s_cmp_ge_u32 s92, s81\n" \
  "s_cbranch_scc1 LP1_%=\n" \
  "s_sleep 16\n" \
  "s_add_u32 s88, s88, 1\n" \
  "s_cmp_lt_u32 s88, " STR(SPIN_CAP) "\n" \
  "s_cbranch_scc1 LP0_%=\n" \
  "v_mov_b32 v5, " STR(CW_DEAD) "*4\n" \
  "v_mov_b32 v6, 1\n" \
  "buffer_store_dword v6, v5, s[76:79], 0 offen sc1\n" \
  "s_branch LP1_%=\n" \
  "LP1_%=:\n" \
  "buffer_load_dwordx4 a[64:67], v7, s[72:75], s84 offen offset:0 sc1\n" \
  "buffer_load_dwordx4 a[68:71], v7, s[72:75], s84 offen offset:1024 sc1\n" \
  "buffer_load_dwordx4 a[72:75], v7, s[72:75], s84 offen offset:2048 sc1\n" \
  "buffer_load_dwordx4 a[76:79], v7, s[72:75], s84 offen offset:3072 sc1\n" \
  "buffer_load_dwordx4 a[80:83], v8, s[72:75], s84 offen offset:0 sc1\n" \
  "buffer_load_dwordx4 a[84:87], v8, s[72:75], s84 offen offset:1024 sc1\n" \
  "buffer_load_dwordx4 a[88:91], v8, s[72:75], s84 offen offset:2048 sc1\n" \
  "buffer_load_dwordx4 a[92:95], v8, s[72:75], s84 offen offset:3072 sc1\n" \
  "buffer_load_dwordx4 a[0:3], v7, s[72:75], s83 offen offset:0 sc1\n" \
  "buffer_load_dwordx4 a[4:7], v7, s[72:75], s83 offen offset:1024 sc1\n" \
  "buffer_load_dwordx4 a[8:11], v7, s[72:75], s83 offen offset:2048 sc1\n" \
  "buffer_load_dwordx4 a[12:15], v7, s[72:75], s83 offen offset:3072 sc1\n" \
  "buffer_load_dwordx4 a[16:19], v8, s[72:75], s83 offen offset:0 sc1\n" \
  "buffer_load_dwordx4 a[20:23], v8, s[72:75], s83 offen offset:1024 sc1\n" \
  "buffer_load_dwordx4 a[24:27], v8, s[72:75], s83 offen offset:2048 sc1\n" \
  "buffer_load_dwordx4 a[28:31], v8, s[72:75], s83 offen offset:3072 sc1\n" \
  "buffer_load_dwordx4 a[96:99], v7, s[72:75], s85 offen offset:0 sc1\n" \
  "buffer_load_dwordx4 a[100:103], v7, s[72:75], s85 offen offset:1024 sc1\n" \
  "buffer_load_dwordx4 a[104:107], v7, s[72:75], s85 offen offset:2048 sc1\n" \
  "buffer_load_dwordx4 a[108:111], v7, s[72:75], s85 offen offset:3072 sc1\n" \
  "buffer_load_dwordx4 a[112:115], v8, s[72:75], s85 offen offset:0 sc1\n" \
  "buffer_load_dwordx4 a[116:119], v8, s[72:75], s85 offen offset:1024 sc1\n" \
  "buffer_load_dwordx4 a[120:123], v8, s[72:75], s85 offen offset:2048 sc1\n" \
  "buffer_load_dwordx4 a[124:127], v8, s[72:75], s85 offen offset:3072 sc1\n" \
  "s_lshl_b32 s87, s82, 19\n" \
  "s_add_u32 s87, s87, " STR(EXP_B) "<<21\n" \
  "s_xor_b32 s87, s87, %6\n" \
  "s_lshr_b32 s80, %3, 3\n" \
  "s_xor_b32 s80, s80, s82\n" \
  "s_and_b32 s80, s80, 7\n" \
  "s_mul_i32 s80, s80, 0x4800\n" \
  "s_and_b32 s80, s80, 0x7000\n" \
  "s_xor_b32 s87, s87, s80\n" \
  "s_and_b32 s80, %3, 7\n" \
  "s_lshl_b32 s80, s80, 16\n" \
  "s_or_b32 s87, s87, s80\n" \
  "s_waitcnt vmcnt(0)\n" \
  ".p2align 6\n" \
  "LA_%=:\n" \
  "s_waitcnt vmcnt(24)\n" \
  "s_add_u32 s81, s82, 2\n" \
  "s_min_u32 s81, s81, 64\n" \
  "s_sub_u32 s84, s81, 1\n" \
  "v_mfma_f32_16x16x32_bf16 v[116:119], a[64:67], a[0:3], v[20:23]\n" \
  "s_lshr_b32 s80, s84, 2\n" \
  "s_xnor_b32 s80, s80, %4\n" \
  "v_mfma_f32_16x16x32_bf16 v[120:123], a[64:67], a[16:19], 0\n" \
  "s_and_b32 s80, s80, 1\n" \
  "s_lshl_b32 s80, s80, 13\n" \
  "v_mfma_f32_16x16x32_bf16 v[124:127], a[80:83], a[16:19], v[20:23]\n" \
  "s_lshl_b32 s83, s84, 19\n" \
  "s_or_b32 s83, s83, s80\n" \
  "v_mfma_f32_16x16x32_bf16 v[116:119], a[68:71], a[4:7], v[116:119]\n" \
  "s_lshl_b32 s80, %3, 14\n" \
  "s_add_u32 s83, s83, s80\n" \
  "v_mfma_f32_16x16x32_bf16 v[120:123], a[68:71], a[20:23], v[120:123]\n" \
  "s_add_u32 s84, s83, " STR(QKV_SZ32) "\n" \
  "s_add_u32 s85, s84, " STR(QKV_SZ32) "\n" \
  "v_mfma_f32_16x16x32_bf16 v[124:127], a[84:87], a[20:23], v[124:127]\n" \
  "s_lshr_b32 s80, s82, 2\n" \
  "s_xnor_b32 s80, s80, %4\n" \
  "v_mfma_f32_16x16x32_bf16 v[116:119], a[72:75], a[8:11], v[116:119]\n" \
  "s_and_b32 s80, s80, 1\n" \
  "s_lshl_b32 s80, s80, 13\n" \
  "v_mfma_f32_16x16x32_bf16 v[120:123], a[72:75], a[24:27], v[120:123]\n" \
  "s_lshl_b32 s86, s82, 19\n" \
  "s_or_b32 s86, s86, s80\n" \
  "v_mfma_f32_16x16x32_bf16 v[124:127], a[88:91], a[24:27], v[124:127]\n" \
  "s_lshr_b32 s80, %3, 1\n" \
  "s_lshl_b32 s80, s80, 14\n" \
  "v_mfma_f32_16x16x32_bf16 v[116:119], a[76:79], a[12:15], v[116:119]\n" \
  "s_add_u32 s86, s86, s80\n" \
  "s_and_b32 s80, %3, 1\n" \
  "v_mfma_f32_16x16x32_bf16 v[120:123], a[76:79], a[28:31], v[120:123]\n" \
  "s_lshl_b32 s80, s80, 12\n" \
  "s_add_u32 s86, s86, s80\n" \
  "v_mfma_f32_16x16x32_bf16 v[124:127], a[92:95], a[28:31], v[124:127]\n" \
  "s_add_u32 s86, s86, 3*" STR(QKV_SZ32) "\n" \
  "s_cmp_lt_u32 s92, s81\n" \
  "s_cbranch_scc1 LG0_%=\n" \
  "LH0_%=:\n" \
  "buffer_load_dwordx4 a[64:67], v7, s[72:75], s84 offen offset:0 sc1\n" \
  "buffer_load_dwordx4 a[68:71], v7, s[72:75], s84 offen offset:1024 sc1\n" \
  "buffer_load_dwordx4 a[72:75], v7, s[72:75], s84 offen offset:2048 sc1\n" \
  "buffer_load_dwordx4 a[76:79], v7, s[72:75], s84 offen offset:3072 sc1\n" \
  "buffer_load_dwordx4 a[80:83], v8, s[72:75], s84 offen offset:0 sc1\n" \
  "buffer_load_dwordx4 a[84:87], v8, s[72:75], s84 offen offset:1024 sc1\n" \
  "v_max3_f32 v144, v116, v117, v118\n" \
  "v_max3_f32 v145, v120, v121, v122\n" \
  "v_max_f32 v144, v144, v119\n" \
  "v_max3_f32 v145, v145, v123, v124\n" \
  "v_max3_f32 v145, v145, v125, v126\n" \
  "v_max_f32 v145, v145, v127\n" \
  "v_mov_b32 v5, v144\n" \
  "v_mov_b32 v6, v145\n" \
  "s_nop 0\n" \
  "v_permlane16_swap_b32 v144, v5\n" \
  "v_permlane16_swap_b32 v145, v6\n" \
  "v_max_f32 v144, v144, v5\n" \
  "v_max_f32 v145, v145, v6\n" \
  "v_mov_b32 v5, v144\n" \
  "v_mov_b32 v6, v145\n" \
  "s_nop 0\n" \
  "v_permlane32_swap_b32 v144, v5\n" \
  "v_permlane32_swap_b32 v145, v6\n" \
  "v_max_f32 v144, v144, v5\n" \
  "v_max_f32 v145, v145, v6\n" \
  "v_mul_f32 v144, s89, v144\n" \
  "v_mul_f32 v145, s89, v145\n" \
  "v_fma_f32 v116, v116, s89, -v144\n" \
  "v_exp_f32 v116, v116\n" \
  "buffer_load_dwordx4 a[88:91], v8, s[72:75], s84 offen offset:2048 sc1\n" \
  "v_fma_f32 v117, v117, s89, -v144\n" \
  "v_exp_f32 v117, v117\n" \
  "buffer_load_dwordx4 a[92:95], v8, s[72:75], s84 offen offset:3072 sc1\n" \
  "v_fma_f32 v118, v118, s89, -v144\n" \
  "v_exp_f32 v118, v118\n" \
  "buffer_load_dwordx4 a[32:35], v7, s[72:75], s83 offen offset:0 sc1\n" \
  "v_fma_f32 v119, v119, s89, -v144\n" \
  "v_exp_f32 v119, v119\n" \
  "buffer_load_dwordx4 a[36:39], v7, s[72:75], s83 offen offset:1024 sc1\n" \
  "v_fma_f32 v120, v120, s89, -v145\n" \
  "v_exp_f32 v120, v120\n" \
  "buffer_load_dwordx4 a[40:43], v7, s[72:75], s83 offen offset:2048 sc1\n" \
  "v_fma_f32 v121, v121, s89, -v145\n" \
  "v_exp_f32 v121, v121\n" \
  "buffer_load_dwordx4 a[44:47], v7, s[72:75], s83 offen offset:3072 sc1\n" \
  "v_fma_f32 v122, v122, s89, -v145\n" \
  "v_exp_f32 v122, v122\n" \
  "buffer_load_dwordx4 a[48:51], v8, s[72:75], s83 offen offset:0 sc1\n" \
  "v_fma_f32 v123, v123, s89, -v145\n" \
  "v_exp_f32 v123, v123\n" \
  "buffer_load_dwordx4 a[52:55], v8, s[72:75], s83 offen offset:1024 sc1\n" \
  "v_fma_f32 v124, v124, s89, -v145\n" \
  "v_exp_f32 v124, v124\n" \
  "buffer_load_dwordx4 a[56:59], v8, s[72:75], s83 offen offset:2048 sc1\n" \
  "v_fma_f32 v125, v125, s89, -v145\n" \
  "v_exp_f32 v125, v125\n" \
  "buffer_load_dwordx4 a[60:63], v8, s[72:75], s83 offen offset:3072 sc1\n" \
  "v_fma_f32 v126, v126, s89, -v145\n" \
  "v_exp_f32 v126, v126\n" \
  "v_fma_f32 v127, v127, s89, -v145\n" \
  "v_exp_f32 v127, v127\n" \
  "s_nop 0\n" \
  "v_cvt_pk_bf16_f32 v128, v116, v117\n" \
  "v_cvt_pk_bf16_f32 v129, v118, v119\n" \
  "v_cvt_pk_bf16_f32 v132, v120, v121\n" \
  "v_cvt_pk_bf16_f32 v133, v122, v123\n" \
  "v_cvt_pk_bf16_f32 v134, v124, v125\n" \
  "v_cvt_pk_bf16_f32 v135, v126, v127\n" \
  "s_waitcnt vmcnt(22)\n" \
  "buffer_store_dword v16, v15, s[76:79], 0 offen sc1\n" \
  "v_mfma_f32_16x16x32_bf16 v[136:139], v[24:27], v[128:131], 0\n" \
  "v_mfma_f32_16x16x32_bf16 v[140:143], v[24:27], v[132:135], 0\n" \
  "v_add_u32 v16, 1, v16\n" \
  "v_mfma_f32_16x16x32_bf16 v[52:55], a[96:99], v[128:131], 0\n" \
  "v_mfma_f32_16x16x32_bf16 v[84:87], a[96:99], v[132:135], 0\n" \
  "buffer_load_dwordx4 a[96:99], v7, s[72:75], s85 offen offset:0 sc1\n" \
  "v_mfma_f32_16x16x32_bf16 v[56:59], a[100:103], v[128:131], 0\n" \
  "v_mfma_f32_16x16x32_bf16 v[88:91], a[100:103], v[132:135], 0\n" \
  "buffer_load_dwordx4 a[100:103], v7, s[72:75], s85 offen offset:1024 sc1\n" \
  "v_mfma_f32_16x16x32_bf16 v[60:63], a[104:107], v[128:131], 0\n" \
  "v_mfma_f32_16x16x32_bf16 v[92:95], a[104:107], v[132:135], 0\n" \
  "buffer_load_dwordx4 a[104:107], v7, s[72:75], s85 offen offset:2048 sc1\n" \
  "v_mfma_f32_16x16x32_bf16 v[64:67], a[108:111], v[128:131], 0\n" \
  "v_mfma_f32_16x16x32_bf16 v[96:99], a[108:111], v[132:135], 0\n" \
  "buffer_load_dwordx4 a[108:111], v7, s[72:75], s85 offen offset:3072 sc1\n" \
  "v_mfma_f32_16x16x32_bf16 v[68:71], a[112:115], v[128:131], 0\n" \
  "v_mfma_f32_16x16x32_bf16 v[100:103], a[112:115], v[132:135], 0\n" \
  "buffer_load_dwordx4 a[112:115], v8, s[72:75], s85 offen offset:0 sc1\n" \
  "v_mfma_f32_16x16x32_bf16 v[72:75], a[116:119], v[128:131], 0\n" \
  "v_mfma_f32_16x16x32_bf16 v[104:107], a[116:119], v[132:135], 0\n" \
  "buffer_load_dwordx4 a[116:119], v8, s[72:75], s85 offen offset:1024 sc1\n" \
  "v_mfma_f32_16x16x32_bf16 v[76:79], a[120:123], v[128:131], 0\n" \
  "v_mfma_f32_16x16x32_bf16 v[108:111], a[120:123], v[132:135], 0\n" \
  "buffer_load_dwordx4 a[120:123], v8, s[72:75], s85 offen offset:2048 sc1\n" \
  "v_mfma_f32_16x16x32_bf16 v[80:83], a[124:127], v[128:131], 0\n" \
  "v_mfma_f32_16x16x32_bf16 v[112:115], a[124:127], v[132:135], 0\n" \
  "buffer_load_dwordx4 a[124:127], v8, s[72:75], s85 offen offset:3072 sc1\n" \
  "buffer_load_dword v14, v13, s[76:79], 0 offen sc1\n" \
  "s_cmp_eq_u32 s90, 0\n" \
  "s_cbranch_scc1 LW0_%=\n" \
  "s_waitcnt vmcnt(26)\n" \
  "LM0_%=:\n" \
  "ds_write_b128 v11, v[44:47]\n" \
  "ds_write_b128 v11, v[48:51] offset:1024\n" \
  "v_mfma_f32_16x16x32_bf16 v[148:151], v[28:31], a[0:3], 0\n" \
  "v_max3_u32 v5, v28, v29, v30\n" \
  "v_max3_u32 v5, v5, v31, v32\n" \
  "v_mfma_f32_16x16x32_bf16 v[152:155], v[28:31], a[16:19], 0\n" \
  "v_max3_u32 v5, v5, v33, v34\n" \
  "v_max3_u32 v5, v5, v35, v36\n" \
  "v_mfma_f32_16x16x32_bf16 v[148:151], v[32:35], a[4:7], v[148:151]\n" \
  "v_max3_u32 v5, v5, v37, v38\n" \
  "v_max3_u32 v5, v5, v39, v40\n" \
  "v_mfma_f32_16x16x32_bf16 v[152:155], v[32:35], a[20:23], v[152:155]\n" \
  "v_max3_u32 v5, v5, v41, v42\n" \
  "v_max3_u32 v5, v5, v43, v44\n" \
  "v_mfma_f32_16x16x32_bf16 v[148:151], v[36:39], a[8:11], v[148:151]\n" \
  "v_max3_u32 v5, v5, v45, v46\n" \
  "v_max3_u32 v5, v5, v47, v48\n" \
  "v_mfma_f32_16x16x32_bf16 v[152:155], v[36:39], a[24:27], v[152:155]\n" \
  "v_max3_u32 v5, v5, v49, v50\n" \
  "v_max_u32 v5, v5, v51\n" \
  "v_mfma_f32_16x16x32_bf16 v[148:151], v[40:43], a[12:15], v[148:151]\n" \
  "v_cmp_eq_u32 vcc, -1, v5\n" \
  "v_mfma_f32_16x16x32_bf16 v[152:155], v[40:43], a[28:31], v[152:155]\n" \
  "ds_read_b64_tr_b16 a[128:129], v12 offset:0\n" \
  "ds_read_b64_tr_b16 a[130:131], v12 offset:256\n" \
  "ds_read_b64_tr_b16 a[132:133], v12 offset:512\n" \
  "ds_read_b64_tr_b16 a[134:135], v12 offset:768\n" \
  "ds_read_b64_tr_b16 a[136:137], v12 offset:1024\n" \
  "ds_read_b64_tr_b16 a[138:139], v12 offset:1280\n" \
  "ds_read_b64_tr_b16 a[140:141], v12 offset:1536\n" \
  "ds_read_b64_tr_b16 a[142:143], v12 offset:1792\n" \
  "s_cbranch_vccnz LR0_%=\n" \
  "v_max_f32 v146, v150, v151\n" \
  "v_max_f32 v147, v154, v155\n" \
  "v_mov_b32 v5, v146\n" \
  "v_mov_b32 v6, v147\n" \
  "s_nop 0\n" \
  "v_permlane16_swap_b32 v146, v5\n" \
  "v_permlane16_swap_b32 v147, v6\n" \
  "v_max_f32 v146, v146, v5\n" \
  "v_max_f32 v147, v147, v6\n" \
  "v_mov_b32 v5, v146\n" \
  "v_mov_b32 v6, v147\n" \
  "s_nop 0\n" \
  "v_permlane32_swap_b32 v146, v5\n" \
  "v_permlane32_swap_b32 v147, v6\n" \
  "v_max_f32 v146, v146, v5\n" \
  "v_max_f32 v147, v147, v6\n" \
  "v_mul_f32 v146, s89, v146\n" \
  "v_mul_f32 v147, s89, v147\n" \
  "v_max_f32 v146, v146, v144\n" \
  "v_max_f32 v147, v147, v145\n" \
  "v_sub_f32 v160, v144, v146\n" \
  "v_sub_f32 v162, v145, v147\n" \
  "v_fma_f32 v150, v150, s89, -v146\n" \
  "v_fma_f32 v151, v151, s89, -v146\n" \
  "v_fma_f32 v154, v154, s89, -v147\n" \
  "v_fma_f32 v155, v155, s89, -v147\n" \
  "v_exp_f32 v150, v150\n" \
  "v_exp_f32 v151, v151\n" \
  "v_exp_f32 v154, v154\n" \
  "v_exp_f32 v155, v155\n" \
  "v_exp_f32 v160, v160\n" \
  "v_exp_f32 v162, v162\n" \
  "v_add_f32 v146, v150, v151\n" \
  "v_add_f32 v147, v154, v155\n" \
  "v_mov_b32 v5, v146\n" \
  "v_mov_b32 v6, v147\n" \
  "s_nop 0\n" \
  "v_permlane16_swap_b32 v146, v5\n" \
  "v_permlane16_swap_b32 v147, v6\n" \
  "v_add_f32 v146, v146, v5\n" \
  "v_add_f32 v147, v147, v6\n" \
  "v_mov_b32 v5, v146\n" \
  "v_mov_b32 v6, v147\n" \
  "s_nop 0\n" \
  "v_permlane32_swap_b32 v146, v5\n" \
  "v_permlane32_swap_b32 v147, v6\n" \
  "v_add_f32 v146, v146, v5\n" \
  "v_add_f32 v147, v147, v6\n" \
  "v_fma_f32 v146, v160, v136, v146\n" \
  "v_fma_f32 v147, v162, v140, v147\n" \
  "v_rcp_f32 v146, v146\n" \
  "v_rcp_f32 v147, v147\n" \
  "v_mul_f32 v160, v160, v146\n" \
  "v_mul_f32 v162, v162, v147\n" \
  "v_mul_f32 v150, v150, v146\n" \
  "v_mul_f32 v151, v151, v146\n" \
  "v_mul_f32 v154, v154, v147\n" \
  "v_mul_f32 v155, v155, v147\n" \
  "v_cvt_pk_bf16_f32 v157, v150, v151\n" \
  "v_cvt_pk_bf16_f32 v159, v154, v155\n" \
  "v_pk_mul_f32 v[52:53], v[52:53], v[160:161] op_sel_hi:[1,0]\n" \
  "v_pk_mul_f32 v[54:55], v[54:55], v[160:161] op_sel_hi:[1,0]\n" \
  "v_pk_mul_f32 v[56:57], v[56:57], v[160:161] op_sel_hi:[1,0]\n" \
  "v_pk_mul_f32 v[58:59], v[58:59], v[160:161] op_sel_hi:[1,0]\n" \
  "v_pk_mul_f32 v[60:61], v[60:61], v[160:161] op_sel_hi:[1,0]\n" \
  "v_pk_mul_f32 v[62:63], v[62:63], v[160:161] op_sel_hi:[1,0]\n" \
  "v_pk_mul_f32 v[64:65], v[64:65], v[160:161] op_sel_hi:[1,0]\n" \
  "v_pk_mul_f32 v[66:67], v[66:67], v[160:161] op_sel_hi:[1,0]\n" \
  "v_pk_mul_f32 v[68:69], v[68:69], v[160:161] op_sel_hi:[1,0]\n" \
  "v_pk_mul_f32 v[70:71], v[70:71], v[160:161] op_sel_hi:[1,0]\n" \
  "v_pk_mul_f32 v[72:73], v[72:73], v[160:161] op_sel_hi:[1,0]\n" \
  "v_pk_mul_f32 v[74:75], v[74:75], v[160:161] op_sel_hi:[1,0]\n" \
  "v_pk_mul_f32 v[76:77], v[76:77], v[160:161] op_sel_hi:[1,0]\n" \
  "v_pk_mul_f32 v[78:79], v[78:79], v[160:161] op_sel_hi:[1,0]\n" \
  "v_pk_mul_f32 v[80:81], v[80:81], v[160:161] op_sel_hi:[1,0]\n" \
  "v_pk_mul_f32 v[82:83], v[82:83], v[160:161] op_sel_hi:[1,0]\n" \
  "v_pk_mul_f32 v[84:85], v[84:85], v[162:163] op_sel_hi:[1,0]\n" \
  "v_pk_mul_f32 v[86:87], v[86:87], v[162:163] op_sel_hi:[1,0]\n" \
  "v_pk_mul_f32 v[88:89], v[88:89], v[162:163] op_sel_hi:[1,0]\n" \
  "v_pk_mul_f32 v[90:91], v[90:91], v[162:163] op_sel_hi:[1,0]\n" \
  "v_pk_mul_f32 v[92:93], v[92:93], v[162:163] op_sel_hi:[1,0]\n" \
  "v_pk_mul_f32 v[94:95], v[94:95], v[162:163] op_sel_hi:[1,0]\n" \
  "v_pk_mul_f32 v[96:97], v[96:97], v[162:163] op_sel_hi:[1,0]\n" \
  "v_pk_mul_f32 v[98:99], v[98:99], v[162:163] op_sel_hi:[1,0]\n" \
  "v_pk_mul_f32 v[100:101], v[100:101], v[162:163] op_sel_hi:[1,0]\n" \
  "v_pk_mul_f32 v[102:103], v[102:103], v[162:163] op_sel_hi:[1,0]\n" \
  "v_pk_mul_f32 v[104:105], v[104:105], v[162:163] op_sel_hi:[1,0]\n" \
  "v_pk_mul_f32 v[106:107], v[106:107], v[162:163] op_sel_hi:[1,0]\n" \
  "v_pk_mul_f32 v[108:109], v[108:109], v[162:163] op_sel_hi:[1,0]\n" \
  "v_pk_mul_f32 v[110:111], v[110:111], v[162:163] op_sel_hi:[1,0]\n" \
  "v_pk_mul_f32 v[112:113], v[112:113], v[162:163] op_sel_hi:[1,0]\n" \
  "v_pk_mul_f32 v[114:115], v[114:115], v[162:163] op_sel_hi:[1,0]\n" \
  "s_waitcnt lgkmcnt(0)\n" \
  "v_mfma_f32_16x16x16_bf16 v[52:55], a[128:129], v[156:157], v[52:55]\n" \
  "v_mfma_f32_16x16x16_bf16 v[56:59], a[130:131], v[156:157], v[56:59]\n" \
  "v_mfma_f32_16x16x16_bf16 v[60:63], a[132:133], v[156:157], v[60:63]\n" \
  "v_mfma_f32_16x16x16_bf16 v[64:67], a[134:135], v[156:157], v[64:67]\n" \
  "v_mfma_f32_16x16x16_bf16 v[68:71], a[136:137], v[156:157], v[68:71]\n" \
  "v_cvt_pk_bf16_f32 v164, v52, v53\n" \
  "v_cvt_pk_bf16_f32 v165, v54, v55\n" \
  "v_mfma_f32_16x16x16_bf16 v[72:75], a[138:139], v[156:157], v[72:75]\n" \
  "v_cvt_pk_bf16_f32 v166, v56, v57\n" \
  "v_cvt_pk_bf16_f32 v167, v58, v59\n" \
  "buffer_store_dwordx4 v[164:167], v7, s[72:75], s86 offen offset:0 sc1\n" \
  "v_mfma_f32_16x16x16_bf16 v[76:79], a[140:141], v[156:157], v[76:79]\n" \
  "v_cvt_pk_bf16_f32 v168, v60, v61\n" \
  "v_cvt_pk_bf16_f32 v169, v62, v63\n" \
  "v_mfma_f32_16x16x16_bf16 v[80:83], a[142:143], v[156:157], v[80:83]\n" \
  "v_cvt_pk_bf16_f32 v170, v64, v65\n" \
  "v_cvt_pk_bf16_f32 v171, v66, v67\n" \
  "buffer_store_dwordx4 v[168:171], v7, s[72:75], s86 offen offset:1024 sc1\n" \
  "v_mfma_f32_16x16x16_bf16 v[84:87], a[128:129], v[158:159], v[84:87]\n" \
  "v_cvt_pk_bf16_f32 v172, v68, v69\n" \
  "v_cvt_pk_bf16_f32 v173, v70, v71\n" \
  "v_mfma_f32_16x16x16_bf16 v[88:91], a[130:131], v[158:159], v[88:91]\n" \
  "v_cvt_pk_bf16_f32 v174, v72, v73\n" \
  "v_cvt_pk_bf16_f32 v175, v74, v75\n" \
  "buffer_store_dwordx4 v[172:175], v7, s[72:75], s86 offen offset:2048 sc1\n" \
  "v_mfma_f32_16x16x16_bf16 v[92:95], a[132:133], v[158:159], v[92:95]\n" \
  "v_cvt_pk_bf16_f32 v176, v76, v77\n" \
  "v_cvt_pk_bf16_f32 v177, v78, v79\n" \
  "v_mfma_f32_16x16x16_bf16 v[96:99], a[134:135], v[158:159], v[96:99]\n" \
  "v_cvt_pk_bf16_f32 v178, v80, v81\n" \
  "v_cvt_pk_bf16_f32 v179, v82, v83\n" \
  "buffer_store_dwordx4 v[176:179], v7, s[72:75], s86 offen offset:3072 sc1\n" \
  "v_mfma_f32_16x16x16_bf16 v[100:103], a[136:137], v[158:159], v[100:103]\n" \
  "v_cvt_pk_bf16_f32 v180, v84, v85\n" \
  "v_cvt_pk_bf16_f32 v181, v86, v87\n" \
  "v_mfma_f32_16x16x16_bf16 v[104:107], a[138:139], v[158:159], v[104:107]\n" \
  "v_cvt_pk_bf16_f32 v182, v88, v89\n" \
  "v_cvt_pk_bf16_f32 v183, v90, v91\n" \
  "buffer_store_dwordx4 v[180:183], v17, s[72:75], s86 offen offset:0 sc1\n" \
  "v_mfma_f32_16x16x16_bf16 v[108:111], a[140:141], v[158:159], v[108:111]\n" \
  "v_cvt_pk_bf16_f32 v184, v92, v93\n" \
  "v_cvt_pk_bf16_f32 v185, v94, v95\n" \
  "v_mfma_f32_16x16x16_bf16 v[112:115], a[142:143], v[158:159], v[112:115]\n" \
  "v_cvt_pk_bf16_f32 v186, v96, v97\n" \
  "v_cvt_pk_bf16_f32 v187, v98, v99\n" \
  "buffer_store_dwordx4 v[184:187], v17, s[72:75], s86 offen offset:1024 sc1\n" \
  "v_cvt_pk_bf16_f32 v188, v100, v101\n" \
  "v_cvt_pk_bf16_f32 v189, v102, v103\n" \
  "v_cvt_pk_bf16_f32 v190, v104, v105\n" \
  "v_cvt_pk_bf16_f32 v191, v106, v107\n" \
  "buffer_store_dwordx4 v[188:191], v17, s[72:75], s86 offen offset:2048 sc1\n" \
  "s_nop 1\n" \
  "v_cvt_pk_bf16_f32 v192, v108, v109\n" \
  "v_cvt_pk_bf16_f32 v193, v110, v111\n" \
  "v_cvt_pk_bf16_f32 v194, v112, v113\n" \
  "v_cvt_pk_bf16_f32 v195, v114, v115\n" \
  "buffer_store_dwordx4 v[192:195], v17, s[72:75], s86 offen offset:3072 sc1\n" \
  "s_add_u32 s82, s82, 1\n" \
  "s_cmp_eq_u32 s82, 64\n" \
  "s_cbranch_scc1 LZ_%=\n" \
  "s_lshl_b32 s87, s82, 19\n" \
  "s_add_u32 s87, s87, " STR(EXP_B) "<<21\n" \
  "s_xor_b32 s87, s87, %6\n" \
  "s_lshr_b32 s80, %3, 3\n" \
  "s_xor_b32 s80, s80, s82\n" \
  "s_and_b32 s80, s80, 7\n" \
  "s_mul_i32 s80, s80, 0x4800\n" \
  "s_and_b32 s80, s80, 0x7000\n" \
  "s_xor_b32 s87, s87, s80\n" \
  "s_and_b32 s80, %3, 7\n" \
  "s_lshl_b32 s80, s80, 16\n" \
  "s_or_b32 s87, s87, s80\n" \
  "s_waitcnt vmcnt(8)\n" \
  "v_readlane_b32 s91, v14, 0\n" \
  "v_readlane_b32 s92, v14, 1\n" \
  "v_readlane_b32 s80, v14, 2\n" \
  "s_min_u32 s92, s92, s80\n" \
  "v_readlane_b32 s80, v14, 3\n" \
  "s_min_u32 s92, s92, s80\n" \
  "v_readlane_b32 s80, v14, 4\n" \
  "s_min_u32 s92, s92, s80\n" \
  "s_cmp_eq_u32 s82, 63\n" \
  "s_addc_u32 s91, s91, 0\n" \
  "s_cmp_gt_u32 s91, s82\n" \
  "s_cselect_b32 s90, 1, 0\n" \
  "s_cselect_b32 s80, 0, 0x80000000\n" \
  "v_or_b32 v9, s80, v7\n" \
  "v_or_b32 v10, s80, v18\n" \
  "buffer_load_dwordx4 v[28:31], v9, s[68:71], s87 offen offset:0 sc1\n" \
  "buffer_load_dwordx4 v[32:35], v9, s[68:71], s87 offen offset:1024 sc1\n" \
  "buffer_load_dwordx4 v[36:39], v9, s[68:71], s87 offen offset:2048 sc1\n" \
  "buffer_load_dwordx4 v[40:43], v9, s[68:71], s87 offen offset:3072 sc1\n" \
  "buffer_load_dwordx4 v[44:47], v10, s[68:71], s87 offen offset:0 sc1\n" \
  "buffer_load_dwordx4 v[48:51], v10, s[68:71], s87 offen offset:1024 sc1\n" \
  "s_waitcnt vmcnt(24)\n" \
  "s_add_u32 s81, s82, 2\n" \
  "s_min_u32 s81, s81, 64\n" \
  "s_sub_u32 s84, s81, 1\n" \
  "v_mfma_f32_16x16x32_bf16 v[116:119], a[64:67], a[32:35], v[20:23]\n" \
  "s_lshr_b32 s80, s84, 2\n" \
  "s_xnor_b32 s80, s80, %4\n" \
  "v_mfma_f32_16x16x32_bf16 v[120:123], a[64:67], a[48:51], 0\n" \
  "s_and_b32 s80, s80, 1\n" \
  "s_lshl_b32 s80, s80, 13\n" \
  "v_mfma_f32_16x16x32_bf16 v[124:127], a[80:83], a[48:51], v[20:23]\n" \
  "s_lshl_b32 s83, s84, 19\n" \
  "s_or_b32 s83, s83, s80\n" \
  "v_mfma_f32_16x16x32_bf16 v[116:119], a[68:71], a[36:39], v[116:119]\n" \
  "s_lshl_b32 s80, %3, 14\n" \
  "s_add_u32 s83, s83, s80\n" \
  "v_mfma_f32_16x16x32_bf16 v[120:123], a[68:71], a[52:55], v[120:123]\n" \
  "s_add_u32 s84, s83, " STR(QKV_SZ32) "\n" \
  "s_add_u32 s85, s84, " STR(QKV_SZ32) "\n" \
  "v_mfma_f32_16x16x32_bf16 v[124:127], a[84:87], a[52:55], v[124:127]\n" \
  "s_lshr_b32 s80, s82, 2\n" \
  "s_xnor_b32 s80, s80, %4\n" \
  "v_mfma_f32_16x16x32_bf16 v[116:119], a[72:75], a[40:43], v[116:119]\n" \
  "s_and_b32 s80, s80, 1\n" \
  "s_lshl_b32 s80, s80, 13\n" \
  "v_mfma_f32_16x16x32_bf16 v[120:123], a[72:75], a[56:59], v[120:123]\n" \
  "s_lshl_b32 s86, s82, 19\n" \
  "s_or_b32 s86, s86, s80\n" \
  "v_mfma_f32_16x16x32_bf16 v[124:127], a[88:91], a[56:59], v[124:127]\n" \
  "s_lshr_b32 s80, %3, 1\n" \
  "s_lshl_b32 s80, s80, 14\n" \
  "v_mfma_f32_16x16x32_bf16 v[116:119], a[76:79], a[44:47], v[116:119]\n" \
  "s_add_u32 s86, s86, s80\n" \
  "s_and_b32 s80, %3, 1\n" \
  "v_mfma_f32_16x16x32_bf16 v[120:123], a[76:79], a[60:63], v[120:123]\n" \
  "s_lshl_b32 s80, s80, 12\n" \
  "s_add_u32 s86, s86, s80\n" \
  "v_mfma_f32_16x16x32_bf16 v[124:127], a[92:95], a[60:63], v[124:127]\n" \
  "s_add_u32 s86, s86, 3*" STR(QKV_SZ32) "\n" \
  "s_cmp_lt_u32 s92, s81\n" \
  "s_cbranch_scc1 LG1_%=\n" \
  "LH1_%=:\n" \
  "buffer_load_dwordx4 a[64:67], v7, s[72:75], s84 offen offset:0 sc1\n" \
  "buffer_load_dwordx4 a[68:71], v7, s[72:75], s84 offen offset:1024 sc1\n" \
  "buffer_load_dwordx4 a[72:75], v7, s[72:75], s84 offen offset:2048 sc1\n" \
  "buffer_load_dwordx4 a[76:79], v7, s[72:75], s84 offen offset:3072 sc1\n" \
  "buffer_load_dwordx4 a[80:83], v8, s[72:75], s84 offen offset:0 sc1\n" \
  "buffer_load_dwordx4 a[84:87], v8, s[72:75], s84 offen offset:1024 sc1\n" \
  "v_max3_f32 v144, v116, v117, v118\n" \
  "v_max3_f32 v145, v120, v121, v122\n" \
  "v_max_f32 v144, v144, v119\n" \
  "v_max3_f32 v145, v145, v123, v124\n" \
  "v_max3_f32 v145, v145, v125, v126\n" \
  "v_max_f32 v145, v145, v127\n" \
  "v_mov_b32 v5, v144\n" \
  "v_mov_b32 v6, v145\n" \
  "s_nop 0\n" \
  "v_permlane16_swap_b32 v144, v5\n" \
  "v_permlane16_swap_b32 v145, v6\n" \
  "v_max_f32 v144, v144, v5\n" \
  "v_max_f32 v145, v145, v6\n" \
  "v_mov_b32 v5, v144\n" \
  "v_mov_b32 v6, v145\n" \
  "s_nop 0\n" \
  "v_permlane32_swap_b32 v144, v5\n" \
  "v_permlane32_swap_b32 v145, v6\n" \
  "v_max_f32 v144, v144, v5\n" \
  "v_max_f32 v145, v145, v6\n" \
  "v_mul_f32 v144, s89, v144\n" \
  "v_mul_f32 v145, s89, v145\n" \
  "v_fma_f32 v116, v116, s89, -v144\n" \
  "v_exp_f32 v116, v116\n" \
  "buffer_load_dwordx4 a[88:91], v8, s[72:75], s84 offen offset:2048 sc1\n" \
  "v_fma_f32 v117, v117, s89, -v144\n" \
  "v_exp_f32 v117, v117\n" \
  "buffer_load_dwordx4 a[92:95], v8, s[72:75], s84 offen offset:3072 sc1\n" \
  "v_fma_f32 v118, v118, s89, -v144\n" \
  "v_exp_f32 v118, v118\n" \
  "buffer_load_dwordx4 a[0:3], v7, s[72:75], s83 offen offset:0 sc1\n" \
  "v_fma_f32 v119, v119, s89, -v144\n" \
  "v_exp_f32 v119, v119\n" \
  "buffer_load_dwordx4 a[4:7], v7, s[72:75], s83 offen offset:1024 sc1\n" \
  "v_fma_f32 v120, v120, s89, -v145\n" \
  "v_exp_f32 v120, v120\n" \
  "buffer_load_dwordx4 a[8:11], v7, s[72:75], s83 offen offset:2048 sc1\n" \
  "v_fma_f32 v121, v121, s89, -v145\n" \
  "v_exp_f32 v121, v121\n" \
  "buffer_load_dwordx4 a[12:15], v7, s[72:75], s83 offen offset:3072 sc1\n" \
  "v_fma_f32 v122, v122, s89, -v145\n" \
  "v_exp_f32 v122, v122\n" \
  "buffer_load_dwordx4 a[16:19], v8, s[72:75], s83 offen offset:0 sc1\n" \
  "v_fma_f32 v123, v123, s89, -v145\n" \
  "v_exp_f32 v123, v123\n" \
  "buffer_load_dwordx4 a[20:23], v8, s[72:75], s83 offen offset:1024 sc1\n" \
  "v_fma_f32 v124, v124, s89, -v145\n" \
  "v_exp_f32 v124, v124\n" \
  "buffer_load_dwordx4 a[24:27], v8, s[72:75], s83 offen offset:2048 sc1\n" \
  "v_fma_f32 v125, v125, s89, -v145\n" \
  "v_exp_f32 v125, v125\n" \
  "buffer_load_dwordx4 a[28:31], v8, s[72:75], s83 offen offset:3072 sc1\n" \
  "v_fma_f32 v126, v126, s89, -v145\n" \
  "v_exp_f32 v126, v126\n" \
  "v_fma_f32 v127, v127, s89, -v145\n" \
  "v_exp_f32 v127, v127\n" \
  "s_nop 0\n" \
  "v_cvt_pk_bf16_f32 v128, v116, v117\n" \
  "v_cvt_pk_bf16_f32 v129, v118, v119\n" \
  "v_cvt_pk_bf16_f32 v132, v120, v121\n" \
  "v_cvt_pk_bf16_f32 v133, v122, v123\n" \
  "v_cvt_pk_bf16_f32 v134, v124, v125\n" \
  "v_cvt_pk_bf16_f32 v135, v126, v127\n" \
  "s_waitcnt vmcnt(22)\n" \
  "buffer_store_dword v16, v15, s[76:79], 0 offen sc1\n" \
  "v_mfma_f32_16x16x32_bf16 v[136:139], v[24:27], v[128:131], 0\n" \
  "v_mfma_f32_16x16x32_bf16 v[140:143], v[24:27], v[132:135], 0\n" \
  "v_add_u32 v16, 1, v16\n" \
  "v_mfma_f32_16x16x32_bf16 v[52:55], a[96:99], v[128:131], 0\n" \
  "v_mfma_f32_16x16x32_bf16 v[84:87], a[96:99], v[132:135], 0\n" \
  "buffer_load_dwordx4 a[96:99], v7, s[72:75], s85 offen offset:0 sc1\n" \
  "v_mfma_f32_16x16x32_bf16 v[56:59], a[100:103], v[128:131], 0\n" \
  "v_mfma_f32_16x16x32_bf16 v[88:91], a[100:103], v[132:135], 0\n" \
  "buffer_load_dwordx4 a[100:103], v7, s[72:75], s85 offen offset:1024 sc1\n" \
  "v_mfma_f32_16x16x32_bf16 v[60:63], a[104:107], v[128:131], 0\n" \
  "v_mfma_f32_16x16x32_bf16 v[92:95], a[104:107], v[132:135], 0\n" \
  "buffer_load_dwordx4 a[104:107], v7, s[72:75], s85 offen offset:2048 sc1\n" \
  "v_mfma_f32_16x16x32_bf16 v[64:67], a[108:111], v[128:131], 0\n" \
  "v_mfma_f32_16x16x32_bf16 v[96:99], a[108:111], v[132:135], 0\n" \
  "buffer_load_dwordx4 a[108:111], v7, s[72:75], s85 offen offset:3072 sc1\n" \
  "v_mfma_f32_16x16x32_bf16 v[68:71], a[112:115], v[128:131], 0\n" \
  "v_mfma_f32_16x16x32_bf16 v[100:103], a[112:115], v[132:135], 0\n" \
  "buffer_load_dwordx4 a[112:115], v8, s[72:75], s85 offen offset:0 sc1\n" \
  "v_mfma_f32_16x16x32_bf16 v[72:75], a[116:119], v[128:131], 0\n" \
  "v_mfma_f32_16x16x32_bf16 v[104:107], a[116:119], v[132:135], 0\n" \
  "buffer_load_dwordx4 a[116:119], v8, s[72:75], s85 offen offset:1024 sc1\n" \
  "v_mfma_f32_16x16x32_bf16 v[76:79], a[120:123], v[128:131], 0\n" \
  "v_mfma_f32_16x16x32_bf16 v[108:111], a[120:123], v[132:135], 0\n" \
  "buffer_load_dwordx4 a[120:123], v8, s[72:75], s85 offen offset:2048 sc1\n" \
  "v_mfma_f32_16x16x32_bf16 v[80:83], a[124:127], v[128:131], 0\n" \
  "v_mfma_f32_16x16x32_bf16 v[112:115], a[124:127], v[132:135], 0\n" \
  "buffer_load_dwordx4 a[124:127], v8, s[72:75], s85 offen offset:3072 sc1\n" \
  "buffer_load_dword v14, v13, s[76:79], 0 offen sc1\n" \
  "s_cmp_eq_u32 s90, 0\n" \
  "s_cbranch_scc1 LW1_%=\n" \
  "s_waitcnt vmcnt(26)\n" \
  "LM1_%=:\n" \
  "ds_write_b128 v11, v[44:47]\n" \
  "ds_write_b128 v11, v[48:51] offset:1024\n" \
  "v_mfma_f32_16x16x32_bf16 v[148:151], v[28:31], a[32:35], 0\n" \
  "v_max3_u32 v5, v28, v29, v30\n" \
  "v_max3_u32 v5, v5, v31, v32\n" \
  "v_mfma_f32_16x16x32_bf16 v[152:155], v[28:31], a[48:51], 0\n" \
  "v_max3_u32 v5, v5, v33, v34\n" \
  "v_max3_u32 v5, v5, v35, v36\n" \
  "v_mfma_f32_16x16x32_bf16 v[148:151], v[32:35], a[36:39], v[148:151]\n" \
  "v_max3_u32 v5, v5, v37, v38\n" \
  "v_max3_u32 v5, v5, v39, v40\n" \
  "v_mfma_f32_16x16x32_bf16 v[152:155], v[32:35], a[52:55], v[152:155]\n" \
  "v_max3_u32 v5, v5, v41, v42\n" \
  "v_max3_u32 v5, v5, v43, v44\n" \
  "v_mfma_f32_16x16x32_bf16 v[148:151], v[36:39], a[40:43], v[148:151]\n" \
  "v_max3_u32 v5, v5, v45, v46\n" \
  "v_max3_u32 v5, v5, v47, v48\n" \
  "v_mfma_f32_16x16x32_bf16 v[152:155], v[36:39], a[56:59], v[152:155]\n" \
  "v_max3_u32 v5, v5, v49, v50\n" \
  "v_max_u32 v5, v5, v51\n" \
  "v_mfma_f32_16x16x32_bf16 v[148:151], v[40:43], a[44:47], v[148:151]\n" \
  "v_cmp_eq_u32 vcc, -1, v5\n" \
  "v_mfma_f32_16x16x32_bf16 v[152:155], v[40:43], a[60:63], v[152:155]\n" \
  "ds_read_b64_tr_b16 a[128:129], v12 offset:0\n" \
  "ds_read_b64_tr_b16 a[130:131], v12 offset:256\n" \
  "ds_read_b64_tr_b16 a[132:133], v12 offset:512\n" \
  "ds_read_b64_tr_b16 a[134:135], v12 offset:768\n" \
  "ds_read_b64_tr_b16 a[136:137], v12 offset:1024\n" \
  "ds_read_b64_tr_b16 a[138:139], v12 offset:1280\n" \
  "ds_read_b64_tr_b16 a[140:141], v12 offset:1536\n" \
  "ds_read_b64_tr_b16 a[142:143], v12 offset:1792\n" \
  "s_cbranch_vccnz LR1_%=\n" \
  "v_max_f32 v146, v150, v151\n" \
  "v_max_f32 v147, v154, v155\n" \
  "v_mov_b32 v5, v146\n" \
  "v_mov_b32 v6, v147\n" \
  "s_nop 0\n" \
  "v_permlane16_swap_b32 v146, v5\n" \
  "v_permlane16_swap_b32 v147, v6\n" \
  "v_max_f32 v146, v146, v5\n" \
  "v_max_f32 v147, v147, v6\n" \
  "v_mov_b32 v5, v146\n" \
  "v_mov_b32 v6, v147\n" \
  "s_nop 0\n" \
  "v_permlane32_swap_b32 v146, v5\n" \
  "v_permlane32_swap_b32 v147, v6\n" \
  "v_max_f32 v146, v146, v5\n" \
  "v_max_f32 v147, v147, v6\n" \
  "v_mul_f32 v146, s89, v146\n" \
  "v_mul_f32 v147, s89, v147\n" \
  "v_max_f32 v146, v146, v144\n" \
  "v_max_f32 v147, v147, v145\n" \
  "v_sub_f32 v160, v144, v146\n" \
  "v_sub_f32 v162, v145, v147\n" \
  "v_fma_f32 v150, v150, s89, -v146\n" \
  "v_fma_f32 v151, v151, s89, -v146\n" \
  "v_fma_f32 v154, v154, s89, -v147\n" \
  "v_fma_f32 v155, v155, s89, -v147\n" \
  "v_exp_f32 v150, v150\n" \
  "v_exp_f32 v151, v151\n" \
  "v_exp_f32 v154, v154\n" \
  "v_exp_f32 v155, v155\n" \
  "v_exp_f32 v160, v160\n" \
  "v_exp_f32 v162, v162\n" \
  "v_add_f32 v146, v150, v151\n" \
  "v_add_f32 v147, v154, v155\n" \
  "v_mov_b32 v5, v146\n" \
  "v_mov_b32 v6, v147\n" \
  "s_nop 0\n" \
  "v_permlane16_swap_b32 v146, v5\n" \
  "v_permlane16_swap_b32 v147, v6\n" \
  "v_add_f32 v146, v146, v5\n" \
  "v_add_f32 v147, v147, v6\n" \
  "v_mov_b32 v5, v146\n" \
  "v_mov_b32 v6, v147\n" \
  "s_nop 0\n" \
  "v_permlane32_swap_b32 v146, v5\n" \
  "v_permlane32_swap_b32 v147, v6\n" \
  "v_add_f32 v146, v146, v5\n" \
  "v_add_f32 v147, v147, v6\n" \
  "v_fma_f32 v146, v160, v136, v146\n" \
  "v_fma_f32 v147, v162, v140, v147\n" \
  "v_rcp_f32 v146, v146\n" \
  "v_rcp_f32 v147, v147\n" \
  "v_mul_f32 v160, v160, v146\n" \
  "v_mul_f32 v162, v162, v147\n" \
  "v_mul_f32 v150, v150, v146\n" \
  "v_mul_f32 v151, v151, v146\n" \
  "v_mul_f32 v154, v154, v147\n" \
  "v_mul_f32 v155, v155, v147\n" \
  "v_cvt_pk_bf16_f32 v157, v150, v151\n" \
  "v_cvt_pk_bf16_f32 v159, v154, v155\n" \
  "v_pk_mul_f32 v[52:53], v[52:53], v[160:161] op_sel_hi:[1,0]\n" \
  "v_pk_mul_f32 v[54:55], v[54:55], v[160:161] op_sel_hi:[1,0]\n" \
  "v_pk_mul_f32 v[56:57], v[56:57], v[160:161] op_sel_hi:[1,0]\n" \
  "v_pk_mul_f32 v[58:59], v[58:59], v[160:161] op_sel_hi:[1,0]\n" \
  "v_pk_mul_f32 v[60:61], v[60:61], v[160:161] op_sel_hi:[1,0]\n" \
  "v_pk_mul_f32 v[62:63], v[62:63], v[160:161] op_sel_hi:[1,0]\n" \
  "v_pk_mul_f32 v[64:65], v[64:65], v[160:161] op_sel_hi:[1,0]\n" \
  "v_pk_mul_f32 v[66:67], v[66:67], v[160:161] op_sel_hi:[1,0]\n" \
  "v_pk_mul_f32 v[68:69], v[68:69], v[160:161] op_sel_hi:[1,0]\n" \
  "v_pk_mul_f32 v[70:71], v[70:71], v[160:161] op_sel_hi:[1,0]\n" \
  "v_pk_mul_f32 v[72:73], v[72:73], v[160:161] op_sel_hi:[1,0]\n" \
  "v_pk_mul_f32 v[74:75], v[74:75], v[160:161] op_sel_hi:[1,0]\n" \
  "v_pk_mul_f32 v[76:77], v[76:77], v[160:161] op_sel_hi:[1,0]\n" \
  "v_pk_mul_f32 v[78:79], v[78:79], v[160:161] op_sel_hi:[1,0]\n" \
  "v_pk_mul_f32 v[80:81], v[80:81], v[160:161] op_sel_hi:[1,0]\n" \
  "v_pk_mul_f32 v[82:83], v[82:83], v[160:161] op_sel_hi:[1,0]\n" \
  "v_pk_mul_f32 v[84:85], v[84:85], v[162:163] op_sel_hi:[1,0]\n" \
  "v_pk_mul_f32 v[86:87], v[86:87], v[162:163] op_sel_hi:[1,0]\n" \
  "v_pk_mul_f32 v[88:89], v[88:89], v[162:163] op_sel_hi:[1,0]\n" \
  "v_pk_mul_f32 v[90:91], v[90:91], v[162:163] op_sel_hi:[1,0]\n" \
  "v_pk_mul_f32 v[92:93], v[92:93], v[162:163] op_sel_hi:[1,0]\n" \
  "v_pk_mul_f32 v[94:95], v[94:95], v[162:163] op_sel_hi:[1,0]\n" \
  "v_pk_mul_f32 v[96:97], v[96:97], v[162:163] op_sel_hi:[1,0]\n" \
  "v_pk_mul_f32 v[98:99], v[98:99], v[162:163] op_sel_hi:[1,0]\n" \
  "v_pk_mul_f32 v[100:101], v[100:101], v[162:163] op_sel_hi:[1,0]\n" \
  "v_pk_mul_f32 v[102:103], v[102:103], v[162:163] op_sel_hi:[1,0]\n" \
  "v_pk_mul_f32 v[104:105], v[104:105], v[162:163] op_sel_hi:[1,0]\n" \
  "v_pk_mul_f32 v[106:107], v[106:107], v[162:163] op_sel_hi:[1,0]\n" \
  "v_pk_mul_f32 v[108:109], v[108:109], v[162:163] op_sel_hi:[1,0]\n" \
  "v_pk_mul_f32 v[110:111], v[110:111], v[162:163] op_sel_hi:[1,0]\n" \
  "v_pk_mul_f32 v[112:113], v[112:113], v[162:163] op_sel_hi:[1,0]\n" \
  "v_pk_mul_f32 v[114:115], v[114:115], v[162:163] op_sel_hi:[1,0]\n" \
  "s_waitcnt lgkmcnt(0)\n" \
  "v_mfma_f32_16x16x16_bf16 v[52:55], a[128:129], v[156:157], v[52:55]\n" \
  "v_mfma_f32_16x16x16_bf16 v[56:59], a[130:131], v[156:157], v[56:59]\n" \
  "v_mfma_f32_16x16x16_bf16 v[60:63], a[132:133], v[156:157], v[60:63]\n" \
  "v_mfma_f32_16x16x16_bf16 v[64:67], a[134:135], v[156:157], v[64:67]\n" \
  "v_mfma_f32_16x16x16_bf16 v[68:71], a[136:137], v[156:157], v[68:71]\n" \
  "v_cvt_pk_bf16_f32 v164, v52, v53\n" \
  "v_cvt_pk_bf16_f32 v165, v54, v55\n" \
  "v_mfma_f32_16x16x16_bf16 v[72:75], a[138:139], v[156:157], v[72:75]\n" \
  "v_cvt_pk_bf16_f32 v166, v56, v57\n" \
  "v_cvt_pk_bf16_f32 v167, v58, v59\n" \
  "buffer_store_dwordx4 v[164:167], v7, s[72:75], s86 offen offset:0 sc1\n" \
  "v_mfma_f32_16x16x16_bf16 v[76:79], a[140:141], v[156:157], v[76:79]\n" \
  "v_cvt_pk_bf16_f32 v168, v60, v61\n" \
  "v_cvt_pk_bf16_f32 v169, v62, v63\n" \
  "v_mfma_f32_16x16x16_bf16 v[80:83], a[142:143], v[156:157], v[80:83]\n" \
  "v_cvt_pk_bf16_f32 v170, v64, v65\n" \
  "v_cvt_pk_bf16_f32 v171, v66, v67\n" \
  "buffer_store_dwordx4 v[168:171], v7, s[72:75], s86 offen offset:1024 sc1\n" \
  "v_mfma_f32_16x16x16_bf16 v[84:87], a[128:129], v[158:159], v[84:87]\n" \
  "v_cvt_pk_bf16_f32 v172, v68, v69\n" \
  "v_cvt_pk_bf16_f32 v173, v70, v71\n" \
  "v_mfma_f32_16x16x16_bf16 v[88:91], a[130:131], v[158:159], v[88:91]\n" \
  "v_cvt_pk_bf16_f32 v174, v72, v73\n" \
  "v_cvt_pk_bf16_f32 v175, v74, v75\n" \
  "buffer_store_dwordx4 v[172:175], v7, s[72:75], s86 offen offset:2048 sc1\n" \
  "v_mfma_f32_16x16x16_bf16 v[92:95], a[132:133], v[158:159], v[92:95]\n" \
  "v_cvt_pk_bf16_f32 v176, v76, v77\n" \
  "v_cvt_pk_bf16_f32 v177, v78, v79\n" \
  "v_mfma_f32_16x16x16_bf16 v[96:99], a[134:135], v[158:159], v[96:99]\n" \
  "v_cvt_pk_bf16_f32 v178, v80, v81\n" \
  "v_cvt_pk_bf16_f32 v179, v82, v83\n" \
  "buffer_store_dwordx4 v[176:179], v7, s[72:75], s86 offen offset:3072 sc1\n" \
  "v_mfma_f32_16x16x16_bf16 v[100:103], a[136:137], v[158:159], v[100:103]\n" \
  "v_cvt_pk_bf16_f32 v180, v84, v85\n" \
  "v_cvt_pk_bf16_f32 v181, v86, v87\n" \
  "v_mfma_f32_16x16x16_bf16 v[104:107], a[138:139], v[158:159], v[104:107]\n" \
  "v_cvt_pk_bf16_f32 v182, v88, v89\n" \
  "v_cvt_pk_bf16_f32 v183, v90, v91\n" \
  "buffer_store_dwordx4 v[180:183], v17, s[72:75], s86 offen offset:0 sc1\n" \
  "v_mfma_f32_16x16x16_bf16 v[108:111], a[140:141], v[158:159], v[108:111]\n" \
  "v_cvt_pk_bf16_f32 v184, v92, v93\n" \
  "v_cvt_pk_bf16_f32 v185, v94, v95\n" \
  "v_mfma_f32_16x16x16_bf16 v[112:115], a[142:143], v[158:159], v[112:115]\n" \
  "v_cvt_pk_bf16_f32 v186, v96, v97\n" \
  "v_cvt_pk_bf16_f32 v187, v98, v99\n" \
  "buffer_store_dwordx4 v[184:187], v17, s[72:75], s86 offen offset:1024 sc1\n" \
  "v_cvt_pk_bf16_f32 v188, v100, v101\n" \
  "v_cvt_pk_bf16_f32 v189, v102, v103\n" \
  "v_cvt_pk_bf16_f32 v190, v104, v105\n" \
  "v_cvt_pk_bf16_f32 v191, v106, v107\n" \
  "buffer_store_dwordx4 v[188:191], v17, s[72:75], s86 offen offset:2048 sc1\n" \
  "s_nop 1\n" \
  "v_cvt_pk_bf16_f32 v192, v108, v109\n" \
  "v_cvt_pk_bf16_f32 v193, v110, v111\n" \
  "v_cvt_pk_bf16_f32 v194, v112, v113\n" \
  "v_cvt_pk_bf16_f32 v195, v114, v115\n" \
  "buffer_store_dwordx4 v[192:195], v17, s[72:75], s86 offen offset:3072 sc1\n" \
  "s_add_u32 s82, s82, 1\n" \
  "s_cmp_eq_u32 s82, 64\n" \
  "s_cbranch_scc1 LZ_%=\n" \
  "s_lshl_b32 s87, s82, 19\n" \
  "s_add_u32 s87, s87, " STR(EXP_B) "<<21\n" \
  "s_xor_b32 s87, s87, %6\n" \
  "s_lshr_b32 s80, %3, 3\n" \
  "s_xor_b32 s80, s80, s82\n" \
  "s_and_b32 s80, s80, 7\n" \
  "s_mul_i32 s80, s80, 0x4800\n" \
  "s_and_b32 s80, s80, 0x7000\n" \
  "s_xor_b32 s87, s87, s80\n" \
  "s_and_b32 s80, %3, 7\n" \
  "s_lshl_b32 s80, s80, 16\n" \
  "s_or_b32 s87, s87, s80\n" \
  "s_waitcnt vmcnt(8)\n" \
  "v_readlane_b32 s91, v14, 0\n" \
  "v_readlane_b32 s92, v14, 1\n" \
  "v_readlane_b32 s80, v14, 2\n" \
  "s_min_u32 s92, s92, s80\n" \
  "v_readlane_b32 s80, v14, 3\n" \
  "s_min_u32 s92, s92, s80\n" \
  "v_readlane_b32 s80, v14, 4\n" \
  "s_min_u32 s92, s92, s80\n" \
  "s_cmp_eq_u32 s82, 63\n" \
  "s_addc_u32 s91, s91, 0\n" \
  "s_cmp_gt_u32 s91, s82\n" \
  "s_cselect_b32 s90, 1, 0\n" \
  "s_cselect_b32 s80, 0, 0x80000000\n" \
  "v_or_b32 v9, s80, v7\n" \
  "v_or_b32 v10, s80, v18\n" \
  "buffer_load_dwordx4 v[28:31], v9, s[68:71], s87 offen offset:0 sc1\n" \
  "buffer_load_dwordx4 v[32:35], v9, s[68:71], s87 offen offset:1024 sc1\n" \
  "buffer_load_dwordx4 v[36:39], v9, s[68:71], s87 offen offset:2048 sc1\n" \
  "buffer_load_dwordx4 v[40:43], v9, s[68:71], s87 offen offset:3072 sc1\n" \
  "buffer_load_dwordx4 v[44:47], v10, s[68:71], s87 offen offset:0 sc1\n" \
  "buffer_load_dwordx4 v[48:51], v10, s[68:71], s87 offen offset:1024 sc1\n" \
  "s_branch LA_%=\n" \
  "LG0_%=:\n" \
  "buffer_load_dword v14, v13, s[76:79], 0 offen sc1\n" \
  "s_waitcnt vmcnt(0)\n" \
  "v_readlane_b32 s91, v14, 0\n" \
  "v_readlane_b32 s92, v14, 1\n" \
  "v_readlane_b32 s80, v14, 2\n" \
  "s_min_u32 s92, s92, s80\n" \
  "v_readlane_b32 s80, v14, 3\n" \
  "s_min_u32 s92, s92, s80\n" \
  "v_readlane_b32 s80, v14, 4\n" \
  "s_min_u32 s92, s92, s80\n" \
  "s_cmp_ge_u32 s92, s81\n" \
  "s_cbranch_scc1 LH0_%=\n" \
  "s_sleep 4\n" \
  "s_add_u32 s88, s88, 1\n" \
  "s_cmp_lt_u32 s88, " STR(SPIN_CAP) "\n" \
  "s_cbranch_scc1 LG0_%=\n" \
  "v_mov_b32 v5, " STR(CW_DEAD) "*4\n" \
  "v_mov_b32 v6, 1\n" \
  "buffer_store_dword v6, v5, s[76:79], 0 offen sc1\n" \
  "s_branch LH0_%=\n" \
  "LW0_%=:\n" \
  "s_add_u32 s81, s82, 1\n" \
  "LQ0_%=:\n" \
  "buffer_load_dword v14, v13, s[76:79], 0 offen sc1\n" \
  "s_waitcnt vmcnt(0)\n" \
  "v_readlane_b32 s91, v14, 0\n" \
  "v_readlane_b32 s92, v14, 1\n" \
  "v_readlane_b32 s80, v14, 2\n" \
  "s_min_u32 s92, s92, s80\n" \
  "v_readlane_b32 s80, v14, 3\n" \
  "s_min_u32 s92, s92, s80\n" \
  "v_readlane_b32 s80, v14, 4\n" \
  "s_min_u32 s92, s92, s80\n" \
  "s_cmp_ge_u32 s91, s81\n" \
  "s_cbranch_scc1 LR0_%=\n" \
  "s_sleep 2\n" \
  "s_add_u32 s88, s88, 1\n" \
  "s_cmp_lt_u32 s88, " STR(SPIN_CAP) "\n" \
  "s_cbranch_scc1 LQ0_%=\n" \
  "v_mov_b32 v5, " STR(CW_DEAD) "*4\n" \
  "v_mov_b32 v6, 1\n" \
  "buffer_store_dword v6, v5, s[76:79], 0 offen sc1\n" \
  "s_branch LR0_%=\n" \
  "LR0_%=:\n" \
  "buffer_load_dwordx4 v[28:31], v7, s[68:71], s87 offen offset:0 sc1\n" \
  "buffer_load_dwordx4 v[32:35], v7, s[68:71], s87 offen offset:1024 sc1\n" \
  "buffer_load_dwordx4 v[36:39], v7, s[68:71], s87 offen offset:2048 sc1\n" \
  "buffer_load_dwordx4 v[40:43], v7, s[68:71], s87 offen offset:3072 sc1\n" \
  "buffer_load_dwordx4 v[44:47], v18, s[68:71], s87 offen offset:0 sc1\n" \
  "buffer_load_dwordx4 v[48:51], v18, s[68:71], s87 offen offset:1024 sc1\n" \
  "s_waitcnt vmcnt(0)\n" \
  "v_max3_u32 v5, v28, v29, v30\n" \
  "v_max3_u32 v5, v5, v31, v32\n" \
  "v_max3_u32 v5, v5, v33, v34\n" \
  "v_max3_u32 v5, v5, v35, v36\n" \
  "v_max3_u32 v5, v5, v37, v38\n" \
  "v_max3_u32 v5, v5, v39, v40\n" \
  "v_max3_u32 v5, v5, v41, v42\n" \
  "v_max3_u32 v5, v5, v43, v44\n" \
  "v_max3_u32 v5, v5, v45, v46\n" \
  "v_max3_u32 v5, v5, v47, v48\n" \
  "v_max3_u32 v5, v5, v49, v50\n" \
  "v_max_u32 v5, v5, v51\n" \
  "v_cmp_eq_u32 vcc, -1, v5\n" \
  "s_cbranch_vccz LU0_%=\n" \
  "s_sleep 1\n" \
  "s_add_u32 s88, s88, 1\n" \
  "s_cmp_lt_u32 s88, " STR(SPIN_CAP) "\n" \
  "s_cbranch_scc1 LR0_%=\n" \
  "v_mov_b32 v5, " STR(CW_DEAD) "*4\n" \
  "v_mov_b32 v6, 1\n" \
  "buffer_store_dword v6, v5, s[76:79], 0 offen sc1\n" \
  "s_branch LV0_%=\n" \
  "LV0_%=:\n" \
  "v_mov_b64 v[28:29], 0\n" \
  "v_mov_b64 v[30:31], 0\n" \
  "v_mov_b64 v[32:33], 0\n" \
  "v_mov_b64 v[34:35], 0\n" \
  "v_mov_b64 v[36:37], 0\n" \
  "v_mov_b64 v[38:39], 0\n" \
  "v_mov_b64 v[40:41], 0\n" \
  "v_mov_b64 v[42:43], 0\n" \
  "v_mov_b64 v[44:45], 0\n" \
  "v_mov_b64 v[46:47], 0\n" \
  "v_mov_b64 v[48:49], 0\n" \
  "v_mov_b64 v[50:51], 0\n" \
  "LU0_%=:\n" \
  "s_nop 1\n" \
  "s_branch LM0_%=\n" \
  "LG1_%=:\n" \
  "buffer_load_dword v14, v13, s[76:79], 0 offen sc1\n" \
  "s_waitcnt vmcnt(0)\n" \
  "v_readlane_b32 s91, v14, 0\n" \
  "v_readlane_b32 s92, v14, 1\n" \
  "v_readlane_b32 s80, v14, 2\n" \
  "s_min_u32 s92, s92, s80\n" \
  "v_readlane_b32 s80, v14, 3\n" \
  "s_min_u32 s92, s92, s80\n" \
  "v_readlane_b32 s80, v14, 4\n" \
  "s_min_u32 s92, s92, s80\n" \
  "s_cmp_ge_u32 s92, s81\n" \
  "s_cbranch_scc1 LH1_%=\n" \
  "s_sleep 4\n" \
  "s_add_u32 s88, s88, 1\n" \
  "s_cmp_lt_u32 s88, " STR(SPIN_CAP) "\n" \
  "s_cbranch_scc1 LG1_%=\n" \
  "v_mov_b32 v5, " STR(CW_DEAD) "*4\n" \
  "v_mov_b32 v6, 1\n" \
  "buffer_store_dword v6, v5, s[76:79], 0 offen sc1\n" \
  "s_branch LH1_%=\n" \
  "LW1_%=:\n" \
  "s_add_u32 s81, s82, 1\n" \
  "LQ1_%=:\n" \
  "buffer_load_dword v14, v13, s[76:79], 0 offen sc1\n" \
  "s_waitcnt vmcnt(0)\n" \
  "v_readlane_b32 s91, v14, 0\n" \
  "v_readlane_b32 s92, v14, 1\n" \
  "v_readlane_b32 s80, v14, 2\n" \
  "s_min_u32 s92, s92, s80\n" \
  "v_readlane_b32 s80, v14, 3\n" \
  "s_min_u32 s92, s92, s80\n" \
  "v_readlane_b32 s80, v14, 4\n" \
  "s_min_u32 s92, s92, s80\n" \
  "s_cmp_ge_u32 s91, s81\n" \
  "s_cbranch_scc1 LR1_%=\n" \
  "s_sleep 2\n" \
  "s_add_u32 s88, s88, 1\n" \
  "s_cmp_lt_u32 s88, " STR(SPIN_CAP) "\n" \
  "s_cbranch_scc1 LQ1_%=\n" \
  "v_mov_b32 v5, " STR(CW_DEAD) "*4\n" \
  "v_mov_b32 v6, 1\n" \
  "buffer_store_dword v6, v5, s[76:79], 0 offen sc1\n" \
  "s_branch LR1_%=\n" \
  "LR1_%=:\n" \
  "buffer_load_dwordx4 v[28:31], v7, s[68:71], s87 offen offset:0 sc1\n" \
  "buffer_load_dwordx4 v[32:35], v7, s[68:71], s87 offen offset:1024 sc1\n" \
  "buffer_load_dwordx4 v[36:39], v7, s[68:71], s87 offen offset:2048 sc1\n" \
  "buffer_load_dwordx4 v[40:43], v7, s[68:71], s87 offen offset:3072 sc1\n" \
  "buffer_load_dwordx4 v[44:47], v18, s[68:71], s87 offen offset:0 sc1\n" \
  "buffer_load_dwordx4 v[48:51], v18, s[68:71], s87 offen offset:1024 sc1\n" \
  "s_waitcnt vmcnt(0)\n" \
  "v_max3_u32 v5, v28, v29, v30\n" \
  "v_max3_u32 v5, v5, v31, v32\n" \
  "v_max3_u32 v5, v5, v33, v34\n" \
  "v_max3_u32 v5, v5, v35, v36\n" \
  "v_max3_u32 v5, v5, v37, v38\n" \
  "v_max3_u32 v5, v5, v39, v40\n" \
  "v_max3_u32 v5, v5, v41, v42\n" \
  "v_max3_u32 v5, v5, v43, v44\n" \
  "v_max3_u32 v5, v5, v45, v46\n" \
  "v_max3_u32 v5, v5, v47, v48\n" \
  "v_max3_u32 v5, v5, v49, v50\n" \
  "v_max_u32 v5, v5, v51\n" \
  "v_cmp_eq_u32 vcc, -1, v5\n" \
  "s_cbranch_vccz LU1_%=\n" \
  "s_sleep 1\n" \
  "s_add_u32 s88, s88, 1\n" \
  "s_cmp_lt_u32 s88, " STR(SPIN_CAP) "\n" \
  "s_cbranch_scc1 LR1_%=\n" \
  "v_mov_b32 v5, " STR(CW_DEAD) "*4\n" \
  "v_mov_b32 v6, 1\n" \
  "buffer_store_dword v6, v5, s[76:79], 0 offen sc1\n" \
  "s_branch LV1_%=\n" \
  "LV1_%=:\n" \
  "v_mov_b64 v[28:29], 0\n" \
  "v_mov_b64 v[30:31], 0\n" \
  "v_mov_b64 v[32:33], 0\n" \
  "v_mov_b64 v[34:35], 0\n" \
  "v_mov_b64 v[36:37], 0\n" \
  "v_mov_b64 v[38:39], 0\n" \
  "v_mov_b64 v[40:41], 0\n" \
  "v_mov_b64 v[42:43], 0\n" \
  "v_mov_b64 v[44:45], 0\n" \
  "v_mov_b64 v[46:47], 0\n" \
  "v_mov_b64 v[48:49], 0\n" \
  "v_mov_b64 v[50:51], 0\n" \
  "LU1_%=:\n" \
  "s_nop 1\n" \
  "s_branch LM1_%=\n" \
  "LZ_%=:\n" \
  "s_waitcnt vmcnt(0)\n" \
  "buffer_store_dword v16, v15, s[76:79], 0 offen sc1\n" \
  "s_waitcnt vmcnt(0)\n" \
  ""

#define CONV_ASM \
  "s_waitcnt lgkmcnt(0)\n" \
  "ds_read_b128 v[176:179], %7 offset:0\n" \
  "ds_read_b128 v[180:183], %7 offset:16\n" \
  "ds_read_b128 v[184:187], %7 offset:32\n" \
  "ds_read_b128 v[188:191], %7 offset:48\n" \
  "ds_read_b64 a[0:1], %5 offset:32\n" \
  "ds_read_b64 a[2:3], %5 offset:40\n" \
  "ds_read_b128 a[4:7], %6 offset:2880\n" \
  "ds_read_b128 a[8:11], %6 offset:320\n" \
  "ds_read_b64 a[12:13], %5 offset:2080\n" \
  "ds_read_b64 a[14:15], %5 offset:2088\n" \
  "ds_read_b64 a[16:17], %5 offset:0\n" \
  "ds_read_b64 a[18:19], %5 offset:8\n" \
  "ds_read_b128 a[20:23], %6 offset:2912\n" \
  "ds_read_b128 a[24:27], %6 offset:352\n" \
  "ds_read_b64 a[28:29], %5 offset:2048\n" \
  "ds_read_b64 a[30:31], %5 offset:2056\n" \
  "v_mov_b64 v[192:193], 0\n" \
  "v_mov_b64 v[194:195], 0\n" \
  "v_mov_b64 v[196:197], 0\n" \
  "v_mov_b64 v[198:199], 0\n" \
  "v_mov_b64 v[200:201], 0\n" \
  "v_mov_b64 v[202:203], 0\n" \
  "v_mov_b64 v[204:205], 0\n" \
  "v_mov_b64 v[206:207], 0\n" \
  "v_mov_b64 v[208:209], 0\n" \
  "v_mov_b64 v[210:211], 0\n" \
  "v_mov_b64 v[212:213], 0\n" \
  "v_mov_b64 v[214:215], 0\n" \
  "v_mov_b64 v[216:217], 0\n" \
  "v_mov_b64 v[218:219], 0\n" \
  "v_mov_b64 v[220:221], 0\n" \
  "v_mov_b64 v[222:223], 0\n" \
  "s_movk_i32 s68, 8\n" \
  "s_mov_b32 s69, 0xffff0000\n" \
  ".p2align 5\n" \
  "LC_%=:\n" \
  "s_waitcnt lgkmcnt(6)\n" \
  "v_mfma_f32_32x32x16_bf16 %1, a[0:3], a[4:7], %1\n" \
  "ds_read_b64 a[32:33], %5 offset:96\n" \
  "ds_read_b64 a[34:35], %5 offset:104\n" \
  "v_mul_f32 v224, v176, %11\n" \
  "v_mul_f32 v225, v177, %11\n" \
  "v_mul_f32 v228, v176, %8\n" \
  "v_mfma_f32_16x16x32_bf16 %2, v[196:199], v[192:195], %2\n" \
  "v_mul_f32 v229, v177, %8\n" \
  "v_mul_f32 v232, v176, %9\n" \
  "v_mul_f32 v233, v177, %9\n" \
  "v_mfma_f32_16x16x32_bf16 %3, v[200:203], v[192:195], %3\n" \
  "v_mul_f32 v236, v176, %10\n" \
  "v_mul_f32 v237, v177, %10\n" \
  "v_exp_f32 v224, v224\n" \
  "v_mfma_f32_16x16x32_bf16 %4, v[204:207], v[192:195], %4\n" \
  "v_mfma_f32_32x32x16_bf16 %0, a[0:3], a[8:11], %0\n" \
  "ds_read_b128 a[36:39], %6 offset:2800\n" \
  "ds_read_b128 a[40:43], %6 offset:240\n" \
  "v_fract_f32 v225, v225\n" \
  "v_exp_f32 v228, v228\n" \
  "v_fract_f32 v229, v229\n" \
  "v_mfma_f32_16x16x32_bf16 %2, v[212:215], v[192:195], %2\n" \
  "v_exp_f32 v232, v232\n" \
  "v_fract_f32 v233, v233\n" \
  "v_exp_f32 v236, v236\n" \
  "v_mfma_f32_16x16x32_bf16 %3, v[216:219], v[192:195], %3\n" \
  "v_fract_f32 v237, v237\n" \
  "v_cos_f32 v226, v225\n" \
  "v_sin_f32 v227, v225\n" \
  "v_mfma_f32_16x16x32_bf16 %4, v[220:223], v[192:195], %4\n" \
  "v_mfma_f32_32x32x16_bf16 %1, a[12:15], a[8:11], %1\n" \
  "ds_read_b64 a[44:45], %5 offset:2144\n" \
  "ds_read_b64 a[46:47], %5 offset:2152\n" \
  "v_cos_f32 v230, v229\n" \
  "v_sin_f32 v231, v229\n" \
  "v_cos_f32 v234, v233\n" \
  "v_mfma_f32_16x16x32_bf16 %2, v[196:199], v[208:211], %2\n" \
  "v_sin_f32 v235, v233\n" \
  "v_cos_f32 v238, v237\n" \
  "v_sin_f32 v239, v237\n" \
  "v_mfma_f32_16x16x32_bf16 %3, v[200:203], v[208:211], %3\n" \
  "v_mul_f32 v226, v224, v226\n" \
  "v_mul_f32 v227, v224, v227\n" \
  "v_mul_f32 v230, v228, v230\n" \
  "v_mfma_f32_16x16x32_bf16 %4, v[204:207], v[208:211], %4\n" \
  "s_waitcnt lgkmcnt(6)\n" \
  "v_mfma_f32_32x32x16_bf16 %1, a[16:19], a[20:23], %1\n" \
  "ds_read_b64 a[48:49], %5 offset:64\n" \
  "ds_read_b64 a[50:51], %5 offset:72\n" \
  "v_mul_f32 v231, v228, v231\n" \
  "v_mul_f32 v234, v232, v234\n" \
  "v_mul_f32 v235, v232, v235\n" \
  "v_mul_f32 v238, v236, v238\n" \
  "v_mul_f32 v239, v236, v239\n" \
  "v_mul_f32 v224, v179, v227\n" \
  "v_mul_f32 v225, v179, v226\n" \
  "v_fma_f32 v226, v178, v226, -v224\n" \
  "v_fma_f32 v227, -v178, v227, -v225\n" \
  "ds_read_b128 v[176:179], %7 offset:256\n" \
  "v_cvt_pk_bf16_f32 v196, v230, v231\n" \
  "v_cvt_pk_bf16_f32 v200, v234, v235\n" \
  "v_mfma_f32_32x32x16_bf16 %0, a[16:19], a[24:27], %0\n" \
  "ds_read_b128 a[52:55], %6 offset:2832\n" \
  "ds_read_b128 a[56:59], %6 offset:272\n" \
  "v_cvt_pk_bf16_f32 v204, v238, v239\n" \
  "v_cvt_pk_bf16_f32 v192, v226, v227\n" \
  "v_lshlrev_b32 v228, 16, v196\n" \
  "v_and_b32 v229, s69, v196\n" \
  "v_lshlrev_b32 v232, 16, v200\n" \
  "v_and_b32 v233, s69, v200\n" \
  "v_lshlrev_b32 v236, 16, v204\n" \
  "v_and_b32 v237, s69, v204\n" \
  "v_lshlrev_b32 v224, 16, v192\n" \
  "v_and_b32 v225, s69, v192\n" \
  "v_sub_f32 v230, v230, v228\n" \
  "v_sub_f32 v231, v231, v229\n" \
  "v_mfma_f32_32x32x16_bf16 %1, a[28:31], a[24:27], %1\n" \
  "ds_read_b64 a[60:61], %5 offset:2112\n" \
  "ds_read_b64 a[62:63], %5 offset:2120\n" \
  "v_sub_f32 v234, v234, v232\n" \
  "v_sub_f32 v235, v235, v233\n" \
  "v_sub_f32 v238, v238, v236\n" \
  "v_sub_f32 v239, v239, v237\n" \
  "v_sub_f32 v226, v226, v224\n" \
  "v_sub_f32 v227, v227, v225\n" \
  "v_cvt_pk_bf16_f32 v212, v230, v231\n" \
  "v_cvt_pk_bf16_f32 v216, v234, v235\n" \
  "v_cvt_pk_bf16_f32 v220, v238, v239\n" \
  "v_cvt_pk_bf16_f32 v208, v226, v227\n" \
  "s_waitcnt lgkmcnt(7)\n" \
  "v_mfma_f32_32x32x16_bf16 %1, a[32:35], a[36:39], %1\n" \
  "ds_read_b64 a[0:1], %5 offset:160\n" \
  "ds_read_b64 a[2:3], %5 offset:168\n" \
  "v_mul_f32 v224, v180, %11\n" \
  "v_mul_f32 v225, v181, %11\n" \
  "v_mul_f32 v228, v180, %8\n" \
  "v_mul_f32 v229, v181, %8\n" \
  "v_mul_f32 v232, v180, %9\n" \
  "v_mul_f32 v233, v181, %9\n" \
  "v_mul_f32 v236, v180, %10\n" \
  "v_mul_f32 v237, v181, %10\n" \
  "v_exp_f32 v224, v224\n" \
  "v_fract_f32 v225, v225\n" \
  "v_exp_f32 v228, v228\n" \
  "v_mfma_f32_32x32x16_bf16 %0, a[32:35], a[40:43], %0\n" \
  "ds_read_b128 a[4:7], %6 offset:2720\n" \
  "ds_read_b128 a[8:11], %6 offset:160\n" \
  "v_fract_f32 v229, v229\n" \
  "v_exp_f32 v232, v232\n" \
  "v_fract_f32 v233, v233\n" \
  "v_exp_f32 v236, v236\n" \
  "v_fract_f32 v237, v237\n" \
  "v_cos_f32 v226, v225\n" \
  "v_sin_f32 v227, v225\n" \
  "v_cos_f32 v230, v229\n" \
  "v_sin_f32 v231, v229\n" \
  "v_cos_f32 v234, v233\n" \
  "v_sin_f32 v235, v233\n" \
  "v_mfma_f32_32x32x16_bf16 %1, a[44:47], a[40:43], %1\n" \
  "ds_read_b64 a[12:13], %5 offset:2208\n" \
  "ds_read_b64 a[14:15], %5 offset:2216\n" \
  "v_cos_f32 v238, v237\n" \
  "v_sin_f32 v239, v237\n" \
  "v_mul_f32 v226, v224, v226\n" \
  "v_mul_f32 v227, v224, v227\n" \
  "v_mul_f32 v230, v228, v230\n" \
  "v_mul_f32 v231, v228, v231\n" \
  "v_mul_f32 v234, v232, v234\n" \
  "v_mul_f32 v235, v232, v235\n" \
  "v_mul_f32 v238, v236, v238\n" \
  "v_mul_f32 v239, v236, v239\n" \
  "v_mul_f32 v224, v183, v227\n" \
  "s_waitcnt lgkmcnt(6)\n" \
  "v_mfma_f32_32x32x16_bf16 %1, a[48:51], a[52:55], %1\n" \
  "ds_read_b64 a[16:17], %5 offset:128\n" \
  "ds_read_b64 a[18:19], %5 offset:136\n" \
  "v_mul_f32 v225, v183, v226\n" \
  "v_fma_f32 v226, v182, v226, -v224\n" \
  "v_fma_f32 v227, -v182, v227, -v225\n" \
  "ds_read_b128 v[180:183], %7 offset:272\n" \
  "v_cvt_pk_bf16_f32 v197, v230, v231\n" \
  "v_cvt_pk_bf16_f32 v201, v234, v235\n" \
  "v_cvt_pk_bf16_f32 v205, v238, v239\n" \
  "v_cvt_pk_bf16_f32 v193, v226, v227\n" \
  "v_lshlrev_b32 v228, 16, v197\n" \
  "v_and_b32 v229, s69, v197\n" \
  "v_lshlrev_b32 v232, 16, v201\n" \
  "v_mfma_f32_32x32x16_bf16 %0, a[48:51], a[56:59], %0\n" \
  "ds_read_b128 a[20:23], %6 offset:2752\n" \
  "ds_read_b128 a[24:27], %6 offset:192\n" \
  "v_and_b32 v233, s69, v201\n" \
  "v_lshlrev_b32 v236, 16, v205\n" \
  "v_and_b32 v237, s69, v205\n" \
  "v_lshlrev_b32 v224, 16, v193\n" \
  "v_and_b32 v225, s69, v193\n" \
  "v_sub_f32 v230, v230, v228\n" \
  "v_sub_f32 v231, v231, v229\n" \
  "v_sub_f32 v234, v234, v232\n" \
  "v_sub_f32 v235, v235, v233\n" \
  "v_sub_f32 v238, v238, v236\n" \
  "v_sub_f32 v239, v239, v237\n" \
  "v_mfma_f32_32x32x16_bf16 %1, a[60:63], a[56:59], %1\n" \
  "ds_read_b64 a[28:29], %5 offset:2176\n" \
  "ds_read_b64 a[30:31], %5 offset:2184\n" \
  "v_sub_f32 v226, v226, v224\n" \
  "v_sub_f32 v227, v227, v225\n" \
  "v_cvt_pk_bf16_f32 v213, v230, v231\n" \
  "v_cvt_pk_bf16_f32 v217, v234, v235\n" \
  "v_cvt_pk_bf16_f32 v221, v238, v239\n" \
  "v_cvt_pk_bf16_f32 v209, v226, v227\n" \
  "s_waitcnt lgkmcnt(7)\n" \
  "v_mfma_f32_32x32x16_bf16 %1, a[0:3], a[4:7], %1\n" \
  "ds_read_b64 a[32:33], %5 offset:224\n" \
  "ds_read_b64 a[34:35], %5 offset:232\n" \
  "v_mul_f32 v224, v184, %11\n" \
  "v_mul_f32 v225, v185, %11\n" \
  "v_mul_f32 v228, v184, %8\n" \
  "v_mul_f32 v229, v185, %8\n" \
  "v_mul_f32 v232, v184, %9\n" \
  "v_mul_f32 v233, v185, %9\n" \
  "v_mul_f32 v236, v184, %10\n" \
  "v_mul_f32 v237, v185, %10\n" \
  "v_exp_f32 v224, v224\n" \
  "v_fract_f32 v225, v225\n" \
  "v_exp_f32 v228, v228\n" \
  "v_mfma_f32_32x32x16_bf16 %0, a[0:3], a[8:11], %0\n" \
  "ds_read_b128 a[36:39], %6 offset:2640\n" \
  "ds_read_b128 a[40:43], %6 offset:80\n" \
  "v_fract_f32 v229, v229\n" \
  "v_exp_f32 v232, v232\n" \
  "v_fract_f32 v233, v233\n" \
  "v_exp_f32 v236, v236\n" \
  "v_fract_f32 v237, v237\n" \
  "v_cos_f32 v226, v225\n" \
  "v_sin_f32 v227, v225\n" \
  "v_cos_f32 v230, v229\n" \
  "v_sin_f32 v231, v229\n" \
  "v_cos_f32 v234, v233\n" \
  "v_sin_f32 v235, v233\n" \
  "v_mfma_f32_32x32x16_bf16 %1, a[12:15], a[8:11], %1\n" \
  "ds_read_b64 a[44:45], %5 offset:2272\n" \
  "ds_read_b64 a[46:47], %5 offset:2280\n" \
  "v_cos_f32 v238, v237\n" \
  "v_sin_f32 v239, v237\n" \
  "v_mul_f32 v226, v224, v226\n" \
  "v_mul_f32 v227, v224, v227\n" \
  "v_mul_f32 v230, v228, v230\n" \
  "v_mul_f32 v231, v228, v231\n" \
  "v_mul_f32 v234, v232, v234\n" \
  "v_mul_f32 v235, v232, v235\n" \
  "v_mul_f32 v238, v236, v238\n" \
  "v_mul_f32 v239, v236, v239\n" \
  "v_mul_f32 v224, v187, v227\n" \
  "s_waitcnt lgkmcnt(6)\n" \
  "v_mfma_f32_32x32x16_bf16 %1, a[16:19], a[20:23], %1\n" \
  "ds_read_b64 a[48:49], %5 offset:192\n" \
  "ds_read_b64 a[50:51], %5 offset:200\n" \
  "v_mul_f32 v225, v187, v226\n" \
  "v_fma_f32 v226, v186, v226, -v224\n" \
  "v_fma_f32 v227, -v186, v227, -v225\n" \
  "ds_read_b128 v[184:187], %7 offset:288\n" \
  "v_cvt_pk_bf16_f32 v198, v230, v231\n" \
  "v_cvt_pk_bf16_f32 v202, v234, v235\n" \
  "v_cvt_pk_bf16_f32 v206, v238, v239\n" \
  "v_cvt_pk_bf16_f32 v194, v226, v227\n" \
  "v_lshlrev_b32 v228, 16, v198\n" \
  "v_and_b32 v229, s69, v198\n" \
  "v_lshlrev_b32 v232, 16, v202\n" \
  "v_mfma_f32_32x32x16_bf16 %0, a[16:19], a[24:27], %0\n" \
  "ds_read_b128 a[52:55], %6 offset:2672\n" \
  "ds_read_b128 a[56:59], %6 offset:112\n" \
  "v_and_b32 v233, s69, v202\n" \
  "v_lshlrev_b32 v236, 16, v206\n" \
  "v_and_b32 v237, s69, v206\n" \
  "v_lshlrev_b32 v224, 16, v194\n" \
  "v_and_b32 v225, s69, v194\n" \
  "v_sub_f32 v230, v230, v228\n" \
  "v_sub_f32 v231, v231, v229\n" \
  "v_sub_f32 v234, v234, v232\n" \
  "v_sub_f32 v235, v235, v233\n" \
  "v_sub_f32 v238, v238, v236\n" \
  "v_sub_f32 v239, v239, v237\n" \
  "v_mfma_f32_32x32x16_bf16 %1, a[28:31], a[24:27], %1\n" \
  "ds_read_b64 a[60:61], %5 offset:2240\n" \
  "ds_read_b64 a[62:63], %5 offset:2248\n" \
  "v_sub_f32 v226, v226, v224\n" \
  "v_sub_f32 v227, v227, v225\n" \
  "v_cvt_pk_bf16_f32 v214, v230, v231\n" \
  "v_cvt_pk_bf16_f32 v218, v234, v235\n" \
  "v_cvt_pk_bf16_f32 v222, v238, v239\n" \
  "v_cvt_pk_bf16_f32 v210, v226, v227\n" \
  "s_waitcnt lgkmcnt(7)\n" \
  "v_mfma_f32_32x32x16_bf16 %1, a[32:35], a[36:39], %1\n" \
  "ds_read_b64 a[0:1], %5 offset:288\n" \
  "ds_read_b64 a[2:3], %5 offset:296\n" \
  "v_mul_f32 v224, v188, %11\n" \
  "v_mul_f32 v225, v189, %11\n" \
  "v_mul_f32 v228, v188, %8\n" \
  "v_mul_f32 v229, v189, %8\n" \
  "v_mul_f32 v232, v188, %9\n" \
  "v_mul_f32 v233, v189, %9\n" \
  "v_mul_f32 v236, v188, %10\n" \
  "v_mul_f32 v237, v189, %10\n" \
  "v_exp_f32 v224, v224\n" \
  "v_fract_f32 v225, v225\n" \
  "v_exp_f32 v228, v228\n" \
  "v_mfma_f32_32x32x16_bf16 %0, a[32:35], a[40:43], %0\n" \
  "ds_read_b128 a[4:7], %6 offset:2560\n" \
  "ds_read_b128 a[8:11], %6 offset:0\n" \
  "v_fract_f32 v229, v229\n" \
  "v_exp_f32 v232, v232\n" \
  "v_fract_f32 v233, v233\n" \
  "v_exp_f32 v236, v236\n" \
  "v_fract_f32 v237, v237\n" \
  "v_cos_f32 v226, v225\n" \
  "v_sin_f32 v227, v225\n" \
  "v_cos_f32 v230, v229\n" \
  "v_sin_f32 v231, v229\n" \
  "v_cos_f32 v234, v233\n" \
  "v_sin_f32 v235, v233\n" \
  "v_mfma_f32_32x32x16_bf16 %1, a[44:47], a[40:43], %1\n" \
  "ds_read_b64 a[12:13], %5 offset:2336\n" \
  "ds_read_b64 a[14:15], %5 offset:2344\n" \
  "v_cos_f32 v238, v237\n" \
  "v_sin_f32 v239, v237\n" \
  "v_mul_f32 v226, v224, v226\n" \
  "v_mul_f32 v227, v224, v227\n" \
  "v_mul_f32 v230, v228, v230\n" \
  "v_mul_f32 v231, v228, v231\n" \
  "v_mul_f32 v234, v232, v234\n" \
  "v_mul_f32 v235, v232, v235\n" \
  "v_mul_f32 v238, v236, v238\n" \
  "v_mul_f32 v239, v236, v239\n" \
  "v_mul_f32 v224, v191, v227\n" \
  "s_waitcnt lgkmcnt(6)\n" \
  "v_mfma_f32_32x32x16_bf16 %1, a[48:51], a[52:55], %1\n" \
  "ds_read_b64 a[16:17], %5 offset:256\n" \
  "ds_read_b64 a[18:19], %5 offset:264\n" \
  "v_mul_f32 v225, v191, v226\n" \
  "v_fma_f32 v226, v190, v226, -v224\n" \
  "v_fma_f32 v227, -v190, v227, -v225\n" \
  "ds_read_b128 v[188:191], %7 offset:304\n" \
  "v_cvt_pk_bf16_f32 v199, v230, v231\n" \
  "v_cvt_pk_bf16_f32 v203, v234, v235\n" \
  "v_cvt_pk_bf16_f32 v207, v238, v239\n" \
  "v_cvt_pk_bf16_f32 v195, v226, v227\n" \
  "v_lshlrev_b32 v228, 16, v199\n" \
  "v_and_b32 v229, s69, v199\n" \
  "v_lshlrev_b32 v232, 16, v203\n" \
  "v_mfma_f32_32x32x16_bf16 %0, a[48:51], a[56:59], %0\n" \
  "ds_read_b128 a[20:23], %6 offset:2592\n" \
  "ds_read_b128 a[24:27], %6 offset:32\n" \
  "v_and_b32 v233, s69, v203\n" \
  "v_lshlrev_b32 v236, 16, v207\n" \
  "v_and_b32 v237, s69, v207\n" \
  "v_lshlrev_b32 v224, 16, v195\n" \
  "v_and_b32 v225, s69, v195\n" \
  "v_sub_f32 v230, v230, v228\n" \
  "v_sub_f32 v231, v231, v229\n" \
  "v_sub_f32 v234, v234, v232\n" \
  "v_sub_f32 v235, v235, v233\n" \
  "v_sub_f32 v238, v238, v236\n" \
  "v_sub_f32 v239, v239, v237\n" \
  "v_mfma_f32_32x32x16_bf16 %1, a[60:63], a[56:59], %1\n" \
  "ds_read_b64 a[28:29], %5 offset:2304\n" \
  "ds_read_b64 a[30:31], %5 offset:2312\n" \
  "v_sub_f32 v226, v226, v224\n" \
  "v_sub_f32 v227, v227, v225\n" \
  "v_cvt_pk_bf16_f32 v215, v230, v231\n" \
  "v_cvt_pk_bf16_f32 v219, v234, v235\n" \
  "v_cvt_pk_bf16_f32 v223, v238, v239\n" \
  "v_cvt_pk_bf16_f32 v211, v226, v227\n" \
  "v_add_u32 %5, 256, %5\n" \
  "v_subrev_u32 %6, 320, %6\n" \
  "v_add_u32 %7, 256, %7\n" \
  "s_sub_u32 s68, s68, 1\n" \
  "s_cmp_lg_u32 s68, 0\n" \
  "s_cbranch_scc1 LC_%=\n" \
  "v_mfma_f32_16x16x32_bf16 %2, v[196:199], v[192:195], %2\n" \
  "v_mfma_f32_16x16x32_bf16 %3, v[200:203], v[192:195], %3\n" \
  "v_mfma_f32_16x16x32_bf16 %4, v[204:207], v[192:195], %4\n" \
  "v_mfma_f32_16x16x32_bf16 %2, v[212:215], v[192:195], %2\n" \
  "v_mfma_f32_16x16x32_bf16 %3, v[216:219], v[192:195], %3\n" \
  "v_mfma_f32_16x16x32_bf16 %4, v[220:223], v[192:195], %4\n" \
  "v_mfma_f32_16x16x32_bf16 %2, v[196:199], v[208:211], %2\n" \
  "v_mfma_f32_16x16x32_bf16 %3, v[200:203], v[208:211], %3\n" \
  "v_mfma_f32_16x16x32_bf16 %4, v[204:207], v[208:211], %4\n" \
  "s_waitcnt lgkmcnt(0)\n" \
  "s_nop 15\n" \
  ""

#define CONV_CLOBBERS "a0", "a1", "a2", "a3", "a4", "a5", "a6", "a7", "a8", "a9", "a10", "a11", "a12", "a13", "a14", "a15", "a16", "a17", "a18", "a19", "a20", "a21", "a22", "a23", "a24", "a25", "a26", "a27", "a28", "a29", "a30", "a31", "a32", "a33", "a34", "a35", "a36", "a37", "a38", "a39", "a40", "a41", "a42", "a43", "a44", "a45", "a46", "a47", "a48", "a49", "a50", "a51", "a52", "a53", "a54", "a55", "a56", "a57", "a58", "a59", "a60", "a61", "a62", "a63", \
  "v176", "v177", "v178", "v179", "v180", "v181", "v182", "v183", "v184", "v185", "v186", "v187", "v188", "v189", "v190", "v191", "v192", "v193", "v194", "v195", "v196", "v197", "v198", "v199", "v200", "v201", "v202", "v203", "v204", "v205", "v206", "v207", "v208", "v209", "v210", "v211", "v212", "v213", "v214", "v215", "v216", "v217", "v218", "v219", "v220", "v221", "v222", "v223", "v224", "v225", "v226", "v227", "v228", "v229", "v230", "v231", "v232", "v233", "v234", "v235", "v236", "v237", "v238", "v239", "s68", "s69", "scc", "memory"
#define REC_CLOBBERS \
  "v1", "v2", "v3", "v4", "v5", "v6", "v7", "v8", "v9", "v10", "v11", "v12", "v13", "v14", "v15", "v16", "v17", "v18", "v19", "v20", "v21", "v22",\
  "v23", "v24", "v25", "v26", "v27", "v28", "v29", "v30", "v31", "v32", "v33", "v34", "v35", "v36", "v37", "v38", "v39", "v40", "v41", "v42", "v43",\
  "v44", "v45", "v46", "v47", "v48", "v49", "v50", "v51", "v52", "v53", "v54", "v55", "v56", "v57", "v58", "v59", "v60", "v61", "v62", "v63", "v64",\
  "v65", "v66", "v67", "v68", "v69", "v70", "v71", "v72", "v73", "v74", "v75", "v76", "v77", "v78", "v79", "v80", "v81", "v82", "v83", "v84", "v85",\
  "v86", "v87", "v88", "v89", "v90", "v91", "v92", "v93", "v94", "v95", "v96", "v97", "v98", "v99", "v100", "v101", "v102", "v103", "v104", "v105",\
  "v106", "v107", "v108", "v109", "v110", "v111", "v112", "v113", "v114", "v115", "v116", "v117", "v118", "v119", "v120", "v121", "v122", "v123",\
  "v124", "v125", "v126", "v127", "v128", "v129", "v130", "v131", "v132", "v133", "v134", "v135", "v136", "v137", "v138", "v139", "v140", "v141",\
  "v142", "v143", "v144", "v145", "v146", "v147", "v148", "v149", "v150", "v151", "v152", "v153", "v154", "v155", "v156", "v157", "v158", "v159",\
  "v160", "v161", "v162", "v163", "v164", "v165", "v166", "v167", "v168", "v169", "v170", "v171", "v172", "v173", "v174", "v175", "v176", "v177",\
  "v178", "v179", "v180", "v181", "v182", "v183", "v184", "v185", "v186", "v187", "v188", "v189", "v190", "v191", "v192", "v193", "v194", "v195",\
  "v196", "v197", "v198", "v199", "v200", "v201", "v202", "v203", "v204", "v205", "v206", "v207", "v208", "v209", "v210", "v211", "v212", "v213",\
  "v214", "v215", "v216", "v217", "v218", "v219", "v220", "v221", "v222", "v223", "v224", "v225", "v226", "v227", "v228", "v229", "v230", "v231",\
  "v232", "v233", "v234", "v235", "v236", "v237", "v238", "v239", "v240", "v241", "v242", "v243", "v244", "v245", "v246", "v247", "v248", "v249",\
  "v250", "v251", "v252", "v253", "v254", "v255", "a0", "a1", "a2", "a3", "a4", "a5", "a6", "a7", "a8", "a9", "a10", "a11", "a12", "a13", "a14", "a15",\
  "a16", "a17", "a18", "a19", "a20", "a21", "a22", "a23", "a24", "a25", "a26", "a27", "a28", "a29", "a30", "a31", "a32", "a33", "a34", "a35", "a36",\
  "a37", "a38", "a39", "a40", "a41", "a42", "a43", "a44", "a45", "a46", "a47", "a48", "a49", "a50", "a51", "a52", "a53", "a54", "a55", "a56", "a57",\
  "a58", "a59", "a60", "a61", "a62", "a63", "a64", "a65", "a66", "a67", "a68", "a69", "a70", "a71", "a72", "a73", "a74", "a75", "a76", "a77", "a78",\
  "a79", "a80", "a81", "a82", "a83", "a84", "a85", "a86", "a87", "a88", "a89", "a90", "a91", "a92", "a93", "a94", "a95", "a96", "a97", "a98", "a99",\
  "a100", "a101", "a102", "a103", "a104", "a105", "a106", "a107", "a108", "a109", "a110", "a111", "a112", "a113", "a114", "a115", "a116", "a117",\
  "a118", "a119", "a120", "a121", "a122", "a123", "a124", "a125", "a126", "a127", "a128", "a129", "a130", "a131", "a132", "a133", "a134", "a135",\
  "a136", "a137", "a138", "a139", "a140", "a141", "a142", "a143", "a144", "a145", "a146", "a147", "a148", "a149", "a150", "a151", "a152", "a153",\
  "a154", "a155", "a156", "a157", "a158", "a159", "a160", "a161", "a162", "a163", "a164", "a165", "a166", "a167", "a168", "a169", "a170", "a171",\
  "a172", "a173", "a174", "a175", "a176", "a177", "a178", "a179", "a180", "a181", "a182", "a183", "a184", "a185", "a186", "a187", "a188", "a189",\
  "a190", "a191", "a192", "a193", "a194", "a195", "a196", "a197", "a198", "a199", "a200", "a201", "a202", "a203", "a204", "a205", "a206", "a207",\
  "a208", "a209", "a210", "a211", "a212", "a213", "a214", "a215", "a216", "a217", "a218", "a219", "a220", "a221", "a222", "a223", "a224", "a225",\
  "a226", "a227", "a228", "a229", "a230", "a231", "a232", "a233", "a234", "a235", "a236", "a237", "a238", "a239", "a240", "a241", "a242", "a243",\
  "a244", "a245", "a246", "a247", "a248", "a249", "a250", "a251", "a252", "a253", "a254", "a255", "s28", "s29", "s30", "s31", "s34", "s35", "s36", "s37", "s38", "s39", "s40", "s41", "s42",\
  "s43", "s44", "s45", "s46", "s47", "s48", "s49", "s50", "s51", "s52", "s53", "s54", "s55", "s56", "s57", "s58", "s59", "s60", "s61", "s62", "s63",\
  "s64", "s65", "s66", "s67", "s68", "s69", "s70", "s71", "s72", "s73", "s74", "s75", "s76", "s77", "s78", "s79", "s80", "s81", "s82", "s83", "s84",\
  "s85", "s86", "s87", "s88", "s89", "s90", "s91", "s92", "s93", "s94", "s95", "s96", "s97", "s98", "s99", "vcc", "scc", "memory"
__device__ __forceinline__ void role_rec(const Args& a, char LAS* smem, const int rk, const u64 tag) {
  const int cu = rk;
  const int tid = threadIdx.x, w = __builtin_amdgcn_readfirstlane(tid >> 6);
  const int head = cu >> 2, j = cu & 3;
  const u16* pq = a.Wq + head * DH + 64 * (j >> 1);
  const u16* pk = a.Wk + head * DH + 64 * (j >> 1);
  const u16* pv = a.Wv + head * DH + 64 * ((j >> 1) ^ 1);
  const u16* po = a.Wo + (size_t)head * DH * DM + 256 * j;
  asm volatile(REC_ASM
               :: "s"(pq), "s"(pk), "s"(pv), "s"(po), "s"(a.ex), "s"(a.ctrl), "s"(cu), "s"(w), "s"(tag), "s"(a.mem0 + (size_t)(4 * (cu & 1)) * DM + 16 * (cu >> 1))
               : REC_CLOBBERS);
  __builtin_unreachable();
}
#define S_ROW 4160
#define S_WSZ (4 * S_ROW + 96 * 80 + 2048 + 64)
#define T_LD 132
__device__ __forceinline__ u32 offT(u32 pol, u32 ch) { return (ch >> 1) * 16384 + ((~(pol >> (ch >> 8)) & 1u) << 13) + (ch & 1) * 4096; }
__device__ __forceinline__ void wait_all(u32* ctrl, int word, int n, int slp) {
  const int lane = threadIdx.x & 63;
  for (int spins = 0;; ++spins) {
    asm volatile("" ::: "memory");
    const u32 v = __hip_atomic_load(ctrl + word + 32 * min(lane, n - 1), RLX_AGENT);
    if (__builtin_amdgcn_ballot_w64(v == 0) == 0) break;
    if (spins > SPIN_CAP) { set_dead(ctrl); break; }
    if ((spins & 63) == 63 && __builtin_amdgcn_readfirstlane((int)is_dead(ctrl))) break;
    sleepn(slp);
  }
  asm volatile("" ::: "memory");
}
__device__ __forceinline__ void ssm_in(const Args& a, char LAS* smem, const int rb, const u32 pol) {
  const int tid = threadIdx.x;
  const __amdgpu_buffer_rsrc_t rt = __builtin_amdgcn_make_buffer_rsrc(a.ex + WS_UT_OFF, 0, 8 << 20, 0x00020000);
#pragma unroll
  for (int q = 0; q < 4; ++q) {
    u32x4 x[8];
#pragma unroll
    for (int i = 0; i < 4; ++i) {
      const u16* p = a.u + (size_t)(64 * rb + 2 * (8 * q + 2 * i + (tid >> 7))) * SSM_D + (tid & 127) * 8;
      x[2 * i] = *(const u32x4*)p; x[2 * i + 1] = *(const u32x4*)(p + SSM_D);
    }
#pragma unroll
    for (int i = 0; i < 4; ++i)
#pragma unroll
      for (int e = 0; e < 4; ++e) {
        char LAS* o = smem + ((tid & 127) + 256 * e) * T_LD + (8 * q + 2 * i + (tid >> 7)) * 4;
        *(LAS u32*)o = __builtin_amdgcn_perm(x[2 * i + 1][e], x[2 * i][e], 0x05040100u);
        *(LAS u32*)(o + 128 * T_LD) = __builtin_amdgcn_perm(x[2 * i + 1][e], x[2 * i][e], 0x07060302u);
      }
  }
  __syncthreads();
#pragma unroll 4
  for (int i = 0; i < 32; ++i) {
    const int sl = 32 * i + (tid >> 3), ch = (sl & 127) * 8 + (sl >> 7);
    const LAS u32* p = (const LAS u32*)(smem + sl * T_LD + (tid & 7) * 16);
    __builtin_amdgcn_raw_buffer_store_b128((u32x4){p[0], p[1], p[2], p[3]}, rt, offT(pol, ch) + rb * 128 + (tid & 7) * 16, 0, SC1);
  }
  WAIT_VM0();
  __syncthreads();
  if (tid == 0) __hip_atomic_store(a.ctrl + CW_UIN + rb * 32, 1u, RLX_AGENT);
}
__device__ __forceinline__ void ssm_out(const Args& a, char LAS* smem, const int rb, const int hb, const u32 pol) {
  const int tid = threadIdx.x;
  const __amdgpu_buffer_rsrc_t rt = __builtin_amdgcn_make_buffer_rsrc(a.ex + WS_UT_OFF, 0, 8 << 20, 0x00020000);
  const __amdgpu_buffer_rsrc_t ro = __builtin_amdgcn_make_buffer_rsrc(a.out, 0, S_LEN * OUTLD * 2, 0x00020000);
  wait_all(a.ctrl, CW_YOUT + hb * 48 * 32, 48, 8);
  u32x4 x[16];
#pragma unroll
  for (int i = 0; i < 16; ++i) {
    const int sl = 32 * i + (tid >> 3), ch = (sl & 63) * 8 + (sl >> 6);
    x[i] = __builtin_amdgcn_raw_buffer_load_b128(rt, offT(pol, 512 * hb + ch) + rb * 128 + (tid & 7) * 16, 0, SC1);
  }
#pragma unroll
  for (int i = 0; i < 16; ++i) {
    LAS u32* p = (LAS u32*)(smem + (32 * i + (tid >> 3)) * T_LD + (tid & 7) * 16);
#pragma unroll
    for (int e = 0; e < 4; ++e) p[e] = x[i][e];
  }
  __syncthreads();
#pragma unroll 2
  for (int i = 0; i < 8; ++i) {
    const int rp = 4 * i + (tid >> 6);
    u32x4 v0, v1;
#pragma unroll
    for (int e = 0; e < 4; ++e) {
      const char LAS* o = smem + ((tid & 63) + 128 * e) * T_LD + rp * 4;
      const u32 x0 = *(const LAS u32*)o, x1 = *(const LAS u32*)(o + 64 * T_LD);
      v0[e] = __builtin_amdgcn_perm(x1, x0, 0x05040100u); v1[e] = __builtin_amdgcn_perm(x1, x0, 0x07060302u);
    }
    const u32 oo = ((64 * rb + 2 * rp) * OUTLD + DM + 512 * hb + (tid & 63) * 8) * 2;
    __builtin_amdgcn_raw_buffer_store_b128(v0, ro, oo, 0, OAUX);
    __builtin_amdgcn_raw_buffer_store_b128(v1, ro, oo + OUTLD * 2, 0, OAUX);
  }
}
__device__ __forceinline__ u32 lo_hi(u32 lo, u32 hi) { return __builtin_amdgcn_perm(hi, lo, 0x07060100u); }
__device__ __forceinline__ void role_ssm(const Args& a, char LAS* smem, const int cx, const int rank, const u64 tag) {
  u32 pol = 0;
  const int tid = threadIdx.x, lane = tid & 63, w = __builtin_amdgcn_readfirstlane(tid >> 6);
  const int c = lane & 15, g = lane >> 4, b = lane & 31, h = lane >> 5;
  const __amdgpu_buffer_rsrc_t rt = __builtin_amdgcn_make_buffer_rsrc(a.ex + WS_UT_OFF, 0, 8 << 20, 0x00020000);
  char LAS* lu = smem + w * S_WSZ;
  char LAS* lw = lu + S_ROW;
  char LAS* lk = lw + 3 * S_ROW + 31 * 80;
  char LAS* lp = lk + 65 * 80;
  if (lane < 4) *(LAS u32x4*)(lu + lane * 16) = (u32x4){0, 0, 0, 0};
  for (int i = lane; i < 160; i += 64) *(LAS u32x4*)(lw + 3 * S_ROW + i * 16) = (u32x4){0, 0, 0, 0};
  float la[3];
  la[0] = (float)c;
  la[1] = c < 8 ? (float)(256 + 2 * c) : (float)(512 + 256 * ((c - 8) >> 2) + 4 * ((c - 8) & 3));
  la[2] = c < 8 ? (float)(1024 + 256 * (c >> 1) + 8 * (c & 1)) : (c == 8 ? 2048.f : 0.f);
  const float ly = (float)(16 * c);
  u32x4 un[4];
  auto fetch_u = [&](int d) {
#pragma unroll
    for (int i = 0; i < 4; ++i) un[i] = __builtin_amdgcn_raw_buffer_load_b128(rt, lane * 16 + i * 1024, offT(pol, d), SC1);
  };
  u32 pr[6]; u16 pdt, pds;
  auto fetch_p = [&](int d) {
    const size_t o = (size_t)d * SSM_N + 2 * lane;
    pr[0] = *(const u32*)(a.A_re + o); pr[1] = *(const u32*)(a.A_im + o); pr[2] = *(const u32*)(a.B_re + o);
    pr[3] = *(const u32*)(a.B_im + o); pr[4] = *(const u32*)(a.C_re + o); pr[5] = *(const u32*)(a.C_im + o);
    pdt = a.log_dt[d]; pds = a.Dskip[d];
  };
#define SST(i)
  const int first = cx * 256 + rank * 3 + w, last = cx * 256 + 256;
  const int p0 = 32 + b - 8 * h - 7, cp = p0 & 3;
  f32x4 ka[3];
  f32x16 y0, y1;
  float dsn = 0.f;
  auto params = [&]() {
    const float dt = rbf(expf(bf2f(pdt)));
    dsn = bf2f(pds);
#pragma unroll
    for (int i = 0; i < 2; ++i) {
      auto el = [&](int k) { return __uint_as_float(i ? (pr[k] & 0xffff0000u) : (pr[k] << 16)); };
      const float br = dt * el(2), bi = dt * el(3), cr = el(4), ci = el(5);
      *(LAS f32x4*)(lp + (2 * lane + i) * 16) = (f32x4){dt * el(0) * 1.4426950408889634f, dt * el(1) * 0.15915494309189535f, cr * br - ci * bi, cr * bi + ci * br};
    }
  };
  auto products = [&]() {
    u32 ra = (u32)(uintptr_t)(lu + cp * S_ROW + (p0 - cp) * 2 - 32), rb = (u32)(uintptr_t)(lk + 16 * h + 80 * (b - 3)), rp = (u32)(uintptr_t)(lp + g * 64);
#pragma unroll
    for (int t = 0; t < 3; ++t) ka[t] = (f32x4){0, 0, 0, 0};
#pragma unroll
    for (int i = 0; i < 16; ++i) { y0[i] = 0.f; y1[i] = 0.f; }
    asm volatile(CONV_ASM : "+v"(y0), "+v"(y1), "+v"(ka[0]), "+v"(ka[1]), "+v"(ka[2]), "+v"(ra), "+v"(rb), "+v"(rp)
                 : "v"(la[0]), "v"(la[1]), "v"(la[2]), "v"(ly) : CONV_CLOBBERS);
  };
  fetch_p(first);
  {
    for (int d = first - 24; d < last; d += 24) {
      const int nxt = d + 24 < last ? d + 24 : d;
      const float ds = dsn;
      SST(0);
      if (d >= first) {
      {
        const int l = 16 * c + 4 * g;
        *(LAS u32x2*)(lk + (1 + (l >> 5)) * 80 + ((l & 31) ^ 4) * 2) = (u32x2){pk2(ka[0][3], ka[0][2]), pk2(ka[0][1], ka[0][0])};
        const int j1 = g < 2 ? 8 * c + 4 * g : 128 + 64 * (g - 2) + 4 * c;
        *(LAS u32x4*)(lp + j1 * 4) = (u32x4){pk2(ka[1][0], ka[1][0]), pk2(ka[1][1], ka[1][1]), pk2(ka[1][2], ka[1][2]), pk2(ka[1][3], ka[1][3])};
        if (g < 2) {
          *(LAS u32x2*)(lp + (256 + 64 * g + 2 * c) * 4) = (u32x2){pk2(ka[2][0], ka[2][0]), pk2(ka[2][1], ka[2][1])};
          *(LAS u32x2*)(lp + (256 + 64 * g + 32 + 2 * c) * 4) = (u32x2){pk2(ka[2][2], ka[2][2]), pk2(ka[2][3], ka[2][3])};
        } else if (lane == 32) *(LAS u32*)(lp + 384 * 4) = pk2(ka[2][0], ka[2][0]);
        if (lane < 32) {
          const u32x4 v = *(const LAS u32x4*)(lp + lane * 16);
          const u32 v4 = *(const LAS u32*)(lp + lane * 16 + 16);
          *(LAS u32x4*)(lk + (9 + (lane >> 2)) * 80 + (lane & 3) * 16) = (u32x4){lo_hi(v4, v[3]), v[2], lo_hi(v[2], v[1]), v[0]};
        }
        {
          const u32x2 v = *(const LAS u32x2*)(lp + 512 + lane * 8);
          const u32 v2 = *(const LAS u32*)(lp + 512 + lane * 8 + 8);
          *(LAS u32x4*)(lk + (17 + (lane >> 2)) * 80 + (lane & 3) * 16) = (u32x4){v2, v[1], lo_hi(v[1], v[0]), v[0]};
        }
        {
          const u32x2 v = *(const LAS u32x2*)(lp + 1024 + lane * 8);
          const u32 v2 = *(const LAS u32*)(lp + 1024 + lane * 8 + 8);
          char LAS* o = lk + (33 + (lane >> 1)) * 80 + (lane & 1) * 32;
          *(LAS u32x4*)o = (u32x4){v[1], lo_hi(v[1], v[0]), v[0], v[0]};
          *(LAS u32x4*)(o + 16) = (u32x4){v2, v2, v[1], v[1]};
        }
      }
      if (d == first) { pol = pol_of(wait_ready(a.ctrl, tag, 4 + cx)); wait_all(a.ctrl, CW_UIN, 32, 8); fetch_u(d); }
#pragma unroll
      for (int i = 0; i < 4; ++i) *(LAS u32x4*)(lu + 64 + lane * 16 + i * 1024) = un[i];
      if (nxt != d) fetch_u(nxt);
#pragma unroll
      for (int i = 0; i < 5; ++i) {
        const int q = lane + 64 * i;
        if (i < 4 || lane < 4) {
          const u32x4 x = *(const LAS u32x4*)(lu + q * 16);
          u32x2 y = *(const LAS u32x2*)(lu + q * 16 + 16);
          *(LAS u32x4*)(lw + q * 16) = (u32x4){__builtin_amdgcn_alignbit(x[1], x[0], 16), __builtin_amdgcn_alignbit(x[2], x[1], 16), __builtin_amdgcn_alignbit(x[3], x[2], 16), __builtin_amdgcn_alignbit(y[0], x[3], 16)};
          *(LAS u32x4*)(lw + S_ROW + q * 16) = (u32x4){x[1], x[2], x[3], y[0]};
          *(LAS u32x4*)(lw + 2 * S_ROW + q * 16) = (u32x4){__builtin_amdgcn_alignbit(x[2], x[1], 16), __builtin_amdgcn_alignbit(x[3], x[2], 16), __builtin_amdgcn_alignbit(y[0], x[3], 16), __builtin_amdgcn_alignbit(y[1], y[0], 16)};
        }
      }
      }
      SST(1);
      params();
      fetch_p(nxt + 24 < last ? nxt + 24 : nxt);
      SST(2);
      products();
      if (d < first) continue;
      SST(4);
#pragma unroll
      for (int cg = 0; cg < 2; ++cg)
#pragma unroll
        for (int i4 = 0; i4 < 4; ++i4) {
          LAS u32x2* up = (LAS u32x2*)(lu + 64 + (32 * (32 * cg + b) + 8 * i4 + 4 * h) * 2);
          const u32x2 uv = *up;
          float v[4];
#pragma unroll
          for (int e = 0; e < 4; ++e) v[e] = cg ? y1[4 * i4 + e] : y0[4 * i4 + e];
          const u32 ya = pk2(v[0], v[1]), yb = pk2(v[2], v[3]);
          const u32 sa = pk2(__uint_as_float(uv[0] << 16) * ds, __uint_as_float(uv[0] & 0xffff0000u) * ds);
          const u32 sb = pk2(__uint_as_float(uv[1] << 16) * ds, __uint_as_float(uv[1] & 0xffff0000u) * ds);
          *up = (u32x2){pk2(__uint_as_float(ya << 16) + __uint_as_float(sa << 16), __uint_as_float(ya & 0xffff0000u) + __uint_as_float(sa & 0xffff0000u)),
                        pk2(__uint_as_float(yb << 16) + __uint_as_float(sb << 16), __uint_as_float(yb & 0xffff0000u) + __uint_as_float(sb & 0xffff0000u))};
        }
#pragma unroll
      for (int i = 0; i < 4; ++i) __builtin_amdgcn_raw_buffer_store_b128(*(const LAS u32x4*)(lu + 64 + lane * 16 + i * 1024), rt, lane * 16 + i * 1024, offT(pol, d), SC1);
      SST(5);
    }
    WAIT_VM0();
    if (lane == 0) __hip_atomic_store(a.ctrl + CW_YOUT + (cx * 24 + rank * 3 + w) * 32, 1u, RLX_AGENT);
  }
}
#define BULK_CLOBBERS \
  "v4", "v5", "v6", "v7", "v8", "v9", "v10", "v11", "v12", "v13", "v14", "v15", "v16", "v17", "v18", "v19", "v20", "v21", "v22", "v23", "v24", "v25",\
  "v26", "v27", "v28", "v29", "v30", "v31", "v32", "v33", "v34", "v35", "v36", "v37", "v38", "v39", "v40", "v41", "v42", "v43", "v44", "v45", "v46",\
  "v47", "v48", "v49", "v50", "v51", "v52", "v53", "v54", "v55", "v56", "v57", "v58", "v59", "v60", "v61", "v62", "v63", "v64", "v65", "v66", "v67",\
  "v68", "v69", "v70", "v71", "v72", "v73", "v74", "v75", "v76", "v77", "v78", "v79", "v80", "v81", "v82", "v83", "v84", "v85", "v86", "v87", "v88",\
  "v89", "v90", "v91", "v92", "v93", "v94", "v95", "v96", "v97", "v98", "v99", "v100", "v101", "v102", "v103", "v104", "v105", "v106", "v107", "v108",\
  "v109", "v110", "v111", "v112", "v113", "v114", "v115", "v116", "v117", "v118", "v119", "v120", "v121", "v122", "v123", "v124", "v125", "v126",\
  "v127", "v128", "v129", "v130", "v131", "v132", "v133", "v134", "v135", "v136", "v137", "v138", "v139", "v140", "v141", "v142", "v143", "v144",\
  "v145", "v146", "v147", "v148", "v149", "v150", "v151", "v152", "v153", "v154", "v155", "v156", "v157", "v158", "v159", "v160", "v161", "v162",\
  "v163", "v164", "v165", "v166", "v167", "v168", "v169", "v170", "v171", "v172", "v173", "v174", "v175", "v176", "v177", "v178", "v179", "v180",\
  "v181", "v182", "v183", "v184", "v185", "v186", "v187", "v188", "v189", "v190", "v191", "v192", "v193", "v194", "v195", "v196", "v197", "v198",\
  "v199", "v200", "v201", "v202", "v203", "v204", "v205", "v206", "v207", "v208", "v209", "v210", "v211", "v212", "v213", "v214", "v215", "v216",\
  "v217", "v218", "v219", "v220", "v221", "v222", "v223", "v224", "v225", "v226", "v227", "v228", "v229", "v230", "v231", "v232", "v233", "v234",\
  "v235", "v236", "v237", "v238", "v239", "v240", "v241", "v242", "v243", "v244", "v245", "v246", "v247", "v248", "v249", "v250", "v251", "v252",\
  "v253", "v254", "v255", "a0", "a1", "a2", "a3", "a4", "a5", "a6", "a7", "a8", "a9", "a10", "a11", "a12", "a13", "a14", "a15", "a16", "a17", "a18",\
  "a19", "a20", "a21", "a22", "a23", "a24", "a25", "a26", "a27", "a28", "a29", "a30", "a31", "a32", "a33", "a34", "a35", "a36", "a37", "a38", "a39",\
  "a40", "a41", "a42", "a43", "a44", "a45", "a46", "a47", "a48", "a49", "a50", "a51", "a52", "a53", "a54", "a55", "a56", "a57", "a58", "a59", "a60",\
  "a61", "a62", "a63", "a64", "a65", "a66", "a67", "a68", "a69", "a70", "a71", "a72", "a73", "a74", "a75", "a76", "a77", "a78", "a79", "a80", "a81",\
  "a82", "a83", "a84", "a85", "a86", "a87", "a88", "a89", "a90", "a91", "a92", "a93", "a94", "a95", "a96", "a97", "a98", "a99", "a100", "a101", "a102",\
  "a103", "a104", "a105", "a106", "a107", "a108", "a109", "a110", "a111", "a112", "a113", "a114", "a115", "a116", "a117", "a118", "a119", "a120",\
  "a121", "a122", "a123", "a124", "a125", "a126", "a127", "a128", "a129", "a130", "a131", "a132", "a133", "a134", "a135", "a136", "a137", "a138",\
  "a139", "a140", "a141", "a142", "a143", "a144", "a145", "a146", "a147", "a148", "a149", "a150", "a151", "a152", "a153", "a154", "a155", "a156",\
  "a157", "a158", "a159", "a160", "a161", "a162", "a163", "a164", "a165", "a166", "a167", "a168", "a169", "a170", "a171", "a172", "a173", "a174",\
  "a175", "a176", "a177", "a178", "a179", "a180", "a181", "a182", "a183", "a184", "a185", "a186", "a187", "a188", "a189", "a190", "a191", "a192",\
  "a193", "a194", "a195", "a196", "a197", "a198", "a199", "a200", "a201", "a202", "a203", "a204", "a205", "a206", "a207", "a208", "a209", "a210",\
  "a211", "a212", "a213", "a214", "a215", "a216", "a217", "a218", "a219", "a220", "a221", "a222", "a223", "a224", "a225", "a226", "a227", "a228",\
  "a229", "a230", "a231", "a232", "a233", "a234", "a235", "a236", "a237", "a238", "a239", "a240", "a241", "a242", "a243", "a244", "a245", "a246",\
  "a247", "a248", "a249", "a250", "a251", "a252", "a253", "a254", "a255", "s68", "s69", "s70", "s71", "s72", "s73", "s74", "s75", "s76", "s77", "s78",\
  "s79", "s80", "s81", "s82", "s83", "s84", "s85", "s86", "s87", "s88", "s89", "s90", "s91", "s92", "s93", "s94", "s95", "s96", "vcc", "scc", "memory"
#define GEMM_CLOBBERS \
  "v4", "v5", "v6", "v7", "v8", "v9", "v10", "v11", "v12", "v13", "v14", "v15", "v16", "v17", "v18", "v19", "v20", "v21", "v22", "v23", "v24", "v25",\
  "v26", "v27", "v28", "v29", "v30", "v31", "v32", "v33", "v34", "v35", "v36", "v37", "v38", "v39", "v40", "v41", "v42", "v43", "v44", "v45", "v46",\
  "v47", "v48", "v49", "v50", "v51", "v52", "v53", "v54", "v55", "v56", "v57", "v58", "v59", "v60", "v61", "v62", "v63", "v64", "v65", "v66", "v67",\
  "v68", "v69", "v70", "v71", "v72", "v73", "v74", "v75", "v76", "v77", "v78", "v79", "v80", "v81", "v82", "v83", "v84", "v85", "v86", "v87", "v88",\
  "v89", "v90", "v91", "v92", "v93", "v94", "v95", "v96", "v97", "v98", "v99", "v116", "v117", "v118", "v119", "v120", "v121", "v122", "v123", "v124",\
  "v125", "v126", "v127", "v128", "v129", "v130", "v131", "v132", "v133", "v134", "v135", "v136", "v137", "v138", "v139", "v140", "v141", "v142",\
  "v143", "v144", "v145", "v146", "v147", "v148", "v149", "v150", "v151", "v152", "v153", "v154", "v155", "v156", "v157", "v158", "v159", "v160",\
  "v161", "v162", "v163", "v164", "v165", "v166", "v167", "v168", "v169", "v170", "v171", "v172", "v173", "v174", "v175", "v176", "v177", "v178",\
  "v179", "v180", "v181", "v182", "v183", "v184", "v185", "v186", "v187", "v188", "v189", "v190", "v191", "v192", "v193", "v194", "v195", "v196",\
  "v197", "v198", "v199", "v200", "v201", "v202", "v203", "v204", "v205", "v206", "v207", "v208", "v209", "v210", "v211", "v212", "v213", "v214",\
  "v215", "v216", "v217", "v218", "v219", "v220", "v221", "v222", "v223", "v224", "v225", "v226", "v227", "v228", "v229", "v230", "v231", "v232",\
  "v233", "v234", "v235", "v236", "v237", "v238", "v239", "v240", "v241", "v242", "v243", "v244", "v245", "v246", "v247", "v248", "v249", "v250",\
  "v251", "v252", "v253", "v254", "v255", "a0", "a1", "a2", "a3", "a4", "a5", "a6", "a7", "a8", "a9", "a10", "a11", "a12", "a13", "a14", "a15", "a16",\
  "a17", "a18", "a19", "a20", "a21", "a22", "a23", "a24", "a25", "a26", "a27", "a28", "a29", "a30", "a31", "a32", "a33", "a34", "a35", "a36", "a37",\
  "a38", "a39", "a40", "a41", "a42", "a43", "a44", "a45", "a46", "a47", "a48", "a49", "a50", "a51", "a52", "a53", "a54", "a55", "a56", "a57", "a58",\
  "a59", "a60", "a61", "a62", "a63", "a64", "a65", "a66", "a67", "a68", "a69", "a70", "a71", "a72", "a73", "a74", "a75", "a76", "a77", "a78", "a79",\
  "a80", "a81", "a82", "a83", "a84", "a85", "a86", "a87", "a88", "a89", "a90", "a91", "a92", "a93", "a94", "a95", "a96", "a97", "a98", "a99", "a100",\
  "a101", "a102", "a103", "a104", "a105", "a106", "a107", "a108", "a109", "a110", "a111", "a112", "a113", "a114", "a115", "a116", "a117", "a118",\
  "a119", "a120", "a121", "a122", "a123", "a124", "a125", "a126", "a127", "a128", "a129", "a130", "a131", "a132", "a133", "a134", "a135", "a136",\
  "a137", "a138", "a139", "a140", "a141", "a142", "a143", "a144", "a145", "a146", "a147", "a148", "a149", "a150", "a151", "a152", "a153", "a154",\
  "a155", "a156", "a157", "a158", "a159", "a160", "a161", "a162", "a163", "a164", "a165", "a166", "a167", "a168", "a169", "a170", "a171", "a172",\
  "a173", "a174", "a175", "a176", "a177", "a178", "a179", "a180", "a181", "a182", "a183", "a184", "a185", "a186", "a187", "a188", "a189", "a190",\
  "a191", "a192", "a193", "a194", "a195", "a196", "a197", "a198", "a199", "a200", "a201", "a202", "a203", "a204", "a205", "a206", "a207", "a208",\
  "a209", "a210", "a211", "a212", "a213", "a214", "a215", "a216", "a217", "a218", "a219", "a220", "a221", "a222", "a223", "a224", "a225", "a226",\
  "a227", "a228", "a229", "a230", "a231", "a232", "a233", "a234", "a235", "a236", "a237", "a238", "a239", "a240", "a241", "a242", "a243", "a244",\
  "a245", "a246", "a247", "a248", "a249", "a250", "a251", "a252", "a253", "a254", "a255", "s68", "s69", "s70", "s71", "s72", "s73", "s74", "s75", "s76",\
  "s77", "s78", "s79", "s80", "s81", "s82", "s83", "s84", "s85", "s86", "s87", "s88", "s89", "s90", "s91", "s92", "s93", "s94", "s95", "s96", "vcc", "scc", "memory"
__device__ __forceinline__ void role_gemm(const Args& a, char LAS* smem, const int gi, const u64 tag, const int grp) {
  const int w = __builtin_amdgcn_readfirstlane(threadIdx.x >> 6);
  u32 p13;
  asm volatile(GEMM_ASM
               : "=&s"(p13) : "s"(a.Wq + 64 * gi), "s"(a.Wk + 64 * (gi ^ 1)), "s"(a.Wv + 64 * (gi ^ 2)), "s"(a.x), "s"(a.ex + WS_Q_OFF), "s"(a.ctrl), "s"(gi), "s"(w), "s"((u32)(uintptr_t)smem), "s"(tag), "s"(grp)
               : GEMM_CLOBBERS);
  ssm_out(a, smem, gi & 31, gi >> 5, pol_of(p13 << 13));
}
__device__ __forceinline__ void role_xo(const Args& a, char LAS* smem, const int xi, const u64 tag, const int grp) {
  const int w = __builtin_amdgcn_readfirstlane(threadIdx.x >> 6);
  sleepn(XO_SLEEP);
  const u32 pol = pol_of(wait_ready(a.ctrl, tag, grp));
  ssm_in(a, smem, xi, pol);
  sleepn(XO_SLEEP2);
  asm volatile(XO_ASM
               :: "s"(a.Wo + 64 * (xi >> 1)), "s"(a.ex + WS_OX_OFF), "s"(a.out), "s"(a.ctrl), "s"(xi), "s"(w), "s"((u32)(uintptr_t)smem), "s"(pol)
               : BULK_CLOBBERS);
}
__device__ __forceinline__ void role_xa(const Args& a, char LAS* smem, const int head, const u64 tag, const int grp) {
  const int lane = threadIdx.x & 63;
  if (head == 0) make_ready(a.ctrl, tag);
  sleepn(XPRE);
  {
    u32 acc = 0;
    for (int i = 0; i < 16; ++i) acc |= *(const u32*)((const char*)a.x + ((size_t)(i * 32 + head) * 64 + lane) * 128);
    const u16* pp[6] = {a.A_re, a.A_im, a.B_re, a.B_im, a.C_re, a.C_im};
#pragma unroll
    for (int i = 0; i < 6; ++i) acc |= *(const u32*)((const char*)pp[i] + ((size_t)head * 64 + lane) * 128);
    asm volatile("" :: "v"(acc));
  }
  const u32 code = wait_ready(a.ctrl, tag, grp), pol = pol_of(code);
  asm volatile(XA_ASM
               :: "s"(a.ex), "s"(a.ex + WS_Q_OFF), "s"(a.ctrl), "s"(head), "s"(pol), "s"((u32)(uintptr_t)smem + 3 * S_WSZ), "s"(code)
               : BULK_CLOBBERS);
}
__global__ void __launch_bounds__(256, 1) mega_kernel(Args a) {
  __shared__ __attribute__((aligned(16))) char smem_raw[163840];
  char LAS* smem = (char LAS*)smem_raw;
  const int tid = threadIdx.x, b = blockIdx.x;
  const u32 xcc = (u32)__builtin_amdgcn_s_getreg(20 | (3 << 11)) & 7u;
  const int rk = b >> 3, pg = (rk & 12) == 4 ? 24 + (rk >> 4) * 4 + (rk & 3) : rk - (rk > 7 ? 4 : 0) - (rk > 23 ? 4 : 0);
  if (tid < 64) {
    u64 t = __hip_atomic_load((u64*)(a.ctrl + CW_READY + (tid & 7) * 32), RLX_AGENT);
#pragma unroll
    for (int k = 1; k < 8; k *= 2) { const u64 o = __shfl_xor(t, k); t = o > t ? o : t; }
    t += 1;
    if (tid == 0) {
      int role = -1;
      for (int k = 0; k < 256 && role < 0; ++k) {
        const int r = (int)(xcc * 32 + (b >> 3) + k) & 255;
        if (__hip_atomic_exchange((u64*)(a.ctrl + CW_CLAIM + r * 32), t, RLX_AGENT) != t) role = r;
      }
      *(LAS u64*)smem = t;
      *(LAS int*)(smem + 8) = role;
    }
  } else if (tid < 128) {
    u32 b0 = 0, b1 = 0;
    if (xcc < 4 && pg < EX_PAGES) {
      const __amdgpu_buffer_rsrc_t rs = __builtin_amdgcn_make_buffer_rsrc(a.ex, 0, (int)EX_BYTES, 0x00020000);
      const u32 bit = (pg & 4) ? 16384u : 4096u, flip = ((pg & 4) ? xcc : xcc >> 1) & 1u;
      const u32 o0 = ((u32)pg << 21) + (b & 7) * 32768 + ((pg >> 1) & 1) * 8192 + (pg & 15) * 256 + flip * bit + (tid < 72 ? (tid - 64) * 16 : 0x80000000u), o1 = o0 ^ bit;
      b0 = b1 = 4095u;
#pragma unroll 1
      for (int rep = 0; rep < NPROBE; ++rep) {
        u64 t0, t1, t2;
        asm volatile("s_memtime %0\n\tbuffer_store_dwordx4 %3, %4, %6, 0 offen sc1\n\ts_waitcnt vmcnt(0)\n\ts_memtime %1\n\tbuffer_store_dwordx4 %3, %5, %6, 0 offen sc1\n\t"
                     "s_waitcnt vmcnt(0)\n\ts_memtime %2\n\ts_waitcnt lgkmcnt(0)"
                     : "=&s"(t0), "=&s"(t1), "=&s"(t2) : "v"((u32x4){SENT, SENT, SENT, SENT}), "v"(o0), "v"(o1), "s"(rs) : "memory");
        b0 = min(b0, (u32)(t1 - t0)); b1 = min(b1, (u32)(t2 - t1));
      }
    }
    if (tid == 64) *(LAS u32*)(smem + 12) = b0 | (b1 << 16);
  }
  __syncthreads();
  const u32x4 st = *(LAS u32x4*)smem;
  const int role = __builtin_amdgcn_readfirstlane(st[2]);
  const u64 tag = (u64)(u32)__builtin_amdgcn_readfirstlane(st[0]) | ((u64)(u32)__builtin_amdgcn_readfirstlane(st[1]) << 32);
  if (tid >= 64 && tid < 72)
    __builtin_amdgcn_raw_buffer_store_b128((u32x4){st[0], st[1], st[3], 0u}, __builtin_amdgcn_make_buffer_rsrc(a.ctrl, 0, CTRL_BYTES, 0x00020000), (tid - 64) * 16, (CW_MEAS + (pg * 8 + (b & 7)) * 32) * 4, SC1);
  __syncthreads();
  if (role < 0) return;
#define DONE(c, w)
  const int cx = (role - 128) >> 5, t = role & 31;
  if (role < 128) role_rec(args(), smem, role, tag);
  else if (t < 8) { role_xo(args(), smem, ((cx >> 1) * 8 + t) * 2 + (cx & 1), tag, 4 + cx); DONE(tid == 0, 6200 + role); }
  else if (t >= 16) { role_gemm(args(), smem, (cx >> 1) * 32 + (t - 16) * 2 + (cx & 1), tag, 4 + cx); DONE(tid == 0, 6200 + role); }
  else if (__builtin_amdgcn_readfirstlane(tid >> 6) == 3) { role_xa(args(), smem, cx * 8 + t - 8, tag, 4 + cx); DONE(tid == 192, 7320 + cx * 8 + t - 8); }
  else { role_ssm(args(), smem, cx, t - 8, tag); DONE(tid == 0, 6200 + role); }
}

extern "C" void kernel_launch(void* const* d_in, const int* in_sizes, int n_in, void* d_out, int out_size,
                              void* d_ws, size_t ws_size, hipStream_t stream) {
  Args a;
  a.x = (const u16*)d_in[0]; a.mem0 = (const u16*)d_in[1];
  a.Wq = (const u16*)d_in[2]; a.Wk = (const u16*)d_in[3]; a.Wv = (const u16*)d_in[4]; a.Wo = (const u16*)d_in[5];
  a.u = (const u16*)d_in[7]; a.A_re = (const u16*)d_in[8]; a.A_im = (const u16*)d_in[9];
  a.B_re = (const u16*)d_in[10]; a.B_im = (const u16*)d_in[11]; a.C_re = (const u16*)d_in[12]; a.C_im = (const u16*)d_in[13];
  a.log_dt = (const u16*)d_in[14]; a.Dskip = (const u16*)d_in[15];
  a.out = (u16*)d_out;
  char* ws = (char*)d_ws;
  a.ctrl = (u32*)ws;
  a.ex = (char*)(((uintptr_t)ws + CTRL_BYTES + (2u << 20) - 1) & ~(uintptr_t)((2u << 20) - 1));
  mega_kernel<<<256, 256, 0, stream>>>(a);
}
```
